```python
import torch

def _softmax(x, axis=-1):
    return torch.softmax(x, dim=axis)

def _gelu(x):
    return torch.nn.functional.gelu(x, approximate='tanh')

def _silu(x):
    return torch.nn.functional.silu(x)

def _sigmoid(x):
    return torch.sigmoid(x)

def _layernorm(x, axis=-1, eps=1e-05):
    m = x.mean(dim=axis, keepdim=True)
    v = x.var(dim=axis, keepdim=True, unbiased=False)
    return (x - m) / torch.sqrt(v + eps)

def _scatter_add(tgt, idx, src, axis=0):
    return tgt.clone().index_add_(axis, idx, src)

def _segment_sum(src, seg, n):
    out = torch.zeros((n,) + src.shape[1:], dtype=src.dtype)
    return out.index_add_(0, seg, src)

def _pad(x, pw):
    flat = tuple((v for pair in reversed(pw) for v in pair))
    return torch.nn.functional.pad(x, flat)
kernel_name = 'recurrent_scan__ssm_conv_h3__mixer_gmlp__prompt'

def setup_inputs():

    _w = lambda a: (lambda t: t.to(torch.bfloat16) if t.is_floating_point() else t)(torch.as_tensor(a))

    def _A_setup():
        B, S, D = (1, 2048, 640)
        ks = 4
        return {'q': _w((lambda *s: torch.randn(*s, dtype=torch.bfloat16) * 0.05)(B, S, D)), 'k': _w((lambda *s: torch.randn(*s, dtype=torch.bfloat16) * 0.2)(B, S, D)), 'v': _w((lambda *s: torch.randn(*s, dtype=torch.bfloat16) * 0.2)(B, S, D)), 'shift_k': _w((lambda *s: torch.randn(*s, dtype=torch.bfloat16) * 0.2)(D, ks) * 0.5), 'ssm_k': _w((lambda *s: torch.randn(*s, dtype=torch.bfloat16) * 0.2)(D, S) * (1.0 / S ** 0.5)), 'ssm_D': _w((lambda *s: torch.randn(*s, dtype=torch.bfloat16) * 0.2)(D))}
    _A_in = _A_setup()

    def _B_setup():
        B, S, D, F = (1, 2048, 640, 1536)
        Fh = F // 2
        return {'x': _w((lambda *s: torch.randn(*s, dtype=torch.bfloat16) * 0.05)(B, S, D)), 'w_in': _w((lambda *s: torch.randn(*s, dtype=torch.bfloat16) * 0.8)(D, 2 * Fh)), 'w_sp': _w(0.01 * (lambda *s: torch.randn(*s, dtype=torch.bfloat16) * 0.8)(S, S)), 'b_sp': _w(1.0 + 0.01 * (lambda *s: torch.randn(*s, dtype=torch.bfloat16) * 0.1)(S)), 'w_out': _w((lambda *s: torch.randn(*s, dtype=torch.bfloat16) * 0.1)(Fh, D))}
    _B_in = _B_setup()
    return {'A_q': _A_in['q'], 'A_k': _A_in['k'], 'A_v': _A_in['v'], 'A_shift_k': _A_in['shift_k'], 'A_ssm_k': _A_in['ssm_k'], 'A_ssm_D': _A_in['ssm_D'], 'B_x': _B_in['x'], 'B_w_in': _B_in['w_in'], 'B_w_sp': _B_in['w_sp'], 'B_b_sp': _B_in['b_sp'], 'B_w_out': _B_in['w_out']}

def reference(A_q, A_k, A_v, A_shift_k, A_ssm_k, A_ssm_D, B_x, B_w_in, B_w_sp, B_b_sp, B_w_out):

    def _A_ref(q, k, v, shift_k, ssm_k, ssm_D):
        B, S, D = q.shape
        ks = shift_k.shape[-1]
        k_pad = _pad(k, ((0, 0), (ks - 1, 0), (0, 0)))
        k_shift = torch.zeros_like(k)
        for t in range(ks):
            k_shift = k_shift + k_pad[:, t:t + S, :] * shift_k[:, ks - 1 - t][None, None, :]
        kv = k_shift * v
        L = 2 * S
        kv_pad = _pad(kv, ((0, 0), (0, S), (0, 0)))
        kf_pad = _pad(ssm_k, ((0, 0), (0, S)))
        kv_f = (lambda x, axes=(-2, -1): torch.fft.rfft2(x.float(), dim=axes))(kv_pad[:, None, :, :], axes=(1, 2))
        kf_f = (lambda x, axes=(-2, -1): torch.fft.rfft2(x.float(), dim=axes))(kf_pad[:, None, :], axes=(1, 2))
        kf_f = (lambda x, *d: x.permute(*d))(kf_f, 1, 2, 0)
        y_f = kv_f * kf_f[None, :, :, :]
        y = (lambda x, s, axes=(-2, -1): torch.fft.irfft2(x, s=s, dim=axes).to(torch.bfloat16))(y_f, (1, L), axes=(1, 2))[:, 0, :S, :]
        y = y + kv * ssm_D[None, None, :]
        return q * y

    def _B_ref(x, w_in, w_sp, b_sp, w_out):
        B, S, D = x.shape
        F = w_in.shape[-1]
        Fh = F // 2
        z = _gelu(torch.einsum('bsd,df->bsf', x, w_in))
        z1 = z[..., :Fh].clone()
        z2 = _layernorm(z[..., Fh:], axis=-1)
        g = torch.einsum('ts,bsf->btf', w_sp, z2) + b_sp[None, :, None]
        return torch.einsum('bsf,fd->bsd', z1 * g, w_out)
    x = A_q
    xn = _layernorm(x, axis=-1)
    s = _A_ref(xn, A_k, A_v, A_shift_k, A_ssm_k, A_ssm_D)
    g = _silu(_B_ref(xn, B_w_in, B_w_sp, B_b_sp, B_w_out))
    return x + s * g
```

```cpp
#define HTK 0
#define HTI 0
#define HTW 0
#define HTR 0
#include <hip/hip_runtime.h>
#include <hip/hip_bf16.h>
#include <stdint.h>
#include <type_traits>

typedef unsigned short u16;
typedef __attribute__((ext_vector_type(8))) short bf16x8;
typedef __attribute__((ext_vector_type(4))) short s16x4;
typedef __attribute__((ext_vector_type(4))) float f32x4;
typedef __attribute__((ext_vector_type(16))) float f32x16;
typedef __attribute__((ext_vector_type(4))) unsigned int u32x4;
typedef __attribute__((ext_vector_type(3))) unsigned int u32x3;
typedef __attribute__((ext_vector_type(2))) unsigned int u32x2;

#define LDS_AS __attribute__((address_space(3)))
#define IC(n) std::integral_constant<int, n>{}
#define SB() __builtin_amdgcn_sched_barrier(0)

#define STAMP(k, i) do {} while (0)
#define STAMPW(k, i, w) do {} while (0)
#define TS_DECL do {} while (0)
#define TS(i) do {} while (0)
#define TS_OUT(k, base, n, w) do {} while (0)

#define RT(k, i) do {} while (0)

#define AB_DECL do {} while (0)
#define AB(i) do {} while (0)
#define ABS(i) do {} while (0)
#define AB_OUT(n) do {} while (0)

#define AK_DECL do {} while (0)
#define AK(i) do {} while (0)
#define AK_OUT(n) do {} while (0)

#undef AK_DECL
#undef AK
#undef AK_OUT
#undef AB_DECL
#undef AB
#undef AB_OUT
__device__ unsigned long long g_ak[256][8][16];
#define AK_DECL unsigned long long ak_[16] = {}
#define AK(i) ak_[i] = __builtin_amdgcn_s_memrealtime()
#define AK_OUT(n) do { if ((threadIdx.x & 63) == 0 && threadIdx.x < 512) for (int i_ = 0; i_ < n; ++i_) g_ak[blockIdx.x][threadIdx.x >> 6][i_] = ak_[i_] ? ak_[i_] : ak_[0]; } while (0)
#define AB_DECL unsigned long long ab_[16]
#define AB(i) ab_[i] = __builtin_amdgcn_s_memrealtime()
#define AB_OUT(n) do { if ((threadIdx.x >> 6) == (HTK ? HTW : 0)) { unsigned long long ref_; \
  if (HTK) ref_ = ab_[HTI]; else { \
    unsigned long long b0_ = g_ak[0][HTW][0]; long long acc_ = HTR == 0 ? (1ll << 60) : (HTR == 1 ? -(1ll << 60) : 0); \
    for (int q_ = 0; q_ < 4; ++q_) { long long v_ = (long long)(g_ak[(threadIdx.x & 63) * 4 + q_][HTW][HTI] - b0_); acc_ = HTR == 0 ? (v_ < acc_ ? v_ : acc_) : (HTR == 1 ? (v_ > acc_ ? v_ : acc_) : acc_ + v_); } \
    for (int o_ = 32; o_ > 0; o_ >>= 1) { long long v_ = __shfl_xor(acc_, o_); acc_ = HTR == 0 ? (v_ < acc_ ? v_ : acc_) : (HTR == 1 ? (v_ > acc_ ? v_ : acc_) : acc_ + v_); } \
    if (HTR == 2) acc_ /= 256; ref_ = b0_ + acc_; } \
  while (__builtin_amdgcn_s_memrealtime() < ref_ + 6000) __builtin_amdgcn_s_sleep(1); } } while (0)
#define MAPM 0
#define XE 0
constexpr int S_ = 2048, D_ = 640, FH_ = 768, F2_ = 1536;

__device__ __forceinline__ float bf2f(u16 v) { return __uint_as_float(((unsigned)v) << 16); }
typedef __attribute__((ext_vector_type(2))) __bf16 bf16x2n;
typedef __attribute__((ext_vector_type(2))) float f32x2;
__device__ __forceinline__ unsigned pack2(float lo, float hi) {
  f32x2 v = {lo, hi};
  bf16x2n r = __builtin_convertvector(v, bf16x2n);
  return __builtin_bit_cast(unsigned, r);
}
__device__ __forceinline__ float lo16(unsigned w) { return __uint_as_float(w << 16); }
__device__ __forceinline__ float hi16(unsigned w) { return __uint_as_float(w & 0xFFFF0000u); }
__device__ __forceinline__ u16 f2bf(float f) { return (u16)(pack2(f, f) & 0xFFFFu); }
__device__ __forceinline__ float rbf(float f) { return lo16(pack2(f, f)); }

template <int CTRL>
__device__ __forceinline__ float dppf(float v) {
  return __builtin_bit_cast(float, __builtin_amdgcn_update_dpp(0, __builtin_bit_cast(int, v), CTRL, 0xF, 0xF, true));
}
__device__ __forceinline__ float sum16(float v) {
  v += dppf<0xB1>(v); v += dppf<0x4E>(v); v += dppf<0x141>(v); v += dppf<0x140>(v);
  return v;
}
__device__ __forceinline__ float gelu_tanh(float x) {
  const float kBeta = 0.7978845608028654f;
  const float kKappa = 0.044715f;
  float inner = kBeta * (x + kKappa * x * x * x);
  return x * __builtin_amdgcn_rcpf(1.0f + __expf(-2.0f * inner));
}

template <int NSUB>
__device__ __forceinline__ unsigned boff(unsigned row, unsigned ch) {
  return (unsigned)(NSUB * 512) * (row >> 3) + 512u * (ch >> 2) + 64u * (row & 7) + 16u * ((ch & 3) ^ ((row >> 2) & 3));
}
template <int OFF>
__device__ __forceinline__ s16x4 tr_asm(unsigned a) {
  s16x4 r;
  asm volatile("ds_read_b64_tr_b16 %0, %1 offset:%2" : "=&v"(r) : "v"(a), "i"(OFF) : "memory");
  return r;
}
struct TrBase { unsigned b[2][2]; };
template <int NSUB>
__device__ __forceinline__ TrBase tr_base(unsigned lane, unsigned ldsbase) {
  const unsigned g = lane >> 4, q = (lane & 15) >> 2, p = lane & 3;
  TrBase t;
#pragma unroll
  for (int c1 = 0; c1 < 2; ++c1)
#pragma unroll
    for (int h = 0; h < 2; ++h)
      t.b[c1][h] = ldsbase + boff<NSUB>(8 * g + 4 * h + q, 2 * c1 + (p >> 1)) + 8 * (p & 1);
  return t;
}
template <int NSUB, int KS, int C>
__device__ __forceinline__ void tr_frag_issue(const TrBase& t, unsigned stage_off, s16x4& lo, s16x4& hi) {
  constexpr int OFF = NSUB * 512 * 4 * KS + 512 * (C >> 1);
  lo = tr_asm<OFF>(t.b[C & 1][0] + stage_off);
  hi = tr_asm<OFF>(t.b[C & 1][1] + stage_off);
}
__device__ __forceinline__ bf16x8 join8(s16x4 lo, s16x4 hi) {
  bf16x8 r;
  r[0] = lo[0]; r[1] = lo[1]; r[2] = lo[2]; r[3] = lo[3];
  r[4] = hi[0]; r[5] = hi[1]; r[6] = hi[2]; r[7] = hi[3];
  return r;
}
#define WAIT_LGKM0() asm volatile("s_waitcnt lgkmcnt(0)" ::: "memory")
__device__ __forceinline__ unsigned lds_addr(const void* p) {
  return (unsigned)(uintptr_t)(LDS_AS const char*)p;
}

#define RAW_BARRIER() do { asm volatile("s_waitcnt lgkmcnt(0)" ::: "memory"); __builtin_amdgcn_s_barrier(); } while (0)
#define WAIT_VM(n) asm volatile("s_waitcnt vmcnt(" #n ")" ::: "memory")

__device__ __forceinline__ void flag_wait(unsigned faddr, unsigned target) {
  while (true) {
    unsigned v;
    asm volatile("ds_read_b32 %0, %1\n\ts_waitcnt lgkmcnt(0)" : "=&v"(v) : "v"(faddr) : "memory");
    v = __builtin_amdgcn_readfirstlane(v);
    if (v >= target) break;
    __builtin_amdgcn_s_sleep(1);
  }
}
__device__ __forceinline__ void flag_wait_asm(unsigned faddr, unsigned target) {
  unsigned v, sv;
  asm volatile(".Lfw_%=:\n\tds_read_b32 %0, %2\n\ts_waitcnt lgkmcnt(0)\n\tv_readfirstlane_b32 %1, %0\n\ts_nop 0\n\t"
               "s_cmp_ge_u32 %1, %3\n\ts_cbranch_scc1 .Lfd_%=\n\ts_sleep 1\n\ts_branch .Lfw_%=\n\t.Lfd_%=:"
               : "=&v"(v), "=&s"(sv) : "v"(faddr), "s"(target) : "memory", "scc");
}
__device__ __forceinline__ void flag_signal(unsigned faddr, int lane) {
  if (lane == 0) {
    unsigned one = 1u;
    asm volatile("ds_add_u32 %0, %1" :: "v"(faddr), "v"(one) : "memory");
  }
}
__device__ __forceinline__ void glds16(const void* g, char* lds) {
  __builtin_amdgcn_global_load_lds((const unsigned*)g, (unsigned*)lds, 16, 0, 0);
}
__device__ __forceinline__ void glds_a(const u16* gbase, int ld, char* stageA, int ins, int lane) {
  const int row = ins * 8 + (lane >> 3);
  const int ch = (lane & 7) ^ (row & 7);
  glds16(gbase + (size_t)row * ld + ch * 8, stageA + ins * 1024);
}
__device__ __forceinline__ bf16x8 read_a(const char* stageA, int row, int kchunk) {
  return *(const bf16x8*)(stageA + row * 128 + ((kchunk ^ (row & 7)) << 4));
}
template <int NSUB, int BNV>
__device__ __forceinline__ void glds_b(const u16* gbase, int ld, char* stageB, int ins, int lane) {
  const int o = ins * 1024 + lane * 16;
  const int rg = o / (NSUB * 512), rem = o % (NSUB * 512);
  const int sub = rem >> 9, rr = (rem >> 6) & 7, slot = (rem >> 4) & 3;
  const int row = rg * 8 + rr;
  int ch = sub * 4 + (slot ^ ((row >> 2) & 3));
  if (BNV < NSUB * 32) ch = min(ch, BNV / 8 - 1);
  glds16(gbase + (size_t)row * ld + ch * 8, stageB + ins * 1024);
}

constexpr int L1_A_BYTES = 64 * 128;
constexpr int L1_B_BYTES = 64 * 192 * 2;
constexpr int L1_STAGE = L1_A_BYTES + L1_B_BYTES;
constexpr int L1_LDS = 5 * L1_STAGE;
constexpr unsigned HOLE = 0xFFFFFFFFu;
constexpr int Z1X = S_ * FH_ * 2;
constexpr int KVX = D_ * S_ * 2;
#define S0 7

__device__ __forceinline__ f32x2 unpk(unsigned w) { f32x2 r = {lo16(w), hi16(w)}; return r; }
__device__ __forceinline__ unsigned pk2(f32x2 v) { return pack2(v[0], v[1]); }
__device__ __forceinline__ f32x2 gelu2(f32x2 x) {
  const f32x2 c1 = {-2.3022082f, -2.3022082f}, c2 = {-0.10294324f, -0.10294324f};
  const f32x2 a = x * __builtin_elementwise_fma(x * x, c2, c1);
  f32x2 e = {__builtin_amdgcn_exp2f(a[0]), __builtin_amdgcn_exp2f(a[1])};
  e = e + (f32x2){1.0f, 1.0f};
  const f32x2 r = {__builtin_amdgcn_rcpf(e[0]), __builtin_amdgcn_rcpf(e[1])};
  return x * r;
}
__device__ __forceinline__ float allred_8(float v) {
  v += __uint_as_float(__builtin_amdgcn_mov_dpp(__float_as_uint(v), 0xB1, 0xF, 0xF, true));
  v += __uint_as_float(__builtin_amdgcn_mov_dpp(__float_as_uint(v), 0x4E, 0xF, 0xF, true));
  v += __uint_as_float(__builtin_amdgcn_mov_dpp(__float_as_uint(v), 0x141, 0xF, 0xF, true));
  return v;
}
__device__ __forceinline__ float allred_g(float v) {
  const auto a = __builtin_amdgcn_permlane16_swap(__float_as_uint(v), __float_as_uint(v), false, false);
  v = __uint_as_float(a[0]) + __uint_as_float(a[1]);
  const auto c = __builtin_amdgcn_permlane32_swap(__float_as_uint(v), __float_as_uint(v), false, false);
  return __uint_as_float(c[0]) + __uint_as_float(c[1]);
}

#define WT1 0
#define WTS "sc1"
#define ST16(p, v, bit) do { if (WT1 & (bit)) asm volatile("global_store_dwordx4 %0, %1, off " WTS "\n\ts_nop 1" :: "v"(p), "v"(v) : "memory"); else *(u32x4*)(p) = (v); } while (0)
__global__ __launch_bounds__(768) void k_l1(
    const u16* __restrict__ x, const u16* __restrict__ kin, const u16* __restrict__ vin,
    const u16* __restrict__ shift_k, const u16* __restrict__ w_in,
    u16* __restrict__ z1, u16* __restrict__ z2, float2* __restrict__ st2, float2* __restrict__ xst,
    u16* __restrict__ kvT, const u16* __restrict__ ssm_k, const u16* __restrict__ w_out, const u16* __restrict__ w_sp,
    u16* __restrict__ woF) {
  extern __shared__ __attribute__((aligned(16))) char smem[];
  const int tid = threadIdx.x, lane = tid & 63, wave = __builtin_amdgcn_readfirstlane(tid >> 6);
  const int b = blockIdx.x;
  const int xcd = b & 7, idx = b >> 3;
  const int rb = 8 * (xcd >> 1) + (idx >> 2), cb = 4 * (xcd & 1) + (idx & 3);
  const int row0 = rb * 64;
  const int n0 = cb * 192;
  const int g4 = lane >> 4, l15 = lane & 15;
  STAMP(0, 0);
  RT(0, 0);
  AK_DECL;
  AK(0);

  char* sE = smem + L1_STAGE;
  float2* sRow = (float2*)sE;
  float* sCs = (float*)(sE + 1024);
  float* sStat = (float*)(sE + 2048);
  char* sHand = sE + 8192;
  char* sTr = sE + 40960 + wave * 2048;
  const bool is_z2 = cb >= 4;
  auto epi = [&](const f32x4 (&t)[2][2], int wm, int cg, int swp, int rsw) {
    f32x4 csv[2];
#pragma unroll
    for (int jj = 0; jj < 2; ++jj) csv[jj] = *(const f32x4*)(sCs + cg * 32 + (jj ^ swp) * 16 + 4 * g4);
    f32x2 gv[2][2][2];
    unsigned pk[2][2][2];
#pragma unroll
    for (int i = 0; i < 2; ++i) {
      const float2 rs = sRow[wm * 32 + (i ^ rsw) * 16 + l15];
      const f32x2 nm = {-rs.x, -rs.x}, rd = {rs.y, rs.y};
#pragma unroll
      for (int jj = 0; jj < 2; ++jj)
#pragma unroll
        for (int h = 0; h < 2; ++h) {
          const f32x2 av = {t[i][jj][2 * h], t[i][jj][2 * h + 1]};
          const f32x2 cv = {csv[jj][2 * h], csv[jj][2 * h + 1]};
          gv[i][jj][h] = gelu2(__builtin_elementwise_fma(nm, cv, av) * rd);
          pk[i][jj][h] = pk2(gv[i][jj][h]);
        }
    }
    if (!is_z2) {
      const int lp = l15 + 16 * (2 * ((g4 & 1) ^ swp) + (g4 >> 1));
      char* zdst = (char*)z1 + (size_t)((rb * 4 + wm * 2) * 24 + cb * 6 + cg) * 1024 + lp * 16;
      u32x4 o[2];
#pragma unroll
      for (int i = 0; i < 2; ++i) {
        const auto sa = __builtin_amdgcn_permlane16_swap(pk[i][0][0], pk[i][1][0], false, false);
        const auto sb = __builtin_amdgcn_permlane16_swap(pk[i][0][1], pk[i][1][1], false, false);
        o[i][0] = sa[0]; o[i][1] = sb[0]; o[i][2] = sa[1]; o[i][3] = sb[1];
      }
#pragma unroll
      for (int i = 0; i < 2; ++i) ST16(zdst + (i ^ rsw) * 24 * 1024, ((u32x4){o[i][0], o[i][1], o[i][2], HOLE}), 1);
      *(u32x2*)((char*)z1 + Z1X + ((rb * 2 + wm) * 24 + cb * 6 + cg) * 512 + lp * 8) = (u32x2){rsw ? o[1][3] : o[0][3], rsw ? o[0][3] : o[1][3]};
    } else {
#pragma unroll
      for (int jj = 0; jj < 2; ++jj) {
        char* wb = sTr + l15 * 64 + 8 * ((4 * (jj ^ swp) + g4) ^ (l15 >> 1));
#pragma unroll
        for (int i = 0; i < 2; ++i) *(u32x2*)(wb + (i ^ rsw) * 1024) = (u32x2){pk[i][jj][0], pk[i][jj][1]};
      }
      const unsigned q = l15 >> 2, p = lane & 3;
      const unsigned ra = lds_addr(sTr) + (8 * g4 + q) * 64 + 8 * (p ^ (4 * (g4 & 1) + (q >> 1)));
      s16x4 tr[2][2];
      tr[0][0] = tr_asm<0>(ra); tr[0][1] = tr_asm<256>(ra ^ 16u);
      tr[1][0] = tr_asm<0>(ra ^ 32u); tr[1][1] = tr_asm<256>(ra ^ 48u);
      float sv[2][2], pv[2];
#pragma unroll
      for (int i = 0; i < 2; ++i) {
        const unsigned p0 = __float_as_uint(gv[i][0][0][0]);
        const auto pa = __builtin_amdgcn_permlane16_swap(p0, p0, false, false);
        const auto pb = __builtin_amdgcn_permlane32_swap(pa[0], pa[0], false, false);
        pv[i] = __uint_as_float(pb[0]);
        const f32x2 pp = {pv[i], pv[i]};
        const f32x2 d0 = gv[i][0][0] - pp, d1 = gv[i][0][1] - pp, d2 = gv[i][1][0] - pp, d3 = gv[i][1][1] - pp;
        const f32x2 s1 = (d0 + d1) + (d2 + d3);
        const f32x2 s2 = __builtin_elementwise_fma(d0, d0, d1 * d1) + __builtin_elementwise_fma(d2, d2, d3 * d3);
        sv[i][0] = s1[0] + s1[1]; sv[i][1] = s2[0] + s2[1];
      }
      const auto u0 = __builtin_amdgcn_permlane16_swap(__float_as_uint(sv[0][0]), __float_as_uint(sv[0][1]), false, false);
      const auto u1 = __builtin_amdgcn_permlane16_swap(__float_as_uint(sv[1][0]), __float_as_uint(sv[1][1]), false, false);
      const float w0 = __uint_as_float(u0[0]) + __uint_as_float(u0[1]), w1 = __uint_as_float(u1[0]) + __uint_as_float(u1[1]);
      const auto u2 = __builtin_amdgcn_permlane32_swap(__float_as_uint(w0), __float_as_uint(w1), false, false);
      float* sd = sStat + ((wm * 32 + ((g4 >> 1) ^ rsw) * 16 + l15) * 6 + cg) * 4 + (g4 & 1);
      sd[0] = __uint_as_float(u2[0]) + __uint_as_float(u2[1]);
      sd[2] = (g4 >> 1) ? pv[1] : pv[0];
      asm volatile("s_waitcnt lgkmcnt(0)" : "+v"(tr[0][0]), "+v"(tr[0][1]), "+v"(tr[1][0]), "+v"(tr[1][1]) :: "memory");
      char* zdst = (char*)z2 + (size_t)((rb * 2 + wm) * 48 + (cb - 4) * 12 + cg * 2) * 1024 + lane * 16;
      const bool tag = ((rb * 2 + wm) & 15) >= S0;
      u32x4 o[2];
#pragma unroll
      for (int jj = 0; jj < 2; ++jj) {
        o[jj] = __builtin_bit_cast(u32x4, join8(tr[jj][0], tr[jj][1]));
        ST16(zdst + jj * 1024, ((u32x4){o[jj][0], o[jj][1], o[jj][2], tag ? HOLE : o[jj][3]}), 2);
      }
      *(u32x2*)((char*)z2 + Z1X + ((rb * 2 + wm) * 24 + (cb - 4) * 6 + cg) * 512 + lane * 8) = (u32x2){o[0][3], o[1][3]};
    }
  };

  if (__builtin_expect(wave >= 4, 1)) {
    const int lw = wave - 4;
    const unsigned voA = (unsigned)((lw * 8 + (lane >> 3)) * (D_ * 2) + (((lane & 7) ^ (lane >> 3)) << 4));
    const unsigned voB = (unsigned)((lw * 8 + ((lane >> 2) & 7)) * (F2_ * 2) + (lane >> 5) * 64 + (((lane & 3) ^ ((lw * 2 + ((lane >> 4) & 1)) & 3)) << 4));
    const char* gA = (const char*)x + (size_t)row0 * (D_ * 2);
    const char* gB = (const char*)w_in + n0 * 2;
    const unsigned lbase = __builtin_amdgcn_readfirstlane(lds_addr(smem));
    auto issue = [&](int kt) {
      const unsigned st = lbase + (kt % 5) * L1_STAGE;
      asm volatile("s_mov_b32 m0, %2\n\ts_nop 0\n\tglobal_load_lds_dwordx4 %0, %1" :: "v"(voA), "s"(gA + kt * 128), "s"(st + lw * 1024) : "memory");
#pragma unroll
      for (int i = 0; i < 3; ++i)
        asm volatile("s_mov_b32 m0, %2\n\ts_nop 0\n\tglobal_load_lds_dwordx4 %0, %1"
                     :: "v"(voB), "s"(gB + (size_t)kt * (64 * F2_ * 2) + i * 128), "s"(st + L1_A_BYTES + (lw * 3 + i) * 1024) : "memory");
    };
#define LPRIO 2
    __builtin_amdgcn_s_setprio(LPRIO);
    issue(0);
    AK(1);
    __builtin_amdgcn_s_barrier();
    issue(1); issue(2);
    AK(2);
    const int ks0 = row0 + lw * 8, kd = cb * 80 + min(lane, 39) * 2;
    const int wfr = ((xcd >> 1) * 32 + idx) * 8 + lw, wfi = min(wfr, 959);
    u32x4 sw;
    const char* kvp[3];
#pragma unroll
    for (int i = 0; i < 3; ++i) {
      const int id = min(i * 64 + lane, 189), r = id / 10, c = id % 10;
      kvp[i] = (r < 11 ? (const char*)kin + (size_t)max(ks0 - 3 + r, 0) * (D_ * 2) : (const char*)vin + (size_t)(ks0 + r - 11) * (D_ * 2)) + cb * 160 + c * 16;
    }
    auto kvload = [&]() {
#pragma unroll
      for (int i = 0; i < 3; ++i)
        asm volatile("s_mov_b32 m0, %1\n\ts_nop 0\n\tglobal_load_lds_dwordx4 %0, off" :: "v"(kvp[i]), "s"(lbase + lw * 3072 + i * 1024) : "memory");
      asm volatile("s_mov_b32 m0, %2\n\ts_nop 0\n\tglobal_load_lds_dwordx4 %0, %1"
                   :: "v"((unsigned)((lane >> 1) * (D_ * 2) + (lane & 1) * 16)), "s"(w_out + (size_t)(wfi / 40) * 32 * D_ + (wfi % 40) * 16), "s"(lbase + 24576 + lw * 1024) : "memory");
      asm volatile("global_load_dwordx4 %0, %1, %2" : "=&v"(sw) : "v"(kd * 8), "s"(shift_k) : "memory");
    };
    auto womath = [&]() {
      const unsigned ra = lbase + 24576 + lw * 1024 + (8 * g4 + (l15 >> 2)) * 32 + 8 * (lane & 3);
      s16x4 lo = tr_asm<0>(ra), hi = tr_asm<128>(ra);
      asm volatile("s_waitcnt lgkmcnt(0)" : "+v"(lo), "+v"(hi) :: "memory");
      ST16((char*)woF + (size_t)wfi * 1024 + lane * 16, __builtin_bit_cast(u32x4, join8(lo, hi)), 4);
    };
    auto kvmath = [&]() {
      unsigned kk[11], vv[8];
      const char* kl = smem + lw * 3072 + min(lane, 39) * 4;
#pragma unroll
      for (int i = 0; i < 11; ++i) kk[i] = *(const unsigned*)(kl + i * 160);
#pragma unroll
      for (int i = 0; i < 8; ++i) vv[i] = *(const unsigned*)(kl + (11 + i) * 160);
      const f32x2 w0 = {lo16(sw[0]), lo16(sw[2])}, w1 = {hi16(sw[0]), hi16(sw[2])};
      const f32x2 w2 = {lo16(sw[1]), lo16(sw[3])}, w3 = {hi16(sw[1]), hi16(sw[3])};
      f32x2 kf[11], rr[8];
#pragma unroll
      for (int i = 0; i < 11; ++i) kf[i] = unpk((i < 3 && ks0 == 0) ? 0u : kk[i]);
#pragma unroll
      for (int i = 0; i < 8; ++i) {
        f32x2 a0 = kf[i] * w3;
        a0 = __builtin_elementwise_fma(kf[i + 1], w2, a0);
        a0 = __builtin_elementwise_fma(kf[i + 2], w1, a0);
        a0 = __builtin_elementwise_fma(kf[i + 3], w0, a0);
        rr[i] = unpk(pk2(a0)) * unpk(vv[i]);
      }
      u32x4 oa, ob;
#pragma unroll
      for (int e = 0; e < 4; ++e) { oa[e] = pack2(rr[2 * e][0], rr[2 * e + 1][0]); ob[e] = pack2(rr[2 * e][1], rr[2 * e + 1][1]); }
      if (lane < 40) {
        ST16(kvT + (size_t)kd * S_ + ks0, ((u32x4){oa[0], oa[1], oa[2], HOLE}), 8);
        ST16(kvT + (size_t)(kd + 1) * S_ + ks0, ((u32x4){ob[0], ob[1], ob[2], HOLE}), 8);
        *(u32x2*)((char*)kvT + KVX + kd * 1024 + ks0) = (u32x2){oa[3], ob[3]};
      }
    };
#define PFK 8
#define PFF 4
#define KVM 1
#define SPF 0
#define SP0 1
#define SPN 3
    unsigned long long pfs = 0;
    const char* spb = (const char*)w_sp + (size_t)(row0 + (SPF == 1 ? cb * 8 + lw : (idx & 3) * 16 + lw * 2)) * 4096;
    auto spf = [&](int i) {
      const int r = i & (SPF == 1 ? 0 : 1), k = SPF == 1 ? i : i >> 1;
      asm volatile("s_load_dwordx2 %0, %1, %2" : "+s"(pfs) : "s"(spb), "i"(r * 4096 + (k & 3) * 1024 + (k >> 2) * 256 + 124) : "memory");
    };
    constexpr int NPF = PFF == 1 ? 2 : 1;
    unsigned pfv, pfw = 0;
    auto prefetch = [&]() {
      const int t_ = tid - 256;
      if (PFF == 1) {
        const char* pw = (const char*)w_sp + (size_t)(row0 + (idx & 3) * 16 + (t_ >> 5)) * 4096 + (t_ & 31) * 128;
        const int t2 = t_ % 96;
        const char* pa = (const char*)ssm_k + (t2 < 64 ? (size_t)b * 8192 + t2 * 128 : (size_t)(512 + (b & 127)) * 4096 + (t2 & 31) * 128);
        asm volatile("global_load_dword %0, %1, off" : "=&v"(pfw) : "v"(pa) : "memory");
        asm volatile("global_load_dword %0, %1, off" : "=&v"(pfv) : "v"(pw) : "memory");
      } else {
        constexpr int NW = 64 * PFF;
        const int t2 = min(max(t_ - NW, 0), 95);
        const char* pw = (const char*)w_sp + (size_t)(row0 + (idx & 3) * 16 + (t_ / (4 * PFF)) % 16) * 4096 + ((t_ / PFF) & 3) * 1024 + (t_ % PFF) * 128;
        const char* pa = (const char*)ssm_k + (t2 < 64 ? (size_t)b * 8192 + t2 * 128 : (size_t)(512 + (b & 127)) * 4096 + (t2 & 31) * 128);
        const char* pc_ = (!SPF && t_ < NW) ? pw : pa;
        asm volatile("global_load_dword %0, %1, off" : "=&v"(pfv) : "v"(pc_) : "memory");
      }
    };
#pragma unroll
    for (int kt = 0; kt < 10; ++kt) {
      const int nv = (kt + 1 < 10 ? 4 : 0) + (kt + 2 < 10 ? 4 : 0) + (kt >= 8 ? 5 : 0) + ((PFK < kt && kt <= PFK + 3) ? NPF : 0);
      asm volatile("s_waitcnt vmcnt(%0)" :: "n"(nv) : "memory");
      __builtin_amdgcn_s_barrier();
      if (kt == 0) AK(3); if (kt == 1) AK(4); if (kt == 3) AK(5); if (kt == 5) AK(6); if (kt == 7) AK(7); if (kt == 9) AK(8);
      if (kt + 3 < 10) issue(kt + 3);
      if (kt == 7) kvload();
      if (kt == PFK) prefetch();
      if (SPF && kt >= SP0) {
#pragma unroll
        for (int i = (kt - SP0) * SPN; i < (kt - SP0 + 1) * SPN && i < 16 * SPF; ++i) spf(i);
      }
    }
    __builtin_amdgcn_s_setprio(0);
    if (KVM == 0) {
      asm volatile("s_waitcnt vmcnt(%1)" : "+v"(sw) : "n"(PFK >= 7 ? NPF : 0) : "memory");
      kvmath();
      if (!is_z2 && wfr < 960) womath();
    }
    SB();
    asm volatile("s_waitcnt lgkmcnt(0)" : "+s"(pfs) :: "memory");
    RAW_BARRIER();
    AK(9);
    {
      const int w = lw >> 1, gs = lw & 1;
      f32x4 t4[2][2];
#pragma unroll
      for (int q = 0; q < 4; ++q) t4[q >> 1][q & 1] = *(const f32x4*)(sHand + ((w * 2 + gs) * 4 + q) * 1024 + lane * 16);
      epi(t4, w >> 1, (w & 1) * 3 + 1 + gs, 0, 0);
    }
    AK(10);
    if (is_z2) RAW_BARRIER();
    AK(11);
    if (KVM == 1) {
      SB();
      if (is_z2) asm volatile("s_waitcnt vmcnt(%0)" :: "n"(2 + (PFK >= 7 ? NPF : 0)) : "memory");
      asm volatile("s_waitcnt vmcnt(%1)" : "+v"(sw) : "n"(3 + (PFK >= 7 ? NPF : 0)) : "memory");
      kvmath();
      if (!is_z2 && wfr < 960) womath();
    }
    AK(12);
    asm volatile("s_waitcnt vmcnt(0)" : "+v"(pfw), "+v"(pfv) :: "memory");
    AK(13);
    AK_OUT(14);
    RT(0, 1);
    return;
  }

  unsigned pcv, pfx;
  {
    unsigned long long pc; unsigned clen;
    asm volatile("s_getpc_b64 %0\n.Lpc_here_%=:\n\ts_mov_b32 %1, k_end-.Lpc_here_%=" : "=s"(pc), "=s"(clen));
    const unsigned o = idx < 2 ? min((unsigned)((idx * 4 + wave) * 8192 + lane * 128), clen - 4u) & ~3u : 0u;
    asm volatile("global_load_dword %0, %1, %2" : "=&v"(pcv) : "v"(o), "s"(pc) : "memory");
  }
  __builtin_amdgcn_s_barrier();
  auto pfx_issue = [&]() {
    const int t2 = max(tid - 80, 0), r2 = min(t2 / 3, 39);
    const char* pa = (const char*)x + (size_t)(row0 + (idx & 3) * 16 + tid / 5) * (D_ * 2) + 640 + (tid % 5) * 128;
    const char* pb = (const char*)w_in + (size_t)(320 + (idx >> 2) * 40 + r2) * (F2_ * 2) + n0 * 2 + (t2 % 3) * 128;
    const char* pc_ = tid < 80 ? pa : pb;
    asm volatile("global_load_dword %0, %1, off" : "=&v"(pfx) : "v"(pc_) : "memory");
  };
#define PFXM 0
  if (PFXM == 0) pfx_issue();
  if (PFXM == 2) pfx = 0;
  const int wm = wave >> 1, wn = wave & 1;
  f32x4 acc[2][6], acs[3], accS = {0.f, 0.f, 0.f, 0.f}, accG = {0.f, 0.f, 0.f, 0.f};
#pragma unroll
  for (int j = 0; j < 6; ++j) {
    acc[0][j] = (f32x4){0.f, 0.f, 0.f, 0.f};
    acc[1][j] = (f32x4){0.f, 0.f, 0.f, 0.f};
  }
#pragma unroll
  for (int j = 0; j < 3; ++j) acs[j] = (f32x4){0.f, 0.f, 0.f, 0.f};
  unsigned tb[2][2], aA[2][2];
#pragma unroll
  for (int c1 = 0; c1 < 2; ++c1)
#pragma unroll
    for (int h = 0; h < 2; ++h)
      tb[c1][h] = lds_addr(smem) + L1_A_BYTES + wn * 3 * 512 + boff<6>(8 * g4 + 4 * h + (l15 >> 2), 2 * (c1 ^ wm) + ((lane & 3) >> 1)) + 8 * (lane & 1);
#pragma unroll
  for (int ks = 0; ks < 2; ++ks)
#pragma unroll
    for (int i = 0; i < 2; ++i) aA[ks][i] = lds_addr(smem) + (wm * 32 + (i ^ wn) * 16 + l15) * 128 + (((ks * 4 + g4) ^ (l15 & 7)) << 4);
  bf16x8 ones;
#pragma unroll
  for (int e = 0; e < 8; ++e) ones[e] = (short)0x3F80;
  int cnt = 10, slot = 0, sdl;
  SB();
  __builtin_amdgcn_s_barrier();
  STAMP(0, 1);
  if (PFXM == 1) pfx_issue();
  asm volatile(
      "s_nop 1\n\t"
      "ds_read_b128 v[96:99], %21\n\t"
      "ds_read_b64_tr_b16 v[104:105], %17 offset:0\n\t"
      "ds_read_b64_tr_b16 v[106:107], %18 offset:0\n\t"
      "ds_read_b128 v[100:103], %22\n\t"
      "ds_read_b64_tr_b16 v[108:109], %19 offset:0\n\t"
      "ds_read_b64_tr_b16 v[110:111], %20 offset:0\n\t"
      "ds_read_b64_tr_b16 v[112:113], %17 offset:512\n\t"
      "ds_read_b64_tr_b16 v[114:115], %18 offset:512\n\t"
      "ds_read_b64_tr_b16 v[116:117], %19 offset:512\n\t"
      "ds_read_b64_tr_b16 v[118:119], %20 offset:512\n\t"
      "ds_read_b64_tr_b16 v[120:121], %17 offset:1024\n\t"
      "ds_read_b64_tr_b16 v[122:123], %18 offset:1024\n\t"
      "ds_read_b64_tr_b16 v[124:125], %19 offset:1024\n\t"
      "ds_read_b64_tr_b16 v[126:127], %20 offset:1024\n\t"
      ".Ll1_%=:\n\t"
      "s_waitcnt lgkmcnt(11)\n\t"
      "v_mfma_f32_16x16x32_bf16 %0, v[104:107], v[96:99], %0\n\t"
      "ds_read_b128 v[128:131], %23\n\t"
      "s_waitcnt lgkmcnt(11)\n\t"
      "v_mfma_f32_16x16x32_bf16 %6, v[104:107], v[100:103], %6\n\t"
      "ds_read_b64_tr_b16 v[136:137], %17 offset:12288\n\t"
      "s_waitcnt lgkmcnt(10)\n\t"
      "v_mfma_f32_16x16x32_bf16 %1, v[108:111], v[96:99], %1\n\t"
      "ds_read_b64_tr_b16 v[138:139], %18 offset:12288\n\t"
      "v_mfma_f32_16x16x32_bf16 %7, v[108:111], v[100:103], %7\n\t"
      "ds_read_b128 v[132:135], %24\n\t"
      "s_waitcnt lgkmcnt(10)\n\t"
      "v_mfma_f32_16x16x32_bf16 %2, v[112:115], v[96:99], %2\n\t"
      "ds_read_b64_tr_b16 v[140:141], %19 offset:12288\n\t"
      "v_mfma_f32_16x16x32_bf16 %8, v[112:115], v[100:103], %8\n\t"
      "ds_read_b64_tr_b16 v[142:143], %20 offset:12288\n\t"
      "s_waitcnt lgkmcnt(10)\n\t"
      "v_mfma_f32_16x16x32_bf16 %3, v[116:119], v[96:99], %3\n\t"
      "ds_read_b64_tr_b16 v[144:145], %17 offset:12800\n\t"
      "v_mfma_f32_16x16x32_bf16 %9, v[116:119], v[100:103], %9\n\t"
      "ds_read_b64_tr_b16 v[146:147], %18 offset:12800\n\t"
      "s_waitcnt lgkmcnt(10)\n\t"
      "v_mfma_f32_16x16x32_bf16 %4, v[120:123], v[96:99], %4\n\t"
      "ds_read_b64_tr_b16 v[148:149], %19 offset:12800\n\t"
      "v_mfma_f32_16x16x32_bf16 %10, v[120:123], v[100:103], %10\n\t"
      "ds_read_b64_tr_b16 v[150:151], %20 offset:12800\n\t"
      "s_waitcnt lgkmcnt(10)\n\t"
      "v_mfma_f32_16x16x32_bf16 %5, v[124:127], v[96:99], %5\n\t"
      "ds_read_b64_tr_b16 v[152:153], %17 offset:13312\n\t"
      "v_mfma_f32_16x16x32_bf16 %11, v[124:127], v[100:103], %11\n\t"
      "ds_read_b64_tr_b16 v[154:155], %18 offset:13312\n\t"
      "v_mfma_f32_16x16x32_bf16 %15, %28, v[96:99], %15\n\t"
      "ds_read_b64_tr_b16 v[156:157], %19 offset:13312\n\t"
      "v_mfma_f32_16x16x32_bf16 %16, v[96:99], v[96:99], %16\n\t"
      "ds_read_b64_tr_b16 v[158:159], %20 offset:13312\n\t"
      "v_mfma_f32_16x16x32_bf16 %12, v[104:107], %28, %12\n\t"
      "v_mfma_f32_16x16x32_bf16 %13, v[112:115], %28, %13\n\t"
      "v_mfma_f32_16x16x32_bf16 %14, v[120:123], %28, %14\n\t"
      "s_add_u32 %26, %26, 1\n\t"
      "s_cmp_eq_u32 %26, 5\n\t"
      "s_cselect_b32 %27, 0xfffd8000, 0\n\t"
      "s_cselect_b32 %26, 0, %26\n\t"
      "s_add_u32 %27, %27, 0x8000\n\t"
      "s_sub_u32 %25, %25, 1\n\t"
      "s_cmp_eq_u32 %25, 0\n\t"
      "s_cbranch_scc1 .Ll1s_%=\n\t"
      "s_barrier\n\t"
      ".Ll1s_%=:\n\t"
      "s_waitcnt lgkmcnt(11)\n\t"
      "v_mfma_f32_16x16x32_bf16 %0, v[136:139], v[128:131], %0\n\t"
      "v_add_u32 %21, %27, %21\n\t"
      "ds_read_b128 v[96:99], %21\n\t"
      "s_waitcnt lgkmcnt(11)\n\t"
      "v_mfma_f32_16x16x32_bf16 %6, v[136:139], v[132:135], %6\n\t"
      "v_add_u32 %17, %27, %17\n\t"
      "ds_read_b64_tr_b16 v[104:105], %17 offset:0\n\t"
      "s_waitcnt lgkmcnt(10)\n\t"
      "v_mfma_f32_16x16x32_bf16 %1, v[140:143], v[128:131], %1\n\t"
      "v_add_u32 %18, %27, %18\n\t"
      "ds_read_b64_tr_b16 v[106:107], %18 offset:0\n\t"
      "v_mfma_f32_16x16x32_bf16 %7, v[140:143], v[132:135], %7\n\t"
      "v_add_u32 %22, %27, %22\n\t"
      "ds_read_b128 v[100:103], %22\n\t"
      "s_waitcnt lgkmcnt(10)\n\t"
      "v_mfma_f32_16x16x32_bf16 %2, v[144:147], v[128:131], %2\n\t"
      "v_add_u32 %19, %27, %19\n\t"
      "ds_read_b64_tr_b16 v[108:109], %19 offset:0\n\t"
      "v_mfma_f32_16x16x32_bf16 %8, v[144:147], v[132:135], %8\n\t"
      "v_add_u32 %20, %27, %20\n\t"
      "ds_read_b64_tr_b16 v[110:111], %20 offset:0\n\t"
      "s_waitcnt lgkmcnt(10)\n\t"
      "v_mfma_f32_16x16x32_bf16 %3, v[148:151], v[128:131], %3\n\t"
      "v_add_u32 %23, %27, %23\n\t"
      "ds_read_b64_tr_b16 v[112:113], %17 offset:512\n\t"
      "v_mfma_f32_16x16x32_bf16 %9, v[148:151], v[132:135], %9\n\t"
      "v_add_u32 %24, %27, %24\n\t"
      "ds_read_b64_tr_b16 v[114:115], %18 offset:512\n\t"
      "s_waitcnt lgkmcnt(10)\n\t"
      "v_mfma_f32_16x16x32_bf16 %4, v[152:155], v[128:131], %4\n\t"
      "ds_read_b64_tr_b16 v[116:117], %19 offset:512\n\t"
      "v_mfma_f32_16x16x32_bf16 %10, v[152:155], v[132:135], %10\n\t"
      "ds_read_b64_tr_b16 v[118:119], %20 offset:512\n\t"
      "s_waitcnt lgkmcnt(10)\n\t"
      "v_mfma_f32_16x16x32_bf16 %5, v[156:159], v[128:131], %5\n\t"
      "ds_read_b64_tr_b16 v[120:121], %17 offset:1024\n\t"
      "v_mfma_f32_16x16x32_bf16 %11, v[156:159], v[132:135], %11\n\t"
      "ds_read_b64_tr_b16 v[122:123], %18 offset:1024\n\t"
      "v_mfma_f32_16x16x32_bf16 %15, %28, v[128:131], %15\n\t"
      "ds_read_b64_tr_b16 v[124:125], %19 offset:1024\n\t"
      "v_mfma_f32_16x16x32_bf16 %16, v[128:131], v[128:131], %16\n\t"
      "ds_read_b64_tr_b16 v[126:127], %20 offset:1024\n\t"
      "v_mfma_f32_16x16x32_bf16 %12, v[136:139], %28, %12\n\t"
      "v_mfma_f32_16x16x32_bf16 %13, v[144:147], %28, %13\n\t"
      "v_mfma_f32_16x16x32_bf16 %14, v[152:155], %28, %14\n\t"
      "s_cmp_lg_u32 %25, 0\n\t"
      "s_cbranch_scc1 .Ll1_%=\n\t"
      "s_waitcnt lgkmcnt(0)\n\t"
      "s_nop 15\n\t"
      "s_nop 7\n\t"
      : "+v"(acc[0][0]), "+v"(acc[0][1]), "+v"(acc[0][2]), "+v"(acc[0][3]), "+v"(acc[0][4]), "+v"(acc[0][5]),
        "+v"(acc[1][0]), "+v"(acc[1][1]), "+v"(acc[1][2]), "+v"(acc[1][3]), "+v"(acc[1][4]), "+v"(acc[1][5]),
        "+v"(acs[0]), "+v"(acs[1]), "+v"(acs[2]), "+v"(accS), "+v"(accG), "+v"(tb[0][0]), "+v"(tb[0][1]), "+v"(tb[1][0]), "+v"(tb[1][1]),
        "+v"(aA[0][0]), "+v"(aA[0][1]), "+v"(aA[1][0]), "+v"(aA[1][1]), "+s"(cnt), "+s"(slot), "=&s"(sdl)
      : "v"(ones)
      : "memory", "scc", "v96", "v97", "v98", "v99", "v100", "v101", "v102", "v103", "v104", "v105", "v106", "v107", "v108", "v109", "v110", "v111", "v112", "v113", "v114", "v115", "v116", "v117", "v118", "v119", "v120", "v121", "v122", "v123", "v124", "v125", "v126", "v127", "v128", "v129", "v130", "v131", "v132", "v133", "v134", "v135", "v136", "v137", "v138", "v139", "v140", "v141", "v142", "v143", "v144", "v145", "v146", "v147", "v148", "v149", "v150", "v151", "v152", "v153", "v154", "v155", "v156", "v157", "v158", "v159");
  STAMP(0, 3);
  AK(8);
#define PFC 0
  unsigned pfc = 0;
  auto pfc_issue = [&]() {
    const char* pw = (const char*)w_sp + (size_t)(row0 + (idx & 3) * 16 + (tid >> 4)) * 4096 + ((tid >> 2) & 3) * 1024 + (4 + (tid & 3)) * 128;
    asm volatile("global_load_dword %0, %1, off" : "=&v"(pfc) : "v"(pw) : "memory");
  };
  if (PFC == 1) pfc_issue();

#pragma unroll
  for (int gs = 0; gs < 2; ++gs)
#pragma unroll
    for (int q = 0; q < 4; ++q)
      *(f32x4*)(sHand + ((wave * 2 + gs) * 4 + (q ^ wm ^ (2 * wn))) * 1024 + lane * 16) = acc[q >> 1][2 + 2 * gs + (q & 1)];
  if (l15 == 0) {
#pragma unroll
    for (int j = 0; j < 3; ++j) *(f32x4*)(sCs + wn * 96 + (2 * j + wm) * 16 + 4 * g4) = acs[j];
  }
  {
    const int r = wm * 32 + wn * 16 + l15;
    const bool dg = g4 == (l15 >> 2);
    const float mean = accS[0] * (1.0f / 640.0f);
    const float s2 = (l15 & 2) ? ((l15 & 1) ? accG[3] : accG[2]) : ((l15 & 1) ? accG[1] : accG[0]);
    float sq = s2 - accS[0] * mean;
    if (__builtin_expect(__builtin_amdgcn_ballot_w64(dg && !(sq >= 1e-3f * accS[0] * mean)) != 0, 0)) {
      const f32x2 mm = {mean, mean};
      f32x2 q2 = {0.f, 0.f};
      const u32x4* xp = (const u32x4*)(x + (size_t)(row0 + r) * D_ + g4 * 160);
#pragma unroll 1
      for (int c = 0; c < 20; ++c) {
        const u32x4 xv = xp[c];
#pragma unroll
        for (int e = 0; e < 4; ++e) { const f32x2 d = unpk(xv[e]) - mm; q2 = __builtin_elementwise_fma(d, d, q2); }
      }
      sq = allred_g(q2[0] + q2[1]);
    }
    const float mbf = rbf(mean);
    const float vbf = rbf(fmaxf(sq, 0.f) * (1.0f / 640.0f));
    const float den = rbf(__builtin_amdgcn_sqrtf(rbf(vbf + 1e-5f)));
    const float rden = __builtin_amdgcn_rcpf(den);
    if (dg) {
      sRow[r] = make_float2(mbf, rden);
      if (cb == 0) xst[row0 + r] = make_float2(mbf, rden);
    }
  }
  TS_DECL;
  TS(0);
  AK(7);
  RAW_BARRIER();
  AK(9);
  TS(1);
  if (PFC == 2) pfc_issue();
  {
    f32x4 t4[2][2];
#pragma unroll
    for (int q = 0; q < 4; ++q) t4[q >> 1][q & 1] = acc[q >> 1][q & 1];
    epi(t4, wm, wn * 3, wm, wn);
  }
  TS(2); AK(10);
  if (is_z2) RAW_BARRIER();
  TS(3); AK(11);
  TS(4);
  if (is_z2 && tid < 64) {
    float mc[6];
    float sum = 0.f, m2 = 0.f;
#pragma unroll
    for (int c = 0; c < 6; ++c) {
      const f32x4 pp = *(const f32x4*)(sStat + (tid * 6 + c) * 4);
      const float dl = pp[0] * (1.0f / 32.0f);
      mc[c] = pp[2] + dl;
      sum += mc[c];
      m2 += fmaxf(pp[1] - pp[0] * dl, 0.f);
    }
    const float mb = sum * (1.0f / 6.0f);
    float dq = 0.f;
#pragma unroll
    for (int c = 0; c < 6; ++c) { const float dd = mc[c] - mb; dq += dd * dd; }
    st2[(size_t)(cb - 4) * S_ + row0 + tid] = make_float2(mb, m2 + 32.0f * dq);
  }
  TS(5); AK(12);
  asm volatile("s_waitcnt vmcnt(0)" : "+v"(pcv), "+v"(pfx), "+v"(pfc) :: "memory");
  TS(6); AK(13);
  AK_OUT(14);
  TS_OUT(0, 8, 7, TSW);
  STAMP(0, 4);
  RT(0, 1);
}

constexpr int CV_C = 4608, CV_CS = 4160, CV_BUF = CV_C + 2 * CV_CS;
__device__ __forceinline__ void conv_core(unsigned buf, int lane, f32x4 (&ac)[8]) {
  const int l15 = lane & 15, g4 = lane >> 4;
  const int c = (l15 + 1) & 1;
  unsigned aa[4];
#pragma unroll
  for (int k = 0; k < 4; ++k) aa[k] = buf + CV_C + c * CV_CS + (2031 - l15 + 8 * g4 - c) * 2 - 512 - 1024 * k;
  unsigned bb = buf + 512 + 32 * l15 - 32 + 16 * g4;
  u32x2 A[2][8][2];
  u32x4 B[2][8];
  constexpr int TT[36] = {7, 6, 5, 4, 7, 6, 5, 4, 7, 6, 5, 3, 7, 6, 5, 4, 7, 6, 3, 2, 7, 6, 5, 4, 7, 3, 2, 1, 7, 6, 5, 4, 3, 2, 1, 0};
  constexpr int AA[36] = {0, 0, 0, 0, 1, 1, 1, 1, 2, 2, 2, 0, 3, 3, 3, 2, 4, 4, 1, 0, 5, 5, 4, 3, 6, 2, 1, 0, 7, 6, 5, 4, 3, 2, 1, 0};
  auto ld1 = [&](int p, int n) {
    if (n < 16) {
      const int a = n >> 1, o = ((a & 1) ? 0 : 128) + 2 * (n & 1);
      asm volatile("ds_read2_b32 %0, %1 offset0:%2 offset1:%3" : "=&v"(A[p][(n >> 1) & 7][n & 1]) : "v"(aa[(a >> 1) & 3]), "i"(o), "i"(o + 1) : "memory");
    } else
      asm volatile("ds_read_b128 %0, %1 offset:%2" : "=&v"(B[p][n & 7]) : "v"(bb), "i"(512 * (n & 7)) : "memory");
  };
  auto next = [&](int dl) {
#pragma unroll
    for (int k = 0; k < 4; ++k) aa[k] += dl;
    bb += dl;
    SB();
  };
  auto land = [&](int p) {
    asm volatile("s_waitcnt lgkmcnt(0)" : "+v"(A[p][0][0]), "+v"(A[p][0][1]), "+v"(A[p][1][0]), "+v"(A[p][1][1]), "+v"(A[p][2][0]), "+v"(A[p][2][1]),
                 "+v"(A[p][3][0]), "+v"(A[p][3][1]), "+v"(A[p][4][0]), "+v"(A[p][4][1]), "+v"(A[p][5][0]), "+v"(A[p][5][1]), "+v"(A[p][6][0]), "+v"(A[p][6][1]),
                 "+v"(A[p][7][0]), "+v"(A[p][7][1]), "+v"(B[p][0]), "+v"(B[p][1]), "+v"(B[p][2]), "+v"(B[p][3]), "+v"(B[p][4]), "+v"(B[p][5]),
                 "+v"(B[p][6]), "+v"(B[p][7]) :: "memory");
    SB();
  };
  auto mm = [&](int p) {
#pragma unroll
    for (int n = 0; n < 36; ++n) {
      const int t = TT[n], a = AA[n];
      const u32x4 av = {A[p][a][0][0], A[p][a][0][1], A[p][a][1][0], A[p][a][1][1]};
      asm volatile("v_mfma_f32_16x16x32_bf16 %0, %1, %2, %0" : "+v"(ac[t]) : "v"(av), "v"(B[p][t - a]));
      if (n < 24) ld1(p ^ 1, n);
      SB();
    }
    land(p ^ 1);
  };
#pragma unroll
  for (int t = 0; t < 8; ++t) ac[t] = (f32x4){0.f, 0.f, 0.f, 0.f};
#pragma unroll
  for (int n = 0; n < 24; ++n) ld1(0, n);
  land(0);
#pragma unroll 1
  for (int r = 0; r < (XE & 1 ? 0 : 8); r += 2) {
    next(-64);
    mm(0);
    next(r < 6 ? -64 : 0);
    mm(1);
  }
  asm volatile("s_nop 15" : "+v"(ac[0]), "+v"(ac[1]), "+v"(ac[2]), "+v"(ac[3]), "+v"(ac[4]), "+v"(ac[5]), "+v"(ac[6]), "+v"(ac[7]));
}

#define GTB 1
#define PMAP 1
#define GNL 0
#define PSL "1"
#define LP3 3
#define HPL 1
#define HSC ""
#define XSRC hf
#define CDR 0
#define SPR 1
#define DLY -1
#define PSLN 2
#define NUP 2
#define SGS 13
#define CVT 4u
#define BARP 0
#define XNS S0
#define CLP 1
#define NRP 0
#define CVW 0
#define ROT 0
#define XAM 7
#define XBM 15
#define YMP 3
#define YEX 1
#define ANT ""
#define BNT ""
#define XAS 128
#define TBL 1
#define PW0 4
#define WPF 0
#define ZSL 0
#define ZS2 0
#define PPF 4
#define WTA 16
#define NW1 10
#define NW0 0
constexpr int M_FLAG = 0;
constexpr int M_RT = 128;
constexpr int M_MT = M_RT + 8192;
constexpr int M_CV = M_MT + 4096;
constexpr int M_RING = M_CV + 3 * CV_BUF;
constexpr int P3_C = 128;
constexpr int P3_G = P3_C + 72 * 1024;
constexpr int P3_Y = M_RING + 4 * 24576;
constexpr int M_LDS = P3_Y + 8 * 1536;
static_assert(P3_G + 68 * 1024 <= P3_Y && M_LDS <= 163840 && M_RING <= P3_G, "LDS map");
#define GAS __attribute__((address_space(1)))
__device__ __forceinline__ void flag_publish(unsigned* f) { __hip_atomic_store((GAS unsigned*)f, 1u, __ATOMIC_RELAXED, __HIP_MEMORY_SCOPE_AGENT); }
__device__ __forceinline__ unsigned umx(unsigned a, unsigned b) { return a > b ? a : b; }
__device__ __forceinline__ unsigned max4(const u32x4& v, unsigned m) { return umx(umx(umx(m, v[0]), v[1]), umx(v[2], v[3])); }
__device__ __forceinline__ unsigned nohole(unsigned a) { return a < HOLE - 1 ? a : HOLE - 1; }

__device__ __forceinline__ void row_stat(const float (&mj)[4], const float (&qj)[4], float& mean, float& r) {
  mean = 0.25f * ((mj[0] + mj[1]) + (mj[2] + mj[3]));
  const float d0 = mj[0] - mean, d1 = mj[1] - mean, d2 = mj[2] - mean, d3 = mj[3] - mean;
  const float m2 = (qj[0] + qj[1]) + (qj[2] + qj[3]) + 192.0f * ((d0 * d0 + d1 * d1) + (d2 * d2 + d3 * d3));
  const float vbf = rbf(m2 * (1.0f / 768.0f));
  r = __builtin_amdgcn_rcpf(rbf(__builtin_amdgcn_sqrtf(rbf(vbf + 1e-5f))));
}
struct NormLd { u32x3 z[6]; u32x2 x[3]; f32x2 p[4]; };
__device__ __forceinline__ void norm_load(NormLd& n, const u16* z2f, const float2* st2, int ks, int cb, int lane) {
#pragma unroll
  for (int j = 0; j < 4; ++j) n.p[j] = *(const f32x2*)(st2 + j * S_ + ks * 32 + (lane >> 4) * 8 + (lane & 7));
#pragma unroll
  for (int j = 0; j < 6; ++j) n.z[j] = *(const u32x3*)((const char*)z2f + (ks * 48 + cb * 6 + j) * 1024 + lane * 16);
#pragma unroll
  for (int j = 0; j < 3; ++j) n.x[j] = *(const u32x2*)((const char*)z2f + Z1X + (ks * 24 + cb * 3 + j) * 512 + lane * 8);
  SB();
}
template <int E> __device__ __forceinline__ f32x2 bc2(float v) { return (f32x2){dppf<0x150 + 2 * E>(v), dppf<0x151 + 2 * E>(v)}; }
__device__ __forceinline__ void norm_store(const NormLd& n, u16* z2f, int ks, int cb, int lane) {
  const float mj[4] = {n.p[0][0], n.p[1][0], n.p[2][0], n.p[3][0]}, qj[4] = {n.p[0][1], n.p[1][1], n.p[2][1], n.p[3][1]};
  float mean, r;
  row_stat(mj, qj, mean, r);
  mean = rbf(mean);
  const f32x2 me[4] = {bc2<0>(mean), bc2<1>(mean), bc2<2>(mean), bc2<3>(mean)}, re[4] = {bc2<0>(r), bc2<1>(r), bc2<2>(r), bc2<3>(r)};
  const auto zrs = __builtin_amdgcn_make_buffer_rsrc(z2f, 0, S_ * FH_ * 2, 0x00020000);
#pragma unroll
  for (int j = 0; j < 6; ++j) {
    u32x4 o;
#pragma unroll
    for (int e = 0; e < 4; ++e) o[e] = pk2((unpk(e < 3 ? n.z[j][e < 3 ? e : 0] : n.x[j >> 1][j & 1]) - me[e]) * re[e]);
    o[3] = nohole(o[3]);
    __builtin_amdgcn_raw_buffer_store_b128(o, zrs, (ks * 48 + cb * 6 + j) * 1024 + lane * 16, 0, WTA);
  }
}
__global__ __launch_bounds__(64) void k_n(u16* z2f, const float2* __restrict__ st2) {
  const int ks = blockIdx.x >> 3;
  if ((ks & 15) < S0) return;
  NormLd n;
  norm_load(n, z2f, st2, ks, blockIdx.x & 7, threadIdx.x);
  norm_store(n, z2f, ks, blockIdx.x & 7, threadIdx.x);
}

__global__ __launch_bounds__(512) void k_m(
    const u16* __restrict__ w_sp, const u16* __restrict__ b_sp, u16* hf,
    u16* z2f, const float2* __restrict__ st2, u16* yT,
    const u16* __restrict__ ssm_k, const u16* __restrict__ ssm_D, const u16* __restrict__ woF,
    const u16* __restrict__ x, const float2* __restrict__ xst, u16* __restrict__ out) {
  extern __shared__ __attribute__((aligned(16))) char smem[];
  const int tid = threadIdx.x, lane = tid & 63, wave = __builtin_amdgcn_readfirstlane(tid >> 6);
  const int b = blockIdx.x;
  const int xcd = b & 7, idx = b >> 3;
  const int rb = 8 * (xcd >> 1) + (idx >> 2), cb = 4 * (xcd & 1) + (idx & 3);
  const int pw = (wave + ROT * cb) & 7;
  const int t0 = rb * 64;
  const int g4 = lane >> 4, l15 = lane & 15;
  STAMP(2, 0);
  RT(1, 0);
  AB_DECL;
  AB(0);
  const unsigned flg = lds_addr(smem) + M_FLAG;
  bf16x8 Wq[15];
  const char* wg = (const char*)woF + (size_t)(pw * 120 + cb * 5) * 1024;
  const int mt = wave & 3, kh = wave >> 2, d0 = cb * 80;
  const int row = t0 + mt * 16 + l15;
  const size_t xo = (size_t)row * D_ + d0;
  u32x2 xv[3], st;
  const auto hrs = __builtin_amdgcn_make_buffer_rsrc(hf, 0, S_ * FH_ * 2, 0x00020000);
  auto ldP1 = [&](int n) {
    if (n < 15)
      asm volatile("global_load_dwordx4 %0, %1, %2 offset:%3" : "=&v"(Wq[n < 15 ? n : 0]) : "v"((unsigned)(lane * 16)), "s"(wg + (n / 5) * 40960 + 2048), "i"((n % 5) * 1024 - 2048) : "memory");
    else if (n < 18) {
      const int q = n < 18 ? n - 15 : 0;
      asm volatile("global_load_dwordx2 %0, %1, %2 offset:%3" : "=&v"(xv[q]) : "v"((unsigned)(l15 * D_ * 2 + 8 * g4)),
                   "s"(x + (size_t)(t0 + mt * 16) * D_ + d0 + (q < 2 ? 32 * kh : 64)), "i"(q == 1 ? 32 : 0) : "memory");
    } else
      asm volatile("global_load_dwordx2 %0, %1, %2" : "=&v"(st) : "v"((unsigned)(l15 * 8)), "s"(xst + t0 + mt * 16) : "memory");
  };
  int tmo = 1 << 16;
  u32x4 yv[2];
  auto yoff = [&](int n) {
    const int c = n ? 64 + (lane & 31) : lane, r = c >> 1;
    return (unsigned)(((r < 32 ? 32 * kh + r : 32 + r) * S_ + 8 * (c & 1)) * 2);
  };
  const u16* const yb = yT + (size_t)d0 * S_ + t0 + 16 * mt;
  auto ldy0 = [&]() {
#pragma unroll
    for (int n = 0; n < 2; ++n) asm volatile("global_load_dwordx4 %0, %1, %2 " HSC : "=&v"(yv[n]) : "v"(yoff(n)), "s"(yb) : "memory");
  };
  auto signal1 = [&](unsigned a) {
    const unsigned one = 1u;
    asm volatile("s_mov_b64 exec, 1\n\tds_add_u32 %0, %1\n\ts_mov_b64 exec, -1" :: "v"(a), "v"(one) : "memory");
  };
  auto drain = [&](int nc) {
    SB();
    asm volatile("s_waitcnt vmcnt(%21)"  : "+v"(Wq[0]), "+v"(Wq[1]), "+v"(Wq[2]), "+v"(Wq[3]), "+v"(Wq[4]), "+v"(Wq[5]), "+v"(Wq[6]), "+v"(Wq[7]),
                 "+v"(Wq[8]), "+v"(Wq[9]), "+v"(Wq[10]), "+v"(Wq[11]), "+v"(Wq[12]), "+v"(Wq[13]), "+v"(Wq[14]),
                 "+v"(xv[0]), "+v"(xv[1]), "+v"(xv[2]), "+v"(st), "+v"(yv[0]), "+v"(yv[1]) : "i"(nc) : "memory");
  };
  auto drain1 = [&](int nc) {
    SB();
#pragma unroll
    for (int n = 0; n < NW1; ++n) asm volatile("s_waitcnt vmcnt(%1)" : "+v"(Wq[n]) : "i"(nc) : "memory");
  };
  const int cw = wave & 3;
  const unsigned pfo = (unsigned)((lane >> 4) * 4096 + ((lane >> 2) & 3) * 1024 + (4 + (lane & 3)) * 128);
  f32x4 sp[4][2];
  auto ldst = [&](int q) {
#pragma unroll
    for (int j = 0; j < 4; ++j)
      asm volatile("global_load_dwordx4 %0, %1, %2 offset:%3" : "=&v"(sp[j][q]) : "v"((unsigned)(lane * 16)), "s"(st2 + j * S_ + cw * 512), "i"(q * 1024) : "memory");
    SB();
  };
  auto table = [&](int q, int nv) {
    asm volatile("s_waitcnt vmcnt(%4)" : "+v"(sp[0][q]), "+v"(sp[1][q]), "+v"(sp[2][q]), "+v"(sp[3][q]) : "i"(nv) : "memory");
    float mv[2], rv[2];
#pragma unroll
    for (int h = 0; h < 2; ++h) {
      const float mj[4] = {sp[0][q][2 * h], sp[1][q][2 * h], sp[2][q][2 * h], sp[3][q][2 * h]};
      const float qj[4] = {sp[0][q][2 * h + 1], sp[1][q][2 * h + 1], sp[2][q][2 * h + 1], sp[3][q][2 * h + 1]};
      row_stat(mj, qj, mv[h], rv[h]);
    }
    *(f32x2*)(smem + M_RT + cw * 2048 + q * 512 + lane * 8) = (f32x2){rv[0], rv[1]};
    *(unsigned*)(smem + M_MT + cw * 1024 + q * 256 + lane * 4) = pack2(mv[0], mv[1]);
    if (!GTB || q) flag_signal(flg + 16 + cw * 4, lane);
    SB();
  };
  if (wave >= 4) {
    TS_DECL;
    auto pre = [&]() {
      ldst(0);
      if (TBL) ldst(1);
      __builtin_amdgcn_s_barrier();
      SB();
      if (!TBL) ldst(1);
    };
    TS(0);
    if (cw < (b < 128 ? 3 : 2)) {
      if (!GTB) pre();
      const int d = cw < 2 ? 2 * b + cw : 512 + b;
      char* cb_ = smem + M_CV + cw * CV_BUF;
      u32x4 hc[4], uc[4];
      unsigned hp[4], ux[4];
      float Dv;
      auto ldc = [&]() {
#pragma unroll
        for (int k = 0; k < 4; ++k) {
          const int a = k * 64 + lane;
          hc[k] = *(const u32x4*)(ssm_k + (size_t)d * S_ + a * 8);
          hp[k] = *(const unsigned*)(ssm_k + (size_t)d * S_ + max(a * 8 - 2, 0));
          uc[k] = *(const u32x4*)(yT + (size_t)d * S_ + a * 8);
          ux[k] = *(const unsigned*)((const char*)yT + KVX + (d >> 1) * 2048 + a * 8 + (d & 1) * 4);
        }
        Dv = bf2f(ssm_D[d]);
        SB();
      };
      ldst(1);
      if (GTB == 1) ldc();
      ABS(1);
      __builtin_amdgcn_s_barrier();
      SB();
      ABS(2);
      if (GTB == 2) ldc();
      ABS(3);
      table(1, 16);
      ABS(4);
      TS(1); AB(1);
      if (lane < 36) *(u32x4*)(cb_ + (lane < 32 ? lane * 16 : CV_C + ((lane >> 1) & 1) * CV_CS + 4096 + (lane & 1) * 16)) = (u32x4){0u, 0u, 0u, 0u};
#pragma unroll
      for (int k = 0; k < 4; ++k) {
        const int a = k * 64 + lane;
        const unsigned w[5] = {a ? hp[k] : 0u, hc[k][0], hc[k][1], hc[k][2], hc[k][3]};
        u32x4 sw, mx;
#pragma unroll
        for (int i = 0; i < 4; ++i) {
          sw[i] = (w[4 - i] >> 16) | (w[4 - i] << 16);
          mx[i] = (w[4 - i] & 0xFFFFu) | (w[3 - i] & 0xFFFF0000u);
        }
        char* cp = cb_ + CV_C + (2040 - 8 * a) * 2;
        *(u32x4*)(cp) = sw;
        *(u32x4*)(cp + CV_CS) = mx;
        *(u32x4*)(cb_ + 512 + a * 16) = (u32x4){uc[k][0], uc[k][1], uc[k][2], ux[k]};
      }
      TS(2); AB(2); ABS(5);
      f32x4 ac[8];
      conv_core(lds_addr(cb_), lane, ac);
      AB(1);
      const auto yrs = __builtin_amdgcn_make_buffer_rsrc(yT, 0, D_ * S_ * 2, 0x00020000);
      u32x2 kvp[8];
#pragma unroll
      for (int t = 0; t < 8; ++t) kvp[t] = *(const u32x2*)(cb_ + 512 + (256 * t + 16 * l15 + 4 * g4) * 2);
#pragma unroll
      for (int k = 0; k < 4; ++k) {
        u32x2 ov[2];
#pragma unroll
        for (int t = 0; t < 2; ++t)
#pragma unroll
          for (int h = 0; h < 2; ++h) {
            const unsigned cv = pack2(ac[2 * k + t][2 * h], ac[2 * k + t][2 * h + 1]);
            const unsigned kd = pack2(lo16(kvp[2 * k + t][h]) * Dv, hi16(kvp[2 * k + t][h]) * Dv);
            ov[t][h] = pack2(lo16(cv) + lo16(kd), hi16(cv) + hi16(kd));
          }
        const auto s0 = __builtin_amdgcn_permlane16_swap(ov[0][0], ov[1][0], false, false);
        const auto s1 = __builtin_amdgcn_permlane16_swap(ov[0][1], ov[1][1], false, false);
        const u32x4 o4 = {s0[0], s1[0], s0[1], nohole(s1[1])};
        __builtin_amdgcn_raw_buffer_store_b128(o4, yrs, (d * S_ + 256 * (2 * k + (g4 & 1)) + 16 * l15 + 8 * (g4 >> 1)) * 2, 0, WTA);
      }
    } else {
      const int ks = 2 * (rb & 24) + (cw == 3 ? 8 : 0) + (rb & 7);
      NormLd nl;
      ldst(1);
      if (GNL) norm_load(nl, z2f, st2, ks, cb, lane);
      SB();
      __builtin_amdgcn_s_barrier();
      SB();
      if (!GNL) norm_load(nl, z2f, st2, ks, cb, lane);
      unsigned pf = 0;
      auto pfw = [&]() {
#pragma unroll
        for (int n = 0; n < 4; ++n)
          asm volatile("global_load_dword %0, %1, %2" : "+v"(pf) : "v"(pfo), "s"(w_sp + (size_t)(t0 + (idx & 3) * 16 + n * 4) * S_) : "memory");
        SB();
      };
      if (PPF == 1) pfw();
      if (!GTB && TBL != 2) table(0, 17);
      if (PPF == 2) pfw();
      if (NRP == 0) table(1, 13);
      if (PPF == 3) pfw();
      AB(1);
      if (out && (ks & 15) >= S0) norm_store(nl, z2f, ks, cb, lane);
      SB();
      if (NRP == 1) table(1, 0);
      if (PPF == 4 && cw == 3) pfw();
      __builtin_amdgcn_s_waitcnt(0x0F70);
      asm volatile("" : "+v"(pf));
      AB(2);
    }
    SB();
    TS(3); TS_OUT(2, 8, 4, 4); AB(3);
    if (!out) { RT(1, 1); return; }
    WAIT_LGKM0();
    flag_signal(flg + 40, lane);
    flag_wait(flg + 44, CVT);
    AB(4);
#pragma unroll
    for (int n = 0; n < NW1; ++n) ldP1(n);
    drain1(0);
  } else {
  if (tid < 32) *(unsigned*)(smem + M_FLAG + tid * 4) = 0u;
  const u16* Ab = w_sp + (size_t)t0 * S_ + wave * 512;
  const char* Bs = (const char*)z2f + (size_t)(wave * 16 * 48 + cb * 6) * 1024 + 2048;
  const unsigned boff = (unsigned)(lane * 16);
  char* ring = smem + M_RING + wave * 24576;
  const unsigned ringl = __builtin_amdgcn_readfirstlane(lds_addr(ring));
  const unsigned aoff = (unsigned)((lane >> 3) * 4096 + (((lane & 7) ^ (lane >> 3)) << 4));
  bf16x8 Bf[3][6];
  TS_DECL;
  auto ldA1 = [&](auto dc, int i) {
    constexpr int d = decltype(dc)::value;
    const unsigned ao = (XE & 4) && d >= 2 ? aoff & 0x70u : aoff, rl = ringl;
    const char* ab = (const char*)Ab + ((wave + 1 + (i >> 1)) & 3) * 65536;
    asm volatile("s_mov_b32 m0, %2\n\ts_nop 0\n\tglobal_load_lds_dwordx4 %0, %1 " ANT
                 :: "v"(ao), "s"(ab + ((i & 1) * 32768 + (d & XAM) * XAS)), "s"(rl + (d % 3) * 8192 + i * 1024) : "memory");
  };
  auto ldA = [&](auto dc) {
#pragma unroll
    for (int i = 0; i < 8; ++i) ldA1(dc, i);
  };
  auto ldB1 = [&](auto sc, int j) {
    constexpr int s = decltype(sc)::value;
    bf16x8& B = Bf[s % 3][j];
    const unsigned bo = (XE & 2) && s >= 3 ? boff & 0x70u : boff;
    const char* bs = Bs + (size_t)(s & XBM) * 49152;
    asm volatile("global_load_dwordx4 %0, %1, %2 offset:%3 " BNT : "=&v"(B) : "v"(bo), "s"(bs), "i"(j * 1024 - 2048) : "memory");
  };
  auto ldB = [&](auto sc) {
#pragma unroll
    for (int j = 0; j < 6; ++j) ldB1(sc, j);
  };
  if (GTB) ldst(0);
  ldA(IC(0)); ldB(IC(0));
  if (BARP == 0) RAW_BARRIER();
  if (BARP == 3) table(0, 14);
  ldB(IC(1));
  if (BARP == 4) table(0, 20);
  ldA(IC(1));
  unsigned pfg = 0;
  if (PPF == 6)
    asm volatile("global_load_dword %0, %1, %2" : "=&v"(pfg) : "v"(pfo), "s"(w_sp + (size_t)(t0 + (idx & 3) * 16 + wave * 4) * S_) : "memory");
  if (BARP == 1) RAW_BARRIER();
  TS(6);
  if (GTB) { if (BARP < 3) table(0, 28 + (PPF == 6)); }
  else flag_wait_asm(flg + 16 + wave * 4, 1u);
  if (BARP >= 2) RAW_BARRIER();
  f32x4 acc[4][6], cacc[4];
  const unsigned rta = lds_addr(smem) + M_RT + (wave * 512 + g4 * 8) * 4;
  const unsigned mta = lds_addr(smem) + M_MT + (wave * 512 + g4 * 8) * 2;
  unsigned ara[2];
#pragma unroll
  for (int ks = 0; ks < 2; ++ks) ara[ks] = lds_addr(ring) + l15 * 128 + (((ks * 4 + g4) ^ (l15 & 7)) << 4);
  u32x4 af[4];
  f32x4 rT[2][2]; u32x4 mT[2];
  auto rdA = [&](auto sc, int i) {
    constexpr int s = decltype(sc)::value;
    u32x4& o = af[i];
    const unsigned ad = ara[s & 1];
    asm volatile("ds_read_b128 %0, %1 offset:%2" : "=&v"(o) : "v"(ad), "i"(((s >> 1) % 3) * 8192 + i * 2048) : "memory");
  };
  auto rdT = [&](auto sc) {
    constexpr int s = decltype(sc)::value;
    f32x4 &o0 = rT[s & 1][0], &o1 = rT[s & 1][1]; u32x4& om = mT[s & 1];
    const unsigned ra = rta, ma = mta;
    asm volatile("ds_read_b128 %0, %1 offset:%2" : "=&v"(o0) : "v"(ra), "i"(s * 128) : "memory");
    asm volatile("ds_read_b128 %0, %1 offset:%2" : "=&v"(o1) : "v"(ra), "i"(s * 128 + 16) : "memory");
    asm volatile("ds_read_b128 %0, %1 offset:%2" : "=&v"(om) : "v"(ma), "i"(s * 64) : "memory");
  };
  constexpr int NSC = 20, NPM = 3;
  float tl, th;
  auto scop = [&](int i, int p, int n) {
    const int e = n / 5, k = n % 5;
    if (k == 0) tl = lo16(af[i][e]);
    else if (k == 1) th = hi16(af[i][e]);
    else if (k == 2) tl = __builtin_fmaf(tl, rT[p][e >> 1][(e & 1) * 2], 0.0f);
    else if (k == 3) th *= rT[p][e >> 1][(e & 1) * 2 + 1];
    else af[i][e] = pack2(tl, th);
  };
  TS(7);
  asm volatile("s_waitcnt vmcnt(%0) lgkmcnt(0)" :: "n"(20 + (PPF == 6)) : "memory");
  rdT(IC(0));
#pragma unroll
  for (int i = 0; i < 4; ++i) rdA(IC(0), i);
  asm volatile("s_waitcnt lgkmcnt(3)" : "+v"(af[0]), "+v"(rT[0][0]), "+v"(rT[0][1]), "+v"(mT[0]) :: "memory");
#pragma unroll
  for (int n = 0; n < NSC; ++n) scop(0, 0, n);
  SB();
  const unsigned eo = (unsigned)((l15 + 16 * (2 * (g4 & 1) + (g4 >> 1))) * 16);
  const size_t eb = (size_t)((rb * 4 + wave) * 24 + cb * 3) * 1024;
  u32x3 zv[3];
  unsigned zx[3], bsv;
  auto ldZ = [&]() {
#pragma unroll
    for (int q = 0; q < 3; ++q)
      asm volatile("global_load_dwordx3 %0, %1, %2 offset:%3" : "=&v"(zv[q]) : "v"(eo), "s"((const char*)hf + eb), "i"(q * 1024) : "memory");
#pragma unroll
    for (int q = 0; q < 3; ++q)
      asm volatile("global_load_dword %0, %1, %2 offset:%3" : "=&v"(zx[q]) : "v"(eo >> 1),
                   "s"((const char*)hf + Z1X + ((rb * 2 + (wave >> 1)) * 24 + cb * 3) * 512 + (wave & 1) * 4), "i"(q * 512) : "memory");
    asm volatile("global_load_ushort %0, %1, %2" : "=&v"(bsv) : "v"((unsigned)(l15 * 2)), "s"(b_sp + t0 + wave * 16) : "memory");
  };
  const unsigned xa = ringl + lane * 16, xc = ringl + lane * 4;
  auto step = [&](auto sc) {
    constexpr int s = decltype(sc)::value, sl = s % 3, p = s & 1;
    constexpr int NV = s == 0 ? 14 + (PPF == 6) : (s == 2 ? 28 : (s < 12 ? 20 : (s == 12 ? 12 : (LP3 ? 19 - 6 * (s - 13) : 19))));
    constexpr int DA = s == 0 ? 2 : (((s & 1) && (s + 5) / 2 < 8) ? (s + 5) / 2 : -1);
    asm volatile("s_waitcnt vmcnt(%7) lgkmcnt(2)\n\ts_nop 1"
                 : "+v"(Bf[sl][0]), "+v"(Bf[sl][1]), "+v"(Bf[sl][2]), "+v"(Bf[sl][3]), "+v"(Bf[sl][4]), "+v"(Bf[sl][5]), "+v"(af[1]) : "n"(NV) : "memory");
    if constexpr (s == 1) asm volatile("" : "+v"(pfg));
    if constexpr (s == 3) flag_wait_asm(flg + 16 + wave * 4, GTB ? 1u : 2u);
    if constexpr (s >= S0) {
      auto d3 = [&](int j) { return __builtin_bit_cast(u32x4, Bf[sl][j])[3]; };
      auto bad = [&]() { return __builtin_amdgcn_ballot_w64(umx(umx(umx(d3(0), d3(1)), d3(2)), umx(umx(d3(3), d3(4)), d3(5))) == HOLE) != 0; };
      if (__builtin_expect(bad(), 0)) {
        do {
          --tmo;
          asm volatile("s_sleep 2\n\tglobal_load_dwordx4 %0, %6, %7 offset:-2048 sc1\n\tglobal_load_dwordx4 %1, %6, %7 offset:-1024 sc1\n\t"
                       "global_load_dwordx4 %2, %6, %7 sc1\n\tglobal_load_dwordx4 %3, %6, %7 offset:1024 sc1\n\t"
                       "global_load_dwordx4 %4, %6, %7 offset:2048 sc1\n\tglobal_load_dwordx4 %5, %6, %7 offset:3072 sc1\n\ts_waitcnt vmcnt(0)"
                       : "+v"(Bf[sl][0]), "+v"(Bf[sl][1]), "+v"(Bf[sl][2]), "+v"(Bf[sl][3]), "+v"(Bf[sl][4]), "+v"(Bf[sl][5])
                       : "v"(boff), "s"(Bs + (size_t)s * 49152) : "memory");
        } while (bad() && tmo > 0);
      }
    }
    if constexpr (s == SGS) signal1(flg + 44);
    if constexpr (s < 15) ABS(s + 1);
    SB();
#pragma unroll
    for (int i = 0; i < 4; ++i) {
      if (i == 1) asm volatile("s_waitcnt lgkmcnt(%2)" : "+v"(af[2]), "+v"(af[3]) : "n"(s < 15 ? (s + 1 < S0 ? 4 : 1) : 0) : "memory");
      if (i == 3 && s + 1 < S0) asm volatile("s_waitcnt lgkmcnt(2)" : "+v"(af[0]), "+v"(rT[p ^ 1][0]), "+v"(rT[p ^ 1][1]), "+v"(mT[p ^ 1]) :: "memory");
      if (i == 3 && s + 1 >= S0 && s < 15) asm volatile("s_waitcnt lgkmcnt(2)" : "+v"(af[0]) :: "memory");
      const bf16x8 av = __builtin_bit_cast(bf16x8, af[i]);
#pragma unroll
      for (int j = 0; j < 7; ++j) {
        f32x4& ac = j < 6 ? acc[i][j < 6 ? j : 0] : cacc[i];
        const bf16x8 bb = j < 6 ? Bf[sl][j < 6 ? j : 0] : __builtin_bit_cast(bf16x8, mT[p]);
        if constexpr (s == 0) asm volatile("v_mfma_f32_16x16x32_bf16 %0, %1, %2, 0" : "=&v"(ac) : "v"(bb), "v"(av));
        else if (j < 6 || s < XNS) asm volatile("v_mfma_f32_16x16x32_bf16 %0, %1, %2, %0" : "+v"(ac) : "v"(bb), "v"(av));
        SB();
#pragma unroll
        for (int n = NPM * j; n < NPM * j + NPM && n < NSC; ++n) {
          if (i < 3) { if (s < XNS) scop(i + 1, p, n); }
          else if (s < 15 && s + 1 < XNS) scop(0, p ^ 1, n);
          SB();
        }
        if constexpr (s == 0) { if (i == 0 && j < 6) ldB1(IC(2), j); }
        if constexpr (DA >= 0) { if ((i == 1 || i == 2) && j < 4) ldA1(IC(DA < 0 ? 0 : DA), (i - 1) * 4 + j); }
        if constexpr (s + 3 < 16) { if (i == 3 && j < 6) ldB1(IC(s + 3), j); }
        if constexpr (!LP3 && (s == 13 || s == 14)) { if (i == 3 && j < 6) ldP1((s - 13) * 6 + j); }
        if constexpr (s == 15) {
          if (i > 0 && j > 0) asm volatile("ds_write_b128 %0, %1 offset:%2" :: "v"(xa), "v"(acc[i > 0 ? i - 1 : 0][j > 0 ? j - 1 : 0]), "i"((i - 1) * 6144 + (j - 1) * 1024) : "memory");
          if (!LP3) {
            if (i == 2 && j > 0) ldP1(11 + j);
            if (i == 3 && j == 1) ldP1(18);
            if (i == 3 && j == 2) ldy0();
          }
          if (LP3 == 2) { if (i >= 2 && j < 6) ldP1((i - 2) * 6 + j); }
          if (LP3 == 3) { if (i >= 2 && j < 6 && (i - 2) * 6 + j < NW0) ldP1((i - 2) * 6 + j); }
        }
        SB();
      }
      if constexpr (s == 15) {
        if (i > 0) {
          asm volatile("ds_write_b32 %0, %1 offset:%2" :: "v"(xc), "v"(cacc[i > 0 ? i - 1 : 0][0]), "i"(18432 + (i - 1) * 256),
                       "v"(cacc[i > 0 ? i - 1 : 0]) : "memory");
          signal1(flg + ((wave + i) & 3) * 4);
        }
      }
      asm volatile("s_nop 1");
      if constexpr (s < 15) { if (i == 0 && s + 1 < S0) rdT(IC(s + 1)); rdA(IC(s + 1), i); }
      SB();
    }
    if constexpr (s == 12) ldZ();
    SB();
  };
  TS(0); AB(1);
  step(IC(0)); TS(1); step(IC(1)); step(IC(2)); step(IC(3)); TS(2); AB(3); step(IC(4)); step(IC(5)); step(IC(6)); step(IC(7)); TS(3);
  step(IC(8)); step(IC(9)); step(IC(10)); step(IC(11)); TS(4); step(IC(12)); step(IC(13)); step(IC(14)); step(IC(15)); TS(5); AB(2);
  STAMP(2, 1);
  if (LP3 && LP3 != 3) {
#pragma unroll
    for (int n = LP3 == 2 ? 12 : 0; n < 19; ++n) ldP1(n);
    ldy0();
    SB();
  }
  if (LP3 == 3) {
#pragma unroll
    for (int n = NW0; n < NW1; ++n) ldP1(n);
    SB();
  }
  flag_wait_asm(flg + wave * 4, 3u);
  AB(4);
  f32x4 own[6];
  {
    f32x4 pr[3][6];
    float pc[3];
#pragma unroll
    for (int q = 0; q < 3; ++q) {
      const char* src = smem + M_RING + ((wave + 3 - q) & 3) * 24576;
      pc[q] = *(const float*)(src + 18432 + q * 256 + lane * 4);
#pragma unroll
      for (int j = 0; j < 6; ++j) pr[q][j] = *(const f32x4*)(src + q * 6144 + j * 1024 + lane * 16);
    }
    asm volatile("s_nop 7" : "+v"(acc[3][0]), "+v"(acc[3][1]), "+v"(acc[3][2]), "+v"(acc[3][3]), "+v"(acc[3][4]), "+v"(acc[3][5]), "+v"(cacc[3]));
    const float c = (cacc[3][0] + pc[0]) + (pc[1] + pc[2]);
#pragma unroll
    for (int j = 0; j < 6; ++j) own[j] = ((acc[3][j] - c) + pr[0][j]) + (pr[1][j] + pr[2][j]);
  }
  asm volatile("s_waitcnt vmcnt(%7)" : "+v"(zv[0]), "+v"(zv[1]), "+v"(zv[2]), "+v"(zx[0]), "+v"(zx[1]), "+v"(zx[2]), "+v"(bsv) : "i"(LP3 == 3 ? NW1 : 21) : "memory");
  SB();
  {
    const float bs = lo16(bsv);
    unsigned pk[6][2];
#pragma unroll
    for (int j = 0; j < 6; ++j)
#pragma unroll
      for (int h = 0; h < 2; ++h) {
        const unsigned r0 = pack2(own[j][2 * h], own[j][2 * h + 1]);
        pk[j][h] = pack2(lo16(r0) + bs, hi16(r0) + bs);
      }
#pragma unroll
    for (int q = 0; q < 3; ++q) {
      const auto s0 = __builtin_amdgcn_permlane16_swap(pk[2 * q][0], pk[2 * q + 1][0], false, false);
      const auto s1 = __builtin_amdgcn_permlane16_swap(pk[2 * q][1], pk[2 * q + 1][1], false, false);
      u32x4 gv; gv[0] = s0[0]; gv[1] = s1[0]; gv[2] = s0[1]; gv[3] = s1[1];
      u32x4 o4;
#pragma unroll
      for (int e = 0; e < 4; ++e) {
        const unsigned z = e < 3 ? zv[q][e < 3 ? e : 0] : zx[q];
        o4[e] = pack2(lo16(z) * lo16(gv[e]), hi16(z) * hi16(gv[e]));
      }
      o4[3] = nohole(o4[3]);
      __builtin_amdgcn_raw_buffer_store_b128(o4, hrs, (int)eb + eo + q * 1024, 0, WTA);
    }
  }
  TS_OUT(1, 0, 8, 0); TS_OUT(1, 8, 8, 3); AB(5);
  drain1(3);
  if (!out) { RT(1, 1); return; }
  STAMP(2, 2);
  }
  TS_DECL;
  TS(0);
  unsigned hoff[4];
#pragma unroll
  for (int it = 0; it < 4; ++it) hoff[it] = (unsigned)(((rb * 4 + ((mt + 1 + it) & 3)) * 24 + 3 * pw) * 1024 + lane * 16);
  u32x4 Hq[4][3];
  auto ldy = [&]() {
#pragma unroll
    for (int n = 0; n < 2; ++n) asm volatile("global_load_dwordx4 %0, %1, %2 sc1" : "=&v"(yv[n]) : "v"(yoff(n)), "s"(yb) : "memory");
  };
  auto ldh = [&](int it) {
#pragma unroll
    for (int k = 0; k < 3; ++k)
      asm volatile("global_load_dwordx4 %0, %1, %2 offset:%3 sc1" : "=&v"(Hq[it][k]) : "v"(hoff[it]), "s"(XSRC), "i"(k * 1024) : "memory");
  };
  auto ldh1 = [&](int it, int k) {
    asm volatile("global_load_dwordx4 %0, %1, %2 offset:%3 " HSC : "=&v"(Hq[it][k]) : "v"(hoff[it]), "s"(XSRC), "i"(k * 1024) : "memory");
  };
  auto ldh0 = [&](int it) {
#pragma unroll
    for (int k = 0; k < 3; ++k) ldh1(it, k);
  };
  f32x2 xf[3][2], sv[3][2];
  auto ymath = [&]() {
    char* yi = smem + P3_Y + wave * 1536;
#pragma unroll
    for (int n = 0; n < 2; ++n) *(u32x4*)(yi + (n ? 64 + (lane & 31) : lane) * 16) = yv[n];
    const unsigned ya = lds_addr(yi) + (4 * g4 + (l15 >> 2)) * 32 + 8 * (lane & 3);
    s16x4 yr[3] = {tr_asm<0>(ya), tr_asm<512>(ya), tr_asm<1024>(ya)};
    asm volatile("s_waitcnt lgkmcnt(0)" : "+v"(yr[0]), "+v"(yr[1]), "+v"(yr[2]) :: "memory");
    const f32x2 mean = {__uint_as_float(st[0]), __uint_as_float(st[0])}, rd = {__uint_as_float(st[1]), __uint_as_float(st[1])};
#pragma unroll
    for (int q = 0; q < 3; ++q) {
      const u32x2 yw = __builtin_bit_cast(u32x2, yr[q]);
#pragma unroll
      for (int h = 0; h < 2; ++h) {
        xf[q][h] = unpk(xv[q][h]);
        sv[q][h] = ((xf[q][h] - mean) * rd) * unpk(yw[h]);
        asm volatile("" : "+v"(sv[q][h]), "+v"(xf[q][h]));
      }
    }
    SB();
  };
#pragma unroll
  for (int n = NW1; n < 19; ++n) ldP1(n);
  if (YMP == 5) ldy0();
  SB();
  if (wave >= 4) AB(5);
  asm volatile("s_waitcnt vmcnt(%0)" :: "i"(19 - NW1 + (YMP == 5 ? 2 : 0)) : "memory");
  bool ybad;
  AB(6);
  signal1(flg + 48 + 4 * kh);
  flag_wait_asm(flg + 48, 4u);
  TS(1); AB(7);
  if (SPR) __builtin_amdgcn_s_setprio(3);
  ldh0(0);
  if (SPR) __builtin_amdgcn_s_setprio(2);
  ldh0(1);
  if (SPR) __builtin_amdgcn_s_setprio(1);
  if (NUP > 2) ldh0(2);
  if (SPR) __builtin_amdgcn_s_setprio(0);
  if (NUP > 3) ldh0(3);
  if (YMP < 3) ldy0();
  SB();
  AB(8);
  TS(2); AB(9);
  flag_wait_asm(flg + 40, kh ? 4u : 0u);
  TS(3);
  char* const px = smem + (kh ? P3_C + mt * 18432 : P3_G + mt * 17408) + lane * 16;
  f32x4 ac3[5], o[3];
  auto comp = [&](auto itc) {
    constexpr int it = decltype(itc)::value;
    constexpr int NW = 3 * ((it + NUP < 4 ? it + NUP : 4) - it - 1) + (LP3 == 3 && YMP != 5 && (YMP < 3 ? it < 2 : it >= 2) ? 2 : 0);
    asm volatile("s_waitcnt vmcnt(%3)" : "+v"(Hq[it][0]), "+v"(Hq[it][1]), "+v"(Hq[it][2]) : "n"(NW) : "memory");
    if constexpr (LP3 == 3 && it == 0)
      asm volatile("" : "+v"(Wq[0]), "+v"(Wq[1]), "+v"(Wq[2]), "+v"(Wq[3]), "+v"(Wq[4]), "+v"(Wq[5]), "+v"(Wq[6]), "+v"(Wq[7]),
                   "+v"(Wq[8]), "+v"(Wq[9]), "+v"(Wq[10]), "+v"(Wq[11]), "+v"(Wq[12]), "+v"(Wq[13]), "+v"(Wq[14]),
                   "+v"(xv[0]), "+v"(xv[1]), "+v"(xv[2]), "+v"(st) :: "memory");
    auto bad = [&]() { return __builtin_amdgcn_ballot_w64(umx(umx(Hq[it][0][3], Hq[it][1][3]), Hq[it][2][3]) == HOLE) != 0; };
    if (__builtin_expect(bad(), 0)) {
      do {
        --tmo;
        asm volatile("s_sleep 2\n\tglobal_load_dwordx4 %0, %3, %4 sc1\n\tglobal_load_dwordx4 %1, %3, %4 offset:1024 sc1\n\t"
                     "global_load_dwordx4 %2, %3, %4 offset:2048 sc1\n\ts_waitcnt vmcnt(0)"
                     : "+v"(Hq[it][0]), "+v"(Hq[it][1]), "+v"(Hq[it][2]) : "v"(hoff[it]), "s"(hf) : "memory");
      } while (bad() && tmo > 0);
    }
    SB();
#pragma unroll
    for (int k = 0; k < 3; ++k) {
#pragma unroll
      for (int j = 0; j < 5; ++j) {
        ac3[j] = __builtin_amdgcn_mfma_f32_16x16x32_bf16(Wq[k * 5 + j], __builtin_bit_cast(bf16x8, Hq[it][k]), k ? ac3[j] : (f32x4){0.f, 0.f, 0.f, 0.f}, 0, 0, 0);
        if constexpr (it + NUP < 4) { if (k == 0 && j < 3) { SB(); ldh1(it + NUP < 4 ? it + NUP : 0, j); SB(); } }
        if constexpr (YMP >= 3 && YMP != 5 && it == 1) { if (k == 1 && j == 0) { SB(); ldy0(); SB(); } }
      }
    }
    SB();
    if constexpr (it < 3) {
#pragma unroll
      for (int j = 0; j < 5; ++j) *(f32x4*)(px + (it * 5 + j) * 1024) = ac3[j];
      signal1(flg + 64 + kh * 16 + ((mt + 1 + it) & 3) * 4);
    }
    SB();
  };
  auto gather = [&](auto cc) {
    constexpr int cv = decltype(cc)::value;
    flag_wait_asm(flg + 64 + cv * 16 + mt * 4, cv ? 5u : 3u);
#pragma unroll
    for (int it = 0; it < 3 + cv; ++it) {
      const int ms = (mt - 1 - it) & 3;
      const char* ps = smem + ((it < 3 ? cv : !kh) ? P3_C + ms * 18432 : P3_G + ms * 17408) + lane * 16;
      const char* p0 = ps + (it < 3 ? it * 5 + 2 * kh : 15) * 1024;
      const f32x4 a0 = *(const f32x4*)p0, a1 = *(const f32x4*)(p0 + 1024);
      if (!cv && it == 0) { o[0] = a0; o[1] = a1; } else { o[0] += a0; o[1] += a1; }
    }
    if (!kh) {
#pragma unroll
      for (int it = 0; it < 3 + cv; ++it) {
        const int ms = (mt - 1 - it) & 3;
        const char* ps = smem + ((it < 3 ? cv : 1) ? P3_C + ms * 18432 : P3_G + ms * 17408) + lane * 16;
        o[2] += *(const f32x4*)(ps + (it < 3 ? it * 5 + 4 : 17) * 1024);
      }
    }
    SB();
  };
  o[2] = (f32x4){0.f, 0.f, 0.f, 0.f};
  comp(IC(0)); AB(10); comp(IC(1));
  if (YMP == 5) {
    asm volatile("" : "+v"(yv[0]), "+v"(yv[1]) :: "memory");
    if (__builtin_amdgcn_ballot_w64(umx(yv[0][3], yv[1][3]) == HOLE) != 0) {
      asm volatile("global_load_dwordx4 %0, %2, %4 sc1\n\tglobal_load_dwordx4 %1, %3, %4 sc1" : "+v"(yv[0]), "+v"(yv[1]) : "v"(yoff(0)), "v"(yoff(1)), "s"(yb) : "memory");
    }
    SB();
  }
  if (YMP == 1) {
    asm volatile("s_waitcnt vmcnt(6)" : "+v"(yv[0]), "+v"(yv[1]) :: "memory");
    ybad = __builtin_amdgcn_ballot_w64(umx(yv[0][3], yv[1][3]) == HOLE) != 0;
    ymath();
  }
  comp(IC(2));
  if (YMP == 2) {
    asm volatile("" : "+v"(yv[0]), "+v"(yv[1]) :: "memory");
    ybad = __builtin_amdgcn_ballot_w64(umx(yv[0][3], yv[1][3]) == HOLE) != 0;
    ymath();
  }
  TS(4); AB(11);
  gather(IC(0));
  if (YMP == 4) {
    asm volatile("s_waitcnt vmcnt(0)" : "+v"(yv[0]), "+v"(yv[1]) :: "memory");
    ybad = __builtin_amdgcn_ballot_w64(umx(yv[0][3], yv[1][3]) == HOLE) != 0;
    ymath();
  }
  AB(12);
  comp(IC(3));
  TS(5); AB(13);
  {
    *(f32x4*)(px + 15 * 1024) = kh ? ac3[0] : ac3[2];
    *(f32x4*)(px + 16 * 1024) = kh ? ac3[1] : ac3[3];
    if (kh) *(f32x4*)(px + 17 * 1024) = ac3[4];
    signal1(flg + 80 + mt * 4);
    SB();
    o[0] += kh ? ac3[2] : ac3[0]; o[1] += kh ? ac3[3] : ac3[1]; o[2] += ac3[4];
    if (YMP == 3 || YMP == 5) {
      asm volatile("s_waitcnt vmcnt(0)" : "+v"(yv[0]), "+v"(yv[1]) :: "memory");
      ybad = __builtin_amdgcn_ballot_w64(umx(yv[0][3], yv[1][3]) == HOLE) != 0;
    }
    if (__builtin_expect(ybad, 0)) {
      do {
        --tmo;
        ldy();
        asm volatile("s_waitcnt vmcnt(0)" : "+v"(yv[0]), "+v"(yv[1]) :: "memory");
      } while (__builtin_amdgcn_ballot_w64(umx(yv[0][3], yv[1][3]) == HOLE) != 0 && tmo > 0);
      if (YMP != 3 && YMP != 5) ymath();
    }
    if (YMP == 3 || YMP == 5) ymath();
    TS(6); AB(14);
    gather(IC(1));
    auto fin = [&](const f32x4& o4, int q, unsigned (&pk)[2]) {
#pragma unroll
      for (int h = 0; h < 2; ++h) {
        const f32x2 o = {o4[2 * h], o4[2 * h + 1]};
        const f32x2 a = o * (f32x2){-1.4426950f, -1.4426950f};
        const f32x2 e = (f32x2){__builtin_amdgcn_exp2f(a[0]), __builtin_amdgcn_exp2f(a[1])} + (f32x2){1.0f, 1.0f};
        const f32x2 r = {__builtin_amdgcn_rcpf(e[0]), __builtin_amdgcn_rcpf(e[1])};
        pk[h] = pk2(__builtin_elementwise_fma(o * r, sv[q][h], xf[q][h]));
      }
    };
    unsigned pa[2], pb[2];
    fin(o[0], 0, pa);
    fin(o[1], 1, pb);
    const auto s0 = __builtin_amdgcn_permlane16_swap(pa[0], pb[0], false, false);
    const auto s1 = __builtin_amdgcn_permlane16_swap(pa[1], pb[1], false, false);
    *(u32x4*)(out + xo + 32 * kh + 16 * (g4 & 1) + 8 * (g4 >> 1)) = (u32x4){s0[0], s1[0], s0[1], s1[1]};
    if (!kh) {
      fin(o[2], 2, pa);
      *(u32x2*)(out + xo + 64 + 4 * g4) = (u32x2){pa[0], pa[1]};
    }
  }
  TS(7); AB(15);
  AB_OUT(16);
  RT(1, 1);
}

extern "C" __global__ void k_end() {}

#define X4 0
#define LA4 4
constexpr int G3_RED = 4 * 3072;
constexpr int G3_LDS = G3_RED + 4 * 15 * 1024;

__global__ __launch_bounds__(256) void k_l4(
    const u16* __restrict__ hf, const u16* __restrict__ woF, const u16* __restrict__ x,
    const float2* __restrict__ xst, const u16* __restrict__ yT, u16* __restrict__ out) {
  extern __shared__ __attribute__((aligned(16))) char smem[];
  const int tid = threadIdx.x, lane = tid & 63, wave = __builtin_amdgcn_readfirstlane(tid >> 6);
  const int b = blockIdx.x;
  const int xcd = b & 7, idx = b >> 3;
  const int rb = 8 * (xcd >> 1) + (idx >> 2), cb = 4 * (xcd & 1) + (idx & 3);
  const int t0 = rb * 64, d0 = cb * 80;
  const int g4 = lane >> 4, l15 = lane & 15;
  RT(2, 0);
  TS_DECL;
  TS(0);
  const unsigned boff16 = (unsigned)(lane * 16);
  const char* Hb = (const char*)hf + (size_t)(rb * 4 * 24 + wave * 6) * 1024;
  const char* Wb = (const char*)woF + (size_t)(wave * 6 * 40 + cb * 5) * 1024;
  u32x4 Hf[6][4], Wf[6][5];
  auto ldH = [&](int s, int k) { Hf[s][k] = *(const u32x4*)(Hb + (size_t)((wave + 1 + k) & 3) * 24576 + s * 1024 + boff16); };
  auto ldG = [&](int s) {
#pragma unroll
    for (int j = 0; j < 5; ++j) Wf[s][j] = *(const u32x4*)(Wb + (size_t)s * 40960 + j * 1024 + boff16);
    ldH(s, 0); ldH(s, 1);
    SB();
  };
  const unsigned ly = __builtin_amdgcn_readfirstlane(lds_addr(smem) + wave * 3072);
  {
    const char* yb = (const char*)(yT + (size_t)d0 * S_ + t0 + wave * 16);
#pragma unroll
    for (int n = 0; n < 3; ++n) {
      const int c = min(n * 64 + lane, 159);
      asm volatile("s_mov_b32 m0, %2\n\ts_nop 0\n\tglobal_load_lds_dwordx4 %0, %1"
                   :: "v"((unsigned)((c >> 1) * (S_ * 2) + (c & 1) * 16)), "s"(yb), "s"(ly + n * 1024) : "memory");
    }
  }
  SB();
  ldG(0); ldG(1);
  const int row = t0 + wave * 16 + l15;
  const size_t xo = (size_t)row * D_ + d0;
  u32x2 xv[5];
  const float2 st = xst[row];
#pragma unroll
  for (int j = 0; j < 5; ++j) xv[j] = *(const u32x2*)(x + xo + 16 * j + 4 * g4);
  SB();
  ldG(2); ldG(3);
  TS(1);
  f32x4 acc[4][5];
  auto mm = [&](int s, int k) {
#pragma unroll
    for (int j = 0; j < 5; ++j) {
      const f32x4 c = s ? acc[k][j] : (f32x4){0.f, 0.f, 0.f, 0.f};
      acc[k][j] = __builtin_amdgcn_mfma_f32_16x16x32_bf16(__builtin_bit_cast(bf16x8, Wf[s][j]), __builtin_bit_cast(bf16x8, Hf[s][k]), c, 0, 0, 0);
    }
    SB();
  };
  auto put = [&](int k, int j) {
    *(f32x4*)(smem + G3_RED + (((wave + 1 + k) & 3) * 3 + k) * 5120 + j * 1024 + lane * 16) = acc[k][j];
    SB();
  };
  f32x4 pr[3][5];
  auto get = [&](int q) {
#pragma unroll
    for (int j = 0; j < 5; ++j) pr[q][j] = *(const f32x4*)(smem + G3_RED + (wave * 3 + q) * 5120 + j * 1024 + lane * 16);
    SB();
  };
  auto ldN = [&](int n) {
    if (n < 6) ldG(n);
    else if (n < 8) {
#pragma unroll
      for (int s = 0; s < 6; ++s) ldH(s, n - 4);
      SB();
    }
  };
  mm(0, 0); mm(0, 1); ldN(LA4);
  s16x4 yr[5];
  {
    const unsigned ya = ly + (4 * g4 + (l15 >> 2)) * 32 + 8 * (lane & 3);
#pragma unroll
    for (int j = 0; j < 5; ++j)
      asm volatile("ds_read_b64_tr_b16 %0, %1 offset:%2" : "=&v"(yr[j]) : "v"(ya), "i"(j * 512) : "memory");
  }
  mm(1, 0); mm(1, 1); ldN(LA4 + 1);
  asm volatile("s_waitcnt lgkmcnt(0)" : "+v"(yr[0]), "+v"(yr[1]), "+v"(yr[2]), "+v"(yr[3]), "+v"(yr[4]) :: "memory");
  f32x2 xf[5][2], sv[5][2];
  {
    const f32x2 mean = {st.x, st.x}, rd = {st.y, st.y};
#pragma unroll
    for (int j = 0; j < 5; ++j) {
      const u32x2 yw = __builtin_bit_cast(u32x2, yr[j]);
#pragma unroll
      for (int h = 0; h < 2; ++h) {
        xf[j][h] = unpk(xv[j][h]);
        sv[j][h] = ((xf[j][h] - mean) * rd) * unpk(yw[h]);
      }
    }
    SB();
  }
  mm(2, 0); mm(2, 1); ldN(LA4 + 2);
  mm(3, 0); mm(3, 1); ldN(LA4 + 3);
  mm(4, 0); mm(4, 1);
  mm(5, 0); mm(5, 1);
  TS(2);
  mm(0, 2); put(0, 0); put(0, 1); put(0, 2);
  mm(1, 2); put(0, 3); put(0, 4); put(1, 0);
  mm(2, 2); put(1, 1); put(1, 2); put(1, 3);
  mm(3, 2); put(1, 4);
  mm(4, 2);
  mm(5, 2);
  TS(3);
  RAW_BARRIER();
  SB();
  get(0);
  mm(0, 3);
  put(2, 0); put(2, 1); put(2, 2); put(2, 3); put(2, 4);
  get(1);
  mm(1, 3);
  mm(2, 3);
  TS(4);
  RAW_BARRIER();
  SB();
  TS(5);
  get(2);
#pragma unroll
  for (int j = 0; j < 5; ++j) pr[0][j] += pr[1][j];
  SB();
  mm(3, 3);
  mm(4, 3);
#pragma unroll
  for (int j = 0; j < 5; ++j) pr[0][j] += pr[2][j];
  SB();
  mm(5, 3);
  TS(6);
  unsigned pk[5][2];
#pragma unroll
  for (int j = 0; j < 5; ++j) {
    const f32x4 o4 = acc[3][j] + pr[0][j];
#pragma unroll
    for (int h = 0; h < 2; ++h) {
      const f32x2 o = {o4[2 * h], o4[2 * h + 1]};
      const f32x2 a = o * (f32x2){-1.4426950f, -1.4426950f};
      const f32x2 e = (f32x2){__builtin_amdgcn_exp2f(a[0]), __builtin_amdgcn_exp2f(a[1])} + (f32x2){1.0f, 1.0f};
      const f32x2 r = {__builtin_amdgcn_rcpf(e[0]), __builtin_amdgcn_rcpf(e[1])};
      pk[j][h] = pk2(__builtin_elementwise_fma(o * r, sv[j][h], xf[j][h]));
    }
  }
  const int dq = 16 * (g4 & 1) + 8 * (g4 >> 1);
#pragma unroll
  for (int q = 0; q < 2; ++q) {
    const auto s0 = __builtin_amdgcn_permlane16_swap(pk[2 * q][0], pk[2 * q + 1][0], false, false);
    const auto s1 = __builtin_amdgcn_permlane16_swap(pk[2 * q][1], pk[2 * q + 1][1], false, false);
    u32x4 o4; o4[0] = s0[0]; o4[1] = s1[0]; o4[2] = s0[1]; o4[3] = s1[1];
    *(u32x4*)(out + xo + 32 * q + dq) = o4;
  }
  *(u32x2*)(out + xo + 64 + 4 * g4) = (u32x2){pk[4][0], pk[4][1]};
  TS(7);
  RT(2, 1);
}

extern "C" void kernel_launch(void* const* d_in, const int* in_sizes, int n_in,
                              void* d_out, int out_size, void* d_ws, size_t ws_size,
                              hipStream_t stream) {
  const u16* A_q = (const u16*)d_in[0];
  const u16* A_k = (const u16*)d_in[1];
  const u16* A_v = (const u16*)d_in[2];
  const u16* A_shift_k = (const u16*)d_in[3];
  const u16* A_ssm_k = (const u16*)d_in[4];
  const u16* A_ssm_D = (const u16*)d_in[5];
  const u16* B_w_in = (const u16*)d_in[7];
  const u16* B_w_sp = (const u16*)d_in[8];
  const u16* B_b_sp = (const u16*)d_in[9];
  const u16* B_w_out = (const u16*)d_in[10];
  u16* out = (u16*)d_out;

  char* ws = (char*)d_ws;
  size_t off = 0;
  auto carve = [&](size_t bytes) { char* p = ws + off; off += (bytes + 255) & ~(size_t)255; return p; };
  u16* z1 = (u16*)carve((size_t)S_ * FH_ * 2 * 5 / 4);
  u16* z2 = (u16*)carve((size_t)S_ * FH_ * 2 * 5 / 4);
  float2* st2 = (float2*)carve((size_t)4 * S_ * 8);
  float2* xst = (float2*)carve((size_t)S_ * 8);
  u16* kvT = (u16*)carve((size_t)D_ * S_ * 2 * 5 / 4);
  u16* woF = (u16*)carve((size_t)FH_ * D_ * 2);

  int dev = 0, cus = 0;
  (void)hipGetDevice(&dev);
  (void)hipDeviceGetAttribute(&cus, hipDeviceAttributeMultiprocessorCount, dev);
  const bool fused = cus >= 256;
  (void)hipFuncSetAttribute((const void*)k_l1, hipFuncAttributeMaxDynamicSharedMemorySize, L1_LDS);
  (void)hipFuncSetAttribute((const void*)k_m, hipFuncAttributeMaxDynamicSharedMemorySize, M_LDS);
  k_l1<<<256, 768, L1_LDS, stream>>>(A_q, A_k, A_v, A_shift_k, B_w_in, z1, z2, st2, xst, kvT, A_ssm_k, B_w_out, B_w_sp, woF);
  if (!fused) k_n<<<512, 64, 0, stream>>>(z2, st2);
  k_m<<<256, 512, M_LDS, stream>>>(B_w_sp, B_b_sp, z1, z2, st2, kvT, A_ssm_k, A_ssm_D, woF, A_q, xst, fused ? out : nullptr);
  if (!fused) {
    (void)hipFuncSetAttribute((const void*)k_l4, hipFuncAttributeMaxDynamicSharedMemorySize, G3_LDS);
    k_l4<<<256, 256, G3_LDS, stream>>>(z1, woF, A_q, xst, kvT, out);
  }
}
```

```cpp
#define HTK 0
#define HTI 0
#define HTW 0
#define HTR 0
#define HEADT 0
#define TAILT 0
#define HG 1
#define TG 1
#define OWT 16
#include <hip/hip_runtime.h>
#include <hip/hip_bf16.h>
#include <stdint.h>
#include <type_traits>

typedef unsigned short u16;
typedef __attribute__((ext_vector_type(8))) short bf16x8;
typedef __attribute__((ext_vector_type(4))) short s16x4;
typedef __attribute__((ext_vector_type(4))) float f32x4;
typedef __attribute__((ext_vector_type(16))) float f32x16;
typedef __attribute__((ext_vector_type(4))) unsigned int u32x4;
typedef __attribute__((ext_vector_type(3))) unsigned int u32x3;
typedef __attribute__((ext_vector_type(2))) unsigned int u32x2;

#define LDS_AS __attribute__((address_space(3)))
#define IC(n) std::integral_constant<int, n>{}
#define SB() __builtin_amdgcn_sched_barrier(0)

#define STAMP(k, i) do {} while (0)
#define STAMPW(k, i, w) do {} while (0)
#define TS_DECL do {} while (0)
#define TS(i) do {} while (0)
#define TS_OUT(k, base, n, w) do {} while (0)

#define RT(k, i) do {} while (0)

#define AB_DECL do {} while (0)
#define AB(i) do {} while (0)
#define ABS(i) do {} while (0)
#define AB_OUT(n) do {} while (0)

#define AK_DECL do {} while (0)
#define AK(i) do {} while (0)
#define AK_OUT(n) do {} while (0)

#undef AK_DECL
#undef AK
#undef AK_OUT
#undef AB_DECL
#undef AB
#undef AB_OUT
__device__ unsigned long long g_ak[256][8][16];
#define AK_DECL unsigned long long ak_[16] = {}
#define AK(i) ak_[i] = __builtin_amdgcn_s_memrealtime()
#define AK_OUT(n) do { if ((threadIdx.x & 63) == 0 && threadIdx.x < 512) for (int i_ = 0; i_ < n; ++i_) g_ak[blockIdx.x][threadIdx.x >> 6][i_] = ak_[i_] ? ak_[i_] : ak_[0]; } while (0)
#define AB_DECL unsigned long long ab_[16]
#define AB(i) ab_[i] = __builtin_amdgcn_s_memrealtime()
#define AB_OUT(n) do { if ((threadIdx.x >> 6) == (HTK ? HTW : 0)) { unsigned long long ref_; \
  if (HTK) ref_ = ab_[HTI]; else { \
    unsigned long long b0_ = g_ak[0][HTW][0]; long long acc_ = HTR == 0 ? (1ll << 60) : (HTR == 1 ? -(1ll << 60) : 0); \
    for (int q_ = 0; q_ < 4; ++q_) { long long v_ = (long long)(g_ak[(threadIdx.x & 63) * 4 + q_][HTW][HTI] - b0_); acc_ = HTR == 0 ? (v_ < acc_ ? v_ : acc_) : (HTR == 1 ? (v_ > acc_ ? v_ : acc_) : acc_ + v_); } \
    for (int o_ = 32; o_ > 0; o_ >>= 1) { long long v_ = __shfl_xor(acc_, o_); acc_ = HTR == 0 ? (v_ < acc_ ? v_ : acc_) : (HTR == 1 ? (v_ > acc_ ? v_ : acc_) : acc_ + v_); } \
    if (HTR == 2) acc_ /= 256; ref_ = b0_ + acc_; } \
  while (__builtin_amdgcn_s_memrealtime() < ref_ + 6000) __builtin_amdgcn_s_sleep(1); } } while (0)

__device__ unsigned long long g_t0[4];
__global__ void k_h() { if (threadIdx.x == 0) g_t0[0] = __builtin_amdgcn_s_memrealtime(); }
__global__ void k_z() {
  unsigned long long ref_;
  { unsigned long long b0_ = g_ak[0][0][0]; long long acc_ = (1ll << 60);
    for (int q_ = 0; q_ < 4; ++q_) { long long v_ = (long long)(g_ak[(threadIdx.x & 63) * 4 + q_][0][0] - b0_); acc_ = v_ < acc_ ? v_ : acc_; }
    for (int o_ = 32; o_ > 0; o_ >>= 1) { long long v_ = __shfl_xor(acc_, o_); acc_ = v_ < acc_ ? v_ : acc_; }
    ref_ = b0_ + acc_; }
  while (__builtin_amdgcn_s_memrealtime() < ref_ + 6000) __builtin_amdgcn_s_sleep(1);
}
#define MAPM 0
#define XE 0
constexpr int S_ = 2048, D_ = 640, FH_ = 768, F2_ = 1536;

__device__ __forceinline__ float bf2f(u16 v) { return __uint_as_float(((unsigned)v) << 16); }
typedef __attribute__((ext_vector_type(2))) __bf16 bf16x2n;
typedef __attribute__((ext_vector_type(2))) float f32x2;
__device__ __forceinline__ unsigned pack2(float lo, float hi) {
  f32x2 v = {lo, hi};
  bf16x2n r = __builtin_convertvector(v, bf16x2n);
  return __builtin_bit_cast(unsigned, r);
}
__device__ __forceinline__ float lo16(unsigned w) { return __uint_as_float(w << 16); }
__device__ __forceinline__ float hi16(unsigned w) { return __uint_as_float(w & 0xFFFF0000u); }
__device__ __forceinline__ u16 f2bf(float f) { return (u16)(pack2(f, f) & 0xFFFFu); }
__device__ __forceinline__ float rbf(float f) { return lo16(pack2(f, f)); }

template <int CTRL>
__device__ __forceinline__ float dppf(float v) {
  return __builtin_bit_cast(float, __builtin_amdgcn_update_dpp(0, __builtin_bit_cast(int, v), CTRL, 0xF, 0xF, true));
}
__device__ __forceinline__ float sum16(float v) {
  v += dppf<0xB1>(v); v += dppf<0x4E>(v); v += dppf<0x141>(v); v += dppf<0x140>(v);
  return v;
}
__device__ __forceinline__ float gelu_tanh(float x) {
  const float kBeta = 0.7978845608028654f;
  const float kKappa = 0.044715f;
  float inner = kBeta * (x + kKappa * x * x * x);
  return x * __builtin_amdgcn_rcpf(1.0f + __expf(-2.0f * inner));
}

template <int NSUB>
__device__ __forceinline__ unsigned boff(unsigned row, unsigned ch) {
  return (unsigned)(NSUB * 512) * (row >> 3) + 512u * (ch >> 2) + 64u * (row & 7) + 16u * ((ch & 3) ^ ((row >> 2) & 3));
}
template <int OFF>
__device__ __forceinline__ s16x4 tr_asm(unsigned a) {
  s16x4 r;
  asm volatile("ds_read_b64_tr_b16 %0, %1 offset:%2" : "=&v"(r) : "v"(a), "i"(OFF) : "memory");
  return r;
}
struct TrBase { unsigned b[2][2]; };
template <int NSUB>
__device__ __forceinline__ TrBase tr_base(unsigned lane, unsigned ldsbase) {
  const unsigned g = lane >> 4, q = (lane & 15) >> 2, p = lane & 3;
  TrBase t;
#pragma unroll
  for (int c1 = 0; c1 < 2; ++c1)
#pragma unroll
    for (int h = 0; h < 2; ++h)
      t.b[c1][h] = ldsbase + boff<NSUB>(8 * g + 4 * h + q, 2 * c1 + (p >> 1)) + 8 * (p & 1);
  return t;
}
template <int NSUB, int KS, int C>
__device__ __forceinline__ void tr_frag_issue(const TrBase& t, unsigned stage_off, s16x4& lo, s16x4& hi) {
  constexpr int OFF = NSUB * 512 * 4 * KS + 512 * (C >> 1);
  lo = tr_asm<OFF>(t.b[C & 1][0] + stage_off);
  hi = tr_asm<OFF>(t.b[C & 1][1] + stage_off);
}
__device__ __forceinline__ bf16x8 join8(s16x4 lo, s16x4 hi) {
  bf16x8 r;
  r[0] = lo[0]; r[1] = lo[1]; r[2] = lo[2]; r[3] = lo[3];
  r[4] = hi[0]; r[5] = hi[1]; r[6] = hi[2]; r[7] = hi[3];
  return r;
}
#define WAIT_LGKM0() asm volatile("s_waitcnt lgkmcnt(0)" ::: "memory")
__device__ __forceinline__ unsigned lds_addr(const void* p) {
  return (unsigned)(uintptr_t)(LDS_AS const char*)p;
}

#define RAW_BARRIER() do { asm volatile("s_waitcnt lgkmcnt(0)" ::: "memory"); __builtin_amdgcn_s_barrier(); } while (0)
#define WAIT_VM(n) asm volatile("s_waitcnt vmcnt(" #n ")" ::: "memory")

__device__ __forceinline__ void flag_wait(unsigned faddr, unsigned target) {
  while (true) {
    unsigned v;
    asm volatile("ds_read_b32 %0, %1\n\ts_waitcnt lgkmcnt(0)" : "=&v"(v) : "v"(faddr) : "memory");
    v = __builtin_amdgcn_readfirstlane(v);
    if (v >= target) break;
    __builtin_amdgcn_s_sleep(1);
  }
}
__device__ __forceinline__ void flag_wait_asm(unsigned faddr, unsigned target) {
  unsigned v, sv;
  asm volatile(".Lfw_%=:\n\tds_read_b32 %0, %2\n\ts_waitcnt lgkmcnt(0)\n\tv_readfirstlane_b32 %1, %0\n\ts_nop 0\n\t"
               "s_cmp_ge_u32 %1, %3\n\ts_cbranch_scc1 .Lfd_%=\n\ts_sleep 1\n\ts_branch .Lfw_%=\n\t.Lfd_%=:"
               : "=&v"(v), "=&s"(sv) : "v"(faddr), "s"(target) : "memory", "scc");
}
__device__ __forceinline__ void flag_signal(unsigned faddr, int lane) {
  if (lane == 0) {
    unsigned one = 1u;
    asm volatile("ds_add_u32 %0, %1" :: "v"(faddr), "v"(one) : "memory");
  }
}
__device__ __forceinline__ void glds16(const void* g, char* lds) {
  __builtin_amdgcn_global_load_lds((const unsigned*)g, (unsigned*)lds, 16, 0, 0);
}
__device__ __forceinline__ void glds_a(const u16* gbase, int ld, char* stageA, int ins, int lane) {
  const int row = ins * 8 + (lane >> 3);
  const int ch = (lane & 7) ^ (row & 7);
  glds16(gbase + (size_t)row * ld + ch * 8, stageA + ins * 1024);
}
__device__ __forceinline__ bf16x8 read_a(const char* stageA, int row, int kchunk) {
  return *(const bf16x8*)(stageA + row * 128 + ((kchunk ^ (row & 7)) << 4));
}
template <int NSUB, int BNV>
__device__ __forceinline__ void glds_b(const u16* gbase, int ld, char* stageB, int ins, int lane) {
  const int o = ins * 1024 + lane * 16;
  const int rg = o / (NSUB * 512), rem = o % (NSUB * 512);
  const int sub = rem >> 9, rr = (rem >> 6) & 7, slot = (rem >> 4) & 3;
  const int row = rg * 8 + rr;
  int ch = sub * 4 + (slot ^ ((row >> 2) & 3));
  if (BNV < NSUB * 32) ch = min(ch, BNV / 8 - 1);
  glds16(gbase + (size_t)row * ld + ch * 8, stageB + ins * 1024);
}

constexpr int L1_A_BYTES = 64 * 128;
constexpr int L1_B_BYTES = 64 * 192 * 2;
constexpr int L1_STAGE = L1_A_BYTES + L1_B_BYTES;
constexpr int L1_LDS = 5 * L1_STAGE;
constexpr unsigned HOLE = 0xFFFFFFFFu;
constexpr int Z1X = S_ * FH_ * 2;
constexpr int KVX = D_ * S_ * 2;
#define S0 7

__device__ __forceinline__ f32x2 unpk(unsigned w) { f32x2 r = {lo16(w), hi16(w)}; return r; }
__device__ __forceinline__ unsigned pk2(f32x2 v) { return pack2(v[0], v[1]); }
__device__ __forceinline__ f32x2 gelu2(f32x2 x) {
  const f32x2 c1 = {-2.3022082f, -2.3022082f}, c2 = {-0.10294324f, -0.10294324f};
  const f32x2 a = x * __builtin_elementwise_fma(x * x, c2, c1);
  f32x2 e = {__builtin_amdgcn_exp2f(a[0]), __builtin_amdgcn_exp2f(a[1])};
  e = e + (f32x2){1.0f, 1.0f};
  const f32x2 r = {__builtin_amdgcn_rcpf(e[0]), __builtin_amdgcn_rcpf(e[1])};
  return x * r;
}
__device__ __forceinline__ float allred_8(float v) {
  v += __uint_as_float(__builtin_amdgcn_mov_dpp(__float_as_uint(v), 0xB1, 0xF, 0xF, true));
  v += __uint_as_float(__builtin_amdgcn_mov_dpp(__float_as_uint(v), 0x4E, 0xF, 0xF, true));
  v += __uint_as_float(__builtin_amdgcn_mov_dpp(__float_as_uint(v), 0x141, 0xF, 0xF, true));
  return v;
}
__device__ __forceinline__ float allred_g(float v) {
  const auto a = __builtin_amdgcn_permlane16_swap(__float_as_uint(v), __float_as_uint(v), false, false);
  v = __uint_as_float(a[0]) + __uint_as_float(a[1]);
  const auto c = __builtin_amdgcn_permlane32_swap(__float_as_uint(v), __float_as_uint(v), false, false);
  return __uint_as_float(c[0]) + __uint_as_float(c[1]);
}

#define WT1 0
#define WTS "sc1"
#define ST16(p, v, bit) do { if (WT1 & (bit)) asm volatile("global_store_dwordx4 %0, %1, off " WTS "\n\ts_nop 1" :: "v"(p), "v"(v) : "memory"); else *(u32x4*)(p) = (v); } while (0)
__global__ __launch_bounds__(768) void k_l1(
    const u16* __restrict__ x, const u16* __restrict__ kin, const u16* __restrict__ vin,
    const u16* __restrict__ shift_k, const u16* __restrict__ w_in,
    u16* __restrict__ z1, u16* __restrict__ z2, float2* __restrict__ st2, float2* __restrict__ xst,
    u16* __restrict__ kvT, const u16* __restrict__ ssm_k, const u16* __restrict__ w_out, const u16* __restrict__ w_sp,
    u16* __restrict__ woF) {
  extern __shared__ __attribute__((aligned(16))) char smem[];
  const int tid = threadIdx.x, lane = tid & 63, wave = __builtin_amdgcn_readfirstlane(tid >> 6);
  const int b = blockIdx.x;
  const int xcd = b & 7, idx = b >> 3;
  const int rb = 8 * (xcd >> 1) + (idx >> 2), cb = 4 * (xcd & 1) + (idx & 3);
  const int row0 = rb * 64;
  const int n0 = cb * 192;
  const int g4 = lane >> 4, l15 = lane & 15;
  STAMP(0, 0);
  RT(0, 0);
  AK_DECL;
  AK(0);

  char* sE = smem + L1_STAGE;
  float2* sRow = (float2*)sE;
  float* sCs = (float*)(sE + 1024);
  float* sStat = (float*)(sE + 2048);
  char* sHand = sE + 8192;
  char* sTr = sE + 40960 + wave * 2048;
  const bool is_z2 = cb >= 4;
  auto epi = [&](const f32x4 (&t)[2][2], int wm, int cg, int swp, int rsw) {
    f32x4 csv[2];
#pragma unroll
    for (int jj = 0; jj < 2; ++jj) csv[jj] = *(const f32x4*)(sCs + cg * 32 + (jj ^ swp) * 16 + 4 * g4);
    f32x2 gv[2][2][2];
    unsigned pk[2][2][2];
#pragma unroll
    for (int i = 0; i < 2; ++i) {
      const float2 rs = sRow[wm * 32 + (i ^ rsw) * 16 + l15];
      const f32x2 nm = {-rs.x, -rs.x}, rd = {rs.y, rs.y};
#pragma unroll
      for (int jj = 0; jj < 2; ++jj)
#pragma unroll
        for (int h = 0; h < 2; ++h) {
          const f32x2 av = {t[i][jj][2 * h], t[i][jj][2 * h + 1]};
          const f32x2 cv = {csv[jj][2 * h], csv[jj][2 * h + 1]};
          gv[i][jj][h] = gelu2(__builtin_elementwise_fma(nm, cv, av) * rd);
          pk[i][jj][h] = pk2(gv[i][jj][h]);
        }
    }
    if (!is_z2) {
      const int lp = l15 + 16 * (2 * ((g4 & 1) ^ swp) + (g4 >> 1));
      char* zdst = (char*)z1 + (size_t)((rb * 4 + wm * 2) * 24 + cb * 6 + cg) * 1024 + lp * 16;
      u32x4 o[2];
#pragma unroll
      for (int i = 0; i < 2; ++i) {
        const auto sa = __builtin_amdgcn_permlane16_swap(pk[i][0][0], pk[i][1][0], false, false);
        const auto sb = __builtin_amdgcn_permlane16_swap(pk[i][0][1], pk[i][1][1], false, false);
        o[i][0] = sa[0]; o[i][1] = sb[0]; o[i][2] = sa[1]; o[i][3] = sb[1];
      }
#pragma unroll
      for (int i = 0; i < 2; ++i) ST16(zdst + (i ^ rsw) * 24 * 1024, ((u32x4){o[i][0], o[i][1], o[i][2], HOLE}), 1);
      *(u32x2*)((char*)z1 + Z1X + ((rb * 2 + wm) * 24 + cb * 6 + cg) * 512 + lp * 8) = (u32x2){rsw ? o[1][3] : o[0][3], rsw ? o[0][3] : o[1][3]};
    } else {
#pragma unroll
      for (int jj = 0; jj < 2; ++jj) {
        char* wb = sTr + l15 * 64 + 8 * ((4 * (jj ^ swp) + g4) ^ (l15 >> 1));
#pragma unroll
        for (int i = 0; i < 2; ++i) *(u32x2*)(wb + (i ^ rsw) * 1024) = (u32x2){pk[i][jj][0], pk[i][jj][1]};
      }
      const unsigned q = l15 >> 2, p = lane & 3;
      const unsigned ra = lds_addr(sTr) + (8 * g4 + q) * 64 + 8 * (p ^ (4 * (g4 & 1) + (q >> 1)));
      s16x4 tr[2][2];
      tr[0][0] = tr_asm<0>(ra); tr[0][1] = tr_asm<256>(ra ^ 16u);
      tr[1][0] = tr_asm<0>(ra ^ 32u); tr[1][1] = tr_asm<256>(ra ^ 48u);
      float sv[2][2], pv[2];
#pragma unroll
      for (int i = 0; i < 2; ++i) {
        const unsigned p0 = __float_as_uint(gv[i][0][0][0]);
        const auto pa = __builtin_amdgcn_permlane16_swap(p0, p0, false, false);
        const auto pb = __builtin_amdgcn_permlane32_swap(pa[0], pa[0], false, false);
        pv[i] = __uint_as_float(pb[0]);
        const f32x2 pp = {pv[i], pv[i]};
        const f32x2 d0 = gv[i][0][0] - pp, d1 = gv[i][0][1] - pp, d2 = gv[i][1][0] - pp, d3 = gv[i][1][1] - pp;
        const f32x2 s1 = (d0 + d1) + (d2 + d3);
        const f32x2 s2 = __builtin_elementwise_fma(d0, d0, d1 * d1) + __builtin_elementwise_fma(d2, d2, d3 * d3);
        sv[i][0] = s1[0] + s1[1]; sv[i][1] = s2[0] + s2[1];
      }
      const auto u0 = __builtin_amdgcn_permlane16_swap(__float_as_uint(sv[0][0]), __float_as_uint(sv[0][1]), false, false);
      const auto u1 = __builtin_amdgcn_permlane16_swap(__float_as_uint(sv[1][0]), __float_as_uint(sv[1][1]), false, false);
      const float w0 = __uint_as_float(u0[0]) + __uint_as_float(u0[1]), w1 = __uint_as_float(u1[0]) + __uint_as_float(u1[1]);
      const auto u2 = __builtin_amdgcn_permlane32_swap(__float_as_uint(w0), __float_as_uint(w1), false, false);
      float* sd = sStat + ((wm * 32 + ((g4 >> 1) ^ rsw) * 16 + l15) * 6 + cg) * 4 + (g4 & 1);
      sd[0] = __uint_as_float(u2[0]) + __uint_as_float(u2[1]);
      sd[2] = (g4 >> 1) ? pv[1] : pv[0];
      asm volatile("s_waitcnt lgkmcnt(0)" : "+v"(tr[0][0]), "+v"(tr[0][1]), "+v"(tr[1][0]), "+v"(tr[1][1]) :: "memory");
      char* zdst = (char*)z2 + (size_t)((rb * 2 + wm) * 48 + (cb - 4) * 12 + cg * 2) * 1024 + lane * 16;
      const bool tag = ((rb * 2 + wm) & 15) >= S0;
      u32x4 o[2];
#pragma unroll
      for (int jj = 0; jj < 2; ++jj) {
        o[jj] = __builtin_bit_cast(u32x4, join8(tr[jj][0], tr[jj][1]));
        ST16(zdst + jj * 1024, ((u32x4){o[jj][0], o[jj][1], o[jj][2], tag ? HOLE : o[jj][3]}), 2);
      }
      *(u32x2*)((char*)z2 + Z1X + ((rb * 2 + wm) * 24 + (cb - 4) * 6 + cg) * 512 + lane * 8) = (u32x2){o[0][3], o[1][3]};
    }
  };

  if (__builtin_expect(wave >= 4, 1)) {
    const int lw = wave - 4;
    const unsigned voA = (unsigned)((lw * 8 + (lane >> 3)) * (D_ * 2) + (((lane & 7) ^ (lane >> 3)) << 4));
    const unsigned voB = (unsigned)((lw * 8 + ((lane >> 2) & 7)) * (F2_ * 2) + (lane >> 5) * 64 + (((lane & 3) ^ ((lw * 2 + ((lane >> 4) & 1)) & 3)) << 4));
    const char* gA = (const char*)x + (size_t)row0 * (D_ * 2);
    const char* gB = (const char*)w_in + n0 * 2;
    const unsigned lbase = __builtin_amdgcn_readfirstlane(lds_addr(smem));
    auto issue = [&](int kt) {
      const unsigned st = lbase + (kt % 5) * L1_STAGE;
      asm volatile("s_mov_b32 m0, %2\n\ts_nop 0\n\tglobal_load_lds_dwordx4 %0, %1" :: "v"(voA), "s"(gA + kt * 128), "s"(st + lw * 1024) : "memory");
#pragma unroll
      for (int i = 0; i < 3; ++i)
        asm volatile("s_mov_b32 m0, %2\n\ts_nop 0\n\tglobal_load_lds_dwordx4 %0, %1"
                     :: "v"(voB), "s"(gB + (size_t)kt * (64 * F2_ * 2) + i * 128), "s"(st + L1_A_BYTES + (lw * 3 + i) * 1024) : "memory");
    };
#define LPRIO 2
    __builtin_amdgcn_s_setprio(LPRIO);
    issue(0);
    AK(1);
    __builtin_amdgcn_s_barrier();
    issue(1); issue(2);
    AK(2);
    const int ks0 = row0 + lw * 8, kd = cb * 80 + min(lane, 39) * 2;
    const int wfr = ((xcd >> 1) * 32 + idx) * 8 + lw, wfi = min(wfr, 959);
    u32x4 sw;
    const char* kvp[3];
#pragma unroll
    for (int i = 0; i < 3; ++i) {
      const int id = min(i * 64 + lane, 189), r = id / 10, c = id % 10;
      kvp[i] = (r < 11 ? (const char*)kin + (size_t)max(ks0 - 3 + r, 0) * (D_ * 2) : (const char*)vin + (size_t)(ks0 + r - 11) * (D_ * 2)) + cb * 160 + c * 16;
    }
    auto kvload = [&]() {
#pragma unroll
      for (int i = 0; i < 3; ++i)
        asm volatile("s_mov_b32 m0, %1\n\ts_nop 0\n\tglobal_load_lds_dwordx4 %0, off" :: "v"(kvp[i]), "s"(lbase + lw * 3072 + i * 1024) : "memory");
      asm volatile("s_mov_b32 m0, %2\n\ts_nop 0\n\tglobal_load_lds_dwordx4 %0, %1"
                   :: "v"((unsigned)((lane >> 1) * (D_ * 2) + (lane & 1) * 16)), "s"(w_out + (size_t)(wfi / 40) * 32 * D_ + (wfi % 40) * 16), "s"(lbase + 24576 + lw * 1024) : "memory");
      asm volatile("global_load_dwordx4 %0, %1, %2" : "=&v"(sw) : "v"(kd * 8), "s"(shift_k) : "memory");
    };
    auto womath = [&]() {
      const unsigned ra = lbase + 24576 + lw * 1024 + (8 * g4 + (l15 >> 2)) * 32 + 8 * (lane & 3);
      s16x4 lo = tr_asm<0>(ra), hi = tr_asm<128>(ra);
      asm volatile("s_waitcnt lgkmcnt(0)" : "+v"(lo), "+v"(hi) :: "memory");
      ST16((char*)woF + (size_t)wfi * 1024 + lane * 16, __builtin_bit_cast(u32x4, join8(lo, hi)), 4);
    };
    auto kvmath = [&]() {
      unsigned kk[11], vv[8];
      const char* kl = smem + lw * 3072 + min(lane, 39) * 4;
#pragma unroll
      for (int i = 0; i < 11; ++i) kk[i] = *(const unsigned*)(kl + i * 160);
#pragma unroll
      for (int i = 0; i < 8; ++i) vv[i] = *(const unsigned*)(kl + (11 + i) * 160);
      const f32x2 w0 = {lo16(sw[0]), lo16(sw[2])}, w1 = {hi16(sw[0]), hi16(sw[2])};
      const f32x2 w2 = {lo16(sw[1]), lo16(sw[3])}, w3 = {hi16(sw[1]), hi16(sw[3])};
      f32x2 kf[11], rr[8];
#pragma unroll
      for (int i = 0; i < 11; ++i) kf[i] = unpk((i < 3 && ks0 == 0) ? 0u : kk[i]);
#pragma unroll
      for (int i = 0; i < 8; ++i) {
        f32x2 a0 = kf[i] * w3;
        a0 = __builtin_elementwise_fma(kf[i + 1], w2, a0);
        a0 = __builtin_elementwise_fma(kf[i + 2], w1, a0);
        a0 = __builtin_elementwise_fma(kf[i + 3], w0, a0);
        rr[i] = unpk(pk2(a0)) * unpk(vv[i]);
      }
      u32x4 oa, ob;
#pragma unroll
      for (int e = 0; e < 4; ++e) { oa[e] = pack2(rr[2 * e][0], rr[2 * e + 1][0]); ob[e] = pack2(rr[2 * e][1], rr[2 * e + 1][1]); }
      if (lane < 40) {
        ST16(kvT + (size_t)kd * S_ + ks0, ((u32x4){oa[0], oa[1], oa[2], HOLE}), 8);
        ST16(kvT + (size_t)(kd + 1) * S_ + ks0, ((u32x4){ob[0], ob[1], ob[2], HOLE}), 8);
        *(u32x2*)((char*)kvT + KVX + kd * 1024 + ks0) = (u32x2){oa[3], ob[3]};
      }
    };
#define PFK 8
#define PFF 4
#define KVM 1
#define SPF 0
#define SP0 1
#define SPN 3
    unsigned long long pfs = 0;
    const char* spb = (const char*)w_sp + (size_t)(row0 + (SPF == 1 ? cb * 8 + lw : (idx & 3) * 16 + lw * 2)) * 4096;
    auto spf = [&](int i) {
      const int r = i & (SPF == 1 ? 0 : 1), k = SPF == 1 ? i : i >> 1;
      asm volatile("s_load_dwordx2 %0, %1, %2" : "+s"(pfs) : "s"(spb), "i"(r * 4096 + (k & 3) * 1024 + (k >> 2) * 256 + 124) : "memory");
    };
    constexpr int NPF = PFF == 1 ? 2 : 1;
    unsigned pfv, pfw = 0;
    auto prefetch = [&]() {
      const int t_ = tid - 256;
      if (PFF == 1) {
        const char* pw = (const char*)w_sp + (size_t)(row0 + (idx & 3) * 16 + (t_ >> 5)) * 4096 + (t_ & 31) * 128;
        const int t2 = t_ % 96;
        const char* pa = (const char*)ssm_k + (t2 < 64 ? (size_t)b * 8192 + t2 * 128 : (size_t)(512 + (b & 127)) * 4096 + (t2 & 31) * 128);
        asm volatile("global_load_dword %0, %1, off" : "=&v"(pfw) : "v"(pa) : "memory");
        asm volatile("global_load_dword %0, %1, off" : "=&v"(pfv) : "v"(pw) : "memory");
      } else {
        constexpr int NW = 64 * PFF;
        const int t2 = min(max(t_ - NW, 0), 95);
        const char* pw = (const char*)w_sp + (size_t)(row0 + (idx & 3) * 16 + (t_ / (4 * PFF)) % 16) * 4096 + ((t_ / PFF) & 3) * 1024 + (t_ % PFF) * 128;
        const char* pa = (const char*)ssm_k + (t2 < 64 ? (size_t)b * 8192 + t2 * 128 : (size_t)(512 + (b & 127)) * 4096 + (t2 & 31) * 128);
        const char* pc_ = (!SPF && t_ < NW) ? pw : pa;
        asm volatile("global_load_dword %0, %1, off" : "=&v"(pfv) : "v"(pc_) : "memory");
      }
    };
#pragma unroll
    for (int kt = 0; kt < 10; ++kt) {
      const int nv = (kt + 1 < 10 ? 4 : 0) + (kt + 2 < 10 ? 4 : 0) + (kt >= 8 ? 5 : 0) + ((PFK < kt && kt <= PFK + 3) ? NPF : 0);
      asm volatile("s_waitcnt vmcnt(%0)" :: "n"(nv) : "memory");
      __builtin_amdgcn_s_barrier();
      if (kt == 0) AK(3); if (kt == 1) AK(4); if (kt == 3) AK(5); if (kt == 5) AK(6); if (kt == 7) AK(7); if (kt == 9) AK(8);
      if (kt + 3 < 10) issue(kt + 3);
      if (kt == 7) kvload();
      if (kt == PFK) prefetch();
      if (SPF && kt >= SP0) {
#pragma unroll
        for (int i = (kt - SP0) * SPN; i < (kt - SP0 + 1) * SPN && i < 16 * SPF; ++i) spf(i);
      }
    }
    __builtin_amdgcn_s_setprio(0);
    if (KVM == 0) {
      asm volatile("s_waitcnt vmcnt(%1)" : "+v"(sw) : "n"(PFK >= 7 ? NPF : 0) : "memory");
      kvmath();
      if (!is_z2 && wfr < 960) womath();
    }
    SB();
    asm volatile("s_waitcnt lgkmcnt(0)" : "+s"(pfs) :: "memory");
    RAW_BARRIER();
    AK(9);
    {
      const int w = lw >> 1, gs = lw & 1;
      f32x4 t4[2][2];
#pragma unroll
      for (int q = 0; q < 4; ++q) t4[q >> 1][q & 1] = *(const f32x4*)(sHand + ((w * 2 + gs) * 4 + q) * 1024 + lane * 16);
      epi(t4, w >> 1, (w & 1) * 3 + 1 + gs, 0, 0);
    }
    AK(10);
    if (is_z2) RAW_BARRIER();
    AK(11);
    if (KVM == 1) {
      SB();
      if (is_z2) asm volatile("s_waitcnt vmcnt(%0)" :: "n"(2 + (PFK >= 7 ? NPF : 0)) : "memory");
      asm volatile("s_waitcnt vmcnt(%1)" : "+v"(sw) : "n"(3 + (PFK >= 7 ? NPF : 0)) : "memory");
      kvmath();
      if (!is_z2 && wfr < 960) womath();
    }
    AK(12);
    asm volatile("s_waitcnt vmcnt(0)" : "+v"(pfw), "+v"(pfv) :: "memory");
    AK(13);
    AK_OUT(14);
    RT(0, 1);
    return;
  }

  unsigned pcv, pfx;
  {
    unsigned long long pc; unsigned clen;
    asm volatile("s_getpc_b64 %0\n.Lpc_here_%=:\n\ts_mov_b32 %1, k_end-.Lpc_here_%=" : "=s"(pc), "=s"(clen));
    const unsigned o = idx < 2 ? min((unsigned)((idx * 4 + wave) * 8192 + lane * 128), clen - 4u) & ~3u : 0u;
    asm volatile("global_load_dword %0, %1, %2" : "=&v"(pcv) : "v"(o), "s"(pc) : "memory");
  }
  __builtin_amdgcn_s_barrier();
  auto pfx_issue = [&]() {
    const int t2 = max(tid - 80, 0), r2 = min(t2 / 3, 39);
    const char* pa = (const char*)x + (size_t)(row0 + (idx & 3) * 16 + tid / 5) * (D_ * 2) + 640 + (tid % 5) * 128;
    const char* pb = (const char*)w_in + (size_t)(320 + (idx >> 2) * 40 + r2) * (F2_ * 2) + n0 * 2 + (t2 % 3) * 128;
    const char* pc_ = tid < 80 ? pa : pb;
    asm volatile("global_load_dword %0, %1, off" : "=&v"(pfx) : "v"(pc_) : "memory");
  };
#define PFXM 0
  if (PFXM == 0) pfx_issue();
  if (PFXM == 2) pfx = 0;
  const int wm = wave >> 1, wn = wave & 1;
  f32x4 acc[2][6], acs[3], accS = {0.f, 0.f, 0.f, 0.f}, accG = {0.f, 0.f, 0.f, 0.f};
#pragma unroll
  for (int j = 0; j < 6; ++j) {
    acc[0][j] = (f32x4){0.f, 0.f, 0.f, 0.f};
    acc[1][j] = (f32x4){0.f, 0.f, 0.f, 0.f};
  }
#pragma unroll
  for (int j = 0; j < 3; ++j) acs[j] = (f32x4){0.f, 0.f, 0.f, 0.f};
  unsigned tb[2][2], aA[2][2];
#pragma unroll
  for (int c1 = 0; c1 < 2; ++c1)
#pragma unroll
    for (int h = 0; h < 2; ++h)
      tb[c1][h] = lds_addr(smem) + L1_A_BYTES + wn * 3 * 512 + boff<6>(8 * g4 + 4 * h + (l15 >> 2), 2 * (c1 ^ wm) + ((lane & 3) >> 1)) + 8 * (lane & 1);
#pragma unroll
  for (int ks = 0; ks < 2; ++ks)
#pragma unroll
    for (int i = 0; i < 2; ++i) aA[ks][i] = lds_addr(smem) + (wm * 32 + (i ^ wn) * 16 + l15) * 128 + (((ks * 4 + g4) ^ (l15 & 7)) << 4);
  bf16x8 ones;
#pragma unroll
  for (int e = 0; e < 8; ++e) ones[e] = (short)0x3F80;
  int cnt = 10, slot = 0, sdl;
  SB();
  __builtin_amdgcn_s_barrier();
  STAMP(0, 1);
  if (PFXM == 1) pfx_issue();
  asm volatile(
      "s_nop 1\n\t"
      "ds_read_b128 v[96:99], %21\n\t"
      "ds_read_b64_tr_b16 v[104:105], %17 offset:0\n\t"
      "ds_read_b64_tr_b16 v[106:107], %18 offset:0\n\t"
      "ds_read_b128 v[100:103], %22\n\t"
      "ds_read_b64_tr_b16 v[108:109], %19 offset:0\n\t"
      "ds_read_b64_tr_b16 v[110:111], %20 offset:0\n\t"
      "ds_read_b64_tr_b16 v[112:113], %17 offset:512\n\t"
      "ds_read_b64_tr_b16 v[114:115], %18 offset:512\n\t"
      "ds_read_b64_tr_b16 v[116:117], %19 offset:512\n\t"
      "ds_read_b64_tr_b16 v[118:119], %20 offset:512\n\t"
      "ds_read_b64_tr_b16 v[120:121], %17 offset:1024\n\t"
      "ds_read_b64_tr_b16 v[122:123], %18 offset:1024\n\t"
      "ds_read_b64_tr_b16 v[124:125], %19 offset:1024\n\t"
      "ds_read_b64_tr_b16 v[126:127], %20 offset:1024\n\t"
      ".Ll1_%=:\n\t"
      "s_waitcnt lgkmcnt(11)\n\t"
      "v_mfma_f32_16x16x32_bf16 %0, v[104:107], v[96:99], %0\n\t"
      "ds_read_b128 v[128:131], %23\n\t"
      "s_waitcnt lgkmcnt(11)\n\t"
      "v_mfma_f32_16x16x32_bf16 %6, v[104:107], v[100:103], %6\n\t"
      "ds_read_b64_tr_b16 v[136:137], %17 offset:12288\n\t"
      "s_waitcnt lgkmcnt(10)\n\t"
      "v_mfma_f32_16x16x32_bf16 %1, v[108:111], v[96:99], %1\n\t"
      "ds_read_b64_tr_b16 v[138:139], %18 offset:12288\n\t"
      "v_mfma_f32_16x16x32_bf16 %7, v[108:111], v[100:103], %7\n\t"
      "ds_read_b128 v[132:135], %24\n\t"
      "s_waitcnt lgkmcnt(10)\n\t"
      "v_mfma_f32_16x16x32_bf16 %2, v[112:115], v[96:99], %2\n\t"
      "ds_read_b64_tr_b16 v[140:141], %19 offset:12288\n\t"
      "v_mfma_f32_16x16x32_bf16 %8, v[112:115], v[100:103], %8\n\t"
      "ds_read_b64_tr_b16 v[142:143], %20 offset:12288\n\t"
      "s_waitcnt lgkmcnt(10)\n\t"
      "v_mfma_f32_16x16x32_bf16 %3, v[116:119], v[96:99], %3\n\t"
      "ds_read_b64_tr_b16 v[144:145], %17 offset:12800\n\t"
      "v_mfma_f32_16x16x32_bf16 %9, v[116:119], v[100:103], %9\n\t"
      "ds_read_b64_tr_b16 v[146:147], %18 offset:12800\n\t"
      "s_waitcnt lgkmcnt(10)\n\t"
      "v_mfma_f32_16x16x32_bf16 %4, v[120:123], v[96:99], %4\n\t"
      "ds_read_b64_tr_b16 v[148:149], %19 offset:12800\n\t"
      "v_mfma_f32_16x16x32_bf16 %10, v[120:123], v[100:103], %10\n\t"
      "ds_read_b64_tr_b16 v[150:151], %20 offset:12800\n\t"
      "s_waitcnt lgkmcnt(10)\n\t"
      "v_mfma_f32_16x16x32_bf16 %5, v[124:127], v[96:99], %5\n\t"
      "ds_read_b64_tr_b16 v[152:153], %17 offset:13312\n\t"
      "v_mfma_f32_16x16x32_bf16 %11, v[124:127], v[100:103], %11\n\t"
      "ds_read_b64_tr_b16 v[154:155], %18 offset:13312\n\t"
      "v_mfma_f32_16x16x32_bf16 %15, %28, v[96:99], %15\n\t"
      "ds_read_b64_tr_b16 v[156:157], %19 offset:13312\n\t"
      "v_mfma_f32_16x16x32_bf16 %16, v[96:99], v[96:99], %16\n\t"
      "ds_read_b64_tr_b16 v[158:159], %20 offset:13312\n\t"
      "v_mfma_f32_16x16x32_bf16 %12, v[104:107], %28, %12\n\t"
      "v_mfma_f32_16x16x32_bf16 %13, v[112:115], %28, %13\n\t"
      "v_mfma_f32_16x16x32_bf16 %14, v[120:123], %28, %14\n\t"
      "s_add_u32 %26, %26, 1\n\t"
      "s_cmp_eq_u32 %26, 5\n\t"
      "s_cselect_b32 %27, 0xfffd8000, 0\n\t"
      "s_cselect_b32 %26, 0, %26\n\t"
      "s_add_u32 %27, %27, 0x8000\n\t"
      "s_sub_u32 %25, %25, 1\n\t"
      "s_cmp_eq_u32 %25, 0\n\t"
      "s_cbranch_scc1 .Ll1s_%=\n\t"
      "s_barrier\n\t"
      ".Ll1s_%=:\n\t"
      "s_waitcnt lgkmcnt(11)\n\t"
      "v_mfma_f32_16x16x32_bf16 %0, v[136:139], v[128:131], %0\n\t"
      "v_add_u32 %21, %27, %21\n\t"
      "ds_read_b128 v[96:99], %21\n\t"
      "s_waitcnt lgkmcnt(11)\n\t"
      "v_mfma_f32_16x16x32_bf16 %6, v[136:139], v[132:135], %6\n\t"
      "v_add_u32 %17, %27, %17\n\t"
      "ds_read_b64_tr_b16 v[104:105], %17 offset:0\n\t"
      "s_waitcnt lgkmcnt(10)\n\t"
      "v_mfma_f32_16x16x32_bf16 %1, v[140:143], v[128:131], %1\n\t"
      "v_add_u32 %18, %27, %18\n\t"
      "ds_read_b64_tr_b16 v[106:107], %18 offset:0\n\t"
      "v_mfma_f32_16x16x32_bf16 %7, v[140:143], v[132:135], %7\n\t"
      "v_add_u32 %22, %27, %22\n\t"
      "ds_read_b128 v[100:103], %22\n\t"
      "s_waitcnt lgkmcnt(10)\n\t"
      "v_mfma_f32_16x16x32_bf16 %2, v[144:147], v[128:131], %2\n\t"
      "v_add_u32 %19, %27, %19\n\t"
      "ds_read_b64_tr_b16 v[108:109], %19 offset:0\n\t"
      "v_mfma_f32_16x16x32_bf16 %8, v[144:147], v[132:135], %8\n\t"
      "v_add_u32 %20, %27, %20\n\t"
      "ds_read_b64_tr_b16 v[110:111], %20 offset:0\n\t"
      "s_waitcnt lgkmcnt(10)\n\t"
      "v_mfma_f32_16x16x32_bf16 %3, v[148:151], v[128:131], %3\n\t"
      "v_add_u32 %23, %27, %23\n\t"
      "ds_read_b64_tr_b16 v[112:113], %17 offset:512\n\t"
      "v_mfma_f32_16x16x32_bf16 %9, v[148:151], v[132:135], %9\n\t"
      "v_add_u32 %24, %27, %24\n\t"
      "ds_read_b64_tr_b16 v[114:115], %18 offset:512\n\t"
      "s_waitcnt lgkmcnt(10)\n\t"
      "v_mfma_f32_16x16x32_bf16 %4, v[152:155], v[128:131], %4\n\t"
      "ds_read_b64_tr_b16 v[116:117], %19 offset:512\n\t"
      "v_mfma_f32_16x16x32_bf16 %10, v[152:155], v[132:135], %10\n\t"
      "ds_read_b64_tr_b16 v[118:119], %20 offset:512\n\t"
      "s_waitcnt lgkmcnt(10)\n\t"
      "v_mfma_f32_16x16x32_bf16 %5, v[156:159], v[128:131], %5\n\t"
      "ds_read_b64_tr_b16 v[120:121], %17 offset:1024\n\t"
      "v_mfma_f32_16x16x32_bf16 %11, v[156:159], v[132:135], %11\n\t"
      "ds_read_b64_tr_b16 v[122:123], %18 offset:1024\n\t"
      "v_mfma_f32_16x16x32_bf16 %15, %28, v[128:131], %15\n\t"
      "ds_read_b64_tr_b16 v[124:125], %19 offset:1024\n\t"
      "v_mfma_f32_16x16x32_bf16 %16, v[128:131], v[128:131], %16\n\t"
      "ds_read_b64_tr_b16 v[126:127], %20 offset:1024\n\t"
      "v_mfma_f32_16x16x32_bf16 %12, v[136:139], %28, %12\n\t"
      "v_mfma_f32_16x16x32_bf16 %13, v[144:147], %28, %13\n\t"
      "v_mfma_f32_16x16x32_bf16 %14, v[152:155], %28, %14\n\t"
      "s_cmp_lg_u32 %25, 0\n\t"
      "s_cbranch_scc1 .Ll1_%=\n\t"
      "s_waitcnt lgkmcnt(0)\n\t"
      "s_nop 15\n\t"
      "s_nop 7\n\t"
      : "+v"(acc[0][0]), "+v"(acc[0][1]), "+v"(acc[0][2]), "+v"(acc[0][3]), "+v"(acc[0][4]), "+v"(acc[0][5]),
        "+v"(acc[1][0]), "+v"(acc[1][1]), "+v"(acc[1][2]), "+v"(acc[1][3]), "+v"(acc[1][4]), "+v"(acc[1][5]),
        "+v"(acs[0]), "+v"(acs[1]), "+v"(acs[2]), "+v"(accS), "+v"(accG), "+v"(tb[0][0]), "+v"(tb[0][1]), "+v"(tb[1][0]), "+v"(tb[1][1]),
        "+v"(aA[0][0]), "+v"(aA[0][1]), "+v"(aA[1][0]), "+v"(aA[1][1]), "+s"(cnt), "+s"(slot), "=&s"(sdl)
      : "v"(ones)
      : "memory", "scc", "v96", "v97", "v98", "v99", "v100", "v101", "v102", "v103", "v104", "v105", "v106", "v107", "v108", "v109", "v110", "v111", "v112", "v113", "v114", "v115", "v116", "v117", "v118", "v119", "v120", "v121", "v122", "v123", "v124", "v125", "v126", "v127", "v128", "v129", "v130", "v131", "v132", "v133", "v134", "v135", "v136", "v137", "v138", "v139", "v140", "v141", "v142", "v143", "v144", "v145", "v146", "v147", "v148", "v149", "v150", "v151", "v152", "v153", "v154", "v155", "v156", "v157", "v158", "v159");
  STAMP(0, 3);
  AK(8);
#define PFC 0
  unsigned pfc = 0;
  auto pfc_issue = [&]() {
    const char* pw = (const char*)w_sp + (size_t)(row0 + (idx & 3) * 16 + (tid >> 4)) * 4096 + ((tid >> 2) & 3) * 1024 + (4 + (tid & 3)) * 128;
    asm volatile("global_load_dword %0, %1, off" : "=&v"(pfc) : "v"(pw) : "memory");
  };
  if (PFC == 1) pfc_issue();

#pragma unroll
  for (int gs = 0; gs < 2; ++gs)
#pragma unroll
    for (int q = 0; q < 4; ++q)
      *(f32x4*)(sHand + ((wave * 2 + gs) * 4 + (q ^ wm ^ (2 * wn))) * 1024 + lane * 16) = acc[q >> 1][2 + 2 * gs + (q & 1)];
  if (l15 == 0) {
#pragma unroll
    for (int j = 0; j < 3; ++j) *(f32x4*)(sCs + wn * 96 + (2 * j + wm) * 16 + 4 * g4) = acs[j];
  }
  {
    const int r = wm * 32 + wn * 16 + l15;
    const bool dg = g4 == (l15 >> 2);
    const float mean = accS[0] * (1.0f / 640.0f);
    const float s2 = (l15 & 2) ? ((l15 & 1) ? accG[3] : accG[2]) : ((l15 & 1) ? accG[1] : accG[0]);
    float sq = s2 - accS[0] * mean;
    if (__builtin_expect(__builtin_amdgcn_ballot_w64(dg && !(sq >= 1e-3f * accS[0] * mean)) != 0, 0)) {
      const f32x2 mm = {mean, mean};
      f32x2 q2 = {0.f, 0.f};
      const u32x4* xp = (const u32x4*)(x + (size_t)(row0 + r) * D_ + g4 * 160);
#pragma unroll 1
      for (int c = 0; c < 20; ++c) {
        const u32x4 xv = xp[c];
#pragma unroll
        for (int e = 0; e < 4; ++e) { const f32x2 d = unpk(xv[e]) - mm; q2 = __builtin_elementwise_fma(d, d, q2); }
      }
      sq = allred_g(q2[0] + q2[1]);
    }
    const float mbf = rbf(mean);
    const float vbf = rbf(fmaxf(sq, 0.f) * (1.0f / 640.0f));
    const float den = rbf(__builtin_amdgcn_sqrtf(rbf(vbf + 1e-5f)));
    const float rden = __builtin_amdgcn_rcpf(den);
    if (dg) {
      sRow[r] = make_float2(mbf, rden);
      if (cb == 0) xst[row0 + r] = make_float2(mbf, rden);
    }
  }
  TS_DECL;
  TS(0);
  AK(7);
  RAW_BARRIER();
  AK(9);
  TS(1);
  if (PFC == 2) pfc_issue();
  {
    f32x4 t4[2][2];
#pragma unroll
    for (int q = 0; q < 4; ++q) t4[q >> 1][q & 1] = acc[q >> 1][q & 1];
    epi(t4, wm, wn * 3, wm, wn);
  }
  TS(2); AK(10);
  if (is_z2) RAW_BARRIER();
  TS(3); AK(11);
  TS(4);
  if (is_z2 && tid < 64) {
    float mc[6];
    float sum = 0.f, m2 = 0.f;
#pragma unroll
    for (int c = 0; c < 6; ++c) {
      const f32x4 pp = *(const f32x4*)(sStat + (tid * 6 + c) * 4);
      const float dl = pp[0] * (1.0f / 32.0f);
      mc[c] = pp[2] + dl;
      sum += mc[c];
      m2 += fmaxf(pp[1] - pp[0] * dl, 0.f);
    }
    const float mb = sum * (1.0f / 6.0f);
    float dq = 0.f;
#pragma unroll
    for (int c = 0; c < 6; ++c) { const float dd = mc[c] - mb; dq += dd * dd; }
    st2[(size_t)(cb - 4) * S_ + row0 + tid] = make_float2(mb, m2 + 32.0f * dq);
  }
  TS(5); AK(12);
  asm volatile("s_waitcnt vmcnt(0)" : "+v"(pcv), "+v"(pfx), "+v"(pfc) :: "memory");
  TS(6); AK(13);
  AK_OUT(14);
  TS_OUT(0, 8, 7, TSW);
  STAMP(0, 4);
  RT(0, 1);
}

constexpr int CV_C = 4608, CV_CS = 4160, CV_BUF = CV_C + 2 * CV_CS;
__device__ __forceinline__ void conv_core(unsigned buf, int lane, f32x4 (&ac)[8]) {
  const int l15 = lane & 15, g4 = lane >> 4;
  const int c = (l15 + 1) & 1;
  unsigned aa[4];
#pragma unroll
  for (int k = 0; k < 4; ++k) aa[k] = buf + CV_C + c * CV_CS + (2031 - l15 + 8 * g4 - c) * 2 - 512 - 1024 * k;
  unsigned bb = buf + 512 + 32 * l15 - 32 + 16 * g4;
  u32x2 A[2][8][2];
  u32x4 B[2][8];
  constexpr int TT[36] = {7, 6, 5, 4, 7, 6, 5, 4, 7, 6, 5, 3, 7, 6, 5, 4, 7, 6, 3, 2, 7, 6, 5, 4, 7, 3, 2, 1, 7, 6, 5, 4, 3, 2, 1, 0};
  constexpr int AA[36] = {0, 0, 0, 0, 1, 1, 1, 1, 2, 2, 2, 0, 3, 3, 3, 2, 4, 4, 1, 0, 5, 5, 4, 3, 6, 2, 1, 0, 7, 6, 5, 4, 3, 2, 1, 0};
  auto ld1 = [&](int p, int n) {
    if (n < 16) {
      const int a = n >> 1, o = ((a & 1) ? 0 : 128) + 2 * (n & 1);
      asm volatile("ds_read2_b32 %0, %1 offset0:%2 offset1:%3" : "=&v"(A[p][(n >> 1) & 7][n & 1]) : "v"(aa[(a >> 1) & 3]), "i"(o), "i"(o + 1) : "memory");
    } else
      asm volatile("ds_read_b128 %0, %1 offset:%2" : "=&v"(B[p][n & 7]) : "v"(bb), "i"(512 * (n & 7)) : "memory");
  };
  auto next = [&](int dl) {
#pragma unroll
    for (int k = 0; k < 4; ++k) aa[k] += dl;
    bb += dl;
    SB();
  };
  auto land = [&](int p) {
    asm volatile("s_waitcnt lgkmcnt(0)" : "+v"(A[p][0][0]), "+v"(A[p][0][1]), "+v"(A[p][1][0]), "+v"(A[p][1][1]), "+v"(A[p][2][0]), "+v"(A[p][2][1]),
                 "+v"(A[p][3][0]), "+v"(A[p][3][1]), "+v"(A[p][4][0]), "+v"(A[p][4][1]), "+v"(A[p][5][0]), "+v"(A[p][5][1]), "+v"(A[p][6][0]), "+v"(A[p][6][1]),
                 "+v"(A[p][7][0]), "+v"(A[p][7][1]), "+v"(B[p][0]), "+v"(B[p][1]), "+v"(B[p][2]), "+v"(B[p][3]), "+v"(B[p][4]), "+v"(B[p][5]),
                 "+v"(B[p][6]), "+v"(B[p][7]) :: "memory");
    SB();
  };
  auto mm = [&](int p) {
#pragma unroll
    for (int n = 0; n < 36; ++n) {
      const int t = TT[n], a = AA[n];
      const u32x4 av = {A[p][a][0][0], A[p][a][0][1], A[p][a][1][0], A[p][a][1][1]};
      asm volatile("v_mfma_f32_16x16x32_bf16 %0, %1, %2, %0" : "+v"(ac[t]) : "v"(av), "v"(B[p][t - a]));
      if (n < 24) ld1(p ^ 1, n);
      SB();
    }
    land(p ^ 1);
  };
#pragma unroll
  for (int t = 0; t < 8; ++t) ac[t] = (f32x4){0.f, 0.f, 0.f, 0.f};
#pragma unroll
  for (int n = 0; n < 24; ++n) ld1(0, n);
  land(0);
#pragma unroll 1
  for (int r = 0; r < (XE & 1 ? 0 : 8); r += 2) {
    next(-64);
    mm(0);
    next(r < 6 ? -64 : 0);
    mm(1);
  }
  asm volatile("s_nop 15" : "+v"(ac[0]), "+v"(ac[1]), "+v"(ac[2]), "+v"(ac[3]), "+v"(ac[4]), "+v"(ac[5]), "+v"(ac[6]), "+v"(ac[7]));
}

#define GTB 1
#define PMAP 1
#define GNL 0
#define PSL "1"
#define LP3 3
#define HPL 1
#define HSC ""
#define XSRC hf
#define CDR 0
#define SPR 1
#define DLY -1
#define PSLN 2
#define NUP 2
#define SGS 13
#define CVT 4u
#define BARP 0
#define XNS S0
#define CLP 1
#define NRP 0
#define CVW 0
#define ROT 0
#define XAM 7
#define XBM 15
#define YMP 3
#define YEX 1
#define ANT ""
#define BNT ""
#define XAS 128
#define TBL 1
#define PW0 4
#define WPF 0
#define ZSL 0
#define ZS2 0
#define PPF 4
#define WTA 16
#define NW1 10
#define NW0 0
constexpr int M_FLAG = 0;
constexpr int M_RT = 128;
constexpr int M_MT = M_RT + 8192;
constexpr int M_CV = M_MT + 4096;
constexpr int M_RING = M_CV + 3 * CV_BUF;
constexpr int P3_C = 128;
constexpr int P3_G = P3_C + 72 * 1024;
constexpr int P3_Y = M_RING + 4 * 24576;
constexpr int M_LDS = P3_Y + 8 * 1536;
static_assert(P3_G + 68 * 1024 <= P3_Y && M_LDS <= 163840 && M_RING <= P3_G, "LDS map");
#define GAS __attribute__((address_space(1)))
__device__ __forceinline__ void flag_publish(unsigned* f) { __hip_atomic_store((GAS unsigned*)f, 1u, __ATOMIC_RELAXED, __HIP_MEMORY_SCOPE_AGENT); }
__device__ __forceinline__ unsigned umx(unsigned a, unsigned b) { return a > b ? a : b; }
__device__ __forceinline__ unsigned max4(const u32x4& v, unsigned m) { return umx(umx(umx(m, v[0]), v[1]), umx(v[2], v[3])); }
__device__ __forceinline__ unsigned nohole(unsigned a) { return a < HOLE - 1 ? a : HOLE - 1; }

__device__ __forceinline__ void row_stat(const float (&mj)[4], const float (&qj)[4], float& mean, float& r) {
  mean = 0.25f * ((mj[0] + mj[1]) + (mj[2] + mj[3]));
  const float d0 = mj[0] - mean, d1 = mj[1] - mean, d2 = mj[2] - mean, d3 = mj[3] - mean;
  const float m2 = (qj[0] + qj[1]) + (qj[2] + qj[3]) + 192.0f * ((d0 * d0 + d1 * d1) + (d2 * d2 + d3 * d3));
  const float vbf = rbf(m2 * (1.0f / 768.0f));
  r = __builtin_amdgcn_rcpf(rbf(__builtin_amdgcn_sqrtf(rbf(vbf + 1e-5f))));
}
struct NormLd { u32x3 z[6]; u32x2 x[3]; f32x2 p[4]; };
__device__ __forceinline__ void norm_load(NormLd& n, const u16* z2f, const float2* st2, int ks, int cb, int lane) {
#pragma unroll
  for (int j = 0; j < 4; ++j) n.p[j] = *(const f32x2*)(st2 + j * S_ + ks * 32 + (lane >> 4) * 8 + (lane & 7));
#pragma unroll
  for (int j = 0; j < 6; ++j) n.z[j] = *(const u32x3*)((const char*)z2f + (ks * 48 + cb * 6 + j) * 1024 + lane * 16);
#pragma unroll
  for (int j = 0; j < 3; ++j) n.x[j] = *(const u32x2*)((const char*)z2f + Z1X + (ks * 24 + cb * 3 + j) * 512 + lane * 8);
  SB();
}
template <int E> __device__ __forceinline__ f32x2 bc2(float v) { return (f32x2){dppf<0x150 + 2 * E>(v), dppf<0x151 + 2 * E>(v)}; }
__device__ __forceinline__ void norm_store(const NormLd& n, u16* z2f, int ks, int cb, int lane) {
  const float mj[4] = {n.p[0][0], n.p[1][0], n.p[2][0], n.p[3][0]}, qj[4] = {n.p[0][1], n.p[1][1], n.p[2][1], n.p[3][1]};
  float mean, r;
  row_stat(mj, qj, mean, r);
  mean = rbf(mean);
  const f32x2 me[4] = {bc2<0>(mean), bc2<1>(mean), bc2<2>(mean), bc2<3>(mean)}, re[4] = {bc2<0>(r), bc2<1>(r), bc2<2>(r), bc2<3>(r)};
  const auto zrs = __builtin_amdgcn_make_buffer_rsrc(z2f, 0, S_ * FH_ * 2, 0x00020000);
#pragma unroll
  for (int j = 0; j < 6; ++j) {
    u32x4 o;
#pragma unroll
    for (int e = 0; e < 4; ++e) o[e] = pk2((unpk(e < 3 ? n.z[j][e < 3 ? e : 0] : n.x[j >> 1][j & 1]) - me[e]) * re[e]);
    o[3] = nohole(o[3]);
    __builtin_amdgcn_raw_buffer_store_b128(o, zrs, (ks * 48 + cb * 6 + j) * 1024 + lane * 16, 0, WTA);
  }
}
__global__ __launch_bounds__(64) void k_n(u16* z2f, const float2* __restrict__ st2) {
  const int ks = blockIdx.x >> 3;
  if ((ks & 15) < S0) return;
  NormLd n;
  norm_load(n, z2f, st2, ks, blockIdx.x & 7, threadIdx.x);
  norm_store(n, z2f, ks, blockIdx.x & 7, threadIdx.x);
}

__global__ __launch_bounds__(512) void k_m(
    const u16* __restrict__ w_sp, const u16* __restrict__ b_sp, u16* hf,
    u16* z2f, const float2* __restrict__ st2, u16* yT,
    const u16* __restrict__ ssm_k, const u16* __restrict__ ssm_D, const u16* __restrict__ woF,
    const u16* __restrict__ x, const float2* __restrict__ xst, u16* __restrict__ out) {
  extern __shared__ __attribute__((aligned(16))) char smem[];
  const int tid = threadIdx.x, lane = tid & 63, wave = __builtin_amdgcn_readfirstlane(tid >> 6);
  const int b = blockIdx.x;
  const int xcd = b & 7, idx = b >> 3;
  const int rb = 8 * (xcd >> 1) + (idx >> 2), cb = 4 * (xcd & 1) + (idx & 3);
  const int pw = (wave + ROT * cb) & 7;
  const int t0 = rb * 64;
  const int g4 = lane >> 4, l15 = lane & 15;
  STAMP(2, 0);
  RT(1, 0);
  AB_DECL;
  AB(0);
  const unsigned flg = lds_addr(smem) + M_FLAG;
  bf16x8 Wq[15];
  const char* wg = (const char*)woF + (size_t)(pw * 120 + cb * 5) * 1024;
  const int mt = wave & 3, kh = wave >> 2, d0 = cb * 80;
  const int row = t0 + mt * 16 + l15;
  const size_t xo = (size_t)row * D_ + d0;
  u32x2 xv[3], st;
  const auto hrs = __builtin_amdgcn_make_buffer_rsrc(hf, 0, S_ * FH_ * 2, 0x00020000);
  auto ldP1 = [&](int n) {
    if (n < 15)
      asm volatile("global_load_dwordx4 %0, %1, %2 offset:%3" : "=&v"(Wq[n < 15 ? n : 0]) : "v"((unsigned)(lane * 16)), "s"(wg + (n / 5) * 40960 + 2048), "i"((n % 5) * 1024 - 2048) : "memory");
    else if (n < 18) {
      const int q = n < 18 ? n - 15 : 0;
      asm volatile("global_load_dwordx2 %0, %1, %2 offset:%3" : "=&v"(xv[q]) : "v"((unsigned)(l15 * D_ * 2 + 8 * g4)),
                   "s"(x + (size_t)(t0 + mt * 16) * D_ + d0 + (q < 2 ? 32 * kh : 64)), "i"(q == 1 ? 32 : 0) : "memory");
    } else
      asm volatile("global_load_dwordx2 %0, %1, %2" : "=&v"(st) : "v"((unsigned)(l15 * 8)), "s"(xst + t0 + mt * 16) : "memory");
  };
  int tmo = 1 << 16;
  u32x4 yv[2];
  auto yoff = [&](int n) {
    const int c = n ? 64 + (lane & 31) : lane, r = c >> 1;
    return (unsigned)(((r < 32 ? 32 * kh + r : 32 + r) * S_ + 8 * (c & 1)) * 2);
  };
  const u16* const yb = yT + (size_t)d0 * S_ + t0 + 16 * mt;
  auto ldy0 = [&]() {
#pragma unroll
    for (int n = 0; n < 2; ++n) asm volatile("global_load_dwordx4 %0, %1, %2 " HSC : "=&v"(yv[n]) : "v"(yoff(n)), "s"(yb) : "memory");
  };
  auto signal1 = [&](unsigned a) {
    const unsigned one = 1u;
    asm volatile("s_mov_b64 exec, 1\n\tds_add_u32 %0, %1\n\ts_mov_b64 exec, -1" :: "v"(a), "v"(one) : "memory");
  };
  auto drain = [&](int nc) {
    SB();
    asm volatile("s_waitcnt vmcnt(%21)"  : "+v"(Wq[0]), "+v"(Wq[1]), "+v"(Wq[2]), "+v"(Wq[3]), "+v"(Wq[4]), "+v"(Wq[5]), "+v"(Wq[6]), "+v"(Wq[7]),
                 "+v"(Wq[8]), "+v"(Wq[9]), "+v"(Wq[10]), "+v"(Wq[11]), "+v"(Wq[12]), "+v"(Wq[13]), "+v"(Wq[14]),
                 "+v"(xv[0]), "+v"(xv[1]), "+v"(xv[2]), "+v"(st), "+v"(yv[0]), "+v"(yv[1]) : "i"(nc) : "memory");
  };
  auto drain1 = [&](int nc) {
    SB();
#pragma unroll
    for (int n = 0; n < NW1; ++n) asm volatile("s_waitcnt vmcnt(%1)" : "+v"(Wq[n]) : "i"(nc) : "memory");
  };
  const int cw = wave & 3;
  const unsigned pfo = (unsigned)((lane >> 4) * 4096 + ((lane >> 2) & 3) * 1024 + (4 + (lane & 3)) * 128);
  f32x4 sp[4][2];
  auto ldst = [&](int q) {
#pragma unroll
    for (int j = 0; j < 4; ++j)
      asm volatile("global_load_dwordx4 %0, %1, %2 offset:%3" : "=&v"(sp[j][q]) : "v"((unsigned)(lane * 16)), "s"(st2 + j * S_ + cw * 512), "i"(q * 1024) : "memory");
    SB();
  };
  auto table = [&](int q, int nv) {
    asm volatile("s_waitcnt vmcnt(%4)" : "+v"(sp[0][q]), "+v"(sp[1][q]), "+v"(sp[2][q]), "+v"(sp[3][q]) : "i"(nv) : "memory");
    float mv[2], rv[2];
#pragma unroll
    for (int h = 0; h < 2; ++h) {
      const float mj[4] = {sp[0][q][2 * h], sp[1][q][2 * h], sp[2][q][2 * h], sp[3][q][2 * h]};
      const float qj[4] = {sp[0][q][2 * h + 1], sp[1][q][2 * h + 1], sp[2][q][2 * h + 1], sp[3][q][2 * h + 1]};
      row_stat(mj, qj, mv[h], rv[h]);
    }
    *(f32x2*)(smem + M_RT + cw * 2048 + q * 512 + lane * 8) = (f32x2){rv[0], rv[1]};
    *(unsigned*)(smem + M_MT + cw * 1024 + q * 256 + lane * 4) = pack2(mv[0], mv[1]);
    if (!GTB || q) flag_signal(flg + 16 + cw * 4, lane);
    SB();
  };
  if (wave >= 4) {
    TS_DECL;
    auto pre = [&]() {
      ldst(0);
      if (TBL) ldst(1);
      __builtin_amdgcn_s_barrier();
      SB();
      if (!TBL) ldst(1);
    };
    TS(0);
    if (cw < (b < 128 ? 3 : 2)) {
      if (!GTB) pre();
      const int d = cw < 2 ? 2 * b + cw : 512 + b;
      char* cb_ = smem + M_CV + cw * CV_BUF;
      u32x4 hc[4], uc[4];
      unsigned hp[4], ux[4];
      float Dv;
      auto ldc = [&]() {
#pragma unroll
        for (int k = 0; k < 4; ++k) {
          const int a = k * 64 + lane;
          hc[k] = *(const u32x4*)(ssm_k + (size_t)d * S_ + a * 8);
          hp[k] = *(const unsigned*)(ssm_k + (size_t)d * S_ + max(a * 8 - 2, 0));
          uc[k] = *(const u32x4*)(yT + (size_t)d * S_ + a * 8);
          ux[k] = *(const unsigned*)((const char*)yT + KVX + (d >> 1) * 2048 + a * 8 + (d & 1) * 4);
        }
        Dv = bf2f(ssm_D[d]);
        SB();
      };
      ldst(1);
      if (GTB == 1) ldc();
      ABS(1);
      __builtin_amdgcn_s_barrier();
      SB();
      ABS(2);
      if (GTB == 2) ldc();
      ABS(3);
      table(1, 16);
      ABS(4);
      TS(1); AB(1);
      if (lane < 36) *(u32x4*)(cb_ + (lane < 32 ? lane * 16 : CV_C + ((lane >> 1) & 1) * CV_CS + 4096 + (lane & 1) * 16)) = (u32x4){0u, 0u, 0u, 0u};
#pragma unroll
      for (int k = 0; k < 4; ++k) {
        const int a = k * 64 + lane;
        const unsigned w[5] = {a ? hp[k] : 0u, hc[k][0], hc[k][1], hc[k][2], hc[k][3]};
        u32x4 sw, mx;
#pragma unroll
        for (int i = 0; i < 4; ++i) {
          sw[i] = (w[4 - i] >> 16) | (w[4 - i] << 16);
          mx[i] = (w[4 - i] & 0xFFFFu) | (w[3 - i] & 0xFFFF0000u);
        }
        char* cp = cb_ + CV_C + (2040 - 8 * a) * 2;
        *(u32x4*)(cp) = sw;
        *(u32x4*)(cp + CV_CS) = mx;
        *(u32x4*)(cb_ + 512 + a * 16) = (u32x4){uc[k][0], uc[k][1], uc[k][2], ux[k]};
      }
      TS(2); AB(2); ABS(5);
      f32x4 ac[8];
      conv_core(lds_addr(cb_), lane, ac);
      AB(1);
      const auto yrs = __builtin_amdgcn_make_buffer_rsrc(yT, 0, D_ * S_ * 2, 0x00020000);
      u32x2 kvp[8];
#pragma unroll
      for (int t = 0; t < 8; ++t) kvp[t] = *(const u32x2*)(cb_ + 512 + (256 * t + 16 * l15 + 4 * g4) * 2);
#pragma unroll
      for (int k = 0; k < 4; ++k) {
        u32x2 ov[2];
#pragma unroll
        for (int t = 0; t < 2; ++t)
#pragma unroll
          for (int h = 0; h < 2; ++h) {
            const unsigned cv = pack2(ac[2 * k + t][2 * h], ac[2 * k + t][2 * h + 1]);
            const unsigned kd = pack2(lo16(kvp[2 * k + t][h]) * Dv, hi16(kvp[2 * k + t][h]) * Dv);
            ov[t][h] = pack2(lo16(cv) + lo16(kd), hi16(cv) + hi16(kd));
          }
        const auto s0 = __builtin_amdgcn_permlane16_swap(ov[0][0], ov[1][0], false, false);
        const auto s1 = __builtin_amdgcn_permlane16_swap(ov[0][1], ov[1][1], false, false);
        const u32x4 o4 = {s0[0], s1[0], s0[1], nohole(s1[1])};
        __builtin_amdgcn_raw_buffer_store_b128(o4, yrs, (d * S_ + 256 * (2 * k + (g4 & 1)) + 16 * l15 + 8 * (g4 >> 1)) * 2, 0, WTA);
      }
    } else {
      const int ks = 2 * (rb & 24) + (cw == 3 ? 8 : 0) + (rb & 7);
      NormLd nl;
      ldst(1);
      if (GNL) norm_load(nl, z2f, st2, ks, cb, lane);
      SB();
      __builtin_amdgcn_s_barrier();
      SB();
      if (!GNL) norm_load(nl, z2f, st2, ks, cb, lane);
      unsigned pf = 0;
      auto pfw = [&]() {
#pragma unroll
        for (int n = 0; n < 4; ++n)
          asm volatile("global_load_dword %0, %1, %2" : "+v"(pf) : "v"(pfo), "s"(w_sp + (size_t)(t0 + (idx & 3) * 16 + n * 4) * S_) : "memory");
        SB();
      };
      if (PPF == 1) pfw();
      if (!GTB && TBL != 2) table(0, 17);
      if (PPF == 2) pfw();
      if (NRP == 0) table(1, 13);
      if (PPF == 3) pfw();
      AB(1);
      if (out && (ks & 15) >= S0) norm_store(nl, z2f, ks, cb, lane);
      SB();
      if (NRP == 1) table(1, 0);
      if (PPF == 4 && cw == 3) pfw();
      __builtin_amdgcn_s_waitcnt(0x0F70);
      asm volatile("" : "+v"(pf));
      AB(2);
    }
    SB();
    TS(3); TS_OUT(2, 8, 4, 4); AB(3);
    if (!out) { RT(1, 1); return; }
    WAIT_LGKM0();
    flag_signal(flg + 40, lane);
    flag_wait(flg + 44, CVT);
    AB(4);
#pragma unroll
    for (int n = 0; n < NW1; ++n) ldP1(n);
    drain1(0);
  } else {
  if (tid < 32) *(unsigned*)(smem + M_FLAG + tid * 4) = 0u;
  const u16* Ab = w_sp + (size_t)t0 * S_ + wave * 512;
  const char* Bs = (const char*)z2f + (size_t)(wave * 16 * 48 + cb * 6) * 1024 + 2048;
  const unsigned boff = (unsigned)(lane * 16);
  char* ring = smem + M_RING + wave * 24576;
  const unsigned ringl = __builtin_amdgcn_readfirstlane(lds_addr(ring));
  const unsigned aoff = (unsigned)((lane >> 3) * 4096 + (((lane & 7) ^ (lane >> 3)) << 4));
  bf16x8 Bf[3][6];
  TS_DECL;
  auto ldA1 = [&](auto dc, int i) {
    constexpr int d = decltype(dc)::value;
    const unsigned ao = (XE & 4) && d >= 2 ? aoff & 0x70u : aoff, rl = ringl;
    const char* ab = (const char*)Ab + ((wave + 1 + (i >> 1)) & 3) * 65536;
    asm volatile("s_mov_b32 m0, %2\n\ts_nop 0\n\tglobal_load_lds_dwordx4 %0, %1 " ANT
                 :: "v"(ao), "s"(ab + ((i & 1) * 32768 + (d & XAM) * XAS)), "s"(rl + (d % 3) * 8192 + i * 1024) : "memory");
  };
  auto ldA = [&](auto dc) {
#pragma unroll
    for (int i = 0; i < 8; ++i) ldA1(dc, i);
  };
  auto ldB1 = [&](auto sc, int j) {
    constexpr int s = decltype(sc)::value;
    bf16x8& B = Bf[s % 3][j];
    const unsigned bo = (XE & 2) && s >= 3 ? boff & 0x70u : boff;
    const char* bs = Bs + (size_t)(s & XBM) * 49152;
    asm volatile("global_load_dwordx4 %0, %1, %2 offset:%3 " BNT : "=&v"(B) : "v"(bo), "s"(bs), "i"(j * 1024 - 2048) : "memory");
  };
  auto ldB = [&](auto sc) {
#pragma unroll
    for (int j = 0; j < 6; ++j) ldB1(sc, j);
  };
  if (GTB) ldst(0);
  ldA(IC(0)); ldB(IC(0));
  if (BARP == 0) RAW_BARRIER();
  if (BARP == 3) table(0, 14);
  ldB(IC(1));
  if (BARP == 4) table(0, 20);
  ldA(IC(1));
  unsigned pfg = 0;
  if (PPF == 6)
    asm volatile("global_load_dword %0, %1, %2" : "=&v"(pfg) : "v"(pfo), "s"(w_sp + (size_t)(t0 + (idx & 3) * 16 + wave * 4) * S_) : "memory");
  if (BARP == 1) RAW_BARRIER();
  TS(6);
  if (GTB) { if (BARP < 3) table(0, 28 + (PPF == 6)); }
  else flag_wait_asm(flg + 16 + wave * 4, 1u);
  if (BARP >= 2) RAW_BARRIER();
  f32x4 acc[4][6], cacc[4];
  const unsigned rta = lds_addr(smem) + M_RT + (wave * 512 + g4 * 8) * 4;
  const unsigned mta = lds_addr(smem) + M_MT + (wave * 512 + g4 * 8) * 2;
  unsigned ara[2];
#pragma unroll
  for (int ks = 0; ks < 2; ++ks) ara[ks] = lds_addr(ring) + l15 * 128 + (((ks * 4 + g4) ^ (l15 & 7)) << 4);
  u32x4 af[4];
  f32x4 rT[2][2]; u32x4 mT[2];
  auto rdA = [&](auto sc, int i) {
    constexpr int s = decltype(sc)::value;
    u32x4& o = af[i];
    const unsigned ad = ara[s & 1];
    asm volatile("ds_read_b128 %0, %1 offset:%2" : "=&v"(o) : "v"(ad), "i"(((s >> 1) % 3) * 8192 + i * 2048) : "memory");
  };
  auto rdT = [&](auto sc) {
    constexpr int s = decltype(sc)::value;
    f32x4 &o0 = rT[s & 1][0], &o1 = rT[s & 1][1]; u32x4& om = mT[s & 1];
    const unsigned ra = rta, ma = mta;
    asm volatile("ds_read_b128 %0, %1 offset:%2" : "=&v"(o0) : "v"(ra), "i"(s * 128) : "memory");
    asm volatile("ds_read_b128 %0, %1 offset:%2" : "=&v"(o1) : "v"(ra), "i"(s * 128 + 16) : "memory");
    asm volatile("ds_read_b128 %0, %1 offset:%2" : "=&v"(om) : "v"(ma), "i"(s * 64) : "memory");
  };
  constexpr int NSC = 20, NPM = 3;
  float tl, th;
  auto scop = [&](int i, int p, int n) {
    const int e = n / 5, k = n % 5;
    if (k == 0) tl = lo16(af[i][e]);
    else if (k == 1) th = hi16(af[i][e]);
    else if (k == 2) tl = __builtin_fmaf(tl, rT[p][e >> 1][(e & 1) * 2], 0.0f);
    else if (k == 3) th *= rT[p][e >> 1][(e & 1) * 2 + 1];
    else af[i][e] = pack2(tl, th);
  };
  TS(7);
  asm volatile("s_waitcnt vmcnt(%0) lgkmcnt(0)" :: "n"(20 + (PPF == 6)) : "memory");
  rdT(IC(0));
#pragma unroll
  for (int i = 0; i < 4; ++i) rdA(IC(0), i);
  asm volatile("s_waitcnt lgkmcnt(3)" : "+v"(af[0]), "+v"(rT[0][0]), "+v"(rT[0][1]), "+v"(mT[0]) :: "memory");
#pragma unroll
  for (int n = 0; n < NSC; ++n) scop(0, 0, n);
  SB();
  const unsigned eo = (unsigned)((l15 + 16 * (2 * (g4 & 1) + (g4 >> 1))) * 16);
  const size_t eb = (size_t)((rb * 4 + wave) * 24 + cb * 3) * 1024;
  u32x3 zv[3];
  unsigned zx[3], bsv;
  auto ldZ = [&]() {
#pragma unroll
    for (int q = 0; q < 3; ++q)
      asm volatile("global_load_dwordx3 %0, %1, %2 offset:%3" : "=&v"(zv[q]) : "v"(eo), "s"((const char*)hf + eb), "i"(q * 1024) : "memory");
#pragma unroll
    for (int q = 0; q < 3; ++q)
      asm volatile("global_load_dword %0, %1, %2 offset:%3" : "=&v"(zx[q]) : "v"(eo >> 1),
                   "s"((const char*)hf + Z1X + ((rb * 2 + (wave >> 1)) * 24 + cb * 3) * 512 + (wave & 1) * 4), "i"(q * 512) : "memory");
    asm volatile("global_load_ushort %0, %1, %2" : "=&v"(bsv) : "v"((unsigned)(l15 * 2)), "s"(b_sp + t0 + wave * 16) : "memory");
  };
  const unsigned xa = ringl + lane * 16, xc = ringl + lane * 4;
  auto step = [&](auto sc) {
    constexpr int s = decltype(sc)::value, sl = s % 3, p = s & 1;
    constexpr int NV = s == 0 ? 14 + (PPF == 6) : (s == 2 ? 28 : (s < 12 ? 20 : (s == 12 ? 12 : (LP3 ? 19 - 6 * (s - 13) : 19))));
    constexpr int DA = s == 0 ? 2 : (((s & 1) && (s + 5) / 2 < 8) ? (s + 5) / 2 : -1);
    asm volatile("s_waitcnt vmcnt(%7) lgkmcnt(2)\n\ts_nop 1"
                 : "+v"(Bf[sl][0]), "+v"(Bf[sl][1]), "+v"(Bf[sl][2]), "+v"(Bf[sl][3]), "+v"(Bf[sl][4]), "+v"(Bf[sl][5]), "+v"(af[1]) : "n"(NV) : "memory");
    if constexpr (s == 1) asm volatile("" : "+v"(pfg));
    if constexpr (s == 3) flag_wait_asm(flg + 16 + wave * 4, GTB ? 1u : 2u);
    if constexpr (s >= S0) {
      auto d3 = [&](int j) { return __builtin_bit_cast(u32x4, Bf[sl][j])[3]; };
      auto bad = [&]() { return __builtin_amdgcn_ballot_w64(umx(umx(umx(d3(0), d3(1)), d3(2)), umx(umx(d3(3), d3(4)), d3(5))) == HOLE) != 0; };
      if (__builtin_expect(bad(), 0)) {
        do {
          --tmo;
          asm volatile("s_sleep 2\n\tglobal_load_dwordx4 %0, %6, %7 offset:-2048 sc1\n\tglobal_load_dwordx4 %1, %6, %7 offset:-1024 sc1\n\t"
                       "global_load_dwordx4 %2, %6, %7 sc1\n\tglobal_load_dwordx4 %3, %6, %7 offset:1024 sc1\n\t"
                       "global_load_dwordx4 %4, %6, %7 offset:2048 sc1\n\tglobal_load_dwordx4 %5, %6, %7 offset:3072 sc1\n\ts_waitcnt vmcnt(0)"
                       : "+v"(Bf[sl][0]), "+v"(Bf[sl][1]), "+v"(Bf[sl][2]), "+v"(Bf[sl][3]), "+v"(Bf[sl][4]), "+v"(Bf[sl][5])
                       : "v"(boff), "s"(Bs + (size_t)s * 49152) : "memory");
        } while (bad() && tmo > 0);
      }
    }
    if constexpr (s == SGS) signal1(flg + 44);
    if constexpr (s < 15) ABS(s + 1);
    SB();
#pragma unroll
    for (int i = 0; i < 4; ++i) {
      if (i == 1) asm volatile("s_waitcnt lgkmcnt(%2)" : "+v"(af[2]), "+v"(af[3]) : "n"(s < 15 ? (s + 1 < S0 ? 4 : 1) : 0) : "memory");
      if (i == 3 && s + 1 < S0) asm volatile("s_waitcnt lgkmcnt(2)" : "+v"(af[0]), "+v"(rT[p ^ 1][0]), "+v"(rT[p ^ 1][1]), "+v"(mT[p ^ 1]) :: "memory");
      if (i == 3 && s + 1 >= S0 && s < 15) asm volatile("s_waitcnt lgkmcnt(2)" : "+v"(af[0]) :: "memory");
      const bf16x8 av = __builtin_bit_cast(bf16x8, af[i]);
#pragma unroll
      for (int j = 0; j < 7; ++j) {
        f32x4& ac = j < 6 ? acc[i][j < 6 ? j : 0] : cacc[i];
        const bf16x8 bb = j < 6 ? Bf[sl][j < 6 ? j : 0] : __builtin_bit_cast(bf16x8, mT[p]);
        if constexpr (s == 0) asm volatile("v_mfma_f32_16x16x32_bf16 %0, %1, %2, 0" : "=&v"(ac) : "v"(bb), "v"(av));
        else if (j < 6 || s < XNS) asm volatile("v_mfma_f32_16x16x32_bf16 %0, %1, %2, %0" : "+v"(ac) : "v"(bb), "v"(av));
        SB();
#pragma unroll
        for (int n = NPM * j; n < NPM * j + NPM && n < NSC; ++n) {
          if (i < 3) { if (s < XNS) scop(i + 1, p, n); }
          else if (s < 15 && s + 1 < XNS) scop(0, p ^ 1, n);
          SB();
        }
        if constexpr (s == 0) { if (i == 0 && j < 6) ldB1(IC(2), j); }
        if constexpr (DA >= 0) { if ((i == 1 || i == 2) && j < 4) ldA1(IC(DA < 0 ? 0 : DA), (i - 1) * 4 + j); }
        if constexpr (s + 3 < 16) { if (i == 3 && j < 6) ldB1(IC(s + 3), j); }
        if constexpr (!LP3 && (s == 13 || s == 14)) { if (i == 3 && j < 6) ldP1((s - 13) * 6 + j); }
        if constexpr (s == 15) {
          if (i > 0 && j > 0) asm volatile("ds_write_b128 %0, %1 offset:%2" :: "v"(xa), "v"(acc[i > 0 ? i - 1 : 0][j > 0 ? j - 1 : 0]), "i"((i - 1) * 6144 + (j - 1) * 1024) : "memory");
          if (!LP3) {
            if (i == 2 && j > 0) ldP1(11 + j);
            if (i == 3 && j == 1) ldP1(18);
            if (i == 3 && j == 2) ldy0();
          }
          if (LP3 == 2) { if (i >= 2 && j < 6) ldP1((i - 2) * 6 + j); }
          if (LP3 == 3) { if (i >= 2 && j < 6 && (i - 2) * 6 + j < NW0) ldP1((i - 2) * 6 + j); }
        }
        SB();
      }
      if constexpr (s == 15) {
        if (i > 0) {
          asm volatile("ds_write_b32 %0, %1 offset:%2" :: "v"(xc), "v"(cacc[i > 0 ? i - 1 : 0][0]), "i"(18432 + (i - 1) * 256),
                       "v"(cacc[i > 0 ? i - 1 : 0]) : "memory");
          signal1(flg + ((wave + i) & 3) * 4);
        }
      }
      asm volatile("s_nop 1");
      if constexpr (s < 15) { if (i == 0 && s + 1 < S0) rdT(IC(s + 1)); rdA(IC(s + 1), i); }
      SB();
    }
    if constexpr (s == 12) ldZ();
    SB();
  };
  TS(0); AB(1);
  step(IC(0)); TS(1); step(IC(1)); step(IC(2)); step(IC(3)); TS(2); AB(3); step(IC(4)); step(IC(5)); step(IC(6)); step(IC(7)); TS(3);
  step(IC(8)); step(IC(9)); step(IC(10)); step(IC(11)); TS(4); step(IC(12)); step(IC(13)); step(IC(14)); step(IC(15)); TS(5); AB(2);
  STAMP(2, 1);
  if (LP3 && LP3 != 3) {
#pragma unroll
    for (int n = LP3 == 2 ? 12 : 0; n < 19; ++n) ldP1(n);
    ldy0();
    SB();
  }
  if (LP3 == 3) {
#pragma unroll
    for (int n = NW0; n < NW1; ++n) ldP1(n);
    SB();
  }
  flag_wait_asm(flg + wave * 4, 3u);
  AB(4);
  f32x4 own[6];
  {
    f32x4 pr[3][6];
    float pc[3];
#pragma unroll
    for (int q = 0; q < 3; ++q) {
      const char* src = smem + M_RING + ((wave + 3 - q) & 3) * 24576;
      pc[q] = *(const float*)(src + 18432 + q * 256 + lane * 4);
#pragma unroll
      for (int j = 0; j < 6; ++j) pr[q][j] = *(const f32x4*)(src + q * 6144 + j * 1024 + lane * 16);
    }
    asm volatile("s_nop 7" : "+v"(acc[3][0]), "+v"(acc[3][1]), "+v"(acc[3][2]), "+v"(acc[3][3]), "+v"(acc[3][4]), "+v"(acc[3][5]), "+v"(cacc[3]));
    const float c = (cacc[3][0] + pc[0]) + (pc[1] + pc[2]);
#pragma unroll
    for (int j = 0; j < 6; ++j) own[j] = ((acc[3][j] - c) + pr[0][j]) + (pr[1][j] + pr[2][j]);
  }
  asm volatile("s_waitcnt vmcnt(%7)" : "+v"(zv[0]), "+v"(zv[1]), "+v"(zv[2]), "+v"(zx[0]), "+v"(zx[1]), "+v"(zx[2]), "+v"(bsv) : "i"(LP3 == 3 ? NW1 : 21) : "memory");
  SB();
  {
    const float bs = lo16(bsv);
    unsigned pk[6][2];
#pragma unroll
    for (int j = 0; j < 6; ++j)
#pragma unroll
      for (int h = 0; h < 2; ++h) {
        const unsigned r0 = pack2(own[j][2 * h], own[j][2 * h + 1]);
        pk[j][h] = pack2(lo16(r0) + bs, hi16(r0) + bs);
      }
#pragma unroll
    for (int q = 0; q < 3; ++q) {
      const auto s0 = __builtin_amdgcn_permlane16_swap(pk[2 * q][0], pk[2 * q + 1][0], false, false);
      const auto s1 = __builtin_amdgcn_permlane16_swap(pk[2 * q][1], pk[2 * q + 1][1], false, false);
      u32x4 gv; gv[0] = s0[0]; gv[1] = s1[0]; gv[2] = s0[1]; gv[3] = s1[1];
      u32x4 o4;
#pragma unroll
      for (int e = 0; e < 4; ++e) {
        const unsigned z = e < 3 ? zv[q][e < 3 ? e : 0] : zx[q];
        o4[e] = pack2(lo16(z) * lo16(gv[e]), hi16(z) * hi16(gv[e]));
      }
      o4[3] = nohole(o4[3]);
      __builtin_amdgcn_raw_buffer_store_b128(o4, hrs, (int)eb + eo + q * 1024, 0, WTA);
    }
  }
  TS_OUT(1, 0, 8, 0); TS_OUT(1, 8, 8, 3); AB(5);
  drain1(3);
  if (!out) { RT(1, 1); return; }
  STAMP(2, 2);
  }
  TS_DECL;
  TS(0);
  unsigned hoff[4];
#pragma unroll
  for (int it = 0; it < 4; ++it) hoff[it] = (unsigned)(((rb * 4 + ((mt + 1 + it) & 3)) * 24 + 3 * pw) * 1024 + lane * 16);
  u32x4 Hq[4][3];
  auto ldy = [&]() {
#pragma unroll
    for (int n = 0; n < 2; ++n) asm volatile("global_load_dwordx4 %0, %1, %2 sc1" : "=&v"(yv[n]) : "v"(yoff(n)), "s"(yb) : "memory");
  };
  auto ldh = [&](int it) {
#pragma unroll
    for (int k = 0; k < 3; ++k)
      asm volatile("global_load_dwordx4 %0, %1, %2 offset:%3 sc1" : "=&v"(Hq[it][k]) : "v"(hoff[it]), "s"(XSRC), "i"(k * 1024) : "memory");
  };
  auto ldh1 = [&](int it, int k) {
    asm volatile("global_load_dwordx4 %0, %1, %2 offset:%3 " HSC : "=&v"(Hq[it][k]) : "v"(hoff[it]), "s"(XSRC), "i"(k * 1024) : "memory");
  };
  auto ldh0 = [&](int it) {
#pragma unroll
    for (int k = 0; k < 3; ++k) ldh1(it, k);
  };
  f32x2 xf[3][2], sv[3][2];
  auto ymath = [&]() {
    char* yi = smem + P3_Y + wave * 1536;
#pragma unroll
    for (int n = 0; n < 2; ++n) *(u32x4*)(yi + (n ? 64 + (lane & 31) : lane) * 16) = yv[n];
    const unsigned ya = lds_addr(yi) + (4 * g4 + (l15 >> 2)) * 32 + 8 * (lane & 3);
    s16x4 yr[3] = {tr_asm<0>(ya), tr_asm<512>(ya), tr_asm<1024>(ya)};
    asm volatile("s_waitcnt lgkmcnt(0)" : "+v"(yr[0]), "+v"(yr[1]), "+v"(yr[2]) :: "memory");
    const f32x2 mean = {__uint_as_float(st[0]), __uint_as_float(st[0])}, rd = {__uint_as_float(st[1]), __uint_as_float(st[1])};
#pragma unroll
    for (int q = 0; q < 3; ++q) {
      const u32x2 yw = __builtin_bit_cast(u32x2, yr[q]);
#pragma unroll
      for (int h = 0; h < 2; ++h) {
        xf[q][h] = unpk(xv[q][h]);
        sv[q][h] = ((xf[q][h] - mean) * rd) * unpk(yw[h]);
        asm volatile("" : "+v"(sv[q][h]), "+v"(xf[q][h]));
      }
    }
    SB();
  };
#pragma unroll
  for (int n = NW1; n < 19; ++n) ldP1(n);
  if (YMP == 5) ldy0();
  SB();
  if (wave >= 4) AB(5);
  asm volatile("s_waitcnt vmcnt(%0)" :: "i"(19 - NW1 + (YMP == 5 ? 2 : 0)) : "memory");
  bool ybad;
  AB(6);
  signal1(flg + 48 + 4 * kh);
  flag_wait_asm(flg + 48, 4u);
  TS(1); AB(7);
  if (SPR) __builtin_amdgcn_s_setprio(3);
  ldh0(0);
  if (SPR) __builtin_amdgcn_s_setprio(2);
  ldh0(1);
  if (SPR) __builtin_amdgcn_s_setprio(1);
  if (NUP > 2) ldh0(2);
  if (SPR) __builtin_amdgcn_s_setprio(0);
  if (NUP > 3) ldh0(3);
  if (YMP < 3) ldy0();
  SB();
  AB(8);
  TS(2); AB(9);
  flag_wait_asm(flg + 40, kh ? 4u : 0u);
  TS(3);
  char* const px = smem + (kh ? P3_C + mt * 18432 : P3_G + mt * 17408) + lane * 16;
  f32x4 ac3[5], o[3];
  auto comp = [&](auto itc) {
    constexpr int it = decltype(itc)::value;
    constexpr int NW = 3 * ((it + NUP < 4 ? it + NUP : 4) - it - 1) + (LP3 == 3 && YMP != 5 && (YMP < 3 ? it < 2 : it >= 2) ? 2 : 0);
    asm volatile("s_waitcnt vmcnt(%3)" : "+v"(Hq[it][0]), "+v"(Hq[it][1]), "+v"(Hq[it][2]) : "n"(NW) : "memory");
    if constexpr (LP3 == 3 && it == 0)
      asm volatile("" : "+v"(Wq[0]), "+v"(Wq[1]), "+v"(Wq[2]), "+v"(Wq[3]), "+v"(Wq[4]), "+v"(Wq[5]), "+v"(Wq[6]), "+v"(Wq[7]),
                   "+v"(Wq[8]), "+v"(Wq[9]), "+v"(Wq[10]), "+v"(Wq[11]), "+v"(Wq[12]), "+v"(Wq[13]), "+v"(Wq[14]),
                   "+v"(xv[0]), "+v"(xv[1]), "+v"(xv[2]), "+v"(st) :: "memory");
    auto bad = [&]() { return __builtin_amdgcn_ballot_w64(umx(umx(Hq[it][0][3], Hq[it][1][3]), Hq[it][2][3]) == HOLE) != 0; };
    if (__builtin_expect(bad(), 0)) {
      do {
        --tmo;
        asm volatile("s_sleep 2\n\tglobal_load_dwordx4 %0, %3, %4 sc1\n\tglobal_load_dwordx4 %1, %3, %4 offset:1024 sc1\n\t"
                     "global_load_dwordx4 %2, %3, %4 offset:2048 sc1\n\ts_waitcnt vmcnt(0)"
                     : "+v"(Hq[it][0]), "+v"(Hq[it][1]), "+v"(Hq[it][2]) : "v"(hoff[it]), "s"(hf) : "memory");
      } while (bad() && tmo > 0);
    }
    SB();
#pragma unroll
    for (int k = 0; k < 3; ++k) {
#pragma unroll
      for (int j = 0; j < 5; ++j) {
        ac3[j] = __builtin_amdgcn_mfma_f32_16x16x32_bf16(Wq[k * 5 + j], __builtin_bit_cast(bf16x8, Hq[it][k]), k ? ac3[j] : (f32x4){0.f, 0.f, 0.f, 0.f}, 0, 0, 0);
        if constexpr (it + NUP < 4) { if (k == 0 && j < 3) { SB(); ldh1(it + NUP < 4 ? it + NUP : 0, j); SB(); } }
        if constexpr (YMP >= 3 && YMP != 5 && it == 1) { if (k == 1 && j == 0) { SB(); ldy0(); SB(); } }
      }
    }
    SB();
    if constexpr (it < 3) {
#pragma unroll
      for (int j = 0; j < 5; ++j) *(f32x4*)(px + (it * 5 + j) * 1024) = ac3[j];
      signal1(flg + 64 + kh * 16 + ((mt + 1 + it) & 3) * 4);
    }
    SB();
  };
  auto gather = [&](auto cc) {
    constexpr int cv = decltype(cc)::value;
    flag_wait_asm(flg + 64 + cv * 16 + mt * 4, cv ? 5u : 3u);
#pragma unroll
    for (int it = 0; it < 3 + cv; ++it) {
      const int ms = (mt - 1 - it) & 3;
      const char* ps = smem + ((it < 3 ? cv : !kh) ? P3_C + ms * 18432 : P3_G + ms * 17408) + lane * 16;
      const char* p0 = ps + (it < 3 ? it * 5 + 2 * kh : 15) * 1024;
      const f32x4 a0 = *(const f32x4*)p0, a1 = *(const f32x4*)(p0 + 1024);
      if (!cv && it == 0) { o[0] = a0; o[1] = a1; } else { o[0] += a0; o[1] += a1; }
    }
    if (!kh) {
#pragma unroll
      for (int it = 0; it < 3 + cv; ++it) {
        const int ms = (mt - 1 - it) & 3;
        const char* ps = smem + ((it < 3 ? cv : 1) ? P3_C + ms * 18432 : P3_G + ms * 17408) + lane * 16;
        o[2] += *(const f32x4*)(ps + (it < 3 ? it * 5 + 4 : 17) * 1024);
      }
    }
    SB();
  };
  o[2] = (f32x4){0.f, 0.f, 0.f, 0.f};
  comp(IC(0)); AB(10); comp(IC(1));
  if (YMP == 5) {
    asm volatile("" : "+v"(yv[0]), "+v"(yv[1]) :: "memory");
    if (__builtin_amdgcn_ballot_w64(umx(yv[0][3], yv[1][3]) == HOLE) != 0) {
      asm volatile("global_load_dwordx4 %0, %2, %4 sc1\n\tglobal_load_dwordx4 %1, %3, %4 sc1" : "+v"(yv[0]), "+v"(yv[1]) : "v"(yoff(0)), "v"(yoff(1)), "s"(yb) : "memory");
    }
    SB();
  }
  if (YMP == 1) {
    asm volatile("s_waitcnt vmcnt(6)" : "+v"(yv[0]), "+v"(yv[1]) :: "memory");
    ybad = __builtin_amdgcn_ballot_w64(umx(yv[0][3], yv[1][3]) == HOLE) != 0;
    ymath();
  }
  comp(IC(2));
  if (YMP == 2) {
    asm volatile("" : "+v"(yv[0]), "+v"(yv[1]) :: "memory");
    ybad = __builtin_amdgcn_ballot_w64(umx(yv[0][3], yv[1][3]) == HOLE) != 0;
    ymath();
  }
  TS(4); AB(11);
  gather(IC(0));
  if (YMP == 4) {
    asm volatile("s_waitcnt vmcnt(0)" : "+v"(yv[0]), "+v"(yv[1]) :: "memory");
    ybad = __builtin_amdgcn_ballot_w64(umx(yv[0][3], yv[1][3]) == HOLE) != 0;
    ymath();
  }
  AB(12);
  comp(IC(3));
  TS(5); AB(13);
  {
    *(f32x4*)(px + 15 * 1024) = kh ? ac3[0] : ac3[2];
    *(f32x4*)(px + 16 * 1024) = kh ? ac3[1] : ac3[3];
    if (kh) *(f32x4*)(px + 17 * 1024) = ac3[4];
    signal1(flg + 80 + mt * 4);
    SB();
    o[0] += kh ? ac3[2] : ac3[0]; o[1] += kh ? ac3[3] : ac3[1]; o[2] += ac3[4];
    if (YMP == 3 || YMP == 5) {
      asm volatile("s_waitcnt vmcnt(0)" : "+v"(yv[0]), "+v"(yv[1]) :: "memory");
      ybad = __builtin_amdgcn_ballot_w64(umx(yv[0][3], yv[1][3]) == HOLE) != 0;
    }
    if (__builtin_expect(ybad, 0)) {
      do {
        --tmo;
        ldy();
        asm volatile("s_waitcnt vmcnt(0)" : "+v"(yv[0]), "+v"(yv[1]) :: "memory");
      } while (__builtin_amdgcn_ballot_w64(umx(yv[0][3], yv[1][3]) == HOLE) != 0 && tmo > 0);
      if (YMP != 3 && YMP != 5) ymath();
    }
    if (YMP == 3 || YMP == 5) ymath();
    TS(6); AB(14);
    gather(IC(1));
    auto fin = [&](const f32x4& o4, int q, unsigned (&pk)[2]) {
#pragma unroll
      for (int h = 0; h < 2; ++h) {
        const f32x2 o = {o4[2 * h], o4[2 * h + 1]};
        const f32x2 a = o * (f32x2){-1.4426950f, -1.4426950f};
        const f32x2 e = (f32x2){__builtin_amdgcn_exp2f(a[0]), __builtin_amdgcn_exp2f(a[1])} + (f32x2){1.0f, 1.0f};
        const f32x2 r = {__builtin_amdgcn_rcpf(e[0]), __builtin_amdgcn_rcpf(e[1])};
        pk[h] = pk2(__builtin_elementwise_fma(o * r, sv[q][h], xf[q][h]));
      }
    };
    unsigned pa[2], pb[2];
    fin(o[0], 0, pa);
    fin(o[1], 1, pb);
    const auto s0 = __builtin_amdgcn_permlane16_swap(pa[0], pb[0], false, false);
    const auto s1 = __builtin_amdgcn_permlane16_swap(pa[1], pb[1], false, false);
    const auto ors = __builtin_amdgcn_make_buffer_rsrc(out, 0, S_ * D_ * 2, 0x00020000);
    __builtin_amdgcn_raw_buffer_store_b128((u32x4){s0[0], s1[0], s0[1], s1[1]}, ors, ((int)xo + 32 * kh + 16 * (g4 & 1) + 8 * (g4 >> 1)) * 2, 0, OWT);
    if (!kh) {
      fin(o[2], 2, pa);
      __builtin_amdgcn_raw_buffer_store_b64((u32x2){pa[0], pa[1]}, ors, ((int)xo + 64 + 4 * g4) * 2, 0, OWT);
    }
  }
  TS(7); AB(15);
  AB_OUT(16);
  RT(1, 1);
}

extern "C" __global__ void k_end() {}

#define X4 0
#define LA4 4
constexpr int G3_RED = 4 * 3072;
constexpr int G3_LDS = G3_RED + 4 * 15 * 1024;

__global__ __launch_bounds__(256) void k_l4(
    const u16* __restrict__ hf, const u16* __restrict__ woF, const u16* __restrict__ x,
    const float2* __restrict__ xst, const u16* __restrict__ yT, u16* __restrict__ out) {
  extern __shared__ __attribute__((aligned(16))) char smem[];
  const int tid = threadIdx.x, lane = tid & 63, wave = __builtin_amdgcn_readfirstlane(tid >> 6);
  const int b = blockIdx.x;
  const int xcd = b & 7, idx = b >> 3;
  const int rb = 8 * (xcd >> 1) + (idx >> 2), cb = 4 * (xcd & 1) + (idx & 3);
  const int t0 = rb * 64, d0 = cb * 80;
  const int g4 = lane >> 4, l15 = lane & 15;
  RT(2, 0);
  TS_DECL;
  TS(0);
  const unsigned boff16 = (unsigned)(lane * 16);
  const char* Hb = (const char*)hf + (size_t)(rb * 4 * 24 + wave * 6) * 1024;
  const char* Wb = (const char*)woF + (size_t)(wave * 6 * 40 + cb * 5) * 1024;
  u32x4 Hf[6][4], Wf[6][5];
  auto ldH = [&](int s, int k) { Hf[s][k] = *(const u32x4*)(Hb + (size_t)((wave + 1 + k) & 3) * 24576 + s * 1024 + boff16); };
  auto ldG = [&](int s) {
#pragma unroll
    for (int j = 0; j < 5; ++j) Wf[s][j] = *(const u32x4*)(Wb + (size_t)s * 40960 + j * 1024 + boff16);
    ldH(s, 0); ldH(s, 1);
    SB();
  };
  const unsigned ly = __builtin_amdgcn_readfirstlane(lds_addr(smem) + wave * 3072);
  {
    const char* yb = (const char*)(yT + (size_t)d0 * S_ + t0 + wave * 16);
#pragma unroll
    for (int n = 0; n < 3; ++n) {
      const int c = min(n * 64 + lane, 159);
      asm volatile("s_mov_b32 m0, %2\n\ts_nop 0\n\tglobal_load_lds_dwordx4 %0, %1"
                   :: "v"((unsigned)((c >> 1) * (S_ * 2) + (c & 1) * 16)), "s"(yb), "s"(ly + n * 1024) : "memory");
    }
  }
  SB();
  ldG(0); ldG(1);
  const int row = t0 + wave * 16 + l15;
  const size_t xo = (size_t)row * D_ + d0;
  u32x2 xv[5];
  const float2 st = xst[row];
#pragma unroll
  for (int j = 0; j < 5; ++j) xv[j] = *(const u32x2*)(x + xo + 16 * j + 4 * g4);
  SB();
  ldG(2); ldG(3);
  TS(1);
  f32x4 acc[4][5];
  auto mm = [&](int s, int k) {
#pragma unroll
    for (int j = 0; j < 5; ++j) {
      const f32x4 c = s ? acc[k][j] : (f32x4){0.f, 0.f, 0.f, 0.f};
      acc[k][j] = __builtin_amdgcn_mfma_f32_16x16x32_bf16(__builtin_bit_cast(bf16x8, Wf[s][j]), __builtin_bit_cast(bf16x8, Hf[s][k]), c, 0, 0, 0);
    }
    SB();
  };
  auto put = [&](int k, int j) {
    *(f32x4*)(smem + G3_RED + (((wave + 1 + k) & 3) * 3 + k) * 5120 + j * 1024 + lane * 16) = acc[k][j];
    SB();
  };
  f32x4 pr[3][5];
  auto get = [&](int q) {
#pragma unroll
    for (int j = 0; j < 5; ++j) pr[q][j] = *(const f32x4*)(smem + G3_RED + (wave * 3 + q) * 5120 + j * 1024 + lane * 16);
    SB();
  };
  auto ldN = [&](int n) {
    if (n < 6) ldG(n);
    else if (n < 8) {
#pragma unroll
      for (int s = 0; s < 6; ++s) ldH(s, n - 4);
      SB();
    }
  };
  mm(0, 0); mm(0, 1); ldN(LA4);
  s16x4 yr[5];
  {
    const unsigned ya = ly + (4 * g4 + (l15 >> 2)) * 32 + 8 * (lane & 3);
#pragma unroll
    for (int j = 0; j < 5; ++j)
      asm volatile("ds_read_b64_tr_b16 %0, %1 offset:%2" : "=&v"(yr[j]) : "v"(ya), "i"(j * 512) : "memory");
  }
  mm(1, 0); mm(1, 1); ldN(LA4 + 1);
  asm volatile("s_waitcnt lgkmcnt(0)" : "+v"(yr[0]), "+v"(yr[1]), "+v"(yr[2]), "+v"(yr[3]), "+v"(yr[4]) :: "memory");
  f32x2 xf[5][2], sv[5][2];
  {
    const f32x2 mean = {st.x, st.x}, rd = {st.y, st.y};
#pragma unroll
    for (int j = 0; j < 5; ++j) {
      const u32x2 yw = __builtin_bit_cast(u32x2, yr[j]);
#pragma unroll
      for (int h = 0; h < 2; ++h) {
        xf[j][h] = unpk(xv[j][h]);
        sv[j][h] = ((xf[j][h] - mean) * rd) * unpk(yw[h]);
      }
    }
    SB();
  }
  mm(2, 0); mm(2, 1); ldN(LA4 + 2);
  mm(3, 0); mm(3, 1); ldN(LA4 + 3);
  mm(4, 0); mm(4, 1);
  mm(5, 0); mm(5, 1);
  TS(2);
  mm(0, 2); put(0, 0); put(0, 1); put(0, 2);
  mm(1, 2); put(0, 3); put(0, 4); put(1, 0);
  mm(2, 2); put(1, 1); put(1, 2); put(1, 3);
  mm(3, 2); put(1, 4);
  mm(4, 2);
  mm(5, 2);
  TS(3);
  RAW_BARRIER();
  SB();
  get(0);
  mm(0, 3);
  put(2, 0); put(2, 1); put(2, 2); put(2, 3); put(2, 4);
  get(1);
  mm(1, 3);
  mm(2, 3);
  TS(4);
  RAW_BARRIER();
  SB();
  TS(5);
  get(2);
#pragma unroll
  for (int j = 0; j < 5; ++j) pr[0][j] += pr[1][j];
  SB();
  mm(3, 3);
  mm(4, 3);
#pragma unroll
  for (int j = 0; j < 5; ++j) pr[0][j] += pr[2][j];
  SB();
  mm(5, 3);
  TS(6);
  unsigned pk[5][2];
#pragma unroll
  for (int j = 0; j < 5; ++j) {
    const f32x4 o4 = acc[3][j] + pr[0][j];
#pragma unroll
    for (int h = 0; h < 2; ++h) {
      const f32x2 o = {o4[2 * h], o4[2 * h + 1]};
      const f32x2 a = o * (f32x2){-1.4426950f, -1.4426950f};
      const f32x2 e = (f32x2){__builtin_amdgcn_exp2f(a[0]), __builtin_amdgcn_exp2f(a[1])} + (f32x2){1.0f, 1.0f};
      const f32x2 r = {__builtin_amdgcn_rcpf(e[0]), __builtin_amdgcn_rcpf(e[1])};
      pk[j][h] = pk2(__builtin_elementwise_fma(o * r, sv[j][h], xf[j][h]));
    }
  }
  const int dq = 16 * (g4 & 1) + 8 * (g4 >> 1);
#pragma unroll
  for (int q = 0; q < 2; ++q) {
    const auto s0 = __builtin_amdgcn_permlane16_swap(pk[2 * q][0], pk[2 * q + 1][0], false, false);
    const auto s1 = __builtin_amdgcn_permlane16_swap(pk[2 * q][1], pk[2 * q + 1][1], false, false);
    u32x4 o4; o4[0] = s0[0]; o4[1] = s1[0]; o4[2] = s0[1]; o4[3] = s1[1];
    *(u32x4*)(out + xo + 32 * q + dq) = o4;
  }
  *(u32x2*)(out + xo + 64 + 4 * g4) = (u32x2){pk[4][0], pk[4][1]};
  TS(7);
  RT(2, 1);
}

extern "C" void kernel_launch(void* const* d_in, const int* in_sizes, int n_in,
                              void* d_out, int out_size, void* d_ws, size_t ws_size,
                              hipStream_t stream) {
  const u16* A_q = (const u16*)d_in[0];
  const u16* A_k = (const u16*)d_in[1];
  const u16* A_v = (const u16*)d_in[2];
  const u16* A_shift_k = (const u16*)d_in[3];
  const u16* A_ssm_k = (const u16*)d_in[4];
  const u16* A_ssm_D = (const u16*)d_in[5];
  const u16* B_w_in = (const u16*)d_in[7];
  const u16* B_w_sp = (const u16*)d_in[8];
  const u16* B_b_sp = (const u16*)d_in[9];
  const u16* B_w_out = (const u16*)d_in[10];
  u16* out = (u16*)d_out;

  char* ws = (char*)d_ws;
  size_t off = 0;
  auto carve = [&](size_t bytes) { char* p = ws + off; off += (bytes + 255) & ~(size_t)255; return p; };
  u16* z1 = (u16*)carve((size_t)S_ * FH_ * 2 * 5 / 4);
  u16* z2 = (u16*)carve((size_t)S_ * FH_ * 2 * 5 / 4);
  float2* st2 = (float2*)carve((size_t)4 * S_ * 8);
  float2* xst = (float2*)carve((size_t)S_ * 8);
  u16* kvT = (u16*)carve((size_t)D_ * S_ * 2 * 5 / 4);
  u16* woF = (u16*)carve((size_t)FH_ * D_ * 2);

  int dev = 0, cus = 0;
  (void)hipGetDevice(&dev);
  (void)hipDeviceGetAttribute(&cus, hipDeviceAttributeMultiprocessorCount, dev);
  const bool fused = cus >= 256;
  (void)hipFuncSetAttribute((const void*)k_l1, hipFuncAttributeMaxDynamicSharedMemorySize, L1_LDS);
  (void)hipFuncSetAttribute((const void*)k_m, hipFuncAttributeMaxDynamicSharedMemorySize, M_LDS);
  k_l1<<<256, 768, L1_LDS, stream>>>(A_q, A_k, A_v, A_shift_k, B_w_in, z1, z2, st2, xst, kvT, A_ssm_k, B_w_out, B_w_sp, woF);
  if (!fused) k_n<<<512, 64, 0, stream>>>(z2, st2);
  k_m<<<256, 512, M_LDS, stream>>>(B_w_sp, B_b_sp, z1, z2, st2, kvT, A_ssm_k, A_ssm_D, woF, A_q, xst, fused ? out : nullptr);
  if (!fused) {
    (void)hipFuncSetAttribute((const void*)k_l4, hipFuncAttributeMaxDynamicSharedMemorySize, G3_LDS);
    k_l4<<<256, 256, G3_LDS, stream>>>(z1, woF, A_q, xst, kvT, out);
  }
}
```

```cpp
#define NTA ""
#define NTB ""
#define NTK ""
#define NTP ""
#define NTX ""
#define NTQ ""
#define ANT ""
#include <hip/hip_runtime.h>
#include <hip/hip_bf16.h>
#include <stdint.h>
#include <type_traits>

typedef unsigned short u16;
typedef __attribute__((ext_vector_type(8))) short bf16x8;
typedef __attribute__((ext_vector_type(4))) short s16x4;
typedef __attribute__((ext_vector_type(4))) float f32x4;
typedef __attribute__((ext_vector_type(16))) float f32x16;
typedef __attribute__((ext_vector_type(4))) unsigned int u32x4;
typedef __attribute__((ext_vector_type(3))) unsigned int u32x3;
typedef __attribute__((ext_vector_type(2))) unsigned int u32x2;

#define LDS_AS __attribute__((address_space(3)))
#define IC(n) std::integral_constant<int, n>{}
#define SB() __builtin_amdgcn_sched_barrier(0)

#define STAMP(k, i) do {} while (0)
#define STAMPW(k, i, w) do {} while (0)
#define TS_DECL do {} while (0)
#define TS(i) do {} while (0)
#define TS_OUT(k, base, n, w) do {} while (0)

#define RT(k, i) do {} while (0)

#define AB_DECL do {} while (0)
#define AB(i) do {} while (0)
#define ABS(i) do {} while (0)
#define AB_OUT(n) do {} while (0)

#define AK_DECL do {} while (0)
#define AK(i) do {} while (0)
#define AK_OUT(n) do {} while (0)
#define MAPM 0
#define XE 0
constexpr int S_ = 2048, D_ = 640, FH_ = 768, F2_ = 1536;

__device__ __forceinline__ float bf2f(u16 v) { return __uint_as_float(((unsigned)v) << 16); }
typedef __attribute__((ext_vector_type(2))) __bf16 bf16x2n;
typedef __attribute__((ext_vector_type(2))) float f32x2;
__device__ __forceinline__ unsigned pack2(float lo, float hi) {
  f32x2 v = {lo, hi};
  bf16x2n r = __builtin_convertvector(v, bf16x2n);
  return __builtin_bit_cast(unsigned, r);
}
__device__ __forceinline__ float lo16(unsigned w) { return __uint_as_float(w << 16); }
__device__ __forceinline__ float hi16(unsigned w) { return __uint_as_float(w & 0xFFFF0000u); }
__device__ __forceinline__ u16 f2bf(float f) { return (u16)(pack2(f, f) & 0xFFFFu); }
__device__ __forceinline__ float rbf(float f) { return lo16(pack2(f, f)); }

template <int CTRL>
__device__ __forceinline__ float dppf(float v) {
  return __builtin_bit_cast(float, __builtin_amdgcn_update_dpp(0, __builtin_bit_cast(int, v), CTRL, 0xF, 0xF, true));
}
__device__ __forceinline__ float sum16(float v) {
  v += dppf<0xB1>(v); v += dppf<0x4E>(v); v += dppf<0x141>(v); v += dppf<0x140>(v);
  return v;
}
__device__ __forceinline__ float gelu_tanh(float x) {
  const float kBeta = 0.7978845608028654f;
  const float kKappa = 0.044715f;
  float inner = kBeta * (x + kKappa * x * x * x);
  return x * __builtin_amdgcn_rcpf(1.0f + __expf(-2.0f * inner));
}

template <int NSUB>
__device__ __forceinline__ unsigned boff(unsigned row, unsigned ch) {
  return (unsigned)(NSUB * 512) * (row >> 3) + 512u * (ch >> 2) + 64u * (row & 7) + 16u * ((ch & 3) ^ ((row >> 2) & 3));
}
template <int OFF>
__device__ __forceinline__ s16x4 tr_asm(unsigned a) {
  s16x4 r;
  asm volatile("ds_read_b64_tr_b16 %0, %1 offset:%2" : "=&v"(r) : "v"(a), "i"(OFF) : "memory");
  return r;
}
struct TrBase { unsigned b[2][2]; };
template <int NSUB>
__device__ __forceinline__ TrBase tr_base(unsigned lane, unsigned ldsbase) {
  const unsigned g = lane >> 4, q = (lane & 15) >> 2, p = lane & 3;
  TrBase t;
#pragma unroll
  for (int c1 = 0; c1 < 2; ++c1)
#pragma unroll
    for (int h = 0; h < 2; ++h)
      t.b[c1][h] = ldsbase + boff<NSUB>(8 * g + 4 * h + q, 2 * c1 + (p >> 1)) + 8 * (p & 1);
  return t;
}
template <int NSUB, int KS, int C>
__device__ __forceinline__ void tr_frag_issue(const TrBase& t, unsigned stage_off, s16x4& lo, s16x4& hi) {
  constexpr int OFF = NSUB * 512 * 4 * KS + 512 * (C >> 1);
  lo = tr_asm<OFF>(t.b[C & 1][0] + stage_off);
  hi = tr_asm<OFF>(t.b[C & 1][1] + stage_off);
}
__device__ __forceinline__ bf16x8 join8(s16x4 lo, s16x4 hi) {
  bf16x8 r;
  r[0] = lo[0]; r[1] = lo[1]; r[2] = lo[2]; r[3] = lo[3];
  r[4] = hi[0]; r[5] = hi[1]; r[6] = hi[2]; r[7] = hi[3];
  return r;
}
#define WAIT_LGKM0() asm volatile("s_waitcnt lgkmcnt(0)" ::: "memory")
__device__ __forceinline__ unsigned lds_addr(const void* p) {
  return (unsigned)(uintptr_t)(LDS_AS const char*)p;
}

#define RAW_BARRIER() do { asm volatile("s_waitcnt lgkmcnt(0)" ::: "memory"); __builtin_amdgcn_s_barrier(); } while (0)
#define WAIT_VM(n) asm volatile("s_waitcnt vmcnt(" #n ")" ::: "memory")

__device__ __forceinline__ void flag_wait(unsigned faddr, unsigned target) {
  while (true) {
    unsigned v;
    asm volatile("ds_read_b32 %0, %1\n\ts_waitcnt lgkmcnt(0)" : "=&v"(v) : "v"(faddr) : "memory");
    v = __builtin_amdgcn_readfirstlane(v);
    if (v >= target) break;
    __builtin_amdgcn_s_sleep(1);
  }
}
__device__ __forceinline__ void flag_wait_asm(unsigned faddr, unsigned target) {
  unsigned v, sv;
  asm volatile(".Lfw_%=:\n\tds_read_b32 %0, %2\n\ts_waitcnt lgkmcnt(0)\n\tv_readfirstlane_b32 %1, %0\n\ts_nop 0\n\t"
               "s_cmp_ge_u32 %1, %3\n\ts_cbranch_scc1 .Lfd_%=\n\ts_sleep 1\n\ts_branch .Lfw_%=\n\t.Lfd_%=:"
               : "=&v"(v), "=&s"(sv) : "v"(faddr), "s"(target) : "memory", "scc");
}
__device__ __forceinline__ void flag_signal(unsigned faddr, int lane) {
  if (lane == 0) {
    unsigned one = 1u;
    asm volatile("ds_add_u32 %0, %1" :: "v"(faddr), "v"(one) : "memory");
  }
}
__device__ __forceinline__ void glds16(const void* g, char* lds) {
  __builtin_amdgcn_global_load_lds((const unsigned*)g, (unsigned*)lds, 16, 0, 0);
}
__device__ __forceinline__ void glds_a(const u16* gbase, int ld, char* stageA, int ins, int lane) {
  const int row = ins * 8 + (lane >> 3);
  const int ch = (lane & 7) ^ (row & 7);
  glds16(gbase + (size_t)row * ld + ch * 8, stageA + ins * 1024);
}
__device__ __forceinline__ bf16x8 read_a(const char* stageA, int row, int kchunk) {
  return *(const bf16x8*)(stageA + row * 128 + ((kchunk ^ (row & 7)) << 4));
}
template <int NSUB, int BNV>
__device__ __forceinline__ void glds_b(const u16* gbase, int ld, char* stageB, int ins, int lane) {
  const int o = ins * 1024 + lane * 16;
  const int rg = o / (NSUB * 512), rem = o % (NSUB * 512);
  const int sub = rem >> 9, rr = (rem >> 6) & 7, slot = (rem >> 4) & 3;
  const int row = rg * 8 + rr;
  int ch = sub * 4 + (slot ^ ((row >> 2) & 3));
  if (BNV < NSUB * 32) ch = min(ch, BNV / 8 - 1);
  glds16(gbase + (size_t)row * ld + ch * 8, stageB + ins * 1024);
}

constexpr int L1_A_BYTES = 64 * 128;
constexpr int L1_B_BYTES = 64 * 192 * 2;
constexpr int L1_STAGE = L1_A_BYTES + L1_B_BYTES;
constexpr int L1_LDS = 5 * L1_STAGE;
constexpr unsigned HOLE = 0xFFFFFFFFu;
constexpr int Z1X = S_ * FH_ * 2;
constexpr int KVX = D_ * S_ * 2;
#define S0 7

__device__ __forceinline__ f32x2 unpk(unsigned w) { f32x2 r = {lo16(w), hi16(w)}; return r; }
__device__ __forceinline__ unsigned pk2(f32x2 v) { return pack2(v[0], v[1]); }
__device__ __forceinline__ f32x2 gelu2(f32x2 x) {
  const f32x2 c1 = {-2.3022082f, -2.3022082f}, c2 = {-0.10294324f, -0.10294324f};
  const f32x2 a = x * __builtin_elementwise_fma(x * x, c2, c1);
  f32x2 e = {__builtin_amdgcn_exp2f(a[0]), __builtin_amdgcn_exp2f(a[1])};
  e = e + (f32x2){1.0f, 1.0f};
  const f32x2 r = {__builtin_amdgcn_rcpf(e[0]), __builtin_amdgcn_rcpf(e[1])};
  return x * r;
}
__device__ __forceinline__ float allred_8(float v) {
  v += __uint_as_float(__builtin_amdgcn_mov_dpp(__float_as_uint(v), 0xB1, 0xF, 0xF, true));
  v += __uint_as_float(__builtin_amdgcn_mov_dpp(__float_as_uint(v), 0x4E, 0xF, 0xF, true));
  v += __uint_as_float(__builtin_amdgcn_mov_dpp(__float_as_uint(v), 0x141, 0xF, 0xF, true));
  return v;
}
__device__ __forceinline__ float allred_g(float v) {
  const auto a = __builtin_amdgcn_permlane16_swap(__float_as_uint(v), __float_as_uint(v), false, false);
  v = __uint_as_float(a[0]) + __uint_as_float(a[1]);
  const auto c = __builtin_amdgcn_permlane32_swap(__float_as_uint(v), __float_as_uint(v), false, false);
  return __uint_as_float(c[0]) + __uint_as_float(c[1]);
}

#define WT1 0
#define WTS "sc1"
#define ST16(p, v, bit) do { if (WT1 & (bit)) asm volatile("global_store_dwordx4 %0, %1, off " WTS "\n\ts_nop 1" :: "v"(p), "v"(v) : "memory"); else *(u32x4*)(p) = (v); } while (0)
__global__ __launch_bounds__(768) void k_l1(
    const u16* __restrict__ x, const u16* __restrict__ kin, const u16* __restrict__ vin,
    const u16* __restrict__ shift_k, const u16* __restrict__ w_in,
    u16* __restrict__ z1, u16* __restrict__ z2, float2* __restrict__ st2, float2* __restrict__ xst,
    u16* __restrict__ kvT, const u16* __restrict__ ssm_k, const u16* __restrict__ w_out, const u16* __restrict__ w_sp,
    u16* __restrict__ woF) {
  extern __shared__ __attribute__((aligned(16))) char smem[];
  const int tid = threadIdx.x, lane = tid & 63, wave = __builtin_amdgcn_readfirstlane(tid >> 6);
  const int b = blockIdx.x;
  const int xcd = b & 7, idx = b >> 3;
  const int rb = 8 * (xcd >> 1) + (idx >> 2), cb = 4 * (xcd & 1) + (idx & 3);
  const int row0 = rb * 64;
  const int n0 = cb * 192;
  const int g4 = lane >> 4, l15 = lane & 15;
  STAMP(0, 0);
  RT(0, 0);
  AK_DECL;
  AK(0);

  char* sE = smem + L1_STAGE;
  float2* sRow = (float2*)sE;
  float* sCs = (float*)(sE + 1024);
  float* sStat = (float*)(sE + 2048);
  char* sHand = sE + 8192;
  char* sTr = sE + 40960 + wave * 2048;
  const bool is_z2 = cb >= 4;
  auto epi = [&](const f32x4 (&t)[2][2], int wm, int cg, int swp, int rsw) {
    f32x4 csv[2];
#pragma unroll
    for (int jj = 0; jj < 2; ++jj) csv[jj] = *(const f32x4*)(sCs + cg * 32 + (jj ^ swp) * 16 + 4 * g4);
    f32x2 gv[2][2][2];
    unsigned pk[2][2][2];
#pragma unroll
    for (int i = 0; i < 2; ++i) {
      const float2 rs = sRow[wm * 32 + (i ^ rsw) * 16 + l15];
      const f32x2 nm = {-rs.x, -rs.x}, rd = {rs.y, rs.y};
#pragma unroll
      for (int jj = 0; jj < 2; ++jj)
#pragma unroll
        for (int h = 0; h < 2; ++h) {
          const f32x2 av = {t[i][jj][2 * h], t[i][jj][2 * h + 1]};
          const f32x2 cv = {csv[jj][2 * h], csv[jj][2 * h + 1]};
          gv[i][jj][h] = gelu2(__builtin_elementwise_fma(nm, cv, av) * rd);
          pk[i][jj][h] = pk2(gv[i][jj][h]);
        }
    }
    if (!is_z2) {
      const int lp = l15 + 16 * (2 * ((g4 & 1) ^ swp) + (g4 >> 1));
      char* zdst = (char*)z1 + (size_t)((rb * 4 + wm * 2) * 24 + cb * 6 + cg) * 1024 + lp * 16;
      u32x4 o[2];
#pragma unroll
      for (int i = 0; i < 2; ++i) {
        const auto sa = __builtin_amdgcn_permlane16_swap(pk[i][0][0], pk[i][1][0], false, false);
        const auto sb = __builtin_amdgcn_permlane16_swap(pk[i][0][1], pk[i][1][1], false, false);
        o[i][0] = sa[0]; o[i][1] = sb[0]; o[i][2] = sa[1]; o[i][3] = sb[1];
      }
#pragma unroll
      for (int i = 0; i < 2; ++i) ST16(zdst + (i ^ rsw) * 24 * 1024, ((u32x4){o[i][0], o[i][1], o[i][2], HOLE}), 1);
      *(u32x2*)((char*)z1 + Z1X + ((rb * 2 + wm) * 24 + cb * 6 + cg) * 512 + lp * 8) = (u32x2){rsw ? o[1][3] : o[0][3], rsw ? o[0][3] : o[1][3]};
    } else {
#pragma unroll
      for (int jj = 0; jj < 2; ++jj) {
        char* wb = sTr + l15 * 64 + 8 * ((4 * (jj ^ swp) + g4) ^ (l15 >> 1));
#pragma unroll
        for (int i = 0; i < 2; ++i) *(u32x2*)(wb + (i ^ rsw) * 1024) = (u32x2){pk[i][jj][0], pk[i][jj][1]};
      }
      const unsigned q = l15 >> 2, p = lane & 3;
      const unsigned ra = lds_addr(sTr) + (8 * g4 + q) * 64 + 8 * (p ^ (4 * (g4 & 1) + (q >> 1)));
      s16x4 tr[2][2];
      tr[0][0] = tr_asm<0>(ra); tr[0][1] = tr_asm<256>(ra ^ 16u);
      tr[1][0] = tr_asm<0>(ra ^ 32u); tr[1][1] = tr_asm<256>(ra ^ 48u);
      float sv[2][2], pv[2];
#pragma unroll
      for (int i = 0; i < 2; ++i) {
        const unsigned p0 = __float_as_uint(gv[i][0][0][0]);
        const auto pa = __builtin_amdgcn_permlane16_swap(p0, p0, false, false);
        const auto pb = __builtin_amdgcn_permlane32_swap(pa[0], pa[0], false, false);
        pv[i] = __uint_as_float(pb[0]);
        const f32x2 pp = {pv[i], pv[i]};
        const f32x2 d0 = gv[i][0][0] - pp, d1 = gv[i][0][1] - pp, d2 = gv[i][1][0] - pp, d3 = gv[i][1][1] - pp;
        const f32x2 s1 = (d0 + d1) + (d2 + d3);
        const f32x2 s2 = __builtin_elementwise_fma(d0, d0, d1 * d1) + __builtin_elementwise_fma(d2, d2, d3 * d3);
        sv[i][0] = s1[0] + s1[1]; sv[i][1] = s2[0] + s2[1];
      }
      const auto u0 = __builtin_amdgcn_permlane16_swap(__float_as_uint(sv[0][0]), __float_as_uint(sv[0][1]), false, false);
      const auto u1 = __builtin_amdgcn_permlane16_swap(__float_as_uint(sv[1][0]), __float_as_uint(sv[1][1]), false, false);
      const float w0 = __uint_as_float(u0[0]) + __uint_as_float(u0[1]), w1 = __uint_as_float(u1[0]) + __uint_as_float(u1[1]);
      const auto u2 = __builtin_amdgcn_permlane32_swap(__float_as_uint(w0), __float_as_uint(w1), false, false);
      float* sd = sStat + ((wm * 32 + ((g4 >> 1) ^ rsw) * 16 + l15) * 6 + cg) * 4 + (g4 & 1);
      sd[0] = __uint_as_float(u2[0]) + __uint_as_float(u2[1]);
      sd[2] = (g4 >> 1) ? pv[1] : pv[0];
      asm volatile("s_waitcnt lgkmcnt(0)" : "+v"(tr[0][0]), "+v"(tr[0][1]), "+v"(tr[1][0]), "+v"(tr[1][1]) :: "memory");
      char* zdst = (char*)z2 + (size_t)((rb * 2 + wm) * 48 + (cb - 4) * 12 + cg * 2) * 1024 + lane * 16;
      const bool tag = ((rb * 2 + wm) & 15) >= S0;
      u32x4 o[2];
#pragma unroll
      for (int jj = 0; jj < 2; ++jj) {
        o[jj] = __builtin_bit_cast(u32x4, join8(tr[jj][0], tr[jj][1]));
        ST16(zdst + jj * 1024, ((u32x4){o[jj][0], o[jj][1], o[jj][2], tag ? HOLE : o[jj][3]}), 2);
      }
      *(u32x2*)((char*)z2 + Z1X + ((rb * 2 + wm) * 24 + (cb - 4) * 6 + cg) * 512 + lane * 8) = (u32x2){o[0][3], o[1][3]};
    }
  };

  if (__builtin_expect(wave >= 4, 1)) {
    const int lw = wave - 4;
    const unsigned voA = (unsigned)((lw * 8 + (lane >> 3)) * (D_ * 2) + (((lane & 7) ^ (lane >> 3)) << 4));
    const unsigned voB = (unsigned)((lw * 8 + ((lane >> 2) & 7)) * (F2_ * 2) + (lane >> 5) * 64 + (((lane & 3) ^ ((lw * 2 + ((lane >> 4) & 1)) & 3)) << 4));
    const char* gA = (const char*)x + (size_t)row0 * (D_ * 2);
    const char* gB = (const char*)w_in + n0 * 2;
    const unsigned lbase = __builtin_amdgcn_readfirstlane(lds_addr(smem));
    auto issue = [&](int kt) {
      const unsigned st = lbase + (kt % 5) * L1_STAGE;
      asm volatile("s_mov_b32 m0, %2\n\ts_nop 0\n\tglobal_load_lds_dwordx4 %0, %1 " NTA :: "v"(voA), "s"(gA + kt * 128), "s"(st + lw * 1024) : "memory");
#pragma unroll
      for (int i = 0; i < 3; ++i)
        asm volatile("s_mov_b32 m0, %2\n\ts_nop 0\n\tglobal_load_lds_dwordx4 %0, %1 " NTB
                     :: "v"(voB), "s"(gB + (size_t)kt * (64 * F2_ * 2) + i * 128), "s"(st + L1_A_BYTES + (lw * 3 + i) * 1024) : "memory");
    };
#define LPRIO 2
    __builtin_amdgcn_s_setprio(LPRIO);
    issue(0);
    AK(1);
    __builtin_amdgcn_s_barrier();
    issue(1); issue(2);
    AK(2);
    const int ks0 = row0 + lw * 8, kd = cb * 80 + min(lane, 39) * 2;
    const int wfr = ((xcd >> 1) * 32 + idx) * 8 + lw, wfi = min(wfr, 959);
    u32x4 sw;
    const char* kvp[3];
#pragma unroll
    for (int i = 0; i < 3; ++i) {
      const int id = min(i * 64 + lane, 189), r = id / 10, c = id % 10;
      kvp[i] = (r < 11 ? (const char*)kin + (size_t)max(ks0 - 3 + r, 0) * (D_ * 2) : (const char*)vin + (size_t)(ks0 + r - 11) * (D_ * 2)) + cb * 160 + c * 16;
    }
    auto kvload = [&]() {
#pragma unroll
      for (int i = 0; i < 3; ++i)
        asm volatile("s_mov_b32 m0, %1\n\ts_nop 0\n\tglobal_load_lds_dwordx4 %0, off " NTK :: "v"(kvp[i]), "s"(lbase + lw * 3072 + i * 1024) : "memory");
      asm volatile("s_mov_b32 m0, %2\n\ts_nop 0\n\tglobal_load_lds_dwordx4 %0, %1 " NTK
                   :: "v"((unsigned)((lane >> 1) * (D_ * 2) + (lane & 1) * 16)), "s"(w_out + (size_t)(wfi / 40) * 32 * D_ + (wfi % 40) * 16), "s"(lbase + 24576 + lw * 1024) : "memory");
      asm volatile("global_load_dwordx4 %0, %1, %2" : "=&v"(sw) : "v"(kd * 8), "s"(shift_k) : "memory");
    };
    auto womath = [&]() {
      const unsigned ra = lbase + 24576 + lw * 1024 + (8 * g4 + (l15 >> 2)) * 32 + 8 * (lane & 3);
      s16x4 lo = tr_asm<0>(ra), hi = tr_asm<128>(ra);
      asm volatile("s_waitcnt lgkmcnt(0)" : "+v"(lo), "+v"(hi) :: "memory");
      ST16((char*)woF + (size_t)wfi * 1024 + lane * 16, __builtin_bit_cast(u32x4, join8(lo, hi)), 4);
    };
    auto kvmath = [&]() {
      unsigned kk[11], vv[8];
      const char* kl = smem + lw * 3072 + min(lane, 39) * 4;
#pragma unroll
      for (int i = 0; i < 11; ++i) kk[i] = *(const unsigned*)(kl + i * 160);
#pragma unroll
      for (int i = 0; i < 8; ++i) vv[i] = *(const unsigned*)(kl + (11 + i) * 160);
      const f32x2 w0 = {lo16(sw[0]), lo16(sw[2])}, w1 = {hi16(sw[0]), hi16(sw[2])};
      const f32x2 w2 = {lo16(sw[1]), lo16(sw[3])}, w3 = {hi16(sw[1]), hi16(sw[3])};
      f32x2 kf[11], rr[8];
#pragma unroll
      for (int i = 0; i < 11; ++i) kf[i] = unpk((i < 3 && ks0 == 0) ? 0u : kk[i]);
#pragma unroll
      for (int i = 0; i < 8; ++i) {
        f32x2 a0 = kf[i] * w3;
        a0 = __builtin_elementwise_fma(kf[i + 1], w2, a0);
        a0 = __builtin_elementwise_fma(kf[i + 2], w1, a0);
        a0 = __builtin_elementwise_fma(kf[i + 3], w0, a0);
        rr[i] = unpk(pk2(a0)) * unpk(vv[i]);
      }
      u32x4 oa, ob;
#pragma unroll
      for (int e = 0; e < 4; ++e) { oa[e] = pack2(rr[2 * e][0], rr[2 * e + 1][0]); ob[e] = pack2(rr[2 * e][1], rr[2 * e + 1][1]); }
      if (lane < 40) {
        ST16(kvT + (size_t)kd * S_ + ks0, ((u32x4){oa[0], oa[1], oa[2], HOLE}), 8);
        ST16(kvT + (size_t)(kd + 1) * S_ + ks0, ((u32x4){ob[0], ob[1], ob[2], HOLE}), 8);
        *(u32x2*)((char*)kvT + KVX + kd * 1024 + ks0) = (u32x2){oa[3], ob[3]};
      }
    };
#define PFK 8
#define PFF 4
#define KVM 1
#define SPF 0
#define SP0 1
#define SPN 3
    unsigned long long pfs = 0;
    const char* spb = (const char*)w_sp + (size_t)(row0 + (SPF == 1 ? cb * 8 + lw : (idx & 3) * 16 + lw * 2)) * 4096;
    auto spf = [&](int i) {
      const int r = i & (SPF == 1 ? 0 : 1), k = SPF == 1 ? i : i >> 1;
      asm volatile("s_load_dwordx2 %0, %1, %2" : "+s"(pfs) : "s"(spb), "i"(r * 4096 + (k & 3) * 1024 + (k >> 2) * 256 + 124) : "memory");
    };
    constexpr int NPF = PFF == 1 ? 2 : 1;
    unsigned pfv, pfw = 0;
    auto prefetch = [&]() {
      const int t_ = tid - 256;
      if (PFF == 1) {
        const char* pw = (const char*)w_sp + (size_t)(row0 + (idx & 3) * 16 + (t_ >> 5)) * 4096 + (t_ & 31) * 128;
        const int t2 = t_ % 96;
        const char* pa = (const char*)ssm_k + (t2 < 64 ? (size_t)b * 8192 + t2 * 128 : (size_t)(512 + (b & 127)) * 4096 + (t2 & 31) * 128);
        asm volatile("global_load_dword %0, %1, off" : "=&v"(pfw) : "v"(pa) : "memory");
        asm volatile("global_load_dword %0, %1, off" : "=&v"(pfv) : "v"(pw) : "memory");
      } else {
        constexpr int NW = 64 * PFF;
        const int t2 = min(max(t_ - NW, 0), 95);
        const char* pw = (const char*)w_sp + (size_t)(row0 + (idx & 3) * 16 + (t_ / (4 * PFF)) % 16) * 4096 + ((t_ / PFF) & 3) * 1024 + (t_ % PFF) * 128;
        const char* pa = (const char*)ssm_k + (t2 < 64 ? (size_t)b * 8192 + t2 * 128 : (size_t)(512 + (b & 127)) * 4096 + (t2 & 31) * 128);
        const char* pc_ = (!SPF && t_ < NW) ? pw : pa;
        asm volatile("global_load_dword %0, %1, off " NTP : "=&v"(pfv) : "v"(pc_) : "memory");
      }
    };
#pragma unroll
    for (int kt = 0; kt < 10; ++kt) {
      const int nv = (kt + 1 < 10 ? 4 : 0) + (kt + 2 < 10 ? 4 : 0) + (kt >= 8 ? 5 : 0) + ((PFK < kt && kt <= PFK + 3) ? NPF : 0);
      asm volatile("s_waitcnt vmcnt(%0)" :: "n"(nv) : "memory");
      __builtin_amdgcn_s_barrier();
      if (kt == 0) AK(3); if (kt == 1) AK(4); if (kt == 3) AK(5); if (kt == 5) AK(6); if (kt == 7) AK(7); if (kt == 9) AK(8);
      if (kt + 3 < 10) issue(kt + 3);
      if (kt == 7) kvload();
      if (kt == PFK) prefetch();
      if (SPF && kt >= SP0) {
#pragma unroll
        for (int i = (kt - SP0) * SPN; i < (kt - SP0 + 1) * SPN && i < 16 * SPF; ++i) spf(i);
      }
    }
    __builtin_amdgcn_s_setprio(0);
    if (KVM == 0) {
      asm volatile("s_waitcnt vmcnt(%1)" : "+v"(sw) : "n"(PFK >= 7 ? NPF : 0) : "memory");
      kvmath();
      if (!is_z2 && wfr < 960) womath();
    }
    SB();
    asm volatile("s_waitcnt lgkmcnt(0)" : "+s"(pfs) :: "memory");
    RAW_BARRIER();
    AK(9);
    {
      const int w = lw >> 1, gs = lw & 1;
      f32x4 t4[2][2];
#pragma unroll
      for (int q = 0; q < 4; ++q) t4[q >> 1][q & 1] = *(const f32x4*)(sHand + ((w * 2 + gs) * 4 + q) * 1024 + lane * 16);
      epi(t4, w >> 1, (w & 1) * 3 + 1 + gs, 0, 0);
    }
    AK(10);
    if (is_z2) RAW_BARRIER();
    AK(11);
    if (KVM == 1) {
      SB();
      if (is_z2) asm volatile("s_waitcnt vmcnt(%0)" :: "n"(2 + (PFK >= 7 ? NPF : 0)) : "memory");
      asm volatile("s_waitcnt vmcnt(%1)" : "+v"(sw) : "n"(3 + (PFK >= 7 ? NPF : 0)) : "memory");
      kvmath();
      if (!is_z2 && wfr < 960) womath();
    }
    AK(12);
    asm volatile("s_waitcnt vmcnt(0)" : "+v"(pfw), "+v"(pfv) :: "memory");
    AK(13);
    AK_OUT(14);
    RT(0, 1);
    return;
  }

  unsigned pcv, pfx;
  {
    unsigned long long pc; unsigned clen;
    asm volatile("s_getpc_b64 %0\n.Lpc_here_%=:\n\ts_mov_b32 %1, k_end-.Lpc_here_%=" : "=s"(pc), "=s"(clen));
    const unsigned o = idx < 2 ? min((unsigned)((idx * 4 + wave) * 8192 + lane * 128), clen - 4u) & ~3u : 0u;
    asm volatile("global_load_dword %0, %1, %2" : "=&v"(pcv) : "v"(o), "s"(pc) : "memory");
  }
  __builtin_amdgcn_s_barrier();
  auto pfx_issue = [&]() {
    const int t2 = max(tid - 80, 0), r2 = min(t2 / 3, 39);
    const char* pa = (const char*)x + (size_t)(row0 + (idx & 3) * 16 + tid / 5) * (D_ * 2) + 640 + (tid % 5) * 128;
    const char* pb = (const char*)w_in + (size_t)(320 + (idx >> 2) * 40 + r2) * (F2_ * 2) + n0 * 2 + (t2 % 3) * 128;
    const char* pc_ = tid < 80 ? pa : pb;
    asm volatile("global_load_dword %0, %1, off " NTX : "=&v"(pfx) : "v"(pc_) : "memory");
  };
#define PFXM 0
  if (PFXM == 0) pfx_issue();
  if (PFXM == 2) pfx = 0;
  const int wm = wave >> 1, wn = wave & 1;
  f32x4 acc[2][6], acs[3], accS = {0.f, 0.f, 0.f, 0.f}, accG = {0.f, 0.f, 0.f, 0.f};
#pragma unroll
  for (int j = 0; j < 6; ++j) {
    acc[0][j] = (f32x4){0.f, 0.f, 0.f, 0.f};
    acc[1][j] = (f32x4){0.f, 0.f, 0.f, 0.f};
  }
#pragma unroll
  for (int j = 0; j < 3; ++j) acs[j] = (f32x4){0.f, 0.f, 0.f, 0.f};
  unsigned tb[2][2], aA[2][2];
#pragma unroll
  for (int c1 = 0; c1 < 2; ++c1)
#pragma unroll
    for (int h = 0; h < 2; ++h)
      tb[c1][h] = lds_addr(smem) + L1_A_BYTES + wn * 3 * 512 + boff<6>(8 * g4 + 4 * h + (l15 >> 2), 2 * (c1 ^ wm) + ((lane & 3) >> 1)) + 8 * (lane & 1);
#pragma unroll
  for (int ks = 0; ks < 2; ++ks)
#pragma unroll
    for (int i = 0; i < 2; ++i) aA[ks][i] = lds_addr(smem) + (wm * 32 + (i ^ wn) * 16 + l15) * 128 + (((ks * 4 + g4) ^ (l15 & 7)) << 4);
  bf16x8 ones;
#pragma unroll
  for (int e = 0; e < 8; ++e) ones[e] = (short)0x3F80;
  int cnt = 10, slot = 0, sdl;
  SB();
  __builtin_amdgcn_s_barrier();
  STAMP(0, 1);
  if (PFXM == 1) pfx_issue();
  asm volatile(
      "s_nop 1\n\t"
      "ds_read_b128 v[96:99], %21\n\t"
      "ds_read_b64_tr_b16 v[104:105], %17 offset:0\n\t"
      "ds_read_b64_tr_b16 v[106:107], %18 offset:0\n\t"
      "ds_read_b128 v[100:103], %22\n\t"
      "ds_read_b64_tr_b16 v[108:109], %19 offset:0\n\t"
      "ds_read_b64_tr_b16 v[110:111], %20 offset:0\n\t"
      "ds_read_b64_tr_b16 v[112:113], %17 offset:512\n\t"
      "ds_read_b64_tr_b16 v[114:115], %18 offset:512\n\t"
      "ds_read_b64_tr_b16 v[116:117], %19 offset:512\n\t"
      "ds_read_b64_tr_b16 v[118:119], %20 offset:512\n\t"
      "ds_read_b64_tr_b16 v[120:121], %17 offset:1024\n\t"
      "ds_read_b64_tr_b16 v[122:123], %18 offset:1024\n\t"
      "ds_read_b64_tr_b16 v[124:125], %19 offset:1024\n\t"
      "ds_read_b64_tr_b16 v[126:127], %20 offset:1024\n\t"
      ".Ll1_%=:\n\t"
      "s_waitcnt lgkmcnt(11)\n\t"
      "v_mfma_f32_16x16x32_bf16 %0, v[104:107], v[96:99], %0\n\t"
      "ds_read_b128 v[128:131], %23\n\t"
      "s_waitcnt lgkmcnt(11)\n\t"
      "v_mfma_f32_16x16x32_bf16 %6, v[104:107], v[100:103], %6\n\t"
      "ds_read_b64_tr_b16 v[136:137], %17 offset:12288\n\t"
      "s_waitcnt lgkmcnt(10)\n\t"
      "v_mfma_f32_16x16x32_bf16 %1, v[108:111], v[96:99], %1\n\t"
      "ds_read_b64_tr_b16 v[138:139], %18 offset:12288\n\t"
      "v_mfma_f32_16x16x32_bf16 %7, v[108:111], v[100:103], %7\n\t"
      "ds_read_b128 v[132:135], %24\n\t"
      "s_waitcnt lgkmcnt(10)\n\t"
      "v_mfma_f32_16x16x32_bf16 %2, v[112:115], v[96:99], %2\n\t"
      "ds_read_b64_tr_b16 v[140:141], %19 offset:12288\n\t"
      "v_mfma_f32_16x16x32_bf16 %8, v[112:115], v[100:103], %8\n\t"
      "ds_read_b64_tr_b16 v[142:143], %20 offset:12288\n\t"
      "s_waitcnt lgkmcnt(10)\n\t"
      "v_mfma_f32_16x16x32_bf16 %3, v[116:119], v[96:99], %3\n\t"
      "ds_read_b64_tr_b16 v[144:145], %17 offset:12800\n\t"
      "v_mfma_f32_16x16x32_bf16 %9, v[116:119], v[100:103], %9\n\t"
      "ds_read_b64_tr_b16 v[146:147], %18 offset:12800\n\t"
      "s_waitcnt lgkmcnt(10)\n\t"
      "v_mfma_f32_16x16x32_bf16 %4, v[120:123], v[96:99], %4\n\t"
      "ds_read_b64_tr_b16 v[148:149], %19 offset:12800\n\t"
      "v_mfma_f32_16x16x32_bf16 %10, v[120:123], v[100:103], %10\n\t"
      "ds_read_b64_tr_b16 v[150:151], %20 offset:12800\n\t"
      "s_waitcnt lgkmcnt(10)\n\t"
      "v_mfma_f32_16x16x32_bf16 %5, v[124:127], v[96:99], %5\n\t"
      "ds_read_b64_tr_b16 v[152:153], %17 offset:13312\n\t"
      "v_mfma_f32_16x16x32_bf16 %11, v[124:127], v[100:103], %11\n\t"
      "ds_read_b64_tr_b16 v[154:155], %18 offset:13312\n\t"
      "v_mfma_f32_16x16x32_bf16 %15, %28, v[96:99], %15\n\t"
      "ds_read_b64_tr_b16 v[156:157], %19 offset:13312\n\t"
      "v_mfma_f32_16x16x32_bf16 %16, v[96:99], v[96:99], %16\n\t"
      "ds_read_b64_tr_b16 v[158:159], %20 offset:13312\n\t"
      "v_mfma_f32_16x16x32_bf16 %12, v[104:107], %28, %12\n\t"
      "v_mfma_f32_16x16x32_bf16 %13, v[112:115], %28, %13\n\t"
      "v_mfma_f32_16x16x32_bf16 %14, v[120:123], %28, %14\n\t"
      "s_add_u32 %26, %26, 1\n\t"
      "s_cmp_eq_u32 %26, 5\n\t"
      "s_cselect_b32 %27, 0xfffd8000, 0\n\t"
      "s_cselect_b32 %26, 0, %26\n\t"
      "s_add_u32 %27, %27, 0x8000\n\t"
      "s_sub_u32 %25, %25, 1\n\t"
      "s_cmp_eq_u32 %25, 0\n\t"
      "s_cbranch_scc1 .Ll1s_%=\n\t"
      "s_barrier\n\t"
      ".Ll1s_%=:\n\t"
      "s_waitcnt lgkmcnt(11)\n\t"
      "v_mfma_f32_16x16x32_bf16 %0, v[136:139], v[128:131], %0\n\t"
      "v_add_u32 %21, %27, %21\n\t"
      "ds_read_b128 v[96:99], %21\n\t"
      "s_waitcnt lgkmcnt(11)\n\t"
      "v_mfma_f32_16x16x32_bf16 %6, v[136:139], v[132:135], %6\n\t"
      "v_add_u32 %17, %27, %17\n\t"
      "ds_read_b64_tr_b16 v[104:105], %17 offset:0\n\t"
      "s_waitcnt lgkmcnt(10)\n\t"
      "v_mfma_f32_16x16x32_bf16 %1, v[140:143], v[128:131], %1\n\t"
      "v_add_u32 %18, %27, %18\n\t"
      "ds_read_b64_tr_b16 v[106:107], %18 offset:0\n\t"
      "v_mfma_f32_16x16x32_bf16 %7, v[140:143], v[132:135], %7\n\t"
      "v_add_u32 %22, %27, %22\n\t"
      "ds_read_b128 v[100:103], %22\n\t"
      "s_waitcnt lgkmcnt(10)\n\t"
      "v_mfma_f32_16x16x32_bf16 %2, v[144:147], v[128:131], %2\n\t"
      "v_add_u32 %19, %27, %19\n\t"
      "ds_read_b64_tr_b16 v[108:109], %19 offset:0\n\t"
      "v_mfma_f32_16x16x32_bf16 %8, v[144:147], v[132:135], %8\n\t"
      "v_add_u32 %20, %27, %20\n\t"
      "ds_read_b64_tr_b16 v[110:111], %20 offset:0\n\t"
      "s_waitcnt lgkmcnt(10)\n\t"
      "v_mfma_f32_16x16x32_bf16 %3, v[148:151], v[128:131], %3\n\t"
      "v_add_u32 %23, %27, %23\n\t"
      "ds_read_b64_tr_b16 v[112:113], %17 offset:512\n\t"
      "v_mfma_f32_16x16x32_bf16 %9, v[148:151], v[132:135], %9\n\t"
      "v_add_u32 %24, %27, %24\n\t"
      "ds_read_b64_tr_b16 v[114:115], %18 offset:512\n\t"
      "s_waitcnt lgkmcnt(10)\n\t"
      "v_mfma_f32_16x16x32_bf16 %4, v[152:155], v[128:131], %4\n\t"
      "ds_read_b64_tr_b16 v[116:117], %19 offset:512\n\t"
      "v_mfma_f32_16x16x32_bf16 %10, v[152:155], v[132:135], %10\n\t"
      "ds_read_b64_tr_b16 v[118:119], %20 offset:512\n\t"
      "s_waitcnt lgkmcnt(10)\n\t"
      "v_mfma_f32_16x16x32_bf16 %5, v[156:159], v[128:131], %5\n\t"
      "ds_read_b64_tr_b16 v[120:121], %17 offset:1024\n\t"
      "v_mfma_f32_16x16x32_bf16 %11, v[156:159], v[132:135], %11\n\t"
      "ds_read_b64_tr_b16 v[122:123], %18 offset:1024\n\t"
      "v_mfma_f32_16x16x32_bf16 %15, %28, v[128:131], %15\n\t"
      "ds_read_b64_tr_b16 v[124:125], %19 offset:1024\n\t"
      "v_mfma_f32_16x16x32_bf16 %16, v[128:131], v[128:131], %16\n\t"
      "ds_read_b64_tr_b16 v[126:127], %20 offset:1024\n\t"
      "v_mfma_f32_16x16x32_bf16 %12, v[136:139], %28, %12\n\t"
      "v_mfma_f32_16x16x32_bf16 %13, v[144:147], %28, %13\n\t"
      "v_mfma_f32_16x16x32_bf16 %14, v[152:155], %28, %14\n\t"
      "s_cmp_lg_u32 %25, 0\n\t"
      "s_cbranch_scc1 .Ll1_%=\n\t"
      "s_waitcnt lgkmcnt(0)\n\t"
      "s_nop 15\n\t"
      "s_nop 7\n\t"
      : "+v"(acc[0][0]), "+v"(acc[0][1]), "+v"(acc[0][2]), "+v"(acc[0][3]), "+v"(acc[0][4]), "+v"(acc[0][5]),
        "+v"(acc[1][0]), "+v"(acc[1][1]), "+v"(acc[1][2]), "+v"(acc[1][3]), "+v"(acc[1][4]), "+v"(acc[1][5]),
        "+v"(acs[0]), "+v"(acs[1]), "+v"(acs[2]), "+v"(accS), "+v"(accG), "+v"(tb[0][0]), "+v"(tb[0][1]), "+v"(tb[1][0]), "+v"(tb[1][1]),
        "+v"(aA[0][0]), "+v"(aA[0][1]), "+v"(aA[1][0]), "+v"(aA[1][1]), "+s"(cnt), "+s"(slot), "=&s"(sdl)
      : "v"(ones)
      : "memory", "scc", "v96", "v97", "v98", "v99", "v100", "v101", "v102", "v103", "v104", "v105", "v106", "v107", "v108", "v109", "v110", "v111", "v112", "v113", "v114", "v115", "v116", "v117", "v118", "v119", "v120", "v121", "v122", "v123", "v124", "v125", "v126", "v127", "v128", "v129", "v130", "v131", "v132", "v133", "v134", "v135", "v136", "v137", "v138", "v139", "v140", "v141", "v142", "v143", "v144", "v145", "v146", "v147", "v148", "v149", "v150", "v151", "v152", "v153", "v154", "v155", "v156", "v157", "v158", "v159");
  STAMP(0, 3);
  AK(8);
#define PFC 0
  unsigned pfc = 0;
  auto pfc_issue = [&]() {
    const char* pw = (const char*)w_sp + (size_t)(row0 + (idx & 3) * 16 + (tid >> 4)) * 4096 + ((tid >> 2) & 3) * 1024 + (4 + (tid & 3)) * 128;
    asm volatile("global_load_dword %0, %1, off" : "=&v"(pfc) : "v"(pw) : "memory");
  };
  if (PFC == 1) pfc_issue();

#pragma unroll
  for (int gs = 0; gs < 2; ++gs)
#pragma unroll
    for (int q = 0; q < 4; ++q)
      *(f32x4*)(sHand + ((wave * 2 + gs) * 4 + (q ^ wm ^ (2 * wn))) * 1024 + lane * 16) = acc[q >> 1][2 + 2 * gs + (q & 1)];
  if (l15 == 0) {
#pragma unroll
    for (int j = 0; j < 3; ++j) *(f32x4*)(sCs + wn * 96 + (2 * j + wm) * 16 + 4 * g4) = acs[j];
  }
  {
    const int r = wm * 32 + wn * 16 + l15;
    const bool dg = g4 == (l15 >> 2);
    const float mean = accS[0] * (1.0f / 640.0f);
    const float s2 = (l15 & 2) ? ((l15 & 1) ? accG[3] : accG[2]) : ((l15 & 1) ? accG[1] : accG[0]);
    float sq = s2 - accS[0] * mean;
    if (__builtin_expect(__builtin_amdgcn_ballot_w64(dg && !(sq >= 1e-3f * accS[0] * mean)) != 0, 0)) {
      const f32x2 mm = {mean, mean};
      f32x2 q2 = {0.f, 0.f};
      const u32x4* xp = (const u32x4*)(x + (size_t)(row0 + r) * D_ + g4 * 160);
#pragma unroll 1
      for (int c = 0; c < 20; ++c) {
        const u32x4 xv = xp[c];
#pragma unroll
        for (int e = 0; e < 4; ++e) { const f32x2 d = unpk(xv[e]) - mm; q2 = __builtin_elementwise_fma(d, d, q2); }
      }
      sq = allred_g(q2[0] + q2[1]);
    }
    const float mbf = rbf(mean);
    const float vbf = rbf(fmaxf(sq, 0.f) * (1.0f / 640.0f));
    const float den = rbf(__builtin_amdgcn_sqrtf(rbf(vbf + 1e-5f)));
    const float rden = __builtin_amdgcn_rcpf(den);
    if (dg) {
      sRow[r] = make_float2(mbf, rden);
      if (cb == 0) xst[row0 + r] = make_float2(mbf, rden);
    }
  }
  TS_DECL;
  TS(0);
  AK(7);
  RAW_BARRIER();
  AK(9);
  TS(1);
  if (PFC == 2) pfc_issue();
  {
    f32x4 t4[2][2];
#pragma unroll
    for (int q = 0; q < 4; ++q) t4[q >> 1][q & 1] = acc[q >> 1][q & 1];
    epi(t4, wm, wn * 3, wm, wn);
  }
  TS(2); AK(10);
  if (is_z2) RAW_BARRIER();
  TS(3); AK(11);
  TS(4);
  if (is_z2 && tid < 64) {
    float mc[6];
    float sum = 0.f, m2 = 0.f;
#pragma unroll
    for (int c = 0; c < 6; ++c) {
      const f32x4 pp = *(const f32x4*)(sStat + (tid * 6 + c) * 4);
      const float dl = pp[0] * (1.0f / 32.0f);
      mc[c] = pp[2] + dl;
      sum += mc[c];
      m2 += fmaxf(pp[1] - pp[0] * dl, 0.f);
    }
    const float mb = sum * (1.0f / 6.0f);
    float dq = 0.f;
#pragma unroll
    for (int c = 0; c < 6; ++c) { const float dd = mc[c] - mb; dq += dd * dd; }
    st2[(size_t)(cb - 4) * S_ + row0 + tid] = make_float2(mb, m2 + 32.0f * dq);
  }
  TS(5); AK(12);
  asm volatile("s_waitcnt vmcnt(0)" : "+v"(pcv), "+v"(pfx), "+v"(pfc) :: "memory");
  TS(6); AK(13);
  AK_OUT(14);
  TS_OUT(0, 8, 7, TSW);
  STAMP(0, 4);
  RT(0, 1);
}

constexpr int CV_C = 4608, CV_CS = 4160, CV_BUF = CV_C + 2 * CV_CS;
__device__ __forceinline__ void conv_core(unsigned buf, int lane, f32x4 (&ac)[8]) {
  const int l15 = lane & 15, g4 = lane >> 4;
  const int c = (l15 + 1) & 1;
  unsigned aa[4];
#pragma unroll
  for (int k = 0; k < 4; ++k) aa[k] = buf + CV_C + c * CV_CS + (2031 - l15 + 8 * g4 - c) * 2 - 512 - 1024 * k;
  unsigned bb = buf + 512 + 32 * l15 - 32 + 16 * g4;
  u32x2 A[2][8][2];
  u32x4 B[2][8];
  constexpr int TT[36] = {7, 6, 5, 4, 7, 6, 5, 4, 7, 6, 5, 3, 7, 6, 5, 4, 7, 6, 3, 2, 7, 6, 5, 4, 7, 3, 2, 1, 7, 6, 5, 4, 3, 2, 1, 0};
  constexpr int AA[36] = {0, 0, 0, 0, 1, 1, 1, 1, 2, 2, 2, 0, 3, 3, 3, 2, 4, 4, 1, 0, 5, 5, 4, 3, 6, 2, 1, 0, 7, 6, 5, 4, 3, 2, 1, 0};
  auto ld1 = [&](int p, int n) {
    if (n < 16) {
      const int a = n >> 1, o = ((a & 1) ? 0 : 128) + 2 * (n & 1);
      asm volatile("ds_read2_b32 %0, %1 offset0:%2 offset1:%3" : "=&v"(A[p][(n >> 1) & 7][n & 1]) : "v"(aa[(a >> 1) & 3]), "i"(o), "i"(o + 1) : "memory");
    } else
      asm volatile("ds_read_b128 %0, %1 offset:%2" : "=&v"(B[p][n & 7]) : "v"(bb), "i"(512 * (n & 7)) : "memory");
  };
  auto next = [&](int dl) {
#pragma unroll
    for (int k = 0; k < 4; ++k) aa[k] += dl;
    bb += dl;
    SB();
  };
  auto land = [&](int p) {
    asm volatile("s_waitcnt lgkmcnt(0)" : "+v"(A[p][0][0]), "+v"(A[p][0][1]), "+v"(A[p][1][0]), "+v"(A[p][1][1]), "+v"(A[p][2][0]), "+v"(A[p][2][1]),
                 "+v"(A[p][3][0]), "+v"(A[p][3][1]), "+v"(A[p][4][0]), "+v"(A[p][4][1]), "+v"(A[p][5][0]), "+v"(A[p][5][1]), "+v"(A[p][6][0]), "+v"(A[p][6][1]),
                 "+v"(A[p][7][0]), "+v"(A[p][7][1]), "+v"(B[p][0]), "+v"(B[p][1]), "+v"(B[p][2]), "+v"(B[p][3]), "+v"(B[p][4]), "+v"(B[p][5]),
                 "+v"(B[p][6]), "+v"(B[p][7]) :: "memory");
    SB();
  };
  auto mm = [&](int p) {
#pragma unroll
    for (int n = 0; n < 36; ++n) {
      const int t = TT[n], a = AA[n];
      const u32x4 av = {A[p][a][0][0], A[p][a][0][1], A[p][a][1][0], A[p][a][1][1]};
      asm volatile("v_mfma_f32_16x16x32_bf16 %0, %1, %2, %0" : "+v"(ac[t]) : "v"(av), "v"(B[p][t - a]));
      if (n < 24) ld1(p ^ 1, n);
      SB();
    }
    land(p ^ 1);
  };
#pragma unroll
  for (int t = 0; t < 8; ++t) ac[t] = (f32x4){0.f, 0.f, 0.f, 0.f};
#pragma unroll
  for (int n = 0; n < 24; ++n) ld1(0, n);
  land(0);
#pragma unroll 1
  for (int r = 0; r < (XE & 1 ? 0 : 8); r += 2) {
    next(-64);
    mm(0);
    next(r < 6 ? -64 : 0);
    mm(1);
  }
  asm volatile("s_nop 15" : "+v"(ac[0]), "+v"(ac[1]), "+v"(ac[2]), "+v"(ac[3]), "+v"(ac[4]), "+v"(ac[5]), "+v"(ac[6]), "+v"(ac[7]));
}

#define GTB 1
#define PMAP 1
#define GNL 0
#define PSL "1"
#define LP3 3
#define HPL 1
#define HSC ""
#define XSRC hf
#define CDR 0
#define SPR 1
#define DLY -1
#define PSLN 2
#define NUP 2
#define SGS 13
#define CVT 4u
#define BARP 0
#define XNS S0
#define CLP 1
#define NRP 0
#define CVW 0
#define ROT 0
#define XAM 7
#define XBM 15
#define YMP 3
#define YEX 1
#define BNT ""
#define XAS 128
#define TBL 1
#define PW0 4
#define WPF 0
#define ZSL 0
#define ZS2 0
#define PPF 4
#define WTA 16
#define NW1 10
#define NW0 0
constexpr int M_FLAG = 0;
constexpr int M_RT = 128;
constexpr int M_MT = M_RT + 8192;
constexpr int M_CV = M_MT + 4096;
constexpr int M_RING = M_CV + 3 * CV_BUF;
constexpr int P3_C = 128;
constexpr int P3_G = P3_C + 72 * 1024;
constexpr int P3_Y = M_RING + 4 * 24576;
constexpr int M_LDS = P3_Y + 8 * 1536;
static_assert(P3_G + 68 * 1024 <= P3_Y && M_LDS <= 163840 && M_RING <= P3_G, "LDS map");
#define GAS __attribute__((address_space(1)))
__device__ __forceinline__ void flag_publish(unsigned* f) { __hip_atomic_store((GAS unsigned*)f, 1u, __ATOMIC_RELAXED, __HIP_MEMORY_SCOPE_AGENT); }
__device__ __forceinline__ unsigned umx(unsigned a, unsigned b) { return a > b ? a : b; }
__device__ __forceinline__ unsigned max4(const u32x4& v, unsigned m) { return umx(umx(umx(m, v[0]), v[1]), umx(v[2], v[3])); }
__device__ __forceinline__ unsigned nohole(unsigned a) { return a < HOLE - 1 ? a : HOLE - 1; }

__device__ __forceinline__ void row_stat(const float (&mj)[4], const float (&qj)[4], float& mean, float& r) {
  mean = 0.25f * ((mj[0] + mj[1]) + (mj[2] + mj[3]));
  const float d0 = mj[0] - mean, d1 = mj[1] - mean, d2 = mj[2] - mean, d3 = mj[3] - mean;
  const float m2 = (qj[0] + qj[1]) + (qj[2] + qj[3]) + 192.0f * ((d0 * d0 + d1 * d1) + (d2 * d2 + d3 * d3));
  const float vbf = rbf(m2 * (1.0f / 768.0f));
  r = __builtin_amdgcn_rcpf(rbf(__builtin_amdgcn_sqrtf(rbf(vbf + 1e-5f))));
}
struct NormLd { u32x3 z[6]; u32x2 x[3]; f32x2 p[4]; };
__device__ __forceinline__ void norm_load(NormLd& n, const u16* z2f, const float2* st2, int ks, int cb, int lane) {
#pragma unroll
  for (int j = 0; j < 4; ++j) n.p[j] = *(const f32x2*)(st2 + j * S_ + ks * 32 + (lane >> 4) * 8 + (lane & 7));
#pragma unroll
  for (int j = 0; j < 6; ++j) n.z[j] = *(const u32x3*)((const char*)z2f + (ks * 48 + cb * 6 + j) * 1024 + lane * 16);
#pragma unroll
  for (int j = 0; j < 3; ++j) n.x[j] = *(const u32x2*)((const char*)z2f + Z1X + (ks * 24 + cb * 3 + j) * 512 + lane * 8);
  SB();
}
template <int E> __device__ __forceinline__ f32x2 bc2(float v) { return (f32x2){dppf<0x150 + 2 * E>(v), dppf<0x151 + 2 * E>(v)}; }
__device__ __forceinline__ void norm_store(const NormLd& n, u16* z2f, int ks, int cb, int lane) {
  const float mj[4] = {n.p[0][0], n.p[1][0], n.p[2][0], n.p[3][0]}, qj[4] = {n.p[0][1], n.p[1][1], n.p[2][1], n.p[3][1]};
  float mean, r;
  row_stat(mj, qj, mean, r);
  mean = rbf(mean);
  const f32x2 me[4] = {bc2<0>(mean), bc2<1>(mean), bc2<2>(mean), bc2<3>(mean)}, re[4] = {bc2<0>(r), bc2<1>(r), bc2<2>(r), bc2<3>(r)};
  const auto zrs = __builtin_amdgcn_make_buffer_rsrc(z2f, 0, S_ * FH_ * 2, 0x00020000);
#pragma unroll
  for (int j = 0; j < 6; ++j) {
    u32x4 o;
#pragma unroll
    for (int e = 0; e < 4; ++e) o[e] = pk2((unpk(e < 3 ? n.z[j][e < 3 ? e : 0] : n.x[j >> 1][j & 1]) - me[e]) * re[e]);
    o[3] = nohole(o[3]);
    __builtin_amdgcn_raw_buffer_store_b128(o, zrs, (ks * 48 + cb * 6 + j) * 1024 + lane * 16, 0, WTA);
  }
}
__global__ __launch_bounds__(64) void k_n(u16* z2f, const float2* __restrict__ st2) {
  const int ks = blockIdx.x >> 3;
  if ((ks & 15) < S0) return;
  NormLd n;
  norm_load(n, z2f, st2, ks, blockIdx.x & 7, threadIdx.x);
  norm_store(n, z2f, ks, blockIdx.x & 7, threadIdx.x);
}

__global__ __launch_bounds__(512) void k_m(
    const u16* __restrict__ w_sp, const u16* __restrict__ b_sp, u16* hf,
    u16* z2f, const float2* __restrict__ st2, u16* yT,
    const u16* __restrict__ ssm_k, const u16* __restrict__ ssm_D, const u16* __restrict__ woF,
    const u16* __restrict__ x, const float2* __restrict__ xst, u16* __restrict__ out) {
  extern __shared__ __attribute__((aligned(16))) char smem[];
  const int tid = threadIdx.x, lane = tid & 63, wave = __builtin_amdgcn_readfirstlane(tid >> 6);
  const int b = blockIdx.x;
  const int xcd = b & 7, idx = b >> 3;
  const int rb = 8 * (xcd >> 1) + (idx >> 2), cb = 4 * (xcd & 1) + (idx & 3);
  const int pw = (wave + ROT * cb) & 7;
  const int t0 = rb * 64;
  const int g4 = lane >> 4, l15 = lane & 15;
  STAMP(2, 0);
  RT(1, 0);
  AB_DECL;
  AB(0);
  const unsigned flg = lds_addr(smem) + M_FLAG;
  bf16x8 Wq[15];
  const char* wg = (const char*)woF + (size_t)(pw * 120 + cb * 5) * 1024;
  const int mt = wave & 3, kh = wave >> 2, d0 = cb * 80;
  const int row = t0 + mt * 16 + l15;
  const size_t xo = (size_t)row * D_ + d0;
  u32x2 xv[3], st;
  const auto hrs = __builtin_amdgcn_make_buffer_rsrc(hf, 0, S_ * FH_ * 2, 0x00020000);
  auto ldP1 = [&](int n) {
    if (n < 15)
      asm volatile("global_load_dwordx4 %0, %1, %2 offset:%3" : "=&v"(Wq[n < 15 ? n : 0]) : "v"((unsigned)(lane * 16)), "s"(wg + (n / 5) * 40960 + 2048), "i"((n % 5) * 1024 - 2048) : "memory");
    else if (n < 18) {
      const int q = n < 18 ? n - 15 : 0;
      asm volatile("global_load_dwordx2 %0, %1, %2 offset:%3" : "=&v"(xv[q]) : "v"((unsigned)(l15 * D_ * 2 + 8 * g4)),
                   "s"(x + (size_t)(t0 + mt * 16) * D_ + d0 + (q < 2 ? 32 * kh : 64)), "i"(q == 1 ? 32 : 0) : "memory");
    } else
      asm volatile("global_load_dwordx2 %0, %1, %2" : "=&v"(st) : "v"((unsigned)(l15 * 8)), "s"(xst + t0 + mt * 16) : "memory");
  };
  int tmo = 1 << 16;
  u32x4 yv[2];
  auto yoff = [&](int n) {
    const int c = n ? 64 + (lane & 31) : lane, r = c >> 1;
    return (unsigned)(((r < 32 ? 32 * kh + r : 32 + r) * S_ + 8 * (c & 1)) * 2);
  };
  const u16* const yb = yT + (size_t)d0 * S_ + t0 + 16 * mt;
  auto ldy0 = [&]() {
#pragma unroll
    for (int n = 0; n < 2; ++n) asm volatile("global_load_dwordx4 %0, %1, %2 " HSC : "=&v"(yv[n]) : "v"(yoff(n)), "s"(yb) : "memory");
  };
  auto signal1 = [&](unsigned a) {
    const unsigned one = 1u;
    asm volatile("s_mov_b64 exec, 1\n\tds_add_u32 %0, %1\n\ts_mov_b64 exec, -1" :: "v"(a), "v"(one) : "memory");
  };
  auto drain = [&](int nc) {
    SB();
    asm volatile("s_waitcnt vmcnt(%21)"  : "+v"(Wq[0]), "+v"(Wq[1]), "+v"(Wq[2]), "+v"(Wq[3]), "+v"(Wq[4]), "+v"(Wq[5]), "+v"(Wq[6]), "+v"(Wq[7]),
                 "+v"(Wq[8]), "+v"(Wq[9]), "+v"(Wq[10]), "+v"(Wq[11]), "+v"(Wq[12]), "+v"(Wq[13]), "+v"(Wq[14]),
                 "+v"(xv[0]), "+v"(xv[1]), "+v"(xv[2]), "+v"(st), "+v"(yv[0]), "+v"(yv[1]) : "i"(nc) : "memory");
  };
  auto drain1 = [&](int nc) {
    SB();
#pragma unroll
    for (int n = 0; n < NW1; ++n) asm volatile("s_waitcnt vmcnt(%1)" : "+v"(Wq[n]) : "i"(nc) : "memory");
  };
  const int cw = wave & 3;
  const unsigned pfo = (unsigned)((lane >> 4) * 4096 + ((lane >> 2) & 3) * 1024 + (4 + (lane & 3)) * 128);
  f32x4 sp[4][2];
  auto ldst = [&](int q) {
#pragma unroll
    for (int j = 0; j < 4; ++j)
      asm volatile("global_load_dwordx4 %0, %1, %2 offset:%3" : "=&v"(sp[j][q]) : "v"((unsigned)(lane * 16)), "s"(st2 + j * S_ + cw * 512), "i"(q * 1024) : "memory");
    SB();
  };
  auto table = [&](int q, int nv) {
    asm volatile("s_waitcnt vmcnt(%4)" : "+v"(sp[0][q]), "+v"(sp[1][q]), "+v"(sp[2][q]), "+v"(sp[3][q]) : "i"(nv) : "memory");
    float mv[2], rv[2];
#pragma unroll
    for (int h = 0; h < 2; ++h) {
      const float mj[4] = {sp[0][q][2 * h], sp[1][q][2 * h], sp[2][q][2 * h], sp[3][q][2 * h]};
      const float qj[4] = {sp[0][q][2 * h + 1], sp[1][q][2 * h + 1], sp[2][q][2 * h + 1], sp[3][q][2 * h + 1]};
      row_stat(mj, qj, mv[h], rv[h]);
    }
    *(f32x2*)(smem + M_RT + cw * 2048 + q * 512 + lane * 8) = (f32x2){rv[0], rv[1]};
    *(unsigned*)(smem + M_MT + cw * 1024 + q * 256 + lane * 4) = pack2(mv[0], mv[1]);
    if (!GTB || q) flag_signal(flg + 16 + cw * 4, lane);
    SB();
  };
  if (wave >= 4) {
    TS_DECL;
    auto pre = [&]() {
      ldst(0);
      if (TBL) ldst(1);
      __builtin_amdgcn_s_barrier();
      SB();
      if (!TBL) ldst(1);
    };
    TS(0);
    if (cw < (b < 128 ? 3 : 2)) {
      if (!GTB) pre();
      const int d = cw < 2 ? 2 * b + cw : 512 + b;
      char* cb_ = smem + M_CV + cw * CV_BUF;
      u32x4 hc[4], uc[4];
      unsigned hp[4], ux[4];
      float Dv;
      auto ldc = [&]() {
#pragma unroll
        for (int k = 0; k < 4; ++k) {
          const int a = k * 64 + lane;
          hc[k] = *(const u32x4*)(ssm_k + (size_t)d * S_ + a * 8);
          hp[k] = *(const unsigned*)(ssm_k + (size_t)d * S_ + max(a * 8 - 2, 0));
          uc[k] = *(const u32x4*)(yT + (size_t)d * S_ + a * 8);
          ux[k] = *(const unsigned*)((const char*)yT + KVX + (d >> 1) * 2048 + a * 8 + (d & 1) * 4);
        }
        Dv = bf2f(ssm_D[d]);
        SB();
      };
      ldst(1);
      if (GTB == 1) ldc();
      ABS(1);
      __builtin_amdgcn_s_barrier();
      SB();
      ABS(2);
      if (GTB == 2) ldc();
      ABS(3);
      table(1, 16);
      ABS(4);
      TS(1); AB(1);
      if (lane < 36) *(u32x4*)(cb_ + (lane < 32 ? lane * 16 : CV_C + ((lane >> 1) & 1) * CV_CS + 4096 + (lane & 1) * 16)) = (u32x4){0u, 0u, 0u, 0u};
#pragma unroll
      for (int k = 0; k < 4; ++k) {
        const int a = k * 64 + lane;
        const unsigned w[5] = {a ? hp[k] : 0u, hc[k][0], hc[k][1], hc[k][2], hc[k][3]};
        u32x4 sw, mx;
#pragma unroll
        for (int i = 0; i < 4; ++i) {
          sw[i] = (w[4 - i] >> 16) | (w[4 - i] << 16);
          mx[i] = (w[4 - i] & 0xFFFFu) | (w[3 - i] & 0xFFFF0000u);
        }
        char* cp = cb_ + CV_C + (2040 - 8 * a) * 2;
        *(u32x4*)(cp) = sw;
        *(u32x4*)(cp + CV_CS) = mx;
        *(u32x4*)(cb_ + 512 + a * 16) = (u32x4){uc[k][0], uc[k][1], uc[k][2], ux[k]};
      }
      TS(2); AB(2); ABS(5);
      f32x4 ac[8];
      conv_core(lds_addr(cb_), lane, ac);
      AB(1);
      const auto yrs = __builtin_amdgcn_make_buffer_rsrc(yT, 0, D_ * S_ * 2, 0x00020000);
      u32x2 kvp[8];
#pragma unroll
      for (int t = 0; t < 8; ++t) kvp[t] = *(const u32x2*)(cb_ + 512 + (256 * t + 16 * l15 + 4 * g4) * 2);
#pragma unroll
      for (int k = 0; k < 4; ++k) {
        u32x2 ov[2];
#pragma unroll
        for (int t = 0; t < 2; ++t)
#pragma unroll
          for (int h = 0; h < 2; ++h) {
            const unsigned cv = pack2(ac[2 * k + t][2 * h], ac[2 * k + t][2 * h + 1]);
            const unsigned kd = pack2(lo16(kvp[2 * k + t][h]) * Dv, hi16(kvp[2 * k + t][h]) * Dv);
            ov[t][h] = pack2(lo16(cv) + lo16(kd), hi16(cv) + hi16(kd));
          }
        const auto s0 = __builtin_amdgcn_permlane16_swap(ov[0][0], ov[1][0], false, false);
        const auto s1 = __builtin_amdgcn_permlane16_swap(ov[0][1], ov[1][1], false, false);
        const u32x4 o4 = {s0[0], s1[0], s0[1], nohole(s1[1])};
        __builtin_amdgcn_raw_buffer_store_b128(o4, yrs, (d * S_ + 256 * (2 * k + (g4 & 1)) + 16 * l15 + 8 * (g4 >> 1)) * 2, 0, WTA);
      }
    } else {
      const int ks = 2 * (rb & 24) + (cw == 3 ? 8 : 0) + (rb & 7);
      NormLd nl;
      ldst(1);
      if (GNL) norm_load(nl, z2f, st2, ks, cb, lane);
      SB();
      __builtin_amdgcn_s_barrier();
      SB();
      if (!GNL) norm_load(nl, z2f, st2, ks, cb, lane);
      unsigned pf = 0;
      auto pfw = [&]() {
#pragma unroll
        for (int n = 0; n < 4; ++n)
          asm volatile("global_load_dword %0, %1, %2 " NTQ : "+v"(pf) : "v"(pfo), "s"(w_sp + (size_t)(t0 + (idx & 3) * 16 + n * 4) * S_) : "memory");
        SB();
      };
      if (PPF == 1) pfw();
      if (!GTB && TBL != 2) table(0, 17);
      if (PPF == 2) pfw();
      if (NRP == 0) table(1, 13);
      if (PPF == 3) pfw();
      AB(1);
      if (out && (ks & 15) >= S0) norm_store(nl, z2f, ks, cb, lane);
      SB();
      if (NRP == 1) table(1, 0);
      if (PPF == 4 && cw == 3) pfw();
      __builtin_amdgcn_s_waitcnt(0x0F70);
      asm volatile("" : "+v"(pf));
      AB(2);
    }
    SB();
    TS(3); TS_OUT(2, 8, 4, 4); AB(3);
    if (!out) { RT(1, 1); return; }
    WAIT_LGKM0();
    flag_signal(flg + 40, lane);
    flag_wait(flg + 44, CVT);
    AB(4);
#pragma unroll
    for (int n = 0; n < NW1; ++n) ldP1(n);
    drain1(0);
  } else {
  if (tid < 32) *(unsigned*)(smem + M_FLAG + tid * 4) = 0u;
  const u16* Ab = w_sp + (size_t)t0 * S_ + wave * 512;
  const char* Bs = (const char*)z2f + (size_t)(wave * 16 * 48 + cb * 6) * 1024 + 2048;
  const unsigned boff = (unsigned)(lane * 16);
  char* ring = smem + M_RING + wave * 24576;
  const unsigned ringl = __builtin_amdgcn_readfirstlane(lds_addr(ring));
  const unsigned aoff = (unsigned)((lane >> 3) * 4096 + (((lane & 7) ^ (lane >> 3)) << 4));
  bf16x8 Bf[3][6];
  TS_DECL;
  auto ldA1 = [&](auto dc, int i) {
    constexpr int d = decltype(dc)::value;
    const unsigned ao = (XE & 4) && d >= 2 ? aoff & 0x70u : aoff, rl = ringl;
    const char* ab = (const char*)Ab + ((wave + 1 + (i >> 1)) & 3) * 65536;
    asm volatile("s_mov_b32 m0, %2\n\ts_nop 0\n\tglobal_load_lds_dwordx4 %0, %1 " ANT
                 :: "v"(ao), "s"(ab + ((i & 1) * 32768 + (d & XAM) * XAS)), "s"(rl + (d % 3) * 8192 + i * 1024) : "memory");
  };
  auto ldA = [&](auto dc) {
#pragma unroll
    for (int i = 0; i < 8; ++i) ldA1(dc, i);
  };
  auto ldB1 = [&](auto sc, int j) {
    constexpr int s = decltype(sc)::value;
    bf16x8& B = Bf[s % 3][j];
    const unsigned bo = (XE & 2) && s >= 3 ? boff & 0x70u : boff;
    const char* bs = Bs + (size_t)(s & XBM) * 49152;
    asm volatile("global_load_dwordx4 %0, %1, %2 offset:%3 " BNT : "=&v"(B) : "v"(bo), "s"(bs), "i"(j * 1024 - 2048) : "memory");
  };
  auto ldB = [&](auto sc) {
#pragma unroll
    for (int j = 0; j < 6; ++j) ldB1(sc, j);
  };
  if (GTB) ldst(0);
  ldA(IC(0)); ldB(IC(0));
  if (BARP == 0) RAW_BARRIER();
  if (BARP == 3) table(0, 14);
  ldB(IC(1));
  if (BARP == 4) table(0, 20);
  ldA(IC(1));
  unsigned pfg = 0;
  if (PPF == 6)
    asm volatile("global_load_dword %0, %1, %2" : "=&v"(pfg) : "v"(pfo), "s"(w_sp + (size_t)(t0 + (idx & 3) * 16 + wave * 4) * S_) : "memory");
  if (BARP == 1) RAW_BARRIER();
  TS(6);
  if (GTB) { if (BARP < 3) table(0, 28 + (PPF == 6)); }
  else flag_wait_asm(flg + 16 + wave * 4, 1u);
  if (BARP >= 2) RAW_BARRIER();
  f32x4 acc[4][6], cacc[4];
  const unsigned rta = lds_addr(smem) + M_RT + (wave * 512 + g4 * 8) * 4;
  const unsigned mta = lds_addr(smem) + M_MT + (wave * 512 + g4 * 8) * 2;
  unsigned ara[2];
#pragma unroll
  for (int ks = 0; ks < 2; ++ks) ara[ks] = lds_addr(ring) + l15 * 128 + (((ks * 4 + g4) ^ (l15 & 7)) << 4);
  u32x4 af[4];
  f32x4 rT[2][2]; u32x4 mT[2];
  auto rdA = [&](auto sc, int i) {
    constexpr int s = decltype(sc)::value;
    u32x4& o = af[i];
    const unsigned ad = ara[s & 1];
    asm volatile("ds_read_b128 %0, %1 offset:%2" : "=&v"(o) : "v"(ad), "i"(((s >> 1) % 3) * 8192 + i * 2048) : "memory");
  };
  auto rdT = [&](auto sc) {
    constexpr int s = decltype(sc)::value;
    f32x4 &o0 = rT[s & 1][0], &o1 = rT[s & 1][1]; u32x4& om = mT[s & 1];
    const unsigned ra = rta, ma = mta;
    asm volatile("ds_read_b128 %0, %1 offset:%2" : "=&v"(o0) : "v"(ra), "i"(s * 128) : "memory");
    asm volatile("ds_read_b128 %0, %1 offset:%2" : "=&v"(o1) : "v"(ra), "i"(s * 128 + 16) : "memory");
    asm volatile("ds_read_b128 %0, %1 offset:%2" : "=&v"(om) : "v"(ma), "i"(s * 64) : "memory");
  };
  constexpr int NSC = 20, NPM = 3;
  float tl, th;
  auto scop = [&](int i, int p, int n) {
    const int e = n / 5, k = n % 5;
    if (k == 0) tl = lo16(af[i][e]);
    else if (k == 1) th = hi16(af[i][e]);
    else if (k == 2) tl = __builtin_fmaf(tl, rT[p][e >> 1][(e & 1) * 2], 0.0f);
    else if (k == 3) th *= rT[p][e >> 1][(e & 1) * 2 + 1];
    else af[i][e] = pack2(tl, th);
  };
  TS(7);
  asm volatile("s_waitcnt vmcnt(%0) lgkmcnt(0)" :: "n"(20 + (PPF == 6)) : "memory");
  rdT(IC(0));
#pragma unroll
  for (int i = 0; i < 4; ++i) rdA(IC(0), i);
  asm volatile("s_waitcnt lgkmcnt(3)" : "+v"(af[0]), "+v"(rT[0][0]), "+v"(rT[0][1]), "+v"(mT[0]) :: "memory");
#pragma unroll
  for (int n = 0; n < NSC; ++n) scop(0, 0, n);
  SB();
  const unsigned eo = (unsigned)((l15 + 16 * (2 * (g4 & 1) + (g4 >> 1))) * 16);
  const size_t eb = (size_t)((rb * 4 + wave) * 24 + cb * 3) * 1024;
  u32x3 zv[3];
  unsigned zx[3], bsv;
  auto ldZ = [&]() {
#pragma unroll
    for (int q = 0; q < 3; ++q)
      asm volatile("global_load_dwordx3 %0, %1, %2 offset:%3" : "=&v"(zv[q]) : "v"(eo), "s"((const char*)hf + eb), "i"(q * 1024) : "memory");
#pragma unroll
    for (int q = 0; q < 3; ++q)
      asm volatile("global_load_dword %0, %1, %2 offset:%3" : "=&v"(zx[q]) : "v"(eo >> 1),
                   "s"((const char*)hf + Z1X + ((rb * 2 + (wave >> 1)) * 24 + cb * 3) * 512 + (wave & 1) * 4), "i"(q * 512) : "memory");
    asm volatile("global_load_ushort %0, %1, %2" : "=&v"(bsv) : "v"((unsigned)(l15 * 2)), "s"(b_sp + t0 + wave * 16) : "memory");
  };
  const unsigned xa = ringl + lane * 16, xc = ringl + lane * 4;
  auto step = [&](auto sc) {
    constexpr int s = decltype(sc)::value, sl = s % 3, p = s & 1;
    constexpr int NV = s == 0 ? 14 + (PPF == 6) : (s == 2 ? 28 : (s < 12 ? 20 : (s == 12 ? 12 : (LP3 ? 19 - 6 * (s - 13) : 19))));
    constexpr int DA = s == 0 ? 2 : (((s & 1) && (s + 5) / 2 < 8) ? (s + 5) / 2 : -1);
    asm volatile("s_waitcnt vmcnt(%7) lgkmcnt(2)\n\ts_nop 1"
                 : "+v"(Bf[sl][0]), "+v"(Bf[sl][1]), "+v"(Bf[sl][2]), "+v"(Bf[sl][3]), "+v"(Bf[sl][4]), "+v"(Bf[sl][5]), "+v"(af[1]) : "n"(NV) : "memory");
    if constexpr (s == 1) asm volatile("" : "+v"(pfg));
    if constexpr (s == 3) flag_wait_asm(flg + 16 + wave * 4, GTB ? 1u : 2u);
    if constexpr (s >= S0) {
      auto d3 = [&](int j) { return __builtin_bit_cast(u32x4, Bf[sl][j])[3]; };
      auto bad = [&]() { return __builtin_amdgcn_ballot_w64(umx(umx(umx(d3(0), d3(1)), d3(2)), umx(umx(d3(3), d3(4)), d3(5))) == HOLE) != 0; };
      if (__builtin_expect(bad(), 0)) {
        do {
          --tmo;
          asm volatile("s_sleep 2\n\tglobal_load_dwordx4 %0, %6, %7 offset:-2048 sc1\n\tglobal_load_dwordx4 %1, %6, %7 offset:-1024 sc1\n\t"
                       "global_load_dwordx4 %2, %6, %7 sc1\n\tglobal_load_dwordx4 %3, %6, %7 offset:1024 sc1\n\t"
                       "global_load_dwordx4 %4, %6, %7 offset:2048 sc1\n\tglobal_load_dwordx4 %5, %6, %7 offset:3072 sc1\n\ts_waitcnt vmcnt(0)"
                       : "+v"(Bf[sl][0]), "+v"(Bf[sl][1]), "+v"(Bf[sl][2]), "+v"(Bf[sl][3]), "+v"(Bf[sl][4]), "+v"(Bf[sl][5])
                       : "v"(boff), "s"(Bs + (size_t)s * 49152) : "memory");
        } while (bad() && tmo > 0);
      }
    }
    if constexpr (s == SGS) signal1(flg + 44);
    if constexpr (s < 15) ABS(s + 1);
    SB();
#pragma unroll
    for (int i = 0; i < 4; ++i) {
      if (i == 1) asm volatile("s_waitcnt lgkmcnt(%2)" : "+v"(af[2]), "+v"(af[3]) : "n"(s < 15 ? (s + 1 < S0 ? 4 : 1) : 0) : "memory");
      if (i == 3 && s + 1 < S0) asm volatile("s_waitcnt lgkmcnt(2)" : "+v"(af[0]), "+v"(rT[p ^ 1][0]), "+v"(rT[p ^ 1][1]), "+v"(mT[p ^ 1]) :: "memory");
      if (i == 3 && s + 1 >= S0 && s < 15) asm volatile("s_waitcnt lgkmcnt(2)" : "+v"(af[0]) :: "memory");
      const bf16x8 av = __builtin_bit_cast(bf16x8, af[i]);
#pragma unroll
      for (int j = 0; j < 7; ++j) {
        f32x4& ac = j < 6 ? acc[i][j < 6 ? j : 0] : cacc[i];
        const bf16x8 bb = j < 6 ? Bf[sl][j < 6 ? j : 0] : __builtin_bit_cast(bf16x8, mT[p]);
        if constexpr (s == 0) asm volatile("v_mfma_f32_16x16x32_bf16 %0, %1, %2, 0" : "=&v"(ac) : "v"(bb), "v"(av));
        else if (j < 6 || s < XNS) asm volatile("v_mfma_f32_16x16x32_bf16 %0, %1, %2, %0" : "+v"(ac) : "v"(bb), "v"(av));
        SB();
#pragma unroll
        for (int n = NPM * j; n < NPM * j + NPM && n < NSC; ++n) {
          if (i < 3) { if (s < XNS) scop(i + 1, p, n); }
          else if (s < 15 && s + 1 < XNS) scop(0, p ^ 1, n);
          SB();
        }
        if constexpr (s == 0) { if (i == 0 && j < 6) ldB1(IC(2), j); }
        if constexpr (DA >= 0) { if ((i == 1 || i == 2) && j < 4) ldA1(IC(DA < 0 ? 0 : DA), (i - 1) * 4 + j); }
        if constexpr (s + 3 < 16) { if (i == 3 && j < 6) ldB1(IC(s + 3), j); }
        if constexpr (!LP3 && (s == 13 || s == 14)) { if (i == 3 && j < 6) ldP1((s - 13) * 6 + j); }
        if constexpr (s == 15) {
          if (i > 0 && j > 0) asm volatile("ds_write_b128 %0, %1 offset:%2" :: "v"(xa), "v"(acc[i > 0 ? i - 1 : 0][j > 0 ? j - 1 : 0]), "i"((i - 1) * 6144 + (j - 1) * 1024) : "memory");
          if (!LP3) {
            if (i == 2 && j > 0) ldP1(11 + j);
            if (i == 3 && j == 1) ldP1(18);
            if (i == 3 && j == 2) ldy0();
          }
          if (LP3 == 2) { if (i >= 2 && j < 6) ldP1((i - 2) * 6 + j); }
          if (LP3 == 3) { if (i >= 2 && j < 6 && (i - 2) * 6 + j < NW0) ldP1((i - 2) * 6 + j); }
        }
        SB();
      }
      if constexpr (s == 15) {
        if (i > 0) {
          asm volatile("ds_write_b32 %0, %1 offset:%2" :: "v"(xc), "v"(cacc[i > 0 ? i - 1 : 0][0]), "i"(18432 + (i - 1) * 256),
                       "v"(cacc[i > 0 ? i - 1 : 0]) : "memory");
          signal1(flg + ((wave + i) & 3) * 4);
        }
      }
      asm volatile("s_nop 1");
      if constexpr (s < 15) { if (i == 0 && s + 1 < S0) rdT(IC(s + 1)); rdA(IC(s + 1), i); }
      SB();
    }
    if constexpr (s == 12) ldZ();
    SB();
  };
  TS(0); AB(1);
  step(IC(0)); TS(1); step(IC(1)); step(IC(2)); step(IC(3)); TS(2); AB(3); step(IC(4)); step(IC(5)); step(IC(6)); step(IC(7)); TS(3);
  step(IC(8)); step(IC(9)); step(IC(10)); step(IC(11)); TS(4); step(IC(12)); step(IC(13)); step(IC(14)); step(IC(15)); TS(5); AB(2);
  STAMP(2, 1);
  if (LP3 && LP3 != 3) {
#pragma unroll
    for (int n = LP3 == 2 ? 12 : 0; n < 19; ++n) ldP1(n);
    ldy0();
    SB();
  }
  if (LP3 == 3) {
#pragma unroll
    for (int n = NW0; n < NW1; ++n) ldP1(n);
    SB();
  }
  flag_wait_asm(flg + wave * 4, 3u);
  AB(4);
  f32x4 own[6];
  {
    f32x4 pr[3][6];
    float pc[3];
#pragma unroll
    for (int q = 0; q < 3; ++q) {
      const char* src = smem + M_RING + ((wave + 3 - q) & 3) * 24576;
      pc[q] = *(const float*)(src + 18432 + q * 256 + lane * 4);
#pragma unroll
      for (int j = 0; j < 6; ++j) pr[q][j] = *(const f32x4*)(src + q * 6144 + j * 1024 + lane * 16);
    }
    asm volatile("s_nop 7" : "+v"(acc[3][0]), "+v"(acc[3][1]), "+v"(acc[3][2]), "+v"(acc[3][3]), "+v"(acc[3][4]), "+v"(acc[3][5]), "+v"(cacc[3]));
    const float c = (cacc[3][0] + pc[0]) + (pc[1] + pc[2]);
#pragma unroll
    for (int j = 0; j < 6; ++j) own[j] = ((acc[3][j] - c) + pr[0][j]) + (pr[1][j] + pr[2][j]);
  }
  asm volatile("s_waitcnt vmcnt(%7)" : "+v"(zv[0]), "+v"(zv[1]), "+v"(zv[2]), "+v"(zx[0]), "+v"(zx[1]), "+v"(zx[2]), "+v"(bsv) : "i"(LP3 == 3 ? NW1 : 21) : "memory");
  SB();
  {
    const float bs = lo16(bsv);
    unsigned pk[6][2];
#pragma unroll
    for (int j = 0; j < 6; ++j)
#pragma unroll
      for (int h = 0; h < 2; ++h) {
        const unsigned r0 = pack2(own[j][2 * h], own[j][2 * h + 1]);
        pk[j][h] = pack2(lo16(r0) + bs, hi16(r0) + bs);
      }
#pragma unroll
    for (int q = 0; q < 3; ++q) {
      const auto s0 = __builtin_amdgcn_permlane16_swap(pk[2 * q][0], pk[2 * q + 1][0], false, false);
      const auto s1 = __builtin_amdgcn_permlane16_swap(pk[2 * q][1], pk[2 * q + 1][1], false, false);
      u32x4 gv; gv[0] = s0[0]; gv[1] = s1[0]; gv[2] = s0[1]; gv[3] = s1[1];
      u32x4 o4;
#pragma unroll
      for (int e = 0; e < 4; ++e) {
        const unsigned z = e < 3 ? zv[q][e < 3 ? e : 0] : zx[q];
        o4[e] = pack2(lo16(z) * lo16(gv[e]), hi16(z) * hi16(gv[e]));
      }
      o4[3] = nohole(o4[3]);
      __builtin_amdgcn_raw_buffer_store_b128(o4, hrs, (int)eb + eo + q * 1024, 0, WTA);
    }
  }
  TS_OUT(1, 0, 8, 0); TS_OUT(1, 8, 8, 3); AB(5);
  drain1(3);
  if (!out) { RT(1, 1); return; }
  STAMP(2, 2);
  }
  TS_DECL;
  TS(0);
  unsigned hoff[4];
#pragma unroll
  for (int it = 0; it < 4; ++it) hoff[it] = (unsigned)(((rb * 4 + ((mt + 1 + it) & 3)) * 24 + 3 * pw) * 1024 + lane * 16);
  u32x4 Hq[4][3];
  auto ldy = [&]() {
#pragma unroll
    for (int n = 0; n < 2; ++n) asm volatile("global_load_dwordx4 %0, %1, %2 sc1" : "=&v"(yv[n]) : "v"(yoff(n)), "s"(yb) : "memory");
  };
  auto ldh = [&](int it) {
#pragma unroll
    for (int k = 0; k < 3; ++k)
      asm volatile("global_load_dwordx4 %0, %1, %2 offset:%3 sc1" : "=&v"(Hq[it][k]) : "v"(hoff[it]), "s"(XSRC), "i"(k * 1024) : "memory");
  };
  auto ldh1 = [&](int it, int k) {
    asm volatile("global_load_dwordx4 %0, %1, %2 offset:%3 " HSC : "=&v"(Hq[it][k]) : "v"(hoff[it]), "s"(XSRC), "i"(k * 1024) : "memory");
  };
  auto ldh0 = [&](int it) {
#pragma unroll
    for (int k = 0; k < 3; ++k) ldh1(it, k);
  };
  f32x2 xf[3][2], sv[3][2];
  auto ymath = [&]() {
    char* yi = smem + P3_Y + wave * 1536;
#pragma unroll
    for (int n = 0; n < 2; ++n) *(u32x4*)(yi + (n ? 64 + (lane & 31) : lane) * 16) = yv[n];
    const unsigned ya = lds_addr(yi) + (4 * g4 + (l15 >> 2)) * 32 + 8 * (lane & 3);
    s16x4 yr[3] = {tr_asm<0>(ya), tr_asm<512>(ya), tr_asm<1024>(ya)};
    asm volatile("s_waitcnt lgkmcnt(0)" : "+v"(yr[0]), "+v"(yr[1]), "+v"(yr[2]) :: "memory");
    const f32x2 mean = {__uint_as_float(st[0]), __uint_as_float(st[0])}, rd = {__uint_as_float(st[1]), __uint_as_float(st[1])};
#pragma unroll
    for (int q = 0; q < 3; ++q) {
      const u32x2 yw = __builtin_bit_cast(u32x2, yr[q]);
#pragma unroll
      for (int h = 0; h < 2; ++h) {
        xf[q][h] = unpk(xv[q][h]);
        sv[q][h] = ((xf[q][h] - mean) * rd) * unpk(yw[h]);
        asm volatile("" : "+v"(sv[q][h]), "+v"(xf[q][h]));
      }
    }
    SB();
  };
#pragma unroll
  for (int n = NW1; n < 19; ++n) ldP1(n);
  if (YMP == 5) ldy0();
  SB();
  if (wave >= 4) AB(5);
  asm volatile("s_waitcnt vmcnt(%0)" :: "i"(19 - NW1 + (YMP == 5 ? 2 : 0)) : "memory");
  bool ybad;
  AB(6);
  signal1(flg + 48 + 4 * kh);
  flag_wait_asm(flg + 48, 4u);
  TS(1); AB(7);
  if (SPR) __builtin_amdgcn_s_setprio(3);
  ldh0(0);
  if (SPR) __builtin_amdgcn_s_setprio(2);
  ldh0(1);
  if (SPR) __builtin_amdgcn_s_setprio(1);
  if (NUP > 2) ldh0(2);
  if (SPR) __builtin_amdgcn_s_setprio(0);
  if (NUP > 3) ldh0(3);
  if (YMP < 3) ldy0();
  SB();
  AB(8);
  TS(2); AB(9);
  flag_wait_asm(flg + 40, kh ? 4u : 0u);
  TS(3);
  char* const px = smem + (kh ? P3_C + mt * 18432 : P3_G + mt * 17408) + lane * 16;
  f32x4 ac3[5], o[3];
  auto comp = [&](auto itc) {
    constexpr int it = decltype(itc)::value;
    constexpr int NW = 3 * ((it + NUP < 4 ? it + NUP : 4) - it - 1) + (LP3 == 3 && YMP != 5 && (YMP < 3 ? it < 2 : it >= 2) ? 2 : 0);
    asm volatile("s_waitcnt vmcnt(%3)" : "+v"(Hq[it][0]), "+v"(Hq[it][1]), "+v"(Hq[it][2]) : "n"(NW) : "memory");
    if constexpr (LP3 == 3 && it == 0)
      asm volatile("" : "+v"(Wq[0]), "+v"(Wq[1]), "+v"(Wq[2]), "+v"(Wq[3]), "+v"(Wq[4]), "+v"(Wq[5]), "+v"(Wq[6]), "+v"(Wq[7]),
                   "+v"(Wq[8]), "+v"(Wq[9]), "+v"(Wq[10]), "+v"(Wq[11]), "+v"(Wq[12]), "+v"(Wq[13]), "+v"(Wq[14]),
                   "+v"(xv[0]), "+v"(xv[1]), "+v"(xv[2]), "+v"(st) :: "memory");
    auto bad = [&]() { return __builtin_amdgcn_ballot_w64(umx(umx(Hq[it][0][3], Hq[it][1][3]), Hq[it][2][3]) == HOLE) != 0; };
    if (__builtin_expect(bad(), 0)) {
      do {
        --tmo;
        asm volatile("s_sleep 2\n\tglobal_load_dwordx4 %0, %3, %4 sc1\n\tglobal_load_dwordx4 %1, %3, %4 offset:1024 sc1\n\t"
                     "global_load_dwordx4 %2, %3, %4 offset:2048 sc1\n\ts_waitcnt vmcnt(0)"
                     : "+v"(Hq[it][0]), "+v"(Hq[it][1]), "+v"(Hq[it][2]) : "v"(hoff[it]), "s"(hf) : "memory");
      } while (bad() && tmo > 0);
    }
    SB();
#pragma unroll
    for (int k = 0; k < 3; ++k) {
#pragma unroll
      for (int j = 0; j < 5; ++j) {
        ac3[j] = __builtin_amdgcn_mfma_f32_16x16x32_bf16(Wq[k * 5 + j], __builtin_bit_cast(bf16x8, Hq[it][k]), k ? ac3[j] : (f32x4){0.f, 0.f, 0.f, 0.f}, 0, 0, 0);
        if constexpr (it + NUP < 4) { if (k == 0 && j < 3) { SB(); ldh1(it + NUP < 4 ? it + NUP : 0, j); SB(); } }
        if constexpr (YMP >= 3 && YMP != 5 && it == 1) { if (k == 1 && j == 0) { SB(); ldy0(); SB(); } }
      }
    }
    SB();
    if constexpr (it < 3) {
#pragma unroll
      for (int j = 0; j < 5; ++j) *(f32x4*)(px + (it * 5 + j) * 1024) = ac3[j];
      signal1(flg + 64 + kh * 16 + ((mt + 1 + it) & 3) * 4);
    }
    SB();
  };
  auto gather = [&](auto cc) {
    constexpr int cv = decltype(cc)::value;
    flag_wait_asm(flg + 64 + cv * 16 + mt * 4, cv ? 5u : 3u);
#pragma unroll
    for (int it = 0; it < 3 + cv; ++it) {
      const int ms = (mt - 1 - it) & 3;
      const char* ps = smem + ((it < 3 ? cv : !kh) ? P3_C + ms * 18432 : P3_G + ms * 17408) + lane * 16;
      const char* p0 = ps + (it < 3 ? it * 5 + 2 * kh : 15) * 1024;
      const f32x4 a0 = *(const f32x4*)p0, a1 = *(const f32x4*)(p0 + 1024);
      if (!cv && it == 0) { o[0] = a0; o[1] = a1; } else { o[0] += a0; o[1] += a1; }
    }
    if (!kh) {
#pragma unroll
      for (int it = 0; it < 3 + cv; ++it) {
        const int ms = (mt - 1 - it) & 3;
        const char* ps = smem + ((it < 3 ? cv : 1) ? P3_C + ms * 18432 : P3_G + ms * 17408) + lane * 16;
        o[2] += *(const f32x4*)(ps + (it < 3 ? it * 5 + 4 : 17) * 1024);
      }
    }
    SB();
  };
  o[2] = (f32x4){0.f, 0.f, 0.f, 0.f};
  comp(IC(0)); AB(10); comp(IC(1));
  if (YMP == 5) {
    asm volatile("" : "+v"(yv[0]), "+v"(yv[1]) :: "memory");
    if (__builtin_amdgcn_ballot_w64(umx(yv[0][3], yv[1][3]) == HOLE) != 0) {
      asm volatile("global_load_dwordx4 %0, %2, %4 sc1\n\tglobal_load_dwordx4 %1, %3, %4 sc1" : "+v"(yv[0]), "+v"(yv[1]) : "v"(yoff(0)), "v"(yoff(1)), "s"(yb) : "memory");
    }
    SB();
  }
  if (YMP == 1) {
    asm volatile("s_waitcnt vmcnt(6)" : "+v"(yv[0]), "+v"(yv[1]) :: "memory");
    ybad = __builtin_amdgcn_ballot_w64(umx(yv[0][3], yv[1][3]) == HOLE) != 0;
    ymath();
  }
  comp(IC(2));
  if (YMP == 2) {
    asm volatile("" : "+v"(yv[0]), "+v"(yv[1]) :: "memory");
    ybad = __builtin_amdgcn_ballot_w64(umx(yv[0][3], yv[1][3]) == HOLE) != 0;
    ymath();
  }
  TS(4); AB(11);
  gather(IC(0));
  if (YMP == 4) {
    asm volatile("s_waitcnt vmcnt(0)" : "+v"(yv[0]), "+v"(yv[1]) :: "memory");
    ybad = __builtin_amdgcn_ballot_w64(umx(yv[0][3], yv[1][3]) == HOLE) != 0;
    ymath();
  }
  AB(12);
  comp(IC(3));
  TS(5); AB(13);
  {
    *(f32x4*)(px + 15 * 1024) = kh ? ac3[0] : ac3[2];
    *(f32x4*)(px + 16 * 1024) = kh ? ac3[1] : ac3[3];
    if (kh) *(f32x4*)(px + 17 * 1024) = ac3[4];
    signal1(flg + 80 + mt * 4);
    SB();
    o[0] += kh ? ac3[2] : ac3[0]; o[1] += kh ? ac3[3] : ac3[1]; o[2] += ac3[4];
    if (YMP == 3 || YMP == 5) {
      asm volatile("s_waitcnt vmcnt(0)" : "+v"(yv[0]), "+v"(yv[1]) :: "memory");
      ybad = __builtin_amdgcn_ballot_w64(umx(yv[0][3], yv[1][3]) == HOLE) != 0;
    }
    if (__builtin_expect(ybad, 0)) {
      do {
        --tmo;
        ldy();
        asm volatile("s_waitcnt vmcnt(0)" : "+v"(yv[0]), "+v"(yv[1]) :: "memory");
      } while (__builtin_amdgcn_ballot_w64(umx(yv[0][3], yv[1][3]) == HOLE) != 0 && tmo > 0);
      if (YMP != 3 && YMP != 5) ymath();
    }
    if (YMP == 3 || YMP == 5) ymath();
    TS(6); AB(14);
    gather(IC(1));
    auto fin = [&](const f32x4& o4, int q, unsigned (&pk)[2]) {
#pragma unroll
      for (int h = 0; h < 2; ++h) {
        const f32x2 o = {o4[2 * h], o4[2 * h + 1]};
        const f32x2 a = o * (f32x2){-1.4426950f, -1.4426950f};
        const f32x2 e = (f32x2){__builtin_amdgcn_exp2f(a[0]), __builtin_amdgcn_exp2f(a[1])} + (f32x2){1.0f, 1.0f};
        const f32x2 r = {__builtin_amdgcn_rcpf(e[0]), __builtin_amdgcn_rcpf(e[1])};
        pk[h] = pk2(__builtin_elementwise_fma(o * r, sv[q][h], xf[q][h]));
      }
    };
    unsigned pa[2], pb[2];
    fin(o[0], 0, pa);
    fin(o[1], 1, pb);
    const auto s0 = __builtin_amdgcn_permlane16_swap(pa[0], pb[0], false, false);
    const auto s1 = __builtin_amdgcn_permlane16_swap(pa[1], pb[1], false, false);
    *(u32x4*)(out + xo + 32 * kh + 16 * (g4 & 1) + 8 * (g4 >> 1)) = (u32x4){s0[0], s1[0], s0[1], s1[1]};
    if (!kh) {
      fin(o[2], 2, pa);
      *(u32x2*)(out + xo + 64 + 4 * g4) = (u32x2){pa[0], pa[1]};
    }
  }
  TS(7); AB(15);
  AB_OUT(16);
  RT(1, 1);
}

extern "C" __global__ void k_end() {}

#define X4 0
#define LA4 4
constexpr int G3_RED = 4 * 3072;
constexpr int G3_LDS = G3_RED + 4 * 15 * 1024;

__global__ __launch_bounds__(256) void k_l4(
    const u16* __restrict__ hf, const u16* __restrict__ woF, const u16* __restrict__ x,
    const float2* __restrict__ xst, const u16* __restrict__ yT, u16* __restrict__ out) {
  extern __shared__ __attribute__((aligned(16))) char smem[];
  const int tid = threadIdx.x, lane = tid & 63, wave = __builtin_amdgcn_readfirstlane(tid >> 6);
  const int b = blockIdx.x;
  const int xcd = b & 7, idx = b >> 3;
  const int rb = 8 * (xcd >> 1) + (idx >> 2), cb = 4 * (xcd & 1) + (idx & 3);
  const int t0 = rb * 64, d0 = cb * 80;
  const int g4 = lane >> 4, l15 = lane & 15;
  RT(2, 0);
  TS_DECL;
  TS(0);
  const unsigned boff16 = (unsigned)(lane * 16);
  const char* Hb = (const char*)hf + (size_t)(rb * 4 * 24 + wave * 6) * 1024;
  const char* Wb = (const char*)woF + (size_t)(wave * 6 * 40 + cb * 5) * 1024;
  u32x4 Hf[6][4], Wf[6][5];
  auto ldH = [&](int s, int k) { Hf[s][k] = *(const u32x4*)(Hb + (size_t)((wave + 1 + k) & 3) * 24576 + s * 1024 + boff16); };
  auto ldG = [&](int s) {
#pragma unroll
    for (int j = 0; j < 5; ++j) Wf[s][j] = *(const u32x4*)(Wb + (size_t)s * 40960 + j * 1024 + boff16);
    ldH(s, 0); ldH(s, 1);
    SB();
  };
  const unsigned ly = __builtin_amdgcn_readfirstlane(lds_addr(smem) + wave * 3072);
  {
    const char* yb = (const char*)(yT + (size_t)d0 * S_ + t0 + wave * 16);
#pragma unroll
    for (int n = 0; n < 3; ++n) {
      const int c = min(n * 64 + lane, 159);
      asm volatile("s_mov_b32 m0, %2\n\ts_nop 0\n\tglobal_load_lds_dwordx4 %0, %1"
                   :: "v"((unsigned)((c >> 1) * (S_ * 2) + (c & 1) * 16)), "s"(yb), "s"(ly + n * 1024) : "memory");
    }
  }
  SB();
  ldG(0); ldG(1);
  const int row = t0 + wave * 16 + l15;
  const size_t xo = (size_t)row * D_ + d0;
  u32x2 xv[5];
  const float2 st = xst[row];
#pragma unroll
  for (int j = 0; j < 5; ++j) xv[j] = *(const u32x2*)(x + xo + 16 * j + 4 * g4);
  SB();
  ldG(2); ldG(3);
  TS(1);
  f32x4 acc[4][5];
  auto mm = [&](int s, int k) {
#pragma unroll
    for (int j = 0; j < 5; ++j) {
      const f32x4 c = s ? acc[k][j] : (f32x4){0.f, 0.f, 0.f, 0.f};
      acc[k][j] = __builtin_amdgcn_mfma_f32_16x16x32_bf16(__builtin_bit_cast(bf16x8, Wf[s][j]), __builtin_bit_cast(bf16x8, Hf[s][k]), c, 0, 0, 0);
    }
    SB();
  };
  auto put = [&](int k, int j) {
    *(f32x4*)(smem + G3_RED + (((wave + 1 + k) & 3) * 3 + k) * 5120 + j * 1024 + lane * 16) = acc[k][j];
    SB();
  };
  f32x4 pr[3][5];
  auto get = [&](int q) {
#pragma unroll
    for (int j = 0; j < 5; ++j) pr[q][j] = *(const f32x4*)(smem + G3_RED + (wave * 3 + q) * 5120 + j * 1024 + lane * 16);
    SB();
  };
  auto ldN = [&](int n) {
    if (n < 6) ldG(n);
    else if (n < 8) {
#pragma unroll
      for (int s = 0; s < 6; ++s) ldH(s, n - 4);
      SB();
    }
  };
  mm(0, 0); mm(0, 1); ldN(LA4);
  s16x4 yr[5];
  {
    const unsigned ya = ly + (4 * g4 + (l15 >> 2)) * 32 + 8 * (lane & 3);
#pragma unroll
    for (int j = 0; j < 5; ++j)
      asm volatile("ds_read_b64_tr_b16 %0, %1 offset:%2" : "=&v"(yr[j]) : "v"(ya), "i"(j * 512) : "memory");
  }
  mm(1, 0); mm(1, 1); ldN(LA4 + 1);
  asm volatile("s_waitcnt lgkmcnt(0)" : "+v"(yr[0]), "+v"(yr[1]), "+v"(yr[2]), "+v"(yr[3]), "+v"(yr[4]) :: "memory");
  f32x2 xf[5][2], sv[5][2];
  {
    const f32x2 mean = {st.x, st.x}, rd = {st.y, st.y};
#pragma unroll
    for (int j = 0; j < 5; ++j) {
      const u32x2 yw = __builtin_bit_cast(u32x2, yr[j]);
#pragma unroll
      for (int h = 0; h < 2; ++h) {
        xf[j][h] = unpk(xv[j][h]);
        sv[j][h] = ((xf[j][h] - mean) * rd) * unpk(yw[h]);
      }
    }
    SB();
  }
  mm(2, 0); mm(2, 1); ldN(LA4 + 2);
  mm(3, 0); mm(3, 1); ldN(LA4 + 3);
  mm(4, 0); mm(4, 1);
  mm(5, 0); mm(5, 1);
  TS(2);
  mm(0, 2); put(0, 0); put(0, 1); put(0, 2);
  mm(1, 2); put(0, 3); put(0, 4); put(1, 0);
  mm(2, 2); put(1, 1); put(1, 2); put(1, 3);
  mm(3, 2); put(1, 4);
  mm(4, 2);
  mm(5, 2);
  TS(3);
  RAW_BARRIER();
  SB();
  get(0);
  mm(0, 3);
  put(2, 0); put(2, 1); put(2, 2); put(2, 3); put(2, 4);
  get(1);
  mm(1, 3);
  mm(2, 3);
  TS(4);
  RAW_BARRIER();
  SB();
  TS(5);
  get(2);
#pragma unroll
  for (int j = 0; j < 5; ++j) pr[0][j] += pr[1][j];
  SB();
  mm(3, 3);
  mm(4, 3);
#pragma unroll
  for (int j = 0; j < 5; ++j) pr[0][j] += pr[2][j];
  SB();
  mm(5, 3);
  TS(6);
  unsigned pk[5][2];
#pragma unroll
  for (int j = 0; j < 5; ++j) {
    const f32x4 o4 = acc[3][j] + pr[0][j];
#pragma unroll
    for (int h = 0; h < 2; ++h) {
      const f32x2 o = {o4[2 * h], o4[2 * h + 1]};
      const f32x2 a = o * (f32x2){-1.4426950f, -1.4426950f};
      const f32x2 e = (f32x2){__builtin_amdgcn_exp2f(a[0]), __builtin_amdgcn_exp2f(a[1])} + (f32x2){1.0f, 1.0f};
      const f32x2 r = {__builtin_amdgcn_rcpf(e[0]), __builtin_amdgcn_rcpf(e[1])};
      pk[j][h] = pk2(__builtin_elementwise_fma(o * r, sv[j][h], xf[j][h]));
    }
  }
  const int dq = 16 * (g4 & 1) + 8 * (g4 >> 1);
#pragma unroll
  for (int q = 0; q < 2; ++q) {
    const auto s0 = __builtin_amdgcn_permlane16_swap(pk[2 * q][0], pk[2 * q + 1][0], false, false);
    const auto s1 = __builtin_amdgcn_permlane16_swap(pk[2 * q][1], pk[2 * q + 1][1], false, false);
    u32x4 o4; o4[0] = s0[0]; o4[1] = s1[0]; o4[2] = s0[1]; o4[3] = s1[1];
    *(u32x4*)(out + xo + 32 * q + dq) = o4;
  }
  *(u32x2*)(out + xo + 64 + 4 * g4) = (u32x2){pk[4][0], pk[4][1]};
  TS(7);
  RT(2, 1);
}

extern "C" void kernel_launch(void* const* d_in, const int* in_sizes, int n_in,
                              void* d_out, int out_size, void* d_ws, size_t ws_size,
                              hipStream_t stream) {
  const u16* A_q = (const u16*)d_in[0];
  const u16* A_k = (const u16*)d_in[1];
  const u16* A_v = (const u16*)d_in[2];
  const u16* A_shift_k = (const u16*)d_in[3];
  const u16* A_ssm_k = (const u16*)d_in[4];
  const u16* A_ssm_D = (const u16*)d_in[5];
  const u16* B_w_in = (const u16*)d_in[7];
  const u16* B_w_sp = (const u16*)d_in[8];
  const u16* B_b_sp = (const u16*)d_in[9];
  const u16* B_w_out = (const u16*)d_in[10];
  u16* out = (u16*)d_out;

  char* ws = (char*)d_ws;
  size_t off = 0;
  auto carve = [&](size_t bytes) { char* p = ws + off; off += (bytes + 255) & ~(size_t)255; return p; };
  u16* z1 = (u16*)carve((size_t)S_ * FH_ * 2 * 5 / 4);
  u16* z2 = (u16*)carve((size_t)S_ * FH_ * 2 * 5 / 4);
  float2* st2 = (float2*)carve((size_t)4 * S_ * 8);
  float2* xst = (float2*)carve((size_t)S_ * 8);
  u16* kvT = (u16*)carve((size_t)D_ * S_ * 2 * 5 / 4);
  u16* woF = (u16*)carve((size_t)FH_ * D_ * 2);

  int dev = 0, cus = 0;
  (void)hipGetDevice(&dev);
  (void)hipDeviceGetAttribute(&cus, hipDeviceAttributeMultiprocessorCount, dev);
  const bool fused = cus >= 256;
  (void)hipFuncSetAttribute((const void*)k_l1, hipFuncAttributeMaxDynamicSharedMemorySize, L1_LDS);
  (void)hipFuncSetAttribute((const void*)k_m, hipFuncAttributeMaxDynamicSharedMemorySize, M_LDS);
  k_l1<<<256, 768, L1_LDS, stream>>>(A_q, A_k, A_v, A_shift_k, B_w_in, z1, z2, st2, xst, kvT, A_ssm_k, B_w_out, B_w_sp, woF);
  if (!fused) k_n<<<512, 64, 0, stream>>>(z2, st2);
  k_m<<<256, 512, M_LDS, stream>>>(B_w_sp, B_b_sp, z1, z2, st2, kvT, A_ssm_k, A_ssm_D, woF, A_q, xst, fused ? out : nullptr);
  if (!fused) {
    (void)hipFuncSetAttribute((const void*)k_l4, hipFuncAttributeMaxDynamicSharedMemorySize, G3_LDS);
    k_l4<<<256, 256, G3_LDS, stream>>>(z1, woF, A_q, xst, kvT, out);
  }
}
```

```cpp
#define HTK 0
#define HTI 0
#define HTW 0
#define HTR 0
#define PSET 2
#define PNT "nt"
#include <hip/hip_runtime.h>
#include <hip/hip_bf16.h>
#include <stdint.h>
#include <type_traits>

typedef unsigned short u16;
typedef __attribute__((ext_vector_type(8))) short bf16x8;
typedef __attribute__((ext_vector_type(4))) short s16x4;
typedef __attribute__((ext_vector_type(4))) float f32x4;
typedef __attribute__((ext_vector_type(16))) float f32x16;
typedef __attribute__((ext_vector_type(4))) unsigned int u32x4;
typedef __attribute__((ext_vector_type(3))) unsigned int u32x3;
typedef __attribute__((ext_vector_type(2))) unsigned int u32x2;

#define LDS_AS __attribute__((address_space(3)))
#define IC(n) std::integral_constant<int, n>{}
#define SB() __builtin_amdgcn_sched_barrier(0)

#define STAMP(k, i) do {} while (0)
#define STAMPW(k, i, w) do {} while (0)
#define TS_DECL do {} while (0)
#define TS(i) do {} while (0)
#define TS_OUT(k, base, n, w) do {} while (0)

#define RT(k, i) do {} while (0)

#define AB_DECL do {} while (0)
#define AB(i) do {} while (0)
#define ABS(i) do {} while (0)
#define AB_OUT(n) do {} while (0)

#define AK_DECL do {} while (0)
#define AK(i) do {} while (0)
#define AK_OUT(n) do {} while (0)

#undef AK_DECL
#undef AK
#undef AK_OUT
#undef AB_DECL
#undef AB
#undef AB_OUT
__device__ unsigned long long g_ak[256][8][16];
#define AK_DECL unsigned long long ak_[16] = {}
#define AK(i) ak_[i] = __builtin_amdgcn_s_memrealtime()
#define AK_OUT(n) do { if ((threadIdx.x & 63) == 0 && threadIdx.x < 512) for (int i_ = 0; i_ < n; ++i_) g_ak[blockIdx.x][threadIdx.x >> 6][i_] = ak_[i_] ? ak_[i_] : ak_[0]; } while (0)
#define AB_DECL unsigned long long ab_[16]
#define AB(i) ab_[i] = __builtin_amdgcn_s_memrealtime()
#define AB_OUT(n) do { if ((threadIdx.x >> 6) == (HTK ? HTW : 0)) { unsigned long long ref_; \
  if (HTK) ref_ = ab_[HTI]; else { \
    unsigned long long b0_ = g_ak[0][HTW][0]; long long acc_ = HTR == 0 ? (1ll << 60) : (HTR == 1 ? -(1ll << 60) : 0); \
    for (int q_ = 0; q_ < 4; ++q_) { long long v_ = (long long)(g_ak[(threadIdx.x & 63) * 4 + q_][HTW][HTI] - b0_); acc_ = HTR == 0 ? (v_ < acc_ ? v_ : acc_) : (HTR == 1 ? (v_ > acc_ ? v_ : acc_) : acc_ + v_); } \
    for (int o_ = 32; o_ > 0; o_ >>= 1) { long long v_ = __shfl_xor(acc_, o_); acc_ = HTR == 0 ? (v_ < acc_ ? v_ : acc_) : (HTR == 1 ? (v_ > acc_ ? v_ : acc_) : acc_ + v_); } \
    if (HTR == 2) acc_ /= 256; ref_ = b0_ + acc_; } \
  while (__builtin_amdgcn_s_memrealtime() < ref_ + 6000) __builtin_amdgcn_s_sleep(1); } } while (0)

__device__ unsigned long long g_p[256][4][2];
__global__ __launch_bounds__(256) void k_p(const char* w_sp, const char* kk, const char* vv, const char* sk, const char* xx, const char* win) {
  const int tid = threadIdx.x, b = blockIdx.x;
  unsigned long long t0 = __builtin_amdgcn_s_memrealtime();
  unsigned r0 = 0, r1 = 0;
  const char* p1 = xx + (size_t)b * 10240;
  asm volatile("global_load_dword %0, %1, off " PNT : "=&v"(r1) : "v"(p1) : "memory");
  asm volatile("s_waitcnt vmcnt(0)" : "+v"(r0), "+v"(r1) :: "memory");
  unsigned long long t1 = __builtin_amdgcn_s_memrealtime();
  if ((tid & 63) == 0) { g_p[b][tid >> 6][0] = t0; g_p[b][tid >> 6][1] = t1 + ((r0 ^ r1) == 0x12345678u); }
}
#undef AB_OUT
#define AB_OUT(n) do { if ((threadIdx.x >> 6) == 0) { \
    unsigned long long b0_ = g_p[0][0][0]; long long mn_ = (1ll << 60), mx_ = -(1ll << 60); \
    for (int q_ = 0; q_ < 16; ++q_) { const int e_ = (threadIdx.x & 63) * 16 + q_; long long v_ = (long long)(g_p[e_ >> 2][e_ & 3][0] - b0_), w_ = (long long)(g_p[e_ >> 2][e_ & 3][1] - b0_); mn_ = v_ < mn_ ? v_ : mn_; mx_ = w_ > mx_ ? w_ : mx_; } \
    for (int o_ = 32; o_ > 0; o_ >>= 1) { long long v_ = __shfl_xor(mn_, o_), w_ = __shfl_xor(mx_, o_); mn_ = v_ < mn_ ? v_ : mn_; mx_ = w_ > mx_ ? w_ : mx_; } \
    const unsigned long long ref_ = b0_ + mn_ + 10000 + 10 * (mx_ - mn_); \
  while (__builtin_amdgcn_s_memrealtime() < ref_) __builtin_amdgcn_s_sleep(1); } } while (0)
#define MAPM 0
#define XE 0
constexpr int S_ = 2048, D_ = 640, FH_ = 768, F2_ = 1536;

__device__ __forceinline__ float bf2f(u16 v) { return __uint_as_float(((unsigned)v) << 16); }
typedef __attribute__((ext_vector_type(2))) __bf16 bf16x2n;
typedef __attribute__((ext_vector_type(2))) float f32x2;
__device__ __forceinline__ unsigned pack2(float lo, float hi) {
  f32x2 v = {lo, hi};
  bf16x2n r = __builtin_convertvector(v, bf16x2n);
  return __builtin_bit_cast(unsigned, r);
}
__device__ __forceinline__ float lo16(unsigned w) { return __uint_as_float(w << 16); }
__device__ __forceinline__ float hi16(unsigned w) { return __uint_as_float(w & 0xFFFF0000u); }
__device__ __forceinline__ u16 f2bf(float f) { return (u16)(pack2(f, f) & 0xFFFFu); }
__device__ __forceinline__ float rbf(float f) { return lo16(pack2(f, f)); }

template <int CTRL>
__device__ __forceinline__ float dppf(float v) {
  return __builtin_bit_cast(float, __builtin_amdgcn_update_dpp(0, __builtin_bit_cast(int, v), CTRL, 0xF, 0xF, true));
}
__device__ __forceinline__ float sum16(float v) {
  v += dppf<0xB1>(v); v += dppf<0x4E>(v); v += dppf<0x141>(v); v += dppf<0x140>(v);
  return v;
}
__device__ __forceinline__ float gelu_tanh(float x) {
  const float kBeta = 0.7978845608028654f;
  const float kKappa = 0.044715f;
  float inner = kBeta * (x + kKappa * x * x * x);
  return x * __builtin_amdgcn_rcpf(1.0f + __expf(-2.0f * inner));
}

template <int NSUB>
__device__ __forceinline__ unsigned boff(unsigned row, unsigned ch) {
  return (unsigned)(NSUB * 512) * (row >> 3) + 512u * (ch >> 2) + 64u * (row & 7) + 16u * ((ch & 3) ^ ((row >> 2) & 3));
}
template <int OFF>
__device__ __forceinline__ s16x4 tr_asm(unsigned a) {
  s16x4 r;
  asm volatile("ds_read_b64_tr_b16 %0, %1 offset:%2" : "=&v"(r) : "v"(a), "i"(OFF) : "memory");
  return r;
}
struct TrBase { unsigned b[2][2]; };
template <int NSUB>
__device__ __forceinline__ TrBase tr_base(unsigned lane, unsigned ldsbase) {
  const unsigned g = lane >> 4, q = (lane & 15) >> 2, p = lane & 3;
  TrBase t;
#pragma unroll
  for (int c1 = 0; c1 < 2; ++c1)
#pragma unroll
    for (int h = 0; h < 2; ++h)
      t.b[c1][h] = ldsbase + boff<NSUB>(8 * g + 4 * h + q, 2 * c1 + (p >> 1)) + 8 * (p & 1);
  return t;
}
template <int NSUB, int KS, int C>
__device__ __forceinline__ void tr_frag_issue(const TrBase& t, unsigned stage_off, s16x4& lo, s16x4& hi) {
  constexpr int OFF = NSUB * 512 * 4 * KS + 512 * (C >> 1);
  lo = tr_asm<OFF>(t.b[C & 1][0] + stage_off);
  hi = tr_asm<OFF>(t.b[C & 1][1] + stage_off);
}
__device__ __forceinline__ bf16x8 join8(s16x4 lo, s16x4 hi) {
  bf16x8 r;
  r[0] = lo[0]; r[1] = lo[1]; r[2] = lo[2]; r[3] = lo[3];
  r[4] = hi[0]; r[5] = hi[1]; r[6] = hi[2]; r[7] = hi[3];
  return r;
}
#define WAIT_LGKM0() asm volatile("s_waitcnt lgkmcnt(0)" ::: "memory")
__device__ __forceinline__ unsigned lds_addr(const void* p) {
  return (unsigned)(uintptr_t)(LDS_AS const char*)p;
}

#define RAW_BARRIER() do { asm volatile("s_waitcnt lgkmcnt(0)" ::: "memory"); __builtin_amdgcn_s_barrier(); } while (0)
#define WAIT_VM(n) asm volatile("s_waitcnt vmcnt(" #n ")" ::: "memory")

__device__ __forceinline__ void flag_wait(unsigned faddr, unsigned target) {
  while (true) {
    unsigned v;
    asm volatile("ds_read_b32 %0, %1\n\ts_waitcnt lgkmcnt(0)" : "=&v"(v) : "v"(faddr) : "memory");
    v = __builtin_amdgcn_readfirstlane(v);
    if (v >= target) break;
    __builtin_amdgcn_s_sleep(1);
  }
}
__device__ __forceinline__ void flag_wait_asm(unsigned faddr, unsigned target) {
  unsigned v, sv;
  asm volatile(".Lfw_%=:\n\tds_read_b32 %0, %2\n\ts_waitcnt lgkmcnt(0)\n\tv_readfirstlane_b32 %1, %0\n\ts_nop 0\n\t"
               "s_cmp_ge_u32 %1, %3\n\ts_cbranch_scc1 .Lfd_%=\n\ts_sleep 1\n\ts_branch .Lfw_%=\n\t.Lfd_%=:"
               : "=&v"(v), "=&s"(sv) : "v"(faddr), "s"(target) : "memory", "scc");
}
__device__ __forceinline__ void flag_signal(unsigned faddr, int lane) {
  if (lane == 0) {
    unsigned one = 1u;
    asm volatile("ds_add_u32 %0, %1" :: "v"(faddr), "v"(one) : "memory");
  }
}
__device__ __forceinline__ void glds16(const void* g, char* lds) {
  __builtin_amdgcn_global_load_lds((const unsigned*)g, (unsigned*)lds, 16, 0, 0);
}
__device__ __forceinline__ void glds_a(const u16* gbase, int ld, char* stageA, int ins, int lane) {
  const int row = ins * 8 + (lane >> 3);
  const int ch = (lane & 7) ^ (row & 7);
  glds16(gbase + (size_t)row * ld + ch * 8, stageA + ins * 1024);
}
__device__ __forceinline__ bf16x8 read_a(const char* stageA, int row, int kchunk) {
  return *(const bf16x8*)(stageA + row * 128 + ((kchunk ^ (row & 7)) << 4));
}
template <int NSUB, int BNV>
__device__ __forceinline__ void glds_b(const u16* gbase, int ld, char* stageB, int ins, int lane) {
  const int o = ins * 1024 + lane * 16;
  const int rg = o / (NSUB * 512), rem = o % (NSUB * 512);
  const int sub = rem >> 9, rr = (rem >> 6) & 7, slot = (rem >> 4) & 3;
  const int row = rg * 8 + rr;
  int ch = sub * 4 + (slot ^ ((row >> 2) & 3));
  if (BNV < NSUB * 32) ch = min(ch, BNV / 8 - 1);
  glds16(gbase + (size_t)row * ld + ch * 8, stageB + ins * 1024);
}

constexpr int L1_A_BYTES = 64 * 128;
constexpr int L1_B_BYTES = 64 * 192 * 2;
constexpr int L1_STAGE = L1_A_BYTES + L1_B_BYTES;
constexpr int L1_LDS = 5 * L1_STAGE;
constexpr unsigned HOLE = 0xFFFFFFFFu;
constexpr int Z1X = S_ * FH_ * 2;
constexpr int KVX = D_ * S_ * 2;
#define S0 7

__device__ __forceinline__ f32x2 unpk(unsigned w) { f32x2 r = {lo16(w), hi16(w)}; return r; }
__device__ __forceinline__ unsigned pk2(f32x2 v) { return pack2(v[0], v[1]); }
__device__ __forceinline__ f32x2 gelu2(f32x2 x) {
  const f32x2 c1 = {-2.3022082f, -2.3022082f}, c2 = {-0.10294324f, -0.10294324f};
  const f32x2 a = x * __builtin_elementwise_fma(x * x, c2, c1);
  f32x2 e = {__builtin_amdgcn_exp2f(a[0]), __builtin_amdgcn_exp2f(a[1])};
  e = e + (f32x2){1.0f, 1.0f};
  const f32x2 r = {__builtin_amdgcn_rcpf(e[0]), __builtin_amdgcn_rcpf(e[1])};
  return x * r;
}
__device__ __forceinline__ float allred_8(float v) {
  v += __uint_as_float(__builtin_amdgcn_mov_dpp(__float_as_uint(v), 0xB1, 0xF, 0xF, true));
  v += __uint_as_float(__builtin_amdgcn_mov_dpp(__float_as_uint(v), 0x4E, 0xF, 0xF, true));
  v += __uint_as_float(__builtin_amdgcn_mov_dpp(__float_as_uint(v), 0x141, 0xF, 0xF, true));
  return v;
}
__device__ __forceinline__ float allred_g(float v) {
  const auto a = __builtin_amdgcn_permlane16_swap(__float_as_uint(v), __float_as_uint(v), false, false);
  v = __uint_as_float(a[0]) + __uint_as_float(a[1]);
  const auto c = __builtin_amdgcn_permlane32_swap(__float_as_uint(v), __float_as_uint(v), false, false);
  return __uint_as_float(c[0]) + __uint_as_float(c[1]);
}

#define WT1 0
#define WTS "sc1"
#define ST16(p, v, bit) do { if (WT1 & (bit)) asm volatile("global_store_dwordx4 %0, %1, off " WTS "\n\ts_nop 1" :: "v"(p), "v"(v) : "memory"); else *(u32x4*)(p) = (v); } while (0)
__global__ __launch_bounds__(768) void k_l1(
    const u16* __restrict__ x, const u16* __restrict__ kin, const u16* __restrict__ vin,
    const u16* __restrict__ shift_k, const u16* __restrict__ w_in,
    u16* __restrict__ z1, u16* __restrict__ z2, float2* __restrict__ st2, float2* __restrict__ xst,
    u16* __restrict__ kvT, const u16* __restrict__ ssm_k, const u16* __restrict__ w_out, const u16* __restrict__ w_sp,
    u16* __restrict__ woF) {
  extern __shared__ __attribute__((aligned(16))) char smem[];
  const int tid = threadIdx.x, lane = tid & 63, wave = __builtin_amdgcn_readfirstlane(tid >> 6);
  const int b = blockIdx.x;
  const int xcd = b & 7, idx = b >> 3;
  const int rb = 8 * (xcd >> 1) + (idx >> 2), cb = 4 * (xcd & 1) + (idx & 3);
  const int row0 = rb * 64;
  const int n0 = cb * 192;
  const int g4 = lane >> 4, l15 = lane & 15;
  STAMP(0, 0);
  RT(0, 0);
  AK_DECL;
  AK(0);

  char* sE = smem + L1_STAGE;
  float2* sRow = (float2*)sE;
  float* sCs = (float*)(sE + 1024);
  float* sStat = (float*)(sE + 2048);
  char* sHand = sE + 8192;
  char* sTr = sE + 40960 + wave * 2048;
  const bool is_z2 = cb >= 4;
  auto epi = [&](const f32x4 (&t)[2][2], int wm, int cg, int swp, int rsw) {
    f32x4 csv[2];
#pragma unroll
    for (int jj = 0; jj < 2; ++jj) csv[jj] = *(const f32x4*)(sCs + cg * 32 + (jj ^ swp) * 16 + 4 * g4);
    f32x2 gv[2][2][2];
    unsigned pk[2][2][2];
#pragma unroll
    for (int i = 0; i < 2; ++i) {
      const float2 rs = sRow[wm * 32 + (i ^ rsw) * 16 + l15];
      const f32x2 nm = {-rs.x, -rs.x}, rd = {rs.y, rs.y};
#pragma unroll
      for (int jj = 0; jj < 2; ++jj)
#pragma unroll
        for (int h = 0; h < 2; ++h) {
          const f32x2 av = {t[i][jj][2 * h], t[i][jj][2 * h + 1]};
          const f32x2 cv = {csv[jj][2 * h], csv[jj][2 * h + 1]};
          gv[i][jj][h] = gelu2(__builtin_elementwise_fma(nm, cv, av) * rd);
          pk[i][jj][h] = pk2(gv[i][jj][h]);
        }
    }
    if (!is_z2) {
      const int lp = l15 + 16 * (2 * ((g4 & 1) ^ swp) + (g4 >> 1));
      char* zdst = (char*)z1 + (size_t)((rb * 4 + wm * 2) * 24 + cb * 6 + cg) * 1024 + lp * 16;
      u32x4 o[2];
#pragma unroll
      for (int i = 0; i < 2; ++i) {
        const auto sa = __builtin_amdgcn_permlane16_swap(pk[i][0][0], pk[i][1][0], false, false);
        const auto sb = __builtin_amdgcn_permlane16_swap(pk[i][0][1], pk[i][1][1], false, false);
        o[i][0] = sa[0]; o[i][1] = sb[0]; o[i][2] = sa[1]; o[i][3] = sb[1];
      }
#pragma unroll
      for (int i = 0; i < 2; ++i) ST16(zdst + (i ^ rsw) * 24 * 1024, ((u32x4){o[i][0], o[i][1], o[i][2], HOLE}), 1);
      *(u32x2*)((char*)z1 + Z1X + ((rb * 2 + wm) * 24 + cb * 6 + cg) * 512 + lp * 8) = (u32x2){rsw ? o[1][3] : o[0][3], rsw ? o[0][3] : o[1][3]};
    } else {
#pragma unroll
      for (int jj = 0; jj < 2; ++jj) {
        char* wb = sTr + l15 * 64 + 8 * ((4 * (jj ^ swp) + g4) ^ (l15 >> 1));
#pragma unroll
        for (int i = 0; i < 2; ++i) *(u32x2*)(wb + (i ^ rsw) * 1024) = (u32x2){pk[i][jj][0], pk[i][jj][1]};
      }
      const unsigned q = l15 >> 2, p = lane & 3;
      const unsigned ra = lds_addr(sTr) + (8 * g4 + q) * 64 + 8 * (p ^ (4 * (g4 & 1) + (q >> 1)));
      s16x4 tr[2][2];
      tr[0][0] = tr_asm<0>(ra); tr[0][1] = tr_asm<256>(ra ^ 16u);
      tr[1][0] = tr_asm<0>(ra ^ 32u); tr[1][1] = tr_asm<256>(ra ^ 48u);
      float sv[2][2], pv[2];
#pragma unroll
      for (int i = 0; i < 2; ++i) {
        const unsigned p0 = __float_as_uint(gv[i][0][0][0]);
        const auto pa = __builtin_amdgcn_permlane16_swap(p0, p0, false, false);
        const auto pb = __builtin_amdgcn_permlane32_swap(pa[0], pa[0], false, false);
        pv[i] = __uint_as_float(pb[0]);
        const f32x2 pp = {pv[i], pv[i]};
        const f32x2 d0 = gv[i][0][0] - pp, d1 = gv[i][0][1] - pp, d2 = gv[i][1][0] - pp, d3 = gv[i][1][1] - pp;
        const f32x2 s1 = (d0 + d1) + (d2 + d3);
        const f32x2 s2 = __builtin_elementwise_fma(d0, d0, d1 * d1) + __builtin_elementwise_fma(d2, d2, d3 * d3);
        sv[i][0] = s1[0] + s1[1]; sv[i][1] = s2[0] + s2[1];
      }
      const auto u0 = __builtin_amdgcn_permlane16_swap(__float_as_uint(sv[0][0]), __float_as_uint(sv[0][1]), false, false);
      const auto u1 = __builtin_amdgcn_permlane16_swap(__float_as_uint(sv[1][0]), __float_as_uint(sv[1][1]), false, false);
      const float w0 = __uint_as_float(u0[0]) + __uint_as_float(u0[1]), w1 = __uint_as_float(u1[0]) + __uint_as_float(u1[1]);
      const auto u2 = __builtin_amdgcn_permlane32_swap(__float_as_uint(w0), __float_as_uint(w1), false, false);
      float* sd = sStat + ((wm * 32 + ((g4 >> 1) ^ rsw) * 16 + l15) * 6 + cg) * 4 + (g4 & 1);
      sd[0] = __uint_as_float(u2[0]) + __uint_as_float(u2[1]);
      sd[2] = (g4 >> 1) ? pv[1] : pv[0];
      asm volatile("s_waitcnt lgkmcnt(0)" : "+v"(tr[0][0]), "+v"(tr[0][1]), "+v"(tr[1][0]), "+v"(tr[1][1]) :: "memory");
      char* zdst = (char*)z2 + (size_t)((rb * 2 + wm) * 48 + (cb - 4) * 12 + cg * 2) * 1024 + lane * 16;
      const bool tag = ((rb * 2 + wm) & 15) >= S0;
      u32x4 o[2];
#pragma unroll
      for (int jj = 0; jj < 2; ++jj) {
        o[jj] = __builtin_bit_cast(u32x4, join8(tr[jj][0], tr[jj][1]));
        ST16(zdst + jj * 1024, ((u32x4){o[jj][0], o[jj][1], o[jj][2], tag ? HOLE : o[jj][3]}), 2);
      }
      *(u32x2*)((char*)z2 + Z1X + ((rb * 2 + wm) * 24 + (cb - 4) * 6 + cg) * 512 + lane * 8) = (u32x2){o[0][3], o[1][3]};
    }
  };

  if (__builtin_expect(wave >= 4, 1)) {
    const int lw = wave - 4;
    const unsigned voA = (unsigned)((lw * 8 + (lane >> 3)) * (D_ * 2) + (((lane & 7) ^ (lane >> 3)) << 4));
    const unsigned voB = (unsigned)((lw * 8 + ((lane >> 2) & 7)) * (F2_ * 2) + (lane >> 5) * 64 + (((lane & 3) ^ ((lw * 2 + ((lane >> 4) & 1)) & 3)) << 4));
    const char* gA = (const char*)x + (size_t)row0 * (D_ * 2);
    const char* gB = (const char*)w_in + n0 * 2;
    const unsigned lbase = __builtin_amdgcn_readfirstlane(lds_addr(smem));
    auto issue = [&](int kt) {
      const unsigned st = lbase + (kt % 5) * L1_STAGE;
      asm volatile("s_mov_b32 m0, %2\n\ts_nop 0\n\tglobal_load_lds_dwordx4 %0, %1" :: "v"(voA), "s"(gA + kt * 128), "s"(st + lw * 1024) : "memory");
#pragma unroll
      for (int i = 0; i < 3; ++i)
        asm volatile("s_mov_b32 m0, %2\n\ts_nop 0\n\tglobal_load_lds_dwordx4 %0, %1"
                     :: "v"(voB), "s"(gB + (size_t)kt * (64 * F2_ * 2) + i * 128), "s"(st + L1_A_BYTES + (lw * 3 + i) * 1024) : "memory");
    };
#define LPRIO 2
    __builtin_amdgcn_s_setprio(LPRIO);
    issue(0);
    AK(1);
    __builtin_amdgcn_s_barrier();
    issue(1); issue(2);
    AK(2);
    const int ks0 = row0 + lw * 8, kd = cb * 80 + min(lane, 39) * 2;
    const int wfr = ((xcd >> 1) * 32 + idx) * 8 + lw, wfi = min(wfr, 959);
    u32x4 sw;
    const char* kvp[3];
#pragma unroll
    for (int i = 0; i < 3; ++i) {
      const int id = min(i * 64 + lane, 189), r = id / 10, c = id % 10;
      kvp[i] = (r < 11 ? (const char*)kin + (size_t)max(ks0 - 3 + r, 0) * (D_ * 2) : (const char*)vin + (size_t)(ks0 + r - 11) * (D_ * 2)) + cb * 160 + c * 16;
    }
    auto kvload = [&]() {
#pragma unroll
      for (int i = 0; i < 3; ++i)
        asm volatile("s_mov_b32 m0, %1\n\ts_nop 0\n\tglobal_load_lds_dwordx4 %0, off" :: "v"(kvp[i]), "s"(lbase + lw * 3072 + i * 1024) : "memory");
      asm volatile("s_mov_b32 m0, %2\n\ts_nop 0\n\tglobal_load_lds_dwordx4 %0, %1"
                   :: "v"((unsigned)((lane >> 1) * (D_ * 2) + (lane & 1) * 16)), "s"(w_out + (size_t)(wfi / 40) * 32 * D_ + (wfi % 40) * 16), "s"(lbase + 24576 + lw * 1024) : "memory");
      asm volatile("global_load_dwordx4 %0, %1, %2" : "=&v"(sw) : "v"(kd * 8), "s"(shift_k) : "memory");
    };
    auto womath = [&]() {
      const unsigned ra = lbase + 24576 + lw * 1024 + (8 * g4 + (l15 >> 2)) * 32 + 8 * (lane & 3);
      s16x4 lo = tr_asm<0>(ra), hi = tr_asm<128>(ra);
      asm volatile("s_waitcnt lgkmcnt(0)" : "+v"(lo), "+v"(hi) :: "memory");
      ST16((char*)woF + (size_t)wfi * 1024 + lane * 16, __builtin_bit_cast(u32x4, join8(lo, hi)), 4);
    };
    auto kvmath = [&]() {
      unsigned kk[11], vv[8];
      const char* kl = smem + lw * 3072 + min(lane, 39) * 4;
#pragma unroll
      for (int i = 0; i < 11; ++i) kk[i] = *(const unsigned*)(kl + i * 160);
#pragma unroll
      for (int i = 0; i < 8; ++i) vv[i] = *(const unsigned*)(kl + (11 + i) * 160);
      const f32x2 w0 = {lo16(sw[0]), lo16(sw[2])}, w1 = {hi16(sw[0]), hi16(sw[2])};
      const f32x2 w2 = {lo16(sw[1]), lo16(sw[3])}, w3 = {hi16(sw[1]), hi16(sw[3])};
      f32x2 kf[11], rr[8];
#pragma unroll
      for (int i = 0; i < 11; ++i) kf[i] = unpk((i < 3 && ks0 == 0) ? 0u : kk[i]);
#pragma unroll
      for (int i = 0; i < 8; ++i) {
        f32x2 a0 = kf[i] * w3;
        a0 = __builtin_elementwise_fma(kf[i + 1], w2, a0);
        a0 = __builtin_elementwise_fma(kf[i + 2], w1, a0);
        a0 = __builtin_elementwise_fma(kf[i + 3], w0, a0);
        rr[i] = unpk(pk2(a0)) * unpk(vv[i]);
      }
      u32x4 oa, ob;
#pragma unroll
      for (int e = 0; e < 4; ++e) { oa[e] = pack2(rr[2 * e][0], rr[2 * e + 1][0]); ob[e] = pack2(rr[2 * e][1], rr[2 * e + 1][1]); }
      if (lane < 40) {
        ST16(kvT + (size_t)kd * S_ + ks0, ((u32x4){oa[0], oa[1], oa[2], HOLE}), 8);
        ST16(kvT + (size_t)(kd + 1) * S_ + ks0, ((u32x4){ob[0], ob[1], ob[2], HOLE}), 8);
        *(u32x2*)((char*)kvT + KVX + kd * 1024 + ks0) = (u32x2){oa[3], ob[3]};
      }
    };
#define PFK 8
#define PFF 4
#define KVM 1
#define SPF 0
#define SP0 1
#define SPN 3
    unsigned long long pfs = 0;
    const char* spb = (const char*)w_sp + (size_t)(row0 + (SPF == 1 ? cb * 8 + lw : (idx & 3) * 16 + lw * 2)) * 4096;
    auto spf = [&](int i) {
      const int r = i & (SPF == 1 ? 0 : 1), k = SPF == 1 ? i : i >> 1;
      asm volatile("s_load_dwordx2 %0, %1, %2" : "+s"(pfs) : "s"(spb), "i"(r * 4096 + (k & 3) * 1024 + (k >> 2) * 256 + 124) : "memory");
    };
    constexpr int NPF = PFF == 1 ? 2 : 1;
    unsigned pfv, pfw = 0;
    auto prefetch = [&]() {
      const int t_ = tid - 256;
      if (PFF == 1) {
        const char* pw = (const char*)w_sp + (size_t)(row0 + (idx & 3) * 16 + (t_ >> 5)) * 4096 + (t_ & 31) * 128;
        const int t2 = t_ % 96;
        const char* pa = (const char*)ssm_k + (t2 < 64 ? (size_t)b * 8192 + t2 * 128 : (size_t)(512 + (b & 127)) * 4096 + (t2 & 31) * 128);
        asm volatile("global_load_dword %0, %1, off" : "=&v"(pfw) : "v"(pa) : "memory");
        asm volatile("global_load_dword %0, %1, off" : "=&v"(pfv) : "v"(pw) : "memory");
      } else {
        constexpr int NW = 64 * PFF;
        const int t2 = min(max(t_ - NW, 0), 95);
        const char* pw = (const char*)w_sp + (size_t)(row0 + (idx & 3) * 16 + (t_ / (4 * PFF)) % 16) * 4096 + ((t_ / PFF) & 3) * 1024 + (t_ % PFF) * 128;
        const char* pa = (const char*)ssm_k + (t2 < 64 ? (size_t)b * 8192 + t2 * 128 : (size_t)(512 + (b & 127)) * 4096 + (t2 & 31) * 128);
        const char* pc_ = (!SPF && t_ < NW) ? pw : pa;
        asm volatile("global_load_dword %0, %1, off" : "=&v"(pfv) : "v"(pc_) : "memory");
      }
    };
#pragma unroll
    for (int kt = 0; kt < 10; ++kt) {
      const int nv = (kt + 1 < 10 ? 4 : 0) + (kt + 2 < 10 ? 4 : 0) + (kt >= 8 ? 5 : 0) + ((PFK < kt && kt <= PFK + 3) ? NPF : 0);
      asm volatile("s_waitcnt vmcnt(%0)" :: "n"(nv) : "memory");
      __builtin_amdgcn_s_barrier();
      if (kt == 0) AK(3); if (kt == 1) AK(4); if (kt == 3) AK(5); if (kt == 5) AK(6); if (kt == 7) AK(7); if (kt == 9) AK(8);
      if (kt + 3 < 10) issue(kt + 3);
      if (kt == 7) kvload();
      if (kt == PFK) prefetch();
      if (SPF && kt >= SP0) {
#pragma unroll
        for (int i = (kt - SP0) * SPN; i < (kt - SP0 + 1) * SPN && i < 16 * SPF; ++i) spf(i);
      }
    }
    __builtin_amdgcn_s_setprio(0);
    if (KVM == 0) {
      asm volatile("s_waitcnt vmcnt(%1)" : "+v"(sw) : "n"(PFK >= 7 ? NPF : 0) : "memory");
      kvmath();
      if (!is_z2 && wfr < 960) womath();
    }
    SB();
    asm volatile("s_waitcnt lgkmcnt(0)" : "+s"(pfs) :: "memory");
    RAW_BARRIER();
    AK(9);
    {
      const int w = lw >> 1, gs = lw & 1;
      f32x4 t4[2][2];
#pragma unroll
      for (int q = 0; q < 4; ++q) t4[q >> 1][q & 1] = *(const f32x4*)(sHand + ((w * 2 + gs) * 4 + q) * 1024 + lane * 16);
      epi(t4, w >> 1, (w & 1) * 3 + 1 + gs, 0, 0);
    }
    AK(10);
    if (is_z2) RAW_BARRIER();
    AK(11);
    if (KVM == 1) {
      SB();
      if (is_z2) asm volatile("s_waitcnt vmcnt(%0)" :: "n"(2 + (PFK >= 7 ? NPF : 0)) : "memory");
      asm volatile("s_waitcnt vmcnt(%1)" : "+v"(sw) : "n"(3 + (PFK >= 7 ? NPF : 0)) : "memory");
      kvmath();
      if (!is_z2 && wfr < 960) womath();
    }
    AK(12);
    asm volatile("s_waitcnt vmcnt(0)" : "+v"(pfw), "+v"(pfv) :: "memory");
    AK(13);
    AK_OUT(14);
    RT(0, 1);
    return;
  }

  unsigned pcv, pfx;
  {
    unsigned long long pc; unsigned clen;
    asm volatile("s_getpc_b64 %0\n.Lpc_here_%=:\n\ts_mov_b32 %1, k_end-.Lpc_here_%=" : "=s"(pc), "=s"(clen));
    const unsigned o = idx < 2 ? min((unsigned)((idx * 4 + wave) * 8192 + lane * 128), clen - 4u) & ~3u : 0u;
    asm volatile("global_load_dword %0, %1, %2" : "=&v"(pcv) : "v"(o), "s"(pc) : "memory");
  }
  __builtin_amdgcn_s_barrier();
  auto pfx_issue = [&]() {
    const int t2 = max(tid - 80, 0), r2 = min(t2 / 3, 39);
    const char* pa = (const char*)x + (size_t)(row0 + (idx & 3) * 16 + tid / 5) * (D_ * 2) + 640 + (tid % 5) * 128;
    const char* pb = (const char*)w_in + (size_t)(320 + (idx >> 2) * 40 + r2) * (F2_ * 2) + n0 * 2 + (t2 % 3) * 128;
    const char* pc_ = tid < 80 ? pa : pb;
    asm volatile("global_load_dword %0, %1, off" : "=&v"(pfx) : "v"(pc_) : "memory");
  };
#define PFXM 0
  if (PFXM == 0) pfx_issue();
  if (PFXM == 2) pfx = 0;
  const int wm = wave >> 1, wn = wave & 1;
  f32x4 acc[2][6], acs[3], accS = {0.f, 0.f, 0.f, 0.f}, accG = {0.f, 0.f, 0.f, 0.f};
#pragma unroll
  for (int j = 0; j < 6; ++j) {
    acc[0][j] = (f32x4){0.f, 0.f, 0.f, 0.f};
    acc[1][j] = (f32x4){0.f, 0.f, 0.f, 0.f};
  }
#pragma unroll
  for (int j = 0; j < 3; ++j) acs[j] = (f32x4){0.f, 0.f, 0.f, 0.f};
  unsigned tb[2][2], aA[2][2];
#pragma unroll
  for (int c1 = 0; c1 < 2; ++c1)
#pragma unroll
    for (int h = 0; h < 2; ++h)
      tb[c1][h] = lds_addr(smem) + L1_A_BYTES + wn * 3 * 512 + boff<6>(8 * g4 + 4 * h + (l15 >> 2), 2 * (c1 ^ wm) + ((lane & 3) >> 1)) + 8 * (lane & 1);
#pragma unroll
  for (int ks = 0; ks < 2; ++ks)
#pragma unroll
    for (int i = 0; i < 2; ++i) aA[ks][i] = lds_addr(smem) + (wm * 32 + (i ^ wn) * 16 + l15) * 128 + (((ks * 4 + g4) ^ (l15 & 7)) << 4);
  bf16x8 ones;
#pragma unroll
  for (int e = 0; e < 8; ++e) ones[e] = (short)0x3F80;
  int cnt = 10, slot = 0, sdl;
  SB();
  __builtin_amdgcn_s_barrier();
  STAMP(0, 1);
  if (PFXM == 1) pfx_issue();
  asm volatile(
      "s_nop 1\n\t"
      "ds_read_b128 v[96:99], %21\n\t"
      "ds_read_b64_tr_b16 v[104:105], %17 offset:0\n\t"
      "ds_read_b64_tr_b16 v[106:107], %18 offset:0\n\t"
      "ds_read_b128 v[100:103], %22\n\t"
      "ds_read_b64_tr_b16 v[108:109], %19 offset:0\n\t"
      "ds_read_b64_tr_b16 v[110:111], %20 offset:0\n\t"
      "ds_read_b64_tr_b16 v[112:113], %17 offset:512\n\t"
      "ds_read_b64_tr_b16 v[114:115], %18 offset:512\n\t"
      "ds_read_b64_tr_b16 v[116:117], %19 offset:512\n\t"
      "ds_read_b64_tr_b16 v[118:119], %20 offset:512\n\t"
      "ds_read_b64_tr_b16 v[120:121], %17 offset:1024\n\t"
      "ds_read_b64_tr_b16 v[122:123], %18 offset:1024\n\t"
      "ds_read_b64_tr_b16 v[124:125], %19 offset:1024\n\t"
      "ds_read_b64_tr_b16 v[126:127], %20 offset:1024\n\t"
      ".Ll1_%=:\n\t"
      "s_waitcnt lgkmcnt(11)\n\t"
      "v_mfma_f32_16x16x32_bf16 %0, v[104:107], v[96:99], %0\n\t"
      "ds_read_b128 v[128:131], %23\n\t"
      "s_waitcnt lgkmcnt(11)\n\t"
      "v_mfma_f32_16x16x32_bf16 %6, v[104:107], v[100:103], %6\n\t"
      "ds_read_b64_tr_b16 v[136:137], %17 offset:12288\n\t"
      "s_waitcnt lgkmcnt(10)\n\t"
      "v_mfma_f32_16x16x32_bf16 %1, v[108:111], v[96:99], %1\n\t"
      "ds_read_b64_tr_b16 v[138:139], %18 offset:12288\n\t"
      "v_mfma_f32_16x16x32_bf16 %7, v[108:111], v[100:103], %7\n\t"
      "ds_read_b128 v[132:135], %24\n\t"
      "s_waitcnt lgkmcnt(10)\n\t"
      "v_mfma_f32_16x16x32_bf16 %2, v[112:115], v[96:99], %2\n\t"
      "ds_read_b64_tr_b16 v[140:141], %19 offset:12288\n\t"
      "v_mfma_f32_16x16x32_bf16 %8, v[112:115], v[100:103], %8\n\t"
      "ds_read_b64_tr_b16 v[142:143], %20 offset:12288\n\t"
      "s_waitcnt lgkmcnt(10)\n\t"
      "v_mfma_f32_16x16x32_bf16 %3, v[116:119], v[96:99], %3\n\t"
      "ds_read_b64_tr_b16 v[144:145], %17 offset:12800\n\t"
      "v_mfma_f32_16x16x32_bf16 %9, v[116:119], v[100:103], %9\n\t"
      "ds_read_b64_tr_b16 v[146:147], %18 offset:12800\n\t"
      "s_waitcnt lgkmcnt(10)\n\t"
      "v_mfma_f32_16x16x32_bf16 %4, v[120:123], v[96:99], %4\n\t"
      "ds_read_b64_tr_b16 v[148:149], %19 offset:12800\n\t"
      "v_mfma_f32_16x16x32_bf16 %10, v[120:123], v[100:103], %10\n\t"
      "ds_read_b64_tr_b16 v[150:151], %20 offset:12800\n\t"
      "s_waitcnt lgkmcnt(10)\n\t"
      "v_mfma_f32_16x16x32_bf16 %5, v[124:127], v[96:99], %5\n\t"
      "ds_read_b64_tr_b16 v[152:153], %17 offset:13312\n\t"
      "v_mfma_f32_16x16x32_bf16 %11, v[124:127], v[100:103], %11\n\t"
      "ds_read_b64_tr_b16 v[154:155], %18 offset:13312\n\t"
      "v_mfma_f32_16x16x32_bf16 %15, %28, v[96:99], %15\n\t"
      "ds_read_b64_tr_b16 v[156:157], %19 offset:13312\n\t"
      "v_mfma_f32_16x16x32_bf16 %16, v[96:99], v[96:99], %16\n\t"
      "ds_read_b64_tr_b16 v[158:159], %20 offset:13312\n\t"
      "v_mfma_f32_16x16x32_bf16 %12, v[104:107], %28, %12\n\t"
      "v_mfma_f32_16x16x32_bf16 %13, v[112:115], %28, %13\n\t"
      "v_mfma_f32_16x16x32_bf16 %14, v[120:123], %28, %14\n\t"
      "s_add_u32 %26, %26, 1\n\t"
      "s_cmp_eq_u32 %26, 5\n\t"
      "s_cselect_b32 %27, 0xfffd8000, 0\n\t"
      "s_cselect_b32 %26, 0, %26\n\t"
      "s_add_u32 %27, %27, 0x8000\n\t"
      "s_sub_u32 %25, %25, 1\n\t"
      "s_cmp_eq_u32 %25, 0\n\t"
      "s_cbranch_scc1 .Ll1s_%=\n\t"
      "s_barrier\n\t"
      ".Ll1s_%=:\n\t"
      "s_waitcnt lgkmcnt(11)\n\t"
      "v_mfma_f32_16x16x32_bf16 %0, v[136:139], v[128:131], %0\n\t"
      "v_add_u32 %21, %27, %21\n\t"
      "ds_read_b128 v[96:99], %21\n\t"
      "s_waitcnt lgkmcnt(11)\n\t"
      "v_mfma_f32_16x16x32_bf16 %6, v[136:139], v[132:135], %6\n\t"
      "v_add_u32 %17, %27, %17\n\t"
      "ds_read_b64_tr_b16 v[104:105], %17 offset:0\n\t"
      "s_waitcnt lgkmcnt(10)\n\t"
      "v_mfma_f32_16x16x32_bf16 %1, v[140:143], v[128:131], %1\n\t"
      "v_add_u32 %18, %27, %18\n\t"
      "ds_read_b64_tr_b16 v[106:107], %18 offset:0\n\t"
      "v_mfma_f32_16x16x32_bf16 %7, v[140:143], v[132:135], %7\n\t"
      "v_add_u32 %22, %27, %22\n\t"
      "ds_read_b128 v[100:103], %22\n\t"
      "s_waitcnt lgkmcnt(10)\n\t"
      "v_mfma_f32_16x16x32_bf16 %2, v[144:147], v[128:131], %2\n\t"
      "v_add_u32 %19, %27, %19\n\t"
      "ds_read_b64_tr_b16 v[108:109], %19 offset:0\n\t"
      "v_mfma_f32_16x16x32_bf16 %8, v[144:147], v[132:135], %8\n\t"
      "v_add_u32 %20, %27, %20\n\t"
      "ds_read_b64_tr_b16 v[110:111], %20 offset:0\n\t"
      "s_waitcnt lgkmcnt(10)\n\t"
      "v_mfma_f32_16x16x32_bf16 %3, v[148:151], v[128:131], %3\n\t"
      "v_add_u32 %23, %27, %23\n\t"
      "ds_read_b64_tr_b16 v[112:113], %17 offset:512\n\t"
      "v_mfma_f32_16x16x32_bf16 %9, v[148:151], v[132:135], %9\n\t"
      "v_add_u32 %24, %27, %24\n\t"
      "ds_read_b64_tr_b16 v[114:115], %18 offset:512\n\t"
      "s_waitcnt lgkmcnt(10)\n\t"
      "v_mfma_f32_16x16x32_bf16 %4, v[152:155], v[128:131], %4\n\t"
      "ds_read_b64_tr_b16 v[116:117], %19 offset:512\n\t"
      "v_mfma_f32_16x16x32_bf16 %10, v[152:155], v[132:135], %10\n\t"
      "ds_read_b64_tr_b16 v[118:119], %20 offset:512\n\t"
      "s_waitcnt lgkmcnt(10)\n\t"
      "v_mfma_f32_16x16x32_bf16 %5, v[156:159], v[128:131], %5\n\t"
      "ds_read_b64_tr_b16 v[120:121], %17 offset:1024\n\t"
      "v_mfma_f32_16x16x32_bf16 %11, v[156:159], v[132:135], %11\n\t"
      "ds_read_b64_tr_b16 v[122:123], %18 offset:1024\n\t"
      "v_mfma_f32_16x16x32_bf16 %15, %28, v[128:131], %15\n\t"
      "ds_read_b64_tr_b16 v[124:125], %19 offset:1024\n\t"
      "v_mfma_f32_16x16x32_bf16 %16, v[128:131], v[128:131], %16\n\t"
      "ds_read_b64_tr_b16 v[126:127], %20 offset:1024\n\t"
      "v_mfma_f32_16x16x32_bf16 %12, v[136:139], %28, %12\n\t"
      "v_mfma_f32_16x16x32_bf16 %13, v[144:147], %28, %13\n\t"
      "v_mfma_f32_16x16x32_bf16 %14, v[152:155], %28, %14\n\t"
      "s_cmp_lg_u32 %25, 0\n\t"
      "s_cbranch_scc1 .Ll1_%=\n\t"
      "s_waitcnt lgkmcnt(0)\n\t"
      "s_nop 15\n\t"
      "s_nop 7\n\t"
      : "+v"(acc[0][0]), "+v"(acc[0][1]), "+v"(acc[0][2]), "+v"(acc[0][3]), "+v"(acc[0][4]), "+v"(acc[0][5]),
        "+v"(acc[1][0]), "+v"(acc[1][1]), "+v"(acc[1][2]), "+v"(acc[1][3]), "+v"(acc[1][4]), "+v"(acc[1][5]),
        "+v"(acs[0]), "+v"(acs[1]), "+v"(acs[2]), "+v"(accS), "+v"(accG), "+v"(tb[0][0]), "+v"(tb[0][1]), "+v"(tb[1][0]), "+v"(tb[1][1]),
        "+v"(aA[0][0]), "+v"(aA[0][1]), "+v"(aA[1][0]), "+v"(aA[1][1]), "+s"(cnt), "+s"(slot), "=&s"(sdl)
      : "v"(ones)
      : "memory", "scc", "v96", "v97", "v98", "v99", "v100", "v101", "v102", "v103", "v104", "v105", "v106", "v107", "v108", "v109", "v110", "v111", "v112", "v113", "v114", "v115", "v116", "v117", "v118", "v119", "v120", "v121", "v122", "v123", "v124", "v125", "v126", "v127", "v128", "v129", "v130", "v131", "v132", "v133", "v134", "v135", "v136", "v137", "v138", "v139", "v140", "v141", "v142", "v143", "v144", "v145", "v146", "v147", "v148", "v149", "v150", "v151", "v152", "v153", "v154", "v155", "v156", "v157", "v158", "v159");
  STAMP(0, 3);
  AK(8);
#define PFC 0
  unsigned pfc = 0;
  auto pfc_issue = [&]() {
    const char* pw = (const char*)w_sp + (size_t)(row0 + (idx & 3) * 16 + (tid >> 4)) * 4096 + ((tid >> 2) & 3) * 1024 + (4 + (tid & 3)) * 128;
    asm volatile("global_load_dword %0, %1, off" : "=&v"(pfc) : "v"(pw) : "memory");
  };
  if (PFC == 1) pfc_issue();

#pragma unroll
  for (int gs = 0; gs < 2; ++gs)
#pragma unroll
    for (int q = 0; q < 4; ++q)
      *(f32x4*)(sHand + ((wave * 2 + gs) * 4 + (q ^ wm ^ (2 * wn))) * 1024 + lane * 16) = acc[q >> 1][2 + 2 * gs + (q & 1)];
  if (l15 == 0) {
#pragma unroll
    for (int j = 0; j < 3; ++j) *(f32x4*)(sCs + wn * 96 + (2 * j + wm) * 16 + 4 * g4) = acs[j];
  }
  {
    const int r = wm * 32 + wn * 16 + l15;
    const bool dg = g4 == (l15 >> 2);
    const float mean = accS[0] * (1.0f / 640.0f);
    const float s2 = (l15 & 2) ? ((l15 & 1) ? accG[3] : accG[2]) : ((l15 & 1) ? accG[1] : accG[0]);
    float sq = s2 - accS[0] * mean;
    if (__builtin_expect(__builtin_amdgcn_ballot_w64(dg && !(sq >= 1e-3f * accS[0] * mean)) != 0, 0)) {
      const f32x2 mm = {mean, mean};
      f32x2 q2 = {0.f, 0.f};
      const u32x4* xp = (const u32x4*)(x + (size_t)(row0 + r) * D_ + g4 * 160);
#pragma unroll 1
      for (int c = 0; c < 20; ++c) {
        const u32x4 xv = xp[c];
#pragma unroll
        for (int e = 0; e < 4; ++e) { const f32x2 d = unpk(xv[e]) - mm; q2 = __builtin_elementwise_fma(d, d, q2); }
      }
      sq = allred_g(q2[0] + q2[1]);
    }
    const float mbf = rbf(mean);
    const float vbf = rbf(fmaxf(sq, 0.f) * (1.0f / 640.0f));
    const float den = rbf(__builtin_amdgcn_sqrtf(rbf(vbf + 1e-5f)));
    const float rden = __builtin_amdgcn_rcpf(den);
    if (dg) {
      sRow[r] = make_float2(mbf, rden);
      if (cb == 0) xst[row0 + r] = make_float2(mbf, rden);
    }
  }
  TS_DECL;
  TS(0);
  AK(7);
  RAW_BARRIER();
  AK(9);
  TS(1);
  if (PFC == 2) pfc_issue();
  {
    f32x4 t4[2][2];
#pragma unroll
    for (int q = 0; q < 4; ++q) t4[q >> 1][q & 1] = acc[q >> 1][q & 1];
    epi(t4, wm, wn * 3, wm, wn);
  }
  TS(2); AK(10);
  if (is_z2) RAW_BARRIER();
  TS(3); AK(11);
  TS(4);
  if (is_z2 && tid < 64) {
    float mc[6];
    float sum = 0.f, m2 = 0.f;
#pragma unroll
    for (int c = 0; c < 6; ++c) {
      const f32x4 pp = *(const f32x4*)(sStat + (tid * 6 + c) * 4);
      const float dl = pp[0] * (1.0f / 32.0f);
      mc[c] = pp[2] + dl;
      sum += mc[c];
      m2 += fmaxf(pp[1] - pp[0] * dl, 0.f);
    }
    const float mb = sum * (1.0f / 6.0f);
    float dq = 0.f;
#pragma unroll
    for (int c = 0; c < 6; ++c) { const float dd = mc[c] - mb; dq += dd * dd; }
    st2[(size_t)(cb - 4) * S_ + row0 + tid] = make_float2(mb, m2 + 32.0f * dq);
  }
  TS(5); AK(12);
  asm volatile("s_waitcnt vmcnt(0)" : "+v"(pcv), "+v"(pfx), "+v"(pfc) :: "memory");
  TS(6); AK(13);
  AK_OUT(14);
  TS_OUT(0, 8, 7, TSW);
  STAMP(0, 4);
  RT(0, 1);
}

constexpr int CV_C = 4608, CV_CS = 4160, CV_BUF = CV_C + 2 * CV_CS;
__device__ __forceinline__ void conv_core(unsigned buf, int lane, f32x4 (&ac)[8]) {
  const int l15 = lane & 15, g4 = lane >> 4;
  const int c = (l15 + 1) & 1;
  unsigned aa[4];
#pragma unroll
  for (int k = 0; k < 4; ++k) aa[k] = buf + CV_C + c * CV_CS + (2031 - l15 + 8 * g4 - c) * 2 - 512 - 1024 * k;
  unsigned bb = buf + 512 + 32 * l15 - 32 + 16 * g4;
  u32x2 A[2][8][2];
  u32x4 B[2][8];
  constexpr int TT[36] = {7, 6, 5, 4, 7, 6, 5, 4, 7, 6, 5, 3, 7, 6, 5, 4, 7, 6, 3, 2, 7, 6, 5, 4, 7, 3, 2, 1, 7, 6, 5, 4, 3, 2, 1, 0};
  constexpr int AA[36] = {0, 0, 0, 0, 1, 1, 1, 1, 2, 2, 2, 0, 3, 3, 3, 2, 4, 4, 1, 0, 5, 5, 4, 3, 6, 2, 1, 0, 7, 6, 5, 4, 3, 2, 1, 0};
  auto ld1 = [&](int p, int n) {
    if (n < 16) {
      const int a = n >> 1, o = ((a & 1) ? 0 : 128) + 2 * (n & 1);
      asm volatile("ds_read2_b32 %0, %1 offset0:%2 offset1:%3" : "=&v"(A[p][(n >> 1) & 7][n & 1]) : "v"(aa[(a >> 1) & 3]), "i"(o), "i"(o + 1) : "memory");
    } else
      asm volatile("ds_read_b128 %0, %1 offset:%2" : "=&v"(B[p][n & 7]) : "v"(bb), "i"(512 * (n & 7)) : "memory");
  };
  auto next = [&](int dl) {
#pragma unroll
    for (int k = 0; k < 4; ++k) aa[k] += dl;
    bb += dl;
    SB();
  };
  auto land = [&](int p) {
    asm volatile("s_waitcnt lgkmcnt(0)" : "+v"(A[p][0][0]), "+v"(A[p][0][1]), "+v"(A[p][1][0]), "+v"(A[p][1][1]), "+v"(A[p][2][0]), "+v"(A[p][2][1]),
                 "+v"(A[p][3][0]), "+v"(A[p][3][1]), "+v"(A[p][4][0]), "+v"(A[p][4][1]), "+v"(A[p][5][0]), "+v"(A[p][5][1]), "+v"(A[p][6][0]), "+v"(A[p][6][1]),
                 "+v"(A[p][7][0]), "+v"(A[p][7][1]), "+v"(B[p][0]), "+v"(B[p][1]), "+v"(B[p][2]), "+v"(B[p][3]), "+v"(B[p][4]), "+v"(B[p][5]),
                 "+v"(B[p][6]), "+v"(B[p][7]) :: "memory");
    SB();
  };
  auto mm = [&](int p) {
#pragma unroll
    for (int n = 0; n < 36; ++n) {
      const int t = TT[n], a = AA[n];
      const u32x4 av = {A[p][a][0][0], A[p][a][0][1], A[p][a][1][0], A[p][a][1][1]};
      asm volatile("v_mfma_f32_16x16x32_bf16 %0, %1, %2, %0" : "+v"(ac[t]) : "v"(av), "v"(B[p][t - a]));
      if (n < 24) ld1(p ^ 1, n);
      SB();
    }
    land(p ^ 1);
  };
#pragma unroll
  for (int t = 0; t < 8; ++t) ac[t] = (f32x4){0.f, 0.f, 0.f, 0.f};
#pragma unroll
  for (int n = 0; n < 24; ++n) ld1(0, n);
  land(0);
#pragma unroll 1
  for (int r = 0; r < (XE & 1 ? 0 : 8); r += 2) {
    next(-64);
    mm(0);
    next(r < 6 ? -64 : 0);
    mm(1);
  }
  asm volatile("s_nop 15" : "+v"(ac[0]), "+v"(ac[1]), "+v"(ac[2]), "+v"(ac[3]), "+v"(ac[4]), "+v"(ac[5]), "+v"(ac[6]), "+v"(ac[7]));
}

#define GTB 1
#define PMAP 1
#define GNL 0
#define PSL "1"
#define LP3 3
#define HPL 1
#define HSC ""
#define XSRC hf
#define CDR 0
#define SPR 1
#define DLY -1
#define PSLN 2
#define NUP 2
#define SGS 13
#define CVT 4u
#define BARP 0
#define XNS S0
#define CLP 1
#define NRP 0
#define CVW 0
#define ROT 0
#define XAM 7
#define XBM 15
#define YMP 3
#define YEX 1
#define ANT ""
#define BNT ""
#define XAS 128
#define TBL 1
#define PW0 4
#define WPF 0
#define ZSL 0
#define ZS2 0
#define PPF 4
#define WTA 16
#define NW1 10
#define NW0 0
constexpr int M_FLAG = 0;
constexpr int M_RT = 128;
constexpr int M_MT = M_RT + 8192;
constexpr int M_CV = M_MT + 4096;
constexpr int M_RING = M_CV + 3 * CV_BUF;
constexpr int P3_C = 128;
constexpr int P3_G = P3_C + 72 * 1024;
constexpr int P3_Y = M_RING + 4 * 24576;
constexpr int M_LDS = P3_Y + 8 * 1536;
static_assert(P3_G + 68 * 1024 <= P3_Y && M_LDS <= 163840 && M_RING <= P3_G, "LDS map");
#define GAS __attribute__((address_space(1)))
__device__ __forceinline__ void flag_publish(unsigned* f) { __hip_atomic_store((GAS unsigned*)f, 1u, __ATOMIC_RELAXED, __HIP_MEMORY_SCOPE_AGENT); }
__device__ __forceinline__ unsigned umx(unsigned a, unsigned b) { return a > b ? a : b; }
__device__ __forceinline__ unsigned max4(const u32x4& v, unsigned m) { return umx(umx(umx(m, v[0]), v[1]), umx(v[2], v[3])); }
__device__ __forceinline__ unsigned nohole(unsigned a) { return a < HOLE - 1 ? a : HOLE - 1; }

__device__ __forceinline__ void row_stat(const float (&mj)[4], const float (&qj)[4], float& mean, float& r) {
  mean = 0.25f * ((mj[0] + mj[1]) + (mj[2] + mj[3]));
  const float d0 = mj[0] - mean, d1 = mj[1] - mean, d2 = mj[2] - mean, d3 = mj[3] - mean;
  const float m2 = (qj[0] + qj[1]) + (qj[2] + qj[3]) + 192.0f * ((d0 * d0 + d1 * d1) + (d2 * d2 + d3 * d3));
  const float vbf = rbf(m2 * (1.0f / 768.0f));
  r = __builtin_amdgcn_rcpf(rbf(__builtin_amdgcn_sqrtf(rbf(vbf + 1e-5f))));
}
struct NormLd { u32x3 z[6]; u32x2 x[3]; f32x2 p[4]; };
__device__ __forceinline__ void norm_load(NormLd& n, const u16* z2f, const float2* st2, int ks, int cb, int lane) {
#pragma unroll
  for (int j = 0; j < 4; ++j) n.p[j] = *(const f32x2*)(st2 + j * S_ + ks * 32 + (lane >> 4) * 8 + (lane & 7));
#pragma unroll
  for (int j = 0; j < 6; ++j) n.z[j] = *(const u32x3*)((const char*)z2f + (ks * 48 + cb * 6 + j) * 1024 + lane * 16);
#pragma unroll
  for (int j = 0; j < 3; ++j) n.x[j] = *(const u32x2*)((const char*)z2f + Z1X + (ks * 24 + cb * 3 + j) * 512 + lane * 8);
  SB();
}
template <int E> __device__ __forceinline__ f32x2 bc2(float v) { return (f32x2){dppf<0x150 + 2 * E>(v), dppf<0x151 + 2 * E>(v)}; }
__device__ __forceinline__ void norm_store(const NormLd& n, u16* z2f, int ks, int cb, int lane) {
  const float mj[4] = {n.p[0][0], n.p[1][0], n.p[2][0], n.p[3][0]}, qj[4] = {n.p[0][1], n.p[1][1], n.p[2][1], n.p[3][1]};
  float mean, r;
  row_stat(mj, qj, mean, r);
  mean = rbf(mean);
  const f32x2 me[4] = {bc2<0>(mean), bc2<1>(mean), bc2<2>(mean), bc2<3>(mean)}, re[4] = {bc2<0>(r), bc2<1>(r), bc2<2>(r), bc2<3>(r)};
  const auto zrs = __builtin_amdgcn_make_buffer_rsrc(z2f, 0, S_ * FH_ * 2, 0x00020000);
#pragma unroll
  for (int j = 0; j < 6; ++j) {
    u32x4 o;
#pragma unroll
    for (int e = 0; e < 4; ++e) o[e] = pk2((unpk(e < 3 ? n.z[j][e < 3 ? e : 0] : n.x[j >> 1][j & 1]) - me[e]) * re[e]);
    o[3] = nohole(o[3]);
    __builtin_amdgcn_raw_buffer_store_b128(o, zrs, (ks * 48 + cb * 6 + j) * 1024 + lane * 16, 0, WTA);
  }
}
__global__ __launch_bounds__(64) void k_n(u16* z2f, const float2* __restrict__ st2) {
  const int ks = blockIdx.x >> 3;
  if ((ks & 15) < S0) return;
  NormLd n;
  norm_load(n, z2f, st2, ks, blockIdx.x & 7, threadIdx.x);
  norm_store(n, z2f, ks, blockIdx.x & 7, threadIdx.x);
}

__global__ __launch_bounds__(512) void k_m(
    const u16* __restrict__ w_sp, const u16* __restrict__ b_sp, u16* hf,
    u16* z2f, const float2* __restrict__ st2, u16* yT,
    const u16* __restrict__ ssm_k, const u16* __restrict__ ssm_D, const u16* __restrict__ woF,
    const u16* __restrict__ x, const float2* __restrict__ xst, u16* __restrict__ out) {
  extern __shared__ __attribute__((aligned(16))) char smem[];
  const int tid = threadIdx.x, lane = tid & 63, wave = __builtin_amdgcn_readfirstlane(tid >> 6);
  const int b = blockIdx.x;
  const int xcd = b & 7, idx = b >> 3;
  const int rb = 8 * (xcd >> 1) + (idx >> 2), cb = 4 * (xcd & 1) + (idx & 3);
  const int pw = (wave + ROT * cb) & 7;
  const int t0 = rb * 64;
  const int g4 = lane >> 4, l15 = lane & 15;
  STAMP(2, 0);
  RT(1, 0);
  AB_DECL;
  AB(0);
  const unsigned flg = lds_addr(smem) + M_FLAG;
  bf16x8 Wq[15];
  const char* wg = (const char*)woF + (size_t)(pw * 120 + cb * 5) * 1024;
  const int mt = wave & 3, kh = wave >> 2, d0 = cb * 80;
  const int row = t0 + mt * 16 + l15;
  const size_t xo = (size_t)row * D_ + d0;
  u32x2 xv[3], st;
  const auto hrs = __builtin_amdgcn_make_buffer_rsrc(hf, 0, S_ * FH_ * 2, 0x00020000);
  auto ldP1 = [&](int n) {
    if (n < 15)
      asm volatile("global_load_dwordx4 %0, %1, %2 offset:%3" : "=&v"(Wq[n < 15 ? n : 0]) : "v"((unsigned)(lane * 16)), "s"(wg + (n / 5) * 40960 + 2048), "i"((n % 5) * 1024 - 2048) : "memory");
    else if (n < 18) {
      const int q = n < 18 ? n - 15 : 0;
      asm volatile("global_load_dwordx2 %0, %1, %2 offset:%3" : "=&v"(xv[q]) : "v"((unsigned)(l15 * D_ * 2 + 8 * g4)),
                   "s"(x + (size_t)(t0 + mt * 16) * D_ + d0 + (q < 2 ? 32 * kh : 64)), "i"(q == 1 ? 32 : 0) : "memory");
    } else
      asm volatile("global_load_dwordx2 %0, %1, %2" : "=&v"(st) : "v"((unsigned)(l15 * 8)), "s"(xst + t0 + mt * 16) : "memory");
  };
  int tmo = 1 << 16;
  u32x4 yv[2];
  auto yoff = [&](int n) {
    const int c = n ? 64 + (lane & 31) : lane, r = c >> 1;
    return (unsigned)(((r < 32 ? 32 * kh + r : 32 + r) * S_ + 8 * (c & 1)) * 2);
  };
  const u16* const yb = yT + (size_t)d0 * S_ + t0 + 16 * mt;
  auto ldy0 = [&]() {
#pragma unroll
    for (int n = 0; n < 2; ++n) asm volatile("global_load_dwordx4 %0, %1, %2 " HSC : "=&v"(yv[n]) : "v"(yoff(n)), "s"(yb) : "memory");
  };
  auto signal1 = [&](unsigned a) {
    const unsigned one = 1u;
    asm volatile("s_mov_b64 exec, 1\n\tds_add_u32 %0, %1\n\ts_mov_b64 exec, -1" :: "v"(a), "v"(one) : "memory");
  };
  auto drain = [&](int nc) {
    SB();
    asm volatile("s_waitcnt vmcnt(%21)"  : "+v"(Wq[0]), "+v"(Wq[1]), "+v"(Wq[2]), "+v"(Wq[3]), "+v"(Wq[4]), "+v"(Wq[5]), "+v"(Wq[6]), "+v"(Wq[7]),
                 "+v"(Wq[8]), "+v"(Wq[9]), "+v"(Wq[10]), "+v"(Wq[11]), "+v"(Wq[12]), "+v"(Wq[13]), "+v"(Wq[14]),
                 "+v"(xv[0]), "+v"(xv[1]), "+v"(xv[2]), "+v"(st), "+v"(yv[0]), "+v"(yv[1]) : "i"(nc) : "memory");
  };
  auto drain1 = [&](int nc) {
    SB();
#pragma unroll
    for (int n = 0; n < NW1; ++n) asm volatile("s_waitcnt vmcnt(%1)" : "+v"(Wq[n]) : "i"(nc) : "memory");
  };
  const int cw = wave & 3;
  const unsigned pfo = (unsigned)((lane >> 4) * 4096 + ((lane >> 2) & 3) * 1024 + (4 + (lane & 3)) * 128);
  f32x4 sp[4][2];
  auto ldst = [&](int q) {
#pragma unroll
    for (int j = 0; j < 4; ++j)
      asm volatile("global_load_dwordx4 %0, %1, %2 offset:%3" : "=&v"(sp[j][q]) : "v"((unsigned)(lane * 16)), "s"(st2 + j * S_ + cw * 512), "i"(q * 1024) : "memory");
    SB();
  };
  auto table = [&](int q, int nv) {
    asm volatile("s_waitcnt vmcnt(%4)" : "+v"(sp[0][q]), "+v"(sp[1][q]), "+v"(sp[2][q]), "+v"(sp[3][q]) : "i"(nv) : "memory");
    float mv[2], rv[2];
#pragma unroll
    for (int h = 0; h < 2; ++h) {
      const float mj[4] = {sp[0][q][2 * h], sp[1][q][2 * h], sp[2][q][2 * h], sp[3][q][2 * h]};
      const float qj[4] = {sp[0][q][2 * h + 1], sp[1][q][2 * h + 1], sp[2][q][2 * h + 1], sp[3][q][2 * h + 1]};
      row_stat(mj, qj, mv[h], rv[h]);
    }
    *(f32x2*)(smem + M_RT + cw * 2048 + q * 512 + lane * 8) = (f32x2){rv[0], rv[1]};
    *(unsigned*)(smem + M_MT + cw * 1024 + q * 256 + lane * 4) = pack2(mv[0], mv[1]);
    if (!GTB || q) flag_signal(flg + 16 + cw * 4, lane);
    SB();
  };
  if (wave >= 4) {
    TS_DECL;
    auto pre = [&]() {
      ldst(0);
      if (TBL) ldst(1);
      __builtin_amdgcn_s_barrier();
      SB();
      if (!TBL) ldst(1);
    };
    TS(0);
    if (cw < (b < 128 ? 3 : 2)) {
      if (!GTB) pre();
      const int d = cw < 2 ? 2 * b + cw : 512 + b;
      char* cb_ = smem + M_CV + cw * CV_BUF;
      u32x4 hc[4], uc[4];
      unsigned hp[4], ux[4];
      float Dv;
      auto ldc = [&]() {
#pragma unroll
        for (int k = 0; k < 4; ++k) {
          const int a = k * 64 + lane;
          hc[k] = *(const u32x4*)(ssm_k + (size_t)d * S_ + a * 8);
          hp[k] = *(const unsigned*)(ssm_k + (size_t)d * S_ + max(a * 8 - 2, 0));
          uc[k] = *(const u32x4*)(yT + (size_t)d * S_ + a * 8);
          ux[k] = *(const unsigned*)((const char*)yT + KVX + (d >> 1) * 2048 + a * 8 + (d & 1) * 4);
        }
        Dv = bf2f(ssm_D[d]);
        SB();
      };
      ldst(1);
      if (GTB == 1) ldc();
      ABS(1);
      __builtin_amdgcn_s_barrier();
      SB();
      ABS(2);
      if (GTB == 2) ldc();
      ABS(3);
      table(1, 16);
      ABS(4);
      TS(1); AB(1);
      if (lane < 36) *(u32x4*)(cb_ + (lane < 32 ? lane * 16 : CV_C + ((lane >> 1) & 1) * CV_CS + 4096 + (lane & 1) * 16)) = (u32x4){0u, 0u, 0u, 0u};
#pragma unroll
      for (int k = 0; k < 4; ++k) {
        const int a = k * 64 + lane;
        const unsigned w[5] = {a ? hp[k] : 0u, hc[k][0], hc[k][1], hc[k][2], hc[k][3]};
        u32x4 sw, mx;
#pragma unroll
        for (int i = 0; i < 4; ++i) {
          sw[i] = (w[4 - i] >> 16) | (w[4 - i] << 16);
          mx[i] = (w[4 - i] & 0xFFFFu) | (w[3 - i] & 0xFFFF0000u);
        }
        char* cp = cb_ + CV_C + (2040 - 8 * a) * 2;
        *(u32x4*)(cp) = sw;
        *(u32x4*)(cp + CV_CS) = mx;
        *(u32x4*)(cb_ + 512 + a * 16) = (u32x4){uc[k][0], uc[k][1], uc[k][2], ux[k]};
      }
      TS(2); AB(2); ABS(5);
      f32x4 ac[8];
      conv_core(lds_addr(cb_), lane, ac);
      AB(1);
      const auto yrs = __builtin_amdgcn_make_buffer_rsrc(yT, 0, D_ * S_ * 2, 0x00020000);
      u32x2 kvp[8];
#pragma unroll
      for (int t = 0; t < 8; ++t) kvp[t] = *(const u32x2*)(cb_ + 512 + (256 * t + 16 * l15 + 4 * g4) * 2);
#pragma unroll
      for (int k = 0; k < 4; ++k) {
        u32x2 ov[2];
#pragma unroll
        for (int t = 0; t < 2; ++t)
#pragma unroll
          for (int h = 0; h < 2; ++h) {
            const unsigned cv = pack2(ac[2 * k + t][2 * h], ac[2 * k + t][2 * h + 1]);
            const unsigned kd = pack2(lo16(kvp[2 * k + t][h]) * Dv, hi16(kvp[2 * k + t][h]) * Dv);
            ov[t][h] = pack2(lo16(cv) + lo16(kd), hi16(cv) + hi16(kd));
          }
        const auto s0 = __builtin_amdgcn_permlane16_swap(ov[0][0], ov[1][0], false, false);
        const auto s1 = __builtin_amdgcn_permlane16_swap(ov[0][1], ov[1][1], false, false);
        const u32x4 o4 = {s0[0], s1[0], s0[1], nohole(s1[1])};
        __builtin_amdgcn_raw_buffer_store_b128(o4, yrs, (d * S_ + 256 * (2 * k + (g4 & 1)) + 16 * l15 + 8 * (g4 >> 1)) * 2, 0, WTA);
      }
    } else {
      const int ks = 2 * (rb & 24) + (cw == 3 ? 8 : 0) + (rb & 7);
      NormLd nl;
      ldst(1);
      if (GNL) norm_load(nl, z2f, st2, ks, cb, lane);
      SB();
      __builtin_amdgcn_s_barrier();
      SB();
      if (!GNL) norm_load(nl, z2f, st2, ks, cb, lane);
      unsigned pf = 0;
      auto pfw = [&]() {
#pragma unroll
        for (int n = 0; n < 4; ++n)
          asm volatile("global_load_dword %0, %1, %2" : "+v"(pf) : "v"(pfo), "s"(w_sp + (size_t)(t0 + (idx & 3) * 16 + n * 4) * S_) : "memory");
        SB();
      };
      if (PPF == 1) pfw();
      if (!GTB && TBL != 2) table(0, 17);
      if (PPF == 2) pfw();
      if (NRP == 0) table(1, 13);
      if (PPF == 3) pfw();
      AB(1);
      if (out && (ks & 15) >= S0) norm_store(nl, z2f, ks, cb, lane);
      SB();
      if (NRP == 1) table(1, 0);
      if (PPF == 4 && cw == 3) pfw();
      __builtin_amdgcn_s_waitcnt(0x0F70);
      asm volatile("" : "+v"(pf));
      AB(2);
    }
    SB();
    TS(3); TS_OUT(2, 8, 4, 4); AB(3);
    if (!out) { RT(1, 1); return; }
    WAIT_LGKM0();
    flag_signal(flg + 40, lane);
    flag_wait(flg + 44, CVT);
    AB(4);
#pragma unroll
    for (int n = 0; n < NW1; ++n) ldP1(n);
    drain1(0);
  } else {
  if (tid < 32) *(unsigned*)(smem + M_FLAG + tid * 4) = 0u;
  const u16* Ab = w_sp + (size_t)t0 * S_ + wave * 512;
  const char* Bs = (const char*)z2f + (size_t)(wave * 16 * 48 + cb * 6) * 1024 + 2048;
  const unsigned boff = (unsigned)(lane * 16);
  char* ring = smem + M_RING + wave * 24576;
  const unsigned ringl = __builtin_amdgcn_readfirstlane(lds_addr(ring));
  const unsigned aoff = (unsigned)((lane >> 3) * 4096 + (((lane & 7) ^ (lane >> 3)) << 4));
  bf16x8 Bf[3][6];
  TS_DECL;
  auto ldA1 = [&](auto dc, int i) {
    constexpr int d = decltype(dc)::value;
    const unsigned ao = (XE & 4) && d >= 2 ? aoff & 0x70u : aoff, rl = ringl;
    const char* ab = (const char*)Ab + ((wave + 1 + (i >> 1)) & 3) * 65536;
    asm volatile("s_mov_b32 m0, %2\n\ts_nop 0\n\tglobal_load_lds_dwordx4 %0, %1 " ANT
                 :: "v"(ao), "s"(ab + ((i & 1) * 32768 + (d & XAM) * XAS)), "s"(rl + (d % 3) * 8192 + i * 1024) : "memory");
  };
  auto ldA = [&](auto dc) {
#pragma unroll
    for (int i = 0; i < 8; ++i) ldA1(dc, i);
  };
  auto ldB1 = [&](auto sc, int j) {
    constexpr int s = decltype(sc)::value;
    bf16x8& B = Bf[s % 3][j];
    const unsigned bo = (XE & 2) && s >= 3 ? boff & 0x70u : boff;
    const char* bs = Bs + (size_t)(s & XBM) * 49152;
    asm volatile("global_load_dwordx4 %0, %1, %2 offset:%3 " BNT : "=&v"(B) : "v"(bo), "s"(bs), "i"(j * 1024 - 2048) : "memory");
  };
  auto ldB = [&](auto sc) {
#pragma unroll
    for (int j = 0; j < 6; ++j) ldB1(sc, j);
  };
  if (GTB) ldst(0);
  ldA(IC(0)); ldB(IC(0));
  if (BARP == 0) RAW_BARRIER();
  if (BARP == 3) table(0, 14);
  ldB(IC(1));
  if (BARP == 4) table(0, 20);
  ldA(IC(1));
  unsigned pfg = 0;
  if (PPF == 6)
    asm volatile("global_load_dword %0, %1, %2" : "=&v"(pfg) : "v"(pfo), "s"(w_sp + (size_t)(t0 + (idx & 3) * 16 + wave * 4) * S_) : "memory");
  if (BARP == 1) RAW_BARRIER();
  TS(6);
  if (GTB) { if (BARP < 3) table(0, 28 + (PPF == 6)); }
  else flag_wait_asm(flg + 16 + wave * 4, 1u);
  if (BARP >= 2) RAW_BARRIER();
  f32x4 acc[4][6], cacc[4];
  const unsigned rta = lds_addr(smem) + M_RT + (wave * 512 + g4 * 8) * 4;
  const unsigned mta = lds_addr(smem) + M_MT + (wave * 512 + g4 * 8) * 2;
  unsigned ara[2];
#pragma unroll
  for (int ks = 0; ks < 2; ++ks) ara[ks] = lds_addr(ring) + l15 * 128 + (((ks * 4 + g4) ^ (l15 & 7)) << 4);
  u32x4 af[4];
  f32x4 rT[2][2]; u32x4 mT[2];
  auto rdA = [&](auto sc, int i) {
    constexpr int s = decltype(sc)::value;
    u32x4& o = af[i];
    const unsigned ad = ara[s & 1];
    asm volatile("ds_read_b128 %0, %1 offset:%2" : "=&v"(o) : "v"(ad), "i"(((s >> 1) % 3) * 8192 + i * 2048) : "memory");
  };
  auto rdT = [&](auto sc) {
    constexpr int s = decltype(sc)::value;
    f32x4 &o0 = rT[s & 1][0], &o1 = rT[s & 1][1]; u32x4& om = mT[s & 1];
    const unsigned ra = rta, ma = mta;
    asm volatile("ds_read_b128 %0, %1 offset:%2" : "=&v"(o0) : "v"(ra), "i"(s * 128) : "memory");
    asm volatile("ds_read_b128 %0, %1 offset:%2" : "=&v"(o1) : "v"(ra), "i"(s * 128 + 16) : "memory");
    asm volatile("ds_read_b128 %0, %1 offset:%2" : "=&v"(om) : "v"(ma), "i"(s * 64) : "memory");
  };
  constexpr int NSC = 20, NPM = 3;
  float tl, th;
  auto scop = [&](int i, int p, int n) {
    const int e = n / 5, k = n % 5;
    if (k == 0) tl = lo16(af[i][e]);
    else if (k == 1) th = hi16(af[i][e]);
    else if (k == 2) tl = __builtin_fmaf(tl, rT[p][e >> 1][(e & 1) * 2], 0.0f);
    else if (k == 3) th *= rT[p][e >> 1][(e & 1) * 2 + 1];
    else af[i][e] = pack2(tl, th);
  };
  TS(7);
  asm volatile("s_waitcnt vmcnt(%0) lgkmcnt(0)" :: "n"(20 + (PPF == 6)) : "memory");
  rdT(IC(0));
#pragma unroll
  for (int i = 0; i < 4; ++i) rdA(IC(0), i);
  asm volatile("s_waitcnt lgkmcnt(3)" : "+v"(af[0]), "+v"(rT[0][0]), "+v"(rT[0][1]), "+v"(mT[0]) :: "memory");
#pragma unroll
  for (int n = 0; n < NSC; ++n) scop(0, 0, n);
  SB();
  const unsigned eo = (unsigned)((l15 + 16 * (2 * (g4 & 1) + (g4 >> 1))) * 16);
  const size_t eb = (size_t)((rb * 4 + wave) * 24 + cb * 3) * 1024;
  u32x3 zv[3];
  unsigned zx[3], bsv;
  auto ldZ = [&]() {
#pragma unroll
    for (int q = 0; q < 3; ++q)
      asm volatile("global_load_dwordx3 %0, %1, %2 offset:%3" : "=&v"(zv[q]) : "v"(eo), "s"((const char*)hf + eb), "i"(q * 1024) : "memory");
#pragma unroll
    for (int q = 0; q < 3; ++q)
      asm volatile("global_load_dword %0, %1, %2 offset:%3" : "=&v"(zx[q]) : "v"(eo >> 1),
                   "s"((const char*)hf + Z1X + ((rb * 2 + (wave >> 1)) * 24 + cb * 3) * 512 + (wave & 1) * 4), "i"(q * 512) : "memory");
    asm volatile("global_load_ushort %0, %1, %2" : "=&v"(bsv) : "v"((unsigned)(l15 * 2)), "s"(b_sp + t0 + wave * 16) : "memory");
  };
  const unsigned xa = ringl + lane * 16, xc = ringl + lane * 4;
  auto step = [&](auto sc) {
    constexpr int s = decltype(sc)::value, sl = s % 3, p = s & 1;
    constexpr int NV = s == 0 ? 14 + (PPF == 6) : (s == 2 ? 28 : (s < 12 ? 20 : (s == 12 ? 12 : (LP3 ? 19 - 6 * (s - 13) : 19))));
    constexpr int DA = s == 0 ? 2 : (((s & 1) && (s + 5) / 2 < 8) ? (s + 5) / 2 : -1);
    asm volatile("s_waitcnt vmcnt(%7) lgkmcnt(2)\n\ts_nop 1"
                 : "+v"(Bf[sl][0]), "+v"(Bf[sl][1]), "+v"(Bf[sl][2]), "+v"(Bf[sl][3]), "+v"(Bf[sl][4]), "+v"(Bf[sl][5]), "+v"(af[1]) : "n"(NV) : "memory");
    if constexpr (s == 1) asm volatile("" : "+v"(pfg));
    if constexpr (s == 3) flag_wait_asm(flg + 16 + wave * 4, GTB ? 1u : 2u);
    if constexpr (s >= S0) {
      auto d3 = [&](int j) { return __builtin_bit_cast(u32x4, Bf[sl][j])[3]; };
      auto bad = [&]() { return __builtin_amdgcn_ballot_w64(umx(umx(umx(d3(0), d3(1)), d3(2)), umx(umx(d3(3), d3(4)), d3(5))) == HOLE) != 0; };
      if (__builtin_expect(bad(), 0)) {
        do {
          --tmo;
          asm volatile("s_sleep 2\n\tglobal_load_dwordx4 %0, %6, %7 offset:-2048 sc1\n\tglobal_load_dwordx4 %1, %6, %7 offset:-1024 sc1\n\t"
                       "global_load_dwordx4 %2, %6, %7 sc1\n\tglobal_load_dwordx4 %3, %6, %7 offset:1024 sc1\n\t"
                       "global_load_dwordx4 %4, %6, %7 offset:2048 sc1\n\tglobal_load_dwordx4 %5, %6, %7 offset:3072 sc1\n\ts_waitcnt vmcnt(0)"
                       : "+v"(Bf[sl][0]), "+v"(Bf[sl][1]), "+v"(Bf[sl][2]), "+v"(Bf[sl][3]), "+v"(Bf[sl][4]), "+v"(Bf[sl][5])
                       : "v"(boff), "s"(Bs + (size_t)s * 49152) : "memory");
        } while (bad() && tmo > 0);
      }
    }
    if constexpr (s == SGS) signal1(flg + 44);
    if constexpr (s < 15) ABS(s + 1);
    SB();
#pragma unroll
    for (int i = 0; i < 4; ++i) {
      if (i == 1) asm volatile("s_waitcnt lgkmcnt(%2)" : "+v"(af[2]), "+v"(af[3]) : "n"(s < 15 ? (s + 1 < S0 ? 4 : 1) : 0) : "memory");
      if (i == 3 && s + 1 < S0) asm volatile("s_waitcnt lgkmcnt(2)" : "+v"(af[0]), "+v"(rT[p ^ 1][0]), "+v"(rT[p ^ 1][1]), "+v"(mT[p ^ 1]) :: "memory");
      if (i == 3 && s + 1 >= S0 && s < 15) asm volatile("s_waitcnt lgkmcnt(2)" : "+v"(af[0]) :: "memory");
      const bf16x8 av = __builtin_bit_cast(bf16x8, af[i]);
#pragma unroll
      for (int j = 0; j < 7; ++j) {
        f32x4& ac = j < 6 ? acc[i][j < 6 ? j : 0] : cacc[i];
        const bf16x8 bb = j < 6 ? Bf[sl][j < 6 ? j : 0] : __builtin_bit_cast(bf16x8, mT[p]);
        if constexpr (s == 0) asm volatile("v_mfma_f32_16x16x32_bf16 %0, %1, %2, 0" : "=&v"(ac) : "v"(bb), "v"(av));
        else if (j < 6 || s < XNS) asm volatile("v_mfma_f32_16x16x32_bf16 %0, %1, %2, %0" : "+v"(ac) : "v"(bb), "v"(av));
        SB();
#pragma unroll
        for (int n = NPM * j; n < NPM * j + NPM && n < NSC; ++n) {
          if (i < 3) { if (s < XNS) scop(i + 1, p, n); }
          else if (s < 15 && s + 1 < XNS) scop(0, p ^ 1, n);
          SB();
        }
        if constexpr (s == 0) { if (i == 0 && j < 6) ldB1(IC(2), j); }
        if constexpr (DA >= 0) { if ((i == 1 || i == 2) && j < 4) ldA1(IC(DA < 0 ? 0 : DA), (i - 1) * 4 + j); }
        if constexpr (s + 3 < 16) { if (i == 3 && j < 6) ldB1(IC(s + 3), j); }
        if constexpr (!LP3 && (s == 13 || s == 14)) { if (i == 3 && j < 6) ldP1((s - 13) * 6 + j); }
        if constexpr (s == 15) {
          if (i > 0 && j > 0) asm volatile("ds_write_b128 %0, %1 offset:%2" :: "v"(xa), "v"(acc[i > 0 ? i - 1 : 0][j > 0 ? j - 1 : 0]), "i"((i - 1) * 6144 + (j - 1) * 1024) : "memory");
          if (!LP3) {
            if (i == 2 && j > 0) ldP1(11 + j);
            if (i == 3 && j == 1) ldP1(18);
            if (i == 3 && j == 2) ldy0();
          }
          if (LP3 == 2) { if (i >= 2 && j < 6) ldP1((i - 2) * 6 + j); }
          if (LP3 == 3) { if (i >= 2 && j < 6 && (i - 2) * 6 + j < NW0) ldP1((i - 2) * 6 + j); }
        }
        SB();
      }
      if constexpr (s == 15) {
        if (i > 0) {
          asm volatile("ds_write_b32 %0, %1 offset:%2" :: "v"(xc), "v"(cacc[i > 0 ? i - 1 : 0][0]), "i"(18432 + (i - 1) * 256),
                       "v"(cacc[i > 0 ? i - 1 : 0]) : "memory");
          signal1(flg + ((wave + i) & 3) * 4);
        }
      }
      asm volatile("s_nop 1");
      if constexpr (s < 15) { if (i == 0 && s + 1 < S0) rdT(IC(s + 1)); rdA(IC(s + 1), i); }
      SB();
    }
    if constexpr (s == 12) ldZ();
    SB();
  };
  TS(0); AB(1);
  step(IC(0)); TS(1); step(IC(1)); step(IC(2)); step(IC(3)); TS(2); AB(3); step(IC(4)); step(IC(5)); step(IC(6)); step(IC(7)); TS(3);
  step(IC(8)); step(IC(9)); step(IC(10)); step(IC(11)); TS(4); step(IC(12)); step(IC(13)); step(IC(14)); step(IC(15)); TS(5); AB(2);
  STAMP(2, 1);
  if (LP3 && LP3 != 3) {
#pragma unroll
    for (int n = LP3 == 2 ? 12 : 0; n < 19; ++n) ldP1(n);
    ldy0();
    SB();
  }
  if (LP3 == 3) {
#pragma unroll
    for (int n = NW0; n < NW1; ++n) ldP1(n);
    SB();
  }
  flag_wait_asm(flg + wave * 4, 3u);
  AB(4);
  f32x4 own[6];
  {
    f32x4 pr[3][6];
    float pc[3];
#pragma unroll
    for (int q = 0; q < 3; ++q) {
      const char* src = smem + M_RING + ((wave + 3 - q) & 3) * 24576;
      pc[q] = *(const float*)(src + 18432 + q * 256 + lane * 4);
#pragma unroll
      for (int j = 0; j < 6; ++j) pr[q][j] = *(const f32x4*)(src + q * 6144 + j * 1024 + lane * 16);
    }
    asm volatile("s_nop 7" : "+v"(acc[3][0]), "+v"(acc[3][1]), "+v"(acc[3][2]), "+v"(acc[3][3]), "+v"(acc[3][4]), "+v"(acc[3][5]), "+v"(cacc[3]));
    const float c = (cacc[3][0] + pc[0]) + (pc[1] + pc[2]);
#pragma unroll
    for (int j = 0; j < 6; ++j) own[j] = ((acc[3][j] - c) + pr[0][j]) + (pr[1][j] + pr[2][j]);
  }
  asm volatile("s_waitcnt vmcnt(%7)" : "+v"(zv[0]), "+v"(zv[1]), "+v"(zv[2]), "+v"(zx[0]), "+v"(zx[1]), "+v"(zx[2]), "+v"(bsv) : "i"(LP3 == 3 ? NW1 : 21) : "memory");
  SB();
  {
    const float bs = lo16(bsv);
    unsigned pk[6][2];
#pragma unroll
    for (int j = 0; j < 6; ++j)
#pragma unroll
      for (int h = 0; h < 2; ++h) {
        const unsigned r0 = pack2(own[j][2 * h], own[j][2 * h + 1]);
        pk[j][h] = pack2(lo16(r0) + bs, hi16(r0) + bs);
      }
#pragma unroll
    for (int q = 0; q < 3; ++q) {
      const auto s0 = __builtin_amdgcn_permlane16_swap(pk[2 * q][0], pk[2 * q + 1][0], false, false);
      const auto s1 = __builtin_amdgcn_permlane16_swap(pk[2 * q][1], pk[2 * q + 1][1], false, false);
      u32x4 gv; gv[0] = s0[0]; gv[1] = s1[0]; gv[2] = s0[1]; gv[3] = s1[1];
      u32x4 o4;
#pragma unroll
      for (int e = 0; e < 4; ++e) {
        const unsigned z = e < 3 ? zv[q][e < 3 ? e : 0] : zx[q];
        o4[e] = pack2(lo16(z) * lo16(gv[e]), hi16(z) * hi16(gv[e]));
      }
      o4[3] = nohole(o4[3]);
      __builtin_amdgcn_raw_buffer_store_b128(o4, hrs, (int)eb + eo + q * 1024, 0, WTA);
    }
  }
  TS_OUT(1, 0, 8, 0); TS_OUT(1, 8, 8, 3); AB(5);
  drain1(3);
  if (!out) { RT(1, 1); return; }
  STAMP(2, 2);
  }
  TS_DECL;
  TS(0);
  unsigned hoff[4];
#pragma unroll
  for (int it = 0; it < 4; ++it) hoff[it] = (unsigned)(((rb * 4 + ((mt + 1 + it) & 3)) * 24 + 3 * pw) * 1024 + lane * 16);
  u32x4 Hq[4][3];
  auto ldy = [&]() {
#pragma unroll
    for (int n = 0; n < 2; ++n) asm volatile("global_load_dwordx4 %0, %1, %2 sc1" : "=&v"(yv[n]) : "v"(yoff(n)), "s"(yb) : "memory");
  };
  auto ldh = [&](int it) {
#pragma unroll
    for (int k = 0; k < 3; ++k)
      asm volatile("global_load_dwordx4 %0, %1, %2 offset:%3 sc1" : "=&v"(Hq[it][k]) : "v"(hoff[it]), "s"(XSRC), "i"(k * 1024) : "memory");
  };
  auto ldh1 = [&](int it, int k) {
    asm volatile("global_load_dwordx4 %0, %1, %2 offset:%3 " HSC : "=&v"(Hq[it][k]) : "v"(hoff[it]), "s"(XSRC), "i"(k * 1024) : "memory");
  };
  auto ldh0 = [&](int it) {
#pragma unroll
    for (int k = 0; k < 3; ++k) ldh1(it, k);
  };
  f32x2 xf[3][2], sv[3][2];
  auto ymath = [&]() {
    char* yi = smem + P3_Y + wave * 1536;
#pragma unroll
    for (int n = 0; n < 2; ++n) *(u32x4*)(yi + (n ? 64 + (lane & 31) : lane) * 16) = yv[n];
    const unsigned ya = lds_addr(yi) + (4 * g4 + (l15 >> 2)) * 32 + 8 * (lane & 3);
    s16x4 yr[3] = {tr_asm<0>(ya), tr_asm<512>(ya), tr_asm<1024>(ya)};
    asm volatile("s_waitcnt lgkmcnt(0)" : "+v"(yr[0]), "+v"(yr[1]), "+v"(yr[2]) :: "memory");
    const f32x2 mean = {__uint_as_float(st[0]), __uint_as_float(st[0])}, rd = {__uint_as_float(st[1]), __uint_as_float(st[1])};
#pragma unroll
    for (int q = 0; q < 3; ++q) {
      const u32x2 yw = __builtin_bit_cast(u32x2, yr[q]);
#pragma unroll
      for (int h = 0; h < 2; ++h) {
        xf[q][h] = unpk(xv[q][h]);
        sv[q][h] = ((xf[q][h] - mean) * rd) * unpk(yw[h]);
        asm volatile("" : "+v"(sv[q][h]), "+v"(xf[q][h]));
      }
    }
    SB();
  };
#pragma unroll
  for (int n = NW1; n < 19; ++n) ldP1(n);
  if (YMP == 5) ldy0();
  SB();
  if (wave >= 4) AB(5);
  asm volatile("s_waitcnt vmcnt(%0)" :: "i"(19 - NW1 + (YMP == 5 ? 2 : 0)) : "memory");
  bool ybad;
  AB(6);
  signal1(flg + 48 + 4 * kh);
  flag_wait_asm(flg + 48, 4u);
  TS(1); AB(7);
  if (SPR) __builtin_amdgcn_s_setprio(3);
  ldh0(0);
  if (SPR) __builtin_amdgcn_s_setprio(2);
  ldh0(1);
  if (SPR) __builtin_amdgcn_s_setprio(1);
  if (NUP > 2) ldh0(2);
  if (SPR) __builtin_amdgcn_s_setprio(0);
  if (NUP > 3) ldh0(3);
  if (YMP < 3) ldy0();
  SB();
  AB(8);
  TS(2); AB(9);
  flag_wait_asm(flg + 40, kh ? 4u : 0u);
  TS(3);
  char* const px = smem + (kh ? P3_C + mt * 18432 : P3_G + mt * 17408) + lane * 16;
  f32x4 ac3[5], o[3];
  auto comp = [&](auto itc) {
    constexpr int it = decltype(itc)::value;
    constexpr int NW = 3 * ((it + NUP < 4 ? it + NUP : 4) - it - 1) + (LP3 == 3 && YMP != 5 && (YMP < 3 ? it < 2 : it >= 2) ? 2 : 0);
    asm volatile("s_waitcnt vmcnt(%3)" : "+v"(Hq[it][0]), "+v"(Hq[it][1]), "+v"(Hq[it][2]) : "n"(NW) : "memory");
    if constexpr (LP3 == 3 && it == 0)
      asm volatile("" : "+v"(Wq[0]), "+v"(Wq[1]), "+v"(Wq[2]), "+v"(Wq[3]), "+v"(Wq[4]), "+v"(Wq[5]), "+v"(Wq[6]), "+v"(Wq[7]),
                   "+v"(Wq[8]), "+v"(Wq[9]), "+v"(Wq[10]), "+v"(Wq[11]), "+v"(Wq[12]), "+v"(Wq[13]), "+v"(Wq[14]),
                   "+v"(xv[0]), "+v"(xv[1]), "+v"(xv[2]), "+v"(st) :: "memory");
    auto bad = [&]() { return __builtin_amdgcn_ballot_w64(umx(umx(Hq[it][0][3], Hq[it][1][3]), Hq[it][2][3]) == HOLE) != 0; };
    if (__builtin_expect(bad(), 0)) {
      do {
        --tmo;
        asm volatile("s_sleep 2\n\tglobal_load_dwordx4 %0, %3, %4 sc1\n\tglobal_load_dwordx4 %1, %3, %4 offset:1024 sc1\n\t"
                     "global_load_dwordx4 %2, %3, %4 offset:2048 sc1\n\ts_waitcnt vmcnt(0)"
                     : "+v"(Hq[it][0]), "+v"(Hq[it][1]), "+v"(Hq[it][2]) : "v"(hoff[it]), "s"(hf) : "memory");
      } while (bad() && tmo > 0);
    }
    SB();
#pragma unroll
    for (int k = 0; k < 3; ++k) {
#pragma unroll
      for (int j = 0; j < 5; ++j) {
        ac3[j] = __builtin_amdgcn_mfma_f32_16x16x32_bf16(Wq[k * 5 + j], __builtin_bit_cast(bf16x8, Hq[it][k]), k ? ac3[j] : (f32x4){0.f, 0.f, 0.f, 0.f}, 0, 0, 0);
        if constexpr (it + NUP < 4) { if (k == 0 && j < 3) { SB(); ldh1(it + NUP < 4 ? it + NUP : 0, j); SB(); } }
        if constexpr (YMP >= 3 && YMP != 5 && it == 1) { if (k == 1 && j == 0) { SB(); ldy0(); SB(); } }
      }
    }
    SB();
    if constexpr (it < 3) {
#pragma unroll
      for (int j = 0; j < 5; ++j) *(f32x4*)(px + (it * 5 + j) * 1024) = ac3[j];
      signal1(flg + 64 + kh * 16 + ((mt + 1 + it) & 3) * 4);
    }
    SB();
  };
  auto gather = [&](auto cc) {
    constexpr int cv = decltype(cc)::value;
    flag_wait_asm(flg + 64 + cv * 16 + mt * 4, cv ? 5u : 3u);
#pragma unroll
    for (int it = 0; it < 3 + cv; ++it) {
      const int ms = (mt - 1 - it) & 3;
      const char* ps = smem + ((it < 3 ? cv : !kh) ? P3_C + ms * 18432 : P3_G + ms * 17408) + lane * 16;
      const char* p0 = ps + (it < 3 ? it * 5 + 2 * kh : 15) * 1024;
      const f32x4 a0 = *(const f32x4*)p0, a1 = *(const f32x4*)(p0 + 1024);
      if (!cv && it == 0) { o[0] = a0; o[1] = a1; } else { o[0] += a0; o[1] += a1; }
    }
    if (!kh) {
#pragma unroll
      for (int it = 0; it < 3 + cv; ++it) {
        const int ms = (mt - 1 - it) & 3;
        const char* ps = smem + ((it < 3 ? cv : 1) ? P3_C + ms * 18432 : P3_G + ms * 17408) + lane * 16;
        o[2] += *(const f32x4*)(ps + (it < 3 ? it * 5 + 4 : 17) * 1024);
      }
    }
    SB();
  };
  o[2] = (f32x4){0.f, 0.f, 0.f, 0.f};
  comp(IC(0)); AB(10); comp(IC(1));
  if (YMP == 5) {
    asm volatile("" : "+v"(yv[0]), "+v"(yv[1]) :: "memory");
    if (__builtin_amdgcn_ballot_w64(umx(yv[0][3], yv[1][3]) == HOLE) != 0) {
      asm volatile("global_load_dwordx4 %0, %2, %4 sc1\n\tglobal_load_dwordx4 %1, %3, %4 sc1" : "+v"(yv[0]), "+v"(yv[1]) : "v"(yoff(0)), "v"(yoff(1)), "s"(yb) : "memory");
    }
    SB();
  }
  if (YMP == 1) {
    asm volatile("s_waitcnt vmcnt(6)" : "+v"(yv[0]), "+v"(yv[1]) :: "memory");
    ybad = __builtin_amdgcn_ballot_w64(umx(yv[0][3], yv[1][3]) == HOLE) != 0;
    ymath();
  }
  comp(IC(2));
  if (YMP == 2) {
    asm volatile("" : "+v"(yv[0]), "+v"(yv[1]) :: "memory");
    ybad = __builtin_amdgcn_ballot_w64(umx(yv[0][3], yv[1][3]) == HOLE) != 0;
    ymath();
  }
  TS(4); AB(11);
  gather(IC(0));
  if (YMP == 4) {
    asm volatile("s_waitcnt vmcnt(0)" : "+v"(yv[0]), "+v"(yv[1]) :: "memory");
    ybad = __builtin_amdgcn_ballot_w64(umx(yv[0][3], yv[1][3]) == HOLE) != 0;
    ymath();
  }
  AB(12);
  comp(IC(3));
  TS(5); AB(13);
  {
    *(f32x4*)(px + 15 * 1024) = kh ? ac3[0] : ac3[2];
    *(f32x4*)(px + 16 * 1024) = kh ? ac3[1] : ac3[3];
    if (kh) *(f32x4*)(px + 17 * 1024) = ac3[4];
    signal1(flg + 80 + mt * 4);
    SB();
    o[0] += kh ? ac3[2] : ac3[0]; o[1] += kh ? ac3[3] : ac3[1]; o[2] += ac3[4];
    if (YMP == 3 || YMP == 5) {
      asm volatile("s_waitcnt vmcnt(0)" : "+v"(yv[0]), "+v"(yv[1]) :: "memory");
      ybad = __builtin_amdgcn_ballot_w64(umx(yv[0][3], yv[1][3]) == HOLE) != 0;
    }
    if (__builtin_expect(ybad, 0)) {
      do {
        --tmo;
        ldy();
        asm volatile("s_waitcnt vmcnt(0)" : "+v"(yv[0]), "+v"(yv[1]) :: "memory");
      } while (__builtin_amdgcn_ballot_w64(umx(yv[0][3], yv[1][3]) == HOLE) != 0 && tmo > 0);
      if (YMP != 3 && YMP != 5) ymath();
    }
    if (YMP == 3 || YMP == 5) ymath();
    TS(6); AB(14);
    gather(IC(1));
    auto fin = [&](const f32x4& o4, int q, unsigned (&pk)[2]) {
#pragma unroll
      for (int h = 0; h < 2; ++h) {
        const f32x2 o = {o4[2 * h], o4[2 * h + 1]};
        const f32x2 a = o * (f32x2){-1.4426950f, -1.4426950f};
        const f32x2 e = (f32x2){__builtin_amdgcn_exp2f(a[0]), __builtin_amdgcn_exp2f(a[1])} + (f32x2){1.0f, 1.0f};
        const f32x2 r = {__builtin_amdgcn_rcpf(e[0]), __builtin_amdgcn_rcpf(e[1])};
        pk[h] = pk2(__builtin_elementwise_fma(o * r, sv[q][h], xf[q][h]));
      }
    };
    unsigned pa[2], pb[2];
    fin(o[0], 0, pa);
    fin(o[1], 1, pb);
    const auto s0 = __builtin_amdgcn_permlane16_swap(pa[0], pb[0], false, false);
    const auto s1 = __builtin_amdgcn_permlane16_swap(pa[1], pb[1], false, false);
    *(u32x4*)(out + xo + 32 * kh + 16 * (g4 & 1) + 8 * (g4 >> 1)) = (u32x4){s0[0], s1[0], s0[1], s1[1]};
    if (!kh) {
      fin(o[2], 2, pa);
      *(u32x2*)(out + xo + 64 + 4 * g4) = (u32x2){pa[0], pa[1]};
    }
  }
  TS(7); AB(15);
  AB_OUT(16);
  RT(1, 1);
}

extern "C" __global__ void k_end() {}

#define X4 0
#define LA4 4
constexpr int G3_RED = 4 * 3072;
constexpr int G3_LDS = G3_RED + 4 * 15 * 1024;

__global__ __launch_bounds__(256) void k_l4(
    const u16* __restrict__ hf, const u16* __restrict__ woF, const u16* __restrict__ x,
    const float2* __restrict__ xst, const u16* __restrict__ yT, u16* __restrict__ out) {
  extern __shared__ __attribute__((aligned(16))) char smem[];
  const int tid = threadIdx.x, lane = tid & 63, wave = __builtin_amdgcn_readfirstlane(tid >> 6);
  const int b = blockIdx.x;
  const int xcd = b & 7, idx = b >> 3;
  const int rb = 8 * (xcd >> 1) + (idx >> 2), cb = 4 * (xcd & 1) + (idx & 3);
  const int t0 = rb * 64, d0 = cb * 80;
  const int g4 = lane >> 4, l15 = lane & 15;
  RT(2, 0);
  TS_DECL;
  TS(0);
  const unsigned boff16 = (unsigned)(lane * 16);
  const char* Hb = (const char*)hf + (size_t)(rb * 4 * 24 + wave * 6) * 1024;
  const char* Wb = (const char*)woF + (size_t)(wave * 6 * 40 + cb * 5) * 1024;
  u32x4 Hf[6][4], Wf[6][5];
  auto ldH = [&](int s, int k) { Hf[s][k] = *(const u32x4*)(Hb + (size_t)((wave + 1 + k) & 3) * 24576 + s * 1024 + boff16); };
  auto ldG = [&](int s) {
#pragma unroll
    for (int j = 0; j < 5; ++j) Wf[s][j] = *(const u32x4*)(Wb + (size_t)s * 40960 + j * 1024 + boff16);
    ldH(s, 0); ldH(s, 1);
    SB();
  };
  const unsigned ly = __builtin_amdgcn_readfirstlane(lds_addr(smem) + wave * 3072);
  {
    const char* yb = (const char*)(yT + (size_t)d0 * S_ + t0 + wave * 16);
#pragma unroll
    for (int n = 0; n < 3; ++n) {
      const int c = min(n * 64 + lane, 159);
      asm volatile("s_mov_b32 m0, %2\n\ts_nop 0\n\tglobal_load_lds_dwordx4 %0, %1"
                   :: "v"((unsigned)((c >> 1) * (S_ * 2) + (c & 1) * 16)), "s"(yb), "s"(ly + n * 1024) : "memory");
    }
  }
  SB();
  ldG(0); ldG(1);
  const int row = t0 + wave * 16 + l15;
  const size_t xo = (size_t)row * D_ + d0;
  u32x2 xv[5];
  const float2 st = xst[row];
#pragma unroll
  for (int j = 0; j < 5; ++j) xv[j] = *(const u32x2*)(x + xo + 16 * j + 4 * g4);
  SB();
  ldG(2); ldG(3);
  TS(1);
  f32x4 acc[4][5];
  auto mm = [&](int s, int k) {
#pragma unroll
    for (int j = 0; j < 5; ++j) {
      const f32x4 c = s ? acc[k][j] : (f32x4){0.f, 0.f, 0.f, 0.f};
      acc[k][j] = __builtin_amdgcn_mfma_f32_16x16x32_bf16(__builtin_bit_cast(bf16x8, Wf[s][j]), __builtin_bit_cast(bf16x8, Hf[s][k]), c, 0, 0, 0);
    }
    SB();
  };
  auto put = [&](int k, int j) {
    *(f32x4*)(smem + G3_RED + (((wave + 1 + k) & 3) * 3 + k) * 5120 + j * 1024 + lane * 16) = acc[k][j];
    SB();
  };
  f32x4 pr[3][5];
  auto get = [&](int q) {
#pragma unroll
    for (int j = 0; j < 5; ++j) pr[q][j] = *(const f32x4*)(smem + G3_RED + (wave * 3 + q) * 5120 + j * 1024 + lane * 16);
    SB();
  };
  auto ldN = [&](int n) {
    if (n < 6) ldG(n);
    else if (n < 8) {
#pragma unroll
      for (int s = 0; s < 6; ++s) ldH(s, n - 4);
      SB();
    }
  };
  mm(0, 0); mm(0, 1); ldN(LA4);
  s16x4 yr[5];
  {
    const unsigned ya = ly + (4 * g4 + (l15 >> 2)) * 32 + 8 * (lane & 3);
#pragma unroll
    for (int j = 0; j < 5; ++j)
      asm volatile("ds_read_b64_tr_b16 %0, %1 offset:%2" : "=&v"(yr[j]) : "v"(ya), "i"(j * 512) : "memory");
  }
  mm(1, 0); mm(1, 1); ldN(LA4 + 1);
  asm volatile("s_waitcnt lgkmcnt(0)" : "+v"(yr[0]), "+v"(yr[1]), "+v"(yr[2]), "+v"(yr[3]), "+v"(yr[4]) :: "memory");
  f32x2 xf[5][2], sv[5][2];
  {
    const f32x2 mean = {st.x, st.x}, rd = {st.y, st.y};
#pragma unroll
    for (int j = 0; j < 5; ++j) {
      const u32x2 yw = __builtin_bit_cast(u32x2, yr[j]);
#pragma unroll
      for (int h = 0; h < 2; ++h) {
        xf[j][h] = unpk(xv[j][h]);
        sv[j][h] = ((xf[j][h] - mean) * rd) * unpk(yw[h]);
      }
    }
    SB();
  }
  mm(2, 0); mm(2, 1); ldN(LA4 + 2);
  mm(3, 0); mm(3, 1); ldN(LA4 + 3);
  mm(4, 0); mm(4, 1);
  mm(5, 0); mm(5, 1);
  TS(2);
  mm(0, 2); put(0, 0); put(0, 1); put(0, 2);
  mm(1, 2); put(0, 3); put(0, 4); put(1, 0);
  mm(2, 2); put(1, 1); put(1, 2); put(1, 3);
  mm(3, 2); put(1, 4);
  mm(4, 2);
  mm(5, 2);
  TS(3);
  RAW_BARRIER();
  SB();
  get(0);
  mm(0, 3);
  put(2, 0); put(2, 1); put(2, 2); put(2, 3); put(2, 4);
  get(1);
  mm(1, 3);
  mm(2, 3);
  TS(4);
  RAW_BARRIER();
  SB();
  TS(5);
  get(2);
#pragma unroll
  for (int j = 0; j < 5; ++j) pr[0][j] += pr[1][j];
  SB();
  mm(3, 3);
  mm(4, 3);
#pragma unroll
  for (int j = 0; j < 5; ++j) pr[0][j] += pr[2][j];
  SB();
  mm(5, 3);
  TS(6);
  unsigned pk[5][2];
#pragma unroll
  for (int j = 0; j < 5; ++j) {
    const f32x4 o4 = acc[3][j] + pr[0][j];
#pragma unroll
    for (int h = 0; h < 2; ++h) {
      const f32x2 o = {o4[2 * h], o4[2 * h + 1]};
      const f32x2 a = o * (f32x2){-1.4426950f, -1.4426950f};
      const f32x2 e = (f32x2){__builtin_amdgcn_exp2f(a[0]), __builtin_amdgcn_exp2f(a[1])} + (f32x2){1.0f, 1.0f};
      const f32x2 r = {__builtin_amdgcn_rcpf(e[0]), __builtin_amdgcn_rcpf(e[1])};
      pk[j][h] = pk2(__builtin_elementwise_fma(o * r, sv[j][h], xf[j][h]));
    }
  }
  const int dq = 16 * (g4 & 1) + 8 * (g4 >> 1);
#pragma unroll
  for (int q = 0; q < 2; ++q) {
    const auto s0 = __builtin_amdgcn_permlane16_swap(pk[2 * q][0], pk[2 * q + 1][0], false, false);
    const auto s1 = __builtin_amdgcn_permlane16_swap(pk[2 * q][1], pk[2 * q + 1][1], false, false);
    u32x4 o4; o4[0] = s0[0]; o4[1] = s1[0]; o4[2] = s0[1]; o4[3] = s1[1];
    *(u32x4*)(out + xo + 32 * q + dq) = o4;
  }
  *(u32x2*)(out + xo + 64 + 4 * g4) = (u32x2){pk[4][0], pk[4][1]};
  TS(7);
  RT(2, 1);
}

extern "C" void kernel_launch(void* const* d_in, const int* in_sizes, int n_in,
                              void* d_out, int out_size, void* d_ws, size_t ws_size,
                              hipStream_t stream) {
  const u16* A_q = (const u16*)d_in[0];
  const u16* A_k = (const u16*)d_in[1];
  const u16* A_v = (const u16*)d_in[2];
  const u16* A_shift_k = (const u16*)d_in[3];
  const u16* A_ssm_k = (const u16*)d_in[4];
  const u16* A_ssm_D = (const u16*)d_in[5];
  const u16* B_w_in = (const u16*)d_in[7];
  const u16* B_w_sp = (const u16*)d_in[8];
  const u16* B_b_sp = (const u16*)d_in[9];
  const u16* B_w_out = (const u16*)d_in[10];
  u16* out = (u16*)d_out;

  char* ws = (char*)d_ws;
  size_t off = 0;
  auto carve = [&](size_t bytes) { char* p = ws + off; off += (bytes + 255) & ~(size_t)255; return p; };
  u16* z1 = (u16*)carve((size_t)S_ * FH_ * 2 * 5 / 4);
  u16* z2 = (u16*)carve((size_t)S_ * FH_ * 2 * 5 / 4);
  float2* st2 = (float2*)carve((size_t)4 * S_ * 8);
  float2* xst = (float2*)carve((size_t)S_ * 8);
  u16* kvT = (u16*)carve((size_t)D_ * S_ * 2 * 5 / 4);
  u16* woF = (u16*)carve((size_t)FH_ * D_ * 2);

  int dev = 0, cus = 0;
  (void)hipGetDevice(&dev);
  (void)hipDeviceGetAttribute(&cus, hipDeviceAttributeMultiprocessorCount, dev);
  const bool fused = cus >= 256;
  (void)hipFuncSetAttribute((const void*)k_l1, hipFuncAttributeMaxDynamicSharedMemorySize, L1_LDS);
  (void)hipFuncSetAttribute((const void*)k_m, hipFuncAttributeMaxDynamicSharedMemorySize, M_LDS);
  k_p<<<256, 256, 0, stream>>>((const char*)B_w_sp, (const char*)A_k, (const char*)A_v, (const char*)A_ssm_k, (const char*)A_q, (const char*)B_w_in);
  k_l1<<<256, 768, L1_LDS, stream>>>(A_q, A_k, A_v, A_shift_k, B_w_in, z1, z2, st2, xst, kvT, A_ssm_k, B_w_out, B_w_sp, woF);
  if (!fused) k_n<<<512, 64, 0, stream>>>(z2, st2);
  k_m<<<256, 512, M_LDS, stream>>>(B_w_sp, B_b_sp, z1, z2, st2, kvT, A_ssm_k, A_ssm_D, woF, A_q, xst, fused ? out : nullptr);
  if (!fused) {
    (void)hipFuncSetAttribute((const void*)k_l4, hipFuncAttributeMaxDynamicSharedMemorySize, G3_LDS);
    k_l4<<<256, 256, G3_LDS, stream>>>(z1, woF, A_q, xst, kvT, out);
  }
}
```

```cpp
#include <hip/hip_runtime.h>
#include <stdint.h>

typedef unsigned short u16;
typedef __attribute__((ext_vector_type(4))) float f32x4;
typedef __attribute__((ext_vector_type(2))) float f32x2;
typedef __attribute__((ext_vector_type(4))) unsigned int u32x4;
typedef __attribute__((ext_vector_type(2))) unsigned int u32x2;
typedef __attribute__((ext_vector_type(2))) __bf16 bf16x2n;
#define LDS_AS __attribute__((address_space(3)))
#define SB() __builtin_amdgcn_sched_barrier(0)
#define DI __device__ __forceinline__

#define ST_DECL do {} while (0)
#define ST(i) do {} while (0)
#define ST_OUT() do {} while (0)

constexpr int S_ = 2048, D_ = 640;
constexpr unsigned HOLE = 0xFFFFFFFFu;
constexpr int W_ST = 4096, W_FZ = 768 << 10, W_FY = W_FZ + 8192, W_FK = W_FY + 8192, W_KV = 1 << 20, W_Z2 = 8 << 20, W_YY = 12 << 20, W_HH = 16 << 20, W_WO = 20 << 20;

DI unsigned pack2(float lo, float hi) {
  f32x2 v = {lo, hi};
  return __builtin_bit_cast(unsigned, __builtin_convertvector(v, bf16x2n));
}
DI float lo16(unsigned w) { return __uint_as_float(w << 16); }
DI float hi16(unsigned w) { return __uint_as_float(w & 0xFFFF0000u); }
DI float rbf(float f) { return lo16(pack2(f, f)); }
DI f32x2 unpk(unsigned w) { f32x2 r = {lo16(w), hi16(w)}; return r; }
DI unsigned pk2(f32x2 v) { return pack2(v[0], v[1]); }
DI unsigned nohole(unsigned a) { return a < HOLE - 1 ? a : HOLE - 1; }
template <int CTRL> DI float dppf(float v) {
  return __builtin_bit_cast(float, __builtin_amdgcn_update_dpp(0, __builtin_bit_cast(int, v), CTRL, 0xF, 0xF, true));
}
DI f32x2 gelu2(f32x2 t, float k1, float k2, float den) {
  const f32x2 a = t * __builtin_elementwise_fma(t * t, (f32x2){k2, k2}, (f32x2){k1, k1});
  f32x2 e = {__builtin_amdgcn_exp2f(a[0]), __builtin_amdgcn_exp2f(a[1])};
  e = __builtin_elementwise_fma(e, (f32x2){den, den}, (f32x2){den, den});
  const f32x2 r = {__builtin_amdgcn_rcpf(e[0]), __builtin_amdgcn_rcpf(e[1])};
  return t * r;
}
DI float red16(float v) {
  const auto a = __builtin_amdgcn_permlane16_swap(__float_as_uint(v), __float_as_uint(v), false, false);
  return __uint_as_float(a[0]) + __uint_as_float(a[1]);
}
DI float red32(float v) {
  const auto c = __builtin_amdgcn_permlane32_swap(__float_as_uint(v), __float_as_uint(v), false, false);
  return __uint_as_float(c[0]) + __uint_as_float(c[1]);
}
DI float sel32(float a, float b) {
  const auto c = __builtin_amdgcn_permlane32_swap(__float_as_uint(a), __float_as_uint(b), false, false);
  return __uint_as_float(c[0]) + __uint_as_float(c[1]);
}
DI float dot2(unsigned a, unsigned b, float c) {
  return __builtin_amdgcn_fdot2_f32_bf16(__builtin_bit_cast(bf16x2n, a), __builtin_bit_cast(bf16x2n, b), c, false);
}
DI unsigned shr16(unsigned hi, unsigned lo) { return __builtin_amdgcn_alignbit(hi, lo, 16); }
DI unsigned lds_addr(const void* p) { return (unsigned)(uintptr_t)(LDS_AS const char*)p; }

__global__ __launch_bounds__(256, 1) __attribute__((amdgpu_num_vgpr(160))) void k_all(
    const char* x, const char* kin, const char* vin, const char* shift_k, const char* ssm_k, const char* ssm_D,
    const char* w_in, const char* w_sp, const char* b_sp, const char* w_out, char* out, char* ws) {
  extern __shared__ __attribute__((aligned(16))) char smem[];
  ST_DECL;
  ST(0);
  const unsigned tid = threadIdx.x, lane0 = tid & 63, w0 = __builtin_amdgcn_readfirstlane(tid >> 6);
  const unsigned b = blockIdx.x, xcd = b & 7, idx = b >> 3;
  const unsigned rb0 = 8 * (xcd >> 1) + (idx >> 2), cb0 = 4 * (xcd & 1) + (idx & 3);
  const unsigned lb = lds_addr(smem);
  const unsigned lane = lane0, w = w0, rb = rb0, cb = cb0;
  unsigned voA = (rb * 64 + w * 16 + (lane >> 3)) * 1280 + (((lane & 7) ^ (lane >> 3)) << 4), voB[3];
#pragma unroll
  for (int i = 0; i < 3; ++i) { const unsigned o = i * 64 + lane, r = (o * 171) >> 11, c = o - r * 12; voB[i] = (w * 16 + r) * 3072 + cb * 192 + ((c ^ ((r >> 3) << 1)) << 4); }
  const unsigned mX = lb + w * 2048, mW = lb + w * 3072, mA = lb + w * 16384;
  const unsigned rot = (cb * 5) >> 2;
  auto kt = [&](unsigned s) { const unsigned k = s + rot; return k >= 10 ? k - 10 : k; };
  asm volatile("" : "+v"(voA), "+v"(voB[0]), "+v"(voB[1]), "+v"(voB[2]));
  asm volatile("" : "+s"(x), "+s"(kin), "+s"(vin), "+s"(shift_k), "+s"(ssm_k), "+s"(ssm_D), "+s"(w_in), "+s"(w_sp), "+s"(b_sp), "+s"(w_out), "+s"(out), "+s"(ws));
  const auto rX = __builtin_amdgcn_make_buffer_rsrc((void*)x, 0, S_ * D_ * 2, 0x00020000);
  const auto rW = __builtin_amdgcn_make_buffer_rsrc((void*)w_in, 0, 640 * 3072, 0x00020000);
  asm volatile("; named registers" :  :  : "v255", "a255"); SB();
  asm volatile("s_add_u32 m0, %0, 81920" :  : "s"(mX) : "memory", "scc"); SB();
  asm volatile("s_nop 0\n\tbuffer_load_dwordx4 %0, %1, %2 offen lds" :  : "v"(voA), "s"(rX), "s"(kt(0) * 128u + 0u) : "memory"); SB();
  asm volatile("s_add_u32 m0, %0, 82944" :  : "s"(mX) : "memory", "scc"); SB();
  asm volatile("s_nop 0\n\tbuffer_load_dwordx4 %0, %1, %2 offen lds" :  : "v"(voA), "s"(rX), "s"(kt(0) * 128u + 10240u) : "memory"); SB();
  asm volatile("s_add_u32 m0, %0, 0" :  : "s"(mW) : "memory", "scc"); SB();
  asm volatile("s_nop 0\n\tbuffer_load_dwordx4 %0, %1, %2 offen lds" :  : "v"(voB[0]), "s"(rW), "s"(kt(0) * 196608u + 1536u) : "memory"); SB();
  asm volatile("s_add_u32 m0, %0, 1024" :  : "s"(mW) : "memory", "scc"); SB();
  asm volatile("s_nop 0\n\tbuffer_load_dwordx4 %0, %1, %2 offen lds" :  : "v"(voB[1]), "s"(rW), "s"(kt(0) * 196608u + 1536u) : "memory"); SB();
  asm volatile("s_add_u32 m0, %0, 2048" :  : "s"(mW) : "memory", "scc"); SB();
  asm volatile("s_nop 0\n\tbuffer_load_dwordx4 %0, %1, %2 offen lds" :  : "v"(voB[2]), "s"(rW), "s"(kt(0) * 196608u + 1536u) : "memory"); SB();
  {
    unsigned long long pc; unsigned clen;
    asm volatile("s_getpc_b64 %0\n.Lpc_here_%=:\n\ts_mov_b32 %1, k_end-.Lpc_here_%=" : "=s"(pc), "=s"(clen));
    if (idx < 2) {
      const unsigned o = min((idx * 4 + w) * 8192 + lane * 128, clen - 4u) & ~3u;
      asm volatile("global_load_dword v254, %0, %1" :: "v"(o), "s"(pc) : "memory");
    }
    SB();
  }
  const auto rWS = __builtin_amdgcn_make_buffer_rsrc((void*)ws, 0, 32 << 20, 0x00020000);
  asm volatile("s_add_u32 m0, %0, 90112" :  : "s"(mX) : "memory", "scc"); SB();
  asm volatile("s_nop 0\n\tbuffer_load_dwordx4 %0, %1, %2 offen lds" :  : "v"(voA), "s"(rX), "s"(kt(1) * 128u + 0u) : "memory"); SB();
  asm volatile("s_add_u32 m0, %0, 91136" :  : "s"(mX) : "memory", "scc"); SB();
  asm volatile("s_nop 0\n\tbuffer_load_dwordx4 %0, %1, %2 offen lds" :  : "v"(voA), "s"(rX), "s"(kt(1) * 128u + 10240u) : "memory"); SB();
  asm volatile("s_add_u32 m0, %0, 12288" :  : "s"(mW) : "memory", "scc"); SB();
  asm volatile("s_nop 0\n\tbuffer_load_dwordx4 %0, %1, %2 offen lds" :  : "v"(voB[0]), "s"(rW), "s"(kt(1) * 196608u + 1536u) : "memory"); SB();
  asm volatile("s_add_u32 m0, %0, 13312" :  : "s"(mW) : "memory", "scc"); SB();
  asm volatile("s_nop 0\n\tbuffer_load_dwordx4 %0, %1, %2 offen lds" :  : "v"(voB[1]), "s"(rW), "s"(kt(1) * 196608u + 1536u) : "memory"); SB();
  asm volatile("s_add_u32 m0, %0, 14336" :  : "s"(mW) : "memory", "scc"); SB();
  asm volatile("s_nop 0\n\tbuffer_load_dwordx4 %0, %1, %2 offen lds" :  : "v"(voB[2]), "s"(rW), "s"(kt(1) * 196608u + 1536u) : "memory"); SB();
  { unsigned lane = lane0, w = w0, rb = rb0, cb = cb0;
  asm volatile("" : "+v"(lane)); asm volatile("" : "+s"(w), "+s"(rb), "+s"(cb)); SB();
  const unsigned wm = w >> 1, wn = w & 1, g4 = lane >> 4, l15 = lane & 15, q4 = l15 >> 2, p4 = l15 & 3;
  const unsigned nch = 3 - (rb >> 4), ch0 = cb * 80 + rb * 2 + min(rb, 16u);
  const unsigned l16 = lane * 16;
  const unsigned vZ = W_Z2 + ((rb * 2 + wm) * 48 + cb * 6 + wn * 3) * 1024 + l16;
  const unsigned vH4 = W_HH + ((rb * 4 + wm * 2) * 24 + cb * 3 + wn * 2) * 1024 + l16, vH2 = W_HH + ((rb * 4 + w) * 24 + cb * 3 + 1) * 1024 + l16;
  const unsigned vY = W_YY + ch0 * 4096 + w * 1024 + l16;
  const unsigned c2o = (nch - 2) * 2, c2s = 8192u + ((rb >> 4) << 30), w3s = (w + 1) >> 2 << 30;
  (void)wm; (void)wn; (void)g4; (void)q4; (void)p4; (void)vZ; (void)vH4; (void)vH2; (void)vY; (void)c2o; (void)c2s; (void)w3s;
  const auto rK = __builtin_amdgcn_make_buffer_rsrc((void*)kin, 0, S_ * D_ * 2, 0x00020000);
  const auto rV = __builtin_amdgcn_make_buffer_rsrc((void*)vin, 0, S_ * D_ * 2, 0x00020000);
  const auto rO = __builtin_amdgcn_make_buffer_rsrc((void*)w_out, 0, 768 * 1280, 0x00020000);
  const auto rA = __builtin_amdgcn_make_buffer_rsrc((void*)w_sp, 0, S_ * S_ * 2, 0x00020000);
  unsigned voK[4], voV[3], voO = (lane >> 1) * 1280 + (lane & 1) * 16, voH = (cb * 80 + 4 * g4 + p4) * 8;
#pragma unroll
  for (int n = 0; n < 4; ++n) { const unsigned o = n * 64 + lane, r = (o * 205) >> 11; voK[n] = (rb * 64 + w * 16 - 4) * 1280 + cb * 160 + r * 1120 + o * 16; }
#pragma unroll
  for (int n = 0; n < 3; ++n) voV[n] = voK[n] + 5120;
  voK[3] |= lane >> 3 << 28; voV[2] |= lane >> 5 << 30;
  const unsigned wof = min(rb * 4 + w, 119u), wok = (wof * 205) >> 10;
  u32x2 sw[5], xv[5];
  unsigned aX[2][2], aW[2][2][3], aV[5];
#pragma unroll
  for (int p = 0; p < 2; ++p) {
#pragma unroll
    for (int i = 0; i < 2; ++i) aX[p][i] = lb + 98304 + (wm * 32 + (i ^ wn) * 16 + l15) * 128 + ((((p ^ wm) * 4 + g4) ^ (l15 & 7)) << 4);
#pragma unroll
    for (int j = 0; j < 3; ++j) {
      aW[0][p][j] = lb + ((p ^ wm) * 32 + 8 * g4 + q4) * 192 + (((wn ? 3 + (j + 1) % 3 : j) ^ (g4 & 1)) << 5) + 8 * p4;
      aW[1][p][j] = aW[0][p][j] + 49152;
    }
  }
#pragma unroll
  for (int j = 0; j < 5; ++j) { const unsigned c = (cb & 3) * 2 + 2 * j + (g4 >> 1); aV[j] = lb + 81920 + (c >> 3) * 8192 + (w * 16 + l15) * 128 + (((c ^ l15) & 7) << 4) + (g4 & 1) * 8; }
  u32x4 ones = {0x3F803F80u, 0x3F803F80u, 0x3F803F80u, 0x3F803F80u}, hole4 = {HOLE, HOLE, HOLE, HOLE};
  f32x4 a2[2][3], a1[2][3], rs, gr, c2[3], c1[3];
  unsigned voS = (rb * 64 + (lane >> 3)) * 4096 + w * 1024 + (((lane & 7) ^ (lane >> 3)) << 4), aA[2];
#pragma unroll
  for (int k = 0; k < 2; ++k) aA[k] = lb + 98304 + w * 16384 + l15 * 128 + (((k * 4 + g4) ^ (l15 & 7)) << 4);
  unsigned vH4_ = vH4, vH2_ = vH2, xw = lb + 98304 + w * 16384 + l15 * 8, xr_ = lb + 98304 + (w ^ 1) * 16384 + l15 * 8;
  unsigned vo = W_ST + ((rb * 2 + wm) * 8 + cb) * 1024 + wn * 128 + ((lane >> 5) ^ wn) * 512 + (l15 >> 3) * 256 + (l15 & 7) * 16;
  asm volatile("" : "+v"(ones), "+v"(hole4), "+v"(voK[0]), "+v"(voK[1]), "+v"(voK[2]), "+v"(voK[3]), "+v"(voV[0]), "+v"(voV[1]), "+v"(voV[2]), "+v"(voO), "+v"(voH),
               "+v"(voS), "+v"(aA[0]), "+v"(aA[1]), "+v"(vH4_), "+v"(vH2_), "+v"(xw), "+v"(xr_), "+v"(aV[0]), "+v"(aV[1]), "+v"(aV[2]), "+v"(aV[3]), "+v"(aV[4]), "+v"(vo));
  asm volatile("" : "+v"(aX[0][0]), "+v"(aX[0][1]), "+v"(aX[1][0]), "+v"(aX[1][1]), "+v"(aW[0][0][0]), "+v"(aW[0][0][1]), "+v"(aW[0][0][2]), "+v"(aW[0][1][0]), "+v"(aW[0][1][1]), "+v"(aW[0][1][2]),
               "+v"(aW[1][0][0]), "+v"(aW[1][0][1]), "+v"(aW[1][0][2]), "+v"(aW[1][1][0]), "+v"(aW[1][1][1]), "+v"(aW[1][1][2]));
  int tmo = 1 << 18;
  unsigned tg0, tg1;
  float xm[2], xd[2], k1[2], k2[2];
  f32x2 xo;
  auto kset = [&]() {
#pragma unroll
    for (int i = 0; i < 2; ++i) { const float r = __builtin_amdgcn_rcpf(xd[i]); k1[i] = -2.3022082f * r; k2[i] = (-0.10294324f * r) * (r * r); }
    asm volatile("" : "+v"(k1[0]), "+v"(k1[1]), "+v"(k2[0]), "+v"(k2[1]));
  };
  ST(1);
  SB();
  asm volatile("s_add_u32 m0, %0, 98304" :  : "s"(mX) : "memory", "scc"); SB();
  asm volatile("s_nop 0\n\tbuffer_load_dwordx4 %0, %1, %2 offen lds" :  : "v"(voA), "s"(rX), "s"(kt(2) * 128u + 0u) : "memory"); SB();
  asm volatile("s_add_u32 m0, %0, 99328" :  : "s"(mX) : "memory", "scc"); SB();
  asm volatile("s_nop 0\n\tbuffer_load_dwordx4 %0, %1, %2 offen lds" :  : "v"(voA), "s"(rX), "s"(kt(2) * 128u + 10240u) : "memory"); SB();
  asm volatile("s_add_u32 m0, %0, 106496" :  : "s"(mX) : "memory", "scc"); SB();
  asm volatile("s_nop 0\n\tbuffer_load_dwordx4 %0, %1, %2 offen lds" :  : "v"(voA), "s"(rX), "s"(kt(3) * 128u + 0u) : "memory"); SB();
  asm volatile("s_add_u32 m0, %0, 107520" :  : "s"(mX) : "memory", "scc"); SB();
  asm volatile("s_nop 0\n\tbuffer_load_dwordx4 %0, %1, %2 offen lds" :  : "v"(voA), "s"(rX), "s"(kt(3) * 128u + 10240u) : "memory"); SB();
  asm volatile("s_add_u32 m0, %0, 24576" :  : "s"(mW) : "memory", "scc"); SB();
  asm volatile("s_nop 0\n\tbuffer_load_dwordx4 %0, %1, %2 offen lds" :  : "v"(voB[0]), "s"(rW), "s"(kt(2) * 196608u + 1536u) : "memory"); SB();
  asm volatile("s_add_u32 m0, %0, 25600" :  : "s"(mW) : "memory", "scc"); SB();
  asm volatile("s_nop 0\n\tbuffer_load_dwordx4 %0, %1, %2 offen lds" :  : "v"(voB[1]), "s"(rW), "s"(kt(2) * 196608u + 1536u) : "memory"); SB();
  asm volatile("s_waitcnt vmcnt(11) lgkmcnt(0)\n\ts_barrier" :  :  : "memory"); SB();
  ST(2);
  asm volatile("s_add_u32 m0, %0, 26624" :  : "s"(mW) : "memory", "scc"); SB();
  asm volatile("s_nop 0\n\tbuffer_load_dwordx4 %0, %1, %2 offen lds" :  : "v"(voB[2]), "s"(rW), "s"(kt(2) * 196608u + 1536u) : "memory"); SB();
  asm volatile("ds_read_b128 a[0:3], %0 offset:0" :  : "v"((aX[0][0] - 16384)) : "memory"); SB();
  asm volatile("ds_read_b128 a[4:7], %0 offset:0" :  : "v"((aX[0][1] - 16384)) : "memory"); SB();
  asm volatile("ds_read_b64_tr_b16 v[160:161], %0 offset:0" :  : "v"(aW[0][0][0]) : "memory"); SB();
  asm volatile("ds_read_b64_tr_b16 v[162:163], %0 offset:768" :  : "v"(aW[0][0][0]) : "memory"); SB();
  asm volatile("ds_read_b64_tr_b16 v[164:165], %0 offset:0" :  : "v"(aW[0][0][1]) : "memory"); SB();
  asm volatile("ds_read_b64_tr_b16 v[166:167], %0 offset:768" :  : "v"(aW[0][0][1]) : "memory"); SB();
  asm volatile("ds_read_b64_tr_b16 v[168:169], %0 offset:0" :  : "v"(aW[0][0][2]) : "memory"); SB();
  asm volatile("ds_read_b64_tr_b16 v[170:171], %0 offset:768" :  : "v"(aW[0][0][2]) : "memory"); SB();
  asm volatile("s_waitcnt lgkmcnt(4)\n\tv_mfma_f32_16x16x32_bf16 %0, v[160:163], a[0:3], 0" : "=&v"(a2[0][0]) :  : ); SB();
  asm volatile("ds_read_b128 a[8:11], %0 offset:0" :  : "v"((aX[1][0] - 16384)) : "memory"); SB();
  asm volatile("s_add_u32 m0, %0, 114688" :  : "s"(mX) : "memory", "scc"); SB();
  asm volatile("v_mfma_f32_16x16x32_bf16 %0, v[160:163], a[4:7], 0" : "=&v"(a2[1][0]) :  : ); SB();
  asm volatile("ds_read_b128 a[12:15], %0 offset:0" :  : "v"((aX[1][1] - 16384)) : "memory"); SB();
  asm volatile("buffer_load_dwordx4 %0, %1, %2 offen lds" :  : "v"(voA), "s"(rX), "s"(kt(4) * 128u + 0u) : "memory"); SB();
  asm volatile("v_mfma_f32_16x16x32_bf16 %0, v[160:163], %1, 0" : "=&v"(c2[0]) : "v"(ones) : ); SB();
  asm volatile("ds_read_b64_tr_b16 v[172:173], %0 offset:0" :  : "v"(aW[0][1][0]) : "memory"); SB();
  asm volatile("s_add_u32 m0, %0, 115712" :  : "s"(mX) : "memory", "scc"); SB();
  asm volatile("s_waitcnt lgkmcnt(5)\n\tv_mfma_f32_16x16x32_bf16 %0, v[164:167], a[0:3], 0" : "=&v"(a2[0][1]) :  : ); SB();
  asm volatile("ds_read_b64_tr_b16 v[174:175], %0 offset:768" :  : "v"(aW[0][1][0]) : "memory"); SB();
  asm volatile("buffer_load_dwordx4 %0, %1, %2 offen lds" :  : "v"(voA), "s"(rX), "s"(kt(4) * 128u + 10240u) : "memory"); SB();
  asm volatile("v_mfma_f32_16x16x32_bf16 %0, v[164:167], a[4:7], 0" : "=&v"(a2[1][1]) :  : ); SB();
  asm volatile("ds_read_b64_tr_b16 v[176:177], %0 offset:0" :  : "v"(aW[0][1][1]) : "memory"); SB();
  asm volatile("s_add_u32 m0, %0, 122880" :  : "s"(mX) : "memory", "scc"); SB();
  asm volatile("v_mfma_f32_16x16x32_bf16 %0, v[164:167], %1, 0" : "=&v"(c2[1]) : "v"(ones) : ); SB();
  asm volatile("ds_read_b64_tr_b16 v[178:179], %0 offset:768" :  : "v"(aW[0][1][1]) : "memory"); SB();
  asm volatile("buffer_load_dwordx4 %0, %1, %2 offen lds" :  : "v"(voA), "s"(rX), "s"(kt(5) * 128u + 0u) : "memory"); SB();
  asm volatile("s_waitcnt lgkmcnt(6)\n\tv_mfma_f32_16x16x32_bf16 %0, v[168:171], a[0:3], 0" : "=&v"(a2[0][2]) :  : ); SB();
  asm volatile("ds_read_b64_tr_b16 v[180:181], %0 offset:0" :  : "v"(aW[0][1][2]) : "memory"); SB();
  asm volatile("v_mfma_f32_16x16x32_bf16 %0, v[168:171], a[4:7], 0" : "=&v"(a2[1][2]) :  : ); SB();
  asm volatile("ds_read_b64_tr_b16 v[182:183], %0 offset:768" :  : "v"(aW[0][1][2]) : "memory"); SB();
  asm volatile("v_mfma_f32_16x16x32_bf16 %0, v[168:171], %1, 0" : "=&v"(c2[2]) : "v"(ones) : ); SB();
  asm volatile("v_mfma_f32_16x16x32_bf16 %0, %1, a[0:3], 0" : "=&v"(rs) : "v"(ones) : ); SB();
  asm volatile("v_mfma_f32_16x16x32_bf16 %0, a[0:3], a[0:3], 0" : "=&v"(gr) :  : ); SB();
  asm volatile("s_add_u32 m0, %0, 123904" :  : "s"(mX) : "memory", "scc"); SB();
  asm volatile("s_nop 0\n\tbuffer_load_dwordx4 %0, %1, %2 offen lds" :  : "v"(voA), "s"(rX), "s"(kt(5) * 128u + 10240u) : "memory"); SB();
  asm volatile("s_waitcnt vmcnt(11) lgkmcnt(0)\n\ts_barrier" :  :  : "memory"); SB();
  asm volatile("s_add_u32 m0, %0, 36864" :  : "s"(mW) : "memory", "scc"); SB();
  asm volatile("s_nop 0\n\tbuffer_load_dwordx4 %0, %1, %2 offen lds" :  : "v"(voB[0]), "s"(rW), "s"(kt(3) * 196608u + 1536u) : "memory"); SB();
  asm volatile("v_mfma_f32_16x16x32_bf16 %0, v[172:175], a[8:11], %0" : "+v"(a2[0][0]) :  : ); SB();
  asm volatile("ds_read_b128 a[16:19], %0 offset:8192" :  : "v"((aX[0][0] - 16384)) : "memory"); SB();
  asm volatile("v_mfma_f32_16x16x32_bf16 %0, v[172:175], a[12:15], %0" : "+v"(a2[1][0]) :  : ); SB();
  asm volatile("ds_read_b128 a[20:23], %0 offset:8192" :  : "v"((aX[0][1] - 16384)) : "memory"); SB();
  asm volatile("v_mfma_f32_16x16x32_bf16 %0, v[172:175], %1, %0" : "+v"(c2[0]) : "v"(ones) : ); SB();
  asm volatile("ds_read_b128 a[24:27], %0 offset:8192" :  : "v"((aX[1][0] - 16384)) : "memory"); SB();
  asm volatile("s_add_u32 m0, %0, 37888" :  : "s"(mW) : "memory", "scc"); SB();
  asm volatile("v_mfma_f32_16x16x32_bf16 %0, v[176:179], a[8:11], %0" : "+v"(a2[0][1]) :  : ); SB();
  asm volatile("ds_read_b128 a[28:31], %0 offset:8192" :  : "v"((aX[1][1] - 16384)) : "memory"); SB();
  asm volatile("buffer_load_dwordx4 %0, %1, %2 offen lds" :  : "v"(voB[1]), "s"(rW), "s"(kt(3) * 196608u + 1536u) : "memory"); SB();
  asm volatile("v_mfma_f32_16x16x32_bf16 %0, v[176:179], a[12:15], %0" : "+v"(a2[1][1]) :  : ); SB();
  asm volatile("ds_read_b64_tr_b16 v[160:161], %0 offset:12288" :  : "v"(aW[0][0][0]) : "memory"); SB();
  asm volatile("v_mfma_f32_16x16x32_bf16 %0, v[176:179], %1, %0" : "+v"(c2[1]) : "v"(ones) : ); SB();
  asm volatile("ds_read_b64_tr_b16 v[162:163], %0 offset:13056" :  : "v"(aW[0][0][0]) : "memory"); SB();
  asm volatile("v_mfma_f32_16x16x32_bf16 %0, v[180:183], a[8:11], %0" : "+v"(a2[0][2]) :  : ); SB();
  asm volatile("ds_read_b64_tr_b16 v[164:165], %0 offset:12288" :  : "v"(aW[0][0][1]) : "memory"); SB();
  asm volatile("v_mfma_f32_16x16x32_bf16 %0, v[180:183], a[12:15], %0" : "+v"(a2[1][2]) :  : ); SB();
  asm volatile("ds_read_b64_tr_b16 v[166:167], %0 offset:13056" :  : "v"(aW[0][0][1]) : "memory"); SB();
  asm volatile("s_add_u32 m0, %0, 38912" :  : "s"(mW) : "memory", "scc"); SB();
  asm volatile("v_mfma_f32_16x16x32_bf16 %0, v[180:183], %1, %0" : "+v"(c2[2]) : "v"(ones) : ); SB();
  asm volatile("ds_read_b64_tr_b16 v[168:169], %0 offset:12288" :  : "v"(aW[0][0][2]) : "memory"); SB();
  asm volatile("buffer_load_dwordx4 %0, %1, %2 offen lds" :  : "v"(voB[2]), "s"(rW), "s"(kt(3) * 196608u + 1536u) : "memory"); SB();
  asm volatile("v_mfma_f32_16x16x32_bf16 %0, %1, a[8:11], %0" : "+v"(rs) : "v"(ones) : ); SB();
  asm volatile("ds_read_b64_tr_b16 v[170:171], %0 offset:13056" :  : "v"(aW[0][0][2]) : "memory"); SB();
  asm volatile("v_mfma_f32_16x16x32_bf16 %0, a[8:11], a[8:11], %0" : "+v"(gr) :  : ); SB();
  asm volatile("ds_read_b64 %0, %1 offset:0" : "=&v"(xv[0]) : "v"(aV[0]) : "memory"); SB();
  asm volatile("ds_read_b64 %0, %1 offset:0" : "=&v"(xv[1]) : "v"(aV[1]) : "memory"); SB();
  asm volatile("ds_read_b64 %0, %1 offset:0" : "=&v"(xv[2]) : "v"(aV[2]) : "memory"); SB();
  asm volatile("ds_read_b64 %0, %1 offset:0" : "=&v"(xv[3]) : "v"(aV[3]) : "memory"); SB();
  asm volatile("ds_read_b64 %0, %1 offset:0" : "=&v"(xv[4]) : "v"(aV[4]) : "memory"); SB();
  asm volatile("s_waitcnt lgkmcnt(9)\n\tv_mfma_f32_16x16x32_bf16 %0, v[160:163], a[16:19], %0" : "+v"(a2[0][0]) :  : ); SB();
  asm volatile("ds_read_b64_tr_b16 v[172:173], %0 offset:12288" :  : "v"(aW[0][1][0]) : "memory"); SB();
  asm volatile("v_mfma_f32_16x16x32_bf16 %0, v[160:163], a[20:23], %0" : "+v"(a2[1][0]) :  : ); SB();
  asm volatile("ds_read_b64_tr_b16 v[174:175], %0 offset:13056" :  : "v"(aW[0][1][0]) : "memory"); SB();
  asm volatile("v_mfma_f32_16x16x32_bf16 %0, v[160:163], %1, %0" : "+v"(c2[0]) : "v"(ones) : ); SB();
  asm volatile("ds_read_b64_tr_b16 v[176:177], %0 offset:12288" :  : "v"(aW[0][1][1]) : "memory"); SB();
  asm volatile("s_waitcnt lgkmcnt(10)\n\tv_mfma_f32_16x16x32_bf16 %0, v[164:167], a[16:19], %0" : "+v"(a2[0][1]) :  : ); SB();
  asm volatile("ds_read_b64_tr_b16 v[178:179], %0 offset:13056" :  : "v"(aW[0][1][1]) : "memory"); SB();
  asm volatile("v_mfma_f32_16x16x32_bf16 %0, v[164:167], a[20:23], %0" : "+v"(a2[1][1]) :  : ); SB();
  asm volatile("ds_read_b64_tr_b16 v[180:181], %0 offset:12288" :  : "v"(aW[0][1][2]) : "memory"); SB();
  asm volatile("v_mfma_f32_16x16x32_bf16 %0, v[164:167], %1, %0" : "+v"(c2[1]) : "v"(ones) : ); SB();
  asm volatile("ds_read_b64_tr_b16 v[182:183], %0 offset:13056" :  : "v"(aW[0][1][2]) : "memory"); SB();
  asm volatile("s_waitcnt lgkmcnt(11)\n\tv_mfma_f32_16x16x32_bf16 %0, v[168:171], a[16:19], %0" : "+v"(a2[0][2]) :  : ); SB();
  asm volatile("v_mfma_f32_16x16x32_bf16 %0, v[168:171], a[20:23], %0" : "+v"(a2[1][2]) :  : ); SB();
  asm volatile("v_mfma_f32_16x16x32_bf16 %0, v[168:171], %1, %0" : "+v"(c2[2]) : "v"(ones) : ); SB();
  asm volatile("v_mfma_f32_16x16x32_bf16 %0, %1, a[16:19], %0" : "+v"(rs) : "v"(ones) : ); SB();
  asm volatile("v_mfma_f32_16x16x32_bf16 %0, a[16:19], a[16:19], %0" : "+v"(gr) :  : ); SB();
  asm volatile("v_mfma_f32_16x16x32_bf16 %0, %1, a[24:27], %0" : "+v"(rs) : "v"(ones) : ); SB();
  asm volatile("v_mfma_f32_16x16x32_bf16 %0, a[24:27], a[24:27], %0" : "+v"(gr) :  : ); SB();
  asm volatile("s_add_u32 m0, %0, 131072" :  : "s"(mX) : "memory", "scc"); SB();
  asm volatile("s_nop 0\n\tbuffer_load_dwordx4 %0, %1, %2 offen lds" :  : "v"(voA), "s"(rX), "s"(kt(6) * 128u + 0u) : "memory"); SB();
  asm volatile("s_add_u32 m0, %0, 132096" :  : "s"(mX) : "memory", "scc"); SB();
  asm volatile("s_nop 0\n\tbuffer_load_dwordx4 %0, %1, %2 offen lds" :  : "v"(voA), "s"(rX), "s"(kt(6) * 128u + 10240u) : "memory"); SB();
  asm volatile("s_add_u32 m0, %0, 139264" :  : "s"(mX) : "memory", "scc"); SB();
  asm volatile("s_nop 0\n\tbuffer_load_dwordx4 %0, %1, %2 offen lds" :  : "v"(voA), "s"(rX), "s"(kt(7) * 128u + 0u) : "memory"); SB();
  asm volatile("s_add_u32 m0, %0, 140288" :  : "s"(mX) : "memory", "scc"); SB();
  asm volatile("s_nop 0\n\tbuffer_load_dwordx4 %0, %1, %2 offen lds" :  : "v"(voA), "s"(rX), "s"(kt(7) * 128u + 10240u) : "memory"); SB();
  asm volatile("s_waitcnt vmcnt(11) lgkmcnt(0)\n\ts_barrier" :  :  : "memory"); SB();
  asm volatile("s_add_u32 m0, %0, 49152" :  : "s"(mW) : "memory", "scc"); SB();
  asm volatile("s_nop 0\n\tbuffer_load_dwordx4 %0, %1, %2 offen lds" :  : "v"(voB[0]), "s"(rW), "s"(kt(4) * 196608u + 1536u) : "memory"); SB();
  asm volatile("" : "+v"(xv[0]), "+v"(xv[1]), "+v"(xv[2]), "+v"(xv[3]), "+v"(xv[4]) :  : ); SB();
  asm volatile("v_mfma_f32_16x16x32_bf16 %0, v[172:175], a[24:27], %0" : "+v"(a2[0][0]) :  : ); SB();
  asm volatile("ds_read_b128 a[32:35], %0 offset:0" :  : "v"(aX[0][0]) : "memory"); SB();
  asm volatile("v_mfma_f32_16x16x32_bf16 %0, v[172:175], a[28:31], %0" : "+v"(a2[1][0]) :  : ); SB();
  asm volatile("ds_read_b128 a[36:39], %0 offset:0" :  : "v"(aX[0][1]) : "memory"); SB();
  asm volatile("v_mfma_f32_16x16x32_bf16 %0, v[172:175], %1, %0" : "+v"(c2[0]) : "v"(ones) : ); SB();
  asm volatile("ds_read_b128 a[40:43], %0 offset:0" :  : "v"(aX[1][0]) : "memory"); SB();
  asm volatile("v_mfma_f32_16x16x32_bf16 %0, v[176:179], a[24:27], %0" : "+v"(a2[0][1]) :  : ); SB();
  asm volatile("ds_read_b128 a[44:47], %0 offset:0" :  : "v"(aX[1][1]) : "memory"); SB();
  asm volatile("v_mfma_f32_16x16x32_bf16 %0, v[176:179], a[28:31], %0" : "+v"(a2[1][1]) :  : ); SB();
  asm volatile("ds_read_b64_tr_b16 v[160:161], %0 offset:24576" :  : "v"(aW[0][0][0]) : "memory"); SB();
  asm volatile("v_mfma_f32_16x16x32_bf16 %0, v[176:179], %1, %0" : "+v"(c2[1]) : "v"(ones) : ); SB();
  asm volatile("ds_read_b64_tr_b16 v[162:163], %0 offset:25344" :  : "v"(aW[0][0][0]) : "memory"); SB();
  asm volatile("v_mfma_f32_16x16x32_bf16 %0, v[180:183], a[24:27], %0" : "+v"(a2[0][2]) :  : ); SB();
  asm volatile("ds_read_b64_tr_b16 v[164:165], %0 offset:24576" :  : "v"(aW[0][0][1]) : "memory"); SB();
  asm volatile("v_mfma_f32_16x16x32_bf16 %0, v[180:183], a[28:31], %0" : "+v"(a2[1][2]) :  : ); SB();
  asm volatile("ds_read_b64_tr_b16 v[166:167], %0 offset:25344" :  : "v"(aW[0][0][1]) : "memory"); SB();
  asm volatile("v_mfma_f32_16x16x32_bf16 %0, v[180:183], %1, %0" : "+v"(c2[2]) : "v"(ones) : ); SB();
  asm volatile("ds_read_b64_tr_b16 v[168:169], %0 offset:24576" :  : "v"(aW[0][0][2]) : "memory"); SB();
  asm volatile("s_add_u32 m0, %0, 50176" :  : "s"(mW) : "memory", "scc"); SB();
  asm volatile("ds_read_b64_tr_b16 v[170:171], %0 offset:25344" :  : "v"(aW[0][0][2]) : "memory"); SB();
  asm volatile("s_waitcnt lgkmcnt(4)\n\tv_mfma_f32_16x16x32_bf16 %0, v[160:163], a[32:35], %0" : "+v"(a2[0][0]) :  : ); SB();
  asm volatile("ds_read_b64_tr_b16 v[172:173], %0 offset:24576" :  : "v"(aW[0][1][0]) : "memory"); SB();
  asm volatile("buffer_load_dwordx4 %0, %1, %2 offen lds" :  : "v"(voB[1]), "s"(rW), "s"(kt(4) * 196608u + 1536u) : "memory"); SB();
  asm volatile("v_mfma_f32_16x16x32_bf16 %0, v[160:163], a[36:39], %0" : "+v"(a2[1][0]) :  : ); SB();
  asm volatile("ds_read_b64_tr_b16 v[174:175], %0 offset:25344" :  : "v"(aW[0][1][0]) : "memory"); SB();
  asm volatile("s_add_u32 m0, %0, 51200" :  : "s"(mW) : "memory", "scc"); SB();
  asm volatile("v_mfma_f32_16x16x32_bf16 %0, v[160:163], %1, %0" : "+v"(c2[0]) : "v"(ones) : ); SB();
  asm volatile("ds_read_b64_tr_b16 v[176:177], %0 offset:24576" :  : "v"(aW[0][1][1]) : "memory"); SB();
  asm volatile("buffer_load_dwordx4 %0, %1, %2 offen lds" :  : "v"(voB[2]), "s"(rW), "s"(kt(4) * 196608u + 1536u) : "memory"); SB();
  asm volatile("s_waitcnt lgkmcnt(5)\n\tv_mfma_f32_16x16x32_bf16 %0, v[164:167], a[32:35], %0" : "+v"(a2[0][1]) :  : ); SB();
  asm volatile("ds_read_b64_tr_b16 v[178:179], %0 offset:25344" :  : "v"(aW[0][1][1]) : "memory"); SB();
  asm volatile("v_mfma_f32_16x16x32_bf16 %0, v[164:167], a[36:39], %0" : "+v"(a2[1][1]) :  : ); SB();
  asm volatile("ds_read_b64_tr_b16 v[180:181], %0 offset:24576" :  : "v"(aW[0][1][2]) : "memory"); SB();
  asm volatile("v_mfma_f32_16x16x32_bf16 %0, v[164:167], %1, %0" : "+v"(c2[1]) : "v"(ones) : ); SB();
  asm volatile("ds_read_b64_tr_b16 v[182:183], %0 offset:25344" :  : "v"(aW[0][1][2]) : "memory"); SB();
  asm volatile("s_add_u32 m0, %0, 147456" :  : "s"(mX) : "memory", "scc"); SB();
  asm volatile("s_waitcnt lgkmcnt(6)\n\tv_mfma_f32_16x16x32_bf16 %0, v[168:171], a[32:35], %0" : "+v"(a2[0][2]) :  : ); SB();
  asm volatile("ds_read_b128 a[48:51], %0 offset:8192" :  : "v"(aX[0][0]) : "memory"); SB();
  asm volatile("buffer_load_dwordx4 %0, %1, %2 offen lds" :  : "v"(voA), "s"(rX), "s"(kt(8) * 128u + 0u) : "memory"); SB();
  asm volatile("v_mfma_f32_16x16x32_bf16 %0, v[168:171], a[36:39], %0" : "+v"(a2[1][2]) :  : ); SB();
  asm volatile("ds_read_b128 a[52:55], %0 offset:8192" :  : "v"(aX[0][1]) : "memory"); SB();
  asm volatile("v_mfma_f32_16x16x32_bf16 %0, v[168:171], %1, %0" : "+v"(c2[2]) : "v"(ones) : ); SB();
  asm volatile("ds_read_b128 a[56:59], %0 offset:8192" :  : "v"(aX[1][0]) : "memory"); SB();
  asm volatile("v_mfma_f32_16x16x32_bf16 %0, %1, a[32:35], %0" : "+v"(rs) : "v"(ones) : ); SB();
  asm volatile("ds_read_b128 a[60:63], %0 offset:8192" :  : "v"(aX[1][1]) : "memory"); SB();
  asm volatile("s_add_u32 m0, %0, 148480" :  : "s"(mX) : "memory", "scc"); SB();
  asm volatile("v_mfma_f32_16x16x32_bf16 %0, a[32:35], a[32:35], %0" : "+v"(gr) :  : ); SB();
  asm volatile("buffer_load_dwordx4 %0, %1, %2 offen lds" :  : "v"(voA), "s"(rX), "s"(kt(8) * 128u + 10240u) : "memory"); SB();
  asm volatile("v_mfma_f32_16x16x32_bf16 %0, %1, a[40:43], %0" : "+v"(rs) : "v"(ones) : ); SB();
  asm volatile("v_mfma_f32_16x16x32_bf16 %0, a[40:43], a[40:43], %0" : "+v"(gr) :  : ); SB();
  asm volatile("s_add_u32 m0, %0, 155648" :  : "s"(mX) : "memory", "scc"); SB();
  asm volatile("s_nop 0\n\tbuffer_load_dwordx4 %0, %1, %2 offen lds" :  : "v"(voA), "s"(rX), "s"(kt(9) * 128u + 0u) : "memory"); SB();
  asm volatile("s_add_u32 m0, %0, 156672" :  : "s"(mX) : "memory", "scc"); SB();
  asm volatile("s_nop 0\n\tbuffer_load_dwordx4 %0, %1, %2 offen lds" :  : "v"(voA), "s"(rX), "s"(kt(9) * 128u + 10240u) : "memory"); SB();
  asm volatile("s_waitcnt vmcnt(11) lgkmcnt(0)\n\ts_barrier" :  :  : "memory"); SB();
  asm volatile("s_add_u32 m0, %0, 61440" :  : "s"(mW) : "memory", "scc"); SB();
  asm volatile("s_nop 0\n\tbuffer_load_dwordx4 %0, %1, %2 offen lds" :  : "v"(voB[0]), "s"(rW), "s"(kt(5) * 196608u + 1536u) : "memory"); SB();
  asm volatile("v_mfma_f32_16x16x32_bf16 %0, v[172:175], a[40:43], %0" : "+v"(a2[0][0]) :  : ); SB();
  asm volatile("ds_read_b64_tr_b16 v[160:161], %0 offset:36864" :  : "v"(aW[0][0][0]) : "memory"); SB();
  asm volatile("v_mfma_f32_16x16x32_bf16 %0, v[172:175], a[44:47], %0" : "+v"(a2[1][0]) :  : ); SB();
  asm volatile("ds_read_b64_tr_b16 v[162:163], %0 offset:37632" :  : "v"(aW[0][0][0]) : "memory"); SB();
  asm volatile("v_mfma_f32_16x16x32_bf16 %0, v[172:175], %1, %0" : "+v"(c2[0]) : "v"(ones) : ); SB();
  asm volatile("ds_read_b64_tr_b16 v[164:165], %0 offset:36864" :  : "v"(aW[0][0][1]) : "memory"); SB();
  asm volatile("v_mfma_f32_16x16x32_bf16 %0, v[176:179], a[40:43], %0" : "+v"(a2[0][1]) :  : ); SB();
  asm volatile("ds_read_b64_tr_b16 v[166:167], %0 offset:37632" :  : "v"(aW[0][0][1]) : "memory"); SB();
  asm volatile("v_mfma_f32_16x16x32_bf16 %0, v[176:179], a[44:47], %0" : "+v"(a2[1][1]) :  : ); SB();
  asm volatile("ds_read_b64_tr_b16 v[168:169], %0 offset:36864" :  : "v"(aW[0][0][2]) : "memory"); SB();
  asm volatile("v_mfma_f32_16x16x32_bf16 %0, v[176:179], %1, %0" : "+v"(c2[1]) : "v"(ones) : ); SB();
  asm volatile("ds_read_b64_tr_b16 v[170:171], %0 offset:37632" :  : "v"(aW[0][0][2]) : "memory"); SB();
  asm volatile("v_mfma_f32_16x16x32_bf16 %0, v[180:183], a[40:43], %0" : "+v"(a2[0][2]) :  : ); SB();
  asm volatile("ds_read_b128 a[64:67], %0 offset:16384" :  : "v"(aX[0][0]) : "memory"); SB();
  asm volatile("v_mfma_f32_16x16x32_bf16 %0, v[180:183], a[44:47], %0" : "+v"(a2[1][2]) :  : ); SB();
  asm volatile("ds_read_b128 a[68:71], %0 offset:16384" :  : "v"(aX[0][1]) : "memory"); SB();
  asm volatile("v_mfma_f32_16x16x32_bf16 %0, v[180:183], %1, %0" : "+v"(c2[2]) : "v"(ones) : ); SB();
  asm volatile("ds_read_b128 a[72:75], %0 offset:16384" :  : "v"(aX[1][0]) : "memory"); SB();
  asm volatile("v_mfma_f32_16x16x32_bf16 %0, %1, a[48:51], %0" : "+v"(rs) : "v"(ones) : ); SB();
  asm volatile("ds_read_b128 a[76:79], %0 offset:16384" :  : "v"(aX[1][1]) : "memory"); SB();
  asm volatile("v_mfma_f32_16x16x32_bf16 %0, a[48:51], a[48:51], %0" : "+v"(gr) :  : ); SB();
  asm volatile("v_mfma_f32_16x16x32_bf16 %0, %1, a[56:59], %0" : "+v"(rs) : "v"(ones) : ); SB();
  asm volatile("s_add_u32 m0, %0, 62464" :  : "s"(mW) : "memory", "scc"); SB();
  asm volatile("s_nop 0\n\tbuffer_load_dwordx4 %0, %1, %2 offen lds" :  : "v"(voB[1]), "s"(rW), "s"(kt(5) * 196608u + 1536u) : "memory"); SB();
  asm volatile("v_mfma_f32_16x16x32_bf16 %0, a[56:59], a[56:59], %0" : "+v"(gr) :  : ); SB();
  asm volatile("s_waitcnt lgkmcnt(8)\n\tv_mfma_f32_16x16x32_bf16 %0, v[160:163], a[48:51], %0" : "+v"(a2[0][0]) :  : ); SB();
  asm volatile("ds_read_b64_tr_b16 v[172:173], %0 offset:36864" :  : "v"(aW[0][1][0]) : "memory"); SB();
  asm volatile("v_mfma_f32_16x16x32_bf16 %0, v[160:163], a[52:55], %0" : "+v"(a2[1][0]) :  : ); SB();
  asm volatile("ds_read_b64_tr_b16 v[174:175], %0 offset:37632" :  : "v"(aW[0][1][0]) : "memory"); SB();
  asm volatile("v_mfma_f32_16x16x32_bf16 %0, v[160:163], %1, %0" : "+v"(c2[0]) : "v"(ones) : ); SB();
  asm volatile("ds_read_b64_tr_b16 v[176:177], %0 offset:36864" :  : "v"(aW[0][1][1]) : "memory"); SB();
  asm volatile("s_waitcnt lgkmcnt(9)\n\tv_mfma_f32_16x16x32_bf16 %0, v[164:167], a[48:51], %0" : "+v"(a2[0][1]) :  : ); SB();
  asm volatile("ds_read_b64_tr_b16 v[178:179], %0 offset:37632" :  : "v"(aW[0][1][1]) : "memory"); SB();
  asm volatile("s_add_u32 m0, %0, 63488" :  : "s"(mW) : "memory", "scc"); SB();
  asm volatile("v_mfma_f32_16x16x32_bf16 %0, v[164:167], a[52:55], %0" : "+v"(a2[1][1]) :  : ); SB();
  asm volatile("ds_read_b64_tr_b16 v[180:181], %0 offset:36864" :  : "v"(aW[0][1][2]) : "memory"); SB();
  asm volatile("buffer_load_dwordx4 %0, %1, %2 offen lds" :  : "v"(voB[2]), "s"(rW), "s"(kt(5) * 196608u + 1536u) : "memory"); SB();
  asm volatile("v_mfma_f32_16x16x32_bf16 %0, v[164:167], %1, %0" : "+v"(c2[1]) : "v"(ones) : ); SB();
  asm volatile("ds_read_b64_tr_b16 v[182:183], %0 offset:37632" :  : "v"(aW[0][1][2]) : "memory"); SB();
  asm volatile("s_waitcnt lgkmcnt(10)\n\tv_mfma_f32_16x16x32_bf16 %0, v[168:171], a[48:51], %0" : "+v"(a2[0][2]) :  : ); SB();
  asm volatile("ds_read_b128 a[80:83], %0 offset:24576" :  : "v"(aX[0][0]) : "memory"); SB();
  asm volatile("v_mfma_f32_16x16x32_bf16 %0, v[168:171], a[52:55], %0" : "+v"(a2[1][2]) :  : ); SB();
  asm volatile("ds_read_b128 a[84:87], %0 offset:24576" :  : "v"(aX[0][1]) : "memory"); SB();
  asm volatile("v_mfma_f32_16x16x32_bf16 %0, v[168:171], %1, %0" : "+v"(c2[2]) : "v"(ones) : ); SB();
  asm volatile("ds_read_b128 a[88:91], %0 offset:24576" :  : "v"(aX[1][0]) : "memory"); SB();
  asm volatile("s_waitcnt lgkmcnt(11)\n\tv_mfma_f32_16x16x32_bf16 %0, %1, a[64:67], %0" : "+v"(rs) : "v"(ones) : ); SB();
  asm volatile("ds_read_b128 a[92:95], %0 offset:24576" :  : "v"(aX[1][1]) : "memory"); SB();
  asm volatile("s_add_u32 m0, %0, 73728" :  : "s"(mW) : "memory", "scc"); SB();
  asm volatile("v_mfma_f32_16x16x32_bf16 %0, a[64:67], a[64:67], %0" : "+v"(gr) :  : ); SB();
  asm volatile("buffer_load_dwordx4 %0, %1, %2 offen lds" :  : "v"(voB[0]), "s"(rW), "s"(kt(6) * 196608u + 1536u) : "memory"); SB();
  asm volatile("s_waitcnt lgkmcnt(10)\n\tv_mfma_f32_16x16x32_bf16 %0, %1, a[72:75], %0" : "+v"(rs) : "v"(ones) : ); SB();
  asm volatile("v_mfma_f32_16x16x32_bf16 %0, a[72:75], a[72:75], %0" : "+v"(gr) :  : ); SB();
  asm volatile("s_add_u32 m0, %0, 74752" :  : "s"(mW) : "memory", "scc"); SB();
  asm volatile("s_nop 0\n\tbuffer_load_dwordx4 %0, %1, %2 offen lds" :  : "v"(voB[1]), "s"(rW), "s"(kt(6) * 196608u + 1536u) : "memory"); SB();
  asm volatile("s_add_u32 m0, %0, 75776" :  : "s"(mW) : "memory", "scc"); SB();
  asm volatile("s_nop 0\n\tbuffer_load_dwordx4 %0, %1, %2 offen lds" :  : "v"(voB[2]), "s"(rW), "s"(kt(6) * 196608u + 1536u) : "memory"); SB();
  asm volatile("s_add_u32 m0, %0, 86016" :  : "s"(mW) : "memory", "scc"); SB();
  asm volatile("s_nop 0\n\tbuffer_load_dwordx4 %0, %1, %2 offen lds" :  : "v"(voB[0]), "s"(rW), "s"(kt(7) * 196608u + 1536u) : "memory"); SB();
  asm volatile("s_waitcnt vmcnt(11) lgkmcnt(0)\n\ts_barrier" :  :  : "memory"); SB();
  asm volatile("s_add_u32 m0, %0, 87040" :  : "s"(mW) : "memory", "scc"); SB();
  asm volatile("s_nop 0\n\tbuffer_load_dwordx4 %0, %1, %2 offen lds" :  : "v"(voB[1]), "s"(rW), "s"(kt(7) * 196608u + 1536u) : "memory"); SB();
  asm volatile("v_mfma_f32_16x16x32_bf16 %0, v[172:175], a[56:59], %0" : "+v"(a2[0][0]) :  : ); SB();
  asm volatile("ds_read_b64_tr_b16 v[160:161], %0 offset:0" :  : "v"(aW[1][0][0]) : "memory"); SB();
  asm volatile("v_mfma_f32_16x16x32_bf16 %0, v[172:175], a[60:63], %0" : "+v"(a2[1][0]) :  : ); SB();
  asm volatile("ds_read_b64_tr_b16 v[162:163], %0 offset:768" :  : "v"(aW[1][0][0]) : "memory"); SB();
  asm volatile("v_mfma_f32_16x16x32_bf16 %0, v[172:175], %1, %0" : "+v"(c2[0]) : "v"(ones) : ); SB();
  asm volatile("ds_read_b64_tr_b16 v[164:165], %0 offset:0" :  : "v"(aW[1][0][1]) : "memory"); SB();
  asm volatile("s_add_u32 m0, %0, 88064" :  : "s"(mW) : "memory", "scc"); SB();
  asm volatile("v_mfma_f32_16x16x32_bf16 %0, v[176:179], a[56:59], %0" : "+v"(a2[0][1]) :  : ); SB();
  asm volatile("ds_read_b64_tr_b16 v[166:167], %0 offset:768" :  : "v"(aW[1][0][1]) : "memory"); SB();
  asm volatile("buffer_load_dwordx4 %0, %1, %2 offen lds" :  : "v"(voB[2]), "s"(rW), "s"(kt(7) * 196608u + 1536u) : "memory"); SB();
  asm volatile("v_mfma_f32_16x16x32_bf16 %0, v[176:179], a[60:63], %0" : "+v"(a2[1][1]) :  : ); SB();
  asm volatile("ds_read_b64_tr_b16 v[168:169], %0 offset:0" :  : "v"(aW[1][0][2]) : "memory"); SB();
  asm volatile("v_mfma_f32_16x16x32_bf16 %0, v[176:179], %1, %0" : "+v"(c2[1]) : "v"(ones) : ); SB();
  asm volatile("ds_read_b64_tr_b16 v[170:171], %0 offset:768" :  : "v"(aW[1][0][2]) : "memory"); SB();
  asm volatile("v_mfma_f32_16x16x32_bf16 %0, v[180:183], a[56:59], %0" : "+v"(a2[0][2]) :  : ); SB();
  asm volatile("ds_read_b128 a[96:99], %0 offset:32768" :  : "v"(aX[0][0]) : "memory"); SB();
  asm volatile("v_mfma_f32_16x16x32_bf16 %0, v[180:183], a[60:63], %0" : "+v"(a2[1][2]) :  : ); SB();
  asm volatile("ds_read_b128 a[100:103], %0 offset:32768" :  : "v"(aX[0][1]) : "memory"); SB();
  asm volatile("s_add_u32 m0, %0, 0" :  : "s"(mW) : "memory", "scc"); SB();
  asm volatile("v_mfma_f32_16x16x32_bf16 %0, v[180:183], %1, %0" : "+v"(c2[2]) : "v"(ones) : ); SB();
  asm volatile("ds_read_b128 a[104:107], %0 offset:32768" :  : "v"(aX[1][0]) : "memory"); SB();
  asm volatile("buffer_load_dwordx4 %0, %1, %2 offen lds" :  : "v"(voB[0]), "s"(rW), "s"(kt(8) * 196608u + 1536u) : "memory"); SB();
  asm volatile("v_mfma_f32_16x16x32_bf16 %0, %1, a[80:83], %0" : "+v"(rs) : "v"(ones) : ); SB();
  asm volatile("ds_read_b128 a[108:111], %0 offset:32768" :  : "v"(aX[1][1]) : "memory"); SB();
  asm volatile("v_mfma_f32_16x16x32_bf16 %0, a[80:83], a[80:83], %0" : "+v"(gr) :  : ); SB();
  asm volatile("v_mfma_f32_16x16x32_bf16 %0, %1, a[88:91], %0" : "+v"(rs) : "v"(ones) : ); SB();
  asm volatile("v_mfma_f32_16x16x32_bf16 %0, a[88:91], a[88:91], %0" : "+v"(gr) :  : ); SB();
  asm volatile("s_add_u32 m0, %0, 1024" :  : "s"(mW) : "memory", "scc"); SB();
  asm volatile("s_nop 0\n\tbuffer_load_dwordx4 %0, %1, %2 offen lds" :  : "v"(voB[1]), "s"(rW), "s"(kt(8) * 196608u + 1536u) : "memory"); SB();
  asm volatile("s_waitcnt lgkmcnt(8)\n\tv_mfma_f32_16x16x32_bf16 %0, v[160:163], a[64:67], %0" : "+v"(a2[0][0]) :  : ); SB();
  asm volatile("ds_read_b64_tr_b16 v[172:173], %0 offset:0" :  : "v"(aW[1][1][0]) : "memory"); SB();
  asm volatile("v_mfma_f32_16x16x32_bf16 %0, v[160:163], a[68:71], %0" : "+v"(a2[1][0]) :  : ); SB();
  asm volatile("ds_read_b64_tr_b16 v[174:175], %0 offset:768" :  : "v"(aW[1][1][0]) : "memory"); SB();
  asm volatile("v_mfma_f32_16x16x32_bf16 %0, v[160:163], %1, %0" : "+v"(c2[0]) : "v"(ones) : ); SB();
  asm volatile("ds_read_b64_tr_b16 v[176:177], %0 offset:0" :  : "v"(aW[1][1][1]) : "memory"); SB();
  asm volatile("s_waitcnt lgkmcnt(9)\n\tv_mfma_f32_16x16x32_bf16 %0, v[164:167], a[64:67], %0" : "+v"(a2[0][1]) :  : ); SB();
  asm volatile("ds_read_b64_tr_b16 v[178:179], %0 offset:768" :  : "v"(aW[1][1][1]) : "memory"); SB();
  asm volatile("s_add_u32 m0, %0, 2048" :  : "s"(mW) : "memory", "scc"); SB();
  asm volatile("v_mfma_f32_16x16x32_bf16 %0, v[164:167], a[68:71], %0" : "+v"(a2[1][1]) :  : ); SB();
  asm volatile("ds_read_b64_tr_b16 v[180:181], %0 offset:0" :  : "v"(aW[1][1][2]) : "memory"); SB();
  asm volatile("buffer_load_dwordx4 %0, %1, %2 offen lds" :  : "v"(voB[2]), "s"(rW), "s"(kt(8) * 196608u + 1536u) : "memory"); SB();
  asm volatile("v_mfma_f32_16x16x32_bf16 %0, v[164:167], %1, %0" : "+v"(c2[1]) : "v"(ones) : ); SB();
  asm volatile("ds_read_b64_tr_b16 v[182:183], %0 offset:768" :  : "v"(aW[1][1][2]) : "memory"); SB();
  asm volatile("s_waitcnt lgkmcnt(10)\n\tv_mfma_f32_16x16x32_bf16 %0, v[168:171], a[64:67], %0" : "+v"(a2[0][2]) :  : ); SB();
  asm volatile("ds_read_b128 a[112:115], %0 offset:40960" :  : "v"(aX[0][0]) : "memory"); SB();
  asm volatile("v_mfma_f32_16x16x32_bf16 %0, v[168:171], a[68:71], %0" : "+v"(a2[1][2]) :  : ); SB();
  asm volatile("ds_read_b128 a[116:119], %0 offset:40960" :  : "v"(aX[0][1]) : "memory"); SB();
  asm volatile("v_mfma_f32_16x16x32_bf16 %0, v[168:171], %1, %0" : "+v"(c2[2]) : "v"(ones) : ); SB();
  asm volatile("ds_read_b128 a[120:123], %0 offset:40960" :  : "v"(aX[1][0]) : "memory"); SB();
  asm volatile("s_waitcnt lgkmcnt(11)\n\tv_mfma_f32_16x16x32_bf16 %0, %1, a[96:99], %0" : "+v"(rs) : "v"(ones) : ); SB();
  asm volatile("ds_read_b128 a[124:127], %0 offset:40960" :  : "v"(aX[1][1]) : "memory"); SB();
  asm volatile("s_add_u32 m0, %0, 12288" :  : "s"(mW) : "memory", "scc"); SB();
  asm volatile("v_mfma_f32_16x16x32_bf16 %0, a[96:99], a[96:99], %0" : "+v"(gr) :  : ); SB();
  asm volatile("buffer_load_dwordx4 %0, %1, %2 offen lds" :  : "v"(voB[0]), "s"(rW), "s"(kt(9) * 196608u + 1536u) : "memory"); SB();
  asm volatile("s_waitcnt lgkmcnt(10)\n\tv_mfma_f32_16x16x32_bf16 %0, %1, a[104:107], %0" : "+v"(rs) : "v"(ones) : ); SB();
  asm volatile("v_mfma_f32_16x16x32_bf16 %0, a[104:107], a[104:107], %0" : "+v"(gr) :  : ); SB();
  asm volatile("s_add_u32 m0, %0, 13312" :  : "s"(mW) : "memory", "scc"); SB();
  asm volatile("s_nop 0\n\tbuffer_load_dwordx4 %0, %1, %2 offen lds" :  : "v"(voB[1]), "s"(rW), "s"(kt(9) * 196608u + 1536u) : "memory"); SB();
  asm volatile("s_waitcnt vmcnt(11) lgkmcnt(0)\n\ts_barrier" :  :  : "memory"); SB();
  asm volatile("s_add_u32 m0, %0, 14336" :  : "s"(mW) : "memory", "scc"); SB();
  asm volatile("s_nop 0\n\tbuffer_load_dwordx4 %0, %1, %2 offen lds" :  : "v"(voB[2]), "s"(rW), "s"(kt(9) * 196608u + 1536u) : "memory"); SB();
  asm volatile("v_mfma_f32_16x16x32_bf16 %0, v[172:175], a[72:75], %0" : "+v"(a2[0][0]) :  : ); SB();
  asm volatile("ds_read_b64_tr_b16 v[160:161], %0 offset:12288" :  : "v"(aW[1][0][0]) : "memory"); SB();
  asm volatile("v_mfma_f32_16x16x32_bf16 %0, v[172:175], a[76:79], %0" : "+v"(a2[1][0]) :  : ); SB();
  asm volatile("ds_read_b64_tr_b16 v[162:163], %0 offset:13056" :  : "v"(aW[1][0][0]) : "memory"); SB();
  asm volatile("v_mfma_f32_16x16x32_bf16 %0, v[172:175], %1, %0" : "+v"(c2[0]) : "v"(ones) : ); SB();
  asm volatile("ds_read_b64_tr_b16 v[164:165], %0 offset:12288" :  : "v"(aW[1][0][1]) : "memory"); SB();
  asm volatile("v_mfma_f32_16x16x32_bf16 %0, v[176:179], a[72:75], %0" : "+v"(a2[0][1]) :  : ); SB();
  asm volatile("ds_read_b64_tr_b16 v[166:167], %0 offset:13056" :  : "v"(aW[1][0][1]) : "memory"); SB();
  asm volatile("buffer_load_dwordx2 v[252:253], off, %0, 0 sc1" :  : "s"(rWS) : "memory"); SB();
  asm volatile("v_mfma_f32_16x16x32_bf16 %0, v[176:179], a[76:79], %0" : "+v"(a2[1][1]) :  : ); SB();
  asm volatile("ds_read_b64_tr_b16 v[168:169], %0 offset:12288" :  : "v"(aW[1][0][2]) : "memory"); SB();
  asm volatile("v_mfma_f32_16x16x32_bf16 %0, v[176:179], %1, %0" : "+v"(c2[1]) : "v"(ones) : ); SB();
  asm volatile("ds_read_b64_tr_b16 v[170:171], %0 offset:13056" :  : "v"(aW[1][0][2]) : "memory"); SB();
  asm volatile("v_mfma_f32_16x16x32_bf16 %0, v[180:183], a[72:75], %0" : "+v"(a2[0][2]) :  : ); SB();
  asm volatile("ds_read_b128 a[128:131], %0 offset:49152" :  : "v"(aX[0][0]) : "memory"); SB();
  asm volatile("v_mfma_f32_16x16x32_bf16 %0, v[180:183], a[76:79], %0" : "+v"(a2[1][2]) :  : ); SB();
  asm volatile("ds_read_b128 a[132:135], %0 offset:49152" :  : "v"(aX[0][1]) : "memory"); SB();
  asm volatile("v_mfma_f32_16x16x32_bf16 %0, v[180:183], %1, %0" : "+v"(c2[2]) : "v"(ones) : ); SB();
  asm volatile("ds_read_b128 a[136:139], %0 offset:49152" :  : "v"(aX[1][0]) : "memory"); SB();
  asm volatile("v_mfma_f32_16x16x32_bf16 %0, %1, a[112:115], %0" : "+v"(rs) : "v"(ones) : ); SB();
  asm volatile("ds_read_b128 a[140:143], %0 offset:49152" :  : "v"(aX[1][1]) : "memory"); SB();
  asm volatile("buffer_store_dwordx4 %1, %0, %2, %3 offen offset:0 sc1\n\ts_nop 1" :  : "v"(vH4_), "v"(hole4), "s"(rWS), "s"(0u) : "memory"); SB();
  asm volatile("v_mfma_f32_16x16x32_bf16 %0, a[112:115], a[112:115], %0" : "+v"(gr) :  : ); SB();
  asm volatile("v_mfma_f32_16x16x32_bf16 %0, %1, a[120:123], %0" : "+v"(rs) : "v"(ones) : ); SB();
  asm volatile("v_mfma_f32_16x16x32_bf16 %0, a[120:123], a[120:123], %0" : "+v"(gr) :  : ); SB();
  asm volatile("s_waitcnt lgkmcnt(8)\n\tv_mfma_f32_16x16x32_bf16 %0, v[160:163], a[80:83], %0" : "+v"(a2[0][0]) :  : ); SB();
  asm volatile("ds_read_b64_tr_b16 v[172:173], %0 offset:12288" :  : "v"(aW[1][1][0]) : "memory"); SB();
  asm volatile("v_mfma_f32_16x16x32_bf16 %0, v[160:163], a[84:87], %0" : "+v"(a2[1][0]) :  : ); SB();
  asm volatile("ds_read_b64_tr_b16 v[174:175], %0 offset:13056" :  : "v"(aW[1][1][0]) : "memory"); SB();
  asm volatile("v_mfma_f32_16x16x32_bf16 %0, v[160:163], %1, %0" : "+v"(c2[0]) : "v"(ones) : ); SB();
  asm volatile("ds_read_b64_tr_b16 v[176:177], %0 offset:12288" :  : "v"(aW[1][1][1]) : "memory"); SB();
  asm volatile("s_waitcnt lgkmcnt(9)\n\tv_mfma_f32_16x16x32_bf16 %0, v[164:167], a[80:83], %0" : "+v"(a2[0][1]) :  : ); SB();
  asm volatile("ds_read_b64_tr_b16 v[178:179], %0 offset:13056" :  : "v"(aW[1][1][1]) : "memory"); SB();
  asm volatile("v_mfma_f32_16x16x32_bf16 %0, v[164:167], a[84:87], %0" : "+v"(a2[1][1]) :  : ); SB();
  asm volatile("ds_read_b64_tr_b16 v[180:181], %0 offset:12288" :  : "v"(aW[1][1][2]) : "memory"); SB();
  asm volatile("v_mfma_f32_16x16x32_bf16 %0, v[164:167], %1, %0" : "+v"(c2[1]) : "v"(ones) : ); SB();
  asm volatile("ds_read_b64_tr_b16 v[182:183], %0 offset:13056" :  : "v"(aW[1][1][2]) : "memory"); SB();
  asm volatile("buffer_store_dwordx4 %1, %0, %2, %3 offen offset:0 sc1\n\ts_nop 1" :  : "v"(vH4_), "v"(hole4), "s"(rWS), "s"(24576u) : "memory"); SB();
  asm volatile("s_waitcnt lgkmcnt(10)\n\tv_mfma_f32_16x16x32_bf16 %0, v[168:171], a[80:83], %0" : "+v"(a2[0][2]) :  : ); SB();
  asm volatile("ds_read_b128 a[144:147], %0 offset:57344" :  : "v"(aX[0][0]) : "memory"); SB();
  asm volatile("v_mfma_f32_16x16x32_bf16 %0, v[168:171], a[84:87], %0" : "+v"(a2[1][2]) :  : ); SB();
  asm volatile("ds_read_b128 a[148:151], %0 offset:57344" :  : "v"(aX[0][1]) : "memory"); SB();
  asm volatile("v_mfma_f32_16x16x32_bf16 %0, v[168:171], %1, %0" : "+v"(c2[2]) : "v"(ones) : ); SB();
  asm volatile("ds_read_b128 a[152:155], %0 offset:57344" :  : "v"(aX[1][0]) : "memory"); SB();
  asm volatile("s_waitcnt lgkmcnt(11)\n\tv_mfma_f32_16x16x32_bf16 %0, %1, a[128:131], %0" : "+v"(rs) : "v"(ones) : ); SB();
  asm volatile("ds_read_b128 a[156:159], %0 offset:57344" :  : "v"(aX[1][1]) : "memory"); SB();
  asm volatile("v_mfma_f32_16x16x32_bf16 %0, a[128:131], a[128:131], %0" : "+v"(gr) :  : ); SB();
  asm volatile("s_waitcnt lgkmcnt(10)\n\tv_mfma_f32_16x16x32_bf16 %0, %1, a[136:139], %0" : "+v"(rs) : "v"(ones) : ); SB();
  asm volatile("v_mfma_f32_16x16x32_bf16 %0, a[136:139], a[136:139], %0" : "+v"(gr) :  : ); SB();
  asm volatile("s_waitcnt vmcnt(12) lgkmcnt(0)\n\ts_barrier" :  :  : "memory"); SB();
  asm volatile("buffer_store_dwordx4 %1, %0, %2, %3 offen offset:0 sc1\n\ts_nop 1" :  : "v"(vH2_), "v"(hole4), "s"(rWS), "s"(0u) : "memory"); SB();
  asm volatile("v_mfma_f32_16x16x32_bf16 %0, v[172:175], a[88:91], %0" : "+v"(a2[0][0]) :  : ); SB();
  asm volatile("ds_read_b64_tr_b16 v[160:161], %0 offset:24576" :  : "v"(aW[1][0][0]) : "memory"); SB();
  asm volatile("v_mfma_f32_16x16x32_bf16 %0, v[172:175], a[92:95], %0" : "+v"(a2[1][0]) :  : ); SB();
  asm volatile("ds_read_b64_tr_b16 v[162:163], %0 offset:25344" :  : "v"(aW[1][0][0]) : "memory"); SB();
  asm volatile("v_mfma_f32_16x16x32_bf16 %0, v[172:175], %1, %0" : "+v"(c2[0]) : "v"(ones) : ); SB();
  asm volatile("ds_read_b64_tr_b16 v[164:165], %0 offset:24576" :  : "v"(aW[1][0][1]) : "memory"); SB();
  asm volatile("v_mfma_f32_16x16x32_bf16 %0, v[176:179], a[88:91], %0" : "+v"(a2[0][1]) :  : ); SB();
  asm volatile("ds_read_b64_tr_b16 v[166:167], %0 offset:25344" :  : "v"(aW[1][0][1]) : "memory"); SB();
  asm volatile("v_mfma_f32_16x16x32_bf16 %0, v[176:179], a[92:95], %0" : "+v"(a2[1][1]) :  : ); SB();
  asm volatile("ds_read_b64_tr_b16 v[168:169], %0 offset:24576" :  : "v"(aW[1][0][2]) : "memory"); SB();
  asm volatile("v_mfma_f32_16x16x32_bf16 %0, v[176:179], %1, %0" : "+v"(c2[1]) : "v"(ones) : ); SB();
  asm volatile("ds_read_b64_tr_b16 v[170:171], %0 offset:25344" :  : "v"(aW[1][0][2]) : "memory"); SB();
  asm volatile("v_mfma_f32_16x16x32_bf16 %0, v[180:183], a[88:91], %0" : "+v"(a2[0][2]) :  : ); SB();
  asm volatile("v_mfma_f32_16x16x32_bf16 %0, v[180:183], a[92:95], %0" : "+v"(a2[1][2]) :  : ); SB();
  asm volatile("v_mfma_f32_16x16x32_bf16 %0, v[180:183], %1, %0" : "+v"(c2[2]) : "v"(ones) : ); SB();
  asm volatile("s_add_u32 m0, %0, 24576" :  : "s"(mW) : "memory", "scc"); SB();
  asm volatile("s_nop 0\n\tbuffer_load_dwordx4 %0, %1, %2 offen lds" :  : "v"(voB[0]), "s"(rW), "s"(kt(0) * 196608u + 0u) : "memory"); SB();
  asm volatile("v_mfma_f32_16x16x32_bf16 %0, %1, a[144:147], %0" : "+v"(rs) : "v"(ones) : ); SB();
  asm volatile("v_mfma_f32_16x16x32_bf16 %0, a[144:147], a[144:147], %0" : "+v"(gr) :  : ); SB();
  asm volatile("v_mfma_f32_16x16x32_bf16 %0, %1, a[152:155], %0" : "+v"(rs) : "v"(ones) : ); SB();
  asm volatile("v_mfma_f32_16x16x32_bf16 %0, a[152:155], a[152:155], %0" : "+v"(gr) :  : ); SB();
  asm volatile("s_waitcnt lgkmcnt(4)\n\tv_mfma_f32_16x16x32_bf16 %0, v[160:163], a[96:99], %0" : "+v"(a2[0][0]) :  : ); SB();
  asm volatile("ds_read_b64_tr_b16 v[172:173], %0 offset:24576" :  : "v"(aW[1][1][0]) : "memory"); SB();
  asm volatile("s_add_u32 m0, %0, 25600" :  : "s"(mW) : "memory", "scc"); SB();
  asm volatile("v_mfma_f32_16x16x32_bf16 %0, v[160:163], a[100:103], %0" : "+v"(a2[1][0]) :  : ); SB();
  asm volatile("ds_read_b64_tr_b16 v[174:175], %0 offset:25344" :  : "v"(aW[1][1][0]) : "memory"); SB();
  asm volatile("buffer_load_dwordx4 %0, %1, %2 offen lds" :  : "v"(voB[1]), "s"(rW), "s"(kt(0) * 196608u + 0u) : "memory"); SB();
  asm volatile("v_mfma_f32_16x16x32_bf16 %0, v[160:163], %1, %0" : "+v"(c2[0]) : "v"(ones) : ); SB();
  asm volatile("ds_read_b64_tr_b16 v[176:177], %0 offset:24576" :  : "v"(aW[1][1][1]) : "memory"); SB();
  asm volatile("s_waitcnt lgkmcnt(5)\n\tv_mfma_f32_16x16x32_bf16 %0, v[164:167], a[96:99], %0" : "+v"(a2[0][1]) :  : ); SB();
  asm volatile("ds_read_b64_tr_b16 v[178:179], %0 offset:25344" :  : "v"(aW[1][1][1]) : "memory"); SB();
  asm volatile("v_mfma_f32_16x16x32_bf16 %0, v[164:167], a[100:103], %0" : "+v"(a2[1][1]) :  : ); SB();
  asm volatile("ds_read_b64_tr_b16 v[180:181], %0 offset:24576" :  : "v"(aW[1][1][2]) : "memory"); SB();
  asm volatile("v_mfma_f32_16x16x32_bf16 %0, v[164:167], %1, %0" : "+v"(c2[1]) : "v"(ones) : ); SB();
  asm volatile("ds_read_b64_tr_b16 v[182:183], %0 offset:25344" :  : "v"(aW[1][1][2]) : "memory"); SB();
  asm volatile("s_waitcnt lgkmcnt(6)\n\tv_mfma_f32_16x16x32_bf16 %0, v[168:171], a[96:99], %0" : "+v"(a2[0][2]) :  : ); SB();
  asm volatile("v_mfma_f32_16x16x32_bf16 %0, v[168:171], a[100:103], %0" : "+v"(a2[1][2]) :  : ); SB();
  asm volatile("s_add_u32 m0, %0, 26624" :  : "s"(mW) : "memory", "scc"); SB();
  asm volatile("s_nop 0\n\tbuffer_load_dwordx4 %0, %1, %2 offen lds" :  : "v"(voB[2]), "s"(rW), "s"(kt(0) * 196608u + 0u) : "memory"); SB();
  asm volatile("v_mfma_f32_16x16x32_bf16 %0, v[168:171], %1, %0" : "+v"(c2[2]) : "v"(ones) : ); SB();
  asm volatile("s_nop 3" : "+v"(rs), "+v"(gr) :  : ); SB();
  {
    const float mean = rs[0] * (1.0f / 640.0f);
    const float dgv = (l15 & 2) ? ((l15 & 1) ? gr[3] : gr[2]) : ((l15 & 1) ? gr[1] : gr[0]);
    float sq = red32(red16(g4 == q4 ? dgv : 0.f)) - rs[0] * mean;
    if (__builtin_expect(__builtin_amdgcn_ballot_w64(!(sq >= 1e-3f * rs[0] * mean)) != 0, 0)) {
      const f32x2 mm = {mean, mean};
      f32x2 q2 = {0.f, 0.f};
      const u32x4* xp = (const u32x4*)(x + (size_t)(rb * 64 + w * 16 + l15) * 1280 + g4 * 320);
#pragma unroll 1
      for (int c = 0; c < 20; ++c) {
        u32x4 xq;
        asm volatile("global_load_dwordx4 %0, %1, off\n\ts_waitcnt vmcnt(0)" : "=&v"(xq) : "v"(xp + c) : "memory");
#pragma unroll
        for (int e = 0; e < 4; ++e) { const f32x2 d = unpk(xq[e]) - mm; q2 = __builtin_elementwise_fma(d, d, q2); }
      }
      sq = red32(red16(q2[0] + q2[1]));
    }
    xm[0] = rbf(mean);
    xd[0] = rbf(__builtin_amdgcn_sqrtf(rbf(rbf(fmaxf(sq, 0.f) * (1.0f / 640.0f)) + 1e-5f)));
    SB();
  }
  asm volatile("ds_write_b64 %0, %1 offset:0" :  : "v"(xw), "v"(((f32x2){xm[0], xd[0]})) : "memory"); SB();
  asm volatile("s_waitcnt vmcnt(13) lgkmcnt(0)\n\ts_barrier" :  :  : "memory"); SB();
  asm volatile("global_load_dwordx2 %0, %1, %2 offset:0" : "=&v"(sw[0]) : "v"(voH), "s"(shift_k) : "memory"); SB();
  asm volatile("global_load_dwordx2 %0, %1, %2 offset:128" : "=&v"(sw[1]) : "v"(voH), "s"(shift_k) : "memory"); SB();
  asm volatile("global_load_dwordx2 %0, %1, %2 offset:256" : "=&v"(sw[2]) : "v"(voH), "s"(shift_k) : "memory"); SB();
  asm volatile("global_load_dwordx2 %0, %1, %2 offset:384" : "=&v"(sw[3]) : "v"(voH), "s"(shift_k) : "memory"); SB();
  asm volatile("v_mfma_f32_16x16x32_bf16 %0, v[172:175], a[104:107], %0" : "+v"(a2[0][0]) :  : ); SB();
  asm volatile("ds_read_b64_tr_b16 v[160:161], %0 offset:36864" :  : "v"(aW[1][0][0]) : "memory"); SB();
  asm volatile("v_mfma_f32_16x16x32_bf16 %0, v[172:175], a[108:111], %0" : "+v"(a2[1][0]) :  : ); SB();
  asm volatile("ds_read_b64_tr_b16 v[162:163], %0 offset:37632" :  : "v"(aW[1][0][0]) : "memory"); SB();
  asm volatile("v_mfma_f32_16x16x32_bf16 %0, v[172:175], %1, %0" : "+v"(c2[0]) : "v"(ones) : ); SB();
  asm volatile("ds_read_b64_tr_b16 v[164:165], %0 offset:36864" :  : "v"(aW[1][0][1]) : "memory"); SB();
  asm volatile("v_mfma_f32_16x16x32_bf16 %0, v[176:179], a[104:107], %0" : "+v"(a2[0][1]) :  : ); SB();
  asm volatile("ds_read_b64_tr_b16 v[166:167], %0 offset:37632" :  : "v"(aW[1][0][1]) : "memory"); SB();
  asm volatile("global_load_dwordx2 %0, %1, %2 offset:512" : "=&v"(sw[4]) : "v"(voH), "s"(shift_k) : "memory"); SB();
  asm volatile("v_mfma_f32_16x16x32_bf16 %0, v[176:179], a[108:111], %0" : "+v"(a2[1][1]) :  : ); SB();
  asm volatile("ds_read_b64_tr_b16 v[168:169], %0 offset:36864" :  : "v"(aW[1][0][2]) : "memory"); SB();
  asm volatile("v_mfma_f32_16x16x32_bf16 %0, v[176:179], %1, %0" : "+v"(c2[1]) : "v"(ones) : ); SB();
  asm volatile("ds_read_b64_tr_b16 v[170:171], %0 offset:37632" :  : "v"(aW[1][0][2]) : "memory"); SB();
  asm volatile("v_mfma_f32_16x16x32_bf16 %0, v[180:183], a[104:107], %0" : "+v"(a2[0][2]) :  : ); SB();
  asm volatile("ds_read_b64 %0, %1 offset:0" : "=&v"(xo) : "v"(xr_) : "memory"); SB();
  asm volatile("s_add_u32 m0, %0, 106496" :  : "s"(mA) : "memory", "scc"); SB();
  asm volatile("v_mfma_f32_16x16x32_bf16 %0, v[180:183], a[108:111], %0" : "+v"(a2[1][2]) :  : ); SB();
  asm volatile("buffer_load_dwordx4 %0, %1, %2 offen lds" :  : "v"(voK[0]), "s"(rK), "s"(0u) : "memory"); SB();
  asm volatile("v_mfma_f32_16x16x32_bf16 %0, v[180:183], %1, %0" : "+v"(c2[2]) : "v"(ones) : ); SB();
  asm volatile("s_waitcnt lgkmcnt(5)\n\tv_mfma_f32_16x16x32_bf16 %0, v[160:163], a[112:115], %0" : "+v"(a2[0][0]) :  : ); SB();
  asm volatile("ds_read_b64_tr_b16 v[172:173], %0 offset:36864" :  : "v"(aW[1][1][0]) : "memory"); SB();
  asm volatile("v_mfma_f32_16x16x32_bf16 %0, v[160:163], a[116:119], %0" : "+v"(a2[1][0]) :  : ); SB();
  asm volatile("ds_read_b64_tr_b16 v[174:175], %0 offset:37632" :  : "v"(aW[1][1][0]) : "memory"); SB();
  asm volatile("v_mfma_f32_16x16x32_bf16 %0, v[160:163], %1, %0" : "+v"(c2[0]) : "v"(ones) : ); SB();
  asm volatile("ds_read_b64_tr_b16 v[176:177], %0 offset:36864" :  : "v"(aW[1][1][1]) : "memory"); SB();
  asm volatile("s_waitcnt lgkmcnt(6)\n\tv_mfma_f32_16x16x32_bf16 %0, v[164:167], a[112:115], %0" : "+v"(a2[0][1]) :  : ); SB();
  asm volatile("ds_read_b64_tr_b16 v[178:179], %0 offset:37632" :  : "v"(aW[1][1][1]) : "memory"); SB();
  asm volatile("v_mfma_f32_16x16x32_bf16 %0, v[164:167], a[116:119], %0" : "+v"(a2[1][1]) :  : ); SB();
  asm volatile("ds_read_b64_tr_b16 v[180:181], %0 offset:36864" :  : "v"(aW[1][1][2]) : "memory"); SB();
  asm volatile("v_mfma_f32_16x16x32_bf16 %0, v[164:167], %1, %0" : "+v"(c2[1]) : "v"(ones) : ); SB();
  asm volatile("ds_read_b64_tr_b16 v[182:183], %0 offset:37632" :  : "v"(aW[1][1][2]) : "memory"); SB();
  asm volatile("s_waitcnt lgkmcnt(7)\n\tv_mfma_f32_16x16x32_bf16 %0, v[168:171], a[112:115], %0" : "+v"(a2[0][2]) :  : ); SB();
  asm volatile("v_mfma_f32_16x16x32_bf16 %0, v[168:171], a[116:119], %0" : "+v"(a2[1][2]) :  : ); SB();
  asm volatile("v_mfma_f32_16x16x32_bf16 %0, v[168:171], %1, %0" : "+v"(c2[2]) : "v"(ones) : ); SB();
  asm volatile("s_waitcnt vmcnt(16) lgkmcnt(0)\n\ts_barrier" :  :  : "memory"); SB();
  asm volatile("s_add_u32 m0, %0, 107520" :  : "s"(mA) : "memory", "scc"); SB();
  asm volatile("s_nop 0\n\tbuffer_load_dwordx4 %0, %1, %2 offen lds" :  : "v"(voK[1]), "s"(rK), "s"(0u) : "memory"); SB();
  asm volatile("" : "+v"(xo) :  : ); SB();
  xm[1] = xo[0]; xd[1] = xo[1]; kset(); SB();
  asm volatile("v_mfma_f32_16x16x32_bf16 %0, v[172:175], a[120:123], %0" : "+v"(a2[0][0]) :  : ); SB();
  asm volatile("ds_read_b64_tr_b16 v[160:161], %0 offset:0" :  : "v"(aW[0][0][0]) : "memory"); SB();
  asm volatile("v_mfma_f32_16x16x32_bf16 %0, v[172:175], a[124:127], %0" : "+v"(a2[1][0]) :  : ); SB();
  asm volatile("ds_read_b64_tr_b16 v[162:163], %0 offset:768" :  : "v"(aW[0][0][0]) : "memory"); SB();
  asm volatile("v_mfma_f32_16x16x32_bf16 %0, v[172:175], %1, %0" : "+v"(c2[0]) : "v"(ones) : ); SB();
  asm volatile("ds_read_b64_tr_b16 v[164:165], %0 offset:0" :  : "v"(aW[0][0][1]) : "memory"); SB();
  asm volatile("v_mfma_f32_16x16x32_bf16 %0, v[176:179], a[120:123], %0" : "+v"(a2[0][1]) :  : ); SB();
  asm volatile("ds_read_b64_tr_b16 v[166:167], %0 offset:768" :  : "v"(aW[0][0][1]) : "memory"); SB();
  asm volatile("v_mfma_f32_16x16x32_bf16 %0, v[176:179], a[124:127], %0" : "+v"(a2[1][1]) :  : ); SB();
  asm volatile("ds_read_b64_tr_b16 v[168:169], %0 offset:0" :  : "v"(aW[0][0][2]) : "memory"); SB();
  asm volatile("v_mfma_f32_16x16x32_bf16 %0, v[176:179], %1, %0" : "+v"(c2[1]) : "v"(ones) : ); SB();
  asm volatile("ds_read_b64_tr_b16 v[170:171], %0 offset:768" :  : "v"(aW[0][0][2]) : "memory"); SB();
  asm volatile("v_mfma_f32_16x16x32_bf16 %0, v[180:183], a[120:123], %0" : "+v"(a2[0][2]) :  : ); SB();
  asm volatile("v_mfma_f32_16x16x32_bf16 %0, v[180:183], a[124:127], %0" : "+v"(a2[1][2]) :  : ); SB();
  asm volatile("v_mfma_f32_16x16x32_bf16 %0, v[180:183], %1, %0" : "+v"(c2[2]) : "v"(ones) : ); SB();
  asm volatile("s_waitcnt lgkmcnt(4)\n\tv_mfma_f32_16x16x32_bf16 %0, v[160:163], a[128:131], %0" : "+v"(a2[0][0]) :  : ); SB();
  asm volatile("ds_read_b64_tr_b16 v[172:173], %0 offset:0" :  : "v"(aW[0][1][0]) : "memory"); SB();
  asm volatile("v_mfma_f32_16x16x32_bf16 %0, v[160:163], a[132:135], %0" : "+v"(a2[1][0]) :  : ); SB();
  asm volatile("ds_read_b64_tr_b16 v[174:175], %0 offset:768" :  : "v"(aW[0][1][0]) : "memory"); SB();
  asm volatile("s_add_u32 m0, %0, 108544" :  : "s"(mA) : "memory", "scc"); SB();
  asm volatile("v_mfma_f32_16x16x32_bf16 %0, v[160:163], %1, %0" : "+v"(c2[0]) : "v"(ones) : ); SB();
  asm volatile("ds_read_b64_tr_b16 v[176:177], %0 offset:0" :  : "v"(aW[0][1][1]) : "memory"); SB();
  asm volatile("buffer_load_dwordx4 %0, %1, %2 offen lds" :  : "v"(voK[2]), "s"(rK), "s"(0u) : "memory"); SB();
  asm volatile("s_waitcnt lgkmcnt(5)\n\tv_mfma_f32_16x16x32_bf16 %0, v[164:167], a[128:131], %0" : "+v"(a2[0][1]) :  : ); SB();
  asm volatile("ds_read_b64_tr_b16 v[178:179], %0 offset:768" :  : "v"(aW[0][1][1]) : "memory"); SB();
  asm volatile("v_mfma_f32_16x16x32_bf16 %0, v[164:167], a[132:135], %0" : "+v"(a2[1][1]) :  : ); SB();
  asm volatile("ds_read_b64_tr_b16 v[180:181], %0 offset:0" :  : "v"(aW[0][1][2]) : "memory"); SB();
  asm volatile("v_mfma_f32_16x16x32_bf16 %0, v[164:167], %1, %0" : "+v"(c2[1]) : "v"(ones) : ); SB();
  asm volatile("ds_read_b64_tr_b16 v[182:183], %0 offset:768" :  : "v"(aW[0][1][2]) : "memory"); SB();
  asm volatile("s_waitcnt lgkmcnt(6)\n\tv_mfma_f32_16x16x32_bf16 %0, v[168:171], a[128:131], %0" : "+v"(a2[0][2]) :  : ); SB();
  asm volatile("v_mfma_f32_16x16x32_bf16 %0, v[168:171], a[132:135], %0" : "+v"(a2[1][2]) :  : ); SB();
  asm volatile("v_mfma_f32_16x16x32_bf16 %0, v[168:171], %1, %0" : "+v"(c2[2]) : "v"(ones) : ); SB();
  asm volatile("s_waitcnt vmcnt(15) lgkmcnt(0)\n\ts_barrier" :  :  : "memory"); SB();
  asm volatile("v_mfma_f32_16x16x32_bf16 %0, v[172:175], a[136:139], %0" : "+v"(a2[0][0]) :  : ); SB();
  asm volatile("ds_read_b64_tr_b16 v[160:161], %0 offset:12288" :  : "v"(aW[0][0][0]) : "memory"); SB();
  asm volatile("v_mfma_f32_16x16x32_bf16 %0, v[172:175], a[140:143], %0" : "+v"(a2[1][0]) :  : ); SB();
  asm volatile("ds_read_b64_tr_b16 v[162:163], %0 offset:13056" :  : "v"(aW[0][0][0]) : "memory"); SB();
  asm volatile("v_mfma_f32_16x16x32_bf16 %0, v[172:175], %1, %0" : "+v"(c2[0]) : "v"(ones) : ); SB();
  asm volatile("ds_read_b64_tr_b16 v[164:165], %0 offset:12288" :  : "v"(aW[0][0][1]) : "memory"); SB();
  asm volatile("s_add_u32 m0, %0, 109568" :  : "s"(mA) : "memory", "scc"); SB();
  asm volatile("v_mfma_f32_16x16x32_bf16 %0, v[176:179], a[136:139], %0" : "+v"(a2[0][1]) :  : ); SB();
  asm volatile("ds_read_b64_tr_b16 v[166:167], %0 offset:13056" :  : "v"(aW[0][0][1]) : "memory"); SB();
  asm volatile("buffer_load_dwordx4 %0, %1, %2 offen lds" :  : "v"(voK[3]), "s"(rK), "s"(0u) : "memory"); SB();
  asm volatile("v_mfma_f32_16x16x32_bf16 %0, v[176:179], a[140:143], %0" : "+v"(a2[1][1]) :  : ); SB();
  asm volatile("ds_read_b64_tr_b16 v[168:169], %0 offset:12288" :  : "v"(aW[0][0][2]) : "memory"); SB();
  asm volatile("v_mfma_f32_16x16x32_bf16 %0, v[176:179], %1, %0" : "+v"(c2[1]) : "v"(ones) : ); SB();
  asm volatile("ds_read_b64_tr_b16 v[170:171], %0 offset:13056" :  : "v"(aW[0][0][2]) : "memory"); SB();
  asm volatile("v_mfma_f32_16x16x32_bf16 %0, v[180:183], a[136:139], %0" : "+v"(a2[0][2]) :  : ); SB();
  asm volatile("v_mfma_f32_16x16x32_bf16 %0, v[180:183], a[140:143], %0" : "+v"(a2[1][2]) :  : ); SB();
  asm volatile("v_mfma_f32_16x16x32_bf16 %0, v[180:183], %1, %0" : "+v"(c2[2]) : "v"(ones) : ); SB();
  asm volatile("s_add_u32 m0, %0, 110592" :  : "s"(mA) : "memory", "scc"); SB();
  asm volatile("s_nop 0\n\tbuffer_load_dwordx4 %0, %1, %2 offen lds" :  : "v"(voV[0]), "s"(rV), "s"(0u) : "memory"); SB();
  asm volatile("s_waitcnt lgkmcnt(4)\n\tv_mfma_f32_16x16x32_bf16 %0, v[160:163], a[144:147], %0" : "+v"(a2[0][0]) :  : ); SB();
  asm volatile("ds_read_b64_tr_b16 v[172:173], %0 offset:12288" :  : "v"(aW[0][1][0]) : "memory"); SB();
  asm volatile("v_mfma_f32_16x16x32_bf16 %0, v[160:163], a[148:151], %0" : "+v"(a2[1][0]) :  : ); SB();
  asm volatile("ds_read_b64_tr_b16 v[174:175], %0 offset:13056" :  : "v"(aW[0][1][0]) : "memory"); SB();
  asm volatile("v_mfma_f32_16x16x32_bf16 %0, v[160:163], %1, %0" : "+v"(c2[0]) : "v"(ones) : ); SB();
  asm volatile("ds_read_b64_tr_b16 v[176:177], %0 offset:12288" :  : "v"(aW[0][1][1]) : "memory"); SB();
  asm volatile("s_waitcnt lgkmcnt(5)\n\tv_mfma_f32_16x16x32_bf16 %0, v[164:167], a[144:147], %0" : "+v"(a2[0][1]) :  : ); SB();
  asm volatile("ds_read_b64_tr_b16 v[178:179], %0 offset:13056" :  : "v"(aW[0][1][1]) : "memory"); SB();
  asm volatile("v_mfma_f32_16x16x32_bf16 %0, v[164:167], a[148:151], %0" : "+v"(a2[1][1]) :  : ); SB();
  asm volatile("ds_read_b64_tr_b16 v[180:181], %0 offset:12288" :  : "v"(aW[0][1][2]) : "memory"); SB();
  asm volatile("v_mfma_f32_16x16x32_bf16 %0, v[164:167], %1, %0" : "+v"(c2[1]) : "v"(ones) : ); SB();
  asm volatile("ds_read_b64_tr_b16 v[182:183], %0 offset:13056" :  : "v"(aW[0][1][2]) : "memory"); SB();
  asm volatile("s_add_u32 m0, %0, 111616" :  : "s"(mA) : "memory", "scc"); SB();
  asm volatile("s_waitcnt lgkmcnt(6)\n\tv_mfma_f32_16x16x32_bf16 %0, v[168:171], a[144:147], %0" : "+v"(a2[0][2]) :  : ); SB();
  asm volatile("buffer_load_dwordx4 %0, %1, %2 offen lds" :  : "v"(voV[1]), "s"(rV), "s"(0u) : "memory"); SB();
  asm volatile("v_mfma_f32_16x16x32_bf16 %0, v[168:171], a[148:151], %0" : "+v"(a2[1][2]) :  : ); SB();
  asm volatile("v_mfma_f32_16x16x32_bf16 %0, v[168:171], %1, %0" : "+v"(c2[2]) : "v"(ones) : ); SB();
  asm volatile("s_waitcnt lgkmcnt(4)\n\tv_mfma_f32_16x16x32_bf16 %0, v[172:175], a[152:155], %0" : "+v"(a2[0][0]) :  : ); SB();
  asm volatile("v_mfma_f32_16x16x32_bf16 %0, v[172:175], a[156:159], %0" : "+v"(a2[1][0]) :  : ); SB();
  asm volatile("v_mfma_f32_16x16x32_bf16 %0, v[172:175], %1, %0" : "+v"(c2[0]) : "v"(ones) : ); SB();
  asm volatile("s_add_u32 m0, %0, 112640" :  : "s"(mA) : "memory", "scc"); SB();
  asm volatile("s_nop 0\n\tbuffer_load_dwordx4 %0, %1, %2 offen lds" :  : "v"(voV[2]), "s"(rV), "s"(0u) : "memory"); SB();
  asm volatile("s_waitcnt lgkmcnt(2)\n\tv_mfma_f32_16x16x32_bf16 %0, v[176:179], a[152:155], %0" : "+v"(a2[0][1]) :  : ); SB();
  asm volatile("v_mfma_f32_16x16x32_bf16 %0, v[176:179], a[156:159], %0" : "+v"(a2[1][1]) :  : ); SB();
  asm volatile("v_mfma_f32_16x16x32_bf16 %0, v[176:179], %1, %0" : "+v"(c2[1]) : "v"(ones) : ); SB();
  asm volatile("s_waitcnt lgkmcnt(0)\n\tv_mfma_f32_16x16x32_bf16 %0, v[180:183], a[152:155], %0" : "+v"(a2[0][2]) :  : ); SB();
  asm volatile("v_mfma_f32_16x16x32_bf16 %0, v[180:183], a[156:159], %0" : "+v"(a2[1][2]) :  : ); SB();
  asm volatile("v_mfma_f32_16x16x32_bf16 %0, v[180:183], %1, %0" : "+v"(c2[2]) : "v"(ones) : ); SB();
  ST(5);
  asm volatile("s_waitcnt vmcnt(18)" :  :  : "memory"); SB();
  asm volatile("v_readfirstlane_b32 %0, v252\n\tv_readfirstlane_b32 %1, v253\n\ts_nop 0\n\ts_add_u32 %0, %0, 1\n\ts_addc_u32 %1, %1, 0" : "=&s"(tg0), "=&s"(tg1) :  : "memory", "scc"); SB();
  const unsigned long long tg = ((unsigned long long)tg1 << 32) | tg0;
  asm volatile("s_nop 3" : "+v"(a2[0][0]), "+v"(a2[0][1]), "+v"(a2[0][2]), "+v"(a2[1][0]), "+v"(a2[1][1]), "+v"(a2[1][2]), "+v"(c2[0]), "+v"(c2[1]), "+v"(c2[2]) :  : ); SB();
  auto act = [&](const f32x4& a, const f32x4& cs, int i, int h) {
    const f32x2 av = {a[2 * h], a[2 * h + 1]}, cv = {cs[2 * h], cs[2 * h + 1]};
    return gelu2(__builtin_elementwise_fma((f32x2){-xm[i], -xm[i]}, cv, av), k1[i], k2[i], xd[i]);
  };
  f32x2 gv[2][3][2];
  float sv[2][2], pv[2];
  gv[0][0][0] = act(a2[0][0], c2[0], 0, 0); gv[0][0][1] = act(a2[0][0], c2[0], 0, 1);
  asm volatile("" : "+v"(gv[0][0][0]), "+v"(gv[0][0][1]));
  SB();
  asm volatile("s_add_u32 m0, %0, 113664" :  : "s"(mA) : "memory", "scc"); SB();
  asm volatile("s_nop 0\n\tbuffer_load_dwordx4 %0, %1, %2 offen lds" :  : "v"(voO), "s"(rO), "s"(wok * 40800 + wof * 32 + cb * 160) : "memory"); SB();
  asm volatile("s_add_u32 m0, %0, 36864" :  : "s"(mW) : "memory", "scc"); SB();
  asm volatile("s_nop 0\n\tbuffer_load_dwordx4 %0, %1, %2 offen lds" :  : "v"(voB[0]), "s"(rW), "s"(kt(1) * 196608u + 0u) : "memory"); SB();
  gv[0][1][0] = act(a2[0][1], c2[1], 0, 0); gv[0][1][1] = act(a2[0][1], c2[1], 0, 1);
  asm volatile("" : "+v"(gv[0][1][0]), "+v"(gv[0][1][1]));
  SB();
  asm volatile("s_add_u32 m0, %0, 37888" :  : "s"(mW) : "memory", "scc"); SB();
  asm volatile("s_nop 0\n\tbuffer_load_dwordx4 %0, %1, %2 offen lds" :  : "v"(voB[1]), "s"(rW), "s"(kt(1) * 196608u + 0u) : "memory"); SB();
  asm volatile("s_add_u32 m0, %0, 38912" :  : "s"(mW) : "memory", "scc"); SB();
  asm volatile("s_nop 0\n\tbuffer_load_dwordx4 %0, %1, %2 offen lds" :  : "v"(voB[2]), "s"(rW), "s"(kt(1) * 196608u + 0u) : "memory"); SB();
  gv[0][2][0] = act(a2[0][2], c2[2], 0, 0); gv[0][2][1] = act(a2[0][2], c2[2], 0, 1);
  asm volatile("" : "+v"(gv[0][2][0]), "+v"(gv[0][2][1]));
  SB();
  asm volatile("s_add_u32 m0, %0, 49152" :  : "s"(mW) : "memory", "scc"); SB();
  asm volatile("s_nop 0\n\tbuffer_load_dwordx4 %0, %1, %2 offen lds" :  : "v"(voB[0]), "s"(rW), "s"(kt(2) * 196608u + 0u) : "memory"); SB();
  asm volatile("s_add_u32 m0, %0, 50176" :  : "s"(mW) : "memory", "scc"); SB();
  asm volatile("s_nop 0\n\tbuffer_load_dwordx4 %0, %1, %2 offen lds" :  : "v"(voB[1]), "s"(rW), "s"(kt(2) * 196608u + 0u) : "memory"); SB();
  gv[1][0][0] = act(a2[1][0], c2[0], 1, 0); gv[1][0][1] = act(a2[1][0], c2[0], 1, 1);
  asm volatile("" : "+v"(gv[1][0][0]), "+v"(gv[1][0][1]));
  SB();
  asm volatile("s_add_u32 m0, %0, 51200" :  : "s"(mW) : "memory", "scc"); SB();
  asm volatile("s_nop 0\n\tbuffer_load_dwordx4 %0, %1, %2 offen lds" :  : "v"(voB[2]), "s"(rW), "s"(kt(2) * 196608u + 0u) : "memory"); SB();
  gv[1][1][0] = act(a2[1][1], c2[1], 1, 0); gv[1][1][1] = act(a2[1][1], c2[1], 1, 1);
  asm volatile("" : "+v"(gv[1][1][0]), "+v"(gv[1][1][1]));
  SB();
  asm volatile("s_add_u32 m0, %0, 61440" :  : "s"(mW) : "memory", "scc"); SB();
  asm volatile("s_nop 0\n\tbuffer_load_dwordx4 %0, %1, %2 offen lds" :  : "v"(voB[0]), "s"(rW), "s"(kt(3) * 196608u + 0u) : "memory"); SB();
  gv[1][2][0] = act(a2[1][2], c2[2], 1, 0); gv[1][2][1] = act(a2[1][2], c2[2], 1, 1);
  asm volatile("" : "+v"(gv[1][2][0]), "+v"(gv[1][2][1]));
  SB();
#pragma unroll
  for (int i = 0; i < 2; ++i) {
    const unsigned p0 = __float_as_uint(gv[i][0][0][0]);
    const auto pa = __builtin_amdgcn_permlane16_swap(p0, p0, false, false);
    const auto pb = __builtin_amdgcn_permlane32_swap(pa[0], pa[0], false, false);
    pv[i] = __uint_as_float(pb[0]);
    const f32x2 pp = {pv[i], pv[i]};
    f32x2 s1 = gv[i][0][0] - pp, s2 = s1 * s1;
#pragma unroll
    for (int n = 1; n < 6; ++n) { const f32x2 d = gv[i][n >> 1][n & 1] - pp; s1 += d; s2 = __builtin_elementwise_fma(d, d, s2); }
    sv[i][0] = s1[0] + s1[1]; sv[i][1] = s2[0] + s2[1];
  }
  {
    const float t1 = red16(sel32(sv[0][0], sv[1][0])), t2 = red16(sel32(sv[0][1], sv[1][1]));
    const float dl = t1 * (1.0f / 48.0f);
    const u32x4 so = {__float_as_uint((lane < 32 ? pv[0] : pv[1]) + dl), __float_as_uint(fmaxf(t2 - t1 * dl, 0.f)), tg0, tg1};
    SB();
  asm volatile("s_waitcnt vmcnt(23)" :  :  : "memory"); SB();
  asm volatile("s_mov_b32 exec_lo, 0xffff\n\ts_mov_b32 exec_hi, 0xffff\n\tbuffer_store_dwordx4 %1, %0, %2, %3 offen offset:0 sc1\n\ts_mov_b64 exec, -1\n\ts_nop 0" :  : "v"(vo), "v"(so), "s"(rWS), "s"(0u) : "memory"); SB();
  }
  ST(6);
  { unsigned lane = lane0, w = w0, rb = rb0, cb = cb0;
  asm volatile("" : "+v"(lane)); asm volatile("" : "+s"(w), "+s"(rb), "+s"(cb)); SB();
  const unsigned wm = w >> 1, wn = w & 1, g4 = lane >> 4, l15 = lane & 15, q4 = l15 >> 2, p4 = l15 & 3;
  const unsigned nch = 3 - (rb >> 4), ch0 = cb * 80 + rb * 2 + min(rb, 16u);
  const unsigned l16 = lane * 16;
  const unsigned vZ = W_Z2 + ((rb * 2 + wm) * 48 + cb * 6 + wn * 3) * 1024 + l16;
  const unsigned vH4 = W_HH + ((rb * 4 + wm * 2) * 24 + cb * 3 + wn * 2) * 1024 + l16, vH2 = W_HH + ((rb * 4 + w) * 24 + cb * 3 + 1) * 1024 + l16;
  const unsigned vY = W_YY + ch0 * 4096 + w * 1024 + l16;
  const unsigned c2o = (nch - 2) * 2, c2s = 8192u + ((rb >> 4) << 30), w3s = (w + 1) >> 2 << 30;
  (void)wm; (void)wn; (void)g4; (void)q4; (void)p4; (void)vZ; (void)vH4; (void)vH2; (void)vY; (void)c2o; (void)c2s; (void)w3s;
  unsigned zp[2][3][2];
#pragma unroll
  for (int n = 0; n < 12; ++n) { zp[n / 6][(n >> 1) % 3][n & 1] = pk2(gv[n / 6][(n >> 1) % 3][n & 1]); asm volatile("" : "+v"(zp[n / 6][(n >> 1) % 3][n & 1])); }
  SB();
  asm volatile("s_add_u32 m0, %0, 62464" :  : "s"(mW) : "memory", "scc"); SB();
  asm volatile("s_nop 0\n\tbuffer_load_dwordx4 %0, %1, %2 offen lds" :  : "v"(voB[1]), "s"(rW), "s"(kt(3) * 196608u + 0u) : "memory"); SB();
  asm volatile("s_add_u32 m0, %0, 63488" :  : "s"(mW) : "memory", "scc"); SB();
  asm volatile("s_nop 0\n\tbuffer_load_dwordx4 %0, %1, %2 offen lds" :  : "v"(voB[2]), "s"(rW), "s"(kt(3) * 196608u + 0u) : "memory"); SB();
  asm volatile("s_add_u32 m0, %0, 73728" :  : "s"(mW) : "memory", "scc"); SB();
  asm volatile("s_nop 0\n\tbuffer_load_dwordx4 %0, %1, %2 offen lds" :  : "v"(voB[0]), "s"(rW), "s"(kt(4) * 196608u + 0u) : "memory"); SB();
  asm volatile("s_add_u32 m0, %0, 74752" :  : "s"(mW) : "memory", "scc"); SB();
  asm volatile("s_nop 0\n\tbuffer_load_dwordx4 %0, %1, %2 offen lds" :  : "v"(voB[1]), "s"(rW), "s"(kt(4) * 196608u + 0u) : "memory"); SB();
  asm volatile("s_add_u32 m0, %0, 75776" :  : "s"(mW) : "memory", "scc"); SB();
  asm volatile("s_nop 0\n\tbuffer_load_dwordx4 %0, %1, %2 offen lds" :  : "v"(voB[2]), "s"(rW), "s"(kt(4) * 196608u + 0u) : "memory"); SB();
  asm volatile("s_add_u32 m0, %0, 86016" :  : "s"(mW) : "memory", "scc"); SB();
  asm volatile("s_nop 0\n\tbuffer_load_dwordx4 %0, %1, %2 offen lds" :  : "v"(voB[0]), "s"(rW), "s"(kt(5) * 196608u + 0u) : "memory"); SB();
  asm volatile("s_waitcnt vmcnt(11) lgkmcnt(0)\n\ts_barrier" :  :  : "memory"); SB();
  asm volatile("ds_read_b64_tr_b16 v[160:161], %0 offset:24576" :  : "v"(aW[0][0][0]) : "memory"); SB();
  asm volatile("ds_read_b64_tr_b16 v[162:163], %0 offset:25344" :  : "v"(aW[0][0][0]) : "memory"); SB();
  asm volatile("ds_read_b64_tr_b16 v[164:165], %0 offset:24576" :  : "v"(aW[0][0][1]) : "memory"); SB();
  asm volatile("ds_read_b64_tr_b16 v[166:167], %0 offset:25344" :  : "v"(aW[0][0][1]) : "memory"); SB();
  asm volatile("ds_read_b64_tr_b16 v[168:169], %0 offset:24576" :  : "v"(aW[0][0][2]) : "memory"); SB();
  asm volatile("ds_read_b64_tr_b16 v[170:171], %0 offset:25344" :  : "v"(aW[0][0][2]) : "memory"); SB();
  u32x2 zt[3][2];
  const unsigned zwa[2] = {lb + 106496 + w * 16384 + (wn * 16 + l15) * 104 + g4 * 8, lb + 106496 + w * 16384 + ((wn ^ 1) * 16 + l15) * 104 + g4 * 8};
  const unsigned zra = lb + 106496 + w * 16384 + (8 * g4 + q4) * 104 + 8 * p4;
  const unsigned vS = W_ST + (rb * 2 + wm) * 8192 + l16;
  const auto rF = __builtin_amdgcn_make_buffer_rsrc((void*)ssm_k, 0, S_ * D_ * 2, 0x00020000);
  const unsigned vF = ch0 * 4096 + w * 1024 + l16, mU = lb + w * 1024;
  const unsigned ka = lb + 106496 + w * 16384 + (4 * p4 + q4) * 160 + 8 * g4, oa = lb + 106496 + 7168 + w * 16384 + (4 * g4 + q4) * 32 + 8 * p4;
  const unsigned kw = lb + 12288 + (4 * g4 + p4) * 144 + w * 32 + q4 * 8, kc0 = w * 24 + (lane >> 3), kr = lb + 12288 + kc0 * 144 + (lane & 7) * 16;
  const unsigned vKV = W_KV + (cb * 80 + kc0) * 4096 + rb * 128 + (lane & 7) * 16, vWO = W_WO + (cb * 120 + min(rb * 4 + w, 119u)) * 1024 + l16;
  const unsigned vFK = W_FK + cb * 1024 + w * 256 + (lane & 31) * 8;
  u32x2 kp[5], kc[5], vv[5], wt[2], ko[5];
  u32x4 kq[3];
  unsigned dD[3];
  const unsigned hpo = w ? ch0 * 4096 + w * 1024 - 4 : 0x40000000u;
  ST(7);
  SB();
  asm volatile("s_add_u32 m0, %0, 87040" :  : "s"(mW) : "memory", "scc"); SB();
  asm volatile("s_nop 0\n\tbuffer_load_dwordx4 %0, %1, %2 offen lds" :  : "v"(voB[1]), "s"(rW), "s"(kt(5) * 196608u + 0u) : "memory"); SB();
  asm volatile("s_add_u32 m0, %0, 88064" :  : "s"(mW) : "memory", "scc"); SB();
  asm volatile("s_nop 0\n\tbuffer_load_dwordx4 %0, %1, %2 offen lds" :  : "v"(voB[2]), "s"(rW), "s"(kt(5) * 196608u + 0u) : "memory"); SB();
  asm volatile("s_waitcnt lgkmcnt(4)\n\tv_mfma_f32_16x16x32_bf16 %0, v[160:163], a[0:3], 0" : "=&v"(a1[0][0]) :  : ); SB();
  asm volatile("ds_read_b64_tr_b16 v[172:173], %0 offset:24576" :  : "v"(aW[0][1][0]) : "memory"); SB();
  asm volatile("ds_read_b64_tr_b16 v[174:175], %0 offset:25344" :  : "v"(aW[0][1][0]) : "memory"); SB();
  asm volatile("s_add_u32 m0, %0, 0" :  : "s"(mW) : "memory", "scc"); SB();
  asm volatile("v_mfma_f32_16x16x32_bf16 %0, v[160:163], a[4:7], 0" : "=&v"(a1[1][0]) :  : ); SB();
  asm volatile("ds_read_b64_tr_b16 v[176:177], %0 offset:24576" :  : "v"(aW[0][1][1]) : "memory"); SB();
  asm volatile("ds_read_b64_tr_b16 v[178:179], %0 offset:25344" :  : "v"(aW[0][1][1]) : "memory"); SB();
  asm volatile("buffer_load_dwordx4 %0, %1, %2 offen lds" :  : "v"(voB[0]), "s"(rW), "s"(kt(6) * 196608u + 0u) : "memory"); SB();
  asm volatile("v_mfma_f32_16x16x32_bf16 %0, v[160:163], %1, 0" : "=&v"(c1[0]) : "v"(ones) : ); SB();
  asm volatile("ds_read_b64_tr_b16 v[180:181], %0 offset:24576" :  : "v"(aW[0][1][2]) : "memory"); SB();
  asm volatile("ds_read_b64_tr_b16 v[182:183], %0 offset:25344" :  : "v"(aW[0][1][2]) : "memory"); SB();
  asm volatile("s_add_u32 m0, %0, 1024" :  : "s"(mW) : "memory", "scc"); SB();
  asm volatile("s_waitcnt lgkmcnt(8)\n\tv_mfma_f32_16x16x32_bf16 %0, v[164:167], a[0:3], 0" : "=&v"(a1[0][1]) :  : ); SB();
  asm volatile("" : "+v"(sw[0]), "+v"(sw[1]), "+v"(sw[2]), "+v"(sw[3]), "+v"(sw[4]) :  : "memory"); SB();
  asm volatile("ds_read_b64_tr_b16 %0, %1 offset:0" : "=&v"(kp[0]) : "v"(ka) : "memory"); SB();
  asm volatile("buffer_load_dwordx4 %0, %1, %2 offen lds" :  : "v"(voB[1]), "s"(rW), "s"(kt(6) * 196608u + 0u) : "memory"); SB();
  asm volatile("v_mfma_f32_16x16x32_bf16 %0, v[164:167], a[4:7], 0" : "=&v"(a1[1][1]) :  : ); SB();
  asm volatile("ds_read_b64_tr_b16 %0, %1 offset:640" : "=&v"(kc[0]) : "v"(ka) : "memory"); SB();
  asm volatile("ds_read_b64_tr_b16 %0, %1 offset:4096" : "=&v"(vv[0]) : "v"(ka) : "memory"); SB();
  asm volatile("s_add_u32 m0, %0, 2048" :  : "s"(mW) : "memory", "scc"); SB();
  asm volatile("v_mfma_f32_16x16x32_bf16 %0, v[164:167], %1, 0" : "=&v"(c1[1]) : "v"(ones) : ); SB();
  asm volatile("ds_read_b64_tr_b16 %0, %1 offset:32" : "=&v"(kp[1]) : "v"(ka) : "memory"); SB();
  asm volatile("ds_read_b64_tr_b16 %0, %1 offset:672" : "=&v"(kc[1]) : "v"(ka) : "memory"); SB();
  asm volatile("buffer_load_dwordx4 %0, %1, %2 offen lds" :  : "v"(voB[2]), "s"(rW), "s"(kt(6) * 196608u + 0u) : "memory"); SB();
  asm volatile("s_waitcnt lgkmcnt(11)\n\tv_mfma_f32_16x16x32_bf16 %0, v[168:171], a[0:3], 0" : "=&v"(a1[0][2]) :  : ); SB();
  asm volatile("ds_read_b64_tr_b16 %0, %1 offset:4128" : "=&v"(vv[1]) : "v"(ka) : "memory"); SB();
  asm volatile("ds_read_b64_tr_b16 %0, %1 offset:64" : "=&v"(kp[2]) : "v"(ka) : "memory"); SB();
  asm volatile("v_mfma_f32_16x16x32_bf16 %0, v[168:171], a[4:7], 0" : "=&v"(a1[1][2]) :  : ); SB();
  asm volatile("ds_read_b64_tr_b16 %0, %1 offset:704" : "=&v"(kc[2]) : "v"(ka) : "memory"); SB();
  asm volatile("ds_read_b64_tr_b16 %0, %1 offset:4160" : "=&v"(vv[2]) : "v"(ka) : "memory"); SB();
  asm volatile("v_mfma_f32_16x16x32_bf16 %0, v[168:171], %1, 0" : "=&v"(c1[2]) : "v"(ones) : ); SB();
  asm volatile("ds_read_b64_tr_b16 %0, %1 offset:96" : "=&v"(kp[3]) : "v"(ka) : "memory"); SB();
  asm volatile("ds_read_b64_tr_b16 %0, %1 offset:736" : "=&v"(kc[3]) : "v"(ka) : "memory"); SB();
  asm volatile("s_waitcnt lgkmcnt(15)\n\tv_mfma_f32_16x16x32_bf16 %0, v[172:175], a[8:11], %0" : "+v"(a1[0][0]) :  : ); SB();
  asm volatile("ds_read_b64_tr_b16 v[160:161], %0 offset:36864" :  : "v"(aW[0][0][0]) : "memory"); SB();
  asm volatile("ds_read_b64_tr_b16 v[162:163], %0 offset:37632" :  : "v"(aW[0][0][0]) : "memory"); SB();
  asm volatile("v_mfma_f32_16x16x32_bf16 %0, v[172:175], a[12:15], %0" : "+v"(a1[1][0]) :  : ); SB();
  asm volatile("ds_read_b64_tr_b16 v[164:165], %0 offset:36864" :  : "v"(aW[0][0][1]) : "memory"); SB();
  asm volatile("ds_read_b64_tr_b16 v[166:167], %0 offset:37632" :  : "v"(aW[0][0][1]) : "memory"); SB();
  asm volatile("s_waitcnt lgkmcnt(15)\n\tv_mfma_f32_16x16x32_bf16 %0, v[176:179], a[8:11], %0" : "+v"(a1[0][1]) :  : ); SB();
  asm volatile("ds_read_b64_tr_b16 v[168:169], %0 offset:36864" :  : "v"(aW[0][0][2]) : "memory"); SB();
  asm volatile("ds_read_b64_tr_b16 v[170:171], %0 offset:37632" :  : "v"(aW[0][0][2]) : "memory"); SB();
  asm volatile("v_mfma_f32_16x16x32_bf16 %0, v[176:179], a[12:15], %0" : "+v"(a1[1][1]) :  : ); SB();
  asm volatile("ds_read_b64_tr_b16 %0, %1 offset:4192" : "=&v"(vv[3]) : "v"(ka) : "memory"); SB();
  asm volatile("ds_read_b64_tr_b16 %0, %1 offset:128" : "=&v"(kp[4]) : "v"(ka) : "memory"); SB();
  asm volatile("v_mfma_f32_16x16x32_bf16 %0, v[180:183], a[8:11], %0" : "+v"(a1[0][2]) :  : ); SB();
  asm volatile("ds_read_b64_tr_b16 %0, %1 offset:768" : "=&v"(kc[4]) : "v"(ka) : "memory"); SB();
  asm volatile("ds_read_b64_tr_b16 %0, %1 offset:4224" : "=&v"(vv[4]) : "v"(ka) : "memory"); SB();
  asm volatile("v_mfma_f32_16x16x32_bf16 %0, v[180:183], a[12:15], %0" : "+v"(a1[1][2]) :  : ); SB();
  asm volatile("ds_read_b64_tr_b16 %0, %1 offset:0" : "=&v"(wt[0]) : "v"(oa) : "memory"); SB();
  asm volatile("ds_read_b64_tr_b16 %0, %1 offset:512" : "=&v"(wt[1]) : "v"(oa) : "memory"); SB();
  asm volatile("s_waitcnt lgkmcnt(0)" : "+v"(kp[0]), "+v"(kc[0]), "+v"(vv[0]), "+v"(kp[1]), "+v"(kc[1]), "+v"(vv[1]), "+v"(kp[2]), "+v"(kc[2]), "+v"(vv[2]), "+v"(kp[3]), "+v"(kc[3]), "+v"(vv[3]), "+v"(kp[4]), "+v"(kc[4]), "+v"(vv[4]), "+v"(wt[0]), "+v"(wt[1]) :  : "memory"); SB();
  asm volatile("ds_write_b64 %0, %1 offset:0" :  : "v"(zwa[0]), "v"(((u32x2){zp[0][0][0], zp[0][0][1]})) : "memory"); SB();
  asm volatile("ds_write_b64 %0, %1 offset:32" :  : "v"(zwa[0]), "v"(((u32x2){zp[0][1][0], zp[0][1][1]})) : "memory"); SB();
  asm volatile("ds_write_b64 %0, %1 offset:64" :  : "v"(zwa[0]), "v"(((u32x2){zp[0][2][0], zp[0][2][1]})) : "memory"); SB();
  asm volatile("ds_write_b64 %0, %1 offset:0" :  : "v"(zwa[1]), "v"(((u32x2){zp[1][0][0], zp[1][0][1]})) : "memory"); SB();
  asm volatile("ds_write_b64 %0, %1 offset:32" :  : "v"(zwa[1]), "v"(((u32x2){zp[1][1][0], zp[1][1][1]})) : "memory"); SB();
  asm volatile("ds_write_b64 %0, %1 offset:64" :  : "v"(zwa[1]), "v"(((u32x2){zp[1][2][0], zp[1][2][1]})) : "memory"); SB();
  asm volatile("ds_read_b64_tr_b16 %0, %1 offset:0" : "=&v"(zt[0][0]) : "v"(zra) : "memory"); SB();
  asm volatile("ds_read_b64_tr_b16 %0, %1 offset:416" : "=&v"(zt[0][1]) : "v"(zra) : "memory"); SB();
  asm volatile("ds_read_b64_tr_b16 %0, %1 offset:32" : "=&v"(zt[1][0]) : "v"(zra) : "memory"); SB();
  asm volatile("ds_read_b64_tr_b16 %0, %1 offset:448" : "=&v"(zt[1][1]) : "v"(zra) : "memory"); SB();
  asm volatile("ds_read_b64_tr_b16 %0, %1 offset:64" : "=&v"(zt[2][0]) : "v"(zra) : "memory"); SB();
  asm volatile("ds_read_b64_tr_b16 %0, %1 offset:480" : "=&v"(zt[2][1]) : "v"(zra) : "memory"); SB();
  asm volatile("buffer_store_dwordx4 %1, %0, %2, %3 offen offset:0 sc1\n\ts_nop 1" :  : "v"(vWO), "v"(((u32x4){wt[0][0], wt[0][1], wt[1][0], wt[1][1]})), "s"(rWS), "s"(0u) : "memory"); SB();
  {
    const unsigned w10 = shr16(sw[0][0], sw[0][0]), w32 = shr16(sw[0][1], sw[0][1]);
    const unsigned s0 = shr16(kp[0][1], kp[0][0]), s1 = shr16(kc[0][0], kp[0][1]), s2 = shr16(kc[0][1], kc[0][0]);
    const float r0 = dot2(s1, w10, dot2(s0, w32, 0.f)), r1 = dot2(kc[0][0], w10, dot2(kp[0][1], w32, 0.f));
    const float r2 = dot2(s2, w10, dot2(s1, w32, 0.f)), r3 = dot2(kc[0][1], w10, dot2(kc[0][0], w32, 0.f));
    ko[0] = (u32x2){pk2((f32x2){r0, r1} * unpk(vv[0][0])), pk2((f32x2){r2, r3} * unpk(vv[0][1]))};
    asm volatile("" : "+v"(ko[0]));
  }
  SB();
  asm volatile("s_add_u32 m0, %0, 98304" :  : "s"(mA) : "memory", "scc"); SB();
  asm volatile("s_nop 0\n\tbuffer_load_dwordx4 %0, %1, %2 offen lds" :  : "v"(voS), "s"(rA), "s"((0 ^ (2 * w)) * 32768) : "memory"); SB();
  asm volatile("s_waitcnt lgkmcnt(0)" : "+v"(zt[0][0]), "+v"(zt[0][1]), "+v"(zt[1][0]), "+v"(zt[1][1]), "+v"(zt[2][0]), "+v"(zt[2][1]) :  : "memory"); SB();
  asm volatile("ds_write_b64 %0, %1 offset:0" :  : "v"(kw), "v"(ko[0]) : "memory"); SB();
  {
    const unsigned w10 = shr16(sw[1][0], sw[1][0]), w32 = shr16(sw[1][1], sw[1][1]);
    const unsigned s0 = shr16(kp[1][1], kp[1][0]), s1 = shr16(kc[1][0], kp[1][1]), s2 = shr16(kc[1][1], kc[1][0]);
    const float r0 = dot2(s1, w10, dot2(s0, w32, 0.f)), r1 = dot2(kc[1][0], w10, dot2(kp[1][1], w32, 0.f));
    const float r2 = dot2(s2, w10, dot2(s1, w32, 0.f)), r3 = dot2(kc[1][1], w10, dot2(kc[1][0], w32, 0.f));
    ko[1] = (u32x2){pk2((f32x2){r0, r1} * unpk(vv[1][0])), pk2((f32x2){r2, r3} * unpk(vv[1][1]))};
    asm volatile("" : "+v"(ko[1]));
  }
  SB();
  asm volatile("s_add_u32 m0, %0, 99328" :  : "s"(mA) : "memory", "scc"); SB();
  asm volatile("s_nop 0\n\tbuffer_load_dwordx4 %0, %1, %2 offen lds" :  : "v"(voS), "s"(rA), "s"((1 ^ (2 * w)) * 32768) : "memory"); SB();
  asm volatile("s_add_u32 m0, %0, 100352" :  : "s"(mA) : "memory", "scc"); SB();
  asm volatile("s_nop 0\n\tbuffer_load_dwordx4 %0, %1, %2 offen lds" :  : "v"(voS), "s"(rA), "s"((2 ^ (2 * w)) * 32768) : "memory"); SB();
  asm volatile("ds_write_b64 %0, %1 offset:2304" :  : "v"(kw), "v"(ko[1]) : "memory"); SB();
  {
    const unsigned w10 = shr16(sw[2][0], sw[2][0]), w32 = shr16(sw[2][1], sw[2][1]);
    const unsigned s0 = shr16(kp[2][1], kp[2][0]), s1 = shr16(kc[2][0], kp[2][1]), s2 = shr16(kc[2][1], kc[2][0]);
    const float r0 = dot2(s1, w10, dot2(s0, w32, 0.f)), r1 = dot2(kc[2][0], w10, dot2(kp[2][1], w32, 0.f));
    const float r2 = dot2(s2, w10, dot2(s1, w32, 0.f)), r3 = dot2(kc[2][1], w10, dot2(kc[2][0], w32, 0.f));
    ko[2] = (u32x2){pk2((f32x2){r0, r1} * unpk(vv[2][0])), pk2((f32x2){r2, r3} * unpk(vv[2][1]))};
    asm volatile("" : "+v"(ko[2]));
  }
  SB();
  asm volatile("s_add_u32 m0, %0, 101376" :  : "s"(mA) : "memory", "scc"); SB();
  asm volatile("s_nop 0\n\tbuffer_load_dwordx4 %0, %1, %2 offen lds" :  : "v"(voS), "s"(rA), "s"((3 ^ (2 * w)) * 32768) : "memory"); SB();
  asm volatile("ds_write_b64 %0, %1 offset:4608" :  : "v"(kw), "v"(ko[2]) : "memory"); SB();
  {
    const unsigned w10 = shr16(sw[3][0], sw[3][0]), w32 = shr16(sw[3][1], sw[3][1]);
    const unsigned s0 = shr16(kp[3][1], kp[3][0]), s1 = shr16(kc[3][0], kp[3][1]), s2 = shr16(kc[3][1], kc[3][0]);
    const float r0 = dot2(s1, w10, dot2(s0, w32, 0.f)), r1 = dot2(kc[3][0], w10, dot2(kp[3][1], w32, 0.f));
    const float r2 = dot2(s2, w10, dot2(s1, w32, 0.f)), r3 = dot2(kc[3][1], w10, dot2(kc[3][0], w32, 0.f));
    ko[3] = (u32x2){pk2((f32x2){r0, r1} * unpk(vv[3][0])), pk2((f32x2){r2, r3} * unpk(vv[3][1]))};
    asm volatile("" : "+v"(ko[3]));
  }
  SB();
  asm volatile("s_add_u32 m0, %0, 102400" :  : "s"(mA) : "memory", "scc"); SB();
  asm volatile("s_nop 0\n\tbuffer_load_dwordx4 %0, %1, %2 offen lds" :  : "v"(voS), "s"(rA), "s"((4 ^ (2 * w)) * 32768) : "memory"); SB();
  asm volatile("s_add_u32 m0, %0, 103424" :  : "s"(mA) : "memory", "scc"); SB();
  asm volatile("s_nop 0\n\tbuffer_load_dwordx4 %0, %1, %2 offen lds" :  : "v"(voS), "s"(rA), "s"((5 ^ (2 * w)) * 32768) : "memory"); SB();
  asm volatile("ds_write_b64 %0, %1 offset:6912" :  : "v"(kw), "v"(ko[3]) : "memory"); SB();
  {
    const unsigned w10 = shr16(sw[4][0], sw[4][0]), w32 = shr16(sw[4][1], sw[4][1]);
    const unsigned s0 = shr16(kp[4][1], kp[4][0]), s1 = shr16(kc[4][0], kp[4][1]), s2 = shr16(kc[4][1], kc[4][0]);
    const float r0 = dot2(s1, w10, dot2(s0, w32, 0.f)), r1 = dot2(kc[4][0], w10, dot2(kp[4][1], w32, 0.f));
    const float r2 = dot2(s2, w10, dot2(s1, w32, 0.f)), r3 = dot2(kc[4][1], w10, dot2(kc[4][0], w32, 0.f));
    ko[4] = (u32x2){pk2((f32x2){r0, r1} * unpk(vv[4][0])), pk2((f32x2){r2, r3} * unpk(vv[4][1]))};
    asm volatile("" : "+v"(ko[4]));
  }
  SB();
  asm volatile("ds_write_b64 %0, %1 offset:9216" :  : "v"(kw), "v"(ko[4]) : "memory"); SB();
  asm volatile("v_mfma_f32_16x16x32_bf16 %0, v[160:163], a[16:19], %0" : "+v"(a1[0][0]) :  : ); SB();
  asm volatile("ds_read_b64_tr_b16 v[172:173], %0 offset:36864" :  : "v"(aW[0][1][0]) : "memory"); SB();
  asm volatile("ds_read_b64_tr_b16 v[174:175], %0 offset:37632" :  : "v"(aW[0][1][0]) : "memory"); SB();
  asm volatile("v_mfma_f32_16x16x32_bf16 %0, v[160:163], a[20:23], %0" : "+v"(a1[1][0]) :  : ); SB();
  asm volatile("ds_read_b64_tr_b16 v[176:177], %0 offset:36864" :  : "v"(aW[0][1][1]) : "memory"); SB();
  asm volatile("ds_read_b64_tr_b16 v[178:179], %0 offset:37632" :  : "v"(aW[0][1][1]) : "memory"); SB();
  asm volatile("v_mfma_f32_16x16x32_bf16 %0, v[160:163], %1, %0" : "+v"(c1[0]) : "v"(ones) : ); SB();
  asm volatile("ds_read_b64_tr_b16 v[180:181], %0 offset:36864" :  : "v"(aW[0][1][2]) : "memory"); SB();
  asm volatile("ds_read_b64_tr_b16 v[182:183], %0 offset:37632" :  : "v"(aW[0][1][2]) : "memory"); SB();
  asm volatile("v_mfma_f32_16x16x32_bf16 %0, v[164:167], a[16:19], %0" : "+v"(a1[0][1]) :  : ); SB();
  asm volatile("v_mfma_f32_16x16x32_bf16 %0, v[164:167], a[20:23], %0" : "+v"(a1[1][1]) :  : ); SB();
  asm volatile("s_add_u32 m0, %0, 104448" :  : "s"(mA) : "memory", "scc"); SB();
  asm volatile("s_nop 0\n\tbuffer_load_dwordx4 %0, %1, %2 offen lds" :  : "v"(voS), "s"(rA), "s"((6 ^ (2 * w)) * 32768) : "memory"); SB();
  asm volatile("v_mfma_f32_16x16x32_bf16 %0, v[164:167], %1, %0" : "+v"(c1[1]) : "v"(ones) : ); SB();
  asm volatile("v_mfma_f32_16x16x32_bf16 %0, v[168:171], a[16:19], %0" : "+v"(a1[0][2]) :  : ); SB();
  asm volatile("v_mfma_f32_16x16x32_bf16 %0, v[168:171], a[20:23], %0" : "+v"(a1[1][2]) :  : ); SB();
  asm volatile("v_mfma_f32_16x16x32_bf16 %0, v[168:171], %1, %0" : "+v"(c1[2]) : "v"(ones) : ); SB();
  asm volatile("s_waitcnt vmcnt(17) lgkmcnt(0)\n\ts_barrier" :  :  : "memory"); SB();
  asm volatile("v_mfma_f32_16x16x32_bf16 %0, v[172:175], a[24:27], %0" : "+v"(a1[0][0]) :  : ); SB();
  asm volatile("ds_read_b64_tr_b16 v[160:161], %0 offset:0" :  : "v"(aW[1][0][0]) : "memory"); SB();
  asm volatile("ds_read_b64_tr_b16 v[162:163], %0 offset:768" :  : "v"(aW[1][0][0]) : "memory"); SB();
  asm volatile("s_add_u32 m0, %0, 105472" :  : "s"(mA) : "memory", "scc"); SB();
  asm volatile("v_mfma_f32_16x16x32_bf16 %0, v[172:175], a[28:31], %0" : "+v"(a1[1][0]) :  : ); SB();
  asm volatile("ds_read_b64_tr_b16 v[164:165], %0 offset:0" :  : "v"(aW[1][0][1]) : "memory"); SB();
  asm volatile("ds_read_b64_tr_b16 v[166:167], %0 offset:768" :  : "v"(aW[1][0][1]) : "memory"); SB();
  asm volatile("buffer_load_dwordx4 %0, %1, %2 offen lds" :  : "v"(voS), "s"(rA), "s"((7 ^ (2 * w)) * 32768) : "memory"); SB();
  asm volatile("v_mfma_f32_16x16x32_bf16 %0, v[176:179], a[24:27], %0" : "+v"(a1[0][1]) :  : ); SB();
  asm volatile("ds_read_b64_tr_b16 v[168:169], %0 offset:0" :  : "v"(aW[1][0][2]) : "memory"); SB();
  asm volatile("ds_read_b64_tr_b16 v[170:171], %0 offset:768" :  : "v"(aW[1][0][2]) : "memory"); SB();
  asm volatile("v_mfma_f32_16x16x32_bf16 %0, v[176:179], a[28:31], %0" : "+v"(a1[1][1]) :  : ); SB();
  asm volatile("ds_read_b128 %0, %1 offset:0" : "=&v"(kq[0]) : "v"(kr) : "memory"); SB();
  asm volatile("ds_read_b128 %0, %1 offset:1152" : "=&v"(kq[1]) : "v"(kr) : "memory"); SB();
  asm volatile("s_add_u32 m0, %0, 106368" :  : "s"(mA) : "memory", "scc"); SB();
  asm volatile("v_mfma_f32_16x16x32_bf16 %0, v[180:183], a[24:27], %0" : "+v"(a1[0][2]) :  : ); SB();
  asm volatile("ds_read_b128 %0, %1 offset:2304" : "=&v"(kq[2]) : "v"(kr) : "memory"); SB();
  asm volatile("buffer_load_dwordx4 %0, %1, %2 offen offset:128 lds" :  : "v"(voS), "s"(rA), "s"((0 ^ (2 * w)) * 32768) : "memory"); SB();
  asm volatile("v_mfma_f32_16x16x32_bf16 %0, v[180:183], a[28:31], %0" : "+v"(a1[1][2]) :  : ); SB();
  asm volatile("s_waitcnt lgkmcnt(7)\n\tv_mfma_f32_16x16x32_bf16 %0, v[160:163], a[32:35], %0" : "+v"(a1[0][0]) :  : ); SB();
  asm volatile("ds_read_b64_tr_b16 v[172:173], %0 offset:0" :  : "v"(aW[1][1][0]) : "memory"); SB();
  asm volatile("ds_read_b64_tr_b16 v[174:175], %0 offset:768" :  : "v"(aW[1][1][0]) : "memory"); SB();
  asm volatile("v_mfma_f32_16x16x32_bf16 %0, v[160:163], a[36:39], %0" : "+v"(a1[1][0]) :  : ); SB();
  asm volatile("ds_read_b64_tr_b16 v[176:177], %0 offset:0" :  : "v"(aW[1][1][1]) : "memory"); SB();
  asm volatile("ds_read_b64_tr_b16 v[178:179], %0 offset:768" :  : "v"(aW[1][1][1]) : "memory"); SB();
  asm volatile("v_mfma_f32_16x16x32_bf16 %0, v[160:163], %1, %0" : "+v"(c1[0]) : "v"(ones) : ); SB();
  asm volatile("ds_read_b64_tr_b16 v[180:181], %0 offset:0" :  : "v"(aW[1][1][2]) : "memory"); SB();
  asm volatile("ds_read_b64_tr_b16 v[182:183], %0 offset:768" :  : "v"(aW[1][1][2]) : "memory"); SB();
  asm volatile("buffer_load_dwordx4 v[184:187], %0, %1, %2 offen offset:0 sc1" :  : "v"(vS), "s"(rWS), "s"(0u) : "memory"); SB();
  asm volatile("s_waitcnt lgkmcnt(11)\n\tv_mfma_f32_16x16x32_bf16 %0, v[164:167], a[32:35], %0" : "+v"(a1[0][1]) :  : ); SB();
  asm volatile("s_waitcnt lgkmcnt(6)" : "+v"(kq[0]), "+v"(kq[1]), "+v"(kq[2]) :  : "memory"); SB();
  asm volatile("v_mfma_f32_16x16x32_bf16 %0, v[164:167], a[36:39], %0" : "+v"(a1[1][1]) :  : ); SB();
  asm volatile("buffer_store_dwordx4 %1, %0, %2, %3 offen offset:0 sc1\n\ts_nop 1" :  : "v"(vKV), "v"(kq[0]), "s"(rWS), "s"(0u) : "memory"); SB();
  asm volatile("v_mfma_f32_16x16x32_bf16 %0, v[164:167], %1, %0" : "+v"(c1[1]) : "v"(ones) : ); SB();
  asm volatile("buffer_store_dwordx4 %1, %0, %2, %3 offen offset:0 sc1\n\ts_nop 1" :  : "v"(vKV), "v"(kq[1]), "s"(rWS), "s"(w3s + 32768u) : "memory"); SB();
  asm volatile("v_mfma_f32_16x16x32_bf16 %0, v[168:171], a[32:35], %0" : "+v"(a1[0][2]) :  : ); SB();
  asm volatile("buffer_store_dwordx4 %1, %0, %2, %3 offen offset:0 sc1\n\ts_nop 1" :  : "v"(vKV), "v"(kq[2]), "s"(rWS), "s"(w3s + 65536u) : "memory"); SB();
  asm volatile("v_mfma_f32_16x16x32_bf16 %0, v[168:171], a[36:39], %0" : "+v"(a1[1][2]) :  : ); SB();
  asm volatile("v_mfma_f32_16x16x32_bf16 %0, v[168:171], %1, %0" : "+v"(c1[2]) : "v"(ones) : ); SB();
  asm volatile("s_waitcnt lgkmcnt(4)\n\tv_mfma_f32_16x16x32_bf16 %0, v[172:175], a[40:43], %0" : "+v"(a1[0][0]) :  : ); SB();
  asm volatile("ds_read_b64_tr_b16 v[160:161], %0 offset:12288" :  : "v"(aW[1][0][0]) : "memory"); SB();
  asm volatile("ds_read_b64_tr_b16 v[162:163], %0 offset:13056" :  : "v"(aW[1][0][0]) : "memory"); SB();
  asm volatile("v_mfma_f32_16x16x32_bf16 %0, v[172:175], a[44:47], %0" : "+v"(a1[1][0]) :  : ); SB();
  asm volatile("ds_read_b64_tr_b16 v[164:165], %0 offset:12288" :  : "v"(aW[1][0][1]) : "memory"); SB();
  asm volatile("ds_read_b64_tr_b16 v[166:167], %0 offset:13056" :  : "v"(aW[1][0][1]) : "memory"); SB();
  asm volatile("s_waitcnt lgkmcnt(6)\n\tv_mfma_f32_16x16x32_bf16 %0, v[176:179], a[40:43], %0" : "+v"(a1[0][1]) :  : ); SB();
  asm volatile("ds_read_b64_tr_b16 v[168:169], %0 offset:12288" :  : "v"(aW[1][0][2]) : "memory"); SB();
  asm volatile("ds_read_b64_tr_b16 v[170:171], %0 offset:13056" :  : "v"(aW[1][0][2]) : "memory"); SB();
  asm volatile("v_mfma_f32_16x16x32_bf16 %0, v[176:179], a[44:47], %0" : "+v"(a1[1][1]) :  : ); SB();
  asm volatile("s_waitcnt lgkmcnt(6)\n\tv_mfma_f32_16x16x32_bf16 %0, v[180:183], a[40:43], %0" : "+v"(a1[0][2]) :  : ); SB();
  asm volatile("v_mfma_f32_16x16x32_bf16 %0, v[180:183], a[44:47], %0" : "+v"(a1[1][2]) :  : ); SB();
  asm volatile("s_waitcnt lgkmcnt(4)\n\tv_mfma_f32_16x16x32_bf16 %0, v[160:163], a[48:51], %0" : "+v"(a1[0][0]) :  : ); SB();
  asm volatile("ds_read_b64_tr_b16 v[172:173], %0 offset:12288" :  : "v"(aW[1][1][0]) : "memory"); SB();
  asm volatile("ds_read_b64_tr_b16 v[174:175], %0 offset:13056" :  : "v"(aW[1][1][0]) : "memory"); SB();
  asm volatile("v_mfma_f32_16x16x32_bf16 %0, v[160:163], a[52:55], %0" : "+v"(a1[1][0]) :  : ); SB();
  asm volatile("ds_read_b64_tr_b16 v[176:177], %0 offset:12288" :  : "v"(aW[1][1][1]) : "memory"); SB();
  asm volatile("ds_read_b64_tr_b16 v[178:179], %0 offset:13056" :  : "v"(aW[1][1][1]) : "memory"); SB();
  asm volatile("v_mfma_f32_16x16x32_bf16 %0, v[160:163], %1, %0" : "+v"(c1[0]) : "v"(ones) : ); SB();
  asm volatile("ds_read_b64_tr_b16 v[180:181], %0 offset:12288" :  : "v"(aW[1][1][2]) : "memory"); SB();
  asm volatile("ds_read_b64_tr_b16 v[182:183], %0 offset:13056" :  : "v"(aW[1][1][2]) : "memory"); SB();
  asm volatile("s_waitcnt lgkmcnt(8)\n\tv_mfma_f32_16x16x32_bf16 %0, v[164:167], a[48:51], %0" : "+v"(a1[0][1]) :  : ); SB();
  asm volatile("v_mfma_f32_16x16x32_bf16 %0, v[164:167], a[52:55], %0" : "+v"(a1[1][1]) :  : ); SB();
  asm volatile("v_mfma_f32_16x16x32_bf16 %0, v[164:167], %1, %0" : "+v"(c1[1]) : "v"(ones) : ); SB();
  asm volatile("s_waitcnt lgkmcnt(6)\n\tv_mfma_f32_16x16x32_bf16 %0, v[168:171], a[48:51], %0" : "+v"(a1[0][2]) :  : ); SB();
  asm volatile("v_mfma_f32_16x16x32_bf16 %0, v[168:171], a[52:55], %0" : "+v"(a1[1][2]) :  : ); SB();
  asm volatile("v_mfma_f32_16x16x32_bf16 %0, v[168:171], %1, %0" : "+v"(c1[2]) : "v"(ones) : ); SB();
  asm volatile("s_waitcnt vmcnt(17) lgkmcnt(0)\n\ts_barrier" :  :  : "memory"); SB();
  asm volatile("v_mfma_f32_16x16x32_bf16 %0, v[172:175], a[56:59], %0" : "+v"(a1[0][0]) :  : ); SB();
  asm volatile("ds_read_b64_tr_b16 v[160:161], %0 offset:24576" :  : "v"(aW[1][0][0]) : "memory"); SB();
  asm volatile("ds_read_b64_tr_b16 v[162:163], %0 offset:25344" :  : "v"(aW[1][0][0]) : "memory"); SB();
  asm volatile("v_mfma_f32_16x16x32_bf16 %0, v[172:175], a[60:63], %0" : "+v"(a1[1][0]) :  : ); SB();
  asm volatile("ds_read_b64_tr_b16 v[164:165], %0 offset:24576" :  : "v"(aW[1][0][1]) : "memory"); SB();
  asm volatile("ds_read_b64_tr_b16 v[166:167], %0 offset:25344" :  : "v"(aW[1][0][1]) : "memory"); SB();
  asm volatile("v_mfma_f32_16x16x32_bf16 %0, v[176:179], a[56:59], %0" : "+v"(a1[0][1]) :  : ); SB();
  asm volatile("ds_read_b64_tr_b16 v[168:169], %0 offset:24576" :  : "v"(aW[1][0][2]) : "memory"); SB();
  asm volatile("ds_read_b64_tr_b16 v[170:171], %0 offset:25344" :  : "v"(aW[1][0][2]) : "memory"); SB();
  asm volatile("v_mfma_f32_16x16x32_bf16 %0, v[176:179], a[60:63], %0" : "+v"(a1[1][1]) :  : ); SB();
  asm volatile("v_mfma_f32_16x16x32_bf16 %0, v[180:183], a[56:59], %0" : "+v"(a1[0][2]) :  : ); SB();
  asm volatile("v_mfma_f32_16x16x32_bf16 %0, v[180:183], a[60:63], %0" : "+v"(a1[1][2]) :  : ); SB();
  asm volatile("buffer_load_dwordx4 v[188:191], %0, %1, %2 offen offset:1024 sc1" :  : "v"(vS), "s"(rWS), "s"(0u) : "memory"); SB();
  asm volatile("s_waitcnt lgkmcnt(4)\n\tv_mfma_f32_16x16x32_bf16 %0, v[160:163], a[64:67], %0" : "+v"(a1[0][0]) :  : ); SB();
  asm volatile("ds_read_b64_tr_b16 v[172:173], %0 offset:24576" :  : "v"(aW[1][1][0]) : "memory"); SB();
  asm volatile("ds_read_b64_tr_b16 v[174:175], %0 offset:25344" :  : "v"(aW[1][1][0]) : "memory"); SB();
  asm volatile("v_mfma_f32_16x16x32_bf16 %0, v[160:163], a[68:71], %0" : "+v"(a1[1][0]) :  : ); SB();
  asm volatile("ds_read_b64_tr_b16 v[176:177], %0 offset:24576" :  : "v"(aW[1][1][1]) : "memory"); SB();
  asm volatile("ds_read_b64_tr_b16 v[178:179], %0 offset:25344" :  : "v"(aW[1][1][1]) : "memory"); SB();
  asm volatile("v_mfma_f32_16x16x32_bf16 %0, v[160:163], %1, %0" : "+v"(c1[0]) : "v"(ones) : ); SB();
  asm volatile("ds_read_b64_tr_b16 v[180:181], %0 offset:24576" :  : "v"(aW[1][1][2]) : "memory"); SB();
  asm volatile("ds_read_b64_tr_b16 v[182:183], %0 offset:25344" :  : "v"(aW[1][1][2]) : "memory"); SB();
  asm volatile("buffer_load_dwordx4 v[192:195], %0, %1, %2 offen offset:2048 sc1" :  : "v"(vS), "s"(rWS), "s"(0u) : "memory"); SB();
  asm volatile("s_waitcnt lgkmcnt(8)\n\tv_mfma_f32_16x16x32_bf16 %0, v[164:167], a[64:67], %0" : "+v"(a1[0][1]) :  : ); SB();
  asm volatile("v_mfma_f32_16x16x32_bf16 %0, v[164:167], a[68:71], %0" : "+v"(a1[1][1]) :  : ); SB();
  asm volatile("v_mfma_f32_16x16x32_bf16 %0, v[164:167], %1, %0" : "+v"(c1[1]) : "v"(ones) : ); SB();
  asm volatile("s_waitcnt lgkmcnt(6)\n\tv_mfma_f32_16x16x32_bf16 %0, v[168:171], a[64:67], %0" : "+v"(a1[0][2]) :  : ); SB();
  asm volatile("v_mfma_f32_16x16x32_bf16 %0, v[168:171], a[68:71], %0" : "+v"(a1[1][2]) :  : ); SB();
  asm volatile("buffer_load_dwordx4 v[196:199], %0, %1, %2 offen offset:3072 sc1" :  : "v"(vS), "s"(rWS), "s"(0u) : "memory"); SB();
  asm volatile("v_mfma_f32_16x16x32_bf16 %0, v[168:171], %1, %0" : "+v"(c1[2]) : "v"(ones) : ); SB();
  asm volatile("s_waitcnt lgkmcnt(4)\n\tv_mfma_f32_16x16x32_bf16 %0, v[172:175], a[72:75], %0" : "+v"(a1[0][0]) :  : ); SB();
  asm volatile("ds_read_b64_tr_b16 v[160:161], %0 offset:36864" :  : "v"(aW[1][0][0]) : "memory"); SB();
  asm volatile("ds_read_b64_tr_b16 v[162:163], %0 offset:37632" :  : "v"(aW[1][0][0]) : "memory"); SB();
  asm volatile("v_mfma_f32_16x16x32_bf16 %0, v[172:175], a[76:79], %0" : "+v"(a1[1][0]) :  : ); SB();
  asm volatile("ds_read_b64_tr_b16 v[164:165], %0 offset:36864" :  : "v"(aW[1][0][1]) : "memory"); SB();
  asm volatile("ds_read_b64_tr_b16 v[166:167], %0 offset:37632" :  : "v"(aW[1][0][1]) : "memory"); SB();
  asm volatile("s_waitcnt lgkmcnt(6)\n\tv_mfma_f32_16x16x32_bf16 %0, v[176:179], a[72:75], %0" : "+v"(a1[0][1]) :  : ); SB();
  asm volatile("ds_read_b64_tr_b16 v[168:169], %0 offset:36864" :  : "v"(aW[1][0][2]) : "memory"); SB();
  asm volatile("ds_read_b64_tr_b16 v[170:171], %0 offset:37632" :  : "v"(aW[1][0][2]) : "memory"); SB();
  asm volatile("v_mfma_f32_16x16x32_bf16 %0, v[176:179], a[76:79], %0" : "+v"(a1[1][1]) :  : ); SB();
  asm volatile("s_waitcnt lgkmcnt(6)\n\tv_mfma_f32_16x16x32_bf16 %0, v[180:183], a[72:75], %0" : "+v"(a1[0][2]) :  : ); SB();
  asm volatile("v_mfma_f32_16x16x32_bf16 %0, v[180:183], a[76:79], %0" : "+v"(a1[1][2]) :  : ); SB();
  asm volatile("buffer_load_dwordx4 v[200:203], %0, %1, %2 offen offset:0 sc1" :  : "v"(vS), "s"(rWS), "s"(4096u) : "memory"); SB();
  ST(8);
  asm volatile("buffer_load_dwordx4 v[204:207], %0, %1, %2 offen offset:1024 sc1" :  : "v"(vS), "s"(rWS), "s"(4096u) : "memory"); SB();
  asm volatile("buffer_load_dwordx4 v[208:211], %0, %1, %2 offen offset:2048 sc1" :  : "v"(vS), "s"(rWS), "s"(4096u) : "memory"); SB();
  asm volatile("buffer_load_dwordx4 v[212:215], %0, %1, %2 offen offset:3072 sc1" :  : "v"(vS), "s"(rWS), "s"(4096u) : "memory"); SB();
  asm volatile("s_add_u32 m0, %0, 12288" :  : "s"(mW) : "memory", "scc"); SB();
  asm volatile("s_nop 0\n\tbuffer_load_dwordx4 %0, %1, %2 offen lds" :  : "v"(voB[0]), "s"(rW), "s"(kt(7) * 196608u + 0u) : "memory"); SB();
  asm volatile("s_add_u32 m0, %0, 13312" :  : "s"(mW) : "memory", "scc"); SB();
  asm volatile("s_nop 0\n\tbuffer_load_dwordx4 %0, %1, %2 offen lds" :  : "v"(voB[1]), "s"(rW), "s"(kt(7) * 196608u + 0u) : "memory"); SB();
  asm volatile("s_add_u32 m0, %0, 14336" :  : "s"(mW) : "memory", "scc"); SB();
  asm volatile("s_nop 0\n\tbuffer_load_dwordx4 %0, %1, %2 offen lds" :  : "v"(voB[2]), "s"(rW), "s"(kt(7) * 196608u + 0u) : "memory"); SB();
  asm volatile("s_add_u32 m0, %0, 24576" :  : "s"(mW) : "memory", "scc"); SB();
  asm volatile("s_nop 0\n\tbuffer_load_dwordx4 %0, %1, %2 offen lds" :  : "v"(voB[0]), "s"(rW), "s"(kt(8) * 196608u + 0u) : "memory"); SB();
  asm volatile("s_waitcnt vmcnt(11)\n\ts_mov_b64 exec, 1\n\tbuffer_store_dwordx2 %0, off, %1, %2 sc1\n\ts_mov_b64 exec, -1" :  : "v"(((u32x2){tg0, tg1})), "s"(rWS), "s"(W_FK + ((cb * 32 + rb) * 4 + w) * 8) : "memory"); SB();
  asm volatile("s_add_u32 m0, %0, 25600" :  : "s"(mW) : "memory", "scc"); SB();
  asm volatile("s_nop 0\n\tbuffer_load_dwordx4 %0, %1, %2 offen lds" :  : "v"(voB[1]), "s"(rW), "s"(kt(8) * 196608u + 0u) : "memory"); SB();
  asm volatile("s_add_u32 m0, %0, 26624" :  : "s"(mW) : "memory", "scc"); SB();
  asm volatile("s_nop 0\n\tbuffer_load_dwordx4 %0, %1, %2 offen lds" :  : "v"(voB[2]), "s"(rW), "s"(kt(8) * 196608u + 0u) : "memory"); SB();
  asm volatile("s_add_u32 m0, %0, 36864" :  : "s"(mW) : "memory", "scc"); SB();
  asm volatile("s_nop 0\n\tbuffer_load_dwordx4 %0, %1, %2 offen lds" :  : "v"(voB[0]), "s"(rW), "s"(kt(9) * 196608u + 0u) : "memory"); SB();
  asm volatile("s_add_u32 m0, %0, 37888" :  : "s"(mW) : "memory", "scc"); SB();
  asm volatile("s_nop 0\n\tbuffer_load_dwordx4 %0, %1, %2 offen lds" :  : "v"(voB[1]), "s"(rW), "s"(kt(9) * 196608u + 0u) : "memory"); SB();
  asm volatile("s_add_u32 m0, %0, 38912" :  : "s"(mW) : "memory", "scc"); SB();
  asm volatile("s_nop 0\n\tbuffer_load_dwordx4 %0, %1, %2 offen lds" :  : "v"(voB[2]), "s"(rW), "s"(kt(9) * 196608u + 0u) : "memory"); SB();
  asm volatile("s_waitcnt vmcnt(10)\n\t.Lc_%=:\n\tv_cmpx_eq_u64 vcc, %1, v[186:187]\n\tv_cmpx_eq_u64 vcc, %1, v[190:191]\n\tv_cmpx_eq_u64 vcc, %1, v[194:195]\n\tv_cmpx_eq_u64 vcc, %1, v[198:199]\n\tv_cmpx_eq_u64 vcc, %1, v[202:203]\n\tv_cmpx_eq_u64 vcc, %1, v[206:207]\n\tv_cmpx_eq_u64 vcc, %1, v[210:211]\n\tv_cmpx_eq_u64 vcc, %1, v[214:215]\n\ts_cmp_eq_u64 vcc, -1\n\ts_mov_b64 exec, -1\n\ts_cbranch_scc0 .Lr_%=\n\t.Ld_%=:\n\t.subsection 1\n\t.Lr_%=:\n\ts_sub_i32 %0, %0, 1\n\ts_cmp_lt_i32 %0, 1\n\ts_cbranch_scc1 .Ld_%=\n\ts_sleep 2\n\ts_movk_i32 vcc_lo, 0x1000\n\tbuffer_load_dwordx4 v[184:187], %2, %3, 0 offen offset:0 sc1\n\tbuffer_load_dwordx4 v[188:191], %2, %3, 0 offen offset:1024 sc1\n\tbuffer_load_dwordx4 v[192:195], %2, %3, 0 offen offset:2048 sc1\n\tbuffer_load_dwordx4 v[196:199], %2, %3, 0 offen offset:3072 sc1\n\tbuffer_load_dwordx4 v[200:203], %2, %3, vcc_lo offen offset:0 sc1\n\tbuffer_load_dwordx4 v[204:207], %2, %3, vcc_lo offen offset:1024 sc1\n\tbuffer_load_dwordx4 v[208:211], %2, %3, vcc_lo offen offset:2048 sc1\n\tbuffer_load_dwordx4 v[212:215], %2, %3, vcc_lo offen offset:3072 sc1\n\ts_waitcnt vmcnt(0)\n\ts_branch .Lc_%=\n\t.subsection 0" : "+s"(tmo) : "s"(tg), "v"(vS), "s"(rWS) : "memory", "vcc", "scc"); SB();
  ST(9);
  {
    f32x2 s4[4], dm[4];
  asm volatile("v_pk_add_f32 %0, v[184:185], v[188:189]\n\tv_pk_add_f32 %1, v[192:193], v[196:197]\n\tv_pk_add_f32 %2, v[200:201], v[204:205]\n\tv_pk_add_f32 %3, v[208:209], v[212:213]" : "=&v"(s4[0]), "=&v"(s4[1]), "=&v"(s4[2]), "=&v"(s4[3]) :  : ); SB();
    const f32x2 sm = (s4[0] + s4[1]) + (s4[2] + s4[3]);
    const float mean = (sm[0] + dppf<0x128>(sm[0])) * (1.0f / 16.0f);
  asm volatile("v_sub_f32 %0, v184, %8\n\tv_sub_f32 %1, v188, %8\n\tv_sub_f32 %2, v192, %8\n\tv_sub_f32 %3, v196, %8\n\tv_sub_f32 %4, v200, %8\n\tv_sub_f32 %5, v204, %8\n\tv_sub_f32 %6, v208, %8\n\tv_sub_f32 %7, v212, %8" : "=&v"(dm[0][0]), "=&v"(dm[0][1]), "=&v"(dm[1][0]), "=&v"(dm[1][1]), "=&v"(dm[2][0]), "=&v"(dm[2][1]), "=&v"(dm[3][0]), "=&v"(dm[3][1]) : "v"(mean) : ); SB();
    const f32x2 dq = __builtin_elementwise_fma(dm[0], dm[0], dm[1] * dm[1]) + __builtin_elementwise_fma(dm[2], dm[2], dm[3] * dm[3]);
    float qs = __builtin_fmaf(dq[0] + dq[1], 48.0f, sm[1]);
    qs += dppf<0x128>(qs);
    const float r = __builtin_amdgcn_rcpf(rbf(__builtin_amdgcn_sqrtf(rbf(rbf(qs * (1.0f / 768.0f)) + 1e-5f))));
    const float mr = -rbf(mean) * r;
    const f32x2 re[4] = {{dppf<0x150>(r), dppf<0x151>(r)}, {dppf<0x152>(r), dppf<0x153>(r)}, {dppf<0x154>(r), dppf<0x155>(r)}, {dppf<0x156>(r), dppf<0x157>(r)}};
    const f32x2 me[4] = {{dppf<0x150>(mr), dppf<0x151>(mr)}, {dppf<0x152>(mr), dppf<0x153>(mr)}, {dppf<0x154>(mr), dppf<0x155>(mr)}, {dppf<0x156>(mr), dppf<0x157>(mr)}};
    u32x4 zo[3];
#pragma unroll
    for (int j = 0; j < 3; ++j)
#pragma unroll
      for (int e = 0; e < 4; ++e) zo[j][e] = pk2(__builtin_elementwise_fma(unpk(zt[j][e >> 1][e & 1]), re[e], me[e]));
    SB();
  asm volatile("buffer_store_dwordx4 %1, %0, %2, %3 offen offset:0 sc1\n\ts_nop 1" :  : "v"(vZ), "v"(zo[0]), "s"(rWS), "s"(0u) : "memory"); SB();
  asm volatile("buffer_store_dwordx4 %1, %0, %2, %3 offen offset:1024 sc1\n\ts_nop 1" :  : "v"(vZ), "v"(zo[1]), "s"(rWS), "s"(0u) : "memory"); SB();
  asm volatile("buffer_store_dwordx4 %1, %0, %2, %3 offen offset:2048 sc1\n\ts_nop 1" :  : "v"(vZ), "v"(zo[2]), "s"(rWS), "s"(0u) : "memory"); SB();
  }
  ST(10);
  asm volatile("s_waitcnt lgkmcnt(4)\n\tv_mfma_f32_16x16x32_bf16 %0, v[160:163], a[80:83], %0" : "+v"(a1[0][0]) :  : ); SB();
  asm volatile("ds_read_b64_tr_b16 v[172:173], %0 offset:36864" :  : "v"(aW[1][1][0]) : "memory"); SB();
  asm volatile("ds_read_b64_tr_b16 v[174:175], %0 offset:37632" :  : "v"(aW[1][1][0]) : "memory"); SB();
  asm volatile("buffer_load_dwordx4 v[216:219], %0, %1, %2 offen" :  : "v"(vF), "s"(rF), "s"(0u) : "memory"); SB();
  asm volatile("v_mfma_f32_16x16x32_bf16 %0, v[160:163], a[84:87], %0" : "+v"(a1[1][0]) :  : ); SB();
  asm volatile("ds_read_b64_tr_b16 v[176:177], %0 offset:36864" :  : "v"(aW[1][1][1]) : "memory"); SB();
  asm volatile("ds_read_b64_tr_b16 v[178:179], %0 offset:37632" :  : "v"(aW[1][1][1]) : "memory"); SB();
  asm volatile("buffer_load_dword v230, off, %0, %1" :  : "s"(rF), "s"(hpo + 0u) : "memory"); SB();
  asm volatile("v_mfma_f32_16x16x32_bf16 %0, v[160:163], %1, %0" : "+v"(c1[0]) : "v"(ones) : ); SB();
  asm volatile("ds_read_b64_tr_b16 v[180:181], %0 offset:36864" :  : "v"(aW[1][1][2]) : "memory"); SB();
  asm volatile("ds_read_b64_tr_b16 v[182:183], %0 offset:37632" :  : "v"(aW[1][1][2]) : "memory"); SB();
  asm volatile("global_load_ushort %0, %1, %2" : "=&v"(dD[0]) : "v"(0u), "s"(ssm_D + (ch0 + 0) * 2) : "memory"); SB();
  asm volatile("s_waitcnt lgkmcnt(8)\n\tv_mfma_f32_16x16x32_bf16 %0, v[164:167], a[80:83], %0" : "+v"(a1[0][1]) :  : ); SB();
  asm volatile("v_mfma_f32_16x16x32_bf16 %0, v[164:167], a[84:87], %0" : "+v"(a1[1][1]) :  : ); SB();
  asm volatile("v_mfma_f32_16x16x32_bf16 %0, v[164:167], %1, %0" : "+v"(c1[1]) : "v"(ones) : ); SB();
  asm volatile("s_waitcnt lgkmcnt(6)\n\tv_mfma_f32_16x16x32_bf16 %0, v[168:171], a[80:83], %0" : "+v"(a1[0][2]) :  : ); SB();
  asm volatile("v_mfma_f32_16x16x32_bf16 %0, v[168:171], a[84:87], %0" : "+v"(a1[1][2]) :  : ); SB();
  asm volatile("v_mfma_f32_16x16x32_bf16 %0, v[168:171], %1, %0" : "+v"(c1[2]) : "v"(ones) : ); SB();
  asm volatile("buffer_load_dwordx4 v[220:223], %0, %1, %2 offen" :  : "v"(vF), "s"(rF), "s"(4096u) : "memory"); SB();
  asm volatile("s_waitcnt vmcnt(14) lgkmcnt(0)\n\ts_barrier" :  :  : "memory"); SB();
  asm volatile("v_mfma_f32_16x16x32_bf16 %0, v[172:175], a[88:91], %0" : "+v"(a1[0][0]) :  : ); SB();
  asm volatile("ds_read_b64_tr_b16 v[160:161], %0 offset:0" :  : "v"(aW[0][0][0]) : "memory"); SB();
  asm volatile("ds_read_b64_tr_b16 v[162:163], %0 offset:768" :  : "v"(aW[0][0][0]) : "memory"); SB();
  asm volatile("v_mfma_f32_16x16x32_bf16 %0, v[172:175], a[92:95], %0" : "+v"(a1[1][0]) :  : ); SB();
  asm volatile("ds_read_b64_tr_b16 v[164:165], %0 offset:0" :  : "v"(aW[0][0][1]) : "memory"); SB();
  asm volatile("ds_read_b64_tr_b16 v[166:167], %0 offset:768" :  : "v"(aW[0][0][1]) : "memory"); SB();
  asm volatile("v_mfma_f32_16x16x32_bf16 %0, v[176:179], a[88:91], %0" : "+v"(a1[0][1]) :  : ); SB();
  asm volatile("ds_read_b64_tr_b16 v[168:169], %0 offset:0" :  : "v"(aW[0][0][2]) : "memory"); SB();
  asm volatile("ds_read_b64_tr_b16 v[170:171], %0 offset:768" :  : "v"(aW[0][0][2]) : "memory"); SB();
  asm volatile("buffer_load_dword v231, off, %0, %1" :  : "s"(rF), "s"(hpo + 4096u) : "memory"); SB();
  asm volatile("v_mfma_f32_16x16x32_bf16 %0, v[176:179], a[92:95], %0" : "+v"(a1[1][1]) :  : ); SB();
  asm volatile("v_mfma_f32_16x16x32_bf16 %0, v[180:183], a[88:91], %0" : "+v"(a1[0][2]) :  : ); SB();
  asm volatile("v_mfma_f32_16x16x32_bf16 %0, v[180:183], a[92:95], %0" : "+v"(a1[1][2]) :  : ); SB();
  asm volatile("s_waitcnt lgkmcnt(4)\n\tv_mfma_f32_16x16x32_bf16 %0, v[160:163], a[96:99], %0" : "+v"(a1[0][0]) :  : ); SB();
  asm volatile("ds_read_b64_tr_b16 v[172:173], %0 offset:0" :  : "v"(aW[0][1][0]) : "memory"); SB();
  asm volatile("ds_read_b64_tr_b16 v[174:175], %0 offset:768" :  : "v"(aW[0][1][0]) : "memory"); SB();
  asm volatile("global_load_ushort %0, %1, %2" : "=&v"(dD[1]) : "v"(0u), "s"(ssm_D + (ch0 + 1) * 2) : "memory"); SB();
  asm volatile("v_mfma_f32_16x16x32_bf16 %0, v[160:163], a[100:103], %0" : "+v"(a1[1][0]) :  : ); SB();
  asm volatile("ds_read_b64_tr_b16 v[176:177], %0 offset:0" :  : "v"(aW[0][1][1]) : "memory"); SB();
  asm volatile("ds_read_b64_tr_b16 v[178:179], %0 offset:768" :  : "v"(aW[0][1][1]) : "memory"); SB();
  asm volatile("v_mfma_f32_16x16x32_bf16 %0, v[160:163], %1, %0" : "+v"(c1[0]) : "v"(ones) : ); SB();
  asm volatile("ds_read_b64_tr_b16 v[180:181], %0 offset:0" :  : "v"(aW[0][1][2]) : "memory"); SB();
  asm volatile("ds_read_b64_tr_b16 v[182:183], %0 offset:768" :  : "v"(aW[0][1][2]) : "memory"); SB();
  asm volatile("s_waitcnt lgkmcnt(8)\n\tv_mfma_f32_16x16x32_bf16 %0, v[164:167], a[96:99], %0" : "+v"(a1[0][1]) :  : ); SB();
  asm volatile("v_mfma_f32_16x16x32_bf16 %0, v[164:167], a[100:103], %0" : "+v"(a1[1][1]) :  : ); SB();
  asm volatile("v_mfma_f32_16x16x32_bf16 %0, v[164:167], %1, %0" : "+v"(c1[1]) : "v"(ones) : ); SB();
  asm volatile("s_waitcnt lgkmcnt(6)\n\tv_mfma_f32_16x16x32_bf16 %0, v[168:171], a[96:99], %0" : "+v"(a1[0][2]) :  : ); SB();
  asm volatile("buffer_load_dwordx4 v[224:227], %0, %1, %2 offen" :  : "v"(vF), "s"(rF), "s"(c2s) : "memory"); SB();
  asm volatile("v_mfma_f32_16x16x32_bf16 %0, v[168:171], a[100:103], %0" : "+v"(a1[1][2]) :  : ); SB();
  asm volatile("v_mfma_f32_16x16x32_bf16 %0, v[168:171], %1, %0" : "+v"(c1[2]) : "v"(ones) : ); SB();
  asm volatile("s_waitcnt lgkmcnt(4)\n\tv_mfma_f32_16x16x32_bf16 %0, v[172:175], a[104:107], %0" : "+v"(a1[0][0]) :  : ); SB();
  asm volatile("ds_read_b64_tr_b16 v[160:161], %0 offset:12288" :  : "v"(aW[0][0][0]) : "memory"); SB();
  asm volatile("ds_read_b64_tr_b16 v[162:163], %0 offset:13056" :  : "v"(aW[0][0][0]) : "memory"); SB();
  asm volatile("v_mfma_f32_16x16x32_bf16 %0, v[172:175], a[108:111], %0" : "+v"(a1[1][0]) :  : ); SB();
  asm volatile("ds_read_b64_tr_b16 v[164:165], %0 offset:12288" :  : "v"(aW[0][0][1]) : "memory"); SB();
  asm volatile("ds_read_b64_tr_b16 v[166:167], %0 offset:13056" :  : "v"(aW[0][0][1]) : "memory"); SB();
  asm volatile("s_waitcnt lgkmcnt(6)\n\tv_mfma_f32_16x16x32_bf16 %0, v[176:179], a[104:107], %0" : "+v"(a1[0][1]) :  : ); SB();
  asm volatile("ds_read_b64_tr_b16 v[168:169], %0 offset:12288" :  : "v"(aW[0][0][2]) : "memory"); SB();
  asm volatile("ds_read_b64_tr_b16 v[170:171], %0 offset:13056" :  : "v"(aW[0][0][2]) : "memory"); SB();
  asm volatile("buffer_load_dword v232, off, %0, %1" :  : "s"(rF), "s"(hpo + c2s) : "memory"); SB();
  asm volatile("v_mfma_f32_16x16x32_bf16 %0, v[176:179], a[108:111], %0" : "+v"(a1[1][1]) :  : ); SB();
  asm volatile("s_waitcnt lgkmcnt(6)\n\tv_mfma_f32_16x16x32_bf16 %0, v[180:183], a[104:107], %0" : "+v"(a1[0][2]) :  : ); SB();
  asm volatile("v_mfma_f32_16x16x32_bf16 %0, v[180:183], a[108:111], %0" : "+v"(a1[1][2]) :  : ); SB();
  asm volatile("s_waitcnt lgkmcnt(4)\n\tv_mfma_f32_16x16x32_bf16 %0, v[160:163], a[112:115], %0" : "+v"(a1[0][0]) :  : ); SB();
  asm volatile("ds_read_b64_tr_b16 v[172:173], %0 offset:12288" :  : "v"(aW[0][1][0]) : "memory"); SB();
  asm volatile("ds_read_b64_tr_b16 v[174:175], %0 offset:13056" :  : "v"(aW[0][1][0]) : "memory"); SB();
  asm volatile("v_mfma_f32_16x16x32_bf16 %0, v[160:163], a[116:119], %0" : "+v"(a1[1][0]) :  : ); SB();
  asm volatile("ds_read_b64_tr_b16 v[176:177], %0 offset:12288" :  : "v"(aW[0][1][1]) : "memory"); SB();
  asm volatile("ds_read_b64_tr_b16 v[178:179], %0 offset:13056" :  : "v"(aW[0][1][1]) : "memory"); SB();
  asm volatile("global_load_ushort %0, %1, %2" : "=&v"(dD[2]) : "v"(0u), "s"(ssm_D + (ch0 + c2o) * 2) : "memory"); SB();
  asm volatile("v_mfma_f32_16x16x32_bf16 %0, v[160:163], %1, %0" : "+v"(c1[0]) : "v"(ones) : ); SB();
  asm volatile("ds_read_b64_tr_b16 v[180:181], %0 offset:12288" :  : "v"(aW[0][1][2]) : "memory"); SB();
  asm volatile("ds_read_b64_tr_b16 v[182:183], %0 offset:13056" :  : "v"(aW[0][1][2]) : "memory"); SB();
  asm volatile("s_waitcnt lgkmcnt(8)\n\tv_mfma_f32_16x16x32_bf16 %0, v[164:167], a[112:115], %0" : "+v"(a1[0][1]) :  : ); SB();
  asm volatile("v_mfma_f32_16x16x32_bf16 %0, v[164:167], a[116:119], %0" : "+v"(a1[1][1]) :  : ); SB();
  asm volatile("v_mfma_f32_16x16x32_bf16 %0, v[164:167], %1, %0" : "+v"(c1[1]) : "v"(ones) : ); SB();
  asm volatile("s_waitcnt lgkmcnt(6)\n\tv_mfma_f32_16x16x32_bf16 %0, v[168:171], a[112:115], %0" : "+v"(a1[0][2]) :  : ); SB();
  asm volatile("v_mfma_f32_16x16x32_bf16 %0, v[168:171], a[116:119], %0" : "+v"(a1[1][2]) :  : ); SB();
  asm volatile("v_mfma_f32_16x16x32_bf16 %0, v[168:171], %1, %0" : "+v"(c1[2]) : "v"(ones) : ); SB();
  asm volatile("s_waitcnt vmcnt(12) lgkmcnt(0)\n\ts_barrier" :  :  : "memory"); SB();
  asm volatile("v_mfma_f32_16x16x32_bf16 %0, v[172:175], a[120:123], %0" : "+v"(a1[0][0]) :  : ); SB();
  asm volatile("ds_read_b64_tr_b16 v[160:161], %0 offset:24576" :  : "v"(aW[0][0][0]) : "memory"); SB();
  asm volatile("ds_read_b64_tr_b16 v[162:163], %0 offset:25344" :  : "v"(aW[0][0][0]) : "memory"); SB();
  asm volatile("v_mfma_f32_16x16x32_bf16 %0, v[172:175], a[124:127], %0" : "+v"(a1[1][0]) :  : ); SB();
  asm volatile("ds_read_b64_tr_b16 v[164:165], %0 offset:24576" :  : "v"(aW[0][0][1]) : "memory"); SB();
  asm volatile("ds_read_b64_tr_b16 v[166:167], %0 offset:25344" :  : "v"(aW[0][0][1]) : "memory"); SB();
  asm volatile("v_mfma_f32_16x16x32_bf16 %0, v[176:179], a[120:123], %0" : "+v"(a1[0][1]) :  : ); SB();
  asm volatile("ds_read_b64_tr_b16 v[168:169], %0 offset:24576" :  : "v"(aW[0][0][2]) : "memory"); SB();
  asm volatile("ds_read_b64_tr_b16 v[170:171], %0 offset:25344" :  : "v"(aW[0][0][2]) : "memory"); SB();
  asm volatile("v_mfma_f32_16x16x32_bf16 %0, v[176:179], a[124:127], %0" : "+v"(a1[1][1]) :  : ); SB();
  asm volatile("v_mfma_f32_16x16x32_bf16 %0, v[180:183], a[120:123], %0" : "+v"(a1[0][2]) :  : ); SB();
  asm volatile("v_mfma_f32_16x16x32_bf16 %0, v[180:183], a[124:127], %0" : "+v"(a1[1][2]) :  : ); SB();
  asm volatile("s_waitcnt lgkmcnt(4)\n\tv_mfma_f32_16x16x32_bf16 %0, v[160:163], a[128:131], %0" : "+v"(a1[0][0]) :  : ); SB();
  asm volatile("ds_read_b64_tr_b16 v[172:173], %0 offset:24576" :  : "v"(aW[0][1][0]) : "memory"); SB();
  asm volatile("ds_read_b64_tr_b16 v[174:175], %0 offset:25344" :  : "v"(aW[0][1][0]) : "memory"); SB();
  asm volatile("v_mfma_f32_16x16x32_bf16 %0, v[160:163], a[132:135], %0" : "+v"(a1[1][0]) :  : ); SB();
  asm volatile("ds_read_b64_tr_b16 v[176:177], %0 offset:24576" :  : "v"(aW[0][1][1]) : "memory"); SB();
  asm volatile("ds_read_b64_tr_b16 v[178:179], %0 offset:25344" :  : "v"(aW[0][1][1]) : "memory"); SB();
  asm volatile("v_mfma_f32_16x16x32_bf16 %0, v[160:163], %1, %0" : "+v"(c1[0]) : "v"(ones) : ); SB();
  asm volatile("ds_read_b64_tr_b16 v[180:181], %0 offset:24576" :  : "v"(aW[0][1][2]) : "memory"); SB();
  asm volatile("ds_read_b64_tr_b16 v[182:183], %0 offset:25344" :  : "v"(aW[0][1][2]) : "memory"); SB();
  asm volatile("s_waitcnt lgkmcnt(8)\n\tv_mfma_f32_16x16x32_bf16 %0, v[164:167], a[128:131], %0" : "+v"(a1[0][1]) :  : ); SB();
  asm volatile("v_mfma_f32_16x16x32_bf16 %0, v[164:167], a[132:135], %0" : "+v"(a1[1][1]) :  : ); SB();
  asm volatile("v_mfma_f32_16x16x32_bf16 %0, v[164:167], %1, %0" : "+v"(c1[1]) : "v"(ones) : ); SB();
  asm volatile("s_waitcnt lgkmcnt(6)\n\tv_mfma_f32_16x16x32_bf16 %0, v[168:171], a[128:131], %0" : "+v"(a1[0][2]) :  : ); SB();
  asm volatile("v_mfma_f32_16x16x32_bf16 %0, v[168:171], a[132:135], %0" : "+v"(a1[1][2]) :  : ); SB();
  asm volatile("v_mfma_f32_16x16x32_bf16 %0, v[168:171], %1, %0" : "+v"(c1[2]) : "v"(ones) : ); SB();
  asm volatile("s_waitcnt lgkmcnt(4)\n\tv_mfma_f32_16x16x32_bf16 %0, v[172:175], a[136:139], %0" : "+v"(a1[0][0]) :  : ); SB();
  asm volatile("ds_read_b64_tr_b16 v[160:161], %0 offset:36864" :  : "v"(aW[0][0][0]) : "memory"); SB();
  asm volatile("ds_read_b64_tr_b16 v[162:163], %0 offset:37632" :  : "v"(aW[0][0][0]) : "memory"); SB();
  asm volatile("v_mfma_f32_16x16x32_bf16 %0, v[172:175], a[140:143], %0" : "+v"(a1[1][0]) :  : ); SB();
  asm volatile("ds_read_b64_tr_b16 v[164:165], %0 offset:36864" :  : "v"(aW[0][0][1]) : "memory"); SB();
  asm volatile("ds_read_b64_tr_b16 v[166:167], %0 offset:37632" :  : "v"(aW[0][0][1]) : "memory"); SB();
  asm volatile("s_waitcnt lgkmcnt(6)\n\tv_mfma_f32_16x16x32_bf16 %0, v[176:179], a[136:139], %0" : "+v"(a1[0][1]) :  : ); SB();
  asm volatile("ds_read_b64_tr_b16 v[168:169], %0 offset:36864" :  : "v"(aW[0][0][2]) : "memory"); SB();
  asm volatile("ds_read_b64_tr_b16 v[170:171], %0 offset:37632" :  : "v"(aW[0][0][2]) : "memory"); SB();
  asm volatile("v_mfma_f32_16x16x32_bf16 %0, v[176:179], a[140:143], %0" : "+v"(a1[1][1]) :  : ); SB();
  asm volatile("s_waitcnt lgkmcnt(6)\n\tv_mfma_f32_16x16x32_bf16 %0, v[180:183], a[136:139], %0" : "+v"(a1[0][2]) :  : ); SB();
  asm volatile("v_mfma_f32_16x16x32_bf16 %0, v[180:183], a[140:143], %0" : "+v"(a1[1][2]) :  : ); SB();
  asm volatile("s_waitcnt lgkmcnt(4)\n\tv_mfma_f32_16x16x32_bf16 %0, v[160:163], a[144:147], %0" : "+v"(a1[0][0]) :  : ); SB();
  asm volatile("ds_read_b64_tr_b16 v[172:173], %0 offset:36864" :  : "v"(aW[0][1][0]) : "memory"); SB();
  asm volatile("ds_read_b64_tr_b16 v[174:175], %0 offset:37632" :  : "v"(aW[0][1][0]) : "memory"); SB();
  asm volatile("v_mfma_f32_16x16x32_bf16 %0, v[160:163], a[148:151], %0" : "+v"(a1[1][0]) :  : ); SB();
  asm volatile("ds_read_b64_tr_b16 v[176:177], %0 offset:36864" :  : "v"(aW[0][1][1]) : "memory"); SB();
  asm volatile("ds_read_b64_tr_b16 v[178:179], %0 offset:37632" :  : "v"(aW[0][1][1]) : "memory"); SB();
  asm volatile("v_mfma_f32_16x16x32_bf16 %0, v[160:163], %1, %0" : "+v"(c1[0]) : "v"(ones) : ); SB();
  asm volatile("ds_read_b64_tr_b16 v[180:181], %0 offset:36864" :  : "v"(aW[0][1][2]) : "memory"); SB();
  asm volatile("ds_read_b64_tr_b16 v[182:183], %0 offset:37632" :  : "v"(aW[0][1][2]) : "memory"); SB();
  asm volatile("s_waitcnt lgkmcnt(8)\n\tv_mfma_f32_16x16x32_bf16 %0, v[164:167], a[144:147], %0" : "+v"(a1[0][1]) :  : ); SB();
  asm volatile("s_add_u32 m0, %0, 107392" :  : "s"(mA) : "memory", "scc"); SB();
  asm volatile("s_nop 0\n\tbuffer_load_dwordx4 %0, %1, %2 offen offset:128 lds" :  : "v"(voS), "s"(rA), "s"((1 ^ (2 * w)) * 32768) : "memory"); SB();
  asm volatile("v_mfma_f32_16x16x32_bf16 %0, v[164:167], a[148:151], %0" : "+v"(a1[1][1]) :  : ); SB();
  asm volatile("v_mfma_f32_16x16x32_bf16 %0, v[164:167], %1, %0" : "+v"(c1[1]) : "v"(ones) : ); SB();
  asm volatile("s_waitcnt lgkmcnt(6)\n\tv_mfma_f32_16x16x32_bf16 %0, v[168:171], a[144:147], %0" : "+v"(a1[0][2]) :  : ); SB();
  asm volatile("v_mfma_f32_16x16x32_bf16 %0, v[168:171], a[148:151], %0" : "+v"(a1[1][2]) :  : ); SB();
  asm volatile("v_mfma_f32_16x16x32_bf16 %0, v[168:171], %1, %0" : "+v"(c1[2]) : "v"(ones) : ); SB();
  asm volatile("s_waitcnt lgkmcnt(4)\n\tv_mfma_f32_16x16x32_bf16 %0, v[172:175], a[152:155], %0" : "+v"(a1[0][0]) :  : ); SB();
  asm volatile("v_mfma_f32_16x16x32_bf16 %0, v[172:175], a[156:159], %0" : "+v"(a1[1][0]) :  : ); SB();
  asm volatile("s_waitcnt lgkmcnt(2)\n\tv_mfma_f32_16x16x32_bf16 %0, v[176:179], a[152:155], %0" : "+v"(a1[0][1]) :  : ); SB();
  asm volatile("v_mfma_f32_16x16x32_bf16 %0, v[176:179], a[156:159], %0" : "+v"(a1[1][1]) :  : ); SB();
  asm volatile("s_waitcnt lgkmcnt(0)\n\tv_mfma_f32_16x16x32_bf16 %0, v[180:183], a[152:155], %0" : "+v"(a1[0][2]) :  : ); SB();
  asm volatile("v_mfma_f32_16x16x32_bf16 %0, v[180:183], a[156:159], %0" : "+v"(a1[1][2]) :  : ); SB();
  asm volatile("buffer_load_dwordx2 v[228:229], %0, %1, %2 offen offset:0 sc1" :  : "v"(vFK), "s"(rWS), "s"(0u) : "memory"); SB();
  ST(11);
  asm volatile("s_nop 7" : "+v"(a1[0][0]), "+v"(a1[0][1]), "+v"(a1[0][2]), "+v"(a1[1][0]), "+v"(a1[1][1]), "+v"(a1[1][2]), "+v"(c1[0]), "+v"(c1[1]), "+v"(c1[2]) :  : ); SB();
  const unsigned ca = lb + 87936 + w * 192 + g4 * 16, cr = lb + 87936 + (w ^ 2) * 192 + g4 * 16;
  asm volatile("ds_write_b128 %0, %1 offset:0" :  : "v"(ca), "v"(c1[0]) : "memory"); SB();
  asm volatile("ds_write_b128 %0, %1 offset:64" :  : "v"(ca), "v"(c1[1]) : "memory"); SB();
  asm volatile("ds_write_b128 %0, %1 offset:128" :  : "v"(ca), "v"(c1[2]) : "memory"); SB();
  asm volatile("s_waitcnt vmcnt(11)\n\ts_mov_b64 exec, 1\n\tbuffer_store_dwordx2 %0, off, %1, %2 sc1\n\ts_mov_b64 exec, -1" :  : "v"(((u32x2){tg0, tg1})), "s"(rWS), "s"(W_FZ + ((cb * 32 + rb) * 4 + w) * 8) : "memory"); SB();
  asm volatile("s_add_u32 m0, %0, 108416" :  : "s"(mA) : "memory", "scc"); SB();
  asm volatile("s_nop 0\n\tbuffer_load_dwordx4 %0, %1, %2 offen offset:128 lds" :  : "v"(voS), "s"(rA), "s"((2 ^ (2 * w)) * 32768) : "memory"); SB();
  asm volatile("s_add_u32 m0, %0, 109440" :  : "s"(mA) : "memory", "scc"); SB();
  asm volatile("s_nop 0\n\tbuffer_load_dwordx4 %0, %1, %2 offen offset:128 lds" :  : "v"(voS), "s"(rA), "s"((3 ^ (2 * w)) * 32768) : "memory"); SB();
  asm volatile("s_add_u32 m0, %0, 110464" :  : "s"(mA) : "memory", "scc"); SB();
  asm volatile("s_nop 0\n\tbuffer_load_dwordx4 %0, %1, %2 offen offset:128 lds" :  : "v"(voS), "s"(rA), "s"((4 ^ (2 * w)) * 32768) : "memory"); SB();
  asm volatile("s_add_u32 m0, %0, 111488" :  : "s"(mA) : "memory", "scc"); SB();
  asm volatile("s_nop 0\n\tbuffer_load_dwordx4 %0, %1, %2 offen offset:128 lds" :  : "v"(voS), "s"(rA), "s"((5 ^ (2 * w)) * 32768) : "memory"); SB();
  asm volatile("s_add_u32 m0, %0, 112512" :  : "s"(mA) : "memory", "scc"); SB();
  asm volatile("s_nop 0\n\tbuffer_load_dwordx4 %0, %1, %2 offen offset:128 lds" :  : "v"(voS), "s"(rA), "s"((6 ^ (2 * w)) * 32768) : "memory"); SB();
  asm volatile("s_add_u32 m0, %0, 113536" :  : "s"(mA) : "memory", "scc"); SB();
  asm volatile("s_nop 0\n\tbuffer_load_dwordx4 %0, %1, %2 offen offset:128 lds" :  : "v"(voS), "s"(rA), "s"((7 ^ (2 * w)) * 32768) : "memory"); SB();
  asm volatile("ds_read_b128 a[160:163], %0 offset:0" :  : "v"(aA[0]) : "memory"); SB();
  asm volatile("ds_read_b128 a[176:179], %0 offset:0" :  : "v"(aA[1]) : "memory"); SB();
  asm volatile("s_waitcnt vmcnt(9)" : "+v"(dD[0]), "+v"(dD[1]), "+v"(dD[2]) :  : "memory"); SB();
  {
    const unsigned ha = lb + 49152 + 4608 + (2040 - 8 * (w * 64 + lane)) * 2;
    const unsigned za = lb + 49152 + min(w, 2u) * 12928 + (lane < 32 ? lane * 16 : 4608 + ((lane >> 1) & 1) * 4160 + 4096 + (lane & 1) * 16);
    u32x4 sw[3], mx[3];
  asm volatile("ds_write_b128 %0, %1 offset:0" :  : "v"(za), "v"(((u32x4){0u, 0u, 0u, 0u})) : "memory"); SB();
  asm volatile("v_mov_b32_dpp v230, v219 wave_shr:1 row_mask:0xf bank_mask:0xf\n\tv_alignbit_b32 %0, v219, v219, 16\n\tv_alignbit_b32 %1, v218, v218, 16\n\tv_alignbit_b32 %2, v217, v217, 16\n\tv_alignbit_b32 %3, v216, v216, 16\n\tv_bfi_b32 %4, %8, v219, v218\n\tv_bfi_b32 %5, %8, v218, v217\n\tv_bfi_b32 %6, %8, v217, v216\n\tv_bfi_b32 %7, %8, v216, v230" : "=&v"(sw[0][0]), "=&v"(sw[0][1]), "=&v"(sw[0][2]), "=&v"(sw[0][3]), "=&v"(mx[0][0]), "=&v"(mx[0][1]), "=&v"(mx[0][2]), "=&v"(mx[0][3]) : "s"(0xFFFFu) : ); SB();
  asm volatile("ds_write_b128 %0, %1 offset:0" :  : "v"(ha), "v"(sw[0]) : "memory"); SB();
  asm volatile("ds_write_b128 %0, %1 offset:4160" :  : "v"(ha), "v"(mx[0]) : "memory"); SB();
  asm volatile("s_waitcnt lgkmcnt(3)" :  :  : "memory"); SB();
  asm volatile("s_add_u32 m0, %0, 98048" :  : "s"(mA) : "memory", "scc"); SB();
  asm volatile("s_nop 0\n\tbuffer_load_dwordx4 %0, %1, %2 offen offset:256 lds" :  : "v"(voS), "s"(rA), "s"((0 ^ (2 * w)) * 32768) : "memory"); SB();
  asm volatile("s_add_u32 m0, %0, 99072" :  : "s"(mA) : "memory", "scc"); SB();
  asm volatile("s_nop 0\n\tbuffer_load_dwordx4 %0, %1, %2 offen offset:256 lds" :  : "v"(voS), "s"(rA), "s"((1 ^ (2 * w)) * 32768) : "memory"); SB();
  asm volatile("v_mov_b32_dpp v231, v223 wave_shr:1 row_mask:0xf bank_mask:0xf\n\tv_alignbit_b32 %0, v223, v223, 16\n\tv_alignbit_b32 %1, v222, v222, 16\n\tv_alignbit_b32 %2, v221, v221, 16\n\tv_alignbit_b32 %3, v220, v220, 16\n\tv_bfi_b32 %4, %8, v223, v222\n\tv_bfi_b32 %5, %8, v222, v221\n\tv_bfi_b32 %6, %8, v221, v220\n\tv_bfi_b32 %7, %8, v220, v231" : "=&v"(sw[1][0]), "=&v"(sw[1][1]), "=&v"(sw[1][2]), "=&v"(sw[1][3]), "=&v"(mx[1][0]), "=&v"(mx[1][1]), "=&v"(mx[1][2]), "=&v"(mx[1][3]) : "s"(0xFFFFu) : ); SB();
  asm volatile("ds_write_b128 %0, %1 offset:12928" :  : "v"(ha), "v"(sw[1]) : "memory"); SB();
  asm volatile("ds_write_b128 %0, %1 offset:17088" :  : "v"(ha), "v"(mx[1]) : "memory"); SB();
  asm volatile("ds_read_b128 a[164:167], %0 offset:2048" :  : "v"(aA[0]) : "memory"); SB();
  asm volatile("ds_read_b128 a[180:183], %0 offset:2048" :  : "v"(aA[1]) : "memory"); SB();
  asm volatile("v_mov_b32_dpp v232, v227 wave_shr:1 row_mask:0xf bank_mask:0xf\n\tv_alignbit_b32 %0, v227, v227, 16\n\tv_alignbit_b32 %1, v226, v226, 16\n\tv_alignbit_b32 %2, v225, v225, 16\n\tv_alignbit_b32 %3, v224, v224, 16\n\tv_bfi_b32 %4, %8, v227, v226\n\tv_bfi_b32 %5, %8, v226, v225\n\tv_bfi_b32 %6, %8, v225, v224\n\tv_bfi_b32 %7, %8, v224, v232" : "=&v"(sw[2][0]), "=&v"(sw[2][1]), "=&v"(sw[2][2]), "=&v"(sw[2][3]), "=&v"(mx[2][0]), "=&v"(mx[2][1]), "=&v"(mx[2][2]), "=&v"(mx[2][3]) : "s"(0xFFFFu) : ); SB();
  asm volatile("ds_write_b128 %0, %1 offset:25856" :  : "v"(ha), "v"(sw[2]) : "memory"); SB();
  asm volatile("ds_write_b128 %0, %1 offset:30016" :  : "v"(ha), "v"(mx[2]) : "memory"); SB();
  }
  asm volatile("s_waitcnt vmcnt(9)\n\t.Lc_%=:\n\tv_cmpx_eq_u64 vcc, %1, v[228:229]\n\ts_cmp_eq_u64 vcc, -1\n\ts_mov_b64 exec, -1\n\ts_cbranch_scc0 .Lr_%=\n\t.Ld_%=:\n\t.subsection 1\n\t.Lr_%=:\n\ts_sub_i32 %0, %0, 1\n\ts_cmp_lt_i32 %0, 1\n\ts_cbranch_scc1 .Ld_%=\n\ts_sleep 2\n\tbuffer_load_dwordx2 v[228:229], %2, %3, 0 offen sc1\n\ts_waitcnt vmcnt(0)\n\ts_branch .Lc_%=\n\t.subsection 0" : "+s"(tmo) : "s"(tg), "v"(vFK), "s"(rWS) : "memory", "vcc", "scc"); SB();
  asm volatile("s_add_u32 m0, %0, 49664" :  : "s"(mU) : "memory", "scc"); SB();
  asm volatile("s_nop 0\n\tbuffer_load_dwordx4 %0, %1, %2 offen sc1 lds" :  : "v"(vF), "s"(rWS), "s"(W_KV + 0u) : "memory"); SB();
  asm volatile("s_add_u32 m0, %0, 62592" :  : "s"(mU) : "memory", "scc"); SB();
  asm volatile("s_nop 0\n\tbuffer_load_dwordx4 %0, %1, %2 offen sc1 lds" :  : "v"(vF), "s"(rWS), "s"(W_KV + 4096u) : "memory"); SB();
  asm volatile("s_add_u32 m0, %0, 75520" :  : "s"(mU) : "memory", "scc"); SB();
  asm volatile("s_nop 0\n\tbuffer_load_dwordx4 %0, %1, %2 offen sc1 lds" :  : "v"(vF), "s"(rWS), "s"(W_KV + c2s) : "memory"); SB();
  ST(12);
  asm volatile("s_waitcnt lgkmcnt(0)\n\ts_barrier" :  :  : "memory"); SB();
  f32x4 co[3];
  asm volatile("ds_read_b128 %0, %1 offset:0" : "=&v"(co[0]) : "v"(cr) : "memory"); SB();
  asm volatile("ds_read_b128 %0, %1 offset:64" : "=&v"(co[1]) : "v"(cr) : "memory"); SB();
  asm volatile("ds_read_b128 %0, %1 offset:128" : "=&v"(co[2]) : "v"(cr) : "memory"); SB();
  asm volatile("s_waitcnt lgkmcnt(0)" : "+v"(co[0]), "+v"(co[1]), "+v"(co[2]) :  : "memory"); SB();
#pragma unroll
  for (int j = 0; j < 3; ++j) {
    const f32x4 cs = c1[j] + co[j];
#pragma unroll
    for (int i = 0; i < 2; ++i) { a1[i][j] = __builtin_elementwise_fma((f32x4){-xm[i], -xm[i], -xm[i], -xm[i]}, cs, a1[i][j]); asm volatile("" : "+v"(a1[i][j])); }
  }
  ST(13);
  SB();
  asm volatile("s_add_u32 m0, %0, 100096" :  : "s"(mA) : "memory", "scc"); SB();
  asm volatile("s_nop 0\n\tbuffer_load_dwordx4 %0, %1, %2 offen offset:256 lds" :  : "v"(voS), "s"(rA), "s"((2 ^ (2 * w)) * 32768) : "memory"); SB();
  asm volatile("s_add_u32 m0, %0, 101120" :  : "s"(mA) : "memory", "scc"); SB();
  asm volatile("s_nop 0\n\tbuffer_load_dwordx4 %0, %1, %2 offen offset:256 lds" :  : "v"(voS), "s"(rA), "s"((3 ^ (2 * w)) * 32768) : "memory"); SB();
  { unsigned lane = lane0, w = w0, rb = rb0, cb = cb0;
  asm volatile("" : "+v"(lane)); asm volatile("" : "+s"(w), "+s"(rb), "+s"(cb)); SB();
  const unsigned wm = w >> 1, wn = w & 1, g4 = lane >> 4, l15 = lane & 15, q4 = l15 >> 2, p4 = l15 & 3;
  const unsigned nch = 3 - (rb >> 4), ch0 = cb * 80 + rb * 2 + min(rb, 16u);
  const unsigned l16 = lane * 16;
  const unsigned vZ = W_Z2 + ((rb * 2 + wm) * 48 + cb * 6 + wn * 3) * 1024 + l16;
  const unsigned vH4 = W_HH + ((rb * 4 + wm * 2) * 24 + cb * 3 + wn * 2) * 1024 + l16, vH2 = W_HH + ((rb * 4 + w) * 24 + cb * 3 + 1) * 1024 + l16;
  const unsigned vY = W_YY + ch0 * 4096 + w * 1024 + l16;
  const unsigned c2o = (nch - 2) * 2, c2s = 8192u + ((rb >> 4) << 30), w3s = (w + 1) >> 2 << 30;
  (void)wm; (void)wn; (void)g4; (void)q4; (void)p4; (void)vZ; (void)vH4; (void)vH2; (void)vY; (void)c2o; (void)c2s; (void)w3s;
  f32x4 ac[8];
  u32x4 cB[2][8];
  unsigned aa[4], bb;
  unsigned cu = w * 2 * nch;
  {
    const unsigned c = (l15 + 1) & 1;
    const unsigned o = lb + 49152 + (cu >> 3) * 12928 - (cu & 7) * 64;
#pragma unroll
    for (int k = 0; k < 4; ++k) aa[k] = o + 4608 + c * 4160 + (2031 - l15 + 8 * g4 - c) * 2 - 512 - 1024 * k;
    bb = o + 512 + 32 * l15 - 32 + 16 * g4;
  }
  unsigned da = lb + 0 + g4 * 256 + ((l15 ^ ((g4 >> 1) << 3)) << 4);
  unsigned vB[2] = {W_Z2 + (w * 768 + cb * 6 + (wn ^ 1) * 3) * 1024 + l16, W_Z2 + (w * 768 + cb * 6 + wn * 3) * 1024 + l16};
  unsigned vFZ = W_FZ + cb * 1024 + w * 256 + (lane & 31) * 8;
  asm volatile("" : "+v"(aa[0]), "+v"(aa[1]), "+v"(aa[2]), "+v"(aa[3]), "+v"(bb), "+v"(da), "+v"(vB[0]), "+v"(vB[1]), "+v"(vFZ));
  SB();
  asm volatile("ds_read_b128 a[168:171], %0 offset:4096" :  : "v"(aA[0]) : "memory"); SB();
  asm volatile("ds_read_b128 a[184:187], %0 offset:4096" :  : "v"(aA[1]) : "memory"); SB();
  asm volatile("s_waitcnt lgkmcnt(0)" :  :  : "memory"); SB();
  asm volatile("s_add_u32 m0, %0, 102144" :  : "s"(mA) : "memory", "scc"); SB();
  asm volatile("s_nop 0\n\tbuffer_load_dwordx4 %0, %1, %2 offen offset:256 lds" :  : "v"(voS), "s"(rA), "s"((4 ^ (2 * w)) * 32768) : "memory"); SB();
  asm volatile("s_add_u32 m0, %0, 103168" :  : "s"(mA) : "memory", "scc"); SB();
  asm volatile("s_nop 0\n\tbuffer_load_dwordx4 %0, %1, %2 offen offset:256 lds" :  : "v"(voS), "s"(rA), "s"((5 ^ (2 * w)) * 32768) : "memory"); SB();
  asm volatile("ds_read_b128 a[172:175], %0 offset:6144" :  : "v"(aA[0]) : "memory"); SB();
  asm volatile("ds_read_b128 a[188:191], %0 offset:6144" :  : "v"(aA[1]) : "memory"); SB();
  asm volatile("s_waitcnt lgkmcnt(0)" :  :  : "memory"); SB();
  asm volatile("s_add_u32 m0, %0, 104192" :  : "s"(mA) : "memory", "scc"); SB();
  asm volatile("s_nop 0\n\tbuffer_load_dwordx4 %0, %1, %2 offen offset:256 lds" :  : "v"(voS), "s"(rA), "s"((6 ^ (2 * w)) * 32768) : "memory"); SB();
  asm volatile("s_add_u32 m0, %0, 105216" :  : "s"(mA) : "memory", "scc"); SB();
  asm volatile("s_nop 0\n\tbuffer_load_dwordx4 %0, %1, %2 offen offset:256 lds" :  : "v"(voS), "s"(rA), "s"((7 ^ (2 * w)) * 32768) : "memory"); SB();
  asm volatile("ds_read_b128 a[192:195], %0 offset:8192" :  : "v"(aA[0]) : "memory"); SB();
  asm volatile("ds_read_b128 a[208:211], %0 offset:8192" :  : "v"(aA[1]) : "memory"); SB();
  asm volatile("s_waitcnt vmcnt(6) lgkmcnt(0)\n\ts_barrier" :  :  : "memory"); SB();
  ST(14);
  asm volatile("ds_read2_b32 v[160:161], %0 offset0:128 offset1:129" :  : "v"(aa[0]) : "memory"); SB();
  asm volatile("ds_read2_b32 v[162:163], %0 offset0:130 offset1:131" :  : "v"(aa[0]) : "memory"); SB();
  asm volatile("ds_read2_b32 v[164:165], %0 offset0:0 offset1:1" :  : "v"(aa[0]) : "memory"); SB();
  asm volatile("ds_read2_b32 v[166:167], %0 offset0:2 offset1:3" :  : "v"(aa[0]) : "memory"); SB();
  asm volatile("ds_read2_b32 v[168:169], %0 offset0:128 offset1:129" :  : "v"(aa[1]) : "memory"); SB();
  asm volatile("ds_read2_b32 v[170:171], %0 offset0:130 offset1:131" :  : "v"(aa[1]) : "memory"); SB();
  asm volatile("ds_read2_b32 v[172:173], %0 offset0:0 offset1:1" :  : "v"(aa[1]) : "memory"); SB();
  asm volatile("ds_read2_b32 v[174:175], %0 offset0:2 offset1:3" :  : "v"(aa[1]) : "memory"); SB();
  asm volatile("ds_read2_b32 v[176:177], %0 offset0:128 offset1:129" :  : "v"(aa[2]) : "memory"); SB();
  asm volatile("ds_read2_b32 v[178:179], %0 offset0:130 offset1:131" :  : "v"(aa[2]) : "memory"); SB();
  asm volatile("ds_read2_b32 v[180:181], %0 offset0:0 offset1:1" :  : "v"(aa[2]) : "memory"); SB();
  asm volatile("ds_read2_b32 v[182:183], %0 offset0:2 offset1:3" :  : "v"(aa[2]) : "memory"); SB();
  asm volatile("ds_read2_b32 v[184:185], %0 offset0:128 offset1:129" :  : "v"(aa[3]) : "memory"); SB();
  asm volatile("ds_read2_b32 v[186:187], %0 offset0:130 offset1:131" :  : "v"(aa[3]) : "memory"); SB();
  asm volatile("ds_read2_b32 v[188:189], %0 offset0:0 offset1:1" :  : "v"(aa[3]) : "memory"); SB();
  asm volatile("ds_read2_b32 v[190:191], %0 offset0:2 offset1:3" :  : "v"(aa[3]) : "memory"); SB();
  asm volatile("ds_read_b128 %0, %1 offset:0" : "=&v"(cB[0][0]) : "v"(bb) : "memory"); SB();
  asm volatile("ds_read_b128 %0, %1 offset:512" : "=&v"(cB[0][1]) : "v"(bb) : "memory"); SB();
  asm volatile("ds_read_b128 %0, %1 offset:1024" : "=&v"(cB[0][2]) : "v"(bb) : "memory"); SB();
  asm volatile("ds_read_b128 %0, %1 offset:1536" : "=&v"(cB[0][3]) : "v"(bb) : "memory"); SB();
  asm volatile("ds_read_b128 %0, %1 offset:2048" : "=&v"(cB[0][4]) : "v"(bb) : "memory"); SB();
  asm volatile("ds_read_b128 %0, %1 offset:2560" : "=&v"(cB[0][5]) : "v"(bb) : "memory"); SB();
  asm volatile("ds_read_b128 %0, %1 offset:3072" : "=&v"(cB[0][6]) : "v"(bb) : "memory"); SB();
  asm volatile("ds_read_b128 %0, %1 offset:3584" : "=&v"(cB[0][7]) : "v"(bb) : "memory"); SB();
  asm volatile("s_add_u32 m0, %0, 106112" :  : "s"(mA) : "memory", "scc"); SB();
  asm volatile("s_nop 0\n\tbuffer_load_dwordx4 %0, %1, %2 offen offset:384 lds" :  : "v"(voS), "s"(rA), "s"((0 ^ (2 * w)) * 32768) : "memory"); SB();
  asm volatile("s_add_u32 m0, %0, 107136" :  : "s"(mA) : "memory", "scc"); SB();
  asm volatile("s_nop 0\n\tbuffer_load_dwordx4 %0, %1, %2 offen offset:384 lds" :  : "v"(voS), "s"(rA), "s"((1 ^ (2 * w)) * 32768) : "memory"); SB();
  asm volatile("s_waitcnt lgkmcnt(0)" : "+v"(cB[0][0]), "+v"(cB[0][1]), "+v"(cB[0][2]), "+v"(cB[0][3]), "+v"(cB[0][4]), "+v"(cB[0][5]), "+v"(cB[0][6]), "+v"(cB[0][7]) :  : "memory"); SB();
  { ++cu; const int dl = (cu & 7) ? -64 : 13376;
#pragma unroll
    for (int k = 0; k < 4; ++k) aa[k] += dl;
    bb += dl; SB(); }
  asm volatile("v_mfma_f32_16x16x32_bf16 %0, v[160:163], %1, 0" : "=&v"(ac[7]) : "v"(cB[0][7]) : ); SB();
  asm volatile("ds_read2_b32 v[192:193], %0 offset0:128 offset1:129" :  : "v"(aa[0]) : "memory"); SB();
  asm volatile("ds_read_b128 a[196:199], %0 offset:10240" :  : "v"(aA[0]) : "memory"); SB();
  asm volatile("v_mfma_f32_16x16x32_bf16 %0, v[160:163], %1, 0" : "=&v"(ac[6]) : "v"(cB[0][6]) : ); SB();
  asm volatile("ds_read2_b32 v[194:195], %0 offset0:130 offset1:131" :  : "v"(aa[0]) : "memory"); SB();
  asm volatile("ds_read_b128 a[212:215], %0 offset:10240" :  : "v"(aA[1]) : "memory"); SB();
  asm volatile("v_mfma_f32_16x16x32_bf16 %0, v[160:163], %1, 0" : "=&v"(ac[5]) : "v"(cB[0][5]) : ); SB();
  asm volatile("ds_read2_b32 v[196:197], %0 offset0:0 offset1:1" :  : "v"(aa[0]) : "memory"); SB();
  asm volatile("v_mfma_f32_16x16x32_bf16 %0, v[160:163], %1, 0" : "=&v"(ac[4]) : "v"(cB[0][4]) : ); SB();
  asm volatile("ds_read2_b32 v[198:199], %0 offset0:2 offset1:3" :  : "v"(aa[0]) : "memory"); SB();
  asm volatile("v_mfma_f32_16x16x32_bf16 %0, v[164:167], %1, %0" : "+v"(ac[7]) : "v"(cB[0][6]) : ); SB();
  asm volatile("ds_read2_b32 v[200:201], %0 offset0:128 offset1:129" :  : "v"(aa[1]) : "memory"); SB();
  asm volatile("v_mfma_f32_16x16x32_bf16 %0, v[164:167], %1, %0" : "+v"(ac[6]) : "v"(cB[0][5]) : ); SB();
  asm volatile("ds_read2_b32 v[202:203], %0 offset0:130 offset1:131" :  : "v"(aa[1]) : "memory"); SB();
  asm volatile("v_mfma_f32_16x16x32_bf16 %0, v[164:167], %1, %0" : "+v"(ac[5]) : "v"(cB[0][4]) : ); SB();
  asm volatile("ds_read2_b32 v[204:205], %0 offset0:0 offset1:1" :  : "v"(aa[1]) : "memory"); SB();
  asm volatile("v_mfma_f32_16x16x32_bf16 %0, v[164:167], %1, %0" : "+v"(ac[4]) : "v"(cB[0][3]) : ); SB();
  asm volatile("ds_read2_b32 v[206:207], %0 offset0:2 offset1:3" :  : "v"(aa[1]) : "memory"); SB();
  asm volatile("v_mfma_f32_16x16x32_bf16 %0, v[168:171], %1, %0" : "+v"(ac[7]) : "v"(cB[0][5]) : ); SB();
  asm volatile("ds_read2_b32 v[208:209], %0 offset0:128 offset1:129" :  : "v"(aa[2]) : "memory"); SB();
  asm volatile("s_waitcnt lgkmcnt(7)" :  :  : "memory"); SB();
  asm volatile("v_mfma_f32_16x16x32_bf16 %0, v[168:171], %1, %0" : "+v"(ac[6]) : "v"(cB[0][4]) : ); SB();
  asm volatile("ds_read2_b32 v[210:211], %0 offset0:130 offset1:131" :  : "v"(aa[2]) : "memory"); SB();
  asm volatile("s_add_u32 m0, %0, 108160" :  : "s"(mA) : "memory", "scc"); SB();
  asm volatile("v_mfma_f32_16x16x32_bf16 %0, v[168:171], %1, %0" : "+v"(ac[5]) : "v"(cB[0][3]) : ); SB();
  asm volatile("ds_read2_b32 v[212:213], %0 offset0:0 offset1:1" :  : "v"(aa[2]) : "memory"); SB();
  asm volatile("buffer_load_dwordx4 %0, %1, %2 offen offset:384 lds" :  : "v"(voS), "s"(rA), "s"((2 ^ (2 * w)) * 32768) : "memory"); SB();
  asm volatile("v_mfma_f32_16x16x32_bf16 %0, v[160:163], %1, 0" : "=&v"(ac[3]) : "v"(cB[0][3]) : ); SB();
  asm volatile("ds_read2_b32 v[214:215], %0 offset0:2 offset1:3" :  : "v"(aa[2]) : "memory"); SB();
  asm volatile("s_add_u32 m0, %0, 109184" :  : "s"(mA) : "memory", "scc"); SB();
  asm volatile("v_mfma_f32_16x16x32_bf16 %0, v[172:175], %1, %0" : "+v"(ac[7]) : "v"(cB[0][4]) : ); SB();
  asm volatile("ds_read2_b32 v[216:217], %0 offset0:128 offset1:129" :  : "v"(aa[3]) : "memory"); SB();
  asm volatile("buffer_load_dwordx4 %0, %1, %2 offen offset:384 lds" :  : "v"(voS), "s"(rA), "s"((3 ^ (2 * w)) * 32768) : "memory"); SB();
  asm volatile("v_mfma_f32_16x16x32_bf16 %0, v[172:175], %1, %0" : "+v"(ac[6]) : "v"(cB[0][3]) : ); SB();
  asm volatile("ds_read2_b32 v[218:219], %0 offset0:130 offset1:131" :  : "v"(aa[3]) : "memory"); SB();
  asm volatile("v_mfma_f32_16x16x32_bf16 %0, v[172:175], %1, %0" : "+v"(ac[5]) : "v"(cB[0][2]) : ); SB();
  asm volatile("ds_read2_b32 v[220:221], %0 offset0:0 offset1:1" :  : "v"(aa[3]) : "memory"); SB();
  asm volatile("ds_read_b128 a[200:203], %0 offset:12288" :  : "v"(aA[0]) : "memory"); SB();
  asm volatile("v_mfma_f32_16x16x32_bf16 %0, v[168:171], %1, %0" : "+v"(ac[4]) : "v"(cB[0][2]) : ); SB();
  asm volatile("ds_read2_b32 v[222:223], %0 offset0:2 offset1:3" :  : "v"(aa[3]) : "memory"); SB();
  asm volatile("ds_read_b128 a[216:219], %0 offset:12288" :  : "v"(aA[1]) : "memory"); SB();
  asm volatile("v_mfma_f32_16x16x32_bf16 %0, v[176:179], %1, %0" : "+v"(ac[7]) : "v"(cB[0][3]) : ); SB();
  asm volatile("ds_read_b128 %0, %1 offset:0" : "=&v"(cB[1][0]) : "v"(bb) : "memory"); SB();
  asm volatile("v_mfma_f32_16x16x32_bf16 %0, v[176:179], %1, %0" : "+v"(ac[6]) : "v"(cB[0][2]) : ); SB();
  asm volatile("ds_read_b128 %0, %1 offset:512" : "=&v"(cB[1][1]) : "v"(bb) : "memory"); SB();
  asm volatile("v_mfma_f32_16x16x32_bf16 %0, v[164:167], %1, %0" : "+v"(ac[3]) : "v"(cB[0][2]) : ); SB();
  asm volatile("ds_read_b128 %0, %1 offset:1024" : "=&v"(cB[1][2]) : "v"(bb) : "memory"); SB();
  asm volatile("v_mfma_f32_16x16x32_bf16 %0, v[160:163], %1, 0" : "=&v"(ac[2]) : "v"(cB[0][2]) : ); SB();
  asm volatile("ds_read_b128 %0, %1 offset:1536" : "=&v"(cB[1][3]) : "v"(bb) : "memory"); SB();
  asm volatile("v_mfma_f32_16x16x32_bf16 %0, v[180:183], %1, %0" : "+v"(ac[7]) : "v"(cB[0][2]) : ); SB();
  asm volatile("ds_read_b128 %0, %1 offset:2048" : "=&v"(cB[1][4]) : "v"(bb) : "memory"); SB();
  asm volatile("v_mfma_f32_16x16x32_bf16 %0, v[180:183], %1, %0" : "+v"(ac[6]) : "v"(cB[0][1]) : ); SB();
  asm volatile("ds_read_b128 %0, %1 offset:2560" : "=&v"(cB[1][5]) : "v"(bb) : "memory"); SB();
  asm volatile("v_mfma_f32_16x16x32_bf16 %0, v[176:179], %1, %0" : "+v"(ac[5]) : "v"(cB[0][1]) : ); SB();
  asm volatile("ds_read_b128 %0, %1 offset:3072" : "=&v"(cB[1][6]) : "v"(bb) : "memory"); SB();
  asm volatile("s_waitcnt lgkmcnt(7)" :  :  : "memory"); SB();
  asm volatile("v_mfma_f32_16x16x32_bf16 %0, v[172:175], %1, %0" : "+v"(ac[4]) : "v"(cB[0][1]) : ); SB();
  asm volatile("ds_read_b128 %0, %1 offset:3584" : "=&v"(cB[1][7]) : "v"(bb) : "memory"); SB();
  asm volatile("s_add_u32 m0, %0, 110208" :  : "s"(mA) : "memory", "scc"); SB();
  asm volatile("v_mfma_f32_16x16x32_bf16 %0, v[184:187], %1, %0" : "+v"(ac[7]) : "v"(cB[0][1]) : ); SB();
  asm volatile("buffer_load_dwordx4 %0, %1, %2 offen offset:384 lds" :  : "v"(voS), "s"(rA), "s"((4 ^ (2 * w)) * 32768) : "memory"); SB();
  asm volatile("s_add_u32 m0, %0, 111232" :  : "s"(mA) : "memory", "scc"); SB();
  asm volatile("v_mfma_f32_16x16x32_bf16 %0, v[168:171], %1, %0" : "+v"(ac[3]) : "v"(cB[0][1]) : ); SB();
  asm volatile("buffer_load_dwordx4 %0, %1, %2 offen offset:384 lds" :  : "v"(voS), "s"(rA), "s"((5 ^ (2 * w)) * 32768) : "memory"); SB();
  asm volatile("v_mfma_f32_16x16x32_bf16 %0, v[164:167], %1, %0" : "+v"(ac[2]) : "v"(cB[0][1]) : ); SB();
  asm volatile("ds_read_b128 a[204:207], %0 offset:14336" :  : "v"(aA[0]) : "memory"); SB();
  asm volatile("ds_read_b128 a[220:223], %0 offset:14336" :  : "v"(aA[1]) : "memory"); SB();
  asm volatile("v_mfma_f32_16x16x32_bf16 %0, v[160:163], %1, 0" : "=&v"(ac[1]) : "v"(cB[0][1]) : ); SB();
  asm volatile("v_mfma_f32_16x16x32_bf16 %0, v[188:191], %1, %0" : "+v"(ac[7]) : "v"(cB[0][0]) : ); SB();
  asm volatile("v_mfma_f32_16x16x32_bf16 %0, v[184:187], %1, %0" : "+v"(ac[6]) : "v"(cB[0][0]) : ); SB();
  asm volatile("v_mfma_f32_16x16x32_bf16 %0, v[180:183], %1, %0" : "+v"(ac[5]) : "v"(cB[0][0]) : ); SB();
  asm volatile("v_mfma_f32_16x16x32_bf16 %0, v[176:179], %1, %0" : "+v"(ac[4]) : "v"(cB[0][0]) : ); SB();
  asm volatile("v_mfma_f32_16x16x32_bf16 %0, v[172:175], %1, %0" : "+v"(ac[3]) : "v"(cB[0][0]) : ); SB();
  asm volatile("v_mfma_f32_16x16x32_bf16 %0, v[168:171], %1, %0" : "+v"(ac[2]) : "v"(cB[0][0]) : ); SB();
  asm volatile("v_mfma_f32_16x16x32_bf16 %0, v[164:167], %1, %0" : "+v"(ac[1]) : "v"(cB[0][0]) : ); SB();
  asm volatile("s_waitcnt lgkmcnt(0)" :  :  : "memory"); SB();
  asm volatile("s_add_u32 m0, %0, 112256" :  : "s"(mA) : "memory", "scc"); SB();
  asm volatile("v_mfma_f32_16x16x32_bf16 %0, v[160:163], %1, 0" : "=&v"(ac[0]) : "v"(cB[0][0]) : ); SB();
  asm volatile("buffer_load_dwordx4 %0, %1, %2 offen offset:384 lds" :  : "v"(voS), "s"(rA), "s"((6 ^ (2 * w)) * 32768) : "memory"); SB();
  asm volatile("s_add_u32 m0, %0, 113280" :  : "s"(mA) : "memory", "scc"); SB();
  asm volatile("" : "+v"(cB[1][0]), "+v"(cB[1][1]), "+v"(cB[1][2]), "+v"(cB[1][3]), "+v"(cB[1][4]), "+v"(cB[1][5]), "+v"(cB[1][6]), "+v"(cB[1][7]) :  : "memory"); SB();
  { ++cu; const int dl = (cu & 7) ? -64 : 13376;
#pragma unroll
    for (int k = 0; k < 4; ++k) aa[k] += dl;
    bb += dl; SB(); }
  asm volatile("v_mfma_f32_16x16x32_bf16 %0, v[192:195], %1, %0" : "+v"(ac[7]) : "v"(cB[1][7]) : ); SB();
  asm volatile("ds_read2_b32 v[160:161], %0 offset0:128 offset1:129" :  : "v"(aa[0]) : "memory"); SB();
  asm volatile("buffer_load_dwordx4 %0, %1, %2 offen offset:384 lds" :  : "v"(voS), "s"(rA), "s"((7 ^ (2 * w)) * 32768) : "memory"); SB();
  asm volatile("v_mfma_f32_16x16x32_bf16 %0, v[192:195], %1, %0" : "+v"(ac[6]) : "v"(cB[1][6]) : ); SB();
  asm volatile("ds_read2_b32 v[162:163], %0 offset0:130 offset1:131" :  : "v"(aa[0]) : "memory"); SB();
  asm volatile("v_mfma_f32_16x16x32_bf16 %0, v[192:195], %1, %0" : "+v"(ac[5]) : "v"(cB[1][5]) : ); SB();
  asm volatile("ds_read2_b32 v[164:165], %0 offset0:0 offset1:1" :  : "v"(aa[0]) : "memory"); SB();
  asm volatile("ds_read_b128 a[224:227], %0 offset:0" :  : "v"(aA[0]) : "memory"); SB();
  asm volatile("v_mfma_f32_16x16x32_bf16 %0, v[192:195], %1, %0" : "+v"(ac[4]) : "v"(cB[1][4]) : ); SB();
  asm volatile("ds_read2_b32 v[166:167], %0 offset0:2 offset1:3" :  : "v"(aa[0]) : "memory"); SB();
  asm volatile("ds_read_b128 a[240:243], %0 offset:0" :  : "v"(aA[1]) : "memory"); SB();
  asm volatile("v_mfma_f32_16x16x32_bf16 %0, v[196:199], %1, %0" : "+v"(ac[7]) : "v"(cB[1][6]) : ); SB();
  asm volatile("ds_read2_b32 v[168:169], %0 offset0:128 offset1:129" :  : "v"(aa[1]) : "memory"); SB();
  asm volatile("v_mfma_f32_16x16x32_bf16 %0, v[196:199], %1, %0" : "+v"(ac[6]) : "v"(cB[1][5]) : ); SB();
  asm volatile("ds_read2_b32 v[170:171], %0 offset0:130 offset1:131" :  : "v"(aa[1]) : "memory"); SB();
  asm volatile("v_mfma_f32_16x16x32_bf16 %0, v[196:199], %1, %0" : "+v"(ac[5]) : "v"(cB[1][4]) : ); SB();
  asm volatile("ds_read2_b32 v[172:173], %0 offset0:0 offset1:1" :  : "v"(aa[1]) : "memory"); SB();
  asm volatile("v_mfma_f32_16x16x32_bf16 %0, v[196:199], %1, %0" : "+v"(ac[4]) : "v"(cB[1][3]) : ); SB();
  asm volatile("ds_read2_b32 v[174:175], %0 offset0:2 offset1:3" :  : "v"(aa[1]) : "memory"); SB();
  asm volatile("v_mfma_f32_16x16x32_bf16 %0, v[200:203], %1, %0" : "+v"(ac[7]) : "v"(cB[1][5]) : ); SB();
  asm volatile("ds_read2_b32 v[176:177], %0 offset0:128 offset1:129" :  : "v"(aa[2]) : "memory"); SB();
  asm volatile("v_mfma_f32_16x16x32_bf16 %0, v[200:203], %1, %0" : "+v"(ac[6]) : "v"(cB[1][4]) : ); SB();
  asm volatile("ds_read2_b32 v[178:179], %0 offset0:130 offset1:131" :  : "v"(aa[2]) : "memory"); SB();
  asm volatile("v_mfma_f32_16x16x32_bf16 %0, v[200:203], %1, %0" : "+v"(ac[5]) : "v"(cB[1][3]) : ); SB();
  asm volatile("ds_read2_b32 v[180:181], %0 offset0:0 offset1:1" :  : "v"(aa[2]) : "memory"); SB();
  asm volatile("s_waitcnt lgkmcnt(7)" :  :  : "memory"); SB();
  asm volatile("v_mfma_f32_16x16x32_bf16 %0, v[192:195], %1, %0" : "+v"(ac[3]) : "v"(cB[1][3]) : ); SB();
  asm volatile("ds_read2_b32 v[182:183], %0 offset0:2 offset1:3" :  : "v"(aa[2]) : "memory"); SB();
  asm volatile("s_add_u32 m0, %0, 97792" :  : "s"(mA) : "memory", "scc"); SB();
  asm volatile("v_mfma_f32_16x16x32_bf16 %0, v[204:207], %1, %0" : "+v"(ac[7]) : "v"(cB[1][4]) : ); SB();
  asm volatile("ds_read2_b32 v[184:185], %0 offset0:128 offset1:129" :  : "v"(aa[3]) : "memory"); SB();
  asm volatile("buffer_load_dwordx4 %0, %1, %2 offen offset:512 lds" :  : "v"(voS), "s"(rA), "s"((0 ^ (2 * w)) * 32768) : "memory"); SB();
  asm volatile("v_mfma_f32_16x16x32_bf16 %0, v[204:207], %1, %0" : "+v"(ac[6]) : "v"(cB[1][3]) : ); SB();
  asm volatile("ds_read2_b32 v[186:187], %0 offset0:130 offset1:131" :  : "v"(aa[3]) : "memory"); SB();
  asm volatile("s_add_u32 m0, %0, 98816" :  : "s"(mA) : "memory", "scc"); SB();
  asm volatile("v_mfma_f32_16x16x32_bf16 %0, v[204:207], %1, %0" : "+v"(ac[5]) : "v"(cB[1][2]) : ); SB();
  asm volatile("ds_read2_b32 v[188:189], %0 offset0:0 offset1:1" :  : "v"(aa[3]) : "memory"); SB();
  asm volatile("buffer_load_dwordx4 %0, %1, %2 offen offset:512 lds" :  : "v"(voS), "s"(rA), "s"((1 ^ (2 * w)) * 32768) : "memory"); SB();
  asm volatile("v_mfma_f32_16x16x32_bf16 %0, v[200:203], %1, %0" : "+v"(ac[4]) : "v"(cB[1][2]) : ); SB();
  asm volatile("ds_read2_b32 v[190:191], %0 offset0:2 offset1:3" :  : "v"(aa[3]) : "memory"); SB();
  asm volatile("s_waitcnt vmcnt(14)" :  :  : "memory"); SB();
  asm volatile("v_mfma_f32_16x16x32_bf16 %0, v[208:211], %1, %0" : "+v"(ac[7]) : "v"(cB[1][3]) : ); SB();
  asm volatile("ds_read_b128 %0, %1 offset:0" : "=&v"(cB[0][0]) : "v"(bb) : "memory"); SB();
  asm volatile("ds_read_b128 a[228:231], %0 offset:2048" :  : "v"(aA[0]) : "memory"); SB();
  asm volatile("v_mfma_f32_16x16x32_bf16 %0, v[208:211], %1, %0" : "+v"(ac[6]) : "v"(cB[1][2]) : ); SB();
  asm volatile("ds_read_b128 %0, %1 offset:512" : "=&v"(cB[0][1]) : "v"(bb) : "memory"); SB();
  asm volatile("ds_read_b128 a[244:247], %0 offset:2048" :  : "v"(aA[1]) : "memory"); SB();
  asm volatile("v_mfma_f32_16x16x32_bf16 %0, v[196:199], %1, %0" : "+v"(ac[3]) : "v"(cB[1][2]) : ); SB();
  asm volatile("ds_read_b128 %0, %1 offset:1024" : "=&v"(cB[0][2]) : "v"(bb) : "memory"); SB();
  asm volatile("v_mfma_f32_16x16x32_bf16 %0, v[192:195], %1, %0" : "+v"(ac[2]) : "v"(cB[1][2]) : ); SB();
  asm volatile("ds_read_b128 %0, %1 offset:1536" : "=&v"(cB[0][3]) : "v"(bb) : "memory"); SB();
  asm volatile("v_mfma_f32_16x16x32_bf16 %0, v[212:215], %1, %0" : "+v"(ac[7]) : "v"(cB[1][2]) : ); SB();
  asm volatile("ds_read_b128 %0, %1 offset:2048" : "=&v"(cB[0][4]) : "v"(bb) : "memory"); SB();
  asm volatile("v_mfma_f32_16x16x32_bf16 %0, v[212:215], %1, %0" : "+v"(ac[6]) : "v"(cB[1][1]) : ); SB();
  asm volatile("ds_read_b128 %0, %1 offset:2560" : "=&v"(cB[0][5]) : "v"(bb) : "memory"); SB();
  asm volatile("v_mfma_f32_16x16x32_bf16 %0, v[208:211], %1, %0" : "+v"(ac[5]) : "v"(cB[1][1]) : ); SB();
  asm volatile("ds_read_b128 %0, %1 offset:3072" : "=&v"(cB[0][6]) : "v"(bb) : "memory"); SB();
  asm volatile("v_mfma_f32_16x16x32_bf16 %0, v[204:207], %1, %0" : "+v"(ac[4]) : "v"(cB[1][1]) : ); SB();
  asm volatile("ds_read_b128 %0, %1 offset:3584" : "=&v"(cB[0][7]) : "v"(bb) : "memory"); SB();
  asm volatile("v_mfma_f32_16x16x32_bf16 %0, v[216:219], %1, %0" : "+v"(ac[7]) : "v"(cB[1][1]) : ); SB();
  asm volatile("v_mfma_f32_16x16x32_bf16 %0, v[200:203], %1, %0" : "+v"(ac[3]) : "v"(cB[1][1]) : ); SB();
  asm volatile("s_waitcnt lgkmcnt(6)" :  :  : "memory"); SB();
  asm volatile("s_add_u32 m0, %0, 99840" :  : "s"(mA) : "memory", "scc"); SB();
  asm volatile("v_mfma_f32_16x16x32_bf16 %0, v[196:199], %1, %0" : "+v"(ac[2]) : "v"(cB[1][1]) : ); SB();
  asm volatile("buffer_load_dwordx4 %0, %1, %2 offen offset:512 lds" :  : "v"(voS), "s"(rA), "s"((2 ^ (2 * w)) * 32768) : "memory"); SB();
  asm volatile("s_add_u32 m0, %0, 100864" :  : "s"(mA) : "memory", "scc"); SB();
  asm volatile("v_mfma_f32_16x16x32_bf16 %0, v[192:195], %1, %0" : "+v"(ac[1]) : "v"(cB[1][1]) : ); SB();
  asm volatile("buffer_load_dwordx4 %0, %1, %2 offen offset:512 lds" :  : "v"(voS), "s"(rA), "s"((3 ^ (2 * w)) * 32768) : "memory"); SB();
  asm volatile("s_waitcnt vmcnt(14)" :  :  : "memory"); SB();
  asm volatile("v_mfma_f32_16x16x32_bf16 %0, v[220:223], %1, %0" : "+v"(ac[7]) : "v"(cB[1][0]) : ); SB();
  asm volatile("ds_read_b128 a[232:235], %0 offset:4096" :  : "v"(aA[0]) : "memory"); SB();
  asm volatile("ds_read_b128 a[248:251], %0 offset:4096" :  : "v"(aA[1]) : "memory"); SB();
  asm volatile("v_mfma_f32_16x16x32_bf16 %0, v[216:219], %1, %0" : "+v"(ac[6]) : "v"(cB[1][0]) : ); SB();
  asm volatile("v_mfma_f32_16x16x32_bf16 %0, v[212:215], %1, %0" : "+v"(ac[5]) : "v"(cB[1][0]) : ); SB();
  asm volatile("v_mfma_f32_16x16x32_bf16 %0, v[208:211], %1, %0" : "+v"(ac[4]) : "v"(cB[1][0]) : ); SB();
  asm volatile("v_mfma_f32_16x16x32_bf16 %0, v[204:207], %1, %0" : "+v"(ac[3]) : "v"(cB[1][0]) : ); SB();
  asm volatile("v_mfma_f32_16x16x32_bf16 %0, v[200:203], %1, %0" : "+v"(ac[2]) : "v"(cB[1][0]) : ); SB();
  asm volatile("v_mfma_f32_16x16x32_bf16 %0, v[196:199], %1, %0" : "+v"(ac[1]) : "v"(cB[1][0]) : ); SB();
  asm volatile("v_mfma_f32_16x16x32_bf16 %0, v[192:195], %1, %0" : "+v"(ac[0]) : "v"(cB[1][0]) : ); SB();
  asm volatile("s_waitcnt lgkmcnt(2)" : "+v"(cB[0][0]), "+v"(cB[0][1]), "+v"(cB[0][2]), "+v"(cB[0][3]), "+v"(cB[0][4]), "+v"(cB[0][5]), "+v"(cB[0][6]), "+v"(cB[0][7]) :  : "memory"); SB();
  if ((cu & 7) == 0 && nch == 3) {
  asm volatile("s_nop 7" : "+v"(ac[0]), "+v"(ac[1]), "+v"(ac[2]), "+v"(ac[3]), "+v"(ac[4]), "+v"(ac[5]), "+v"(ac[6]), "+v"(ac[7]) :  : ); SB();
    const unsigned dd = da + (((cu - 1) >> 3) * 2 + 1) * 8192;
  asm volatile("ds_write_b128 %0, %1 offset:0" :  : "v"(dd), "v"(ac[0]) : "memory"); SB();
  asm volatile("ds_write_b128 %0, %1 offset:1024" :  : "v"(dd), "v"(ac[1]) : "memory"); SB();
  asm volatile("ds_write_b128 %0, %1 offset:2048" :  : "v"(dd), "v"(ac[2]) : "memory"); SB();
  asm volatile("ds_write_b128 %0, %1 offset:3072" :  : "v"(dd), "v"(ac[3]) : "memory"); SB();
  asm volatile("ds_write_b128 %0, %1 offset:4096" :  : "v"(dd), "v"(ac[4]) : "memory"); SB();
  asm volatile("ds_write_b128 %0, %1 offset:5120" :  : "v"(dd), "v"(ac[5]) : "memory"); SB();
  asm volatile("ds_write_b128 %0, %1 offset:6144" :  : "v"(dd), "v"(ac[6]) : "memory"); SB();
  asm volatile("ds_write_b128 %0, %1 offset:7168" :  : "v"(dd), "v"(ac[7]) : "memory"); SB();
#pragma unroll
  for (int t = 0; t < 8; ++t) ac[t] = (f32x4){0.f, 0.f, 0.f, 0.f};
  asm volatile("s_nop 1" : "+v"(ac[0]), "+v"(ac[1]), "+v"(ac[2]), "+v"(ac[3]), "+v"(ac[4]), "+v"(ac[5]), "+v"(ac[6]), "+v"(ac[7]) :  : ); SB();
  }
  { ++cu; const int dl = (cu & 7) ? -64 : 13376;
#pragma unroll
    for (int k = 0; k < 4; ++k) aa[k] += dl;
    bb += dl; SB(); }
  asm volatile("v_mfma_f32_16x16x32_bf16 %0, v[160:163], %1, %0" : "+v"(ac[7]) : "v"(cB[0][7]) : ); SB();
  asm volatile("ds_read2_b32 v[192:193], %0 offset0:128 offset1:129" :  : "v"(aa[0]) : "memory"); SB();
  asm volatile("s_waitcnt lgkmcnt(1)" :  :  : "memory"); SB();
  asm volatile("v_mfma_f32_16x16x32_bf16 %0, v[160:163], %1, %0" : "+v"(ac[6]) : "v"(cB[0][6]) : ); SB();
  asm volatile("ds_read2_b32 v[194:195], %0 offset0:130 offset1:131" :  : "v"(aa[0]) : "memory"); SB();
  asm volatile("s_add_u32 m0, %0, 101888" :  : "s"(mA) : "memory", "scc"); SB();
  asm volatile("v_mfma_f32_16x16x32_bf16 %0, v[160:163], %1, %0" : "+v"(ac[5]) : "v"(cB[0][5]) : ); SB();
  asm volatile("ds_read2_b32 v[196:197], %0 offset0:0 offset1:1" :  : "v"(aa[0]) : "memory"); SB();
  asm volatile("buffer_load_dwordx4 %0, %1, %2 offen offset:512 lds" :  : "v"(voS), "s"(rA), "s"((4 ^ (2 * w)) * 32768) : "memory"); SB();
  asm volatile("v_mfma_f32_16x16x32_bf16 %0, v[160:163], %1, %0" : "+v"(ac[4]) : "v"(cB[0][4]) : ); SB();
  asm volatile("ds_read2_b32 v[198:199], %0 offset0:2 offset1:3" :  : "v"(aa[0]) : "memory"); SB();
  asm volatile("s_add_u32 m0, %0, 102912" :  : "s"(mA) : "memory", "scc"); SB();
  asm volatile("v_mfma_f32_16x16x32_bf16 %0, v[164:167], %1, %0" : "+v"(ac[7]) : "v"(cB[0][6]) : ); SB();
  asm volatile("ds_read2_b32 v[200:201], %0 offset0:128 offset1:129" :  : "v"(aa[1]) : "memory"); SB();
  asm volatile("buffer_load_dwordx4 %0, %1, %2 offen offset:512 lds" :  : "v"(voS), "s"(rA), "s"((5 ^ (2 * w)) * 32768) : "memory"); SB();
  asm volatile("v_mfma_f32_16x16x32_bf16 %0, v[164:167], %1, %0" : "+v"(ac[6]) : "v"(cB[0][5]) : ); SB();
  asm volatile("ds_read2_b32 v[202:203], %0 offset0:130 offset1:131" :  : "v"(aa[1]) : "memory"); SB();
  asm volatile("s_waitcnt vmcnt(14)" :  :  : "memory"); SB();
  asm volatile("v_mfma_f32_16x16x32_bf16 %0, v[164:167], %1, %0" : "+v"(ac[5]) : "v"(cB[0][4]) : ); SB();
  asm volatile("ds_read2_b32 v[204:205], %0 offset0:0 offset1:1" :  : "v"(aa[1]) : "memory"); SB();
  asm volatile("ds_read_b128 a[236:239], %0 offset:6144" :  : "v"(aA[0]) : "memory"); SB();
  asm volatile("v_mfma_f32_16x16x32_bf16 %0, v[164:167], %1, %0" : "+v"(ac[4]) : "v"(cB[0][3]) : ); SB();
  asm volatile("ds_read2_b32 v[206:207], %0 offset0:2 offset1:3" :  : "v"(aa[1]) : "memory"); SB();
  asm volatile("ds_read_b128 a[252:255], %0 offset:6144" :  : "v"(aA[1]) : "memory"); SB();
  asm volatile("v_mfma_f32_16x16x32_bf16 %0, v[168:171], %1, %0" : "+v"(ac[7]) : "v"(cB[0][5]) : ); SB();
  asm volatile("ds_read2_b32 v[208:209], %0 offset0:128 offset1:129" :  : "v"(aa[2]) : "memory"); SB();
  asm volatile("v_mfma_f32_16x16x32_bf16 %0, v[168:171], %1, %0" : "+v"(ac[6]) : "v"(cB[0][4]) : ); SB();
  asm volatile("ds_read2_b32 v[210:211], %0 offset0:130 offset1:131" :  : "v"(aa[2]) : "memory"); SB();
  asm volatile("v_mfma_f32_16x16x32_bf16 %0, v[168:171], %1, %0" : "+v"(ac[5]) : "v"(cB[0][3]) : ); SB();
  asm volatile("ds_read2_b32 v[212:213], %0 offset0:0 offset1:1" :  : "v"(aa[2]) : "memory"); SB();
  asm volatile("v_mfma_f32_16x16x32_bf16 %0, v[160:163], %1, %0" : "+v"(ac[3]) : "v"(cB[0][3]) : ); SB();
  asm volatile("ds_read2_b32 v[214:215], %0 offset0:2 offset1:3" :  : "v"(aa[2]) : "memory"); SB();
  asm volatile("v_mfma_f32_16x16x32_bf16 %0, v[172:175], %1, %0" : "+v"(ac[7]) : "v"(cB[0][4]) : ); SB();
  asm volatile("ds_read2_b32 v[216:217], %0 offset0:128 offset1:129" :  : "v"(aa[3]) : "memory"); SB();
  asm volatile("v_mfma_f32_16x16x32_bf16 %0, v[172:175], %1, %0" : "+v"(ac[6]) : "v"(cB[0][3]) : ); SB();
  asm volatile("ds_read2_b32 v[218:219], %0 offset0:130 offset1:131" :  : "v"(aa[3]) : "memory"); SB();
  asm volatile("v_mfma_f32_16x16x32_bf16 %0, v[172:175], %1, %0" : "+v"(ac[5]) : "v"(cB[0][2]) : ); SB();
  asm volatile("ds_read2_b32 v[220:221], %0 offset0:0 offset1:1" :  : "v"(aa[3]) : "memory"); SB();
  asm volatile("s_waitcnt lgkmcnt(7)" :  :  : "memory"); SB();
  asm volatile("v_mfma_f32_16x16x32_bf16 %0, v[168:171], %1, %0" : "+v"(ac[4]) : "v"(cB[0][2]) : ); SB();
  asm volatile("ds_read2_b32 v[222:223], %0 offset0:2 offset1:3" :  : "v"(aa[3]) : "memory"); SB();
  asm volatile("s_add_u32 m0, %0, 103936" :  : "s"(mA) : "memory", "scc"); SB();
  asm volatile("v_mfma_f32_16x16x32_bf16 %0, v[176:179], %1, %0" : "+v"(ac[7]) : "v"(cB[0][3]) : ); SB();
  asm volatile("ds_read_b128 %0, %1 offset:0" : "=&v"(cB[1][0]) : "v"(bb) : "memory"); SB();
  asm volatile("buffer_load_dwordx4 %0, %1, %2 offen offset:512 lds" :  : "v"(voS), "s"(rA), "s"((6 ^ (2 * w)) * 32768) : "memory"); SB();
  asm volatile("v_mfma_f32_16x16x32_bf16 %0, v[176:179], %1, %0" : "+v"(ac[6]) : "v"(cB[0][2]) : ); SB();
  asm volatile("ds_read_b128 %0, %1 offset:512" : "=&v"(cB[1][1]) : "v"(bb) : "memory"); SB();
  asm volatile("s_add_u32 m0, %0, 104960" :  : "s"(mA) : "memory", "scc"); SB();
  asm volatile("v_mfma_f32_16x16x32_bf16 %0, v[164:167], %1, %0" : "+v"(ac[3]) : "v"(cB[0][2]) : ); SB();
  asm volatile("ds_read_b128 %0, %1 offset:1024" : "=&v"(cB[1][2]) : "v"(bb) : "memory"); SB();
  asm volatile("buffer_load_dwordx4 %0, %1, %2 offen offset:512 lds" :  : "v"(voS), "s"(rA), "s"((7 ^ (2 * w)) * 32768) : "memory"); SB();
  asm volatile("v_mfma_f32_16x16x32_bf16 %0, v[160:163], %1, %0" : "+v"(ac[2]) : "v"(cB[0][2]) : ); SB();
  asm volatile("ds_read_b128 %0, %1 offset:1536" : "=&v"(cB[1][3]) : "v"(bb) : "memory"); SB();
  asm volatile("s_waitcnt vmcnt(14)" :  :  : "memory"); SB();
  asm volatile("v_mfma_f32_16x16x32_bf16 %0, v[180:183], %1, %0" : "+v"(ac[7]) : "v"(cB[0][2]) : ); SB();
  asm volatile("ds_read_b128 %0, %1 offset:2048" : "=&v"(cB[1][4]) : "v"(bb) : "memory"); SB();
  asm volatile("ds_read_b128 a[0:3], %0 offset:8192" :  : "v"(aA[0]) : "memory"); SB();
  asm volatile("v_mfma_f32_16x16x32_bf16 %0, v[180:183], %1, %0" : "+v"(ac[6]) : "v"(cB[0][1]) : ); SB();
  asm volatile("ds_read_b128 %0, %1 offset:2560" : "=&v"(cB[1][5]) : "v"(bb) : "memory"); SB();
  asm volatile("ds_read_b128 a[16:19], %0 offset:8192" :  : "v"(aA[1]) : "memory"); SB();
  asm volatile("v_mfma_f32_16x16x32_bf16 %0, v[176:179], %1, %0" : "+v"(ac[5]) : "v"(cB[0][1]) : ); SB();
  asm volatile("ds_read_b128 %0, %1 offset:3072" : "=&v"(cB[1][6]) : "v"(bb) : "memory"); SB();
  asm volatile("v_mfma_f32_16x16x32_bf16 %0, v[172:175], %1, %0" : "+v"(ac[4]) : "v"(cB[0][1]) : ); SB();
  asm volatile("ds_read_b128 %0, %1 offset:3584" : "=&v"(cB[1][7]) : "v"(bb) : "memory"); SB();
  asm volatile("v_mfma_f32_16x16x32_bf16 %0, v[184:187], %1, %0" : "+v"(ac[7]) : "v"(cB[0][1]) : ); SB();
  asm volatile("v_mfma_f32_16x16x32_bf16 %0, v[168:171], %1, %0" : "+v"(ac[3]) : "v"(cB[0][1]) : ); SB();
  asm volatile("v_mfma_f32_16x16x32_bf16 %0, v[164:167], %1, %0" : "+v"(ac[2]) : "v"(cB[0][1]) : ); SB();
  asm volatile("v_mfma_f32_16x16x32_bf16 %0, v[160:163], %1, %0" : "+v"(ac[1]) : "v"(cB[0][1]) : ); SB();
  asm volatile("v_mfma_f32_16x16x32_bf16 %0, v[188:191], %1, %0" : "+v"(ac[7]) : "v"(cB[0][0]) : ); SB();
  asm volatile("v_mfma_f32_16x16x32_bf16 %0, v[184:187], %1, %0" : "+v"(ac[6]) : "v"(cB[0][0]) : ); SB();
  asm volatile("s_waitcnt lgkmcnt(2)" :  :  : "memory"); SB();
  asm volatile("s_add_u32 m0, %0, 105856" :  : "s"(mA) : "memory", "scc"); SB();
  asm volatile("v_mfma_f32_16x16x32_bf16 %0, v[180:183], %1, %0" : "+v"(ac[5]) : "v"(cB[0][0]) : ); SB();
  asm volatile("buffer_load_dwordx4 %0, %1, %2 offen offset:640 lds" :  : "v"(voS), "s"(rA), "s"((0 ^ (2 * w)) * 32768) : "memory"); SB();
  asm volatile("s_add_u32 m0, %0, 106880" :  : "s"(mA) : "memory", "scc"); SB();
  asm volatile("v_mfma_f32_16x16x32_bf16 %0, v[176:179], %1, %0" : "+v"(ac[4]) : "v"(cB[0][0]) : ); SB();
  asm volatile("buffer_load_dwordx4 %0, %1, %2 offen offset:640 lds" :  : "v"(voS), "s"(rA), "s"((1 ^ (2 * w)) * 32768) : "memory"); SB();
  asm volatile("s_waitcnt vmcnt(14)" :  :  : "memory"); SB();
  asm volatile("v_mfma_f32_16x16x32_bf16 %0, v[172:175], %1, %0" : "+v"(ac[3]) : "v"(cB[0][0]) : ); SB();
  asm volatile("ds_read_b128 a[4:7], %0 offset:10240" :  : "v"(aA[0]) : "memory"); SB();
  asm volatile("ds_read_b128 a[20:23], %0 offset:10240" :  : "v"(aA[1]) : "memory"); SB();
  asm volatile("v_mfma_f32_16x16x32_bf16 %0, v[168:171], %1, %0" : "+v"(ac[2]) : "v"(cB[0][0]) : ); SB();
  asm volatile("v_mfma_f32_16x16x32_bf16 %0, v[164:167], %1, %0" : "+v"(ac[1]) : "v"(cB[0][0]) : ); SB();
  asm volatile("v_mfma_f32_16x16x32_bf16 %0, v[160:163], %1, %0" : "+v"(ac[0]) : "v"(cB[0][0]) : ); SB();
  asm volatile("s_waitcnt lgkmcnt(2)" : "+v"(cB[1][0]), "+v"(cB[1][1]), "+v"(cB[1][2]), "+v"(cB[1][3]), "+v"(cB[1][4]), "+v"(cB[1][5]), "+v"(cB[1][6]), "+v"(cB[1][7]) :  : "memory"); SB();
  { ++cu; const int dl = nch == 2 ? 0 : (cu & 7) ? -64 : 13376;
#pragma unroll
    for (int k = 0; k < 4; ++k) aa[k] += dl;
    bb += dl; SB(); }
  asm volatile("v_mfma_f32_16x16x32_bf16 %0, v[192:195], %1, %0" : "+v"(ac[7]) : "v"(cB[1][7]) : ); SB();
  asm volatile("ds_read2_b32 v[160:161], %0 offset0:128 offset1:129" :  : "v"(aa[0]) : "memory"); SB();
  asm volatile("v_mfma_f32_16x16x32_bf16 %0, v[192:195], %1, %0" : "+v"(ac[6]) : "v"(cB[1][6]) : ); SB();
  asm volatile("ds_read2_b32 v[162:163], %0 offset0:130 offset1:131" :  : "v"(aa[0]) : "memory"); SB();
  asm volatile("s_waitcnt lgkmcnt(2)" :  :  : "memory"); SB();
  asm volatile("v_mfma_f32_16x16x32_bf16 %0, v[192:195], %1, %0" : "+v"(ac[5]) : "v"(cB[1][5]) : ); SB();
  asm volatile("ds_read2_b32 v[164:165], %0 offset0:0 offset1:1" :  : "v"(aa[0]) : "memory"); SB();
  asm volatile("s_add_u32 m0, %0, 107904" :  : "s"(mA) : "memory", "scc"); SB();
  asm volatile("v_mfma_f32_16x16x32_bf16 %0, v[192:195], %1, %0" : "+v"(ac[4]) : "v"(cB[1][4]) : ); SB();
  asm volatile("ds_read2_b32 v[166:167], %0 offset0:2 offset1:3" :  : "v"(aa[0]) : "memory"); SB();
  asm volatile("buffer_load_dwordx4 %0, %1, %2 offen offset:640 lds" :  : "v"(voS), "s"(rA), "s"((2 ^ (2 * w)) * 32768) : "memory"); SB();
  asm volatile("v_mfma_f32_16x16x32_bf16 %0, v[196:199], %1, %0" : "+v"(ac[7]) : "v"(cB[1][6]) : ); SB();
  asm volatile("ds_read2_b32 v[168:169], %0 offset0:128 offset1:129" :  : "v"(aa[1]) : "memory"); SB();
  asm volatile("s_add_u32 m0, %0, 108928" :  : "s"(mA) : "memory", "scc"); SB();
  asm volatile("v_mfma_f32_16x16x32_bf16 %0, v[196:199], %1, %0" : "+v"(ac[6]) : "v"(cB[1][5]) : ); SB();
  asm volatile("ds_read2_b32 v[170:171], %0 offset0:130 offset1:131" :  : "v"(aa[1]) : "memory"); SB();
  asm volatile("buffer_load_dwordx4 %0, %1, %2 offen offset:640 lds" :  : "v"(voS), "s"(rA), "s"((3 ^ (2 * w)) * 32768) : "memory"); SB();
  asm volatile("v_mfma_f32_16x16x32_bf16 %0, v[196:199], %1, %0" : "+v"(ac[5]) : "v"(cB[1][4]) : ); SB();
  asm volatile("ds_read2_b32 v[172:173], %0 offset0:0 offset1:1" :  : "v"(aa[1]) : "memory"); SB();
  asm volatile("s_waitcnt vmcnt(14)" :  :  : "memory"); SB();
  asm volatile("v_mfma_f32_16x16x32_bf16 %0, v[196:199], %1, %0" : "+v"(ac[4]) : "v"(cB[1][3]) : ); SB();
  asm volatile("ds_read2_b32 v[174:175], %0 offset0:2 offset1:3" :  : "v"(aa[1]) : "memory"); SB();
  asm volatile("ds_read_b128 a[8:11], %0 offset:12288" :  : "v"(aA[0]) : "memory"); SB();
  asm volatile("v_mfma_f32_16x16x32_bf16 %0, v[200:203], %1, %0" : "+v"(ac[7]) : "v"(cB[1][5]) : ); SB();
  asm volatile("ds_read2_b32 v[176:177], %0 offset0:128 offset1:129" :  : "v"(aa[2]) : "memory"); SB();
  asm volatile("ds_read_b128 a[24:27], %0 offset:12288" :  : "v"(aA[1]) : "memory"); SB();
  asm volatile("v_mfma_f32_16x16x32_bf16 %0, v[200:203], %1, %0" : "+v"(ac[6]) : "v"(cB[1][4]) : ); SB();
  asm volatile("ds_read2_b32 v[178:179], %0 offset0:130 offset1:131" :  : "v"(aa[2]) : "memory"); SB();
  asm volatile("v_mfma_f32_16x16x32_bf16 %0, v[200:203], %1, %0" : "+v"(ac[5]) : "v"(cB[1][3]) : ); SB();
  asm volatile("ds_read2_b32 v[180:181], %0 offset0:0 offset1:1" :  : "v"(aa[2]) : "memory"); SB();
  asm volatile("v_mfma_f32_16x16x32_bf16 %0, v[192:195], %1, %0" : "+v"(ac[3]) : "v"(cB[1][3]) : ); SB();
  asm volatile("ds_read2_b32 v[182:183], %0 offset0:2 offset1:3" :  : "v"(aa[2]) : "memory"); SB();
  asm volatile("v_mfma_f32_16x16x32_bf16 %0, v[204:207], %1, %0" : "+v"(ac[7]) : "v"(cB[1][4]) : ); SB();
  asm volatile("ds_read2_b32 v[184:185], %0 offset0:128 offset1:129" :  : "v"(aa[3]) : "memory"); SB();
  asm volatile("v_mfma_f32_16x16x32_bf16 %0, v[204:207], %1, %0" : "+v"(ac[6]) : "v"(cB[1][3]) : ); SB();
  asm volatile("ds_read2_b32 v[186:187], %0 offset0:130 offset1:131" :  : "v"(aa[3]) : "memory"); SB();
  asm volatile("v_mfma_f32_16x16x32_bf16 %0, v[204:207], %1, %0" : "+v"(ac[5]) : "v"(cB[1][2]) : ); SB();
  asm volatile("ds_read2_b32 v[188:189], %0 offset0:0 offset1:1" :  : "v"(aa[3]) : "memory"); SB();
  asm volatile("v_mfma_f32_16x16x32_bf16 %0, v[200:203], %1, %0" : "+v"(ac[4]) : "v"(cB[1][2]) : ); SB();
  asm volatile("ds_read2_b32 v[190:191], %0 offset0:2 offset1:3" :  : "v"(aa[3]) : "memory"); SB();
  asm volatile("s_waitcnt lgkmcnt(7)" :  :  : "memory"); SB();
  asm volatile("v_mfma_f32_16x16x32_bf16 %0, v[208:211], %1, %0" : "+v"(ac[7]) : "v"(cB[1][3]) : ); SB();
  asm volatile("ds_read_b128 %0, %1 offset:0" : "=&v"(cB[0][0]) : "v"(bb) : "memory"); SB();
  asm volatile("s_add_u32 m0, %0, 109952" :  : "s"(mA) : "memory", "scc"); SB();
  asm volatile("v_mfma_f32_16x16x32_bf16 %0, v[208:211], %1, %0" : "+v"(ac[6]) : "v"(cB[1][2]) : ); SB();
  asm volatile("ds_read_b128 %0, %1 offset:512" : "=&v"(cB[0][1]) : "v"(bb) : "memory"); SB();
  asm volatile("buffer_load_dwordx4 %0, %1, %2 offen offset:640 lds" :  : "v"(voS), "s"(rA), "s"((4 ^ (2 * w)) * 32768) : "memory"); SB();
  asm volatile("v_mfma_f32_16x16x32_bf16 %0, v[196:199], %1, %0" : "+v"(ac[3]) : "v"(cB[1][2]) : ); SB();
  asm volatile("ds_read_b128 %0, %1 offset:1024" : "=&v"(cB[0][2]) : "v"(bb) : "memory"); SB();
  asm volatile("s_add_u32 m0, %0, 110976" :  : "s"(mA) : "memory", "scc"); SB();
  asm volatile("v_mfma_f32_16x16x32_bf16 %0, v[192:195], %1, %0" : "+v"(ac[2]) : "v"(cB[1][2]) : ); SB();
  asm volatile("ds_read_b128 %0, %1 offset:1536" : "=&v"(cB[0][3]) : "v"(bb) : "memory"); SB();
  asm volatile("buffer_load_dwordx4 %0, %1, %2 offen offset:640 lds" :  : "v"(voS), "s"(rA), "s"((5 ^ (2 * w)) * 32768) : "memory"); SB();
  asm volatile("v_mfma_f32_16x16x32_bf16 %0, v[212:215], %1, %0" : "+v"(ac[7]) : "v"(cB[1][2]) : ); SB();
  asm volatile("ds_read_b128 %0, %1 offset:2048" : "=&v"(cB[0][4]) : "v"(bb) : "memory"); SB();
  asm volatile("s_waitcnt vmcnt(14)" :  :  : "memory"); SB();
  asm volatile("v_mfma_f32_16x16x32_bf16 %0, v[212:215], %1, %0" : "+v"(ac[6]) : "v"(cB[1][1]) : ); SB();
  asm volatile("ds_read_b128 %0, %1 offset:2560" : "=&v"(cB[0][5]) : "v"(bb) : "memory"); SB();
  asm volatile("ds_read_b128 a[12:15], %0 offset:14336" :  : "v"(aA[0]) : "memory"); SB();
  asm volatile("v_mfma_f32_16x16x32_bf16 %0, v[208:211], %1, %0" : "+v"(ac[5]) : "v"(cB[1][1]) : ); SB();
  asm volatile("ds_read_b128 %0, %1 offset:3072" : "=&v"(cB[0][6]) : "v"(bb) : "memory"); SB();
  asm volatile("ds_read_b128 a[28:31], %0 offset:14336" :  : "v"(aA[1]) : "memory"); SB();
  asm volatile("v_mfma_f32_16x16x32_bf16 %0, v[204:207], %1, %0" : "+v"(ac[4]) : "v"(cB[1][1]) : ); SB();
  asm volatile("ds_read_b128 %0, %1 offset:3584" : "=&v"(cB[0][7]) : "v"(bb) : "memory"); SB();
  asm volatile("v_mfma_f32_16x16x32_bf16 %0, v[216:219], %1, %0" : "+v"(ac[7]) : "v"(cB[1][1]) : ); SB();
  asm volatile("v_mfma_f32_16x16x32_bf16 %0, v[200:203], %1, %0" : "+v"(ac[3]) : "v"(cB[1][1]) : ); SB();
  asm volatile("v_mfma_f32_16x16x32_bf16 %0, v[196:199], %1, %0" : "+v"(ac[2]) : "v"(cB[1][1]) : ); SB();
  asm volatile("v_mfma_f32_16x16x32_bf16 %0, v[192:195], %1, %0" : "+v"(ac[1]) : "v"(cB[1][1]) : ); SB();
  asm volatile("v_mfma_f32_16x16x32_bf16 %0, v[220:223], %1, %0" : "+v"(ac[7]) : "v"(cB[1][0]) : ); SB();
  asm volatile("v_mfma_f32_16x16x32_bf16 %0, v[216:219], %1, %0" : "+v"(ac[6]) : "v"(cB[1][0]) : ); SB();
  asm volatile("v_mfma_f32_16x16x32_bf16 %0, v[212:215], %1, %0" : "+v"(ac[5]) : "v"(cB[1][0]) : ); SB();
  asm volatile("s_waitcnt lgkmcnt(1)" :  :  : "memory"); SB();
  asm volatile("s_add_u32 m0, %0, 112000" :  : "s"(mA) : "memory", "scc"); SB();
  asm volatile("v_mfma_f32_16x16x32_bf16 %0, v[208:211], %1, %0" : "+v"(ac[4]) : "v"(cB[1][0]) : ); SB();
  asm volatile("buffer_load_dwordx4 %0, %1, %2 offen offset:640 lds" :  : "v"(voS), "s"(rA), "s"((6 ^ (2 * w)) * 32768) : "memory"); SB();
  asm volatile("s_add_u32 m0, %0, 113024" :  : "s"(mA) : "memory", "scc"); SB();
  asm volatile("v_mfma_f32_16x16x32_bf16 %0, v[204:207], %1, %0" : "+v"(ac[3]) : "v"(cB[1][0]) : ); SB();
  asm volatile("buffer_load_dwordx4 %0, %1, %2 offen offset:640 lds" :  : "v"(voS), "s"(rA), "s"((7 ^ (2 * w)) * 32768) : "memory"); SB();
  asm volatile("s_waitcnt vmcnt(14)" :  :  : "memory"); SB();
  asm volatile("v_mfma_f32_16x16x32_bf16 %0, v[200:203], %1, %0" : "+v"(ac[2]) : "v"(cB[1][0]) : ); SB();
  asm volatile("ds_read_b128 a[32:35], %0 offset:0" :  : "v"(aA[0]) : "memory"); SB();
  asm volatile("ds_read_b128 a[48:51], %0 offset:0" :  : "v"(aA[1]) : "memory"); SB();
  asm volatile("v_mfma_f32_16x16x32_bf16 %0, v[196:199], %1, %0" : "+v"(ac[1]) : "v"(cB[1][0]) : ); SB();
  asm volatile("v_mfma_f32_16x16x32_bf16 %0, v[192:195], %1, %0" : "+v"(ac[0]) : "v"(cB[1][0]) : ); SB();
  asm volatile("s_waitcnt lgkmcnt(2)" : "+v"(cB[0][0]), "+v"(cB[0][1]), "+v"(cB[0][2]), "+v"(cB[0][3]), "+v"(cB[0][4]), "+v"(cB[0][5]), "+v"(cB[0][6]), "+v"(cB[0][7]) :  : "memory"); SB();
  if ((cu & 7) == 0 && nch == 3) {
  asm volatile("s_nop 7" : "+v"(ac[0]), "+v"(ac[1]), "+v"(ac[2]), "+v"(ac[3]), "+v"(ac[4]), "+v"(ac[5]), "+v"(ac[6]), "+v"(ac[7]) :  : ); SB();
    const unsigned dd = da + (((cu - 1) >> 3) * 2 + 1) * 8192;
  asm volatile("ds_write_b128 %0, %1 offset:0" :  : "v"(dd), "v"(ac[0]) : "memory"); SB();
  asm volatile("ds_write_b128 %0, %1 offset:1024" :  : "v"(dd), "v"(ac[1]) : "memory"); SB();
  asm volatile("ds_write_b128 %0, %1 offset:2048" :  : "v"(dd), "v"(ac[2]) : "memory"); SB();
  asm volatile("ds_write_b128 %0, %1 offset:3072" :  : "v"(dd), "v"(ac[3]) : "memory"); SB();
  asm volatile("ds_write_b128 %0, %1 offset:4096" :  : "v"(dd), "v"(ac[4]) : "memory"); SB();
  asm volatile("ds_write_b128 %0, %1 offset:5120" :  : "v"(dd), "v"(ac[5]) : "memory"); SB();
  asm volatile("ds_write_b128 %0, %1 offset:6144" :  : "v"(dd), "v"(ac[6]) : "memory"); SB();
  asm volatile("ds_write_b128 %0, %1 offset:7168" :  : "v"(dd), "v"(ac[7]) : "memory"); SB();
#pragma unroll
  for (int t = 0; t < 8; ++t) ac[t] = (f32x4){0.f, 0.f, 0.f, 0.f};
  asm volatile("s_nop 1" : "+v"(ac[0]), "+v"(ac[1]), "+v"(ac[2]), "+v"(ac[3]), "+v"(ac[4]), "+v"(ac[5]), "+v"(ac[6]), "+v"(ac[7]) :  : ); SB();
  }
  asm volatile("s_waitcnt lgkmcnt(0)" :  :  : "memory"); SB();
  if (nch == 3) {
  { ++cu; const int dl = (cu & 7) ? -64 : 13376;
#pragma unroll
    for (int k = 0; k < 4; ++k) aa[k] += dl;
    bb += dl; SB(); }
  asm volatile("v_mfma_f32_16x16x32_bf16 %0, v[160:163], %1, %0" : "+v"(ac[7]) : "v"(cB[0][7]) : ); SB();
  asm volatile("ds_read2_b32 v[192:193], %0 offset0:128 offset1:129" :  : "v"(aa[0]) : "memory"); SB();
  asm volatile("buffer_load_dwordx2 v[224:225], %0, %1, %2 offen offset:0 sc1" :  : "v"(vFZ), "s"(rWS), "s"(0u) : "memory"); SB();
  asm volatile("v_mfma_f32_16x16x32_bf16 %0, v[160:163], %1, %0" : "+v"(ac[6]) : "v"(cB[0][6]) : ); SB();
  asm volatile("ds_read2_b32 v[194:195], %0 offset0:130 offset1:131" :  : "v"(aa[0]) : "memory"); SB();
  asm volatile("v_mfma_f32_16x16x32_bf16 %0, v[160:163], %1, %0" : "+v"(ac[5]) : "v"(cB[0][5]) : ); SB();
  asm volatile("ds_read2_b32 v[196:197], %0 offset0:0 offset1:1" :  : "v"(aa[0]) : "memory"); SB();
  asm volatile("s_add_u32 m0, %0, 97536" :  : "s"(mA) : "memory", "scc"); SB();
  asm volatile("v_mfma_f32_16x16x32_bf16 %0, v[160:163], %1, %0" : "+v"(ac[4]) : "v"(cB[0][4]) : ); SB();
  asm volatile("ds_read2_b32 v[198:199], %0 offset0:2 offset1:3" :  : "v"(aa[0]) : "memory"); SB();
  asm volatile("buffer_load_dwordx4 %0, %1, %2 offen offset:768 lds" :  : "v"(voS), "s"(rA), "s"((0 ^ (2 * w)) * 32768) : "memory"); SB();
  asm volatile("v_mfma_f32_16x16x32_bf16 %0, v[164:167], %1, %0" : "+v"(ac[7]) : "v"(cB[0][6]) : ); SB();
  asm volatile("ds_read2_b32 v[200:201], %0 offset0:128 offset1:129" :  : "v"(aa[1]) : "memory"); SB();
  asm volatile("v_mfma_f32_16x16x32_bf16 %0, v[164:167], %1, %0" : "+v"(ac[6]) : "v"(cB[0][5]) : ); SB();
  asm volatile("ds_read2_b32 v[202:203], %0 offset0:130 offset1:131" :  : "v"(aa[1]) : "memory"); SB();
  asm volatile("v_mfma_f32_16x16x32_bf16 %0, v[164:167], %1, %0" : "+v"(ac[5]) : "v"(cB[0][4]) : ); SB();
  asm volatile("ds_read2_b32 v[204:205], %0 offset0:0 offset1:1" :  : "v"(aa[1]) : "memory"); SB();
  asm volatile("v_mfma_f32_16x16x32_bf16 %0, v[164:167], %1, %0" : "+v"(ac[4]) : "v"(cB[0][3]) : ); SB();
  asm volatile("ds_read2_b32 v[206:207], %0 offset0:2 offset1:3" :  : "v"(aa[1]) : "memory"); SB();
  asm volatile("v_mfma_f32_16x16x32_bf16 %0, v[168:171], %1, %0" : "+v"(ac[7]) : "v"(cB[0][5]) : ); SB();
  asm volatile("ds_read2_b32 v[208:209], %0 offset0:128 offset1:129" :  : "v"(aa[2]) : "memory"); SB();
  asm volatile("v_mfma_f32_16x16x32_bf16 %0, v[168:171], %1, %0" : "+v"(ac[6]) : "v"(cB[0][4]) : ); SB();
  asm volatile("ds_read2_b32 v[210:211], %0 offset0:130 offset1:131" :  : "v"(aa[2]) : "memory"); SB();
  asm volatile("v_mfma_f32_16x16x32_bf16 %0, v[168:171], %1, %0" : "+v"(ac[5]) : "v"(cB[0][3]) : ); SB();
  asm volatile("ds_read2_b32 v[212:213], %0 offset0:0 offset1:1" :  : "v"(aa[2]) : "memory"); SB();
  asm volatile("v_mfma_f32_16x16x32_bf16 %0, v[160:163], %1, %0" : "+v"(ac[3]) : "v"(cB[0][3]) : ); SB();
  asm volatile("ds_read2_b32 v[214:215], %0 offset0:2 offset1:3" :  : "v"(aa[2]) : "memory"); SB();
  asm volatile("v_mfma_f32_16x16x32_bf16 %0, v[172:175], %1, %0" : "+v"(ac[7]) : "v"(cB[0][4]) : ); SB();
  asm volatile("ds_read2_b32 v[216:217], %0 offset0:128 offset1:129" :  : "v"(aa[3]) : "memory"); SB();
  asm volatile("s_add_u32 m0, %0, 98560" :  : "s"(mA) : "memory", "scc"); SB();
  asm volatile("v_mfma_f32_16x16x32_bf16 %0, v[172:175], %1, %0" : "+v"(ac[6]) : "v"(cB[0][3]) : ); SB();
  asm volatile("ds_read2_b32 v[218:219], %0 offset0:130 offset1:131" :  : "v"(aa[3]) : "memory"); SB();
  asm volatile("buffer_load_dwordx4 %0, %1, %2 offen offset:768 lds" :  : "v"(voS), "s"(rA), "s"((1 ^ (2 * w)) * 32768) : "memory"); SB();
  asm volatile("v_mfma_f32_16x16x32_bf16 %0, v[172:175], %1, %0" : "+v"(ac[5]) : "v"(cB[0][2]) : ); SB();
  asm volatile("ds_read2_b32 v[220:221], %0 offset0:0 offset1:1" :  : "v"(aa[3]) : "memory"); SB();
  asm volatile("s_waitcnt vmcnt(15)" :  :  : "memory"); SB();
  asm volatile("v_mfma_f32_16x16x32_bf16 %0, v[168:171], %1, %0" : "+v"(ac[4]) : "v"(cB[0][2]) : ); SB();
  asm volatile("ds_read2_b32 v[222:223], %0 offset0:2 offset1:3" :  : "v"(aa[3]) : "memory"); SB();
  asm volatile("ds_read_b128 a[36:39], %0 offset:2048" :  : "v"(aA[0]) : "memory"); SB();
  asm volatile("v_mfma_f32_16x16x32_bf16 %0, v[176:179], %1, %0" : "+v"(ac[7]) : "v"(cB[0][3]) : ); SB();
  asm volatile("ds_read_b128 %0, %1 offset:0" : "=&v"(cB[1][0]) : "v"(bb) : "memory"); SB();
  asm volatile("ds_read_b128 a[52:55], %0 offset:2048" :  : "v"(aA[1]) : "memory"); SB();
  asm volatile("v_mfma_f32_16x16x32_bf16 %0, v[176:179], %1, %0" : "+v"(ac[6]) : "v"(cB[0][2]) : ); SB();
  asm volatile("ds_read_b128 %0, %1 offset:512" : "=&v"(cB[1][1]) : "v"(bb) : "memory"); SB();
  asm volatile("v_mfma_f32_16x16x32_bf16 %0, v[164:167], %1, %0" : "+v"(ac[3]) : "v"(cB[0][2]) : ); SB();
  asm volatile("ds_read_b128 %0, %1 offset:1024" : "=&v"(cB[1][2]) : "v"(bb) : "memory"); SB();
  asm volatile("v_mfma_f32_16x16x32_bf16 %0, v[160:163], %1, %0" : "+v"(ac[2]) : "v"(cB[0][2]) : ); SB();
  asm volatile("ds_read_b128 %0, %1 offset:1536" : "=&v"(cB[1][3]) : "v"(bb) : "memory"); SB();
  asm volatile("v_mfma_f32_16x16x32_bf16 %0, v[180:183], %1, %0" : "+v"(ac[7]) : "v"(cB[0][2]) : ); SB();
  asm volatile("ds_read_b128 %0, %1 offset:2048" : "=&v"(cB[1][4]) : "v"(bb) : "memory"); SB();
  asm volatile("v_mfma_f32_16x16x32_bf16 %0, v[180:183], %1, %0" : "+v"(ac[6]) : "v"(cB[0][1]) : ); SB();
  asm volatile("ds_read_b128 %0, %1 offset:2560" : "=&v"(cB[1][5]) : "v"(bb) : "memory"); SB();
  asm volatile("v_mfma_f32_16x16x32_bf16 %0, v[176:179], %1, %0" : "+v"(ac[5]) : "v"(cB[0][1]) : ); SB();
  asm volatile("ds_read_b128 %0, %1 offset:3072" : "=&v"(cB[1][6]) : "v"(bb) : "memory"); SB();
  asm volatile("v_mfma_f32_16x16x32_bf16 %0, v[172:175], %1, %0" : "+v"(ac[4]) : "v"(cB[0][1]) : ); SB();
  asm volatile("ds_read_b128 %0, %1 offset:3584" : "=&v"(cB[1][7]) : "v"(bb) : "memory"); SB();
  asm volatile("s_waitcnt lgkmcnt(7)" :  :  : "memory"); SB();
  asm volatile("v_mfma_f32_16x16x32_bf16 %0, v[184:187], %1, %0" : "+v"(ac[7]) : "v"(cB[0][1]) : ); SB();
  asm volatile("s_add_u32 m0, %0, 99584" :  : "s"(mA) : "memory", "scc"); SB();
  asm volatile("s_nop 0\n\tbuffer_load_dwordx4 %0, %1, %2 offen offset:768 lds" :  : "v"(voS), "s"(rA), "s"((2 ^ (2 * w)) * 32768) : "memory"); SB();
  asm volatile("v_mfma_f32_16x16x32_bf16 %0, v[168:171], %1, %0" : "+v"(ac[3]) : "v"(cB[0][1]) : ); SB();
  asm volatile("v_mfma_f32_16x16x32_bf16 %0, v[164:167], %1, %0" : "+v"(ac[2]) : "v"(cB[0][1]) : ); SB();
  asm volatile("s_add_u32 m0, %0, 100608" :  : "s"(mA) : "memory", "scc"); SB();
  asm volatile("s_nop 0\n\tbuffer_load_dwordx4 %0, %1, %2 offen offset:768 lds" :  : "v"(voS), "s"(rA), "s"((3 ^ (2 * w)) * 32768) : "memory"); SB();
  asm volatile("v_mfma_f32_16x16x32_bf16 %0, v[160:163], %1, %0" : "+v"(ac[1]) : "v"(cB[0][1]) : ); SB();
  asm volatile("s_waitcnt vmcnt(15)" :  :  : "memory"); SB();
  asm volatile("ds_read_b128 a[40:43], %0 offset:4096" :  : "v"(aA[0]) : "memory"); SB();
  asm volatile("v_mfma_f32_16x16x32_bf16 %0, v[188:191], %1, %0" : "+v"(ac[7]) : "v"(cB[0][0]) : ); SB();
  asm volatile("ds_read_b128 a[56:59], %0 offset:4096" :  : "v"(aA[1]) : "memory"); SB();
  asm volatile("v_mfma_f32_16x16x32_bf16 %0, v[184:187], %1, %0" : "+v"(ac[6]) : "v"(cB[0][0]) : ); SB();
  asm volatile("v_mfma_f32_16x16x32_bf16 %0, v[180:183], %1, %0" : "+v"(ac[5]) : "v"(cB[0][0]) : ); SB();
  asm volatile("v_mfma_f32_16x16x32_bf16 %0, v[176:179], %1, %0" : "+v"(ac[4]) : "v"(cB[0][0]) : ); SB();
  asm volatile("v_mfma_f32_16x16x32_bf16 %0, v[172:175], %1, %0" : "+v"(ac[3]) : "v"(cB[0][0]) : ); SB();
  asm volatile("v_mfma_f32_16x16x32_bf16 %0, v[168:171], %1, %0" : "+v"(ac[2]) : "v"(cB[0][0]) : ); SB();
  asm volatile("v_mfma_f32_16x16x32_bf16 %0, v[164:167], %1, %0" : "+v"(ac[1]) : "v"(cB[0][0]) : ); SB();
  asm volatile("v_mfma_f32_16x16x32_bf16 %0, v[160:163], %1, %0" : "+v"(ac[0]) : "v"(cB[0][0]) : ); SB();
  asm volatile("s_waitcnt lgkmcnt(2)" : "+v"(cB[1][0]), "+v"(cB[1][1]), "+v"(cB[1][2]), "+v"(cB[1][3]), "+v"(cB[1][4]), "+v"(cB[1][5]), "+v"(cB[1][6]), "+v"(cB[1][7]) :  : "memory"); SB();
  ++cu;
  asm volatile("v_mfma_f32_16x16x32_bf16 %0, v[192:195], %1, %0" : "+v"(ac[7]) : "v"(cB[1][7]) : ); SB();
  asm volatile("s_waitcnt lgkmcnt(0)" :  :  : "memory"); SB();
  asm volatile("s_add_u32 m0, %0, 101632" :  : "s"(mA) : "memory", "scc"); SB();
  asm volatile("v_mfma_f32_16x16x32_bf16 %0, v[192:195], %1, %0" : "+v"(ac[6]) : "v"(cB[1][6]) : ); SB();
  asm volatile("buffer_load_dwordx4 %0, %1, %2 offen offset:768 lds" :  : "v"(voS), "s"(rA), "s"((4 ^ (2 * w)) * 32768) : "memory"); SB();
  asm volatile("s_add_u32 m0, %0, 102656" :  : "s"(mA) : "memory", "scc"); SB();
  asm volatile("v_mfma_f32_16x16x32_bf16 %0, v[192:195], %1, %0" : "+v"(ac[5]) : "v"(cB[1][5]) : ); SB();
  asm volatile("buffer_load_dwordx4 %0, %1, %2 offen offset:768 lds" :  : "v"(voS), "s"(rA), "s"((5 ^ (2 * w)) * 32768) : "memory"); SB();
  asm volatile("s_waitcnt vmcnt(15)" :  :  : "memory"); SB();
  asm volatile("v_mfma_f32_16x16x32_bf16 %0, v[192:195], %1, %0" : "+v"(ac[4]) : "v"(cB[1][4]) : ); SB();
  asm volatile("ds_read_b128 a[44:47], %0 offset:6144" :  : "v"(aA[0]) : "memory"); SB();
  asm volatile("ds_read_b128 a[60:63], %0 offset:6144" :  : "v"(aA[1]) : "memory"); SB();
  asm volatile("v_mfma_f32_16x16x32_bf16 %0, v[196:199], %1, %0" : "+v"(ac[7]) : "v"(cB[1][6]) : ); SB();
  asm volatile("v_mfma_f32_16x16x32_bf16 %0, v[196:199], %1, %0" : "+v"(ac[6]) : "v"(cB[1][5]) : ); SB();
  asm volatile("v_mfma_f32_16x16x32_bf16 %0, v[196:199], %1, %0" : "+v"(ac[5]) : "v"(cB[1][4]) : ); SB();
  asm volatile("v_mfma_f32_16x16x32_bf16 %0, v[196:199], %1, %0" : "+v"(ac[4]) : "v"(cB[1][3]) : ); SB();
  asm volatile("v_mfma_f32_16x16x32_bf16 %0, v[200:203], %1, %0" : "+v"(ac[7]) : "v"(cB[1][5]) : ); SB();
  asm volatile("v_mfma_f32_16x16x32_bf16 %0, v[200:203], %1, %0" : "+v"(ac[6]) : "v"(cB[1][4]) : ); SB();
  asm volatile("v_mfma_f32_16x16x32_bf16 %0, v[200:203], %1, %0" : "+v"(ac[5]) : "v"(cB[1][3]) : ); SB();
  asm volatile("v_mfma_f32_16x16x32_bf16 %0, v[192:195], %1, %0" : "+v"(ac[3]) : "v"(cB[1][3]) : ); SB();
  asm volatile("s_waitcnt lgkmcnt(0)" :  :  : "memory"); SB();
  asm volatile("s_add_u32 m0, %0, 103680" :  : "s"(mA) : "memory", "scc"); SB();
  asm volatile("v_mfma_f32_16x16x32_bf16 %0, v[204:207], %1, %0" : "+v"(ac[7]) : "v"(cB[1][4]) : ); SB();
  asm volatile("buffer_load_dwordx4 %0, %1, %2 offen offset:768 lds" :  : "v"(voS), "s"(rA), "s"((6 ^ (2 * w)) * 32768) : "memory"); SB();
  asm volatile("v_mfma_f32_16x16x32_bf16 %0, v[204:207], %1, %0" : "+v"(ac[6]) : "v"(cB[1][3]) : ); SB();
  asm volatile("v_mfma_f32_16x16x32_bf16 %0, v[204:207], %1, %0" : "+v"(ac[5]) : "v"(cB[1][2]) : ); SB();
  asm volatile("v_mfma_f32_16x16x32_bf16 %0, v[200:203], %1, %0" : "+v"(ac[4]) : "v"(cB[1][2]) : ); SB();
  asm volatile("v_mfma_f32_16x16x32_bf16 %0, v[208:211], %1, %0" : "+v"(ac[7]) : "v"(cB[1][3]) : ); SB();
  asm volatile("s_add_u32 m0, %0, 104704" :  : "s"(mA) : "memory", "scc"); SB();
  asm volatile("s_nop 0\n\tbuffer_load_dwordx4 %0, %1, %2 offen offset:768 lds" :  : "v"(voS), "s"(rA), "s"((7 ^ (2 * w)) * 32768) : "memory"); SB();
  asm volatile("v_mfma_f32_16x16x32_bf16 %0, v[208:211], %1, %0" : "+v"(ac[6]) : "v"(cB[1][2]) : ); SB();
  asm volatile("s_waitcnt vmcnt(15)" :  :  : "memory"); SB();
  asm volatile("ds_read_b128 a[64:67], %0 offset:8192" :  : "v"(aA[0]) : "memory"); SB();
  asm volatile("v_mfma_f32_16x16x32_bf16 %0, v[196:199], %1, %0" : "+v"(ac[3]) : "v"(cB[1][2]) : ); SB();
  asm volatile("ds_read_b128 a[80:83], %0 offset:8192" :  : "v"(aA[1]) : "memory"); SB();
  asm volatile("v_mfma_f32_16x16x32_bf16 %0, v[192:195], %1, %0" : "+v"(ac[2]) : "v"(cB[1][2]) : ); SB();
  asm volatile("v_mfma_f32_16x16x32_bf16 %0, v[212:215], %1, %0" : "+v"(ac[7]) : "v"(cB[1][2]) : ); SB();
  asm volatile("v_mfma_f32_16x16x32_bf16 %0, v[212:215], %1, %0" : "+v"(ac[6]) : "v"(cB[1][1]) : ); SB();
  asm volatile("v_mfma_f32_16x16x32_bf16 %0, v[208:211], %1, %0" : "+v"(ac[5]) : "v"(cB[1][1]) : ); SB();
  asm volatile("v_mfma_f32_16x16x32_bf16 %0, v[204:207], %1, %0" : "+v"(ac[4]) : "v"(cB[1][1]) : ); SB();
  asm volatile("v_mfma_f32_16x16x32_bf16 %0, v[216:219], %1, %0" : "+v"(ac[7]) : "v"(cB[1][1]) : ); SB();
  asm volatile("v_mfma_f32_16x16x32_bf16 %0, v[200:203], %1, %0" : "+v"(ac[3]) : "v"(cB[1][1]) : ); SB();
  asm volatile("v_mfma_f32_16x16x32_bf16 %0, v[196:199], %1, %0" : "+v"(ac[2]) : "v"(cB[1][1]) : ); SB();
  asm volatile("s_waitcnt lgkmcnt(0)" :  :  : "memory"); SB();
  asm volatile("s_add_u32 m0, %0, 105600" :  : "s"(mA) : "memory", "scc"); SB();
  asm volatile("v_mfma_f32_16x16x32_bf16 %0, v[192:195], %1, %0" : "+v"(ac[1]) : "v"(cB[1][1]) : ); SB();
  asm volatile("buffer_load_dwordx4 %0, %1, %2 offen offset:896 lds" :  : "v"(voS), "s"(rA), "s"((0 ^ (2 * w)) * 32768) : "memory"); SB();
  asm volatile("v_mfma_f32_16x16x32_bf16 %0, v[220:223], %1, %0" : "+v"(ac[7]) : "v"(cB[1][0]) : ); SB();
  asm volatile("v_mfma_f32_16x16x32_bf16 %0, v[216:219], %1, %0" : "+v"(ac[6]) : "v"(cB[1][0]) : ); SB();
  asm volatile("v_mfma_f32_16x16x32_bf16 %0, v[212:215], %1, %0" : "+v"(ac[5]) : "v"(cB[1][0]) : ); SB();
  asm volatile("s_add_u32 m0, %0, 106624" :  : "s"(mA) : "memory", "scc"); SB();
  asm volatile("s_nop 0\n\tbuffer_load_dwordx4 %0, %1, %2 offen offset:896 lds" :  : "v"(voS), "s"(rA), "s"((1 ^ (2 * w)) * 32768) : "memory"); SB();
  asm volatile("v_mfma_f32_16x16x32_bf16 %0, v[208:211], %1, %0" : "+v"(ac[4]) : "v"(cB[1][0]) : ); SB();
  asm volatile("s_waitcnt vmcnt(15)" :  :  : "memory"); SB();
  asm volatile("ds_read_b128 a[68:71], %0 offset:10240" :  : "v"(aA[0]) : "memory"); SB();
  asm volatile("v_mfma_f32_16x16x32_bf16 %0, v[204:207], %1, %0" : "+v"(ac[3]) : "v"(cB[1][0]) : ); SB();
  asm volatile("ds_read_b128 a[84:87], %0 offset:10240" :  : "v"(aA[1]) : "memory"); SB();
  asm volatile("v_mfma_f32_16x16x32_bf16 %0, v[200:203], %1, %0" : "+v"(ac[2]) : "v"(cB[1][0]) : ); SB();
  asm volatile("v_mfma_f32_16x16x32_bf16 %0, v[196:199], %1, %0" : "+v"(ac[1]) : "v"(cB[1][0]) : ); SB();
  asm volatile("v_mfma_f32_16x16x32_bf16 %0, v[192:195], %1, %0" : "+v"(ac[0]) : "v"(cB[1][0]) : ); SB();
  asm volatile("s_waitcnt lgkmcnt(0)" :  :  : "memory"); SB();
  asm volatile("s_nop 7" : "+v"(ac[0]), "+v"(ac[1]), "+v"(ac[2]), "+v"(ac[3]), "+v"(ac[4]), "+v"(ac[5]), "+v"(ac[6]), "+v"(ac[7]) :  : ); SB();
  } else {
  asm volatile("s_waitcnt lgkmcnt(0)" ::: "memory"); SB();
  asm volatile("buffer_load_dwordx2 v[224:225], %0, %1, %2 offen offset:0 sc1" :  : "v"(vFZ), "s"(rWS), "s"(0u) : "memory"); SB();
  asm volatile("s_waitcnt lgkmcnt(0)" ::: "memory"); SB();
  asm volatile("s_waitcnt lgkmcnt(0)" ::: "memory"); SB();
  asm volatile("s_add_u32 m0, %0, 97536" :  : "s"(mA) : "memory", "scc"); SB();
  asm volatile("s_waitcnt lgkmcnt(0)" ::: "memory"); SB();
  asm volatile("buffer_load_dwordx4 %0, %1, %2 offen offset:768 lds" :  : "v"(voS), "s"(rA), "s"((0 ^ (2 * w)) * 32768) : "memory"); SB();
  asm volatile("s_waitcnt lgkmcnt(0)" ::: "memory"); SB();
  asm volatile("s_add_u32 m0, %0, 98560" :  : "s"(mA) : "memory", "scc"); SB();
  asm volatile("s_waitcnt lgkmcnt(0)" ::: "memory"); SB();
  asm volatile("buffer_load_dwordx4 %0, %1, %2 offen offset:768 lds" :  : "v"(voS), "s"(rA), "s"((1 ^ (2 * w)) * 32768) : "memory"); SB();
  asm volatile("s_waitcnt lgkmcnt(0)" ::: "memory"); SB();
  asm volatile("s_waitcnt vmcnt(15)" :  :  : "memory"); SB();
  asm volatile("s_waitcnt lgkmcnt(0)" ::: "memory"); SB();
  asm volatile("ds_read_b128 a[36:39], %0 offset:2048" :  : "v"(aA[0]) : "memory"); SB();
  asm volatile("s_waitcnt lgkmcnt(0)" ::: "memory"); SB();
  asm volatile("ds_read_b128 a[52:55], %0 offset:2048" :  : "v"(aA[1]) : "memory"); SB();
  asm volatile("s_waitcnt lgkmcnt(0)" ::: "memory"); SB();
  asm volatile("s_waitcnt lgkmcnt(0)" :  :  : "memory"); SB();
  asm volatile("s_waitcnt lgkmcnt(0)" ::: "memory"); SB();
  asm volatile("s_add_u32 m0, %0, 99584" :  : "s"(mA) : "memory", "scc"); SB();
  asm volatile("s_waitcnt lgkmcnt(0)" ::: "memory"); SB();
  asm volatile("s_nop 0\n\tbuffer_load_dwordx4 %0, %1, %2 offen offset:768 lds" :  : "v"(voS), "s"(rA), "s"((2 ^ (2 * w)) * 32768) : "memory"); SB();
  asm volatile("s_waitcnt lgkmcnt(0)" ::: "memory"); SB();
  asm volatile("s_add_u32 m0, %0, 100608" :  : "s"(mA) : "memory", "scc"); SB();
  asm volatile("s_waitcnt lgkmcnt(0)" ::: "memory"); SB();
  asm volatile("s_nop 0\n\tbuffer_load_dwordx4 %0, %1, %2 offen offset:768 lds" :  : "v"(voS), "s"(rA), "s"((3 ^ (2 * w)) * 32768) : "memory"); SB();
  asm volatile("s_waitcnt lgkmcnt(0)" ::: "memory"); SB();
  asm volatile("s_waitcnt vmcnt(15)" :  :  : "memory"); SB();
  asm volatile("s_waitcnt lgkmcnt(0)" ::: "memory"); SB();
  asm volatile("ds_read_b128 a[40:43], %0 offset:4096" :  : "v"(aA[0]) : "memory"); SB();
  asm volatile("s_waitcnt lgkmcnt(0)" ::: "memory"); SB();
  asm volatile("ds_read_b128 a[56:59], %0 offset:4096" :  : "v"(aA[1]) : "memory"); SB();
  asm volatile("s_waitcnt lgkmcnt(0)" ::: "memory"); SB();
  asm volatile("s_waitcnt lgkmcnt(0)" :  :  : "memory"); SB();
  asm volatile("s_waitcnt lgkmcnt(0)" ::: "memory"); SB();
  asm volatile("s_add_u32 m0, %0, 101632" :  : "s"(mA) : "memory", "scc"); SB();
  asm volatile("s_waitcnt lgkmcnt(0)" ::: "memory"); SB();
  asm volatile("buffer_load_dwordx4 %0, %1, %2 offen offset:768 lds" :  : "v"(voS), "s"(rA), "s"((4 ^ (2 * w)) * 32768) : "memory"); SB();
  asm volatile("s_waitcnt lgkmcnt(0)" ::: "memory"); SB();
  asm volatile("s_add_u32 m0, %0, 102656" :  : "s"(mA) : "memory", "scc"); SB();
  asm volatile("s_waitcnt lgkmcnt(0)" ::: "memory"); SB();
  asm volatile("buffer_load_dwordx4 %0, %1, %2 offen offset:768 lds" :  : "v"(voS), "s"(rA), "s"((5 ^ (2 * w)) * 32768) : "memory"); SB();
  asm volatile("s_waitcnt lgkmcnt(0)" ::: "memory"); SB();
  asm volatile("s_waitcnt vmcnt(15)" :  :  : "memory"); SB();
  asm volatile("s_waitcnt lgkmcnt(0)" ::: "memory"); SB();
  asm volatile("ds_read_b128 a[44:47], %0 offset:6144" :  : "v"(aA[0]) : "memory"); SB();
  asm volatile("s_waitcnt lgkmcnt(0)" ::: "memory"); SB();
  asm volatile("ds_read_b128 a[60:63], %0 offset:6144" :  : "v"(aA[1]) : "memory"); SB();
  asm volatile("s_waitcnt lgkmcnt(0)" ::: "memory"); SB();
  asm volatile("s_waitcnt lgkmcnt(0)" :  :  : "memory"); SB();
  asm volatile("s_waitcnt lgkmcnt(0)" ::: "memory"); SB();
  asm volatile("s_add_u32 m0, %0, 103680" :  : "s"(mA) : "memory", "scc"); SB();
  asm volatile("s_waitcnt lgkmcnt(0)" ::: "memory"); SB();
  asm volatile("buffer_load_dwordx4 %0, %1, %2 offen offset:768 lds" :  : "v"(voS), "s"(rA), "s"((6 ^ (2 * w)) * 32768) : "memory"); SB();
  asm volatile("s_waitcnt lgkmcnt(0)" ::: "memory"); SB();
  asm volatile("s_add_u32 m0, %0, 104704" :  : "s"(mA) : "memory", "scc"); SB();
  asm volatile("s_waitcnt lgkmcnt(0)" ::: "memory"); SB();
  asm volatile("s_nop 0\n\tbuffer_load_dwordx4 %0, %1, %2 offen offset:768 lds" :  : "v"(voS), "s"(rA), "s"((7 ^ (2 * w)) * 32768) : "memory"); SB();
  asm volatile("s_waitcnt lgkmcnt(0)" ::: "memory"); SB();
  asm volatile("s_waitcnt vmcnt(15)" :  :  : "memory"); SB();
  asm volatile("s_waitcnt lgkmcnt(0)" ::: "memory"); SB();
  asm volatile("ds_read_b128 a[64:67], %0 offset:8192" :  : "v"(aA[0]) : "memory"); SB();
  asm volatile("s_waitcnt lgkmcnt(0)" ::: "memory"); SB();
  asm volatile("ds_read_b128 a[80:83], %0 offset:8192" :  : "v"(aA[1]) : "memory"); SB();
  asm volatile("s_waitcnt lgkmcnt(0)" ::: "memory"); SB();
  asm volatile("s_waitcnt lgkmcnt(0)" :  :  : "memory"); SB();
  asm volatile("s_waitcnt lgkmcnt(0)" ::: "memory"); SB();
  asm volatile("s_add_u32 m0, %0, 105600" :  : "s"(mA) : "memory", "scc"); SB();
  asm volatile("s_waitcnt lgkmcnt(0)" ::: "memory"); SB();
  asm volatile("buffer_load_dwordx4 %0, %1, %2 offen offset:896 lds" :  : "v"(voS), "s"(rA), "s"((0 ^ (2 * w)) * 32768) : "memory"); SB();
  asm volatile("s_waitcnt lgkmcnt(0)" ::: "memory"); SB();
  asm volatile("s_add_u32 m0, %0, 106624" :  : "s"(mA) : "memory", "scc"); SB();
  asm volatile("s_waitcnt lgkmcnt(0)" ::: "memory"); SB();
  asm volatile("s_nop 0\n\tbuffer_load_dwordx4 %0, %1, %2 offen offset:896 lds" :  : "v"(voS), "s"(rA), "s"((1 ^ (2 * w)) * 32768) : "memory"); SB();
  asm volatile("s_waitcnt lgkmcnt(0)" ::: "memory"); SB();
  asm volatile("s_waitcnt vmcnt(15)" :  :  : "memory"); SB();
  asm volatile("s_waitcnt lgkmcnt(0)" ::: "memory"); SB();
  asm volatile("ds_read_b128 a[68:71], %0 offset:10240" :  : "v"(aA[0]) : "memory"); SB();
  asm volatile("s_waitcnt lgkmcnt(0)" ::: "memory"); SB();
  asm volatile("ds_read_b128 a[84:87], %0 offset:10240" :  : "v"(aA[1]) : "memory"); SB();
  asm volatile("s_waitcnt lgkmcnt(0)" ::: "memory"); SB();
  }
  ST(15);
  { unsigned lane = lane0, w = w0, rb = rb0, cb = cb0;
  asm volatile("" : "+v"(lane)); asm volatile("" : "+s"(w), "+s"(rb), "+s"(cb)); SB();
  const unsigned wm = w >> 1, wn = w & 1, g4 = lane >> 4, l15 = lane & 15, q4 = l15 >> 2, p4 = l15 & 3;
  const unsigned nch = 3 - (rb >> 4), ch0 = cb * 80 + rb * 2 + min(rb, 16u);
  const unsigned l16 = lane * 16;
  const unsigned vZ = W_Z2 + ((rb * 2 + wm) * 48 + cb * 6 + wn * 3) * 1024 + l16;
  const unsigned vH4 = W_HH + ((rb * 4 + wm * 2) * 24 + cb * 3 + wn * 2) * 1024 + l16, vH2 = W_HH + ((rb * 4 + w) * 24 + cb * 3 + 1) * 1024 + l16;
  const unsigned vY = W_YY + ch0 * 4096 + w * 1024 + l16;
  const unsigned c2o = (nch - 2) * 2, c2s = 8192u + ((rb >> 4) << 30), w3s = (w + 1) >> 2 << 30;
  (void)wm; (void)wn; (void)g4; (void)q4; (void)p4; (void)vZ; (void)vH4; (void)vH2; (void)vY; (void)c2o; (void)c2s; (void)w3s;
  f32x4 g0[4][3], g1[4][3], pr[2][6], yp[4];
  unsigned dd = da + (((cu - 1) >> 3) * 2 + ((cu & 7) == 0)) * 8192;
  unsigned pw1 = lb + 98304 + w * 16384 + l16, vY_ = vY;
  unsigned ydr = lb + 0 + (w * 2 + (lane >> 5)) * 1024 + (lane & 1) * 512 + ((((lane >> 1) & 15) ^ ((lane & 1) << 3)) << 4), ykr = lb + 49152 + 512 + w * 1024 + l16;
  u32x4 ykv, yo;
  float gt[4], ge[4], Dv;
  unsigned z1[2][3][2];
  asm volatile("" : "+v"(xd[0]), "+v"(xd[1]));
  kset();
  asm volatile("" : "+v"(dd), "+v"(pw1), "+v"(ydr), "+v"(ykr), "+v"(vY_));
  SB();
  asm volatile("s_waitcnt vmcnt(10)\n\t.Lc_%=:\n\tv_cmpx_eq_u64 vcc, %1, v[224:225]\n\ts_cmp_eq_u64 vcc, -1\n\ts_mov_b64 exec, -1\n\ts_cbranch_scc0 .Lr_%=\n\t.Ld_%=:\n\t.subsection 1\n\t.Lr_%=:\n\ts_sub_i32 %0, %0, 1\n\ts_cmp_lt_i32 %0, 1\n\ts_cbranch_scc1 .Ld_%=\n\ts_sleep 2\n\tbuffer_load_dwordx2 v[224:225], %2, %3, 0 offen sc1\n\ts_waitcnt vmcnt(0)\n\ts_branch .Lc_%=\n\t.subsection 0" : "+s"(tmo) : "s"(tg), "v"(vFZ), "s"(rWS) : "memory", "vcc", "scc"); SB();
  asm volatile("buffer_load_dwordx4 v[232:235], %0, %1, %2 offen offset:0 sc1" :  : "v"(vB[0]), "s"(rWS), "s"(0u) : "memory"); SB();
  asm volatile("buffer_load_dwordx4 v[236:239], %0, %1, %2 offen offset:1024 sc1" :  : "v"(vB[0]), "s"(rWS), "s"(0u) : "memory"); SB();
  asm volatile("buffer_load_dwordx4 v[240:243], %0, %1, %2 offen offset:2048 sc1" :  : "v"(vB[0]), "s"(rWS), "s"(0u) : "memory"); SB();
  asm volatile("buffer_load_dwordx4 v[244:247], %0, %1, %2 offen offset:0 sc1" :  : "v"(vB[0]), "s"(rWS), "s"(49152u) : "memory"); SB();
  asm volatile("buffer_load_dwordx4 v[248:251], %0, %1, %2 offen offset:1024 sc1" :  : "v"(vB[0]), "s"(rWS), "s"(49152u) : "memory"); SB();
  ST(16);
  asm volatile("buffer_load_dwordx4 v[252:255], %0, %1, %2 offen offset:2048 sc1" :  : "v"(vB[0]), "s"(rWS), "s"(49152u) : "memory"); SB();
  asm volatile("buffer_load_dwordx4 v[160:163], %0, %1, %2 offen offset:0 sc1" :  : "v"(vB[0]), "s"(rWS), "s"(98304u) : "memory"); SB();
  asm volatile("buffer_load_dwordx4 v[164:167], %0, %1, %2 offen offset:1024 sc1" :  : "v"(vB[0]), "s"(rWS), "s"(98304u) : "memory"); SB();
  asm volatile("buffer_load_dwordx4 v[168:171], %0, %1, %2 offen offset:2048 sc1" :  : "v"(vB[0]), "s"(rWS), "s"(98304u) : "memory"); SB();
  asm volatile("buffer_load_dwordx4 v[172:175], %0, %1, %2 offen offset:0 sc1" :  : "v"(vB[0]), "s"(rWS), "s"(147456u) : "memory"); SB();
  asm volatile("buffer_load_dwordx4 v[176:179], %0, %1, %2 offen offset:1024 sc1" :  : "v"(vB[0]), "s"(rWS), "s"(147456u) : "memory"); SB();
  asm volatile("buffer_load_dwordx4 v[180:183], %0, %1, %2 offen offset:2048 sc1" :  : "v"(vB[0]), "s"(rWS), "s"(147456u) : "memory"); SB();
  asm volatile("buffer_load_dwordx4 v[184:187], %0, %1, %2 offen offset:0 sc1" :  : "v"(vB[0]), "s"(rWS), "s"(196608u) : "memory"); SB();
  asm volatile("buffer_load_dwordx4 v[188:191], %0, %1, %2 offen offset:1024 sc1" :  : "v"(vB[0]), "s"(rWS), "s"(196608u) : "memory"); SB();
  asm volatile("buffer_load_dwordx4 v[192:195], %0, %1, %2 offen offset:2048 sc1" :  : "v"(vB[0]), "s"(rWS), "s"(196608u) : "memory"); SB();
  asm volatile("s_waitcnt vmcnt(12)" :  :  : "memory"); SB();
  asm volatile("v_mfma_f32_16x16x32_bf16 %0, v[232:235], a[160:163], 0" : "=&v"(g0[0][0]) :  : ); SB();
  asm volatile("ds_write_b128 %0, %1 offset:0" :  : "v"(dd), "v"(ac[0]) : "memory"); SB();
  asm volatile("buffer_load_dwordx4 v[196:199], %0, %1, %2 offen offset:0 sc1" :  : "v"(vB[0]), "s"(rWS), "s"(245760u) : "memory"); SB();
  asm volatile("v_mfma_f32_16x16x32_bf16 %0, v[232:235], a[164:167], 0" : "=&v"(g0[1][0]) :  : ); SB();
  asm volatile("buffer_load_dwordx4 v[200:203], %0, %1, %2 offen offset:1024 sc1" :  : "v"(vB[0]), "s"(rWS), "s"(245760u) : "memory"); SB();
  asm volatile("v_mfma_f32_16x16x32_bf16 %0, v[232:235], a[168:171], 0" : "=&v"(g0[2][0]) :  : ); SB();
  asm volatile("v_mfma_f32_16x16x32_bf16 %0, v[232:235], a[172:175], 0" : "=&v"(g0[3][0]) :  : ); SB();
  asm volatile("ds_write_b128 %0, %1 offset:1024" :  : "v"(dd), "v"(ac[1]) : "memory"); SB();
  asm volatile("buffer_load_dwordx4 v[204:207], %0, %1, %2 offen offset:2048 sc1" :  : "v"(vB[0]), "s"(rWS), "s"(245760u) : "memory"); SB();
  asm volatile("v_mfma_f32_16x16x32_bf16 %0, v[236:239], a[160:163], 0" : "=&v"(g0[0][1]) :  : ); SB();
  asm volatile("v_mfma_f32_16x16x32_bf16 %0, v[236:239], a[164:167], 0" : "=&v"(g0[1][1]) :  : ); SB();
  asm volatile("v_mfma_f32_16x16x32_bf16 %0, v[236:239], a[168:171], 0" : "=&v"(g0[2][1]) :  : ); SB();
  asm volatile("ds_write_b128 %0, %1 offset:2048" :  : "v"(dd), "v"(ac[2]) : "memory"); SB();
  asm volatile("buffer_load_dwordx4 v[208:211], %0, %1, %2 offen offset:0 sc1" :  : "v"(vB[0]), "s"(rWS), "s"(294912u) : "memory"); SB();
  asm volatile("v_mfma_f32_16x16x32_bf16 %0, v[236:239], a[172:175], 0" : "=&v"(g0[3][1]) :  : ); SB();
  asm volatile("v_mfma_f32_16x16x32_bf16 %0, v[240:243], a[160:163], 0" : "=&v"(g0[0][2]) :  : ); SB();
  asm volatile("v_mfma_f32_16x16x32_bf16 %0, v[240:243], a[164:167], 0" : "=&v"(g0[1][2]) :  : ); SB();
  asm volatile("ds_write_b128 %0, %1 offset:3072" :  : "v"(dd), "v"(ac[3]) : "memory"); SB();
  asm volatile("buffer_load_dwordx4 v[212:215], %0, %1, %2 offen offset:1024 sc1" :  : "v"(vB[0]), "s"(rWS), "s"(294912u) : "memory"); SB();
  asm volatile("v_mfma_f32_16x16x32_bf16 %0, v[240:243], a[168:171], 0" : "=&v"(g0[2][2]) :  : ); SB();
  asm volatile("v_mfma_f32_16x16x32_bf16 %0, v[240:243], a[172:175], 0" : "=&v"(g0[3][2]) :  : ); SB();
  asm volatile("s_waitcnt vmcnt(14)" :  :  : "memory"); SB();
  asm volatile("v_mfma_f32_16x16x32_bf16 %0, v[244:247], a[176:179], %0" : "+v"(g0[0][0]) :  : ); SB();
  asm volatile("ds_write_b128 %0, %1 offset:4096" :  : "v"(dd), "v"(ac[4]) : "memory"); SB();
  asm volatile("buffer_load_dwordx4 v[216:219], %0, %1, %2 offen offset:2048 sc1" :  : "v"(vB[0]), "s"(rWS), "s"(294912u) : "memory"); SB();
  asm volatile("v_mfma_f32_16x16x32_bf16 %0, v[244:247], a[180:183], %0" : "+v"(g0[1][0]) :  : ); SB();
  asm volatile("v_mfma_f32_16x16x32_bf16 %0, v[244:247], a[184:187], %0" : "+v"(g0[2][0]) :  : ); SB();
  asm volatile("v_mfma_f32_16x16x32_bf16 %0, v[244:247], a[188:191], %0" : "+v"(g0[3][0]) :  : ); SB();
  asm volatile("ds_write_b128 %0, %1 offset:5120" :  : "v"(dd), "v"(ac[5]) : "memory"); SB();
  asm volatile("v_mfma_f32_16x16x32_bf16 %0, v[248:251], a[176:179], %0" : "+v"(g0[0][1]) :  : ); SB();
  asm volatile("v_mfma_f32_16x16x32_bf16 %0, v[248:251], a[180:183], %0" : "+v"(g0[1][1]) :  : ); SB();
  asm volatile("buffer_load_dwordx4 v[220:223], %0, %1, %2 offen offset:0 sc1" :  : "v"(vB[0]), "s"(rWS), "s"(344064u) : "memory"); SB();
  asm volatile("v_mfma_f32_16x16x32_bf16 %0, v[248:251], a[184:187], %0" : "+v"(g0[2][1]) :  : ); SB();
  asm volatile("ds_write_b128 %0, %1 offset:6144" :  : "v"(dd), "v"(ac[6]) : "memory"); SB();
  asm volatile("v_mfma_f32_16x16x32_bf16 %0, v[248:251], a[188:191], %0" : "+v"(g0[3][1]) :  : ); SB();
  asm volatile("v_mfma_f32_16x16x32_bf16 %0, v[252:255], a[176:179], %0" : "+v"(g0[0][2]) :  : ); SB();
  asm volatile("v_mfma_f32_16x16x32_bf16 %0, v[252:255], a[180:183], %0" : "+v"(g0[1][2]) :  : ); SB();
  asm volatile("ds_write_b128 %0, %1 offset:7168" :  : "v"(dd), "v"(ac[7]) : "memory"); SB();
  asm volatile("buffer_load_dwordx4 v[224:227], %0, %1, %2 offen offset:1024 sc1" :  : "v"(vB[0]), "s"(rWS), "s"(344064u) : "memory"); SB();
  asm volatile("v_mfma_f32_16x16x32_bf16 %0, v[252:255], a[184:187], %0" : "+v"(g0[2][2]) :  : ); SB();
  asm volatile("v_mfma_f32_16x16x32_bf16 %0, v[252:255], a[188:191], %0" : "+v"(g0[3][2]) :  : ); SB();
  asm volatile("s_waitcnt vmcnt(14)" :  :  : "memory"); SB();
  asm volatile("v_mfma_f32_16x16x32_bf16 %0, v[160:163], a[192:195], %0" : "+v"(g0[0][0]) :  : ); SB();
  asm volatile("buffer_load_dwordx4 v[228:231], %0, %1, %2 offen offset:2048 sc1" :  : "v"(vB[0]), "s"(rWS), "s"(344064u) : "memory"); SB();
  asm volatile("v_mfma_f32_16x16x32_bf16 %0, v[160:163], a[196:199], %0" : "+v"(g0[1][0]) :  : ); SB();
  asm volatile("v_mfma_f32_16x16x32_bf16 %0, v[160:163], a[200:203], %0" : "+v"(g0[2][0]) :  : ); SB();
  asm volatile("s_waitcnt lgkmcnt(0)\n\ts_barrier" :  :  : "memory"); SB();
  asm volatile("ds_read_b128 %0, %1 offset:0" : "=&v"(yp[0]) : "v"(ydr) : "memory"); SB();
  asm volatile("buffer_load_dwordx4 v[232:235], %0, %1, %2 offen offset:0 sc1" :  : "v"(vB[0]), "s"(rWS), "s"(393216u) : "memory"); SB();
  asm volatile("v_mfma_f32_16x16x32_bf16 %0, v[160:163], a[204:207], %0" : "+v"(g0[3][0]) :  : ); SB();
  asm volatile("ds_read_b128 %0, %1 offset:256" : "=&v"(yp[1]) : "v"(ydr) : "memory"); SB();
  asm volatile("v_mfma_f32_16x16x32_bf16 %0, v[164:167], a[192:195], %0" : "+v"(g0[0][1]) :  : ); SB();
  asm volatile("ds_read_b128 %0, %1 offset:8192" : "=&v"(yp[2]) : "v"(ydr) : "memory"); SB();
  asm volatile("v_mfma_f32_16x16x32_bf16 %0, v[164:167], a[196:199], %0" : "+v"(g0[1][1]) :  : ); SB();
  asm volatile("ds_read_b128 %0, %1 offset:8448" : "=&v"(yp[3]) : "v"(ydr) : "memory"); SB();
  asm volatile("v_mfma_f32_16x16x32_bf16 %0, v[164:167], a[200:203], %0" : "+v"(g0[2][1]) :  : ); SB();
  asm volatile("ds_read_b128 %0, %1 offset:0" : "=&v"(ykv) : "v"(ykr) : "memory"); SB();
  asm volatile("buffer_load_dwordx4 v[236:239], %0, %1, %2 offen offset:1024 sc1" :  : "v"(vB[0]), "s"(rWS), "s"(393216u) : "memory"); SB();
  asm volatile("v_mfma_f32_16x16x32_bf16 %0, v[164:167], a[204:207], %0" : "+v"(g0[3][1]) :  : ); SB();
  asm volatile("v_lshlrev_b32 %0, 16, %1" : "=v"(Dv) : "v"(dD[0]) : ); SB();
  asm volatile("v_mfma_f32_16x16x32_bf16 %0, v[168:171], a[192:195], %0" : "+v"(g0[0][2]) :  : ); SB();
  asm volatile("v_mfma_f32_16x16x32_bf16 %0, v[168:171], a[196:199], %0" : "+v"(g0[1][2]) :  : ); SB();
  asm volatile("v_mfma_f32_16x16x32_bf16 %0, v[168:171], a[200:203], %0" : "+v"(g0[2][2]) :  : ); SB();
  asm volatile("v_mfma_f32_16x16x32_bf16 %0, v[168:171], a[204:207], %0" : "+v"(g0[3][2]) :  : ); SB();
  asm volatile("buffer_load_dwordx4 v[240:243], %0, %1, %2 offen offset:2048 sc1" :  : "v"(vB[0]), "s"(rWS), "s"(393216u) : "memory"); SB();
  asm volatile("s_waitcnt vmcnt(15)" :  :  : "memory"); SB();
  asm volatile("v_mfma_f32_16x16x32_bf16 %0, v[172:175], a[208:211], %0" : "+v"(g0[0][0]) :  : ); SB();
  asm volatile("s_waitcnt lgkmcnt(0)" : "+v"(yp[0]), "+v"(yp[1]), "+v"(yp[2]), "+v"(yp[3]), "+v"(ykv) :  : "memory"); SB();
  asm volatile("v_add_f32 %0, %0, %1" : "+v"(yp[0][0]) : "v"(yp[2][0]) : ); SB();
  asm volatile("v_mfma_f32_16x16x32_bf16 %0, v[172:175], a[212:215], %0" : "+v"(g0[1][0]) :  : ); SB();
  asm volatile("v_add_f32 %0, %0, %1" : "+v"(yp[0][1]) : "v"(yp[2][1]) : ); SB();
  asm volatile("v_add_f32 %0, %0, %1" : "+v"(yp[0][2]) : "v"(yp[2][2]) : ); SB();
  asm volatile("v_mfma_f32_16x16x32_bf16 %0, v[172:175], a[216:219], %0" : "+v"(g0[2][0]) :  : ); SB();
  asm volatile("v_add_f32 %0, %0, %1" : "+v"(yp[0][3]) : "v"(yp[2][3]) : ); SB();
  asm volatile("v_add_f32 %0, %0, %1" : "+v"(yp[1][0]) : "v"(yp[3][0]) : ); SB();
  asm volatile("v_mfma_f32_16x16x32_bf16 %0, v[172:175], a[220:223], %0" : "+v"(g0[3][0]) :  : ); SB();
  asm volatile("v_add_f32 %0, %0, %1" : "+v"(yp[1][1]) : "v"(yp[3][1]) : ); SB();
  asm volatile("v_add_f32 %0, %0, %1" : "+v"(yp[1][2]) : "v"(yp[3][2]) : ); SB();
  asm volatile("buffer_load_dwordx4 v[244:247], %0, %1, %2 offen offset:0 sc1" :  : "v"(vB[0]), "s"(rWS), "s"(442368u) : "memory"); SB();
  asm volatile("v_mfma_f32_16x16x32_bf16 %0, v[176:179], a[208:211], %0" : "+v"(g0[0][1]) :  : ); SB();
  asm volatile("v_add_f32 %0, %0, %1" : "+v"(yp[1][3]) : "v"(yp[3][3]) : ); SB();
  asm volatile("v_lshlrev_b32 %0, 16, %1" : "=v"(yp[2][0]) : "v"(ykv[0]) : ); SB();
  asm volatile("v_mfma_f32_16x16x32_bf16 %0, v[176:179], a[212:215], %0" : "+v"(g0[1][1]) :  : ); SB();
  asm volatile("v_and_b32 %0, 0xffff0000, %1" : "=v"(yp[2][1]) : "v"(ykv[0]) : ); SB();
  asm volatile("v_lshlrev_b32 %0, 16, %1" : "=v"(yp[2][2]) : "v"(ykv[1]) : ); SB();
  asm volatile("v_mfma_f32_16x16x32_bf16 %0, v[176:179], a[216:219], %0" : "+v"(g0[2][1]) :  : ); SB();
  asm volatile("v_and_b32 %0, 0xffff0000, %1" : "=v"(yp[2][3]) : "v"(ykv[1]) : ); SB();
  asm volatile("v_lshlrev_b32 %0, 16, %1" : "=v"(yp[3][0]) : "v"(ykv[2]) : ); SB();
  asm volatile("v_mfma_f32_16x16x32_bf16 %0, v[176:179], a[220:223], %0" : "+v"(g0[3][1]) :  : ); SB();
  asm volatile("v_and_b32 %0, 0xffff0000, %1" : "=v"(yp[3][1]) : "v"(ykv[2]) : ); SB();
  asm volatile("v_lshlrev_b32 %0, 16, %1" : "=v"(yp[3][2]) : "v"(ykv[3]) : ); SB();
  asm volatile("v_mfma_f32_16x16x32_bf16 %0, v[180:183], a[208:211], %0" : "+v"(g0[0][2]) :  : ); SB();
  asm volatile("v_and_b32 %0, 0xffff0000, %1" : "=v"(yp[3][3]) : "v"(ykv[3]) : ); SB();
  asm volatile("v_fmac_f32 %0, %1, %2" : "+v"(yp[0][0]) : "v"(yp[2][0]), "v"(Dv) : ); SB();
  asm volatile("buffer_load_dwordx4 v[248:251], %0, %1, %2 offen offset:1024 sc1" :  : "v"(vB[0]), "s"(rWS), "s"(442368u) : "memory"); SB();
  asm volatile("v_mfma_f32_16x16x32_bf16 %0, v[180:183], a[212:215], %0" : "+v"(g0[1][2]) :  : ); SB();
  asm volatile("v_fmac_f32 %0, %1, %2" : "+v"(yp[0][1]) : "v"(yp[2][1]), "v"(Dv) : ); SB();
  asm volatile("v_fmac_f32 %0, %1, %2" : "+v"(yp[0][2]) : "v"(yp[2][2]), "v"(Dv) : ); SB();
  asm volatile("v_mfma_f32_16x16x32_bf16 %0, v[180:183], a[216:219], %0" : "+v"(g0[2][2]) :  : ); SB();
  asm volatile("v_fmac_f32 %0, %1, %2" : "+v"(yp[0][3]) : "v"(yp[2][3]), "v"(Dv) : ); SB();
  asm volatile("v_fmac_f32 %0, %1, %2" : "+v"(yp[1][0]) : "v"(yp[3][0]), "v"(Dv) : ); SB();
  asm volatile("v_mfma_f32_16x16x32_bf16 %0, v[180:183], a[220:223], %0" : "+v"(g0[3][2]) :  : ); SB();
  asm volatile("v_fmac_f32 %0, %1, %2" : "+v"(yp[1][1]) : "v"(yp[3][1]), "v"(Dv) : ); SB();
  asm volatile("v_fmac_f32 %0, %1, %2" : "+v"(yp[1][2]) : "v"(yp[3][2]), "v"(Dv) : ); SB();
  ST(25);
  asm volatile("s_waitcnt vmcnt(14)" :  :  : "memory"); SB();
  asm volatile("v_mfma_f32_16x16x32_bf16 %0, v[184:187], a[224:227], %0" : "+v"(g0[0][0]) :  : ); SB();
  asm volatile("v_fmac_f32 %0, %1, %2" : "+v"(yp[1][3]) : "v"(yp[3][3]), "v"(Dv) : ); SB();
  asm volatile("v_cvt_pk_bf16_f32 %0, %1, %2" : "=v"(yo[0]) : "v"(yp[0][0]), "v"(yp[0][1]) : ); SB();
  asm volatile("buffer_load_dwordx4 v[252:255], %0, %1, %2 offen offset:2048 sc1" :  : "v"(vB[0]), "s"(rWS), "s"(442368u) : "memory"); SB();
  asm volatile("v_mfma_f32_16x16x32_bf16 %0, v[184:187], a[228:231], %0" : "+v"(g0[1][0]) :  : ); SB();
  asm volatile("v_cvt_pk_bf16_f32 %0, %1, %2" : "=v"(yo[1]) : "v"(yp[0][2]), "v"(yp[0][3]) : ); SB();
  asm volatile("v_cvt_pk_bf16_f32 %0, %1, %2" : "=v"(yo[2]) : "v"(yp[1][0]), "v"(yp[1][1]) : ); SB();
  asm volatile("v_mfma_f32_16x16x32_bf16 %0, v[184:187], a[232:235], %0" : "+v"(g0[2][0]) :  : ); SB();
  asm volatile("v_cvt_pk_bf16_f32 %0, %1, %2" : "=v"(yo[3]) : "v"(yp[1][2]), "v"(yp[1][3]) : ); SB();
  asm volatile("v_mfma_f32_16x16x32_bf16 %0, v[184:187], a[236:239], %0" : "+v"(g0[3][0]) :  : ); SB();
  asm volatile("buffer_store_dwordx4 %1, %0, %2, %3 offen offset:0 sc1\n\ts_nop 1" :  : "v"(vY_), "v"(yo), "s"(rWS), "s"(0u) : "memory"); SB();
  asm volatile("v_mfma_f32_16x16x32_bf16 %0, v[188:191], a[224:227], %0" : "+v"(g0[0][1]) :  : ); SB();
  asm volatile("ds_read_b128 %0, %1 offset:16384" : "=&v"(yp[0]) : "v"(ydr) : "memory"); SB();
  asm volatile("v_mfma_f32_16x16x32_bf16 %0, v[188:191], a[228:231], %0" : "+v"(g0[1][1]) :  : ); SB();
  asm volatile("ds_read_b128 %0, %1 offset:16640" : "=&v"(yp[1]) : "v"(ydr) : "memory"); SB();
  asm volatile("v_mfma_f32_16x16x32_bf16 %0, v[188:191], a[232:235], %0" : "+v"(g0[2][1]) :  : ); SB();
  asm volatile("ds_read_b128 %0, %1 offset:24576" : "=&v"(yp[2]) : "v"(ydr) : "memory"); SB();
  asm volatile("v_mfma_f32_16x16x32_bf16 %0, v[188:191], a[236:239], %0" : "+v"(g0[3][1]) :  : ); SB();
  asm volatile("ds_read_b128 %0, %1 offset:24832" : "=&v"(yp[3]) : "v"(ydr) : "memory"); SB();
  asm volatile("v_mfma_f32_16x16x32_bf16 %0, v[192:195], a[224:227], %0" : "+v"(g0[0][2]) :  : ); SB();
  asm volatile("ds_read_b128 %0, %1 offset:12928" : "=&v"(ykv) : "v"(ykr) : "memory"); SB();
  asm volatile("v_mfma_f32_16x16x32_bf16 %0, v[192:195], a[228:231], %0" : "+v"(g0[1][2]) :  : ); SB();
  asm volatile("v_lshlrev_b32 %0, 16, %1" : "=v"(Dv) : "v"(dD[1]) : ); SB();
  asm volatile("v_mfma_f32_16x16x32_bf16 %0, v[192:195], a[232:235], %0" : "+v"(g0[2][2]) :  : ); SB();
  asm volatile("v_mfma_f32_16x16x32_bf16 %0, v[192:195], a[236:239], %0" : "+v"(g0[3][2]) :  : ); SB();
  asm volatile("buffer_load_dwordx4 v[160:163], %0, %1, %2 offen offset:0 sc1" :  : "v"(vB[0]), "s"(rWS), "s"(491520u) : "memory"); SB();
  asm volatile("s_waitcnt vmcnt(14)" :  :  : "memory"); SB();
  asm volatile("v_mfma_f32_16x16x32_bf16 %0, v[196:199], a[240:243], %0" : "+v"(g0[0][0]) :  : ); SB();
  asm volatile("v_mfma_f32_16x16x32_bf16 %0, v[196:199], a[244:247], %0" : "+v"(g0[1][0]) :  : ); SB();
  asm volatile("v_mfma_f32_16x16x32_bf16 %0, v[196:199], a[248:251], %0" : "+v"(g0[2][0]) :  : ); SB();
  asm volatile("s_waitcnt lgkmcnt(0)" : "+v"(yp[0]), "+v"(yp[1]), "+v"(yp[2]), "+v"(yp[3]), "+v"(ykv) :  : "memory"); SB();
  asm volatile("v_add_f32 %0, %0, %1" : "+v"(yp[0][0]) : "v"(yp[2][0]) : ); SB();
  asm volatile("buffer_load_dwordx4 v[164:167], %0, %1, %2 offen offset:1024 sc1" :  : "v"(vB[0]), "s"(rWS), "s"(491520u) : "memory"); SB();
  asm volatile("v_mfma_f32_16x16x32_bf16 %0, v[196:199], a[252:255], %0" : "+v"(g0[3][0]) :  : ); SB();
  asm volatile("v_add_f32 %0, %0, %1" : "+v"(yp[0][1]) : "v"(yp[2][1]) : ); SB();
  asm volatile("v_add_f32 %0, %0, %1" : "+v"(yp[0][2]) : "v"(yp[2][2]) : ); SB();
  asm volatile("v_mfma_f32_16x16x32_bf16 %0, v[200:203], a[240:243], %0" : "+v"(g0[0][1]) :  : ); SB();
  asm volatile("v_add_f32 %0, %0, %1" : "+v"(yp[0][3]) : "v"(yp[2][3]) : ); SB();
  asm volatile("v_add_f32 %0, %0, %1" : "+v"(yp[1][0]) : "v"(yp[3][0]) : ); SB();
  asm volatile("v_mfma_f32_16x16x32_bf16 %0, v[200:203], a[244:247], %0" : "+v"(g0[1][1]) :  : ); SB();
  asm volatile("v_add_f32 %0, %0, %1" : "+v"(yp[1][1]) : "v"(yp[3][1]) : ); SB();
  asm volatile("v_add_f32 %0, %0, %1" : "+v"(yp[1][2]) : "v"(yp[3][2]) : ); SB();
  asm volatile("v_mfma_f32_16x16x32_bf16 %0, v[200:203], a[248:251], %0" : "+v"(g0[2][1]) :  : ); SB();
  asm volatile("v_add_f32 %0, %0, %1" : "+v"(yp[1][3]) : "v"(yp[3][3]) : ); SB();
  asm volatile("v_lshlrev_b32 %0, 16, %1" : "=v"(yp[2][0]) : "v"(ykv[0]) : ); SB();
  asm volatile("v_mfma_f32_16x16x32_bf16 %0, v[200:203], a[252:255], %0" : "+v"(g0[3][1]) :  : ); SB();
  asm volatile("v_and_b32 %0, 0xffff0000, %1" : "=v"(yp[2][1]) : "v"(ykv[0]) : ); SB();
  asm volatile("v_lshlrev_b32 %0, 16, %1" : "=v"(yp[2][2]) : "v"(ykv[1]) : ); SB();
  asm volatile("buffer_load_dwordx4 v[168:171], %0, %1, %2 offen offset:2048 sc1" :  : "v"(vB[0]), "s"(rWS), "s"(491520u) : "memory"); SB();
  asm volatile("v_mfma_f32_16x16x32_bf16 %0, v[204:207], a[240:243], %0" : "+v"(g0[0][2]) :  : ); SB();
  asm volatile("v_and_b32 %0, 0xffff0000, %1" : "=v"(yp[2][3]) : "v"(ykv[1]) : ); SB();
  asm volatile("v_lshlrev_b32 %0, 16, %1" : "=v"(yp[3][0]) : "v"(ykv[2]) : ); SB();
  asm volatile("v_mfma_f32_16x16x32_bf16 %0, v[204:207], a[244:247], %0" : "+v"(g0[1][2]) :  : ); SB();
  asm volatile("v_and_b32 %0, 0xffff0000, %1" : "=v"(yp[3][1]) : "v"(ykv[2]) : ); SB();
  asm volatile("v_lshlrev_b32 %0, 16, %1" : "=v"(yp[3][2]) : "v"(ykv[3]) : ); SB();
  asm volatile("v_mfma_f32_16x16x32_bf16 %0, v[204:207], a[248:251], %0" : "+v"(g0[2][2]) :  : ); SB();
  asm volatile("v_and_b32 %0, 0xffff0000, %1" : "=v"(yp[3][3]) : "v"(ykv[3]) : ); SB();
  asm volatile("v_fmac_f32 %0, %1, %2" : "+v"(yp[0][0]) : "v"(yp[2][0]), "v"(Dv) : ); SB();
  asm volatile("v_mfma_f32_16x16x32_bf16 %0, v[204:207], a[252:255], %0" : "+v"(g0[3][2]) :  : ); SB();
  asm volatile("v_fmac_f32 %0, %1, %2" : "+v"(yp[0][1]) : "v"(yp[2][1]), "v"(Dv) : ); SB();
  asm volatile("v_fmac_f32 %0, %1, %2" : "+v"(yp[0][2]) : "v"(yp[2][2]), "v"(Dv) : ); SB();
  asm volatile("s_waitcnt vmcnt(13)" :  :  : "memory"); SB();
  asm volatile("v_mfma_f32_16x16x32_bf16 %0, v[208:211], a[0:3], %0" : "+v"(g0[0][0]) :  : ); SB();
  asm volatile("v_fmac_f32 %0, %1, %2" : "+v"(yp[0][3]) : "v"(yp[2][3]), "v"(Dv) : ); SB();
  asm volatile("v_fmac_f32 %0, %1, %2" : "+v"(yp[1][0]) : "v"(yp[3][0]), "v"(Dv) : ); SB();
  asm volatile("buffer_load_dwordx4 v[172:175], %0, %1, %2 offen offset:0 sc1" :  : "v"(vB[0]), "s"(rWS), "s"(540672u) : "memory"); SB();
  asm volatile("v_mfma_f32_16x16x32_bf16 %0, v[208:211], a[4:7], %0" : "+v"(g0[1][0]) :  : ); SB();
  asm volatile("v_fmac_f32 %0, %1, %2" : "+v"(yp[1][1]) : "v"(yp[3][1]), "v"(Dv) : ); SB();
  asm volatile("v_fmac_f32 %0, %1, %2" : "+v"(yp[1][2]) : "v"(yp[3][2]), "v"(Dv) : ); SB();
  asm volatile("v_mfma_f32_16x16x32_bf16 %0, v[208:211], a[8:11], %0" : "+v"(g0[2][0]) :  : ); SB();
  asm volatile("v_fmac_f32 %0, %1, %2" : "+v"(yp[1][3]) : "v"(yp[3][3]), "v"(Dv) : ); SB();
  asm volatile("v_cvt_pk_bf16_f32 %0, %1, %2" : "=v"(yo[0]) : "v"(yp[0][0]), "v"(yp[0][1]) : ); SB();
  asm volatile("v_mfma_f32_16x16x32_bf16 %0, v[208:211], a[12:15], %0" : "+v"(g0[3][0]) :  : ); SB();
  asm volatile("v_cvt_pk_bf16_f32 %0, %1, %2" : "=v"(yo[1]) : "v"(yp[0][2]), "v"(yp[0][3]) : ); SB();
  asm volatile("v_cvt_pk_bf16_f32 %0, %1, %2" : "=v"(yo[2]) : "v"(yp[1][0]), "v"(yp[1][1]) : ); SB();
  asm volatile("v_mfma_f32_16x16x32_bf16 %0, v[212:215], a[0:3], %0" : "+v"(g0[0][1]) :  : ); SB();
  asm volatile("v_cvt_pk_bf16_f32 %0, %1, %2" : "=v"(yo[3]) : "v"(yp[1][2]), "v"(yp[1][3]) : ); SB();
  asm volatile("buffer_load_dwordx4 v[176:179], %0, %1, %2 offen offset:1024 sc1" :  : "v"(vB[0]), "s"(rWS), "s"(540672u) : "memory"); SB();
  asm volatile("v_mfma_f32_16x16x32_bf16 %0, v[212:215], a[4:7], %0" : "+v"(g0[1][1]) :  : ); SB();
  asm volatile("buffer_store_dwordx4 %1, %0, %2, %3 offen offset:0 sc1\n\ts_nop 1" :  : "v"(vY_), "v"(yo), "s"(rWS), "s"(4096u) : "memory"); SB();
  asm volatile("v_mfma_f32_16x16x32_bf16 %0, v[212:215], a[8:11], %0" : "+v"(g0[2][1]) :  : ); SB();
  asm volatile("ds_read_b128 %0, %1 offset:32768" : "=&v"(yp[0]) : "v"(ydr) : "memory"); SB();
  asm volatile("v_mfma_f32_16x16x32_bf16 %0, v[212:215], a[12:15], %0" : "+v"(g0[3][1]) :  : ); SB();
  asm volatile("ds_read_b128 %0, %1 offset:33024" : "=&v"(yp[1]) : "v"(ydr) : "memory"); SB();
  asm volatile("v_mfma_f32_16x16x32_bf16 %0, v[216:219], a[0:3], %0" : "+v"(g0[0][2]) :  : ); SB();
  asm volatile("ds_read_b128 %0, %1 offset:40960" : "=&v"(yp[2]) : "v"(ydr) : "memory"); SB();
  asm volatile("v_mfma_f32_16x16x32_bf16 %0, v[216:219], a[4:7], %0" : "+v"(g0[1][2]) :  : ); SB();
  asm volatile("ds_read_b128 %0, %1 offset:41216" : "=&v"(yp[3]) : "v"(ydr) : "memory"); SB();
  asm volatile("v_mfma_f32_16x16x32_bf16 %0, v[216:219], a[8:11], %0" : "+v"(g0[2][2]) :  : ); SB();
  asm volatile("ds_read_b128 %0, %1 offset:25856" : "=&v"(ykv) : "v"(ykr) : "memory"); SB();
  asm volatile("v_mfma_f32_16x16x32_bf16 %0, v[216:219], a[12:15], %0" : "+v"(g0[3][2]) :  : ); SB();
  asm volatile("v_lshlrev_b32 %0, 16, %1" : "=v"(Dv) : "v"(dD[2]) : ); SB();
  asm volatile("s_waitcnt vmcnt(13)" :  :  : "memory"); SB();
  asm volatile("v_mfma_f32_16x16x32_bf16 %0, v[220:223], a[16:19], %0" : "+v"(g0[0][0]) :  : ); SB();
  asm volatile("buffer_load_dwordx4 v[180:183], %0, %1, %2 offen offset:2048 sc1" :  : "v"(vB[0]), "s"(rWS), "s"(540672u) : "memory"); SB();
  asm volatile("v_mfma_f32_16x16x32_bf16 %0, v[220:223], a[20:23], %0" : "+v"(g0[1][0]) :  : ); SB();
  asm volatile("v_mfma_f32_16x16x32_bf16 %0, v[220:223], a[24:27], %0" : "+v"(g0[2][0]) :  : ); SB();
  asm volatile("v_mfma_f32_16x16x32_bf16 %0, v[220:223], a[28:31], %0" : "+v"(g0[3][0]) :  : ); SB();
  asm volatile("v_mfma_f32_16x16x32_bf16 %0, v[224:227], a[16:19], %0" : "+v"(g0[0][1]) :  : ); SB();
  asm volatile("s_waitcnt lgkmcnt(0)" : "+v"(yp[0]), "+v"(yp[1]), "+v"(yp[2]), "+v"(yp[3]), "+v"(ykv) :  : "memory"); SB();
  asm volatile("v_add_f32 %0, %0, %1" : "+v"(yp[0][0]) : "v"(yp[2][0]) : ); SB();
  asm volatile("v_mfma_f32_16x16x32_bf16 %0, v[224:227], a[20:23], %0" : "+v"(g0[1][1]) :  : ); SB();
  asm volatile("v_add_f32 %0, %0, %1" : "+v"(yp[0][1]) : "v"(yp[2][1]) : ); SB();
  asm volatile("v_add_f32 %0, %0, %1" : "+v"(yp[0][2]) : "v"(yp[2][2]) : ); SB();
  asm volatile("buffer_load_dwordx4 v[184:187], %0, %1, %2 offen offset:0 sc1" :  : "v"(vB[0]), "s"(rWS), "s"(589824u) : "memory"); SB();
  asm volatile("v_mfma_f32_16x16x32_bf16 %0, v[224:227], a[24:27], %0" : "+v"(g0[2][1]) :  : ); SB();
  asm volatile("v_add_f32 %0, %0, %1" : "+v"(yp[0][3]) : "v"(yp[2][3]) : ); SB();
  asm volatile("v_add_f32 %0, %0, %1" : "+v"(yp[1][0]) : "v"(yp[3][0]) : ); SB();
  asm volatile("v_mfma_f32_16x16x32_bf16 %0, v[224:227], a[28:31], %0" : "+v"(g0[3][1]) :  : ); SB();
  asm volatile("v_add_f32 %0, %0, %1" : "+v"(yp[1][1]) : "v"(yp[3][1]) : ); SB();
  asm volatile("v_add_f32 %0, %0, %1" : "+v"(yp[1][2]) : "v"(yp[3][2]) : ); SB();
  asm volatile("v_mfma_f32_16x16x32_bf16 %0, v[228:231], a[16:19], %0" : "+v"(g0[0][2]) :  : ); SB();
  asm volatile("v_add_f32 %0, %0, %1" : "+v"(yp[1][3]) : "v"(yp[3][3]) : ); SB();
  asm volatile("v_lshlrev_b32 %0, 16, %1" : "=v"(yp[2][0]) : "v"(ykv[0]) : ); SB();
  asm volatile("v_mfma_f32_16x16x32_bf16 %0, v[228:231], a[20:23], %0" : "+v"(g0[1][2]) :  : ); SB();
  asm volatile("v_and_b32 %0, 0xffff0000, %1" : "=v"(yp[2][1]) : "v"(ykv[0]) : ); SB();
  asm volatile("v_lshlrev_b32 %0, 16, %1" : "=v"(yp[2][2]) : "v"(ykv[1]) : ); SB();
  asm volatile("buffer_load_dwordx4 v[188:191], %0, %1, %2 offen offset:1024 sc1" :  : "v"(vB[0]), "s"(rWS), "s"(589824u) : "memory"); SB();
  asm volatile("v_mfma_f32_16x16x32_bf16 %0, v[228:231], a[24:27], %0" : "+v"(g0[2][2]) :  : ); SB();
  asm volatile("v_and_b32 %0, 0xffff0000, %1" : "=v"(yp[2][3]) : "v"(ykv[1]) : ); SB();
  asm volatile("v_lshlrev_b32 %0, 16, %1" : "=v"(yp[3][0]) : "v"(ykv[2]) : ); SB();
  asm volatile("v_mfma_f32_16x16x32_bf16 %0, v[228:231], a[28:31], %0" : "+v"(g0[3][2]) :  : ); SB();
  asm volatile("v_and_b32 %0, 0xffff0000, %1" : "=v"(yp[3][1]) : "v"(ykv[2]) : ); SB();
  asm volatile("v_lshlrev_b32 %0, 16, %1" : "=v"(yp[3][2]) : "v"(ykv[3]) : ); SB();
  ST(26);
  asm volatile("s_waitcnt vmcnt(13)" :  :  : "memory"); SB();
  asm volatile("v_mfma_f32_16x16x32_bf16 %0, v[232:235], a[32:35], %0" : "+v"(g0[0][0]) :  : ); SB();
  asm volatile("v_and_b32 %0, 0xffff0000, %1" : "=v"(yp[3][3]) : "v"(ykv[3]) : ); SB();
  asm volatile("v_fmac_f32 %0, %1, %2" : "+v"(yp[0][0]) : "v"(yp[2][0]), "v"(Dv) : ); SB();
  asm volatile("buffer_load_dwordx4 v[192:195], %0, %1, %2 offen offset:2048 sc1" :  : "v"(vB[0]), "s"(rWS), "s"(589824u) : "memory"); SB();
  asm volatile("v_mfma_f32_16x16x32_bf16 %0, v[232:235], a[36:39], %0" : "+v"(g0[1][0]) :  : ); SB();
  asm volatile("v_fmac_f32 %0, %1, %2" : "+v"(yp[0][1]) : "v"(yp[2][1]), "v"(Dv) : ); SB();
  asm volatile("v_fmac_f32 %0, %1, %2" : "+v"(yp[0][2]) : "v"(yp[2][2]), "v"(Dv) : ); SB();
  asm volatile("v_mfma_f32_16x16x32_bf16 %0, v[232:235], a[40:43], %0" : "+v"(g0[2][0]) :  : ); SB();
  asm volatile("v_fmac_f32 %0, %1, %2" : "+v"(yp[0][3]) : "v"(yp[2][3]), "v"(Dv) : ); SB();
  asm volatile("v_fmac_f32 %0, %1, %2" : "+v"(yp[1][0]) : "v"(yp[3][0]), "v"(Dv) : ); SB();
  asm volatile("v_mfma_f32_16x16x32_bf16 %0, v[232:235], a[44:47], %0" : "+v"(g0[3][0]) :  : ); SB();
  asm volatile("v_fmac_f32 %0, %1, %2" : "+v"(yp[1][1]) : "v"(yp[3][1]), "v"(Dv) : ); SB();
  asm volatile("v_fmac_f32 %0, %1, %2" : "+v"(yp[1][2]) : "v"(yp[3][2]), "v"(Dv) : ); SB();
  asm volatile("v_mfma_f32_16x16x32_bf16 %0, v[236:239], a[32:35], %0" : "+v"(g0[0][1]) :  : ); SB();
  asm volatile("v_fmac_f32 %0, %1, %2" : "+v"(yp[1][3]) : "v"(yp[3][3]), "v"(Dv) : ); SB();
  asm volatile("v_cvt_pk_bf16_f32 %0, %1, %2" : "=v"(yo[0]) : "v"(yp[0][0]), "v"(yp[0][1]) : ); SB();
  asm volatile("s_add_u32 m0, %0, 107648" :  : "s"(mA) : "memory", "scc"); SB();
  asm volatile("v_mfma_f32_16x16x32_bf16 %0, v[236:239], a[36:39], %0" : "+v"(g0[1][1]) :  : ); SB();
  asm volatile("v_cvt_pk_bf16_f32 %0, %1, %2" : "=v"(yo[1]) : "v"(yp[0][2]), "v"(yp[0][3]) : ); SB();
  asm volatile("v_cvt_pk_bf16_f32 %0, %1, %2" : "=v"(yo[2]) : "v"(yp[1][0]), "v"(yp[1][1]) : ); SB();
  asm volatile("buffer_load_dwordx4 %0, %1, %2 offen offset:896 lds" :  : "v"(voS), "s"(rA), "s"((2 ^ (2 * w)) * 32768) : "memory"); SB();
  asm volatile("v_mfma_f32_16x16x32_bf16 %0, v[236:239], a[40:43], %0" : "+v"(g0[2][1]) :  : ); SB();
  asm volatile("v_cvt_pk_bf16_f32 %0, %1, %2" : "=v"(yo[3]) : "v"(yp[1][2]), "v"(yp[1][3]) : ); SB();
  asm volatile("v_mfma_f32_16x16x32_bf16 %0, v[236:239], a[44:47], %0" : "+v"(g0[3][1]) :  : ); SB();
  asm volatile("buffer_store_dwordx4 %1, %0, %2, %3 offen offset:0 sc1\n\ts_nop 1" :  : "v"(vY_), "v"(yo), "s"(rWS), "s"(c2s) : "memory"); SB();
  asm volatile("v_mfma_f32_16x16x32_bf16 %0, v[240:243], a[32:35], %0" : "+v"(g0[0][2]) :  : ); SB();
  asm volatile("v_mul_f32 %0, %1, %1" : "=v"(gt[0]) : "v"(a1[0][0][0]) : ); SB();
  asm volatile("v_mfma_f32_16x16x32_bf16 %0, v[240:243], a[36:39], %0" : "+v"(g0[1][2]) :  : ); SB();
  asm volatile("v_mul_f32 %0, %1, %1" : "=v"(gt[1]) : "v"(a1[0][0][1]) : ); SB();
  asm volatile("v_mul_f32 %0, %1, %1" : "=v"(gt[2]) : "v"(a1[0][0][2]) : ); SB();
  asm volatile("v_mfma_f32_16x16x32_bf16 %0, v[240:243], a[40:43], %0" : "+v"(g0[2][2]) :  : ); SB();
  asm volatile("v_mul_f32 %0, %1, %1" : "=v"(gt[3]) : "v"(a1[0][0][3]) : ); SB();
  asm volatile("v_fma_f32 %0, %0, %1, %2" : "+v"(gt[0]) : "v"(k2[0]), "v"(k1[0]) : ); SB();
  asm volatile("v_mfma_f32_16x16x32_bf16 %0, v[240:243], a[44:47], %0" : "+v"(g0[3][2]) :  : ); SB();
  asm volatile("v_fma_f32 %0, %0, %1, %2" : "+v"(gt[1]) : "v"(k2[0]), "v"(k1[0]) : ); SB();
  asm volatile("v_fma_f32 %0, %0, %1, %2" : "+v"(gt[2]) : "v"(k2[0]), "v"(k1[0]) : ); SB();
  asm volatile("s_waitcnt vmcnt(13)" :  :  : "memory"); SB();
  asm volatile("v_mfma_f32_16x16x32_bf16 %0, v[244:247], a[48:51], %0" : "+v"(g0[0][0]) :  : ); SB();
  asm volatile("v_fma_f32 %0, %0, %1, %2" : "+v"(gt[3]) : "v"(k2[0]), "v"(k1[0]) : ); SB();
  asm volatile("v_mul_f32 %0, %0, %1" : "+v"(gt[0]) : "v"(a1[0][0][0]) : ); SB();
  asm volatile("s_add_u32 m0, %0, 108672" :  : "s"(mA) : "memory", "scc"); SB();
  asm volatile("v_mfma_f32_16x16x32_bf16 %0, v[244:247], a[52:55], %0" : "+v"(g0[1][0]) :  : ); SB();
  asm volatile("v_mul_f32 %0, %0, %1" : "+v"(gt[1]) : "v"(a1[0][0][1]) : ); SB();
  asm volatile("v_mul_f32 %0, %0, %1" : "+v"(gt[2]) : "v"(a1[0][0][2]) : ); SB();
  asm volatile("buffer_load_dwordx4 %0, %1, %2 offen offset:896 lds" :  : "v"(voS), "s"(rA), "s"((3 ^ (2 * w)) * 32768) : "memory"); SB();
  asm volatile("v_mfma_f32_16x16x32_bf16 %0, v[244:247], a[56:59], %0" : "+v"(g0[2][0]) :  : ); SB();
  asm volatile("v_mul_f32 %0, %0, %1" : "+v"(gt[3]) : "v"(a1[0][0][3]) : ); SB();
  asm volatile("v_mfma_f32_16x16x32_bf16 %0, v[244:247], a[60:63], %0" : "+v"(g0[3][0]) :  : ); SB();
  asm volatile("v_exp_f32 %0, %1" : "=v"(ge[0]) : "v"(gt[0]) : ); SB();
  asm volatile("ds_read_b128 a[72:75], %0 offset:12288" :  : "v"(aA[0]) : "memory"); SB();
  asm volatile("v_mfma_f32_16x16x32_bf16 %0, v[248:251], a[48:51], %0" : "+v"(g0[0][1]) :  : ); SB();
  asm volatile("v_exp_f32 %0, %1" : "=v"(ge[1]) : "v"(gt[1]) : ); SB();
  asm volatile("ds_read_b128 a[88:91], %0 offset:12288" :  : "v"(aA[1]) : "memory"); SB();
  asm volatile("v_mfma_f32_16x16x32_bf16 %0, v[248:251], a[52:55], %0" : "+v"(g0[1][1]) :  : ); SB();
  asm volatile("v_exp_f32 %0, %1" : "=v"(ge[2]) : "v"(gt[2]) : ); SB();
  asm volatile("v_mfma_f32_16x16x32_bf16 %0, v[248:251], a[56:59], %0" : "+v"(g0[2][1]) :  : ); SB();
  asm volatile("v_exp_f32 %0, %1" : "=v"(ge[3]) : "v"(gt[3]) : ); SB();
  asm volatile("v_mfma_f32_16x16x32_bf16 %0, v[248:251], a[60:63], %0" : "+v"(g0[3][1]) :  : ); SB();
  asm volatile("v_fma_f32 %0, %0, %1, %1" : "+v"(ge[0]) : "v"(xd[0]) : ); SB();
  asm volatile("v_fma_f32 %0, %0, %1, %1" : "+v"(ge[1]) : "v"(xd[0]) : ); SB();
  asm volatile("v_mfma_f32_16x16x32_bf16 %0, v[252:255], a[48:51], %0" : "+v"(g0[0][2]) :  : ); SB();
  asm volatile("v_fma_f32 %0, %0, %1, %1" : "+v"(ge[2]) : "v"(xd[0]) : ); SB();
  asm volatile("v_fma_f32 %0, %0, %1, %1" : "+v"(ge[3]) : "v"(xd[0]) : ); SB();
  asm volatile("v_mfma_f32_16x16x32_bf16 %0, v[252:255], a[52:55], %0" : "+v"(g0[1][2]) :  : ); SB();
  asm volatile("v_rcp_f32 %0, %1" : "=v"(gt[0]) : "v"(ge[0]) : ); SB();
  asm volatile("v_mfma_f32_16x16x32_bf16 %0, v[252:255], a[56:59], %0" : "+v"(g0[2][2]) :  : ); SB();
  asm volatile("v_rcp_f32 %0, %1" : "=v"(gt[1]) : "v"(ge[1]) : ); SB();
  asm volatile("v_mfma_f32_16x16x32_bf16 %0, v[252:255], a[60:63], %0" : "+v"(g0[3][2]) :  : ); SB();
  asm volatile("v_rcp_f32 %0, %1" : "=v"(gt[2]) : "v"(ge[2]) : ); SB();
  asm volatile("s_waitcnt lgkmcnt(0)" :  :  : "memory"); SB();
  asm volatile("s_add_u32 m0, %0, 109696" :  : "s"(mA) : "memory", "scc"); SB();
  asm volatile("s_nop 0\n\tbuffer_load_dwordx4 %0, %1, %2 offen offset:896 lds" :  : "v"(voS), "s"(rA), "s"((4 ^ (2 * w)) * 32768) : "memory"); SB();
  asm volatile("s_add_u32 m0, %0, 110720" :  : "s"(mA) : "memory", "scc"); SB();
  asm volatile("s_nop 0\n\tbuffer_load_dwordx4 %0, %1, %2 offen offset:896 lds" :  : "v"(voS), "s"(rA), "s"((5 ^ (2 * w)) * 32768) : "memory"); SB();
  asm volatile("ds_read_b128 a[76:79], %0 offset:14336" :  : "v"(aA[0]) : "memory"); SB();
  asm volatile("ds_read_b128 a[92:95], %0 offset:14336" :  : "v"(aA[1]) : "memory"); SB();
  asm volatile("s_waitcnt vmcnt(12)" :  :  : "memory"); SB();
  asm volatile("v_mfma_f32_16x16x32_bf16 %0, v[160:163], a[64:67], %0" : "+v"(g0[0][0]) :  : ); SB();
  asm volatile("v_rcp_f32 %0, %1" : "=v"(gt[3]) : "v"(ge[3]) : ); SB();
  asm volatile("s_waitcnt lgkmcnt(0)" :  :  : "memory"); SB();
  asm volatile("v_mfma_f32_16x16x32_bf16 %0, v[160:163], a[68:71], %0" : "+v"(g0[1][0]) :  : ); SB();
  asm volatile("v_mul_f32 %0, %0, %1" : "+v"(gt[0]) : "v"(a1[0][0][0]) : ); SB();
  asm volatile("v_mul_f32 %0, %0, %1" : "+v"(gt[1]) : "v"(a1[0][0][1]) : ); SB();
  asm volatile("s_add_u32 m0, %0, 111744" :  : "s"(mA) : "memory", "scc"); SB();
  asm volatile("v_mfma_f32_16x16x32_bf16 %0, v[160:163], a[72:75], %0" : "+v"(g0[2][0]) :  : ); SB();
  asm volatile("v_mul_f32 %0, %0, %1" : "+v"(gt[2]) : "v"(a1[0][0][2]) : ); SB();
  asm volatile("v_mul_f32 %0, %0, %1" : "+v"(gt[3]) : "v"(a1[0][0][3]) : ); SB();
  asm volatile("buffer_load_dwordx4 %0, %1, %2 offen offset:896 lds" :  : "v"(voS), "s"(rA), "s"((6 ^ (2 * w)) * 32768) : "memory"); SB();
  asm volatile("v_mfma_f32_16x16x32_bf16 %0, v[160:163], a[76:79], %0" : "+v"(g0[3][0]) :  : ); SB();
  asm volatile("v_cvt_pk_bf16_f32 %0, %1, %2" : "=v"(z1[0][0][0]) : "v"(gt[0]), "v"(gt[1]) : ); SB();
  asm volatile("v_cvt_pk_bf16_f32 %0, %1, %2" : "=v"(z1[0][0][1]) : "v"(gt[2]), "v"(gt[3]) : ); SB();
  asm volatile("s_add_u32 m0, %0, 112768" :  : "s"(mA) : "memory", "scc"); SB();
  asm volatile("v_mfma_f32_16x16x32_bf16 %0, v[164:167], a[64:67], %0" : "+v"(g0[0][1]) :  : ); SB();
  asm volatile("v_mul_f32 %0, %1, %1" : "=v"(gt[0]) : "v"(a1[0][1][0]) : ); SB();
  asm volatile("v_mul_f32 %0, %1, %1" : "=v"(gt[1]) : "v"(a1[0][1][1]) : ); SB();
  asm volatile("buffer_load_dwordx4 %0, %1, %2 offen offset:896 lds" :  : "v"(voS), "s"(rA), "s"((7 ^ (2 * w)) * 32768) : "memory"); SB();
  asm volatile("v_mfma_f32_16x16x32_bf16 %0, v[164:167], a[68:71], %0" : "+v"(g0[1][1]) :  : ); SB();
  asm volatile("v_mul_f32 %0, %1, %1" : "=v"(gt[2]) : "v"(a1[0][1][2]) : ); SB();
  asm volatile("v_mul_f32 %0, %1, %1" : "=v"(gt[3]) : "v"(a1[0][1][3]) : ); SB();
  asm volatile("v_mfma_f32_16x16x32_bf16 %0, v[164:167], a[72:75], %0" : "+v"(g0[2][1]) :  : ); SB();
  asm volatile("v_fma_f32 %0, %0, %1, %2" : "+v"(gt[0]) : "v"(k2[0]), "v"(k1[0]) : ); SB();
  asm volatile("v_fma_f32 %0, %0, %1, %2" : "+v"(gt[1]) : "v"(k2[0]), "v"(k1[0]) : ); SB();
  asm volatile("ds_read_b128 a[96:99], %0 offset:0" :  : "v"(aA[0]) : "memory"); SB();
  asm volatile("v_mfma_f32_16x16x32_bf16 %0, v[164:167], a[76:79], %0" : "+v"(g0[3][1]) :  : ); SB();
  asm volatile("v_fma_f32 %0, %0, %1, %2" : "+v"(gt[2]) : "v"(k2[0]), "v"(k1[0]) : ); SB();
  asm volatile("v_fma_f32 %0, %0, %1, %2" : "+v"(gt[3]) : "v"(k2[0]), "v"(k1[0]) : ); SB();
  asm volatile("ds_read_b128 a[112:115], %0 offset:0" :  : "v"(aA[1]) : "memory"); SB();
  asm volatile("v_mfma_f32_16x16x32_bf16 %0, v[168:171], a[64:67], %0" : "+v"(g0[0][2]) :  : ); SB();
  asm volatile("v_mul_f32 %0, %0, %1" : "+v"(gt[0]) : "v"(a1[0][1][0]) : ); SB();
  asm volatile("v_mul_f32 %0, %0, %1" : "+v"(gt[1]) : "v"(a1[0][1][1]) : ); SB();
  asm volatile("v_mfma_f32_16x16x32_bf16 %0, v[168:171], a[68:71], %0" : "+v"(g0[1][2]) :  : ); SB();
  asm volatile("v_mul_f32 %0, %0, %1" : "+v"(gt[2]) : "v"(a1[0][1][2]) : ); SB();
  asm volatile("v_mul_f32 %0, %0, %1" : "+v"(gt[3]) : "v"(a1[0][1][3]) : ); SB();
  asm volatile("ds_read_b128 a[100:103], %0 offset:2048" :  : "v"(aA[0]) : "memory"); SB();
  asm volatile("v_mfma_f32_16x16x32_bf16 %0, v[168:171], a[72:75], %0" : "+v"(g0[2][2]) :  : ); SB();
  asm volatile("v_exp_f32 %0, %1" : "=v"(ge[0]) : "v"(gt[0]) : ); SB();
  asm volatile("ds_read_b128 a[116:119], %0 offset:2048" :  : "v"(aA[1]) : "memory"); SB();
  asm volatile("v_mfma_f32_16x16x32_bf16 %0, v[168:171], a[76:79], %0" : "+v"(g0[3][2]) :  : ); SB();
  asm volatile("v_exp_f32 %0, %1" : "=v"(ge[1]) : "v"(gt[1]) : ); SB();
  asm volatile("ds_read_b128 a[104:107], %0 offset:4096" :  : "v"(aA[0]) : "memory"); SB();
  asm volatile("ds_read_b128 a[120:123], %0 offset:4096" :  : "v"(aA[1]) : "memory"); SB();
  asm volatile("ds_read_b128 a[108:111], %0 offset:6144" :  : "v"(aA[0]) : "memory"); SB();
  asm volatile("ds_read_b128 a[124:127], %0 offset:6144" :  : "v"(aA[1]) : "memory"); SB();
  asm volatile("ds_read_b128 a[128:131], %0 offset:8192" :  : "v"(aA[0]) : "memory"); SB();
  asm volatile("ds_read_b128 a[144:147], %0 offset:8192" :  : "v"(aA[1]) : "memory"); SB();
  asm volatile("buffer_load_dwordx4 v[196:199], %0, %1, %2 offen offset:0 sc1" :  : "v"(vB[0]), "s"(rWS), "s"(638976u) : "memory"); SB();
  asm volatile("buffer_load_dwordx4 v[200:203], %0, %1, %2 offen offset:1024 sc1" :  : "v"(vB[0]), "s"(rWS), "s"(638976u) : "memory"); SB();
  asm volatile("s_waitcnt vmcnt(12)" :  :  : "memory"); SB();
  asm volatile("v_mfma_f32_16x16x32_bf16 %0, v[172:175], a[80:83], %0" : "+v"(g0[0][0]) :  : ); SB();
  asm volatile("v_exp_f32 %0, %1" : "=v"(ge[2]) : "v"(gt[2]) : ); SB();
  asm volatile("buffer_load_dwordx4 v[204:207], %0, %1, %2 offen offset:2048 sc1" :  : "v"(vB[0]), "s"(rWS), "s"(638976u) : "memory"); SB();
  asm volatile("v_mfma_f32_16x16x32_bf16 %0, v[172:175], a[84:87], %0" : "+v"(g0[1][0]) :  : ); SB();
  asm volatile("v_exp_f32 %0, %1" : "=v"(ge[3]) : "v"(gt[3]) : ); SB();
  asm volatile("buffer_load_dwordx4 v[208:211], %0, %1, %2 offen offset:0 sc1" :  : "v"(vB[0]), "s"(rWS), "s"(688128u) : "memory"); SB();
  asm volatile("v_mfma_f32_16x16x32_bf16 %0, v[172:175], a[88:91], %0" : "+v"(g0[2][0]) :  : ); SB();
  asm volatile("v_fma_f32 %0, %0, %1, %1" : "+v"(ge[0]) : "v"(xd[0]) : ); SB();
  asm volatile("v_fma_f32 %0, %0, %1, %1" : "+v"(ge[1]) : "v"(xd[0]) : ); SB();
  asm volatile("v_mfma_f32_16x16x32_bf16 %0, v[172:175], a[92:95], %0" : "+v"(g0[3][0]) :  : ); SB();
  asm volatile("v_fma_f32 %0, %0, %1, %1" : "+v"(ge[2]) : "v"(xd[0]) : ); SB();
  asm volatile("v_fma_f32 %0, %0, %1, %1" : "+v"(ge[3]) : "v"(xd[0]) : ); SB();
  asm volatile("v_mfma_f32_16x16x32_bf16 %0, v[176:179], a[80:83], %0" : "+v"(g0[0][1]) :  : ); SB();
  asm volatile("v_rcp_f32 %0, %1" : "=v"(gt[0]) : "v"(ge[0]) : ); SB();
  asm volatile("buffer_load_dwordx4 v[212:215], %0, %1, %2 offen offset:1024 sc1" :  : "v"(vB[0]), "s"(rWS), "s"(688128u) : "memory"); SB();
  asm volatile("v_mfma_f32_16x16x32_bf16 %0, v[176:179], a[84:87], %0" : "+v"(g0[1][1]) :  : ); SB();
  asm volatile("v_rcp_f32 %0, %1" : "=v"(gt[1]) : "v"(ge[1]) : ); SB();
  asm volatile("v_mfma_f32_16x16x32_bf16 %0, v[176:179], a[88:91], %0" : "+v"(g0[2][1]) :  : ); SB();
  asm volatile("v_rcp_f32 %0, %1" : "=v"(gt[2]) : "v"(ge[2]) : ); SB();
  asm volatile("v_mfma_f32_16x16x32_bf16 %0, v[176:179], a[92:95], %0" : "+v"(g0[3][1]) :  : ); SB();
  asm volatile("v_rcp_f32 %0, %1" : "=v"(gt[3]) : "v"(ge[3]) : ); SB();
  asm volatile("v_mfma_f32_16x16x32_bf16 %0, v[180:183], a[80:83], %0" : "+v"(g0[0][2]) :  : ); SB();
  asm volatile("v_mul_f32 %0, %0, %1" : "+v"(gt[0]) : "v"(a1[0][1][0]) : ); SB();
  asm volatile("v_mul_f32 %0, %0, %1" : "+v"(gt[1]) : "v"(a1[0][1][1]) : ); SB();
  asm volatile("v_mfma_f32_16x16x32_bf16 %0, v[180:183], a[84:87], %0" : "+v"(g0[1][2]) :  : ); SB();
  asm volatile("v_mul_f32 %0, %0, %1" : "+v"(gt[2]) : "v"(a1[0][1][2]) : ); SB();
  asm volatile("v_mul_f32 %0, %0, %1" : "+v"(gt[3]) : "v"(a1[0][1][3]) : ); SB();
  asm volatile("buffer_load_dwordx4 v[216:219], %0, %1, %2 offen offset:2048 sc1" :  : "v"(vB[0]), "s"(rWS), "s"(688128u) : "memory"); SB();
  asm volatile("v_mfma_f32_16x16x32_bf16 %0, v[180:183], a[88:91], %0" : "+v"(g0[2][2]) :  : ); SB();
  asm volatile("v_cvt_pk_bf16_f32 %0, %1, %2" : "=v"(z1[0][1][0]) : "v"(gt[0]), "v"(gt[1]) : ); SB();
  asm volatile("v_cvt_pk_bf16_f32 %0, %1, %2" : "=v"(z1[0][1][1]) : "v"(gt[2]), "v"(gt[3]) : ); SB();
  asm volatile("v_mfma_f32_16x16x32_bf16 %0, v[180:183], a[92:95], %0" : "+v"(g0[3][2]) :  : ); SB();
  asm volatile("v_mul_f32 %0, %1, %1" : "=v"(gt[0]) : "v"(a1[0][2][0]) : ); SB();
  asm volatile("v_mul_f32 %0, %1, %1" : "=v"(gt[1]) : "v"(a1[0][2][1]) : ); SB();
  asm volatile("s_waitcnt vmcnt(13)" :  :  : "memory"); SB();
  asm volatile("s_waitcnt lgkmcnt(8)\n\tv_mfma_f32_16x16x32_bf16 %0, v[184:187], a[96:99], %0" : "+v"(g0[0][0]) :  : ); SB();
  asm volatile("v_mul_f32 %0, %1, %1" : "=v"(gt[2]) : "v"(a1[0][2][2]) : ); SB();
  asm volatile("v_mul_f32 %0, %1, %1" : "=v"(gt[3]) : "v"(a1[0][2][3]) : ); SB();
  asm volatile("buffer_load_dwordx4 v[220:223], %0, %1, %2 offen offset:0 sc1" :  : "v"(vB[0]), "s"(rWS), "s"(737280u) : "memory"); SB();
  asm volatile("s_waitcnt lgkmcnt(6)\n\tv_mfma_f32_16x16x32_bf16 %0, v[184:187], a[100:103], %0" : "+v"(g0[1][0]) :  : ); SB();
  asm volatile("v_fma_f32 %0, %0, %1, %2" : "+v"(gt[0]) : "v"(k2[0]), "v"(k1[0]) : ); SB();
  asm volatile("v_fma_f32 %0, %0, %1, %2" : "+v"(gt[1]) : "v"(k2[0]), "v"(k1[0]) : ); SB();
  asm volatile("s_waitcnt lgkmcnt(4)\n\tv_mfma_f32_16x16x32_bf16 %0, v[184:187], a[104:107], %0" : "+v"(g0[2][0]) :  : ); SB();
  asm volatile("v_fma_f32 %0, %0, %1, %2" : "+v"(gt[2]) : "v"(k2[0]), "v"(k1[0]) : ); SB();
  asm volatile("v_fma_f32 %0, %0, %1, %2" : "+v"(gt[3]) : "v"(k2[0]), "v"(k1[0]) : ); SB();
  asm volatile("s_waitcnt lgkmcnt(2)\n\tv_mfma_f32_16x16x32_bf16 %0, v[184:187], a[108:111], %0" : "+v"(g0[3][0]) :  : ); SB();
  asm volatile("v_mul_f32 %0, %0, %1" : "+v"(gt[0]) : "v"(a1[0][2][0]) : ); SB();
  asm volatile("v_mul_f32 %0, %0, %1" : "+v"(gt[1]) : "v"(a1[0][2][1]) : ); SB();
  asm volatile("buffer_load_dwordx4 v[224:227], %0, %1, %2 offen offset:1024 sc1" :  : "v"(vB[0]), "s"(rWS), "s"(737280u) : "memory"); SB();
  asm volatile("v_mfma_f32_16x16x32_bf16 %0, v[188:191], a[96:99], %0" : "+v"(g0[0][1]) :  : ); SB();
  asm volatile("v_mul_f32 %0, %0, %1" : "+v"(gt[2]) : "v"(a1[0][2][2]) : ); SB();
  asm volatile("v_mul_f32 %0, %0, %1" : "+v"(gt[3]) : "v"(a1[0][2][3]) : ); SB();
  asm volatile("v_mfma_f32_16x16x32_bf16 %0, v[188:191], a[100:103], %0" : "+v"(g0[1][1]) :  : ); SB();
  asm volatile("v_exp_f32 %0, %1" : "=v"(ge[0]) : "v"(gt[0]) : ); SB();
  asm volatile("v_mfma_f32_16x16x32_bf16 %0, v[188:191], a[104:107], %0" : "+v"(g0[2][1]) :  : ); SB();
  asm volatile("v_exp_f32 %0, %1" : "=v"(ge[1]) : "v"(gt[1]) : ); SB();
  asm volatile("v_mfma_f32_16x16x32_bf16 %0, v[188:191], a[108:111], %0" : "+v"(g0[3][1]) :  : ); SB();
  asm volatile("v_exp_f32 %0, %1" : "=v"(ge[2]) : "v"(gt[2]) : ); SB();
  asm volatile("v_mfma_f32_16x16x32_bf16 %0, v[192:195], a[96:99], %0" : "+v"(g0[0][2]) :  : ); SB();
  asm volatile("v_exp_f32 %0, %1" : "=v"(ge[3]) : "v"(gt[3]) : ); SB();
  asm volatile("v_mfma_f32_16x16x32_bf16 %0, v[192:195], a[100:103], %0" : "+v"(g0[1][2]) :  : ); SB();
  asm volatile("v_fma_f32 %0, %0, %1, %1" : "+v"(ge[0]) : "v"(xd[0]) : ); SB();
  asm volatile("v_fma_f32 %0, %0, %1, %1" : "+v"(ge[1]) : "v"(xd[0]) : ); SB();
  asm volatile("v_mfma_f32_16x16x32_bf16 %0, v[192:195], a[104:107], %0" : "+v"(g0[2][2]) :  : ); SB();
  asm volatile("v_fma_f32 %0, %0, %1, %1" : "+v"(ge[2]) : "v"(xd[0]) : ); SB();
  asm volatile("v_fma_f32 %0, %0, %1, %1" : "+v"(ge[3]) : "v"(xd[0]) : ); SB();
  asm volatile("v_mfma_f32_16x16x32_bf16 %0, v[192:195], a[108:111], %0" : "+v"(g0[3][2]) :  : ); SB();
  asm volatile("v_rcp_f32 %0, %1" : "=v"(gt[0]) : "v"(ge[0]) : ); SB();
  asm volatile("s_waitcnt vmcnt(13)\n\ts_mov_b64 exec, 1\n\tbuffer_store_dwordx2 %0, off, %1, %2 sc1\n\ts_mov_b64 exec, -1" :  : "v"(((u32x2){tg0, tg1})), "s"(rWS), "s"(W_FY + ((cb * 4 + w) * 32 + rb) * 8) : "memory"); SB();
  asm volatile("s_waitcnt vmcnt(13)" :  :  : "memory"); SB();
  asm volatile("ds_read_b128 a[132:135], %0 offset:10240" :  : "v"(aA[0]) : "memory"); SB();
  asm volatile("ds_read_b128 a[148:151], %0 offset:10240" :  : "v"(aA[1]) : "memory"); SB();
  asm volatile("buffer_load_dwordx4 v[228:231], %0, %1, %2 offen offset:2048 sc1" :  : "v"(vB[0]), "s"(rWS), "s"(737280u) : "memory"); SB();
  asm volatile("buffer_load_dwordx4 v[232:235], %0, %1, %2 offen offset:0 sc1" :  : "v"(vB[1]), "s"(rWS), "s"(0u) : "memory"); SB();
  asm volatile("s_waitcnt vmcnt(13)" :  :  : "memory"); SB();
  asm volatile("ds_read_b128 a[136:139], %0 offset:12288" :  : "v"(aA[0]) : "memory"); SB();
  asm volatile("ds_read_b128 a[152:155], %0 offset:12288" :  : "v"(aA[1]) : "memory"); SB();
  asm volatile("buffer_load_dwordx4 v[236:239], %0, %1, %2 offen offset:1024 sc1" :  : "v"(vB[1]), "s"(rWS), "s"(0u) : "memory"); SB();
  asm volatile("buffer_load_dwordx4 v[240:243], %0, %1, %2 offen offset:2048 sc1" :  : "v"(vB[1]), "s"(rWS), "s"(0u) : "memory"); SB();
  asm volatile("s_waitcnt vmcnt(13)" :  :  : "memory"); SB();
  asm volatile("ds_read_b128 a[140:143], %0 offset:14336" :  : "v"(aA[0]) : "memory"); SB();
  asm volatile("ds_read_b128 a[156:159], %0 offset:14336" :  : "v"(aA[1]) : "memory"); SB();
  asm volatile("buffer_load_dwordx4 v[244:247], %0, %1, %2 offen offset:0 sc1" :  : "v"(vB[1]), "s"(rWS), "s"(49152u) : "memory"); SB();
  asm volatile("buffer_load_dwordx4 v[248:251], %0, %1, %2 offen offset:1024 sc1" :  : "v"(vB[1]), "s"(rWS), "s"(49152u) : "memory"); SB();
  asm volatile("s_waitcnt vmcnt(12)" :  :  : "memory"); SB();
  asm volatile("v_mfma_f32_16x16x32_bf16 %0, v[196:199], a[112:115], %0" : "+v"(g0[0][0]) :  : ); SB();
  asm volatile("v_rcp_f32 %0, %1" : "=v"(gt[1]) : "v"(ge[1]) : ); SB();
  asm volatile("buffer_load_dwordx4 v[252:255], %0, %1, %2 offen offset:2048 sc1" :  : "v"(vB[1]), "s"(rWS), "s"(49152u) : "memory"); SB();
  asm volatile("v_mfma_f32_16x16x32_bf16 %0, v[196:199], a[116:119], %0" : "+v"(g0[1][0]) :  : ); SB();
  asm volatile("v_rcp_f32 %0, %1" : "=v"(gt[2]) : "v"(ge[2]) : ); SB();
  asm volatile("buffer_load_dwordx4 v[160:163], %0, %1, %2 offen offset:0 sc1" :  : "v"(vB[1]), "s"(rWS), "s"(98304u) : "memory"); SB();
  asm volatile("v_mfma_f32_16x16x32_bf16 %0, v[196:199], a[120:123], %0" : "+v"(g0[2][0]) :  : ); SB();
  asm volatile("v_rcp_f32 %0, %1" : "=v"(gt[3]) : "v"(ge[3]) : ); SB();
  asm volatile("v_mfma_f32_16x16x32_bf16 %0, v[196:199], a[124:127], %0" : "+v"(g0[3][0]) :  : ); SB();
  asm volatile("v_mul_f32 %0, %0, %1" : "+v"(gt[0]) : "v"(a1[0][2][0]) : ); SB();
  asm volatile("v_mul_f32 %0, %0, %1" : "+v"(gt[1]) : "v"(a1[0][2][1]) : ); SB();
  asm volatile("v_mfma_f32_16x16x32_bf16 %0, v[200:203], a[112:115], %0" : "+v"(g0[0][1]) :  : ); SB();
  asm volatile("v_mul_f32 %0, %0, %1" : "+v"(gt[2]) : "v"(a1[0][2][2]) : ); SB();
  asm volatile("v_mul_f32 %0, %0, %1" : "+v"(gt[3]) : "v"(a1[0][2][3]) : ); SB();
  asm volatile("buffer_load_dwordx4 v[164:167], %0, %1, %2 offen offset:1024 sc1" :  : "v"(vB[1]), "s"(rWS), "s"(98304u) : "memory"); SB();
  asm volatile("v_mfma_f32_16x16x32_bf16 %0, v[200:203], a[116:119], %0" : "+v"(g0[1][1]) :  : ); SB();
  asm volatile("v_cvt_pk_bf16_f32 %0, %1, %2" : "=v"(z1[0][2][0]) : "v"(gt[0]), "v"(gt[1]) : ); SB();
  asm volatile("v_cvt_pk_bf16_f32 %0, %1, %2" : "=v"(z1[0][2][1]) : "v"(gt[2]), "v"(gt[3]) : ); SB();
  asm volatile("v_mfma_f32_16x16x32_bf16 %0, v[200:203], a[120:123], %0" : "+v"(g0[2][1]) :  : ); SB();
  asm volatile("v_mul_f32 %0, %1, %1" : "=v"(gt[0]) : "v"(a1[1][0][0]) : ); SB();
  asm volatile("v_mul_f32 %0, %1, %1" : "=v"(gt[1]) : "v"(a1[1][0][1]) : ); SB();
  asm volatile("v_mfma_f32_16x16x32_bf16 %0, v[200:203], a[124:127], %0" : "+v"(g0[3][1]) :  : ); SB();
  asm volatile("v_mul_f32 %0, %1, %1" : "=v"(gt[2]) : "v"(a1[1][0][2]) : ); SB();
  asm volatile("v_mul_f32 %0, %1, %1" : "=v"(gt[3]) : "v"(a1[1][0][3]) : ); SB();
  asm volatile("v_mfma_f32_16x16x32_bf16 %0, v[204:207], a[112:115], %0" : "+v"(g0[0][2]) :  : ); SB();
  asm volatile("v_fma_f32 %0, %0, %1, %2" : "+v"(gt[0]) : "v"(k2[1]), "v"(k1[1]) : ); SB();
  asm volatile("v_fma_f32 %0, %0, %1, %2" : "+v"(gt[1]) : "v"(k2[1]), "v"(k1[1]) : ); SB();
  asm volatile("v_mfma_f32_16x16x32_bf16 %0, v[204:207], a[116:119], %0" : "+v"(g0[1][2]) :  : ); SB();
  asm volatile("v_fma_f32 %0, %0, %1, %2" : "+v"(gt[2]) : "v"(k2[1]), "v"(k1[1]) : ); SB();
  asm volatile("v_fma_f32 %0, %0, %1, %2" : "+v"(gt[3]) : "v"(k2[1]), "v"(k1[1]) : ); SB();
  asm volatile("buffer_load_dwordx4 v[168:171], %0, %1, %2 offen offset:2048 sc1" :  : "v"(vB[1]), "s"(rWS), "s"(98304u) : "memory"); SB();
  asm volatile("v_mfma_f32_16x16x32_bf16 %0, v[204:207], a[120:123], %0" : "+v"(g0[2][2]) :  : ); SB();
  asm volatile("v_mul_f32 %0, %0, %1" : "+v"(gt[0]) : "v"(a1[1][0][0]) : ); SB();
  asm volatile("v_mul_f32 %0, %0, %1" : "+v"(gt[1]) : "v"(a1[1][0][1]) : ); SB();
  asm volatile("v_mfma_f32_16x16x32_bf16 %0, v[204:207], a[124:127], %0" : "+v"(g0[3][2]) :  : ); SB();
  asm volatile("v_mul_f32 %0, %0, %1" : "+v"(gt[2]) : "v"(a1[1][0][2]) : ); SB();
  asm volatile("v_mul_f32 %0, %0, %1" : "+v"(gt[3]) : "v"(a1[1][0][3]) : ); SB();
  asm volatile("s_waitcnt vmcnt(13)" :  :  : "memory"); SB();
  asm volatile("s_waitcnt lgkmcnt(6)\n\tv_mfma_f32_16x16x32_bf16 %0, v[208:211], a[128:131], %0" : "+v"(g0[0][0]) :  : ); SB();
  asm volatile("v_exp_f32 %0, %1" : "=v"(ge[0]) : "v"(gt[0]) : ); SB();
  asm volatile("buffer_load_dwordx4 v[172:175], %0, %1, %2 offen offset:0 sc1" :  : "v"(vB[1]), "s"(rWS), "s"(147456u) : "memory"); SB();
  asm volatile("s_waitcnt lgkmcnt(4)\n\tv_mfma_f32_16x16x32_bf16 %0, v[208:211], a[132:135], %0" : "+v"(g0[1][0]) :  : ); SB();
  asm volatile("v_exp_f32 %0, %1" : "=v"(ge[1]) : "v"(gt[1]) : ); SB();
  asm volatile("s_waitcnt lgkmcnt(2)\n\tv_mfma_f32_16x16x32_bf16 %0, v[208:211], a[136:139], %0" : "+v"(g0[2][0]) :  : ); SB();
  asm volatile("v_exp_f32 %0, %1" : "=v"(ge[2]) : "v"(gt[2]) : ); SB();
  asm volatile("s_waitcnt lgkmcnt(0)\n\tv_mfma_f32_16x16x32_bf16 %0, v[208:211], a[140:143], %0" : "+v"(g0[3][0]) :  : ); SB();
  asm volatile("v_exp_f32 %0, %1" : "=v"(ge[3]) : "v"(gt[3]) : ); SB();
  asm volatile("v_mfma_f32_16x16x32_bf16 %0, v[212:215], a[128:131], %0" : "+v"(g0[0][1]) :  : ); SB();
  asm volatile("v_fma_f32 %0, %0, %1, %1" : "+v"(ge[0]) : "v"(xd[1]) : ); SB();
  asm volatile("v_fma_f32 %0, %0, %1, %1" : "+v"(ge[1]) : "v"(xd[1]) : ); SB();
  asm volatile("buffer_load_dwordx4 v[176:179], %0, %1, %2 offen offset:1024 sc1" :  : "v"(vB[1]), "s"(rWS), "s"(147456u) : "memory"); SB();
  asm volatile("v_mfma_f32_16x16x32_bf16 %0, v[212:215], a[132:135], %0" : "+v"(g0[1][1]) :  : ); SB();
  asm volatile("v_fma_f32 %0, %0, %1, %1" : "+v"(ge[2]) : "v"(xd[1]) : ); SB();
  asm volatile("v_fma_f32 %0, %0, %1, %1" : "+v"(ge[3]) : "v"(xd[1]) : ); SB();
  asm volatile("v_mfma_f32_16x16x32_bf16 %0, v[212:215], a[136:139], %0" : "+v"(g0[2][1]) :  : ); SB();
  asm volatile("v_rcp_f32 %0, %1" : "=v"(gt[0]) : "v"(ge[0]) : ); SB();
  asm volatile("v_mfma_f32_16x16x32_bf16 %0, v[212:215], a[140:143], %0" : "+v"(g0[3][1]) :  : ); SB();
  asm volatile("v_rcp_f32 %0, %1" : "=v"(gt[1]) : "v"(ge[1]) : ); SB();
  asm volatile("v_mfma_f32_16x16x32_bf16 %0, v[216:219], a[128:131], %0" : "+v"(g0[0][2]) :  : ); SB();
  asm volatile("v_rcp_f32 %0, %1" : "=v"(gt[2]) : "v"(ge[2]) : ); SB();
  asm volatile("v_mfma_f32_16x16x32_bf16 %0, v[216:219], a[132:135], %0" : "+v"(g0[1][2]) :  : ); SB();
  asm volatile("v_rcp_f32 %0, %1" : "=v"(gt[3]) : "v"(ge[3]) : ); SB();
  asm volatile("buffer_load_dwordx4 v[180:183], %0, %1, %2 offen offset:2048 sc1" :  : "v"(vB[1]), "s"(rWS), "s"(147456u) : "memory"); SB();
  asm volatile("v_mfma_f32_16x16x32_bf16 %0, v[216:219], a[136:139], %0" : "+v"(g0[2][2]) :  : ); SB();
  asm volatile("v_mul_f32 %0, %0, %1" : "+v"(gt[0]) : "v"(a1[1][0][0]) : ); SB();
  asm volatile("v_mul_f32 %0, %0, %1" : "+v"(gt[1]) : "v"(a1[1][0][1]) : ); SB();
  asm volatile("v_mfma_f32_16x16x32_bf16 %0, v[216:219], a[140:143], %0" : "+v"(g0[3][2]) :  : ); SB();
  asm volatile("v_mul_f32 %0, %0, %1" : "+v"(gt[2]) : "v"(a1[1][0][2]) : ); SB();
  asm volatile("v_mul_f32 %0, %0, %1" : "+v"(gt[3]) : "v"(a1[1][0][3]) : ); SB();
  asm volatile("s_waitcnt vmcnt(12)" :  :  : "memory"); SB();
  asm volatile("v_mfma_f32_16x16x32_bf16 %0, v[220:223], a[144:147], %0" : "+v"(g0[0][0]) :  : ); SB();
  asm volatile("v_cvt_pk_bf16_f32 %0, %1, %2" : "=v"(z1[1][0][0]) : "v"(gt[0]), "v"(gt[1]) : ); SB();
  asm volatile("v_cvt_pk_bf16_f32 %0, %1, %2" : "=v"(z1[1][0][1]) : "v"(gt[2]), "v"(gt[3]) : ); SB();
  asm volatile("buffer_load_dwordx4 v[184:187], %0, %1, %2 offen offset:0 sc1" :  : "v"(vB[1]), "s"(rWS), "s"(196608u) : "memory"); SB();
  asm volatile("v_mfma_f32_16x16x32_bf16 %0, v[220:223], a[148:151], %0" : "+v"(g0[1][0]) :  : ); SB();
  asm volatile("v_mul_f32 %0, %1, %1" : "=v"(gt[0]) : "v"(a1[1][1][0]) : ); SB();
  asm volatile("v_mul_f32 %0, %1, %1" : "=v"(gt[1]) : "v"(a1[1][1][1]) : ); SB();
  asm volatile("buffer_load_dwordx4 v[188:191], %0, %1, %2 offen offset:1024 sc1" :  : "v"(vB[1]), "s"(rWS), "s"(196608u) : "memory"); SB();
  asm volatile("v_mfma_f32_16x16x32_bf16 %0, v[220:223], a[152:155], %0" : "+v"(g0[2][0]) :  : ); SB();
  asm volatile("v_mul_f32 %0, %1, %1" : "=v"(gt[2]) : "v"(a1[1][1][2]) : ); SB();
  asm volatile("v_mul_f32 %0, %1, %1" : "=v"(gt[3]) : "v"(a1[1][1][3]) : ); SB();
  asm volatile("v_mfma_f32_16x16x32_bf16 %0, v[220:223], a[156:159], %0" : "+v"(g0[3][0]) :  : ); SB();
  asm volatile("v_fma_f32 %0, %0, %1, %2" : "+v"(gt[0]) : "v"(k2[1]), "v"(k1[1]) : ); SB();
  asm volatile("v_fma_f32 %0, %0, %1, %2" : "+v"(gt[1]) : "v"(k2[1]), "v"(k1[1]) : ); SB();
  asm volatile("v_mfma_f32_16x16x32_bf16 %0, v[224:227], a[144:147], %0" : "+v"(g0[0][1]) :  : ); SB();
  asm volatile("v_fma_f32 %0, %0, %1, %2" : "+v"(gt[2]) : "v"(k2[1]), "v"(k1[1]) : ); SB();
  asm volatile("v_fma_f32 %0, %0, %1, %2" : "+v"(gt[3]) : "v"(k2[1]), "v"(k1[1]) : ); SB();
  asm volatile("buffer_load_dwordx4 v[192:195], %0, %1, %2 offen offset:2048 sc1" :  : "v"(vB[1]), "s"(rWS), "s"(196608u) : "memory"); SB();
  asm volatile("v_mfma_f32_16x16x32_bf16 %0, v[224:227], a[148:151], %0" : "+v"(g0[1][1]) :  : ); SB();
  asm volatile("v_mul_f32 %0, %0, %1" : "+v"(gt[0]) : "v"(a1[1][1][0]) : ); SB();
  asm volatile("v_mul_f32 %0, %0, %1" : "+v"(gt[1]) : "v"(a1[1][1][1]) : ); SB();
  asm volatile("v_mfma_f32_16x16x32_bf16 %0, v[224:227], a[152:155], %0" : "+v"(g0[2][1]) :  : ); SB();
  asm volatile("v_mul_f32 %0, %0, %1" : "+v"(gt[2]) : "v"(a1[1][1][2]) : ); SB();
  asm volatile("v_mul_f32 %0, %0, %1" : "+v"(gt[3]) : "v"(a1[1][1][3]) : ); SB();
  asm volatile("v_mfma_f32_16x16x32_bf16 %0, v[224:227], a[156:159], %0" : "+v"(g0[3][1]) :  : ); SB();
  asm volatile("v_exp_f32 %0, %1" : "=v"(ge[0]) : "v"(gt[0]) : ); SB();
  asm volatile("v_mfma_f32_16x16x32_bf16 %0, v[228:231], a[144:147], %0" : "+v"(g0[0][2]) :  : ); SB();
  asm volatile("v_exp_f32 %0, %1" : "=v"(ge[1]) : "v"(gt[1]) : ); SB();
  asm volatile("v_mfma_f32_16x16x32_bf16 %0, v[228:231], a[148:151], %0" : "+v"(g0[1][2]) :  : ); SB();
  asm volatile("v_exp_f32 %0, %1" : "=v"(ge[2]) : "v"(gt[2]) : ); SB();
  asm volatile("buffer_load_dwordx4 v[196:199], %0, %1, %2 offen offset:0 sc1" :  : "v"(vB[1]), "s"(rWS), "s"(245760u) : "memory"); SB();
  asm volatile("v_mfma_f32_16x16x32_bf16 %0, v[228:231], a[152:155], %0" : "+v"(g0[2][2]) :  : ); SB();
  asm volatile("v_exp_f32 %0, %1" : "=v"(ge[3]) : "v"(gt[3]) : ); SB();
  asm volatile("v_mfma_f32_16x16x32_bf16 %0, v[228:231], a[156:159], %0" : "+v"(g0[3][2]) :  : ); SB();
  asm volatile("v_fma_f32 %0, %0, %1, %1" : "+v"(ge[0]) : "v"(xd[1]) : ); SB();
  asm volatile("v_fma_f32 %0, %0, %1, %1" : "+v"(ge[1]) : "v"(xd[1]) : ); SB();
  ST(27);
  { unsigned lane = lane0, w = w0, rb = rb0, cb = cb0;
  asm volatile("" : "+v"(lane)); asm volatile("" : "+s"(w), "+s"(rb), "+s"(cb)); SB();
  const unsigned wm = w >> 1, wn = w & 1, g4 = lane >> 4, l15 = lane & 15, q4 = l15 >> 2, p4 = l15 & 3;
  const unsigned nch = 3 - (rb >> 4), ch0 = cb * 80 + rb * 2 + min(rb, 16u);
  const unsigned l16 = lane * 16;
  const unsigned vZ = W_Z2 + ((rb * 2 + wm) * 48 + cb * 6 + wn * 3) * 1024 + l16;
  const unsigned vH4 = W_HH + ((rb * 4 + wm * 2) * 24 + cb * 3 + wn * 2) * 1024 + l16, vH2 = W_HH + ((rb * 4 + w) * 24 + cb * 3 + 1) * 1024 + l16;
  const unsigned vY = W_YY + ch0 * 4096 + w * 1024 + l16;
  const unsigned c2o = (nch - 2) * 2, c2s = 8192u + ((rb >> 4) << 30), w3s = (w + 1) >> 2 << 30;
  (void)wm; (void)wn; (void)g4; (void)q4; (void)p4; (void)vZ; (void)vH4; (void)vH2; (void)vY; (void)c2o; (void)c2s; (void)w3s;
  const unsigned pw2 = lb + 122880 + w * 6144 + l16;
  const unsigned pr1 = lb + 98304 + (w ^ 1) * 16384 + l16, pr3 = lb + 98304 + (w ^ 3) * 16384 + 6144 + l16, pr2 = lb + 122880 + (w ^ 2) * 6144 + l16;
  const unsigned vO = W_WO + (cb * 120 + w * 30) * 1024 + l16, mO = lb + w * 30720;
  const unsigned vFY = W_FY + cb * 1024 + (rb >> 3) * 256 + (lane & 31) * 8;
  unsigned bs[2], vtmp;
  const unsigned vYr = W_YY + (cb * 80 + w * 24 + (lane >> 3)) * 4096 + rb * 128 + (((lane & 7) ^ ((lane >> 3) & 6)) << 4), mY = lb + w * 3072;
  SB();
  asm volatile("s_waitcnt vmcnt(13)" :  :  : "memory"); SB();
  asm volatile("v_mfma_f32_16x16x32_bf16 %0, v[232:235], a[160:163], 0" : "=&v"(g1[0][0]) :  : ); SB();
  asm volatile("v_fma_f32 %0, %0, %1, %1" : "+v"(ge[2]) : "v"(xd[1]) : ); SB();
  asm volatile("v_fma_f32 %0, %0, %1, %1" : "+v"(ge[3]) : "v"(xd[1]) : ); SB();
  asm volatile("global_load_ushort %0, %1, %2" : "=&v"(bs[0]) : "v"((rb * 64 + (w ^ 0) * 16 + l15) * 2), "s"(b_sp) : "memory"); SB();
  asm volatile("v_mfma_f32_16x16x32_bf16 %0, v[232:235], a[164:167], 0" : "=&v"(g1[1][0]) :  : ); SB();
  asm volatile("v_rcp_f32 %0, %1" : "=v"(gt[0]) : "v"(ge[0]) : ); SB();
  asm volatile("v_mfma_f32_16x16x32_bf16 %0, v[232:235], a[168:171], 0" : "=&v"(g1[2][0]) :  : ); SB();
  asm volatile("ds_write_b128 %0, %1 offset:0" :  : "v"(pw1), "v"(g0[1][0]) : "memory"); SB();
  asm volatile("global_load_ushort %0, %1, %2" : "=&v"(bs[1]) : "v"((rb * 64 + (w ^ 1) * 16 + l15) * 2), "s"(b_sp) : "memory"); SB();
  asm volatile("v_mfma_f32_16x16x32_bf16 %0, v[232:235], a[172:175], 0" : "=&v"(g1[3][0]) :  : ); SB();
  asm volatile("v_rcp_f32 %0, %1" : "=v"(gt[1]) : "v"(ge[1]) : ); SB();
  asm volatile("v_mfma_f32_16x16x32_bf16 %0, v[236:239], a[160:163], 0" : "=&v"(g1[0][1]) :  : ); SB();
  asm volatile("v_rcp_f32 %0, %1" : "=v"(gt[2]) : "v"(ge[2]) : ); SB();
  asm volatile("v_mfma_f32_16x16x32_bf16 %0, v[236:239], a[164:167], 0" : "=&v"(g1[1][1]) :  : ); SB();
  asm volatile("ds_write_b128 %0, %1 offset:1024" :  : "v"(pw1), "v"(g0[1][1]) : "memory"); SB();
  asm volatile("buffer_load_dwordx4 v[200:203], %0, %1, %2 offen offset:1024 sc1" :  : "v"(vB[1]), "s"(rWS), "s"(245760u) : "memory"); SB();
  asm volatile("v_mfma_f32_16x16x32_bf16 %0, v[236:239], a[168:171], 0" : "=&v"(g1[2][1]) :  : ); SB();
  asm volatile("v_rcp_f32 %0, %1" : "=v"(gt[3]) : "v"(ge[3]) : ); SB();
  asm volatile("v_mfma_f32_16x16x32_bf16 %0, v[236:239], a[172:175], 0" : "=&v"(g1[3][1]) :  : ); SB();
  asm volatile("v_mul_f32 %0, %0, %1" : "+v"(gt[0]) : "v"(a1[1][1][0]) : ); SB();
  asm volatile("v_mul_f32 %0, %0, %1" : "+v"(gt[1]) : "v"(a1[1][1][1]) : ); SB();
  asm volatile("v_mfma_f32_16x16x32_bf16 %0, v[240:243], a[160:163], 0" : "=&v"(g1[0][2]) :  : ); SB();
  asm volatile("ds_write_b128 %0, %1 offset:2048" :  : "v"(pw1), "v"(g0[1][2]) : "memory"); SB();
  asm volatile("buffer_load_dwordx4 v[204:207], %0, %1, %2 offen offset:2048 sc1" :  : "v"(vB[1]), "s"(rWS), "s"(245760u) : "memory"); SB();
  asm volatile("v_mfma_f32_16x16x32_bf16 %0, v[240:243], a[164:167], 0" : "=&v"(g1[1][2]) :  : ); SB();
  asm volatile("v_mul_f32 %0, %0, %1" : "+v"(gt[2]) : "v"(a1[1][1][2]) : ); SB();
  asm volatile("v_mul_f32 %0, %0, %1" : "+v"(gt[3]) : "v"(a1[1][1][3]) : ); SB();
  asm volatile("v_mfma_f32_16x16x32_bf16 %0, v[240:243], a[168:171], 0" : "=&v"(g1[2][2]) :  : ); SB();
  asm volatile("v_cvt_pk_bf16_f32 %0, %1, %2" : "=v"(z1[1][1][0]) : "v"(gt[0]), "v"(gt[1]) : ); SB();
  asm volatile("v_cvt_pk_bf16_f32 %0, %1, %2" : "=v"(z1[1][1][1]) : "v"(gt[2]), "v"(gt[3]) : ); SB();
  asm volatile("v_mfma_f32_16x16x32_bf16 %0, v[240:243], a[172:175], 0" : "=&v"(g1[3][2]) :  : ); SB();
  asm volatile("ds_write_b128 %0, %1 offset:3072" :  : "v"(pw1), "v"(g0[0][0]) : "memory"); SB();
  asm volatile("s_waitcnt vmcnt(14)" :  :  : "memory"); SB();
  asm volatile("v_mfma_f32_16x16x32_bf16 %0, v[244:247], a[176:179], %0" : "+v"(g1[0][0]) :  : ); SB();
  asm volatile("v_mul_f32 %0, %1, %1" : "=v"(gt[0]) : "v"(a1[1][2][0]) : ); SB();
  asm volatile("v_mul_f32 %0, %1, %1" : "=v"(gt[1]) : "v"(a1[1][2][1]) : ); SB();
  asm volatile("buffer_load_dwordx4 v[208:211], %0, %1, %2 offen offset:0 sc1" :  : "v"(vB[1]), "s"(rWS), "s"(294912u) : "memory"); SB();
  asm volatile("v_mfma_f32_16x16x32_bf16 %0, v[244:247], a[180:183], %0" : "+v"(g1[1][0]) :  : ); SB();
  asm volatile("v_mul_f32 %0, %1, %1" : "=v"(gt[2]) : "v"(a1[1][2][2]) : ); SB();
  asm volatile("v_mul_f32 %0, %1, %1" : "=v"(gt[3]) : "v"(a1[1][2][3]) : ); SB();
  asm volatile("v_mfma_f32_16x16x32_bf16 %0, v[244:247], a[184:187], %0" : "+v"(g1[2][0]) :  : ); SB();
  asm volatile("ds_write_b128 %0, %1 offset:4096" :  : "v"(pw1), "v"(g0[0][1]) : "memory"); SB();
  asm volatile("v_mfma_f32_16x16x32_bf16 %0, v[244:247], a[188:191], %0" : "+v"(g1[3][0]) :  : ); SB();
  asm volatile("v_fma_f32 %0, %0, %1, %2" : "+v"(gt[0]) : "v"(k2[1]), "v"(k1[1]) : ); SB();
  asm volatile("v_fma_f32 %0, %0, %1, %2" : "+v"(gt[1]) : "v"(k2[1]), "v"(k1[1]) : ); SB();
  asm volatile("v_mfma_f32_16x16x32_bf16 %0, v[248:251], a[176:179], %0" : "+v"(g1[0][1]) :  : ); SB();
  asm volatile("v_fma_f32 %0, %0, %1, %2" : "+v"(gt[2]) : "v"(k2[1]), "v"(k1[1]) : ); SB();
  asm volatile("v_fma_f32 %0, %0, %1, %2" : "+v"(gt[3]) : "v"(k2[1]), "v"(k1[1]) : ); SB();
  asm volatile("buffer_load_dwordx4 v[212:215], %0, %1, %2 offen offset:1024 sc1" :  : "v"(vB[1]), "s"(rWS), "s"(294912u) : "memory"); SB();
  asm volatile("v_mfma_f32_16x16x32_bf16 %0, v[248:251], a[180:183], %0" : "+v"(g1[1][1]) :  : ); SB();
  asm volatile("ds_write_b128 %0, %1 offset:5120" :  : "v"(pw1), "v"(g0[0][2]) : "memory"); SB();
  asm volatile("v_mfma_f32_16x16x32_bf16 %0, v[248:251], a[184:187], %0" : "+v"(g1[2][1]) :  : ); SB();
  asm volatile("v_mul_f32 %0, %0, %1" : "+v"(gt[0]) : "v"(a1[1][2][0]) : ); SB();
  asm volatile("v_mul_f32 %0, %0, %1" : "+v"(gt[1]) : "v"(a1[1][2][1]) : ); SB();
  asm volatile("v_mfma_f32_16x16x32_bf16 %0, v[248:251], a[188:191], %0" : "+v"(g1[3][1]) :  : ); SB();
  asm volatile("v_mul_f32 %0, %0, %1" : "+v"(gt[2]) : "v"(a1[1][2][2]) : ); SB();
  asm volatile("v_mul_f32 %0, %0, %1" : "+v"(gt[3]) : "v"(a1[1][2][3]) : ); SB();
  asm volatile("buffer_load_dwordx4 v[216:219], %0, %1, %2 offen offset:2048 sc1" :  : "v"(vB[1]), "s"(rWS), "s"(294912u) : "memory"); SB();
  asm volatile("v_mfma_f32_16x16x32_bf16 %0, v[252:255], a[176:179], %0" : "+v"(g1[0][2]) :  : ); SB();
  asm volatile("ds_write_b128 %0, %1 offset:6144" :  : "v"(pw1), "v"(g0[3][0]) : "memory"); SB();
  asm volatile("v_mfma_f32_16x16x32_bf16 %0, v[252:255], a[180:183], %0" : "+v"(g1[1][2]) :  : ); SB();
  asm volatile("v_exp_f32 %0, %1" : "=v"(ge[0]) : "v"(gt[0]) : ); SB();
  asm volatile("v_mfma_f32_16x16x32_bf16 %0, v[252:255], a[184:187], %0" : "+v"(g1[2][2]) :  : ); SB();
  asm volatile("v_exp_f32 %0, %1" : "=v"(ge[1]) : "v"(gt[1]) : ); SB();
  asm volatile("v_mfma_f32_16x16x32_bf16 %0, v[252:255], a[188:191], %0" : "+v"(g1[3][2]) :  : ); SB();
  asm volatile("ds_write_b128 %0, %1 offset:7168" :  : "v"(pw1), "v"(g0[3][1]) : "memory"); SB();
  asm volatile("buffer_load_dwordx4 v[220:223], %0, %1, %2 offen offset:0 sc1" :  : "v"(vB[1]), "s"(rWS), "s"(344064u) : "memory"); SB();
  asm volatile("s_waitcnt vmcnt(15)" :  :  : "memory"); SB();
  asm volatile("v_mfma_f32_16x16x32_bf16 %0, v[160:163], a[192:195], %0" : "+v"(g1[0][0]) :  : ); SB();
  asm volatile("v_exp_f32 %0, %1" : "=v"(ge[2]) : "v"(gt[2]) : ); SB();
  asm volatile("v_mfma_f32_16x16x32_bf16 %0, v[160:163], a[196:199], %0" : "+v"(g1[1][0]) :  : ); SB();
  asm volatile("v_exp_f32 %0, %1" : "=v"(ge[3]) : "v"(gt[3]) : ); SB();
  asm volatile("v_mfma_f32_16x16x32_bf16 %0, v[160:163], a[200:203], %0" : "+v"(g1[2][0]) :  : ); SB();
  asm volatile("ds_write_b128 %0, %1 offset:8192" :  : "v"(pw1), "v"(g0[3][2]) : "memory"); SB();
  asm volatile("buffer_load_dwordx4 v[224:227], %0, %1, %2 offen offset:1024 sc1" :  : "v"(vB[1]), "s"(rWS), "s"(344064u) : "memory"); SB();
  asm volatile("v_mfma_f32_16x16x32_bf16 %0, v[160:163], a[204:207], %0" : "+v"(g1[3][0]) :  : ); SB();
  asm volatile("v_fma_f32 %0, %0, %1, %1" : "+v"(ge[0]) : "v"(xd[1]) : ); SB();
  asm volatile("v_fma_f32 %0, %0, %1, %1" : "+v"(ge[1]) : "v"(xd[1]) : ); SB();
  asm volatile("v_mfma_f32_16x16x32_bf16 %0, v[164:167], a[192:195], %0" : "+v"(g1[0][1]) :  : ); SB();
  asm volatile("v_fma_f32 %0, %0, %1, %1" : "+v"(ge[2]) : "v"(xd[1]) : ); SB();
  asm volatile("v_fma_f32 %0, %0, %1, %1" : "+v"(ge[3]) : "v"(xd[1]) : ); SB();
  asm volatile("v_mfma_f32_16x16x32_bf16 %0, v[164:167], a[196:199], %0" : "+v"(g1[1][1]) :  : ); SB();
  asm volatile("ds_write_b128 %0, %1 offset:9216" :  : "v"(pw1), "v"(g0[2][0]) : "memory"); SB();
  asm volatile("buffer_load_dwordx4 v[228:231], %0, %1, %2 offen offset:2048 sc1" :  : "v"(vB[1]), "s"(rWS), "s"(344064u) : "memory"); SB();
  asm volatile("v_mfma_f32_16x16x32_bf16 %0, v[164:167], a[200:203], %0" : "+v"(g1[2][1]) :  : ); SB();
  asm volatile("v_rcp_f32 %0, %1" : "=v"(gt[0]) : "v"(ge[0]) : ); SB();
  asm volatile("v_mfma_f32_16x16x32_bf16 %0, v[164:167], a[204:207], %0" : "+v"(g1[3][1]) :  : ); SB();
  asm volatile("v_rcp_f32 %0, %1" : "=v"(gt[1]) : "v"(ge[1]) : ); SB();
  asm volatile("v_mfma_f32_16x16x32_bf16 %0, v[168:171], a[192:195], %0" : "+v"(g1[0][2]) :  : ); SB();
  asm volatile("ds_write_b128 %0, %1 offset:10240" :  : "v"(pw1), "v"(g0[2][1]) : "memory"); SB();
  asm volatile("buffer_load_dwordx4 v[232:235], %0, %1, %2 offen offset:0 sc1" :  : "v"(vB[1]), "s"(rWS), "s"(393216u) : "memory"); SB();
  asm volatile("v_mfma_f32_16x16x32_bf16 %0, v[168:171], a[196:199], %0" : "+v"(g1[1][2]) :  : ); SB();
  asm volatile("v_rcp_f32 %0, %1" : "=v"(gt[2]) : "v"(ge[2]) : ); SB();
  asm volatile("v_mfma_f32_16x16x32_bf16 %0, v[168:171], a[200:203], %0" : "+v"(g1[2][2]) :  : ); SB();
  asm volatile("v_rcp_f32 %0, %1" : "=v"(gt[3]) : "v"(ge[3]) : ); SB();
  asm volatile("v_mfma_f32_16x16x32_bf16 %0, v[168:171], a[204:207], %0" : "+v"(g1[3][2]) :  : ); SB();
  asm volatile("ds_write_b128 %0, %1 offset:11264" :  : "v"(pw1), "v"(g0[2][2]) : "memory"); SB();
  asm volatile("s_waitcnt vmcnt(15)" :  :  : "memory"); SB();
  asm volatile("v_mfma_f32_16x16x32_bf16 %0, v[172:175], a[208:211], %0" : "+v"(g1[0][0]) :  : ); SB();
  asm volatile("v_mul_f32 %0, %0, %1" : "+v"(gt[0]) : "v"(a1[1][2][0]) : ); SB();
  asm volatile("v_mul_f32 %0, %0, %1" : "+v"(gt[1]) : "v"(a1[1][2][1]) : ); SB();
  asm volatile("buffer_load_dwordx4 v[236:239], %0, %1, %2 offen offset:1024 sc1" :  : "v"(vB[1]), "s"(rWS), "s"(393216u) : "memory"); SB();
  asm volatile("v_mfma_f32_16x16x32_bf16 %0, v[172:175], a[212:215], %0" : "+v"(g1[1][0]) :  : ); SB();
  asm volatile("v_mul_f32 %0, %0, %1" : "+v"(gt[2]) : "v"(a1[1][2][2]) : ); SB();
  asm volatile("v_mul_f32 %0, %0, %1" : "+v"(gt[3]) : "v"(a1[1][2][3]) : ); SB();
  asm volatile("v_mfma_f32_16x16x32_bf16 %0, v[172:175], a[216:219], %0" : "+v"(g1[2][0]) :  : ); SB();
  asm volatile("s_waitcnt lgkmcnt(0)\n\ts_barrier" :  :  : "memory"); SB();
  asm volatile("ds_read_b128 %0, %1 offset:0" : "=&v"(pr[0][0]) : "v"(pr1) : "memory"); SB();
  asm volatile("buffer_load_dwordx4 v[240:243], %0, %1, %2 offen offset:2048 sc1" :  : "v"(vB[1]), "s"(rWS), "s"(393216u) : "memory"); SB();
  asm volatile("v_mfma_f32_16x16x32_bf16 %0, v[172:175], a[220:223], %0" : "+v"(g1[3][0]) :  : ); SB();
  asm volatile("ds_read_b128 %0, %1 offset:1024" : "=&v"(pr[0][1]) : "v"(pr1) : "memory"); SB();
  asm volatile("buffer_load_dwordx4 v[244:247], %0, %1, %2 offen offset:0 sc1" :  : "v"(vB[1]), "s"(rWS), "s"(442368u) : "memory"); SB();
  asm volatile("v_mfma_f32_16x16x32_bf16 %0, v[176:179], a[208:211], %0" : "+v"(g1[0][1]) :  : ); SB();
  asm volatile("ds_read_b128 %0, %1 offset:2048" : "=&v"(pr[0][2]) : "v"(pr1) : "memory"); SB();
  asm volatile("v_mfma_f32_16x16x32_bf16 %0, v[176:179], a[212:215], %0" : "+v"(g1[1][1]) :  : ); SB();
  asm volatile("ds_read_b128 %0, %1 offset:3072" : "=&v"(pr[0][3]) : "v"(pr1) : "memory"); SB();
  asm volatile("v_mfma_f32_16x16x32_bf16 %0, v[176:179], a[216:219], %0" : "+v"(g1[2][1]) :  : ); SB();
  asm volatile("ds_read_b128 %0, %1 offset:4096" : "=&v"(pr[0][4]) : "v"(pr1) : "memory"); SB();
  asm volatile("v_mfma_f32_16x16x32_bf16 %0, v[176:179], a[220:223], %0" : "+v"(g1[3][1]) :  : ); SB();
  asm volatile("ds_read_b128 %0, %1 offset:5120" : "=&v"(pr[0][5]) : "v"(pr1) : "memory"); SB();
  asm volatile("buffer_load_dwordx4 v[248:251], %0, %1, %2 offen offset:1024 sc1" :  : "v"(vB[1]), "s"(rWS), "s"(442368u) : "memory"); SB();
  asm volatile("v_mfma_f32_16x16x32_bf16 %0, v[180:183], a[208:211], %0" : "+v"(g1[0][2]) :  : ); SB();
  asm volatile("ds_read_b128 %0, %1 offset:0" : "=&v"(pr[1][0]) : "v"(pr3) : "memory"); SB();
  asm volatile("v_mfma_f32_16x16x32_bf16 %0, v[180:183], a[212:215], %0" : "+v"(g1[1][2]) :  : ); SB();
  asm volatile("ds_read_b128 %0, %1 offset:1024" : "=&v"(pr[1][1]) : "v"(pr3) : "memory"); SB();
  asm volatile("v_mfma_f32_16x16x32_bf16 %0, v[180:183], a[216:219], %0" : "+v"(g1[2][2]) :  : ); SB();
  asm volatile("ds_read_b128 %0, %1 offset:2048" : "=&v"(pr[1][2]) : "v"(pr3) : "memory"); SB();
  asm volatile("buffer_load_dwordx4 v[252:255], %0, %1, %2 offen offset:2048 sc1" :  : "v"(vB[1]), "s"(rWS), "s"(442368u) : "memory"); SB();
  asm volatile("v_mfma_f32_16x16x32_bf16 %0, v[180:183], a[220:223], %0" : "+v"(g1[3][2]) :  : ); SB();
  asm volatile("ds_read_b128 %0, %1 offset:3072" : "=&v"(pr[1][3]) : "v"(pr3) : "memory"); SB();
  ST(28);
  asm volatile("s_waitcnt vmcnt(17)" :  :  : "memory"); SB();
  asm volatile("v_mfma_f32_16x16x32_bf16 %0, v[184:187], a[224:227], %0" : "+v"(g1[0][0]) :  : ); SB();
  asm volatile("ds_read_b128 %0, %1 offset:4096" : "=&v"(pr[1][4]) : "v"(pr3) : "memory"); SB();
  asm volatile("v_mfma_f32_16x16x32_bf16 %0, v[184:187], a[228:231], %0" : "+v"(g1[1][0]) :  : ); SB();
  asm volatile("ds_read_b128 %0, %1 offset:5120" : "=&v"(pr[1][5]) : "v"(pr3) : "memory"); SB();
  asm volatile("v_mfma_f32_16x16x32_bf16 %0, v[184:187], a[232:235], %0" : "+v"(g1[2][0]) :  : ); SB();
  asm volatile("v_cvt_pk_bf16_f32 %0, %1, %2" : "=v"(z1[1][2][0]) : "v"(gt[0]), "v"(gt[1]) : ); SB();
  asm volatile("v_cvt_pk_bf16_f32 %0, %1, %2" : "=v"(z1[1][2][1]) : "v"(gt[2]), "v"(gt[3]) : ); SB();
  asm volatile("v_mfma_f32_16x16x32_bf16 %0, v[184:187], a[236:239], %0" : "+v"(g1[3][0]) :  : ); SB();
  asm volatile("v_mfma_f32_16x16x32_bf16 %0, v[188:191], a[224:227], %0" : "+v"(g1[0][1]) :  : ); SB();
  asm volatile("buffer_load_dwordx4 v[160:163], %0, %1, %2 offen offset:0 sc1" :  : "v"(vB[1]), "s"(rWS), "s"(491520u) : "memory"); SB();
  asm volatile("v_mfma_f32_16x16x32_bf16 %0, v[188:191], a[228:231], %0" : "+v"(g1[1][1]) :  : ); SB();
  asm volatile("v_mfma_f32_16x16x32_bf16 %0, v[188:191], a[232:235], %0" : "+v"(g1[2][1]) :  : ); SB();
  asm volatile("s_waitcnt lgkmcnt(0)" : "+v"(pr[0][0]), "+v"(pr[0][1]), "+v"(pr[0][2]), "+v"(pr[0][3]), "+v"(pr[0][4]), "+v"(pr[0][5]), "+v"(pr[1][0]), "+v"(pr[1][1]), "+v"(pr[1][2]), "+v"(pr[1][3]), "+v"(pr[1][4]), "+v"(pr[1][5]) :  : "memory"); SB();
  asm volatile("s_waitcnt lgkmcnt(0)\n\ts_barrier" :  :  : "memory"); SB();
  asm volatile("buffer_load_dwordx4 v[164:167], %0, %1, %2 offen offset:1024 sc1" :  : "v"(vB[1]), "s"(rWS), "s"(491520u) : "memory"); SB();
  asm volatile("v_mfma_f32_16x16x32_bf16 %0, v[188:191], a[236:239], %0" : "+v"(g1[3][1]) :  : ); SB();
  asm volatile("v_add_f32 %0, %0, %1" : "+v"(pr[0][0][0]) : "v"(pr[1][0][0]) : ); SB();
  asm volatile("v_add_f32 %0, %0, %1" : "+v"(pr[0][0][1]) : "v"(pr[1][0][1]) : ); SB();
  asm volatile("v_mfma_f32_16x16x32_bf16 %0, v[192:195], a[224:227], %0" : "+v"(g1[0][2]) :  : ); SB();
  asm volatile("v_add_f32 %0, %0, %1" : "+v"(pr[0][0][2]) : "v"(pr[1][0][2]) : ); SB();
  asm volatile("v_add_f32 %0, %0, %1" : "+v"(pr[0][0][3]) : "v"(pr[1][0][3]) : ); SB();
  asm volatile("v_mfma_f32_16x16x32_bf16 %0, v[192:195], a[228:231], %0" : "+v"(g1[1][2]) :  : ); SB();
  asm volatile("v_add_f32 %0, %0, %1" : "+v"(pr[0][1][0]) : "v"(pr[1][1][0]) : ); SB();
  asm volatile("v_add_f32 %0, %0, %1" : "+v"(pr[0][1][1]) : "v"(pr[1][1][1]) : ); SB();
  asm volatile("buffer_load_dwordx4 v[168:171], %0, %1, %2 offen offset:2048 sc1" :  : "v"(vB[1]), "s"(rWS), "s"(491520u) : "memory"); SB();
  asm volatile("v_mfma_f32_16x16x32_bf16 %0, v[192:195], a[232:235], %0" : "+v"(g1[2][2]) :  : ); SB();
  asm volatile("v_add_f32 %0, %0, %1" : "+v"(pr[0][1][2]) : "v"(pr[1][1][2]) : ); SB();
  asm volatile("v_add_f32 %0, %0, %1" : "+v"(pr[0][1][3]) : "v"(pr[1][1][3]) : ); SB();
  asm volatile("v_mfma_f32_16x16x32_bf16 %0, v[192:195], a[236:239], %0" : "+v"(g1[3][2]) :  : ); SB();
  asm volatile("v_add_f32 %0, %0, %1" : "+v"(pr[0][2][0]) : "v"(pr[1][2][0]) : ); SB();
  asm volatile("v_add_f32 %0, %0, %1" : "+v"(pr[0][2][1]) : "v"(pr[1][2][1]) : ); SB();
  asm volatile("s_waitcnt vmcnt(15)" :  :  : "memory"); SB();
  asm volatile("v_mfma_f32_16x16x32_bf16 %0, v[196:199], a[240:243], %0" : "+v"(g1[0][0]) :  : ); SB();
  asm volatile("v_add_f32 %0, %0, %1" : "+v"(pr[0][2][2]) : "v"(pr[1][2][2]) : ); SB();
  asm volatile("v_add_f32 %0, %0, %1" : "+v"(pr[0][2][3]) : "v"(pr[1][2][3]) : ); SB();
  asm volatile("v_mfma_f32_16x16x32_bf16 %0, v[196:199], a[244:247], %0" : "+v"(g1[1][0]) :  : ); SB();
  asm volatile("v_add_f32 %0, %0, %1" : "+v"(pr[0][3][0]) : "v"(pr[1][3][0]) : ); SB();
  asm volatile("v_add_f32 %0, %0, %1" : "+v"(pr[0][3][1]) : "v"(pr[1][3][1]) : ); SB();
  asm volatile("v_mfma_f32_16x16x32_bf16 %0, v[196:199], a[248:251], %0" : "+v"(g1[2][0]) :  : ); SB();
  asm volatile("v_add_f32 %0, %0, %1" : "+v"(pr[0][3][2]) : "v"(pr[1][3][2]) : ); SB();
  asm volatile("v_add_f32 %0, %0, %1" : "+v"(pr[0][3][3]) : "v"(pr[1][3][3]) : ); SB();
  asm volatile("buffer_load_dwordx4 v[172:175], %0, %1, %2 offen offset:0 sc1" :  : "v"(vB[1]), "s"(rWS), "s"(540672u) : "memory"); SB();
  asm volatile("v_mfma_f32_16x16x32_bf16 %0, v[196:199], a[252:255], %0" : "+v"(g1[3][0]) :  : ); SB();
  asm volatile("v_add_f32 %0, %0, %1" : "+v"(pr[0][4][0]) : "v"(pr[1][4][0]) : ); SB();
  asm volatile("v_add_f32 %0, %0, %1" : "+v"(pr[0][4][1]) : "v"(pr[1][4][1]) : ); SB();
  asm volatile("v_mfma_f32_16x16x32_bf16 %0, v[200:203], a[240:243], %0" : "+v"(g1[0][1]) :  : ); SB();
  asm volatile("v_add_f32 %0, %0, %1" : "+v"(pr[0][4][2]) : "v"(pr[1][4][2]) : ); SB();
  asm volatile("v_add_f32 %0, %0, %1" : "+v"(pr[0][4][3]) : "v"(pr[1][4][3]) : ); SB();
  asm volatile("v_mfma_f32_16x16x32_bf16 %0, v[200:203], a[244:247], %0" : "+v"(g1[1][1]) :  : ); SB();
  asm volatile("v_add_f32 %0, %0, %1" : "+v"(pr[0][5][0]) : "v"(pr[1][5][0]) : ); SB();
  asm volatile("v_add_f32 %0, %0, %1" : "+v"(pr[0][5][1]) : "v"(pr[1][5][1]) : ); SB();
  asm volatile("v_mfma_f32_16x16x32_bf16 %0, v[200:203], a[248:251], %0" : "+v"(g1[2][1]) :  : ); SB();
  asm volatile("v_add_f32 %0, %0, %1" : "+v"(pr[0][5][2]) : "v"(pr[1][5][2]) : ); SB();
  asm volatile("v_add_f32 %0, %0, %1" : "+v"(pr[0][5][3]) : "v"(pr[1][5][3]) : ); SB();
  asm volatile("buffer_load_dwordx4 v[176:179], %0, %1, %2 offen offset:1024 sc1" :  : "v"(vB[1]), "s"(rWS), "s"(540672u) : "memory"); SB();
  asm volatile("v_mfma_f32_16x16x32_bf16 %0, v[200:203], a[252:255], %0" : "+v"(g1[3][1]) :  : ); SB();
  asm volatile("v_mfma_f32_16x16x32_bf16 %0, v[204:207], a[240:243], %0" : "+v"(g1[0][2]) :  : ); SB();
  asm volatile("v_mfma_f32_16x16x32_bf16 %0, v[204:207], a[244:247], %0" : "+v"(g1[1][2]) :  : ); SB();
  asm volatile("v_mfma_f32_16x16x32_bf16 %0, v[204:207], a[248:251], %0" : "+v"(g1[2][2]) :  : ); SB();
  asm volatile("v_mfma_f32_16x16x32_bf16 %0, v[204:207], a[252:255], %0" : "+v"(g1[3][2]) :  : ); SB();
  asm volatile("s_waitcnt vmcnt(14)" :  :  : "memory"); SB();
  asm volatile("v_mfma_f32_16x16x32_bf16 %0, v[208:211], a[0:3], %0" : "+v"(g1[0][0]) :  : ); SB();
  asm volatile("buffer_load_dwordx4 v[180:183], %0, %1, %2 offen offset:2048 sc1" :  : "v"(vB[1]), "s"(rWS), "s"(540672u) : "memory"); SB();
  asm volatile("v_mfma_f32_16x16x32_bf16 %0, v[208:211], a[4:7], %0" : "+v"(g1[1][0]) :  : ); SB();
  asm volatile("v_mfma_f32_16x16x32_bf16 %0, v[208:211], a[8:11], %0" : "+v"(g1[2][0]) :  : ); SB();
  asm volatile("v_mfma_f32_16x16x32_bf16 %0, v[208:211], a[12:15], %0" : "+v"(g1[3][0]) :  : ); SB();
  asm volatile("v_mfma_f32_16x16x32_bf16 %0, v[212:215], a[0:3], %0" : "+v"(g1[0][1]) :  : ); SB();
  asm volatile("v_mfma_f32_16x16x32_bf16 %0, v[212:215], a[4:7], %0" : "+v"(g1[1][1]) :  : ); SB();
  asm volatile("buffer_load_dwordx4 v[184:187], %0, %1, %2 offen offset:0 sc1" :  : "v"(vB[1]), "s"(rWS), "s"(589824u) : "memory"); SB();
  asm volatile("v_mfma_f32_16x16x32_bf16 %0, v[212:215], a[8:11], %0" : "+v"(g1[2][1]) :  : ); SB();
  asm volatile("v_mfma_f32_16x16x32_bf16 %0, v[212:215], a[12:15], %0" : "+v"(g1[3][1]) :  : ); SB();
  asm volatile("v_mfma_f32_16x16x32_bf16 %0, v[216:219], a[0:3], %0" : "+v"(g1[0][2]) :  : ); SB();
  asm volatile("v_mfma_f32_16x16x32_bf16 %0, v[216:219], a[4:7], %0" : "+v"(g1[1][2]) :  : ); SB();
  asm volatile("v_mfma_f32_16x16x32_bf16 %0, v[216:219], a[8:11], %0" : "+v"(g1[2][2]) :  : ); SB();
  asm volatile("buffer_load_dwordx4 v[188:191], %0, %1, %2 offen offset:1024 sc1" :  : "v"(vB[1]), "s"(rWS), "s"(589824u) : "memory"); SB();
  asm volatile("v_mfma_f32_16x16x32_bf16 %0, v[216:219], a[12:15], %0" : "+v"(g1[3][2]) :  : ); SB();
  asm volatile("s_waitcnt vmcnt(14)" :  :  : "memory"); SB();
  asm volatile("v_mfma_f32_16x16x32_bf16 %0, v[220:223], a[16:19], %0" : "+v"(g1[0][0]) :  : ); SB();
  asm volatile("v_mfma_f32_16x16x32_bf16 %0, v[220:223], a[20:23], %0" : "+v"(g1[1][0]) :  : ); SB();
  asm volatile("v_mfma_f32_16x16x32_bf16 %0, v[220:223], a[24:27], %0" : "+v"(g1[2][0]) :  : ); SB();
  asm volatile("v_mfma_f32_16x16x32_bf16 %0, v[220:223], a[28:31], %0" : "+v"(g1[3][0]) :  : ); SB();
  asm volatile("buffer_load_dwordx4 v[192:195], %0, %1, %2 offen offset:2048 sc1" :  : "v"(vB[1]), "s"(rWS), "s"(589824u) : "memory"); SB();
  asm volatile("v_mfma_f32_16x16x32_bf16 %0, v[224:227], a[16:19], %0" : "+v"(g1[0][1]) :  : ); SB();
  asm volatile("v_mfma_f32_16x16x32_bf16 %0, v[224:227], a[20:23], %0" : "+v"(g1[1][1]) :  : ); SB();
  asm volatile("v_mfma_f32_16x16x32_bf16 %0, v[224:227], a[24:27], %0" : "+v"(g1[2][1]) :  : ); SB();
  asm volatile("v_mfma_f32_16x16x32_bf16 %0, v[224:227], a[28:31], %0" : "+v"(g1[3][1]) :  : ); SB();
  asm volatile("v_mfma_f32_16x16x32_bf16 %0, v[228:231], a[16:19], %0" : "+v"(g1[0][2]) :  : ); SB();
  asm volatile("buffer_load_dwordx4 v[196:199], %0, %1, %2 offen offset:0 sc1" :  : "v"(vB[1]), "s"(rWS), "s"(638976u) : "memory"); SB();
  asm volatile("v_mfma_f32_16x16x32_bf16 %0, v[228:231], a[20:23], %0" : "+v"(g1[1][2]) :  : ); SB();
  asm volatile("v_mfma_f32_16x16x32_bf16 %0, v[228:231], a[24:27], %0" : "+v"(g1[2][2]) :  : ); SB();
  asm volatile("v_mfma_f32_16x16x32_bf16 %0, v[228:231], a[28:31], %0" : "+v"(g1[3][2]) :  : ); SB();
  ST(29);
  asm volatile("s_waitcnt vmcnt(13)" :  :  : "memory"); SB();
  asm volatile("v_mfma_f32_16x16x32_bf16 %0, v[232:235], a[32:35], %0" : "+v"(g1[0][0]) :  : ); SB();
  asm volatile("buffer_load_dwordx4 v[200:203], %0, %1, %2 offen offset:1024 sc1" :  : "v"(vB[1]), "s"(rWS), "s"(638976u) : "memory"); SB();
  asm volatile("v_mfma_f32_16x16x32_bf16 %0, v[232:235], a[36:39], %0" : "+v"(g1[1][0]) :  : ); SB();
  asm volatile("v_mfma_f32_16x16x32_bf16 %0, v[232:235], a[40:43], %0" : "+v"(g1[2][0]) :  : ); SB();
  asm volatile("v_mfma_f32_16x16x32_bf16 %0, v[232:235], a[44:47], %0" : "+v"(g1[3][0]) :  : ); SB();
  asm volatile("v_mfma_f32_16x16x32_bf16 %0, v[236:239], a[32:35], %0" : "+v"(g1[0][1]) :  : ); SB();
  asm volatile("v_mfma_f32_16x16x32_bf16 %0, v[236:239], a[36:39], %0" : "+v"(g1[1][1]) :  : ); SB();
  asm volatile("buffer_load_dwordx4 v[204:207], %0, %1, %2 offen offset:2048 sc1" :  : "v"(vB[1]), "s"(rWS), "s"(638976u) : "memory"); SB();
  asm volatile("v_mfma_f32_16x16x32_bf16 %0, v[236:239], a[40:43], %0" : "+v"(g1[2][1]) :  : ); SB();
  asm volatile("v_mfma_f32_16x16x32_bf16 %0, v[236:239], a[44:47], %0" : "+v"(g1[3][1]) :  : ); SB();
  asm volatile("v_mfma_f32_16x16x32_bf16 %0, v[240:243], a[32:35], %0" : "+v"(g1[0][2]) :  : ); SB();
  asm volatile("v_mfma_f32_16x16x32_bf16 %0, v[240:243], a[36:39], %0" : "+v"(g1[1][2]) :  : ); SB();
  asm volatile("v_mfma_f32_16x16x32_bf16 %0, v[240:243], a[40:43], %0" : "+v"(g1[2][2]) :  : ); SB();
  asm volatile("buffer_load_dwordx4 v[208:211], %0, %1, %2 offen offset:0 sc1" :  : "v"(vB[1]), "s"(rWS), "s"(688128u) : "memory"); SB();
  asm volatile("v_mfma_f32_16x16x32_bf16 %0, v[240:243], a[44:47], %0" : "+v"(g1[3][2]) :  : ); SB();
  asm volatile("s_waitcnt vmcnt(13)" :  :  : "memory"); SB();
  asm volatile("v_mfma_f32_16x16x32_bf16 %0, v[244:247], a[48:51], %0" : "+v"(g1[0][0]) :  : ); SB();
  asm volatile("buffer_load_dwordx4 v[212:215], %0, %1, %2 offen offset:1024 sc1" :  : "v"(vB[1]), "s"(rWS), "s"(688128u) : "memory"); SB();
  asm volatile("v_mfma_f32_16x16x32_bf16 %0, v[244:247], a[52:55], %0" : "+v"(g1[1][0]) :  : ); SB();
  asm volatile("v_mfma_f32_16x16x32_bf16 %0, v[244:247], a[56:59], %0" : "+v"(g1[2][0]) :  : ); SB();
  asm volatile("v_mfma_f32_16x16x32_bf16 %0, v[244:247], a[60:63], %0" : "+v"(g1[3][0]) :  : ); SB();
  asm volatile("v_mfma_f32_16x16x32_bf16 %0, v[248:251], a[48:51], %0" : "+v"(g1[0][1]) :  : ); SB();
  asm volatile("v_mfma_f32_16x16x32_bf16 %0, v[248:251], a[52:55], %0" : "+v"(g1[1][1]) :  : ); SB();
  asm volatile("buffer_load_dwordx4 v[216:219], %0, %1, %2 offen offset:2048 sc1" :  : "v"(vB[1]), "s"(rWS), "s"(688128u) : "memory"); SB();
  asm volatile("v_mfma_f32_16x16x32_bf16 %0, v[248:251], a[56:59], %0" : "+v"(g1[2][1]) :  : ); SB();
  asm volatile("v_mfma_f32_16x16x32_bf16 %0, v[248:251], a[60:63], %0" : "+v"(g1[3][1]) :  : ); SB();
  asm volatile("v_mfma_f32_16x16x32_bf16 %0, v[252:255], a[48:51], %0" : "+v"(g1[0][2]) :  : ); SB();
  asm volatile("v_mfma_f32_16x16x32_bf16 %0, v[252:255], a[52:55], %0" : "+v"(g1[1][2]) :  : ); SB();
  asm volatile("v_mfma_f32_16x16x32_bf16 %0, v[252:255], a[56:59], %0" : "+v"(g1[2][2]) :  : ); SB();
  asm volatile("buffer_load_dwordx4 v[220:223], %0, %1, %2 offen offset:0 sc1" :  : "v"(vB[1]), "s"(rWS), "s"(737280u) : "memory"); SB();
  asm volatile("v_mfma_f32_16x16x32_bf16 %0, v[252:255], a[60:63], %0" : "+v"(g1[3][2]) :  : ); SB();
  asm volatile("s_waitcnt vmcnt(13)" :  :  : "memory"); SB();
  asm volatile("v_mfma_f32_16x16x32_bf16 %0, v[160:163], a[64:67], %0" : "+v"(g1[0][0]) :  : ); SB();
  asm volatile("buffer_load_dwordx4 v[224:227], %0, %1, %2 offen offset:1024 sc1" :  : "v"(vB[1]), "s"(rWS), "s"(737280u) : "memory"); SB();
  asm volatile("v_mfma_f32_16x16x32_bf16 %0, v[160:163], a[68:71], %0" : "+v"(g1[1][0]) :  : ); SB();
  asm volatile("v_mfma_f32_16x16x32_bf16 %0, v[160:163], a[72:75], %0" : "+v"(g1[2][0]) :  : ); SB();
  asm volatile("v_mfma_f32_16x16x32_bf16 %0, v[160:163], a[76:79], %0" : "+v"(g1[3][0]) :  : ); SB();
  asm volatile("v_mfma_f32_16x16x32_bf16 %0, v[164:167], a[64:67], %0" : "+v"(g1[0][1]) :  : ); SB();
  asm volatile("v_mfma_f32_16x16x32_bf16 %0, v[164:167], a[68:71], %0" : "+v"(g1[1][1]) :  : ); SB();
  asm volatile("buffer_load_dwordx4 v[228:231], %0, %1, %2 offen offset:2048 sc1" :  : "v"(vB[1]), "s"(rWS), "s"(737280u) : "memory"); SB();
  asm volatile("v_mfma_f32_16x16x32_bf16 %0, v[164:167], a[72:75], %0" : "+v"(g1[2][1]) :  : ); SB();
  asm volatile("v_mfma_f32_16x16x32_bf16 %0, v[164:167], a[76:79], %0" : "+v"(g1[3][1]) :  : ); SB();
  asm volatile("v_mfma_f32_16x16x32_bf16 %0, v[168:171], a[64:67], %0" : "+v"(g1[0][2]) :  : ); SB();
  asm volatile("v_mfma_f32_16x16x32_bf16 %0, v[168:171], a[68:71], %0" : "+v"(g1[1][2]) :  : ); SB();
  asm volatile("v_mfma_f32_16x16x32_bf16 %0, v[168:171], a[72:75], %0" : "+v"(g1[2][2]) :  : ); SB();
  asm volatile("buffer_load_dwordx2 v[232:233], %0, %1, %2 offen offset:0 sc1" :  : "v"(vFY), "s"(rWS), "s"(0u) : "memory"); SB();
  asm volatile("v_mfma_f32_16x16x32_bf16 %0, v[168:171], a[76:79], %0" : "+v"(g1[3][2]) :  : ); SB();
  ST(30);
  asm volatile("s_waitcnt vmcnt(13)" :  :  : "memory"); SB();
  asm volatile("v_mfma_f32_16x16x32_bf16 %0, v[172:175], a[80:83], %0" : "+v"(g1[0][0]) :  : ); SB();
  asm volatile("s_add_u32 m0, %0, 0" :  : "s"(mO) : "memory", "scc"); SB();
  asm volatile("s_nop 0\n\tbuffer_load_dwordx4 %0, %1, %2 offen sc1 lds" :  : "v"(vO), "s"(rWS), "s"(0u) : "memory"); SB();
  asm volatile("v_mfma_f32_16x16x32_bf16 %0, v[172:175], a[84:87], %0" : "+v"(g1[1][0]) :  : ); SB();
  asm volatile("v_mfma_f32_16x16x32_bf16 %0, v[172:175], a[88:91], %0" : "+v"(g1[2][0]) :  : ); SB();
  asm volatile("v_mfma_f32_16x16x32_bf16 %0, v[172:175], a[92:95], %0" : "+v"(g1[3][0]) :  : ); SB();
  asm volatile("v_mfma_f32_16x16x32_bf16 %0, v[176:179], a[80:83], %0" : "+v"(g1[0][1]) :  : ); SB();
  asm volatile("v_mfma_f32_16x16x32_bf16 %0, v[176:179], a[84:87], %0" : "+v"(g1[1][1]) :  : ); SB();
  asm volatile("s_add_u32 m0, %0, 0" :  : "s"(mO) : "memory", "scc"); SB();
  asm volatile("s_nop 0\n\tbuffer_load_dwordx4 %0, %1, %2 offen offset:1024 sc1 lds" :  : "v"(vO), "s"(rWS), "s"(0u) : "memory"); SB();
  asm volatile("v_mfma_f32_16x16x32_bf16 %0, v[176:179], a[88:91], %0" : "+v"(g1[2][1]) :  : ); SB();
  asm volatile("v_mfma_f32_16x16x32_bf16 %0, v[176:179], a[92:95], %0" : "+v"(g1[3][1]) :  : ); SB();
  asm volatile("v_mfma_f32_16x16x32_bf16 %0, v[180:183], a[80:83], %0" : "+v"(g1[0][2]) :  : ); SB();
  asm volatile("v_mfma_f32_16x16x32_bf16 %0, v[180:183], a[84:87], %0" : "+v"(g1[1][2]) :  : ); SB();
  asm volatile("v_mfma_f32_16x16x32_bf16 %0, v[180:183], a[88:91], %0" : "+v"(g1[2][2]) :  : ); SB();
  asm volatile("s_add_u32 m0, %0, 0" :  : "s"(mO) : "memory", "scc"); SB();
  asm volatile("s_nop 0\n\tbuffer_load_dwordx4 %0, %1, %2 offen offset:2048 sc1 lds" :  : "v"(vO), "s"(rWS), "s"(0u) : "memory"); SB();
  asm volatile("v_mfma_f32_16x16x32_bf16 %0, v[180:183], a[92:95], %0" : "+v"(g1[3][2]) :  : ); SB();
  asm volatile("s_waitcnt vmcnt(13)" :  :  : "memory"); SB();
  asm volatile("v_mfma_f32_16x16x32_bf16 %0, v[184:187], a[96:99], %0" : "+v"(g1[0][0]) :  : ); SB();
  asm volatile("s_add_u32 m0, %0, 0" :  : "s"(mO) : "memory", "scc"); SB();
  asm volatile("s_nop 0\n\tbuffer_load_dwordx4 %0, %1, %2 offen offset:3072 sc1 lds" :  : "v"(vO), "s"(rWS), "s"(0u) : "memory"); SB();
  asm volatile("v_mfma_f32_16x16x32_bf16 %0, v[184:187], a[100:103], %0" : "+v"(g1[1][0]) :  : ); SB();
  asm volatile("v_mfma_f32_16x16x32_bf16 %0, v[184:187], a[104:107], %0" : "+v"(g1[2][0]) :  : ); SB();
  asm volatile("v_mfma_f32_16x16x32_bf16 %0, v[184:187], a[108:111], %0" : "+v"(g1[3][0]) :  : ); SB();
  asm volatile("v_mfma_f32_16x16x32_bf16 %0, v[188:191], a[96:99], %0" : "+v"(g1[0][1]) :  : ); SB();
  asm volatile("v_mfma_f32_16x16x32_bf16 %0, v[188:191], a[100:103], %0" : "+v"(g1[1][1]) :  : ); SB();
  asm volatile("s_add_u32 m0, %0, 4096" :  : "s"(mO) : "memory", "scc"); SB();
  asm volatile("s_nop 0\n\tbuffer_load_dwordx4 %0, %1, %2 offen sc1 lds" :  : "v"(vO), "s"(rWS), "s"(4096u) : "memory"); SB();
  asm volatile("v_mfma_f32_16x16x32_bf16 %0, v[188:191], a[104:107], %0" : "+v"(g1[2][1]) :  : ); SB();
  asm volatile("v_mfma_f32_16x16x32_bf16 %0, v[188:191], a[108:111], %0" : "+v"(g1[3][1]) :  : ); SB();
  asm volatile("v_mfma_f32_16x16x32_bf16 %0, v[192:195], a[96:99], %0" : "+v"(g1[0][2]) :  : ); SB();
  asm volatile("v_mfma_f32_16x16x32_bf16 %0, v[192:195], a[100:103], %0" : "+v"(g1[1][2]) :  : ); SB();
  asm volatile("v_mfma_f32_16x16x32_bf16 %0, v[192:195], a[104:107], %0" : "+v"(g1[2][2]) :  : ); SB();
  asm volatile("s_add_u32 m0, %0, 4096" :  : "s"(mO) : "memory", "scc"); SB();
  asm volatile("s_nop 0\n\tbuffer_load_dwordx4 %0, %1, %2 offen offset:1024 sc1 lds" :  : "v"(vO), "s"(rWS), "s"(4096u) : "memory"); SB();
  asm volatile("v_mfma_f32_16x16x32_bf16 %0, v[192:195], a[108:111], %0" : "+v"(g1[3][2]) :  : ); SB();
  asm volatile("s_waitcnt vmcnt(13)" :  :  : "memory"); SB();
  asm volatile("v_mfma_f32_16x16x32_bf16 %0, v[196:199], a[112:115], %0" : "+v"(g1[0][0]) :  : ); SB();
  asm volatile("s_add_u32 m0, %0, 4096" :  : "s"(mO) : "memory", "scc"); SB();
  asm volatile("s_nop 0\n\tbuffer_load_dwordx4 %0, %1, %2 offen offset:2048 sc1 lds" :  : "v"(vO), "s"(rWS), "s"(4096u) : "memory"); SB();
  asm volatile("v_mfma_f32_16x16x32_bf16 %0, v[196:199], a[116:119], %0" : "+v"(g1[1][0]) :  : ); SB();
  asm volatile("v_mfma_f32_16x16x32_bf16 %0, v[196:199], a[120:123], %0" : "+v"(g1[2][0]) :  : ); SB();
  asm volatile("v_mfma_f32_16x16x32_bf16 %0, v[196:199], a[124:127], %0" : "+v"(g1[3][0]) :  : ); SB();
  asm volatile("v_mfma_f32_16x16x32_bf16 %0, v[200:203], a[112:115], %0" : "+v"(g1[0][1]) :  : ); SB();
  asm volatile("v_mfma_f32_16x16x32_bf16 %0, v[200:203], a[116:119], %0" : "+v"(g1[1][1]) :  : ); SB();
  asm volatile("s_add_u32 m0, %0, 4096" :  : "s"(mO) : "memory", "scc"); SB();
  asm volatile("s_nop 0\n\tbuffer_load_dwordx4 %0, %1, %2 offen offset:3072 sc1 lds" :  : "v"(vO), "s"(rWS), "s"(4096u) : "memory"); SB();
  asm volatile("v_mfma_f32_16x16x32_bf16 %0, v[200:203], a[120:123], %0" : "+v"(g1[2][1]) :  : ); SB();
  asm volatile("v_mfma_f32_16x16x32_bf16 %0, v[200:203], a[124:127], %0" : "+v"(g1[3][1]) :  : ); SB();
  asm volatile("v_mfma_f32_16x16x32_bf16 %0, v[204:207], a[112:115], %0" : "+v"(g1[0][2]) :  : ); SB();
  asm volatile("v_mfma_f32_16x16x32_bf16 %0, v[204:207], a[116:119], %0" : "+v"(g1[1][2]) :  : ); SB();
  asm volatile("v_mfma_f32_16x16x32_bf16 %0, v[204:207], a[120:123], %0" : "+v"(g1[2][2]) :  : ); SB();
  asm volatile("s_add_u32 m0, %0, 8192" :  : "s"(mO) : "memory", "scc"); SB();
  asm volatile("s_nop 0\n\tbuffer_load_dwordx4 %0, %1, %2 offen sc1 lds" :  : "v"(vO), "s"(rWS), "s"(8192u) : "memory"); SB();
  asm volatile("v_mfma_f32_16x16x32_bf16 %0, v[204:207], a[124:127], %0" : "+v"(g1[3][2]) :  : ); SB();
  ST(31);
  asm volatile("s_waitcnt vmcnt(13)" :  :  : "memory"); SB();
  asm volatile("v_mfma_f32_16x16x32_bf16 %0, v[208:211], a[128:131], %0" : "+v"(g1[0][0]) :  : ); SB();
  asm volatile("s_add_u32 m0, %0, 8192" :  : "s"(mO) : "memory", "scc"); SB();
  asm volatile("s_nop 0\n\tbuffer_load_dwordx4 %0, %1, %2 offen offset:1024 sc1 lds" :  : "v"(vO), "s"(rWS), "s"(8192u) : "memory"); SB();
  asm volatile("v_mfma_f32_16x16x32_bf16 %0, v[208:211], a[132:135], %0" : "+v"(g1[1][0]) :  : ); SB();
  asm volatile("v_mfma_f32_16x16x32_bf16 %0, v[208:211], a[136:139], %0" : "+v"(g1[2][0]) :  : ); SB();
  asm volatile("v_mfma_f32_16x16x32_bf16 %0, v[208:211], a[140:143], %0" : "+v"(g1[3][0]) :  : ); SB();
  asm volatile("v_mfma_f32_16x16x32_bf16 %0, v[212:215], a[128:131], %0" : "+v"(g1[0][1]) :  : ); SB();
  asm volatile("v_mfma_f32_16x16x32_bf16 %0, v[212:215], a[132:135], %0" : "+v"(g1[1][1]) :  : ); SB();
  asm volatile("s_add_u32 m0, %0, 8192" :  : "s"(mO) : "memory", "scc"); SB();
  asm volatile("s_nop 0\n\tbuffer_load_dwordx4 %0, %1, %2 offen offset:2048 sc1 lds" :  : "v"(vO), "s"(rWS), "s"(8192u) : "memory"); SB();
  asm volatile("v_mfma_f32_16x16x32_bf16 %0, v[212:215], a[136:139], %0" : "+v"(g1[2][1]) :  : ); SB();
  asm volatile("v_mfma_f32_16x16x32_bf16 %0, v[212:215], a[140:143], %0" : "+v"(g1[3][1]) :  : ); SB();
  asm volatile("v_mfma_f32_16x16x32_bf16 %0, v[216:219], a[128:131], %0" : "+v"(g1[0][2]) :  : ); SB();
  asm volatile("v_mfma_f32_16x16x32_bf16 %0, v[216:219], a[132:135], %0" : "+v"(g1[1][2]) :  : ); SB();
  asm volatile("v_mfma_f32_16x16x32_bf16 %0, v[216:219], a[136:139], %0" : "+v"(g1[2][2]) :  : ); SB();
  asm volatile("s_add_u32 m0, %0, 8192" :  : "s"(mO) : "memory", "scc"); SB();
  asm volatile("s_nop 0\n\tbuffer_load_dwordx4 %0, %1, %2 offen offset:3072 sc1 lds" :  : "v"(vO), "s"(rWS), "s"(8192u) : "memory"); SB();
  asm volatile("v_mfma_f32_16x16x32_bf16 %0, v[216:219], a[140:143], %0" : "+v"(g1[3][2]) :  : ); SB();
  asm volatile("s_waitcnt vmcnt(13)" :  :  : "memory"); SB();
  asm volatile("v_mfma_f32_16x16x32_bf16 %0, v[220:223], a[152:155], %0" : "+v"(g1[2][0]) :  : ); SB();
  asm volatile("v_mfma_f32_16x16x32_bf16 %0, v[224:227], a[152:155], %0" : "+v"(g1[2][1]) :  : ); SB();
  asm volatile("v_mfma_f32_16x16x32_bf16 %0, v[228:231], a[152:155], %0" : "+v"(g1[2][2]) :  : ); SB();
  asm volatile("v_mfma_f32_16x16x32_bf16 %0, v[220:223], a[156:159], %0" : "+v"(g1[3][0]) :  : ); SB();
  asm volatile("v_mfma_f32_16x16x32_bf16 %0, v[224:227], a[156:159], %0" : "+v"(g1[3][1]) :  : ); SB();
  asm volatile("v_mfma_f32_16x16x32_bf16 %0, v[228:231], a[156:159], %0" : "+v"(g1[3][2]) :  : ); SB();
  asm volatile("v_mfma_f32_16x16x32_bf16 %0, v[220:223], a[144:147], %0" : "+v"(g1[0][0]) :  : ); SB();
  asm volatile("v_mfma_f32_16x16x32_bf16 %0, v[224:227], a[144:147], %0" : "+v"(g1[0][1]) :  : ); SB();
  asm volatile("v_mfma_f32_16x16x32_bf16 %0, v[228:231], a[144:147], %0" : "+v"(g1[0][2]) :  : ); SB();
  asm volatile("ds_write_b128 %0, %1 offset:0" :  : "v"(pw2), "v"(g1[2][0]) : "memory"); SB();
  asm volatile("v_mfma_f32_16x16x32_bf16 %0, v[220:223], a[148:151], %0" : "+v"(g1[1][0]) :  : ); SB();
  asm volatile("ds_write_b128 %0, %1 offset:1024" :  : "v"(pw2), "v"(g1[2][1]) : "memory"); SB();
  asm volatile("v_mfma_f32_16x16x32_bf16 %0, v[224:227], a[148:151], %0" : "+v"(g1[1][1]) :  : ); SB();
  asm volatile("ds_write_b128 %0, %1 offset:2048" :  : "v"(pw2), "v"(g1[2][2]) : "memory"); SB();
  asm volatile("v_mfma_f32_16x16x32_bf16 %0, v[228:231], a[148:151], %0" : "+v"(g1[1][2]) :  : ); SB();
  asm volatile("ds_write_b128 %0, %1 offset:3072" :  : "v"(pw2), "v"(g1[3][0]) : "memory"); SB();
  asm volatile("ds_write_b128 %0, %1 offset:4096" :  : "v"(pw2), "v"(g1[3][1]) : "memory"); SB();
  asm volatile("ds_write_b128 %0, %1 offset:5120" :  : "v"(pw2), "v"(g1[3][2]) : "memory"); SB();
  ST(17);
  asm volatile("s_waitcnt lgkmcnt(0)\n\ts_barrier" :  :  : "memory"); SB();
  asm volatile("ds_read_b128 %0, %1 offset:0" : "=&v"(pr[1][0]) : "v"(pr2) : "memory"); SB();
  asm volatile("ds_read_b128 %0, %1 offset:1024" : "=&v"(pr[1][1]) : "v"(pr2) : "memory"); SB();
  asm volatile("ds_read_b128 %0, %1 offset:2048" : "=&v"(pr[1][2]) : "v"(pr2) : "memory"); SB();
  asm volatile("ds_read_b128 %0, %1 offset:3072" : "=&v"(pr[1][3]) : "v"(pr2) : "memory"); SB();
  asm volatile("ds_read_b128 %0, %1 offset:4096" : "=&v"(pr[1][4]) : "v"(pr2) : "memory"); SB();
  asm volatile("ds_read_b128 %0, %1 offset:5120" : "=&v"(pr[1][5]) : "v"(pr2) : "memory"); SB();
  asm volatile("s_waitcnt lgkmcnt(0)" : "+v"(pr[1][0]), "+v"(pr[1][1]), "+v"(pr[1][2]), "+v"(pr[1][3]), "+v"(pr[1][4]), "+v"(pr[1][5]), "+v"(bs[0]), "+v"(bs[1]), "+v"(g1[0][0]), "+v"(g1[0][1]), "+v"(g1[0][2]), "+v"(g1[1][0]), "+v"(g1[1][1]), "+v"(g1[1][2]) :  : "memory"); SB();
  {
    unsigned ho[2][3][2];
#pragma unroll
    for (int i = 0; i < 2; ++i) {
      const f32x2 bv = {lo16(bs[i]), lo16(bs[i])};
#pragma unroll
      for (int j = 0; j < 3; ++j) {
        const f32x4 g = (g1[i][j] + pr[0][i * 3 + j]) + pr[1][i * 3 + j];
#pragma unroll
        for (int h = 0; h < 2; ++h) ho[i][j][h] = pk2(((f32x2){g[2 * h], g[2 * h + 1]} + bv) * unpk(z1[i][j][h]));
      }
    }
    const u32x4 h4[2] = {{ho[0][0][0], ho[0][0][1], ho[0][1][0], nohole(ho[0][1][1])}, {ho[1][0][0], ho[1][0][1], ho[1][1][0], nohole(ho[1][1][1])}};
    const bool sx = wn == (cb >> 2);
    const u32x4 h2 = {sx ? ho[1][2][0] : ho[0][2][0], sx ? ho[1][2][1] : ho[0][2][1], sx ? ho[0][2][0] : ho[1][2][0], nohole(sx ? ho[0][2][1] : ho[1][2][1])};
    SB();
  asm volatile("buffer_store_dwordx4 %1, %0, %2, %3 offen offset:0 sc1\n\ts_nop 1" :  : "v"(vH4), "v"(h4[0]), "s"(rWS), "s"((0 ^ wn) * 24576u) : "memory"); SB();
  asm volatile("buffer_store_dwordx4 %1, %0, %2, %3 offen offset:0 sc1\n\ts_nop 1" :  : "v"(vH4), "v"(h4[1]), "s"(rWS), "s"((1 ^ wn) * 24576u) : "memory"); SB();
  asm volatile("buffer_store_dwordx4 %1, %0, %2, %3 offen offset:0 sc1\n\ts_nop 1" :  : "v"(vH2), "v"(h2), "s"(rWS), "s"(0u) : "memory"); SB();
  }
  if ((rb | cb | w) == 0) asm volatile("buffer_store_dwordx2 %0, off, %1, 0 sc1" :: "v"(((u32x2){tg0, tg1})), "s"(rWS) : "memory");
  SB();
  ST(18);
  asm volatile("s_waitcnt vmcnt(15)\n\t.Lc_%=:\n\tv_cmpx_eq_u64 vcc, %1, v[232:233]\n\ts_cmp_eq_u64 vcc, -1\n\ts_mov_b64 exec, -1\n\ts_cbranch_scc0 .Lr_%=\n\t.Ld_%=:\n\t.subsection 1\n\t.Lr_%=:\n\ts_sub_i32 %0, %0, 1\n\ts_cmp_lt_i32 %0, 1\n\ts_cbranch_scc1 .Ld_%=\n\ts_sleep 2\n\tbuffer_load_dwordx2 v[232:233], %2, %3, 0 offen sc1\n\ts_waitcnt vmcnt(0)\n\ts_branch .Lc_%=\n\t.subsection 0" : "+s"(tmo) : "s"(tg), "v"(vFY), "s"(rWS) : "memory", "vcc", "scc"); SB();
  asm volatile("s_add_u32 m0, %0, 147456" :  : "s"(mY) : "memory", "scc"); SB();
  asm volatile("s_nop 0\n\tbuffer_load_dwordx4 %0, %1, %2 offen sc1 lds" :  : "v"(vYr), "s"(rWS), "s"(0u) : "memory"); SB();
  asm volatile("s_add_u32 m0, %0, 148480" :  : "s"(mY) : "memory", "scc"); SB();
  asm volatile("s_nop 0\n\tbuffer_load_dwordx4 %0, %1, %2 offen sc1 lds" :  : "v"(vYr), "s"(rWS), "s"(w3s + 32768u) : "memory"); SB();
  asm volatile("s_add_u32 m0, %0, 149504" :  : "s"(mY) : "memory", "scc"); SB();
  asm volatile("s_nop 0\n\tbuffer_load_dwordx4 %0, %1, %2 offen sc1 lds" :  : "v"(vYr), "s"(rWS), "s"(w3s + 65536u) : "memory"); SB();
  asm volatile("s_add_u32 m0, %0, 12288" :  : "s"(mO) : "memory", "scc"); SB();
  asm volatile("s_nop 0\n\tbuffer_load_dwordx4 %0, %1, %2 offen sc1 lds" :  : "v"(vO), "s"(rWS), "s"(12288u) : "memory"); SB();
  asm volatile("s_add_u32 m0, %0, 12288" :  : "s"(mO) : "memory", "scc"); SB();
  asm volatile("s_nop 0\n\tbuffer_load_dwordx4 %0, %1, %2 offen offset:1024 sc1 lds" :  : "v"(vO), "s"(rWS), "s"(12288u) : "memory"); SB();
  asm volatile("s_add_u32 m0, %0, 12288" :  : "s"(mO) : "memory", "scc"); SB();
  asm volatile("s_nop 0\n\tbuffer_load_dwordx4 %0, %1, %2 offen offset:2048 sc1 lds" :  : "v"(vO), "s"(rWS), "s"(12288u) : "memory"); SB();
  asm volatile("s_add_u32 m0, %0, 12288" :  : "s"(mO) : "memory", "scc"); SB();
  asm volatile("s_nop 0\n\tbuffer_load_dwordx4 %0, %1, %2 offen offset:3072 sc1 lds" :  : "v"(vO), "s"(rWS), "s"(12288u) : "memory"); SB();
  asm volatile("s_add_u32 m0, %0, 16384" :  : "s"(mO) : "memory", "scc"); SB();
  asm volatile("s_nop 0\n\tbuffer_load_dwordx4 %0, %1, %2 offen sc1 lds" :  : "v"(vO), "s"(rWS), "s"(16384u) : "memory"); SB();
  asm volatile("s_add_u32 m0, %0, 16384" :  : "s"(mO) : "memory", "scc"); SB();
  asm volatile("s_nop 0\n\tbuffer_load_dwordx4 %0, %1, %2 offen offset:1024 sc1 lds" :  : "v"(vO), "s"(rWS), "s"(16384u) : "memory"); SB();
  asm volatile("s_add_u32 m0, %0, 16384" :  : "s"(mO) : "memory", "scc"); SB();
  asm volatile("s_nop 0\n\tbuffer_load_dwordx4 %0, %1, %2 offen offset:2048 sc1 lds" :  : "v"(vO), "s"(rWS), "s"(16384u) : "memory"); SB();
  asm volatile("s_add_u32 m0, %0, 16384" :  : "s"(mO) : "memory", "scc"); SB();
  asm volatile("s_nop 0\n\tbuffer_load_dwordx4 %0, %1, %2 offen offset:3072 sc1 lds" :  : "v"(vO), "s"(rWS), "s"(16384u) : "memory"); SB();
  asm volatile("s_add_u32 m0, %0, 20480" :  : "s"(mO) : "memory", "scc"); SB();
  asm volatile("s_nop 0\n\tbuffer_load_dwordx4 %0, %1, %2 offen sc1 lds" :  : "v"(vO), "s"(rWS), "s"(20480u) : "memory"); SB();
  asm volatile("s_add_u32 m0, %0, 20480" :  : "s"(mO) : "memory", "scc"); SB();
  asm volatile("s_nop 0\n\tbuffer_load_dwordx4 %0, %1, %2 offen offset:1024 sc1 lds" :  : "v"(vO), "s"(rWS), "s"(20480u) : "memory"); SB();
  asm volatile("s_add_u32 m0, %0, 20480" :  : "s"(mO) : "memory", "scc"); SB();
  asm volatile("s_nop 0\n\tbuffer_load_dwordx4 %0, %1, %2 offen offset:2048 sc1 lds" :  : "v"(vO), "s"(rWS), "s"(20480u) : "memory"); SB();
  asm volatile("s_add_u32 m0, %0, 20480" :  : "s"(mO) : "memory", "scc"); SB();
  asm volatile("s_nop 0\n\tbuffer_load_dwordx4 %0, %1, %2 offen offset:3072 sc1 lds" :  : "v"(vO), "s"(rWS), "s"(20480u) : "memory"); SB();
  asm volatile("s_add_u32 m0, %0, 24576" :  : "s"(mO) : "memory", "scc"); SB();
  asm volatile("s_nop 0\n\tbuffer_load_dwordx4 %0, %1, %2 offen sc1 lds" :  : "v"(vO), "s"(rWS), "s"(24576u) : "memory"); SB();
  asm volatile("s_add_u32 m0, %0, 24576" :  : "s"(mO) : "memory", "scc"); SB();
  asm volatile("s_nop 0\n\tbuffer_load_dwordx4 %0, %1, %2 offen offset:1024 sc1 lds" :  : "v"(vO), "s"(rWS), "s"(24576u) : "memory"); SB();
  asm volatile("s_add_u32 m0, %0, 24576" :  : "s"(mO) : "memory", "scc"); SB();
  asm volatile("s_nop 0\n\tbuffer_load_dwordx4 %0, %1, %2 offen offset:2048 sc1 lds" :  : "v"(vO), "s"(rWS), "s"(24576u) : "memory"); SB();
  asm volatile("s_add_u32 m0, %0, 24576" :  : "s"(mO) : "memory", "scc"); SB();
  asm volatile("s_nop 0\n\tbuffer_load_dwordx4 %0, %1, %2 offen offset:3072 sc1 lds" :  : "v"(vO), "s"(rWS), "s"(24576u) : "memory"); SB();
  asm volatile("s_add_u32 m0, %0, 28672" :  : "s"(mO) : "memory", "scc"); SB();
  asm volatile("s_nop 0\n\tbuffer_load_dwordx4 %0, %1, %2 offen sc1 lds" :  : "v"(vO), "s"(rWS), "s"(28672u) : "memory"); SB();
  asm volatile("s_add_u32 m0, %0, 28672" :  : "s"(mO) : "memory", "scc"); SB();
  asm volatile("s_nop 0\n\tbuffer_load_dwordx4 %0, %1, %2 offen offset:1024 sc1 lds" :  : "v"(vO), "s"(rWS), "s"(28672u) : "memory"); SB();
  const unsigned aO = lb + wn * 61440 + l16;
  const unsigned vHr[2] = {W_HH + ((rb * 4 + (w ^ 1)) * 24 + wn * 12) * 1024 + l16, W_HH + ((rb * 4 + w) * 24 + wn * 12) * 1024 + l16};
  f32x4 o3[2][5], pq[5];
  f32x2 xf[5][2], sv[5][2];
  const unsigned vHm[2] = {W_HH + ((rb * 4 + wm * 2) * 24 + wn * 12) * 1024 + l16, W_HH + ((rb * 4 + wm * 2 + 1) * 24 + wn * 12) * 1024 + l16};
  ST(19);
  asm volatile("buffer_load_dwordx4 v[160:163], %0, %1, %2 offen offset:0 sc1" :  : "v"(vHr[0]), "s"(rWS), "s"(0u) : "memory"); SB();
  asm volatile("buffer_load_dwordx4 v[164:167], %0, %1, %2 offen offset:1024 sc1" :  : "v"(vHm[0]), "s"(rWS), "s"(0u) : "memory"); SB();
  asm volatile("buffer_load_dwordx4 v[212:215], %0, %1, %2 offen offset:1024 sc1" :  : "v"(vHm[1]), "s"(rWS), "s"(0u) : "memory"); SB();
  asm volatile("buffer_load_dwordx4 v[168:171], %0, %1, %2 offen offset:2048 sc1" :  : "v"(vHr[0]), "s"(rWS), "s"(0u) : "memory"); SB();
  asm volatile("buffer_load_dwordx4 v[172:175], %0, %1, %2 offen offset:0 sc1" :  : "v"(vHr[0]), "s"(rWS), "s"(3072u) : "memory"); SB();
  asm volatile("buffer_load_dwordx4 v[176:179], %0, %1, %2 offen offset:1024 sc1" :  : "v"(vHm[0]), "s"(rWS), "s"(3072u) : "memory"); SB();
  asm volatile("buffer_load_dwordx4 v[224:227], %0, %1, %2 offen offset:1024 sc1" :  : "v"(vHm[1]), "s"(rWS), "s"(3072u) : "memory"); SB();
  asm volatile("buffer_load_dwordx4 v[180:183], %0, %1, %2 offen offset:2048 sc1" :  : "v"(vHr[0]), "s"(rWS), "s"(3072u) : "memory"); SB();
  asm volatile("buffer_load_dwordx4 v[184:187], %0, %1, %2 offen offset:0 sc1" :  : "v"(vHr[0]), "s"(rWS), "s"(6144u) : "memory"); SB();
  asm volatile("buffer_load_dwordx4 v[188:191], %0, %1, %2 offen offset:1024 sc1" :  : "v"(vHm[0]), "s"(rWS), "s"(6144u) : "memory"); SB();
  asm volatile("buffer_load_dwordx4 v[236:239], %0, %1, %2 offen offset:1024 sc1" :  : "v"(vHm[1]), "s"(rWS), "s"(6144u) : "memory"); SB();
  asm volatile("buffer_load_dwordx4 v[192:195], %0, %1, %2 offen offset:2048 sc1" :  : "v"(vHr[0]), "s"(rWS), "s"(6144u) : "memory"); SB();
  asm volatile("buffer_load_dwordx4 v[196:199], %0, %1, %2 offen offset:0 sc1" :  : "v"(vHr[0]), "s"(rWS), "s"(9216u) : "memory"); SB();
  asm volatile("buffer_load_dwordx4 v[200:203], %0, %1, %2 offen offset:1024 sc1" :  : "v"(vHm[0]), "s"(rWS), "s"(9216u) : "memory"); SB();
  asm volatile("buffer_load_dwordx4 v[248:251], %0, %1, %2 offen offset:1024 sc1" :  : "v"(vHm[1]), "s"(rWS), "s"(9216u) : "memory"); SB();
  asm volatile("buffer_load_dwordx4 v[204:207], %0, %1, %2 offen offset:2048 sc1" :  : "v"(vHr[0]), "s"(rWS), "s"(9216u) : "memory"); SB();
  asm volatile("s_waitcnt vmcnt(16) lgkmcnt(0)\n\ts_barrier" :  :  : "memory"); SB();
  ST(20);
  {
    const unsigned ya = lb + 147456 + (4 * g4 + q4) * 128 + ((w ^ ((2 * g4 + (q4 >> 1)) & 3)) << 5) + 8 * p4;
    u32x2 yr[5];
  asm volatile("ds_read_b64_tr_b16 %0, %1 offset:0" : "=&v"(yr[0]) : "v"(ya) : "memory"); SB();
  asm volatile("ds_read_b64_tr_b16 %0, %1 offset:2048" : "=&v"(yr[1]) : "v"(ya) : "memory"); SB();
  asm volatile("ds_read_b64_tr_b16 %0, %1 offset:4096" : "=&v"(yr[2]) : "v"(ya) : "memory"); SB();
  asm volatile("ds_read_b64_tr_b16 %0, %1 offset:6144" : "=&v"(yr[3]) : "v"(ya) : "memory"); SB();
  asm volatile("ds_read_b64_tr_b16 %0, %1 offset:8192" : "=&v"(yr[4]) : "v"(ya) : "memory"); SB();
  asm volatile("ds_read_b128 a[0:3], %0 offset:0" :  : "v"(aO) : "memory"); SB();
  asm volatile("ds_read_b128 a[4:7], %0 offset:1024" :  : "v"(aO) : "memory"); SB();
  asm volatile("ds_read_b128 a[8:11], %0 offset:2048" :  : "v"(aO) : "memory"); SB();
  asm volatile("ds_read_b128 a[12:15], %0 offset:3072" :  : "v"(aO) : "memory"); SB();
  asm volatile("ds_read_b128 a[16:19], %0 offset:4096" :  : "v"(aO) : "memory"); SB();
  asm volatile("ds_read_b128 a[20:23], %0 offset:5120" :  : "v"(aO) : "memory"); SB();
  asm volatile("ds_read_b128 a[24:27], %0 offset:6144" :  : "v"(aO) : "memory"); SB();
  asm volatile("ds_read_b128 a[28:31], %0 offset:7168" :  : "v"(aO) : "memory"); SB();
  asm volatile("ds_read_b128 a[32:35], %0 offset:8192" :  : "v"(aO) : "memory"); SB();
  asm volatile("ds_read_b128 a[36:39], %0 offset:9216" :  : "v"(aO) : "memory"); SB();
  asm volatile("buffer_load_dwordx4 v[208:211], %0, %1, %2 offen offset:0 sc1" :  : "v"(vHr[1]), "s"(rWS), "s"(0u) : "memory"); SB();
  asm volatile("buffer_load_dwordx4 v[216:219], %0, %1, %2 offen offset:2048 sc1" :  : "v"(vHr[1]), "s"(rWS), "s"(0u) : "memory"); SB();
  asm volatile("buffer_load_dwordx4 v[220:223], %0, %1, %2 offen offset:0 sc1" :  : "v"(vHr[1]), "s"(rWS), "s"(3072u) : "memory"); SB();
  asm volatile("buffer_load_dwordx4 v[228:231], %0, %1, %2 offen offset:2048 sc1" :  : "v"(vHr[1]), "s"(rWS), "s"(3072u) : "memory"); SB();
  asm volatile("buffer_load_dwordx4 v[232:235], %0, %1, %2 offen offset:0 sc1" :  : "v"(vHr[1]), "s"(rWS), "s"(6144u) : "memory"); SB();
  asm volatile("buffer_load_dwordx4 v[240:243], %0, %1, %2 offen offset:2048 sc1" :  : "v"(vHr[1]), "s"(rWS), "s"(6144u) : "memory"); SB();
  asm volatile("buffer_load_dwordx4 v[244:247], %0, %1, %2 offen offset:0 sc1" :  : "v"(vHr[1]), "s"(rWS), "s"(9216u) : "memory"); SB();
  asm volatile("buffer_load_dwordx4 v[252:255], %0, %1, %2 offen offset:2048 sc1" :  : "v"(vHr[1]), "s"(rWS), "s"(9216u) : "memory"); SB();
  asm volatile("s_waitcnt lgkmcnt(10)" : "+v"(yr[0]), "+v"(yr[1]), "+v"(yr[2]), "+v"(yr[3]), "+v"(yr[4]) :  : "memory"); SB();
    asm volatile("" : "+v"(xm[0]), "+v"(xd[0]));
    const float rd = __builtin_amdgcn_rcpf(xd[0]), mr = -xm[0] * rd;
#pragma unroll
    for (int j = 0; j < 5; ++j)
#pragma unroll
      for (int h = 0; h < 2; ++h) {
        xf[j][h] = unpk(xv[j][h]);
        sv[j][h] = __builtin_elementwise_fma(xf[j][h], (f32x2){rd, rd}, (f32x2){mr, mr}) * unpk(yr[j][h]);
        asm volatile("" : "+v"(sv[j][h]), "+v"(xf[j][h]));
      }
    SB();
  }
  asm volatile("s_waitcnt vmcnt(20)\n\t.Lc_%=:\n\tv_max3_u32 %1, v163, v167, v215\n\tv_max_u32 %1, %1, v171\n\tv_cmp_eq_u32 vcc, -1, %1\n\ts_cbranch_vccnz .Lr_%=\n\t.Ld_%=:\n\t.subsection 1\n\t.Lr_%=:\n\ts_sub_i32 %0, %0, 1\n\ts_cmp_lt_i32 %0, 1\n\ts_cbranch_scc1 .Ld_%=\n\ts_sleep 2\n\ts_mov_b32 vcc_lo, 0\n\tbuffer_load_dwordx4 v[160:163], %2, %3, vcc_lo offen offset:0 sc1\n\tbuffer_load_dwordx4 v[164:167], %4, %3, vcc_lo offen offset:1024 sc1\n\tbuffer_load_dwordx4 v[212:215], %5, %3, vcc_lo offen offset:1024 sc1\n\tbuffer_load_dwordx4 v[168:171], %2, %3, vcc_lo offen offset:2048 sc1\n\ts_waitcnt vmcnt(0)\n\ts_branch .Lc_%=\n\t.subsection 0" : "+s"(tmo), "=&v"(vtmp) : "v"(vHr[0]), "s"(rWS), "v"(vHm[0]), "v"(vHm[1]) : "memory", "vcc", "scc"); SB();
  asm volatile("v_swap_b32 v166, v212\n\tv_swap_b32 v167, v213" :  :  : "memory"); SB();
  ST(21);
  asm volatile("s_waitcnt lgkmcnt(5)\n\tv_mfma_f32_16x16x32_bf16 %0, a[0:3], v[160:163], 0" : "=&v"(o3[0][0]) :  : ); SB();
  asm volatile("ds_read_b128 a[40:43], %0 offset:10240" :  : "v"(aO) : "memory"); SB();
  asm volatile("v_mfma_f32_16x16x32_bf16 %0, a[4:7], v[160:163], 0" : "=&v"(o3[0][1]) :  : ); SB();
  asm volatile("ds_read_b128 a[44:47], %0 offset:11264" :  : "v"(aO) : "memory"); SB();
  asm volatile("v_mfma_f32_16x16x32_bf16 %0, a[8:11], v[160:163], 0" : "=&v"(o3[0][2]) :  : ); SB();
  asm volatile("ds_read_b128 a[48:51], %0 offset:12288" :  : "v"(aO) : "memory"); SB();
  asm volatile("v_mfma_f32_16x16x32_bf16 %0, a[12:15], v[160:163], 0" : "=&v"(o3[0][3]) :  : ); SB();
  asm volatile("ds_read_b128 a[52:55], %0 offset:13312" :  : "v"(aO) : "memory"); SB();
  asm volatile("v_mfma_f32_16x16x32_bf16 %0, a[16:19], v[160:163], 0" : "=&v"(o3[0][4]) :  : ); SB();
  asm volatile("ds_read_b128 a[56:59], %0 offset:14336" :  : "v"(aO) : "memory"); SB();
  asm volatile("s_waitcnt lgkmcnt(5)\n\tv_mfma_f32_16x16x32_bf16 %0, a[20:23], v[164:167], %0" : "+v"(o3[0][0]) :  : ); SB();
  asm volatile("ds_read_b128 a[60:63], %0 offset:15360" :  : "v"(aO) : "memory"); SB();
  asm volatile("v_mfma_f32_16x16x32_bf16 %0, a[24:27], v[164:167], %0" : "+v"(o3[0][1]) :  : ); SB();
  asm volatile("ds_read_b128 a[64:67], %0 offset:16384" :  : "v"(aO) : "memory"); SB();
  asm volatile("v_mfma_f32_16x16x32_bf16 %0, a[28:31], v[164:167], %0" : "+v"(o3[0][2]) :  : ); SB();
  asm volatile("ds_read_b128 a[68:71], %0 offset:17408" :  : "v"(aO) : "memory"); SB();
  asm volatile("v_mfma_f32_16x16x32_bf16 %0, a[32:35], v[164:167], %0" : "+v"(o3[0][3]) :  : ); SB();
  asm volatile("ds_read_b128 a[72:75], %0 offset:18432" :  : "v"(aO) : "memory"); SB();
  asm volatile("v_mfma_f32_16x16x32_bf16 %0, a[36:39], v[164:167], %0" : "+v"(o3[0][4]) :  : ); SB();
  asm volatile("ds_read_b128 a[76:79], %0 offset:19456" :  : "v"(aO) : "memory"); SB();
  asm volatile("s_waitcnt lgkmcnt(5)\n\tv_mfma_f32_16x16x32_bf16 %0, a[40:43], v[168:171], %0" : "+v"(o3[0][0]) :  : ); SB();
  asm volatile("ds_read_b128 a[80:83], %0 offset:20480" :  : "v"(aO) : "memory"); SB();
  asm volatile("v_mfma_f32_16x16x32_bf16 %0, a[44:47], v[168:171], %0" : "+v"(o3[0][1]) :  : ); SB();
  asm volatile("ds_read_b128 a[84:87], %0 offset:21504" :  : "v"(aO) : "memory"); SB();
  asm volatile("s_waitcnt vmcnt(16)\n\t.Lc_%=:\n\tv_max3_u32 %1, v175, v179, v227\n\tv_max_u32 %1, %1, v183\n\tv_cmp_eq_u32 vcc, -1, %1\n\ts_cbranch_vccnz .Lr_%=\n\t.Ld_%=:\n\t.subsection 1\n\t.Lr_%=:\n\ts_sub_i32 %0, %0, 1\n\ts_cmp_lt_i32 %0, 1\n\ts_cbranch_scc1 .Ld_%=\n\ts_sleep 2\n\ts_mov_b32 vcc_lo, 3072\n\tbuffer_load_dwordx4 v[172:175], %2, %3, vcc_lo offen offset:0 sc1\n\tbuffer_load_dwordx4 v[176:179], %4, %3, vcc_lo offen offset:1024 sc1\n\tbuffer_load_dwordx4 v[224:227], %5, %3, vcc_lo offen offset:1024 sc1\n\tbuffer_load_dwordx4 v[180:183], %2, %3, vcc_lo offen offset:2048 sc1\n\ts_waitcnt vmcnt(0)\n\ts_branch .Lc_%=\n\t.subsection 0" : "+s"(tmo), "=&v"(vtmp) : "v"(vHr[0]), "s"(rWS), "v"(vHm[0]), "v"(vHm[1]) : "memory", "vcc", "scc"); SB();
  asm volatile("v_swap_b32 v178, v224\n\tv_swap_b32 v179, v225" :  :  : "memory"); SB();
  asm volatile("v_mfma_f32_16x16x32_bf16 %0, a[48:51], v[168:171], %0" : "+v"(o3[0][2]) :  : ); SB();
  asm volatile("ds_read_b128 a[88:91], %0 offset:22528" :  : "v"(aO) : "memory"); SB();
  asm volatile("v_mfma_f32_16x16x32_bf16 %0, a[52:55], v[168:171], %0" : "+v"(o3[0][3]) :  : ); SB();
  asm volatile("ds_read_b128 a[92:95], %0 offset:23552" :  : "v"(aO) : "memory"); SB();
  asm volatile("v_mfma_f32_16x16x32_bf16 %0, a[56:59], v[168:171], %0" : "+v"(o3[0][4]) :  : ); SB();
  asm volatile("ds_read_b128 a[96:99], %0 offset:24576" :  : "v"(aO) : "memory"); SB();
  asm volatile("s_waitcnt lgkmcnt(5)\n\tv_mfma_f32_16x16x32_bf16 %0, a[60:63], v[172:175], %0" : "+v"(o3[0][0]) :  : ); SB();
  asm volatile("ds_read_b128 a[100:103], %0 offset:25600" :  : "v"(aO) : "memory"); SB();
  asm volatile("v_mfma_f32_16x16x32_bf16 %0, a[64:67], v[172:175], %0" : "+v"(o3[0][1]) :  : ); SB();
  asm volatile("ds_read_b128 a[104:107], %0 offset:26624" :  : "v"(aO) : "memory"); SB();
  asm volatile("v_mfma_f32_16x16x32_bf16 %0, a[68:71], v[172:175], %0" : "+v"(o3[0][2]) :  : ); SB();
  asm volatile("ds_read_b128 a[108:111], %0 offset:27648" :  : "v"(aO) : "memory"); SB();
  asm volatile("v_mfma_f32_16x16x32_bf16 %0, a[72:75], v[172:175], %0" : "+v"(o3[0][3]) :  : ); SB();
  asm volatile("ds_read_b128 a[112:115], %0 offset:28672" :  : "v"(aO) : "memory"); SB();
  asm volatile("v_mfma_f32_16x16x32_bf16 %0, a[76:79], v[172:175], %0" : "+v"(o3[0][4]) :  : ); SB();
  asm volatile("ds_read_b128 a[116:119], %0 offset:29696" :  : "v"(aO) : "memory"); SB();
  asm volatile("s_waitcnt lgkmcnt(5)\n\tv_mfma_f32_16x16x32_bf16 %0, a[80:83], v[176:179], %0" : "+v"(o3[0][0]) :  : ); SB();
  asm volatile("ds_read_b128 a[120:123], %0 offset:30720" :  : "v"(aO) : "memory"); SB();
  asm volatile("v_mfma_f32_16x16x32_bf16 %0, a[84:87], v[176:179], %0" : "+v"(o3[0][1]) :  : ); SB();
  asm volatile("ds_read_b128 a[124:127], %0 offset:31744" :  : "v"(aO) : "memory"); SB();
  asm volatile("v_mfma_f32_16x16x32_bf16 %0, a[88:91], v[176:179], %0" : "+v"(o3[0][2]) :  : ); SB();
  asm volatile("ds_read_b128 a[128:131], %0 offset:32768" :  : "v"(aO) : "memory"); SB();
  asm volatile("v_mfma_f32_16x16x32_bf16 %0, a[92:95], v[176:179], %0" : "+v"(o3[0][3]) :  : ); SB();
  asm volatile("ds_read_b128 a[132:135], %0 offset:33792" :  : "v"(aO) : "memory"); SB();
  asm volatile("v_mfma_f32_16x16x32_bf16 %0, a[96:99], v[176:179], %0" : "+v"(o3[0][4]) :  : ); SB();
  asm volatile("ds_read_b128 a[136:139], %0 offset:34816" :  : "v"(aO) : "memory"); SB();
  asm volatile("s_waitcnt lgkmcnt(5)\n\tv_mfma_f32_16x16x32_bf16 %0, a[100:103], v[180:183], %0" : "+v"(o3[0][0]) :  : ); SB();
  asm volatile("ds_read_b128 a[140:143], %0 offset:35840" :  : "v"(aO) : "memory"); SB();
  asm volatile("v_mfma_f32_16x16x32_bf16 %0, a[104:107], v[180:183], %0" : "+v"(o3[0][1]) :  : ); SB();
  asm volatile("ds_read_b128 a[144:147], %0 offset:36864" :  : "v"(aO) : "memory"); SB();
  asm volatile("s_waitcnt vmcnt(12)\n\t.Lc_%=:\n\tv_max3_u32 %1, v187, v191, v239\n\tv_max_u32 %1, %1, v195\n\tv_cmp_eq_u32 vcc, -1, %1\n\ts_cbranch_vccnz .Lr_%=\n\t.Ld_%=:\n\t.subsection 1\n\t.Lr_%=:\n\ts_sub_i32 %0, %0, 1\n\ts_cmp_lt_i32 %0, 1\n\ts_cbranch_scc1 .Ld_%=\n\ts_sleep 2\n\ts_mov_b32 vcc_lo, 6144\n\tbuffer_load_dwordx4 v[184:187], %2, %3, vcc_lo offen offset:0 sc1\n\tbuffer_load_dwordx4 v[188:191], %4, %3, vcc_lo offen offset:1024 sc1\n\tbuffer_load_dwordx4 v[236:239], %5, %3, vcc_lo offen offset:1024 sc1\n\tbuffer_load_dwordx4 v[192:195], %2, %3, vcc_lo offen offset:2048 sc1\n\ts_waitcnt vmcnt(0)\n\ts_branch .Lc_%=\n\t.subsection 0" : "+s"(tmo), "=&v"(vtmp) : "v"(vHr[0]), "s"(rWS), "v"(vHm[0]), "v"(vHm[1]) : "memory", "vcc", "scc"); SB();
  asm volatile("v_swap_b32 v190, v236\n\tv_swap_b32 v191, v237" :  :  : "memory"); SB();
  asm volatile("v_mfma_f32_16x16x32_bf16 %0, a[108:111], v[180:183], %0" : "+v"(o3[0][2]) :  : ); SB();
  asm volatile("ds_read_b128 a[148:151], %0 offset:37888" :  : "v"(aO) : "memory"); SB();
  asm volatile("v_mfma_f32_16x16x32_bf16 %0, a[112:115], v[180:183], %0" : "+v"(o3[0][3]) :  : ); SB();
  asm volatile("ds_read_b128 a[152:155], %0 offset:38912" :  : "v"(aO) : "memory"); SB();
  asm volatile("v_mfma_f32_16x16x32_bf16 %0, a[116:119], v[180:183], %0" : "+v"(o3[0][4]) :  : ); SB();
  asm volatile("ds_read_b128 a[156:159], %0 offset:39936" :  : "v"(aO) : "memory"); SB();
  asm volatile("s_waitcnt lgkmcnt(5)\n\tv_mfma_f32_16x16x32_bf16 %0, a[120:123], v[184:187], %0" : "+v"(o3[0][0]) :  : ); SB();
  asm volatile("ds_read_b128 a[160:163], %0 offset:40960" :  : "v"(aO) : "memory"); SB();
  asm volatile("v_mfma_f32_16x16x32_bf16 %0, a[124:127], v[184:187], %0" : "+v"(o3[0][1]) :  : ); SB();
  asm volatile("ds_read_b128 a[164:167], %0 offset:41984" :  : "v"(aO) : "memory"); SB();
  asm volatile("v_mfma_f32_16x16x32_bf16 %0, a[128:131], v[184:187], %0" : "+v"(o3[0][2]) :  : ); SB();
  asm volatile("ds_read_b128 a[168:171], %0 offset:43008" :  : "v"(aO) : "memory"); SB();
  asm volatile("v_mfma_f32_16x16x32_bf16 %0, a[132:135], v[184:187], %0" : "+v"(o3[0][3]) :  : ); SB();
  asm volatile("ds_read_b128 a[172:175], %0 offset:44032" :  : "v"(aO) : "memory"); SB();
  asm volatile("v_mfma_f32_16x16x32_bf16 %0, a[136:139], v[184:187], %0" : "+v"(o3[0][4]) :  : ); SB();
  asm volatile("ds_read_b128 a[176:179], %0 offset:45056" :  : "v"(aO) : "memory"); SB();
  asm volatile("s_waitcnt lgkmcnt(5)\n\tv_mfma_f32_16x16x32_bf16 %0, a[140:143], v[188:191], %0" : "+v"(o3[0][0]) :  : ); SB();
  asm volatile("ds_read_b128 a[180:183], %0 offset:46080" :  : "v"(aO) : "memory"); SB();
  asm volatile("v_mfma_f32_16x16x32_bf16 %0, a[144:147], v[188:191], %0" : "+v"(o3[0][1]) :  : ); SB();
  asm volatile("ds_read_b128 a[184:187], %0 offset:47104" :  : "v"(aO) : "memory"); SB();
  asm volatile("v_mfma_f32_16x16x32_bf16 %0, a[148:151], v[188:191], %0" : "+v"(o3[0][2]) :  : ); SB();
  asm volatile("ds_read_b128 a[188:191], %0 offset:48128" :  : "v"(aO) : "memory"); SB();
  asm volatile("v_mfma_f32_16x16x32_bf16 %0, a[152:155], v[188:191], %0" : "+v"(o3[0][3]) :  : ); SB();
  asm volatile("ds_read_b128 a[192:195], %0 offset:49152" :  : "v"(aO) : "memory"); SB();
  asm volatile("v_mfma_f32_16x16x32_bf16 %0, a[156:159], v[188:191], %0" : "+v"(o3[0][4]) :  : ); SB();
  asm volatile("ds_read_b128 a[196:199], %0 offset:50176" :  : "v"(aO) : "memory"); SB();
  asm volatile("s_waitcnt lgkmcnt(5)\n\tv_mfma_f32_16x16x32_bf16 %0, a[160:163], v[192:195], %0" : "+v"(o3[0][0]) :  : ); SB();
  asm volatile("ds_read_b128 a[200:203], %0 offset:51200" :  : "v"(aO) : "memory"); SB();
  asm volatile("v_mfma_f32_16x16x32_bf16 %0, a[164:167], v[192:195], %0" : "+v"(o3[0][1]) :  : ); SB();
  asm volatile("ds_read_b128 a[204:207], %0 offset:52224" :  : "v"(aO) : "memory"); SB();
  asm volatile("s_waitcnt vmcnt(8)\n\t.Lc_%=:\n\tv_max3_u32 %1, v199, v203, v251\n\tv_max_u32 %1, %1, v207\n\tv_cmp_eq_u32 vcc, -1, %1\n\ts_cbranch_vccnz .Lr_%=\n\t.Ld_%=:\n\t.subsection 1\n\t.Lr_%=:\n\ts_sub_i32 %0, %0, 1\n\ts_cmp_lt_i32 %0, 1\n\ts_cbranch_scc1 .Ld_%=\n\ts_sleep 2\n\ts_mov_b32 vcc_lo, 9216\n\tbuffer_load_dwordx4 v[196:199], %2, %3, vcc_lo offen offset:0 sc1\n\tbuffer_load_dwordx4 v[200:203], %4, %3, vcc_lo offen offset:1024 sc1\n\tbuffer_load_dwordx4 v[248:251], %5, %3, vcc_lo offen offset:1024 sc1\n\tbuffer_load_dwordx4 v[204:207], %2, %3, vcc_lo offen offset:2048 sc1\n\ts_waitcnt vmcnt(0)\n\ts_branch .Lc_%=\n\t.subsection 0" : "+s"(tmo), "=&v"(vtmp) : "v"(vHr[0]), "s"(rWS), "v"(vHm[0]), "v"(vHm[1]) : "memory", "vcc", "scc"); SB();
  asm volatile("v_swap_b32 v202, v248\n\tv_swap_b32 v203, v249" :  :  : "memory"); SB();
  asm volatile("v_mfma_f32_16x16x32_bf16 %0, a[168:171], v[192:195], %0" : "+v"(o3[0][2]) :  : ); SB();
  asm volatile("ds_read_b128 a[208:211], %0 offset:53248" :  : "v"(aO) : "memory"); SB();
  asm volatile("v_mfma_f32_16x16x32_bf16 %0, a[172:175], v[192:195], %0" : "+v"(o3[0][3]) :  : ); SB();
  asm volatile("ds_read_b128 a[212:215], %0 offset:54272" :  : "v"(aO) : "memory"); SB();
  asm volatile("v_mfma_f32_16x16x32_bf16 %0, a[176:179], v[192:195], %0" : "+v"(o3[0][4]) :  : ); SB();
  asm volatile("ds_read_b128 a[216:219], %0 offset:55296" :  : "v"(aO) : "memory"); SB();
  asm volatile("s_waitcnt lgkmcnt(5)\n\tv_mfma_f32_16x16x32_bf16 %0, a[180:183], v[196:199], %0" : "+v"(o3[0][0]) :  : ); SB();
  asm volatile("ds_read_b128 a[220:223], %0 offset:56320" :  : "v"(aO) : "memory"); SB();
  asm volatile("v_mfma_f32_16x16x32_bf16 %0, a[184:187], v[196:199], %0" : "+v"(o3[0][1]) :  : ); SB();
  asm volatile("ds_read_b128 a[224:227], %0 offset:57344" :  : "v"(aO) : "memory"); SB();
  asm volatile("v_mfma_f32_16x16x32_bf16 %0, a[188:191], v[196:199], %0" : "+v"(o3[0][2]) :  : ); SB();
  asm volatile("ds_read_b128 a[228:231], %0 offset:58368" :  : "v"(aO) : "memory"); SB();
  asm volatile("v_mfma_f32_16x16x32_bf16 %0, a[192:195], v[196:199], %0" : "+v"(o3[0][3]) :  : ); SB();
  asm volatile("ds_read_b128 a[232:235], %0 offset:59392" :  : "v"(aO) : "memory"); SB();
  asm volatile("v_mfma_f32_16x16x32_bf16 %0, a[196:199], v[196:199], %0" : "+v"(o3[0][4]) :  : ); SB();
  asm volatile("ds_read_b128 a[236:239], %0 offset:60416" :  : "v"(aO) : "memory"); SB();
  asm volatile("s_waitcnt lgkmcnt(5)\n\tv_mfma_f32_16x16x32_bf16 %0, a[200:203], v[200:203], %0" : "+v"(o3[0][0]) :  : ); SB();
  asm volatile("v_mfma_f32_16x16x32_bf16 %0, a[204:207], v[200:203], %0" : "+v"(o3[0][1]) :  : ); SB();
  asm volatile("v_mfma_f32_16x16x32_bf16 %0, a[208:211], v[200:203], %0" : "+v"(o3[0][2]) :  : ); SB();
  asm volatile("v_mfma_f32_16x16x32_bf16 %0, a[212:215], v[200:203], %0" : "+v"(o3[0][3]) :  : ); SB();
  asm volatile("v_mfma_f32_16x16x32_bf16 %0, a[216:219], v[200:203], %0" : "+v"(o3[0][4]) :  : ); SB();
  asm volatile("s_waitcnt lgkmcnt(0)\n\tv_mfma_f32_16x16x32_bf16 %0, a[220:223], v[204:207], %0" : "+v"(o3[0][0]) :  : ); SB();
  asm volatile("v_mfma_f32_16x16x32_bf16 %0, a[224:227], v[204:207], %0" : "+v"(o3[0][1]) :  : ); SB();
  asm volatile("s_waitcnt vmcnt(6)\n\t.Lc_%=:\n\tv_max3_u32 %1, v211, v219, v211\n\tv_max_u32 %1, %1, v219\n\tv_cmp_eq_u32 vcc, -1, %1\n\ts_cbranch_vccnz .Lr_%=\n\t.Ld_%=:\n\t.subsection 1\n\t.Lr_%=:\n\ts_sub_i32 %0, %0, 1\n\ts_cmp_lt_i32 %0, 1\n\ts_cbranch_scc1 .Ld_%=\n\ts_sleep 2\n\ts_mov_b32 vcc_lo, 0\n\tbuffer_load_dwordx4 v[208:211], %2, %3, vcc_lo offen offset:0 sc1\n\tbuffer_load_dwordx4 v[216:219], %2, %3, vcc_lo offen offset:2048 sc1\n\ts_waitcnt vmcnt(0)\n\ts_branch .Lc_%=\n\t.subsection 0" : "+s"(tmo), "=&v"(vtmp) : "v"(vHr[1]), "s"(rWS) : "memory", "vcc", "scc"); SB();
  asm volatile("v_mfma_f32_16x16x32_bf16 %0, a[228:231], v[204:207], %0" : "+v"(o3[0][2]) :  : ); SB();
  asm volatile("v_mfma_f32_16x16x32_bf16 %0, a[232:235], v[204:207], %0" : "+v"(o3[0][3]) :  : ); SB();
  asm volatile("v_mfma_f32_16x16x32_bf16 %0, a[236:239], v[204:207], %0" : "+v"(o3[0][4]) :  : ); SB();
  asm volatile("v_mfma_f32_16x16x32_bf16 %0, a[0:3], v[208:211], 0" : "=&v"(o3[1][0]) :  : ); SB();
  asm volatile("v_mfma_f32_16x16x32_bf16 %0, a[4:7], v[208:211], 0" : "=&v"(o3[1][1]) :  : ); SB();
  asm volatile("v_mfma_f32_16x16x32_bf16 %0, a[8:11], v[208:211], 0" : "=&v"(o3[1][2]) :  : ); SB();
  asm volatile("v_mfma_f32_16x16x32_bf16 %0, a[12:15], v[208:211], 0" : "=&v"(o3[1][3]) :  : ); SB();
  asm volatile("ds_write_b128 %0, %1 offset:0" :  : "v"(pw2), "v"(o3[0][0]) : "memory"); SB();
  asm volatile("v_mfma_f32_16x16x32_bf16 %0, a[16:19], v[208:211], 0" : "=&v"(o3[1][4]) :  : ); SB();
  asm volatile("v_mfma_f32_16x16x32_bf16 %0, a[20:23], v[212:215], %0" : "+v"(o3[1][0]) :  : ); SB();
  asm volatile("ds_write_b128 %0, %1 offset:1024" :  : "v"(pw2), "v"(o3[0][1]) : "memory"); SB();
  asm volatile("v_mfma_f32_16x16x32_bf16 %0, a[24:27], v[212:215], %0" : "+v"(o3[1][1]) :  : ); SB();
  asm volatile("v_mfma_f32_16x16x32_bf16 %0, a[28:31], v[212:215], %0" : "+v"(o3[1][2]) :  : ); SB();
  asm volatile("ds_write_b128 %0, %1 offset:2048" :  : "v"(pw2), "v"(o3[0][2]) : "memory"); SB();
  asm volatile("v_mfma_f32_16x16x32_bf16 %0, a[32:35], v[212:215], %0" : "+v"(o3[1][3]) :  : ); SB();
  asm volatile("v_mfma_f32_16x16x32_bf16 %0, a[36:39], v[212:215], %0" : "+v"(o3[1][4]) :  : ); SB();
  asm volatile("ds_write_b128 %0, %1 offset:3072" :  : "v"(pw2), "v"(o3[0][3]) : "memory"); SB();
  asm volatile("v_mfma_f32_16x16x32_bf16 %0, a[40:43], v[216:219], %0" : "+v"(o3[1][0]) :  : ); SB();
  asm volatile("v_mfma_f32_16x16x32_bf16 %0, a[44:47], v[216:219], %0" : "+v"(o3[1][1]) :  : ); SB();
  asm volatile("s_waitcnt vmcnt(4)\n\t.Lc_%=:\n\tv_max3_u32 %1, v223, v231, v223\n\tv_max_u32 %1, %1, v231\n\tv_cmp_eq_u32 vcc, -1, %1\n\ts_cbranch_vccnz .Lr_%=\n\t.Ld_%=:\n\t.subsection 1\n\t.Lr_%=:\n\ts_sub_i32 %0, %0, 1\n\ts_cmp_lt_i32 %0, 1\n\ts_cbranch_scc1 .Ld_%=\n\ts_sleep 2\n\ts_mov_b32 vcc_lo, 3072\n\tbuffer_load_dwordx4 v[220:223], %2, %3, vcc_lo offen offset:0 sc1\n\tbuffer_load_dwordx4 v[228:231], %2, %3, vcc_lo offen offset:2048 sc1\n\ts_waitcnt vmcnt(0)\n\ts_branch .Lc_%=\n\t.subsection 0" : "+s"(tmo), "=&v"(vtmp) : "v"(vHr[1]), "s"(rWS) : "memory", "vcc", "scc"); SB();
  asm volatile("v_mfma_f32_16x16x32_bf16 %0, a[48:51], v[216:219], %0" : "+v"(o3[1][2]) :  : ); SB();
  asm volatile("v_mfma_f32_16x16x32_bf16 %0, a[52:55], v[216:219], %0" : "+v"(o3[1][3]) :  : ); SB();
  asm volatile("ds_write_b128 %0, %1 offset:4096" :  : "v"(pw2), "v"(o3[0][4]) : "memory"); SB();
  asm volatile("v_mfma_f32_16x16x32_bf16 %0, a[56:59], v[216:219], %0" : "+v"(o3[1][4]) :  : ); SB();
  asm volatile("v_mfma_f32_16x16x32_bf16 %0, a[60:63], v[220:223], %0" : "+v"(o3[1][0]) :  : ); SB();
  asm volatile("v_mfma_f32_16x16x32_bf16 %0, a[64:67], v[220:223], %0" : "+v"(o3[1][1]) :  : ); SB();
  asm volatile("v_mfma_f32_16x16x32_bf16 %0, a[68:71], v[220:223], %0" : "+v"(o3[1][2]) :  : ); SB();
  asm volatile("v_mfma_f32_16x16x32_bf16 %0, a[72:75], v[220:223], %0" : "+v"(o3[1][3]) :  : ); SB();
  asm volatile("v_mfma_f32_16x16x32_bf16 %0, a[76:79], v[220:223], %0" : "+v"(o3[1][4]) :  : ); SB();
  asm volatile("v_mfma_f32_16x16x32_bf16 %0, a[80:83], v[224:227], %0" : "+v"(o3[1][0]) :  : ); SB();
  asm volatile("v_mfma_f32_16x16x32_bf16 %0, a[84:87], v[224:227], %0" : "+v"(o3[1][1]) :  : ); SB();
  asm volatile("s_waitcnt lgkmcnt(0)\n\ts_barrier" :  :  : "memory"); SB();
  asm volatile("v_mfma_f32_16x16x32_bf16 %0, a[88:91], v[224:227], %0" : "+v"(o3[1][2]) :  : ); SB();
  asm volatile("v_mfma_f32_16x16x32_bf16 %0, a[92:95], v[224:227], %0" : "+v"(o3[1][3]) :  : ); SB();
  asm volatile("v_mfma_f32_16x16x32_bf16 %0, a[96:99], v[224:227], %0" : "+v"(o3[1][4]) :  : ); SB();
  asm volatile("v_mfma_f32_16x16x32_bf16 %0, a[100:103], v[228:231], %0" : "+v"(o3[1][0]) :  : ); SB();
  asm volatile("v_mfma_f32_16x16x32_bf16 %0, a[104:107], v[228:231], %0" : "+v"(o3[1][1]) :  : ); SB();
  asm volatile("s_waitcnt vmcnt(2)\n\t.Lc_%=:\n\tv_max3_u32 %1, v235, v243, v235\n\tv_max_u32 %1, %1, v243\n\tv_cmp_eq_u32 vcc, -1, %1\n\ts_cbranch_vccnz .Lr_%=\n\t.Ld_%=:\n\t.subsection 1\n\t.Lr_%=:\n\ts_sub_i32 %0, %0, 1\n\ts_cmp_lt_i32 %0, 1\n\ts_cbranch_scc1 .Ld_%=\n\ts_sleep 2\n\ts_mov_b32 vcc_lo, 6144\n\tbuffer_load_dwordx4 v[232:235], %2, %3, vcc_lo offen offset:0 sc1\n\tbuffer_load_dwordx4 v[240:243], %2, %3, vcc_lo offen offset:2048 sc1\n\ts_waitcnt vmcnt(0)\n\ts_branch .Lc_%=\n\t.subsection 0" : "+s"(tmo), "=&v"(vtmp) : "v"(vHr[1]), "s"(rWS) : "memory", "vcc", "scc"); SB();
  asm volatile("v_mfma_f32_16x16x32_bf16 %0, a[108:111], v[228:231], %0" : "+v"(o3[1][2]) :  : ); SB();
  asm volatile("v_mfma_f32_16x16x32_bf16 %0, a[112:115], v[228:231], %0" : "+v"(o3[1][3]) :  : ); SB();
  asm volatile("v_mfma_f32_16x16x32_bf16 %0, a[116:119], v[228:231], %0" : "+v"(o3[1][4]) :  : ); SB();
  asm volatile("v_mfma_f32_16x16x32_bf16 %0, a[120:123], v[232:235], %0" : "+v"(o3[1][0]) :  : ); SB();
  asm volatile("ds_read_b128 %0, %1 offset:0" : "=&v"(pq[0]) : "v"((lb + 122880 + (w ^ 1) * 6144 + l16)) : "memory"); SB();
  asm volatile("v_mfma_f32_16x16x32_bf16 %0, a[124:127], v[232:235], %0" : "+v"(o3[1][1]) :  : ); SB();
  asm volatile("ds_read_b128 %0, %1 offset:1024" : "=&v"(pq[1]) : "v"((lb + 122880 + (w ^ 1) * 6144 + l16)) : "memory"); SB();
  asm volatile("v_mfma_f32_16x16x32_bf16 %0, a[128:131], v[232:235], %0" : "+v"(o3[1][2]) :  : ); SB();
  asm volatile("ds_read_b128 %0, %1 offset:2048" : "=&v"(pq[2]) : "v"((lb + 122880 + (w ^ 1) * 6144 + l16)) : "memory"); SB();
  asm volatile("v_mfma_f32_16x16x32_bf16 %0, a[132:135], v[232:235], %0" : "+v"(o3[1][3]) :  : ); SB();
  asm volatile("ds_read_b128 %0, %1 offset:3072" : "=&v"(pq[3]) : "v"((lb + 122880 + (w ^ 1) * 6144 + l16)) : "memory"); SB();
  asm volatile("v_mfma_f32_16x16x32_bf16 %0, a[136:139], v[232:235], %0" : "+v"(o3[1][4]) :  : ); SB();
  asm volatile("ds_read_b128 %0, %1 offset:4096" : "=&v"(pq[4]) : "v"((lb + 122880 + (w ^ 1) * 6144 + l16)) : "memory"); SB();
  asm volatile("v_mfma_f32_16x16x32_bf16 %0, a[140:143], v[236:239], %0" : "+v"(o3[1][0]) :  : ); SB();
  asm volatile("v_mfma_f32_16x16x32_bf16 %0, a[144:147], v[236:239], %0" : "+v"(o3[1][1]) :  : ); SB();
  asm volatile("v_mfma_f32_16x16x32_bf16 %0, a[148:151], v[236:239], %0" : "+v"(o3[1][2]) :  : ); SB();
  asm volatile("v_mfma_f32_16x16x32_bf16 %0, a[152:155], v[236:239], %0" : "+v"(o3[1][3]) :  : ); SB();
  asm volatile("v_mfma_f32_16x16x32_bf16 %0, a[156:159], v[236:239], %0" : "+v"(o3[1][4]) :  : ); SB();
  asm volatile("v_mfma_f32_16x16x32_bf16 %0, a[160:163], v[240:243], %0" : "+v"(o3[1][0]) :  : ); SB();
  asm volatile("v_mfma_f32_16x16x32_bf16 %0, a[164:167], v[240:243], %0" : "+v"(o3[1][1]) :  : ); SB();
  asm volatile("s_waitcnt vmcnt(0)\n\t.Lc_%=:\n\tv_max3_u32 %1, v247, v255, v247\n\tv_max_u32 %1, %1, v255\n\tv_cmp_eq_u32 vcc, -1, %1\n\ts_cbranch_vccnz .Lr_%=\n\t.Ld_%=:\n\t.subsection 1\n\t.Lr_%=:\n\ts_sub_i32 %0, %0, 1\n\ts_cmp_lt_i32 %0, 1\n\ts_cbranch_scc1 .Ld_%=\n\ts_sleep 2\n\ts_mov_b32 vcc_lo, 9216\n\tbuffer_load_dwordx4 v[244:247], %2, %3, vcc_lo offen offset:0 sc1\n\tbuffer_load_dwordx4 v[252:255], %2, %3, vcc_lo offen offset:2048 sc1\n\ts_waitcnt vmcnt(0)\n\ts_branch .Lc_%=\n\t.subsection 0" : "+s"(tmo), "=&v"(vtmp) : "v"(vHr[1]), "s"(rWS) : "memory", "vcc", "scc"); SB();
  asm volatile("v_mfma_f32_16x16x32_bf16 %0, a[168:171], v[240:243], %0" : "+v"(o3[1][2]) :  : ); SB();
  asm volatile("v_mfma_f32_16x16x32_bf16 %0, a[172:175], v[240:243], %0" : "+v"(o3[1][3]) :  : ); SB();
  asm volatile("v_mfma_f32_16x16x32_bf16 %0, a[176:179], v[240:243], %0" : "+v"(o3[1][4]) :  : ); SB();
  asm volatile("v_mfma_f32_16x16x32_bf16 %0, a[180:183], v[244:247], %0" : "+v"(o3[1][0]) :  : ); SB();
  asm volatile("v_mfma_f32_16x16x32_bf16 %0, a[184:187], v[244:247], %0" : "+v"(o3[1][1]) :  : ); SB();
  asm volatile("v_mfma_f32_16x16x32_bf16 %0, a[188:191], v[244:247], %0" : "+v"(o3[1][2]) :  : ); SB();
  asm volatile("v_mfma_f32_16x16x32_bf16 %0, a[192:195], v[244:247], %0" : "+v"(o3[1][3]) :  : ); SB();
  asm volatile("v_mfma_f32_16x16x32_bf16 %0, a[196:199], v[244:247], %0" : "+v"(o3[1][4]) :  : ); SB();
  asm volatile("v_mfma_f32_16x16x32_bf16 %0, a[200:203], v[248:251], %0" : "+v"(o3[1][0]) :  : ); SB();
  asm volatile("v_mfma_f32_16x16x32_bf16 %0, a[204:207], v[248:251], %0" : "+v"(o3[1][1]) :  : ); SB();
  asm volatile("v_mfma_f32_16x16x32_bf16 %0, a[208:211], v[248:251], %0" : "+v"(o3[1][2]) :  : ); SB();
  asm volatile("v_mfma_f32_16x16x32_bf16 %0, a[212:215], v[248:251], %0" : "+v"(o3[1][3]) :  : ); SB();
  asm volatile("v_mfma_f32_16x16x32_bf16 %0, a[216:219], v[248:251], %0" : "+v"(o3[1][4]) :  : ); SB();
  asm volatile("v_mfma_f32_16x16x32_bf16 %0, a[220:223], v[252:255], %0" : "+v"(o3[1][0]) :  : ); SB();
  asm volatile("v_mfma_f32_16x16x32_bf16 %0, a[224:227], v[252:255], %0" : "+v"(o3[1][1]) :  : ); SB();
  asm volatile("v_mfma_f32_16x16x32_bf16 %0, a[228:231], v[252:255], %0" : "+v"(o3[1][2]) :  : ); SB();
  asm volatile("v_mfma_f32_16x16x32_bf16 %0, a[232:235], v[252:255], %0" : "+v"(o3[1][3]) :  : ); SB();
  asm volatile("v_mfma_f32_16x16x32_bf16 %0, a[236:239], v[252:255], %0" : "+v"(o3[1][4]) :  : ); SB();
  ST(22);
  asm volatile("s_waitcnt lgkmcnt(0)\n\ts_nop 7" : "+v"(o3[1][0]), "+v"(o3[1][1]), "+v"(o3[1][2]), "+v"(o3[1][3]), "+v"(o3[1][4]), "+v"(pq[0]), "+v"(pq[1]), "+v"(pq[2]), "+v"(pq[3]), "+v"(pq[4]) :  : "memory"); SB();
  {
    unsigned pk[5][2];
#pragma unroll
    for (int j = 0; j < 5; ++j) {
      const f32x4 o4 = o3[1][j] + pq[j];
#pragma unroll
      for (int h = 0; h < 2; ++h) {
        const f32x2 o = {o4[2 * h], o4[2 * h + 1]};
        const f32x2 a = o * (f32x2){-1.4426950f, -1.4426950f};
        const f32x2 e = (f32x2){__builtin_amdgcn_exp2f(a[0]), __builtin_amdgcn_exp2f(a[1])} + (f32x2){1.0f, 1.0f};
        const f32x2 r = {__builtin_amdgcn_rcpf(e[0]), __builtin_amdgcn_rcpf(e[1])};
        pk[j][h] = pk2(__builtin_elementwise_fma(o * r, sv[j][h], xf[j][h]));
      }
    }
    char* po = out + (rb * 64 + w * 16 + l15) * 1280 + cb * 160;
    const unsigned dq = 32 * (g4 & 1) + 16 * (g4 >> 1);
#pragma unroll
    for (int q = 0; q < 2; ++q) {
      const auto s0 = __builtin_amdgcn_permlane16_swap(pk[2 * q][0], pk[2 * q + 1][0], false, false);
      const auto s1 = __builtin_amdgcn_permlane16_swap(pk[2 * q][1], pk[2 * q + 1][1], false, false);
      *(u32x4*)(po + 64 * q + dq) = (u32x4){s0[0], s1[0], s0[1], s1[1]};
    }
    *(u32x2*)(po + 128 + 8 * g4) = (u32x2){pk[4][0], pk[4][1]};
  }
  ST(23);
  ST_OUT();
  }}}}}
}

extern "C" __global__ void k_end() {}

extern "C" void kernel_launch(void* const* d_in, const int* in_sizes, int n_in,
                              void* d_out, int out_size, void* d_ws, size_t ws_size,
                              hipStream_t stream) {
  (void)hipFuncSetAttribute((const void*)k_all, hipFuncAttributeMaxDynamicSharedMemorySize, 163840);
  k_all<<<256, 256, 163840, stream>>>((const char*)d_in[0], (const char*)d_in[1], (const char*)d_in[2], (const char*)d_in[3], (const char*)d_in[4],
                                      (const char*)d_in[5], (const char*)d_in[7], (const char*)d_in[8], (const char*)d_in[9], (const char*)d_in[10],
                                      (char*)d_out, (char*)d_ws);
}
```

```cpp
#include <hip/hip_runtime.h>
#include <stdint.h>

typedef unsigned short u16;
typedef __attribute__((ext_vector_type(4))) float f32x4;
typedef __attribute__((ext_vector_type(2))) float f32x2;
typedef __attribute__((ext_vector_type(4))) unsigned int u32x4;
typedef __attribute__((ext_vector_type(2))) unsigned int u32x2;
typedef __attribute__((ext_vector_type(2))) __bf16 bf16x2n;
typedef __attribute__((ext_vector_type(8))) unsigned long long u64x8;
typedef __attribute__((ext_vector_type(4))) unsigned long long u64x4;
#define LDS_AS __attribute__((address_space(3)))
#define GAS __attribute__((address_space(1)))
#define SB() __builtin_amdgcn_sched_barrier(0)
#define DI __device__ __forceinline__

#define SGA
#define ST_DECL do {} while (0)
#define ST(i) do {} while (0)
#define ST_OUT() do {} while (0)

constexpr int S_ = 2048, D_ = 640;
constexpr unsigned HOLE = 0xFFFFFFFFu;
constexpr int W_ST = 4096, W_FZ = 768 << 10, W_FY = W_FZ + 8192, W_FK = W_FY + 8192, W_KV = 1 << 20, W_Z2 = 4 << 20, W_YY = 7 << 20, W_HH = 10 << 20, W_WO = 13 << 20;

DI unsigned pack2(float lo, float hi) {
  f32x2 v = {lo, hi};
  return __builtin_bit_cast(unsigned, __builtin_convertvector(v, bf16x2n));
}
DI float lo16(unsigned w) { return __uint_as_float(w << 16); }
DI float hi16(unsigned w) { return __uint_as_float(w & 0xFFFF0000u); }
DI float rbf(float f) { return lo16(pack2(f, f)); }
DI f32x2 unpk(unsigned w) { f32x2 r = {lo16(w), hi16(w)}; return r; }
DI unsigned pk2(f32x2 v) { return pack2(v[0], v[1]); }
DI unsigned nohole(unsigned a) { return a < HOLE - 1 ? a : HOLE - 1; }
template <int CTRL> DI float dppf(float v) {
  return __builtin_bit_cast(float, __builtin_amdgcn_update_dpp(0, __builtin_bit_cast(int, v), CTRL, 0xF, 0xF, true));
}
DI f32x2 gelu2(f32x2 t, float k1, float k2, float den) {
  const f32x2 a = t * __builtin_elementwise_fma(t * t, (f32x2){k2, k2}, (f32x2){k1, k1});
  f32x2 e = {__builtin_amdgcn_exp2f(a[0]), __builtin_amdgcn_exp2f(a[1])};
  e = __builtin_elementwise_fma(e, (f32x2){den, den}, (f32x2){den, den});
  const f32x2 r = {__builtin_amdgcn_rcpf(e[0]), __builtin_amdgcn_rcpf(e[1])};
  return t * r;
}
DI float red16(float v) {
  const auto a = __builtin_amdgcn_permlane16_swap(__float_as_uint(v), __float_as_uint(v), false, false);
  return __uint_as_float(a[0]) + __uint_as_float(a[1]);
}
DI float red32(float v) {
  const auto c = __builtin_amdgcn_permlane32_swap(__float_as_uint(v), __float_as_uint(v), false, false);
  return __uint_as_float(c[0]) + __uint_as_float(c[1]);
}
DI float sel32(float a, float b) {
  const auto c = __builtin_amdgcn_permlane32_swap(__float_as_uint(a), __float_as_uint(b), false, false);
  return __uint_as_float(c[0]) + __uint_as_float(c[1]);
}
DI float dot2(unsigned a, unsigned b, float c) {
  return __builtin_amdgcn_fdot2_f32_bf16(__builtin_bit_cast(bf16x2n, a), __builtin_bit_cast(bf16x2n, b), c, false);
}
DI unsigned shr16(unsigned hi, unsigned lo) { return __builtin_amdgcn_alignbit(hi, lo, 16); }
DI unsigned lds_addr(const void* p) { return (unsigned)(uintptr_t)(LDS_AS const char*)p; }

__global__ __launch_bounds__(256, 1) __attribute__((amdgpu_num_vgpr(160))) SGA void k_all(
    const char*, const char*, const char*, const char*, const char*, const char*, const char*, const char*, const char*, const char*, char*, char*) {
  extern __shared__ __attribute__((aligned(16))) char smem[];
  ST_DECL;
  ST(0);
  u64x8 ka0; u64x4 ka1;
  asm volatile("s_load_dwordx16 %0, %2, 0x0\n\ts_load_dwordx8 %1, %2, 0x40" : "=&s"(ka0), "=&s"(ka1) : "s"(__builtin_amdgcn_kernarg_segment_ptr()) : "memory"); SB();
  const unsigned tid = threadIdx.x, lane0 = tid & 63, w0 = __builtin_amdgcn_readfirstlane(tid >> 6);
  const unsigned b = blockIdx.x, xcd = b & 7, idx = b >> 3;
  const unsigned rb0 = 8 * (xcd >> 1) + (idx >> 2), cb0 = 4 * (xcd & 1) + (idx & 3);
  const unsigned lb = lds_addr(smem);
  const unsigned lane = lane0, w = w0, rb = rb0, cb = cb0;
  {
    unsigned long long pc; unsigned clen;
    asm volatile("s_getpc_b64 %0\n.Lpc_here_%=:\n\ts_mov_b32 %1, .Lcode_end-.Lpc_here_%=" : "=s"(pc), "=s"(clen));
    const unsigned o = min((((lane & 3) * 4 + w) * 32 + idx) * 128, clen - 4u) & ~3u;
    asm volatile("s_mov_b64 exec, 15\n\tglobal_load_dword v254, %0, %1\n\ts_mov_b64 exec, -1" :: "v"(o), "s"(pc) : "memory");
    SB();
  }
  unsigned voA = (rb * 64 + w * 16 + (lane >> 3)) * 1280 + (((lane & 7) ^ (lane >> 3)) << 4), voB[3];
#pragma unroll
  for (int i = 0; i < 3; ++i) { const unsigned o = i * 64 + lane, r = (o * 171) >> 11, c = o - r * 12; voB[i] = (w * 16 + r) * 3072 + cb * 192 + ((c ^ ((r >> 3) << 1)) << 4); }
  unsigned mX = lb + w * 2048, mW = lb + w * 3072, mA = lb + w * 16384;
  const unsigned rot = (((rb + 2 * cb) & 7) * 10) >> 3;
  unsigned kx[10], kw_[10], voA2 = voA + 10240;
  auto kofs = [&](int s) {
    asm volatile("s_add_u32 %0, %2, %3\n\ts_cmp_ge_u32 %0, 10\n\ts_cselect_b32 %1, 10, 0\n\ts_sub_u32 %0, %0, %1\n\ts_mul_i32 %1, %0, 0x30000\n\ts_lshl_b32 %0, %0, 7"
                 : "=&s"(kx[s]), "=&s"(kw_[s]) : "s"(rot), "i"(s) : "scc");
  };
  kofs(0); kofs(1); kofs(2);
  asm volatile("" : "+v"(voA), "+v"(voA2), "+v"(voB[0]), "+v"(voB[1]), "+v"(voB[2]));
  asm volatile("" : "+s"(mX), "+s"(mW), "+s"(mA)); SB();
  asm volatile("s_waitcnt lgkmcnt(0)" : "+s"(ka0), "+s"(ka1) :: "memory"); SB();
  const char *x = (const char*)ka0[0], *kin = (const char*)ka0[1], *vin = (const char*)ka0[2], *shift_k = (const char*)ka0[3], *ssm_k = (const char*)ka0[4];
  const char *ssm_D = (const char*)ka0[5], *w_in = (const char*)ka0[6], *w_sp = (const char*)ka0[7], *b_sp = (const char*)ka1[0], *w_out = (const char*)ka1[1];
  char *out = (char*)ka1[2], *ws = (char*)ka1[3];
  const auto rX = __builtin_amdgcn_make_buffer_rsrc((void*)x, 0, S_ * D_ * 2, 0x00020000);
  const auto rW = __builtin_amdgcn_make_buffer_rsrc((void*)(w_in + 1536), 0, 640 * 3072 - 1536, 0x00020000);
  asm volatile("; named registers" :  :  : "v255", "a255"); SB();
  asm volatile("s_add_u32 m0, %0, 81920" :  : "s"(mX) : "memory", "scc"); SB();
  asm volatile("s_nop 0\n\tbuffer_load_dwordx4 %0, %1, %2 offen lds" :  : "v"(voA), "s"(rX), "s"(kx[0]) : "memory"); SB();
  asm volatile("s_add_u32 m0, %0, 82944" :  : "s"(mX) : "memory", "scc"); SB();
  asm volatile("s_nop 0\n\tbuffer_load_dwordx4 %0, %1, %2 offen lds" :  : "v"(voA2), "s"(rX), "s"(kx[0]) : "memory"); SB();
  asm volatile("s_add_u32 m0, %0, 49152" :  : "s"(mW) : "memory", "scc"); SB();
  asm volatile("s_nop 0\n\tbuffer_load_dwordx4 %0, %1, %2 offen lds" :  : "v"(voB[0]), "s"(rW), "s"(kw_[0]) : "memory"); SB();
  asm volatile("s_add_u32 m0, %0, 50176" :  : "s"(mW) : "memory", "scc"); SB();
  asm volatile("s_nop 0\n\tbuffer_load_dwordx4 %0, %1, %2 offen lds" :  : "v"(voB[1]), "s"(rW), "s"(kw_[0]) : "memory"); SB();
  asm volatile("s_add_u32 m0, %0, 51200" :  : "s"(mW) : "memory", "scc"); SB();
  asm volatile("s_nop 0\n\tbuffer_load_dwordx4 %0, %1, %2 offen lds" :  : "v"(voB[2]), "s"(rW), "s"(kw_[0]) : "memory"); SB();
  asm volatile("s_add_u32 m0, %0, 90112" :  : "s"(mX) : "memory", "scc"); SB();
  asm volatile("s_nop 0\n\tbuffer_load_dwordx4 %0, %1, %2 offen lds" :  : "v"(voA), "s"(rX), "s"(kx[1]) : "memory"); SB();
  asm volatile("s_add_u32 m0, %0, 91136" :  : "s"(mX) : "memory", "scc"); SB();
  asm volatile("s_nop 0\n\tbuffer_load_dwordx4 %0, %1, %2 offen lds" :  : "v"(voA2), "s"(rX), "s"(kx[1]) : "memory"); SB();
  asm volatile("s_add_u32 m0, %0, 61440" :  : "s"(mW) : "memory", "scc"); SB();
  asm volatile("s_nop 0\n\tbuffer_load_dwordx4 %0, %1, %2 offen lds" :  : "v"(voB[0]), "s"(rW), "s"(kw_[1]) : "memory"); SB();
  asm volatile("s_add_u32 m0, %0, 62464" :  : "s"(mW) : "memory", "scc"); SB();
  asm volatile("s_nop 0\n\tbuffer_load_dwordx4 %0, %1, %2 offen lds" :  : "v"(voB[1]), "s"(rW), "s"(kw_[1]) : "memory"); SB();
  asm volatile("s_add_u32 m0, %0, 63488" :  : "s"(mW) : "memory", "scc"); SB();
  asm volatile("s_nop 0\n\tbuffer_load_dwordx4 %0, %1, %2 offen lds" :  : "v"(voB[2]), "s"(rW), "s"(kw_[1]) : "memory"); SB();
  kofs(3); kofs(4); kofs(5); kofs(6); kofs(7); kofs(8); kofs(9);
  const auto rWS = __builtin_amdgcn_make_buffer_rsrc((void*)ws, 0, 32 << 20, 0x00020000);
  const auto rK = __builtin_amdgcn_make_buffer_rsrc((void*)kin, 0, S_ * D_ * 2, 0x00020000);
  const auto rV = __builtin_amdgcn_make_buffer_rsrc((void*)vin, 0, S_ * D_ * 2, 0x00020000);
  const auto rS = __builtin_amdgcn_make_buffer_rsrc((void*)shift_k, 0, D_ * 8, 0x00020000);
  const auto rO = __builtin_amdgcn_make_buffer_rsrc((void*)w_out, 0, 768 * 1280, 0x00020000);
  const auto rW1 = __builtin_amdgcn_make_buffer_rsrc((void*)w_in, 0, 640 * 3072, 0x00020000);
  { unsigned lane = lane0, w = w0, rb = rb0, cb = cb0;
  asm volatile("" : "+v"(lane)); asm volatile("" : "+s"(w), "+s"(rb), "+s"(cb)); SB();
  const unsigned wm = w >> 1, wn = w & 1, g4 = lane >> 4, l15 = lane & 15, q4 = l15 >> 2, p4 = l15 & 3;
  const unsigned nch = 3 - (rb >> 4), ch0 = cb * 80 + rb * 2 + min(rb, 16u);
  const unsigned l16 = lane * 16;
  const unsigned vZ = W_Z2 + ((rb * 2 + wm) * 48 + cb * 6 + wn * 3) * 1024 + l16;
  const unsigned vH = W_HH + ((rb * 4 + w) * 24 + cb * 3) * 1024 + l16;
  const unsigned vY = W_YY + ch0 * 4096 + w * 1024 + l16;
  const unsigned c2o = (nch - 2) * 2, c2s = 8192u + ((rb >> 4) << 30), w3s = (w + 1) >> 2 << 30;
  (void)wm; (void)wn; (void)g4; (void)q4; (void)p4; (void)vZ; (void)vH; (void)vY; (void)c2o; (void)c2s; (void)w3s;
  const auto rA = __builtin_amdgcn_make_buffer_rsrc((void*)w_sp, 0, S_ * S_ * 2, 0x00020000);
  u32x2 xv[5];
  unsigned ao[8];
#pragma unroll
  for (int n = 0; n < 8; ++n) ao[n] = (n ^ (2 * w)) * 32768;
  asm volatile("" : "+s"(ao[0]), "+s"(ao[1]), "+s"(ao[2]), "+s"(ao[3]), "+s"(ao[4]), "+s"(ao[5]), "+s"(ao[6]), "+s"(ao[7]));
  unsigned voK[4], voV[3], voH = cb * 640 + l16;
#pragma unroll
  for (int n = 0; n < 4; ++n) { const unsigned o = n * 64 + lane, r = (o * 205) >> 11; voK[n] = (rb * 64 + w * 16 - 4) * 1280 + cb * 160 + r * 1120 + o * 16; }
#pragma unroll
  for (int n = 0; n < 3; ++n) voV[n] = voK[n] + 5120;
  const unsigned wof = min(rb * 4 + w, 119u), wok = (wof * 205) >> 10;
  unsigned voO = (lane >> 1) * 1280 + (lane & 1) * 16 + (3 * (wok & 7) + (wok >> 3)) * 40960 + (wof - 5 * wok) * 32 + cb * 160;
  unsigned ka = lb + 106496 + w * 16384 + (4 * p4 + q4) * 160 + 8 * g4, oa = lb + 106496 + 7168 + w * 16384 + (4 * g4 + q4) * 32 + 8 * p4;
  unsigned sa = lb + 106496 + 3328 + w * 16384 + (4 * g4 + p4) * 8;
  unsigned kw = lb + 100352 + (4 * g4 + p4) * 144 + w * 32 + q4 * 8, kw2 = kw + (g4 >> 1) * 12928, kr = lb + 100352 + w * 16384 + (lane >> 3) * 144 + (lane & 7) * 16;
  unsigned vKV = W_KV + (cb * 80 + w * 24 + (lane >> 3)) * 4096 + rb * 128 + (lane & 7) * 16, vWO = W_WO + (cb * 120 + wof) * 1024 + l16;
  asm volatile("" : "+v"(ka), "+v"(oa), "+v"(sa), "+v"(kw), "+v"(kw2), "+v"(kr), "+v"(vKV), "+v"(vWO), "+v"(voK[0]), "+v"(voK[1]), "+v"(voK[2]), "+v"(voK[3]),
               "+v"(voV[0]), "+v"(voV[1]), "+v"(voV[2]), "+v"(voH), "+v"(voO));
  u32x2 kp[5], kc[5], vv[5], sw[5], wt[2], ko[5];
  u32x4 kq[3];
  unsigned aX[2][2], aW[2][2][3], aV[5];
#pragma unroll
  for (int p = 0; p < 2; ++p) {
#pragma unroll
    for (int i = 0; i < 2; ++i) aX[p][i] = lb + 98304 + (wm * 32 + (i ^ wn) * 16 + l15) * 128 + ((((p ^ wm) * 4 + g4) ^ (l15 & 7)) << 4);
#pragma unroll
    for (int j = 0; j < 3; ++j) {
      aW[0][p][j] = lb + ((p ^ wm) * 32 + 8 * g4 + q4) * 192 + (((wn * 3 + j) ^ (g4 & 1)) << 5) + 8 * p4;
      aW[1][p][j] = aW[0][p][j] + 49152;
    }
  }
#pragma unroll
  for (int j = 0; j < 5; ++j) { const unsigned c = cb * 10 + 2 * j + (g4 >> 1), t = (c >> 3) + 10 - rot; aV[j] = lb + 81920 + (t >= 10 ? t - 10 : t) * 8192 + (w * 16 + l15) * 128 + (((c ^ l15) & 7) << 4) + (g4 & 1) * 8; }
  u32x4 ones = {0x3F803F80u, 0x3F803F80u, 0x3F803F80u, 0x3F803F80u}, hole4 = {HOLE, HOLE, HOLE, HOLE};
  f32x4 a2[2][3], a1[2][3], rs, gr, c2[3], c1[3];
  unsigned voS = (rb * 64 + (lane >> 3)) * 4096 + w * 1024 + (((lane & 7) ^ (lane >> 3)) << 4), aA[2];
#pragma unroll
  for (int k = 0; k < 2; ++k) aA[k] = lb + 98304 + w * 16384 + l15 * 128 + (((k * 4 + g4) ^ (l15 & 7)) << 4);
  unsigned vH_ = vH, xw = lb + 98304 + w * 16384 + l15 * 8, xr_ = lb + 98304 + (w ^ 1) * 16384 + l15 * 8;
  unsigned ca = lb + 98304 + 1024 + w * 16384 + g4 * 16, cr = lb + 98304 + 1024 + (w ^ 2) * 16384 + g4 * 16;
  f32x4 co[3];
  unsigned vo = W_ST + ((rb * 2 + wm) * 8 + cb) * 1024 + wn * 128 + ((lane >> 5) ^ wn) * 512 + (l15 >> 3) * 256 + (l15 & 7) * 16;
  asm volatile("" : "+v"(ones), "+v"(hole4), "+v"(ca), "+v"(cr),
               "+v"(voS), "+v"(aA[0]), "+v"(aA[1]), "+v"(vH_), "+v"(xw), "+v"(xr_), "+v"(aV[0]), "+v"(aV[1]), "+v"(aV[2]), "+v"(aV[3]), "+v"(aV[4]), "+v"(vo));
  asm volatile("" : "+v"(aX[0][0]), "+v"(aX[0][1]), "+v"(aX[1][0]), "+v"(aX[1][1]), "+v"(aW[0][0][0]), "+v"(aW[0][0][1]), "+v"(aW[0][0][2]), "+v"(aW[0][1][0]), "+v"(aW[0][1][1]), "+v"(aW[0][1][2]),
               "+v"(aW[1][0][0]), "+v"(aW[1][0][1]), "+v"(aW[1][0][2]), "+v"(aW[1][1][0]), "+v"(aW[1][1][1]), "+v"(aW[1][1][2]));
  int tmo = 1 << 18;
  u32x2 fk, fz, fy;
  unsigned tg0, tg1;
  float xm[2], xd[2], k1[2], k2[2];
  f32x2 xo;
  auto kset = [&]() {
#pragma unroll
    for (int i = 0; i < 2; ++i) { const float r = __builtin_amdgcn_rcpf(xd[i]); k1[i] = -2.3022082f * r; k2[i] = (-0.10294324f * r) * (r * r); }
    asm volatile("" : "+v"(k1[0]), "+v"(k1[1]), "+v"(k2[0]), "+v"(k2[1]));
  };
  ST(1);
  SB();
  asm volatile("s_add_u32 m0, %0, 98304" :  : "s"(mX) : "memory", "scc"); SB();
  asm volatile("s_nop 0\n\tbuffer_load_dwordx4 %0, %1, %2 offen lds" :  : "v"(voA), "s"(rX), "s"(kx[2]) : "memory"); SB();
  asm volatile("s_add_u32 m0, %0, 99328" :  : "s"(mX) : "memory", "scc"); SB();
  asm volatile("s_nop 0\n\tbuffer_load_dwordx4 %0, %1, %2 offen lds" :  : "v"(voA2), "s"(rX), "s"(kx[2]) : "memory"); SB();
  asm volatile("s_add_u32 m0, %0, 106496" :  : "s"(mX) : "memory", "scc"); SB();
  asm volatile("s_nop 0\n\tbuffer_load_dwordx4 %0, %1, %2 offen lds" :  : "v"(voA), "s"(rX), "s"(kx[3]) : "memory"); SB();
  asm volatile("s_add_u32 m0, %0, 107520" :  : "s"(mX) : "memory", "scc"); SB();
  asm volatile("s_nop 0\n\tbuffer_load_dwordx4 %0, %1, %2 offen lds" :  : "v"(voA2), "s"(rX), "s"(kx[3]) : "memory"); SB();
  asm volatile("s_add_u32 m0, %0, 0" :  : "s"(mW) : "memory", "scc"); SB();
  asm volatile("s_nop 0\n\tbuffer_load_dwordx4 %0, %1, %2 offen lds" :  : "v"(voB[0]), "s"(rW), "s"(kw_[2]) : "memory"); SB();
  asm volatile("s_add_u32 m0, %0, 1024" :  : "s"(mW) : "memory", "scc"); SB();
  asm volatile("s_nop 0\n\tbuffer_load_dwordx4 %0, %1, %2 offen lds" :  : "v"(voB[1]), "s"(rW), "s"(kw_[2]) : "memory"); SB();
  asm volatile("s_waitcnt vmcnt(11) lgkmcnt(0)\n\ts_barrier" :  :  : "memory"); SB();
  ST(4);
  asm volatile("ds_read_b128 a[0:3], %0 offset:0" :  : "v"((aX[0][0] - 16384)) : "memory"); SB();
  asm volatile("ds_read_b128 a[4:7], %0 offset:0" :  : "v"((aX[0][1] - 16384)) : "memory"); SB();
  asm volatile("ds_read_b64_tr_b16 v[160:161], %0 offset:0" :  : "v"(aW[1][0][0]) : "memory"); SB();
  asm volatile("ds_read_b64_tr_b16 v[162:163], %0 offset:768" :  : "v"(aW[1][0][0]) : "memory"); SB();
  asm volatile("ds_read_b64_tr_b16 v[164:165], %0 offset:0" :  : "v"(aW[1][0][1]) : "memory"); SB();
  asm volatile("ds_read_b64_tr_b16 v[166:167], %0 offset:768" :  : "v"(aW[1][0][1]) : "memory"); SB();
  asm volatile("ds_read_b64_tr_b16 v[168:169], %0 offset:0" :  : "v"(aW[1][0][2]) : "memory"); SB();
  asm volatile("ds_read_b64_tr_b16 v[170:171], %0 offset:768" :  : "v"(aW[1][0][2]) : "memory"); SB();
  asm volatile("ds_read_b128 a[8:11], %0 offset:0" :  : "v"((aX[1][0] - 16384)) : "memory"); SB();
  asm volatile("ds_read_b128 a[12:15], %0 offset:0" :  : "v"((aX[1][1] - 16384)) : "memory"); SB();
  asm volatile("s_waitcnt lgkmcnt(6)\n\tv_mfma_f32_16x16x32_bf16 %0, v[160:163], a[0:3], 0" : "=&v"(a2[0][0]) :  : ); SB();
  asm volatile("ds_read_b64_tr_b16 v[172:173], %0 offset:0" :  : "v"(aW[1][1][0]) : "memory"); SB();
  asm volatile("s_add_u32 m0, %0, 2048" :  : "s"(mW) : "memory", "scc"); SB();
  asm volatile("v_mfma_f32_16x16x32_bf16 %0, v[160:163], a[4:7], 0" : "=&v"(a2[1][0]) :  : ); SB();
  asm volatile("ds_read_b64_tr_b16 v[174:175], %0 offset:768" :  : "v"(aW[1][1][0]) : "memory"); SB();
  asm volatile("buffer_load_dwordx4 %0, %1, %2 offen lds" :  : "v"(voB[2]), "s"(rW), "s"(kw_[2]) : "memory"); SB();
  asm volatile("v_mfma_f32_16x16x32_bf16 %0, v[160:163], %1, 0" : "=&v"(c2[0]) : "v"(ones) : ); SB();
  asm volatile("ds_read_b64_tr_b16 v[176:177], %0 offset:0" :  : "v"(aW[1][1][1]) : "memory"); SB();
  asm volatile("s_add_u32 m0, %0, 114688" :  : "s"(mX) : "memory", "scc"); SB();
  asm volatile("s_waitcnt lgkmcnt(7)\n\tv_mfma_f32_16x16x32_bf16 %0, v[164:167], a[0:3], 0" : "=&v"(a2[0][1]) :  : ); SB();
  asm volatile("ds_read_b64_tr_b16 v[178:179], %0 offset:768" :  : "v"(aW[1][1][1]) : "memory"); SB();
  asm volatile("buffer_load_dwordx4 %0, %1, %2 offen lds" :  : "v"(voA), "s"(rX), "s"(kx[4]) : "memory"); SB();
  asm volatile("v_mfma_f32_16x16x32_bf16 %0, v[164:167], a[4:7], 0" : "=&v"(a2[1][1]) :  : ); SB();
  asm volatile("ds_read_b64_tr_b16 v[180:181], %0 offset:0" :  : "v"(aW[1][1][2]) : "memory"); SB();
  asm volatile("s_add_u32 m0, %0, 115712" :  : "s"(mX) : "memory", "scc"); SB();
  asm volatile("v_mfma_f32_16x16x32_bf16 %0, v[164:167], %1, 0" : "=&v"(c2[1]) : "v"(ones) : ); SB();
  asm volatile("ds_read_b64_tr_b16 v[182:183], %0 offset:768" :  : "v"(aW[1][1][2]) : "memory"); SB();
  asm volatile("s_waitcnt lgkmcnt(8)\n\tv_mfma_f32_16x16x32_bf16 %0, v[168:171], a[0:3], 0" : "=&v"(a2[0][2]) :  : ); SB();
  asm volatile("buffer_load_dwordx4 %0, %1, %2 offen lds" :  : "v"(voA2), "s"(rX), "s"(kx[4]) : "memory"); SB();
  asm volatile("s_add_u32 m0, %0, 122880" :  : "s"(mX) : "memory", "scc"); SB();
  asm volatile("s_nop 0\n\tbuffer_load_dwordx4 %0, %1, %2 offen lds" :  : "v"(voA), "s"(rX), "s"(kx[5]) : "memory"); SB();
  asm volatile("s_add_u32 m0, %0, 123904" :  : "s"(mX) : "memory", "scc"); SB();
  asm volatile("s_nop 0\n\tbuffer_load_dwordx4 %0, %1, %2 offen lds" :  : "v"(voA2), "s"(rX), "s"(kx[5]) : "memory"); SB();
  asm volatile("s_waitcnt vmcnt(11)\n\ts_barrier" :  :  : "memory"); SB();
  asm volatile("ds_read_b64_tr_b16 v[184:185], %0 offset:12288" :  : "v"(aW[1][0][0]) : "memory"); SB();
  asm volatile("ds_read_b128 a[16:19], %0 offset:8192" :  : "v"((aX[0][0] - 16384)) : "memory"); SB();
  asm volatile("v_mfma_f32_16x16x32_bf16 %0, v[168:171], a[4:7], 0" : "=&v"(a2[1][2]) :  : ); SB();
  asm volatile("ds_read_b64_tr_b16 v[186:187], %0 offset:13056" :  : "v"(aW[1][0][0]) : "memory"); SB();
  asm volatile("ds_read_b128 a[20:23], %0 offset:8192" :  : "v"((aX[0][1] - 16384)) : "memory"); SB();
  asm volatile("v_mfma_f32_16x16x32_bf16 %0, v[168:171], %1, 0" : "=&v"(c2[2]) : "v"(ones) : ); SB();
  asm volatile("ds_read_b64_tr_b16 v[188:189], %0 offset:12288" :  : "v"(aW[1][0][1]) : "memory"); SB();
  asm volatile("ds_read_b128 a[24:27], %0 offset:8192" :  : "v"((aX[1][0] - 16384)) : "memory"); SB();
  asm volatile("v_mfma_f32_16x16x32_bf16 %0, %1, a[0:3], 0" : "=&v"(rs) : "v"(ones) : ); SB();
  asm volatile("ds_read_b64_tr_b16 v[190:191], %0 offset:13056" :  : "v"(aW[1][0][1]) : "memory"); SB();
  asm volatile("ds_read_b128 a[28:31], %0 offset:8192" :  : "v"((aX[1][1] - 16384)) : "memory"); SB();
  asm volatile("v_mfma_f32_16x16x32_bf16 %0, a[0:3], a[0:3], 0" : "=&v"(gr) :  : ); SB();
  asm volatile("ds_read_b64_tr_b16 v[192:193], %0 offset:12288" :  : "v"(aW[1][0][2]) : "memory"); SB();
  asm volatile("s_add_u32 m0, %0, 12288" :  : "s"(mW) : "memory", "scc"); SB();
  asm volatile("s_waitcnt lgkmcnt(13)\n\tv_mfma_f32_16x16x32_bf16 %0, v[172:175], a[8:11], %0" : "+v"(a2[0][0]) :  : ); SB();
  asm volatile("ds_read_b64_tr_b16 v[194:195], %0 offset:13056" :  : "v"(aW[1][0][2]) : "memory"); SB();
  asm volatile("buffer_load_dwordx4 %0, %1, %2 offen lds" :  : "v"(voB[0]), "s"(rW), "s"(kw_[3]) : "memory"); SB();
  asm volatile("v_mfma_f32_16x16x32_bf16 %0, v[172:175], a[12:15], %0" : "+v"(a2[1][0]) :  : ); SB();
  asm volatile("s_add_u32 m0, %0, 13312" :  : "s"(mW) : "memory", "scc"); SB();
  asm volatile("s_waitcnt lgkmcnt(12)\n\tv_mfma_f32_16x16x32_bf16 %0, v[176:179], a[8:11], %0" : "+v"(a2[0][1]) :  : ); SB();
  asm volatile("buffer_load_dwordx4 %0, %1, %2 offen lds" :  : "v"(voB[1]), "s"(rW), "s"(kw_[3]) : "memory"); SB();
  asm volatile("s_add_u32 m0, %0, 14336" :  : "s"(mW) : "memory", "scc"); SB();
  asm volatile("v_mfma_f32_16x16x32_bf16 %0, v[176:179], a[12:15], %0" : "+v"(a2[1][1]) :  : ); SB();
  asm volatile("buffer_load_dwordx4 %0, %1, %2 offen lds" :  : "v"(voB[2]), "s"(rW), "s"(kw_[3]) : "memory"); SB();
  asm volatile("s_waitcnt lgkmcnt(10)\n\tv_mfma_f32_16x16x32_bf16 %0, v[180:183], a[8:11], %0" : "+v"(a2[0][2]) :  : ); SB();
  asm volatile("v_mfma_f32_16x16x32_bf16 %0, v[180:183], a[12:15], %0" : "+v"(a2[1][2]) :  : ); SB();
  asm volatile("v_mfma_f32_16x16x32_bf16 %0, %1, a[8:11], %0" : "+v"(rs) : "v"(ones) : ); SB();
  asm volatile("ds_read_b64_tr_b16 v[160:161], %0 offset:12288" :  : "v"(aW[1][1][0]) : "memory"); SB();
  asm volatile("v_mfma_f32_16x16x32_bf16 %0, a[8:11], a[8:11], %0" : "+v"(gr) :  : ); SB();
  asm volatile("ds_read_b64_tr_b16 v[162:163], %0 offset:13056" :  : "v"(aW[1][1][0]) : "memory"); SB();
  asm volatile("s_waitcnt lgkmcnt(8)\n\tv_mfma_f32_16x16x32_bf16 %0, v[184:187], a[16:19], %0" : "+v"(a2[0][0]) :  : ); SB();
  asm volatile("ds_read_b64_tr_b16 v[164:165], %0 offset:12288" :  : "v"(aW[1][1][1]) : "memory"); SB();
  asm volatile("v_mfma_f32_16x16x32_bf16 %0, v[184:187], a[20:23], %0" : "+v"(a2[1][0]) :  : ); SB();
  asm volatile("ds_read_b64_tr_b16 v[166:167], %0 offset:13056" :  : "v"(aW[1][1][1]) : "memory"); SB();
  asm volatile("v_mfma_f32_16x16x32_bf16 %0, v[184:187], %1, %0" : "+v"(c2[0]) : "v"(ones) : ); SB();
  asm volatile("ds_read_b64_tr_b16 v[168:169], %0 offset:12288" :  : "v"(aW[1][1][2]) : "memory"); SB();
  asm volatile("s_waitcnt lgkmcnt(8)\n\tv_mfma_f32_16x16x32_bf16 %0, v[188:191], a[16:19], %0" : "+v"(a2[0][1]) :  : ); SB();
  asm volatile("ds_read_b64_tr_b16 v[170:171], %0 offset:13056" :  : "v"(aW[1][1][2]) : "memory"); SB();
  asm volatile("v_mfma_f32_16x16x32_bf16 %0, v[188:191], a[20:23], %0" : "+v"(a2[1][1]) :  : ); SB();
  asm volatile("v_mfma_f32_16x16x32_bf16 %0, v[188:191], %1, %0" : "+v"(c2[1]) : "v"(ones) : ); SB();
  asm volatile("s_waitcnt lgkmcnt(6)\n\tv_mfma_f32_16x16x32_bf16 %0, v[192:195], a[16:19], %0" : "+v"(a2[0][2]) :  : ); SB();
  asm volatile("s_add_u32 m0, %0, 131072" :  : "s"(mX) : "memory", "scc"); SB();
  asm volatile("s_nop 0\n\tbuffer_load_dwordx4 %0, %1, %2 offen lds" :  : "v"(voA), "s"(rX), "s"(kx[6]) : "memory"); SB();
  asm volatile("s_add_u32 m0, %0, 132096" :  : "s"(mX) : "memory", "scc"); SB();
  asm volatile("s_nop 0\n\tbuffer_load_dwordx4 %0, %1, %2 offen lds" :  : "v"(voA2), "s"(rX), "s"(kx[6]) : "memory"); SB();
  asm volatile("s_add_u32 m0, %0, 139264" :  : "s"(mX) : "memory", "scc"); SB();
  asm volatile("s_nop 0\n\tbuffer_load_dwordx4 %0, %1, %2 offen lds" :  : "v"(voA), "s"(rX), "s"(kx[7]) : "memory"); SB();
  asm volatile("s_add_u32 m0, %0, 140288" :  : "s"(mX) : "memory", "scc"); SB();
  asm volatile("s_nop 0\n\tbuffer_load_dwordx4 %0, %1, %2 offen lds" :  : "v"(voA2), "s"(rX), "s"(kx[7]) : "memory"); SB();
  asm volatile("s_waitcnt vmcnt(11)\n\ts_barrier" :  :  : "memory"); SB();
  asm volatile("ds_read_b64_tr_b16 v[172:173], %0 offset:0" :  : "v"(aW[0][0][0]) : "memory"); SB();
  asm volatile("ds_read_b128 a[32:35], %0 offset:0" :  : "v"(aX[0][0]) : "memory"); SB();
  asm volatile("v_mfma_f32_16x16x32_bf16 %0, v[192:195], a[20:23], %0" : "+v"(a2[1][2]) :  : ); SB();
  asm volatile("ds_read_b64_tr_b16 v[174:175], %0 offset:768" :  : "v"(aW[0][0][0]) : "memory"); SB();
  asm volatile("ds_read_b128 a[36:39], %0 offset:0" :  : "v"(aX[0][1]) : "memory"); SB();
  asm volatile("v_mfma_f32_16x16x32_bf16 %0, v[192:195], %1, %0" : "+v"(c2[2]) : "v"(ones) : ); SB();
  asm volatile("ds_read_b64_tr_b16 v[176:177], %0 offset:0" :  : "v"(aW[0][0][1]) : "memory"); SB();
  asm volatile("ds_read_b128 a[40:43], %0 offset:0" :  : "v"(aX[1][0]) : "memory"); SB();
  asm volatile("v_mfma_f32_16x16x32_bf16 %0, %1, a[16:19], %0" : "+v"(rs) : "v"(ones) : ); SB();
  asm volatile("ds_read_b64_tr_b16 v[178:179], %0 offset:768" :  : "v"(aW[0][0][1]) : "memory"); SB();
  asm volatile("ds_read_b128 a[44:47], %0 offset:0" :  : "v"(aX[1][1]) : "memory"); SB();
  asm volatile("v_mfma_f32_16x16x32_bf16 %0, a[16:19], a[16:19], %0" : "+v"(gr) :  : ); SB();
  asm volatile("ds_read_b64_tr_b16 v[180:181], %0 offset:0" :  : "v"(aW[0][0][2]) : "memory"); SB();
  asm volatile("s_add_u32 m0, %0, 24576" :  : "s"(mW) : "memory", "scc"); SB();
  asm volatile("s_waitcnt lgkmcnt(13)\n\tv_mfma_f32_16x16x32_bf16 %0, v[160:163], a[24:27], %0" : "+v"(a2[0][0]) :  : ); SB();
  asm volatile("ds_read_b64_tr_b16 v[182:183], %0 offset:768" :  : "v"(aW[0][0][2]) : "memory"); SB();
  asm volatile("ds_read_b128 a[48:51], %0 offset:8192" :  : "v"(aX[0][0]) : "memory"); SB();
  asm volatile("v_mfma_f32_16x16x32_bf16 %0, v[160:163], a[28:31], %0" : "+v"(a2[1][0]) :  : ); SB();
  asm volatile("ds_read_b128 a[52:55], %0 offset:8192" :  : "v"(aX[0][1]) : "memory"); SB();
  asm volatile("s_waitcnt lgkmcnt(14)\n\tv_mfma_f32_16x16x32_bf16 %0, v[164:167], a[24:27], %0" : "+v"(a2[0][1]) :  : ); SB();
  asm volatile("ds_read_b128 a[56:59], %0 offset:8192" :  : "v"(aX[1][0]) : "memory"); SB();
  asm volatile("ds_read_b128 a[60:63], %0 offset:8192" :  : "v"(aX[1][1]) : "memory"); SB();
  asm volatile("v_mfma_f32_16x16x32_bf16 %0, v[164:167], a[28:31], %0" : "+v"(a2[1][1]) :  : ); SB();
  asm volatile("buffer_load_dwordx4 %0, %1, %2 offen lds" :  : "v"(voB[0]), "s"(rW), "s"(kw_[4]) : "memory"); SB();
  asm volatile("s_add_u32 m0, %0, 25600" :  : "s"(mW) : "memory", "scc"); SB();
  asm volatile("s_waitcnt lgkmcnt(14)\n\tv_mfma_f32_16x16x32_bf16 %0, v[168:171], a[24:27], %0" : "+v"(a2[0][2]) :  : ); SB();
  asm volatile("buffer_load_dwordx4 %0, %1, %2 offen lds" :  : "v"(voB[1]), "s"(rW), "s"(kw_[4]) : "memory"); SB();
  asm volatile("s_add_u32 m0, %0, 26624" :  : "s"(mW) : "memory", "scc"); SB();
  asm volatile("v_mfma_f32_16x16x32_bf16 %0, v[168:171], a[28:31], %0" : "+v"(a2[1][2]) :  : ); SB();
  asm volatile("buffer_load_dwordx4 %0, %1, %2 offen lds" :  : "v"(voB[2]), "s"(rW), "s"(kw_[4]) : "memory"); SB();
  asm volatile("s_add_u32 m0, %0, 147456" :  : "s"(mX) : "memory", "scc"); SB();
  asm volatile("v_mfma_f32_16x16x32_bf16 %0, %1, a[24:27], %0" : "+v"(rs) : "v"(ones) : ); SB();
  asm volatile("buffer_load_dwordx4 %0, %1, %2 offen lds" :  : "v"(voA), "s"(rX), "s"(kx[8]) : "memory"); SB();
  asm volatile("v_mfma_f32_16x16x32_bf16 %0, a[24:27], a[24:27], %0" : "+v"(gr) :  : ); SB();
  asm volatile("s_waitcnt lgkmcnt(10)\n\tv_mfma_f32_16x16x32_bf16 %0, v[172:175], a[32:35], %0" : "+v"(a2[0][0]) :  : ); SB();
  asm volatile("ds_read_b64_tr_b16 v[184:185], %0 offset:0" :  : "v"(aW[0][1][0]) : "memory"); SB();
  asm volatile("v_mfma_f32_16x16x32_bf16 %0, v[172:175], a[36:39], %0" : "+v"(a2[1][0]) :  : ); SB();
  asm volatile("ds_read_b64_tr_b16 v[186:187], %0 offset:768" :  : "v"(aW[0][1][0]) : "memory"); SB();
  asm volatile("v_mfma_f32_16x16x32_bf16 %0, v[172:175], %1, %0" : "+v"(c2[0]) : "v"(ones) : ); SB();
  asm volatile("ds_read_b64_tr_b16 v[188:189], %0 offset:0" :  : "v"(aW[0][1][1]) : "memory"); SB();
  asm volatile("s_add_u32 m0, %0, 148480" :  : "s"(mX) : "memory", "scc"); SB();
  asm volatile("s_waitcnt lgkmcnt(10)\n\tv_mfma_f32_16x16x32_bf16 %0, v[176:179], a[32:35], %0" : "+v"(a2[0][1]) :  : ); SB();
  asm volatile("ds_read_b64_tr_b16 v[190:191], %0 offset:768" :  : "v"(aW[0][1][1]) : "memory"); SB();
  asm volatile("buffer_load_dwordx4 %0, %1, %2 offen lds" :  : "v"(voA2), "s"(rX), "s"(kx[8]) : "memory"); SB();
  asm volatile("v_mfma_f32_16x16x32_bf16 %0, v[176:179], a[36:39], %0" : "+v"(a2[1][1]) :  : ); SB();
  asm volatile("ds_read_b64_tr_b16 v[192:193], %0 offset:0" :  : "v"(aW[0][1][2]) : "memory"); SB();
  asm volatile("v_mfma_f32_16x16x32_bf16 %0, v[176:179], %1, %0" : "+v"(c2[1]) : "v"(ones) : ); SB();
  asm volatile("ds_read_b64_tr_b16 v[194:195], %0 offset:768" :  : "v"(aW[0][1][2]) : "memory"); SB();
  asm volatile("s_waitcnt lgkmcnt(10)\n\tv_mfma_f32_16x16x32_bf16 %0, v[180:183], a[32:35], %0" : "+v"(a2[0][2]) :  : ); SB();
  asm volatile("v_mfma_f32_16x16x32_bf16 %0, v[180:183], a[36:39], %0" : "+v"(a2[1][2]) :  : ); SB();
  asm volatile("v_mfma_f32_16x16x32_bf16 %0, v[180:183], %1, %0" : "+v"(c2[2]) : "v"(ones) : ); SB();
  asm volatile("v_mfma_f32_16x16x32_bf16 %0, %1, a[32:35], %0" : "+v"(rs) : "v"(ones) : ); SB();
  asm volatile("s_add_u32 m0, %0, 155648" :  : "s"(mX) : "memory", "scc"); SB();
  asm volatile("s_nop 0\n\tbuffer_load_dwordx4 %0, %1, %2 offen lds" :  : "v"(voA), "s"(rX), "s"(kx[9]) : "memory"); SB();
  asm volatile("v_mfma_f32_16x16x32_bf16 %0, a[32:35], a[32:35], %0" : "+v"(gr) :  : ); SB();
  asm volatile("s_add_u32 m0, %0, 156672" :  : "s"(mX) : "memory", "scc"); SB();
  asm volatile("s_nop 0\n\tbuffer_load_dwordx4 %0, %1, %2 offen lds" :  : "v"(voA2), "s"(rX), "s"(kx[9]) : "memory"); SB();
  asm volatile("s_waitcnt vmcnt(11)\n\ts_barrier" :  :  : "memory"); SB();
  asm volatile("ds_read_b64_tr_b16 v[160:161], %0 offset:12288" :  : "v"(aW[0][0][0]) : "memory"); SB();
  asm volatile("ds_read_b128 a[64:67], %0 offset:16384" :  : "v"(aX[0][0]) : "memory"); SB();
  asm volatile("v_mfma_f32_16x16x32_bf16 %0, %1, a[40:43], %0" : "+v"(rs) : "v"(ones) : ); SB();
  asm volatile("ds_read_b64_tr_b16 v[162:163], %0 offset:13056" :  : "v"(aW[0][0][0]) : "memory"); SB();
  asm volatile("ds_read_b128 a[68:71], %0 offset:16384" :  : "v"(aX[0][1]) : "memory"); SB();
  asm volatile("v_mfma_f32_16x16x32_bf16 %0, a[40:43], a[40:43], %0" : "+v"(gr) :  : ); SB();
  asm volatile("ds_read_b64_tr_b16 v[164:165], %0 offset:12288" :  : "v"(aW[0][0][1]) : "memory"); SB();
  asm volatile("s_add_u32 m0, %0, 36864" :  : "s"(mW) : "memory", "scc"); SB();
  asm volatile("s_waitcnt lgkmcnt(9)\n\tv_mfma_f32_16x16x32_bf16 %0, v[184:187], a[40:43], %0" : "+v"(a2[0][0]) :  : ); SB();
  asm volatile("ds_read_b64_tr_b16 v[166:167], %0 offset:13056" :  : "v"(aW[0][0][1]) : "memory"); SB();
  asm volatile("ds_read_b128 a[72:75], %0 offset:16384" :  : "v"(aX[1][0]) : "memory"); SB();
  asm volatile("v_mfma_f32_16x16x32_bf16 %0, v[184:187], a[44:47], %0" : "+v"(a2[1][0]) :  : ); SB();
  asm volatile("ds_read_b64_tr_b16 v[168:169], %0 offset:12288" :  : "v"(aW[0][0][2]) : "memory"); SB();
  asm volatile("s_waitcnt lgkmcnt(10)\n\tv_mfma_f32_16x16x32_bf16 %0, v[188:191], a[40:43], %0" : "+v"(a2[0][1]) :  : ); SB();
  asm volatile("ds_read_b64_tr_b16 v[170:171], %0 offset:13056" :  : "v"(aW[0][0][2]) : "memory"); SB();
  asm volatile("ds_read_b128 a[76:79], %0 offset:16384" :  : "v"(aX[1][1]) : "memory"); SB();
  asm volatile("v_mfma_f32_16x16x32_bf16 %0, v[188:191], a[44:47], %0" : "+v"(a2[1][1]) :  : ); SB();
  asm volatile("ds_read_b128 a[80:83], %0 offset:24576" :  : "v"(aX[0][0]) : "memory"); SB();
  asm volatile("s_waitcnt lgkmcnt(11)\n\tv_mfma_f32_16x16x32_bf16 %0, v[192:195], a[40:43], %0" : "+v"(a2[0][2]) :  : ); SB();
  asm volatile("ds_read_b128 a[84:87], %0 offset:24576" :  : "v"(aX[0][1]) : "memory"); SB();
  asm volatile("ds_read_b128 a[88:91], %0 offset:24576" :  : "v"(aX[1][0]) : "memory"); SB();
  asm volatile("v_mfma_f32_16x16x32_bf16 %0, v[192:195], a[44:47], %0" : "+v"(a2[1][2]) :  : ); SB();
  asm volatile("ds_read_b128 a[92:95], %0 offset:24576" :  : "v"(aX[1][1]) : "memory"); SB();
  asm volatile("buffer_load_dwordx4 %0, %1, %2 offen lds" :  : "v"(voB[0]), "s"(rW), "s"(kw_[5]) : "memory"); SB();
  asm volatile("v_mfma_f32_16x16x32_bf16 %0, %1, a[48:51], %0" : "+v"(rs) : "v"(ones) : ); SB();
  asm volatile("s_add_u32 m0, %0, 37888" :  : "s"(mW) : "memory", "scc"); SB();
  asm volatile("s_nop 0\n\tbuffer_load_dwordx4 %0, %1, %2 offen lds" :  : "v"(voB[1]), "s"(rW), "s"(kw_[5]) : "memory"); SB();
  asm volatile("v_mfma_f32_16x16x32_bf16 %0, a[48:51], a[48:51], %0" : "+v"(gr) :  : ); SB();
  asm volatile("v_mfma_f32_16x16x32_bf16 %0, %1, a[56:59], %0" : "+v"(rs) : "v"(ones) : ); SB();
  asm volatile("v_mfma_f32_16x16x32_bf16 %0, a[56:59], a[56:59], %0" : "+v"(gr) :  : ); SB();
  asm volatile("s_add_u32 m0, %0, 38912" :  : "s"(mW) : "memory", "scc"); SB();
  asm volatile("s_waitcnt lgkmcnt(11)\n\tv_mfma_f32_16x16x32_bf16 %0, v[160:163], a[48:51], %0" : "+v"(a2[0][0]) :  : ); SB();
  asm volatile("ds_read_b64_tr_b16 v[172:173], %0 offset:12288" :  : "v"(aW[0][1][0]) : "memory"); SB();
  asm volatile("buffer_load_dwordx4 %0, %1, %2 offen lds" :  : "v"(voB[2]), "s"(rW), "s"(kw_[5]) : "memory"); SB();
  asm volatile("v_mfma_f32_16x16x32_bf16 %0, v[160:163], a[52:55], %0" : "+v"(a2[1][0]) :  : ); SB();
  asm volatile("ds_read_b64_tr_b16 v[174:175], %0 offset:13056" :  : "v"(aW[0][1][0]) : "memory"); SB();
  asm volatile("v_mfma_f32_16x16x32_bf16 %0, v[160:163], %1, %0" : "+v"(c2[0]) : "v"(ones) : ); SB();
  asm volatile("ds_read_b64_tr_b16 v[176:177], %0 offset:12288" :  : "v"(aW[0][1][1]) : "memory"); SB();
  asm volatile("s_waitcnt lgkmcnt(11)\n\tv_mfma_f32_16x16x32_bf16 %0, v[164:167], a[48:51], %0" : "+v"(a2[0][1]) :  : ); SB();
  asm volatile("ds_read_b64_tr_b16 v[178:179], %0 offset:13056" :  : "v"(aW[0][1][1]) : "memory"); SB();
  asm volatile("v_mfma_f32_16x16x32_bf16 %0, v[164:167], a[52:55], %0" : "+v"(a2[1][1]) :  : ); SB();
  asm volatile("ds_read_b64_tr_b16 v[180:181], %0 offset:12288" :  : "v"(aW[0][1][2]) : "memory"); SB();
  asm volatile("v_mfma_f32_16x16x32_bf16 %0, v[164:167], %1, %0" : "+v"(c2[1]) : "v"(ones) : ); SB();
  asm volatile("ds_read_b64_tr_b16 v[182:183], %0 offset:13056" :  : "v"(aW[0][1][2]) : "memory"); SB();
  asm volatile("s_add_u32 m0, %0, 49152" :  : "s"(mW) : "memory", "scc"); SB();
  asm volatile("s_waitcnt lgkmcnt(11)\n\tv_mfma_f32_16x16x32_bf16 %0, v[168:171], a[48:51], %0" : "+v"(a2[0][2]) :  : ); SB();
  asm volatile("buffer_load_dwordx4 %0, %1, %2 offen lds" :  : "v"(voB[0]), "s"(rW), "s"(kw_[6]) : "memory"); SB();
  asm volatile("v_mfma_f32_16x16x32_bf16 %0, v[168:171], a[52:55], %0" : "+v"(a2[1][2]) :  : ); SB();
  asm volatile("v_mfma_f32_16x16x32_bf16 %0, v[168:171], %1, %0" : "+v"(c2[2]) : "v"(ones) : ); SB();
  asm volatile("v_mfma_f32_16x16x32_bf16 %0, %1, a[64:67], %0" : "+v"(rs) : "v"(ones) : ); SB();
  asm volatile("v_mfma_f32_16x16x32_bf16 %0, a[64:67], a[64:67], %0" : "+v"(gr) :  : ); SB();
  asm volatile("s_add_u32 m0, %0, 50176" :  : "s"(mW) : "memory", "scc"); SB();
  asm volatile("s_nop 0\n\tbuffer_load_dwordx4 %0, %1, %2 offen lds" :  : "v"(voB[1]), "s"(rW), "s"(kw_[6]) : "memory"); SB();
  asm volatile("s_add_u32 m0, %0, 51200" :  : "s"(mW) : "memory", "scc"); SB();
  asm volatile("s_nop 0\n\tbuffer_load_dwordx4 %0, %1, %2 offen lds" :  : "v"(voB[2]), "s"(rW), "s"(kw_[6]) : "memory"); SB();
  asm volatile("s_add_u32 m0, %0, 61440" :  : "s"(mW) : "memory", "scc"); SB();
  asm volatile("s_nop 0\n\tbuffer_load_dwordx4 %0, %1, %2 offen lds" :  : "v"(voB[0]), "s"(rW), "s"(kw_[7]) : "memory"); SB();
  asm volatile("s_waitcnt vmcnt(11)\n\ts_barrier" :  :  : "memory"); SB();
  asm volatile("ds_read_b64_tr_b16 v[184:185], %0 offset:24576" :  : "v"(aW[0][0][0]) : "memory"); SB();
  asm volatile("s_add_u32 m0, %0, 62464" :  : "s"(mW) : "memory", "scc"); SB();
  asm volatile("s_waitcnt lgkmcnt(11)\n\tv_mfma_f32_16x16x32_bf16 %0, %1, a[72:75], %0" : "+v"(rs) : "v"(ones) : ); SB();
  asm volatile("ds_read_b64_tr_b16 v[186:187], %0 offset:25344" :  : "v"(aW[0][0][0]) : "memory"); SB();
  asm volatile("ds_read_b128 a[96:99], %0 offset:32768" :  : "v"(aX[0][0]) : "memory"); SB();
  asm volatile("v_mfma_f32_16x16x32_bf16 %0, a[72:75], a[72:75], %0" : "+v"(gr) :  : ); SB();
  asm volatile("ds_read_b64_tr_b16 v[188:189], %0 offset:24576" :  : "v"(aW[0][0][1]) : "memory"); SB();
  asm volatile("s_waitcnt lgkmcnt(8)\n\tv_mfma_f32_16x16x32_bf16 %0, v[172:175], a[56:59], %0" : "+v"(a2[0][0]) :  : ); SB();
  asm volatile("ds_read_b64_tr_b16 v[190:191], %0 offset:25344" :  : "v"(aW[0][0][1]) : "memory"); SB();
  asm volatile("ds_read_b128 a[100:103], %0 offset:32768" :  : "v"(aX[0][1]) : "memory"); SB();
  asm volatile("v_mfma_f32_16x16x32_bf16 %0, v[172:175], a[60:63], %0" : "+v"(a2[1][0]) :  : ); SB();
  asm volatile("ds_read_b64_tr_b16 v[192:193], %0 offset:24576" :  : "v"(aW[0][0][2]) : "memory"); SB();
  asm volatile("s_waitcnt lgkmcnt(9)\n\tv_mfma_f32_16x16x32_bf16 %0, v[176:179], a[56:59], %0" : "+v"(a2[0][1]) :  : ); SB();
  asm volatile("ds_read_b64_tr_b16 v[194:195], %0 offset:25344" :  : "v"(aW[0][0][2]) : "memory"); SB();
  asm volatile("ds_read_b128 a[104:107], %0 offset:32768" :  : "v"(aX[1][0]) : "memory"); SB();
  asm volatile("v_mfma_f32_16x16x32_bf16 %0, v[176:179], a[60:63], %0" : "+v"(a2[1][1]) :  : ); SB();
  asm volatile("ds_read_b128 a[108:111], %0 offset:32768" :  : "v"(aX[1][1]) : "memory"); SB();
  asm volatile("s_waitcnt lgkmcnt(10)\n\tv_mfma_f32_16x16x32_bf16 %0, v[180:183], a[56:59], %0" : "+v"(a2[0][2]) :  : ); SB();
  asm volatile("ds_read_b128 a[112:115], %0 offset:40960" :  : "v"(aX[0][0]) : "memory"); SB();
  asm volatile("ds_read_b128 a[116:119], %0 offset:40960" :  : "v"(aX[0][1]) : "memory"); SB();
  asm volatile("v_mfma_f32_16x16x32_bf16 %0, v[180:183], a[60:63], %0" : "+v"(a2[1][2]) :  : ); SB();
  asm volatile("ds_read_b128 a[120:123], %0 offset:40960" :  : "v"(aX[1][0]) : "memory"); SB();
  asm volatile("ds_read_b128 a[124:127], %0 offset:40960" :  : "v"(aX[1][1]) : "memory"); SB();
  asm volatile("v_mfma_f32_16x16x32_bf16 %0, %1, a[80:83], %0" : "+v"(rs) : "v"(ones) : ); SB();
  asm volatile("buffer_load_dwordx4 %0, %1, %2 offen lds" :  : "v"(voB[1]), "s"(rW), "s"(kw_[7]) : "memory"); SB();
  asm volatile("s_add_u32 m0, %0, 63488" :  : "s"(mW) : "memory", "scc"); SB();
  asm volatile("v_mfma_f32_16x16x32_bf16 %0, a[80:83], a[80:83], %0" : "+v"(gr) :  : ); SB();
  asm volatile("buffer_load_dwordx4 %0, %1, %2 offen lds" :  : "v"(voB[2]), "s"(rW), "s"(kw_[7]) : "memory"); SB();
  asm volatile("s_add_u32 m0, %0, 0" :  : "s"(mW) : "memory", "scc"); SB();
  asm volatile("v_mfma_f32_16x16x32_bf16 %0, %1, a[88:91], %0" : "+v"(rs) : "v"(ones) : ); SB();
  asm volatile("buffer_load_dwordx4 %0, %1, %2 offen lds" :  : "v"(voB[0]), "s"(rW), "s"(kw_[8]) : "memory"); SB();
  asm volatile("s_add_u32 m0, %0, 1024" :  : "s"(mW) : "memory", "scc"); SB();
  asm volatile("v_mfma_f32_16x16x32_bf16 %0, a[88:91], a[88:91], %0" : "+v"(gr) :  : ); SB();
  asm volatile("buffer_load_dwordx4 %0, %1, %2 offen lds" :  : "v"(voB[1]), "s"(rW), "s"(kw_[8]) : "memory"); SB();
  asm volatile("s_waitcnt lgkmcnt(12)\n\tv_mfma_f32_16x16x32_bf16 %0, v[184:187], a[64:67], %0" : "+v"(a2[0][0]) :  : ); SB();
  asm volatile("ds_read_b64_tr_b16 v[160:161], %0 offset:24576" :  : "v"(aW[0][1][0]) : "memory"); SB();
  asm volatile("v_mfma_f32_16x16x32_bf16 %0, v[184:187], a[68:71], %0" : "+v"(a2[1][0]) :  : ); SB();
  asm volatile("ds_read_b64_tr_b16 v[162:163], %0 offset:25344" :  : "v"(aW[0][1][0]) : "memory"); SB();
  asm volatile("v_mfma_f32_16x16x32_bf16 %0, v[184:187], %1, %0" : "+v"(c2[0]) : "v"(ones) : ); SB();
  asm volatile("ds_read_b64_tr_b16 v[164:165], %0 offset:24576" :  : "v"(aW[0][1][1]) : "memory"); SB();
  asm volatile("s_waitcnt lgkmcnt(12)\n\tv_mfma_f32_16x16x32_bf16 %0, v[188:191], a[64:67], %0" : "+v"(a2[0][1]) :  : ); SB();
  asm volatile("ds_read_b64_tr_b16 v[166:167], %0 offset:25344" :  : "v"(aW[0][1][1]) : "memory"); SB();
  asm volatile("s_add_u32 m0, %0, 2048" :  : "s"(mW) : "memory", "scc"); SB();
  asm volatile("v_mfma_f32_16x16x32_bf16 %0, v[188:191], a[68:71], %0" : "+v"(a2[1][1]) :  : ); SB();
  asm volatile("ds_read_b64_tr_b16 v[168:169], %0 offset:24576" :  : "v"(aW[0][1][2]) : "memory"); SB();
  asm volatile("buffer_load_dwordx4 %0, %1, %2 offen lds" :  : "v"(voB[2]), "s"(rW), "s"(kw_[8]) : "memory"); SB();
  asm volatile("v_mfma_f32_16x16x32_bf16 %0, v[188:191], %1, %0" : "+v"(c2[1]) : "v"(ones) : ); SB();
  asm volatile("ds_read_b64_tr_b16 v[170:171], %0 offset:25344" :  : "v"(aW[0][1][2]) : "memory"); SB();
  asm volatile("s_waitcnt lgkmcnt(12)\n\tv_mfma_f32_16x16x32_bf16 %0, v[192:195], a[64:67], %0" : "+v"(a2[0][2]) :  : ); SB();
  asm volatile("v_mfma_f32_16x16x32_bf16 %0, v[192:195], a[68:71], %0" : "+v"(a2[1][2]) :  : ); SB();
  asm volatile("v_mfma_f32_16x16x32_bf16 %0, v[192:195], %1, %0" : "+v"(c2[2]) : "v"(ones) : ); SB();
  asm volatile("v_mfma_f32_16x16x32_bf16 %0, %1, a[96:99], %0" : "+v"(rs) : "v"(ones) : ); SB();
  asm volatile("v_mfma_f32_16x16x32_bf16 %0, a[96:99], a[96:99], %0" : "+v"(gr) :  : ); SB();
  asm volatile("s_waitcnt vmcnt(9)\n\ts_barrier" :  :  : "memory"); SB();
  asm volatile("ds_read_b64_tr_b16 v[172:173], %0 offset:36864" :  : "v"(aW[0][0][0]) : "memory"); SB();
  asm volatile("s_add_u32 m0, %0, 12288" :  : "s"(mW) : "memory", "scc"); SB();
  asm volatile("s_waitcnt lgkmcnt(11)\n\tv_mfma_f32_16x16x32_bf16 %0, %1, a[104:107], %0" : "+v"(rs) : "v"(ones) : ); SB();
  asm volatile("ds_read_b64_tr_b16 v[174:175], %0 offset:37632" :  : "v"(aW[0][0][0]) : "memory"); SB();
  asm volatile("ds_read_b128 a[128:131], %0 offset:49152" :  : "v"(aX[0][0]) : "memory"); SB();
  asm volatile("v_mfma_f32_16x16x32_bf16 %0, a[104:107], a[104:107], %0" : "+v"(gr) :  : ); SB();
  asm volatile("ds_read_b64_tr_b16 v[176:177], %0 offset:36864" :  : "v"(aW[0][0][1]) : "memory"); SB();
  asm volatile("s_waitcnt lgkmcnt(8)\n\tv_mfma_f32_16x16x32_bf16 %0, v[160:163], a[72:75], %0" : "+v"(a2[0][0]) :  : ); SB();
  asm volatile("ds_read_b64_tr_b16 v[178:179], %0 offset:37632" :  : "v"(aW[0][0][1]) : "memory"); SB();
  asm volatile("ds_read_b128 a[132:135], %0 offset:49152" :  : "v"(aX[0][1]) : "memory"); SB();
  asm volatile("v_mfma_f32_16x16x32_bf16 %0, v[160:163], a[76:79], %0" : "+v"(a2[1][0]) :  : ); SB();
  asm volatile("ds_read_b64_tr_b16 v[180:181], %0 offset:36864" :  : "v"(aW[0][0][2]) : "memory"); SB();
  asm volatile("buffer_load_dwordx4 %0, %1, %2 offen lds" :  : "v"(voB[0]), "s"(rW), "s"(kw_[9]) : "memory"); SB();
  asm volatile("s_waitcnt lgkmcnt(9)\n\tv_mfma_f32_16x16x32_bf16 %0, v[164:167], a[72:75], %0" : "+v"(a2[0][1]) :  : ); SB();
  asm volatile("ds_read_b64_tr_b16 v[182:183], %0 offset:37632" :  : "v"(aW[0][0][2]) : "memory"); SB();
  asm volatile("ds_read_b128 a[136:139], %0 offset:49152" :  : "v"(aX[1][0]) : "memory"); SB();
  asm volatile("v_mfma_f32_16x16x32_bf16 %0, v[164:167], a[76:79], %0" : "+v"(a2[1][1]) :  : ); SB();
  asm volatile("ds_read_b128 a[140:143], %0 offset:49152" :  : "v"(aX[1][1]) : "memory"); SB();
  asm volatile("s_add_u32 m0, %0, 13312" :  : "s"(mW) : "memory", "scc"); SB();
  asm volatile("s_waitcnt lgkmcnt(10)\n\tv_mfma_f32_16x16x32_bf16 %0, v[168:171], a[72:75], %0" : "+v"(a2[0][2]) :  : ); SB();
  asm volatile("ds_read_b128 a[144:147], %0 offset:57344" :  : "v"(aX[0][0]) : "memory"); SB();
  asm volatile("ds_read_b128 a[148:151], %0 offset:57344" :  : "v"(aX[0][1]) : "memory"); SB();
  asm volatile("v_mfma_f32_16x16x32_bf16 %0, v[168:171], a[76:79], %0" : "+v"(a2[1][2]) :  : ); SB();
  asm volatile("ds_read_b128 a[152:155], %0 offset:57344" :  : "v"(aX[1][0]) : "memory"); SB();
  asm volatile("ds_read_b128 a[156:159], %0 offset:57344" :  : "v"(aX[1][1]) : "memory"); SB();
  asm volatile("v_mfma_f32_16x16x32_bf16 %0, %1, a[112:115], %0" : "+v"(rs) : "v"(ones) : ); SB();
  asm volatile("buffer_load_dwordx4 %0, %1, %2 offen lds" :  : "v"(voB[1]), "s"(rW), "s"(kw_[9]) : "memory"); SB();
  asm volatile("s_add_u32 m0, %0, 14336" :  : "s"(mW) : "memory", "scc"); SB();
  asm volatile("v_mfma_f32_16x16x32_bf16 %0, a[112:115], a[112:115], %0" : "+v"(gr) :  : ); SB();
  asm volatile("ds_read_b64 %0, %1 offset:0" : "=&v"(xv[0]) : "v"(aV[0]) : "memory"); SB();
  asm volatile("ds_read_b64 %0, %1 offset:0" : "=&v"(xv[1]) : "v"(aV[1]) : "memory"); SB();
  asm volatile("v_mfma_f32_16x16x32_bf16 %0, %1, a[120:123], %0" : "+v"(rs) : "v"(ones) : ); SB();
  asm volatile("ds_read_b64 %0, %1 offset:0" : "=&v"(xv[2]) : "v"(aV[2]) : "memory"); SB();
  asm volatile("ds_read_b64 %0, %1 offset:0" : "=&v"(xv[3]) : "v"(aV[3]) : "memory"); SB();
  asm volatile("v_mfma_f32_16x16x32_bf16 %0, a[120:123], a[120:123], %0" : "+v"(gr) :  : ); SB();
  asm volatile("ds_read_b64 %0, %1 offset:0" : "=&v"(xv[4]) : "v"(aV[4]) : "memory"); SB();
  asm volatile("s_waitcnt lgkmcnt(15)\n\tv_mfma_f32_16x16x32_bf16 %0, v[172:175], a[80:83], %0" : "+v"(a2[0][0]) :  : ); SB();
  asm volatile("ds_read_b64_tr_b16 v[184:185], %0 offset:36864" :  : "v"(aW[0][1][0]) : "memory"); SB();
  asm volatile("buffer_load_dwordx4 %0, %1, %2 offen lds" :  : "v"(voB[2]), "s"(rW), "s"(kw_[9]) : "memory"); SB();
  asm volatile("v_mfma_f32_16x16x32_bf16 %0, v[172:175], a[84:87], %0" : "+v"(a2[1][0]) :  : ); SB();
  asm volatile("ds_read_b64_tr_b16 v[186:187], %0 offset:37632" :  : "v"(aW[0][1][0]) : "memory"); SB();
  asm volatile("buffer_load_dwordx2 v[252:253], off, %0, 0 sc1" :  : "s"(rWS) : "memory"); SB();
  asm volatile("v_mfma_f32_16x16x32_bf16 %0, v[172:175], %1, %0" : "+v"(c2[0]) : "v"(ones) : ); SB();
  asm volatile("ds_read_b64_tr_b16 v[188:189], %0 offset:36864" :  : "v"(aW[0][1][1]) : "memory"); SB();
  asm volatile("s_waitcnt lgkmcnt(15)\n\tv_mfma_f32_16x16x32_bf16 %0, v[176:179], a[80:83], %0" : "+v"(a2[0][1]) :  : ); SB();
  asm volatile("ds_read_b64_tr_b16 v[190:191], %0 offset:37632" :  : "v"(aW[0][1][1]) : "memory"); SB();
  asm volatile("v_mfma_f32_16x16x32_bf16 %0, v[176:179], a[84:87], %0" : "+v"(a2[1][1]) :  : ); SB();
  asm volatile("ds_read_b64_tr_b16 v[192:193], %0 offset:36864" :  : "v"(aW[0][1][2]) : "memory"); SB();
  asm volatile("v_mfma_f32_16x16x32_bf16 %0, v[176:179], %1, %0" : "+v"(c2[1]) : "v"(ones) : ); SB();
  asm volatile("ds_read_b64_tr_b16 v[194:195], %0 offset:37632" :  : "v"(aW[0][1][2]) : "memory"); SB();
  asm volatile("s_waitcnt lgkmcnt(15)\n\tv_mfma_f32_16x16x32_bf16 %0, v[180:183], a[80:83], %0" : "+v"(a2[0][2]) :  : ); SB();
  asm volatile("v_mfma_f32_16x16x32_bf16 %0, v[180:183], a[84:87], %0" : "+v"(a2[1][2]) :  : ); SB();
  asm volatile("v_mfma_f32_16x16x32_bf16 %0, v[180:183], %1, %0" : "+v"(c2[2]) : "v"(ones) : ); SB();
  asm volatile("v_mfma_f32_16x16x32_bf16 %0, %1, a[128:131], %0" : "+v"(rs) : "v"(ones) : ); SB();
  asm volatile("v_mfma_f32_16x16x32_bf16 %0, a[128:131], a[128:131], %0" : "+v"(gr) :  : ); SB();
  asm volatile("s_waitcnt vmcnt(10)\n\ts_barrier" :  :  : "memory"); SB();
  asm volatile("ds_read_b64_tr_b16 v[160:161], %0 offset:0" :  : "v"(aW[1][0][0]) : "memory"); SB();
  asm volatile("s_add_u32 m0, %0, 24576" :  : "s"(mW) : "memory", "scc"); SB();
  asm volatile("v_mfma_f32_16x16x32_bf16 %0, %1, a[136:139], %0" : "+v"(rs) : "v"(ones) : ); SB();
  asm volatile("ds_read_b64_tr_b16 v[162:163], %0 offset:768" :  : "v"(aW[1][0][0]) : "memory"); SB();
  asm volatile("buffer_load_dwordx4 %0, %1, %2 offen lds" :  : "v"(voB[0]), "s"(rW1), "s"(kw_[0]) : "memory"); SB();
  asm volatile("v_mfma_f32_16x16x32_bf16 %0, a[136:139], a[136:139], %0" : "+v"(gr) :  : ); SB();
  asm volatile("ds_read_b64_tr_b16 v[164:165], %0 offset:0" :  : "v"(aW[1][0][1]) : "memory"); SB();
  asm volatile("s_add_u32 m0, %0, 25600" :  : "s"(mW) : "memory", "scc"); SB();
  asm volatile("s_waitcnt lgkmcnt(7)\n\tv_mfma_f32_16x16x32_bf16 %0, v[184:187], a[88:91], %0" : "+v"(a2[0][0]) :  : ); SB();
  asm volatile("ds_read_b64_tr_b16 v[166:167], %0 offset:768" :  : "v"(aW[1][0][1]) : "memory"); SB();
  asm volatile("buffer_load_dwordx4 %0, %1, %2 offen lds" :  : "v"(voB[1]), "s"(rW1), "s"(kw_[0]) : "memory"); SB();
  asm volatile("v_mfma_f32_16x16x32_bf16 %0, v[184:187], a[92:95], %0" : "+v"(a2[1][0]) :  : ); SB();
  asm volatile("ds_read_b64_tr_b16 v[168:169], %0 offset:0" :  : "v"(aW[1][0][2]) : "memory"); SB();
  asm volatile("s_add_u32 m0, %0, 26624" :  : "s"(mW) : "memory", "scc"); SB();
  asm volatile("s_waitcnt lgkmcnt(7)\n\tv_mfma_f32_16x16x32_bf16 %0, v[188:191], a[88:91], %0" : "+v"(a2[0][1]) :  : ); SB();
  asm volatile("ds_read_b64_tr_b16 v[170:171], %0 offset:768" :  : "v"(aW[1][0][2]) : "memory"); SB();
  asm volatile("buffer_load_dwordx4 %0, %1, %2 offen lds" :  : "v"(voB[2]), "s"(rW1), "s"(kw_[0]) : "memory"); SB();
  asm volatile("v_mfma_f32_16x16x32_bf16 %0, v[188:191], a[92:95], %0" : "+v"(a2[1][1]) :  : ); SB();
  asm volatile("s_waitcnt lgkmcnt(6)\n\tv_mfma_f32_16x16x32_bf16 %0, v[192:195], a[88:91], %0" : "+v"(a2[0][2]) :  : ); SB();
  asm volatile("v_mfma_f32_16x16x32_bf16 %0, v[192:195], a[92:95], %0" : "+v"(a2[1][2]) :  : ); SB();
  asm volatile("v_mfma_f32_16x16x32_bf16 %0, %1, a[144:147], %0" : "+v"(rs) : "v"(ones) : ); SB();
  asm volatile("ds_read_b64_tr_b16 v[172:173], %0 offset:0" :  : "v"(aW[1][1][0]) : "memory"); SB();
  asm volatile("v_mfma_f32_16x16x32_bf16 %0, a[144:147], a[144:147], %0" : "+v"(gr) :  : ); SB();
  asm volatile("ds_read_b64_tr_b16 v[174:175], %0 offset:768" :  : "v"(aW[1][1][0]) : "memory"); SB();
  asm volatile("v_mfma_f32_16x16x32_bf16 %0, %1, a[152:155], %0" : "+v"(rs) : "v"(ones) : ); SB();
  asm volatile("ds_read_b64_tr_b16 v[176:177], %0 offset:0" :  : "v"(aW[1][1][1]) : "memory"); SB();
  asm volatile("v_mfma_f32_16x16x32_bf16 %0, a[152:155], a[152:155], %0" : "+v"(gr) :  : ); SB();
  asm volatile("ds_read_b64_tr_b16 v[178:179], %0 offset:768" :  : "v"(aW[1][1][1]) : "memory"); SB();
  asm volatile("" : "+v"(xv[0]), "+v"(xv[1]), "+v"(xv[2]), "+v"(xv[3]), "+v"(xv[4]) :  : "memory"); SB();
  asm volatile("s_waitcnt lgkmcnt(8)\n\tv_mfma_f32_16x16x32_bf16 %0, v[160:163], a[96:99], %0" : "+v"(a2[0][0]) :  : ); SB();
  asm volatile("ds_read_b64_tr_b16 v[180:181], %0 offset:0" :  : "v"(aW[1][1][2]) : "memory"); SB();
  asm volatile("v_mfma_f32_16x16x32_bf16 %0, v[160:163], a[100:103], %0" : "+v"(a2[1][0]) :  : ); SB();
  asm volatile("ds_read_b64_tr_b16 v[182:183], %0 offset:768" :  : "v"(aW[1][1][2]) : "memory"); SB();
  asm volatile("v_mfma_f32_16x16x32_bf16 %0, v[160:163], %1, %0" : "+v"(c2[0]) : "v"(ones) : ); SB();
  asm volatile("s_waitcnt vmcnt(10)\n\ts_barrier" :  :  : "memory"); SB();
  asm volatile("ds_read_b64_tr_b16 v[184:185], %0 offset:12288" :  : "v"(aW[1][0][0]) : "memory"); SB();
  asm volatile("s_add_u32 m0, %0, 106496" :  : "s"(mA) : "memory", "scc"); SB();
  asm volatile("s_waitcnt lgkmcnt(9)\n\tv_mfma_f32_16x16x32_bf16 %0, v[164:167], a[96:99], %0" : "+v"(a2[0][1]) :  : ); SB();
  asm volatile("ds_read_b64_tr_b16 v[186:187], %0 offset:13056" :  : "v"(aW[1][0][0]) : "memory"); SB();
  asm volatile("buffer_load_dwordx4 %0, %1, %2 offen lds" :  : "v"(voK[0]), "s"(rK), "s"(0u) : "memory"); SB();
  asm volatile("v_mfma_f32_16x16x32_bf16 %0, v[164:167], a[100:103], %0" : "+v"(a2[1][1]) :  : ); SB();
  asm volatile("ds_read_b64_tr_b16 v[188:189], %0 offset:12288" :  : "v"(aW[1][0][1]) : "memory"); SB();
  asm volatile("s_add_u32 m0, %0, 107520" :  : "s"(mA) : "memory", "scc"); SB();
  asm volatile("v_mfma_f32_16x16x32_bf16 %0, v[164:167], %1, %0" : "+v"(c2[1]) : "v"(ones) : ); SB();
  asm volatile("ds_read_b64_tr_b16 v[190:191], %0 offset:13056" :  : "v"(aW[1][0][1]) : "memory"); SB();
  asm volatile("s_waitcnt lgkmcnt(10)\n\tv_mfma_f32_16x16x32_bf16 %0, v[168:171], a[96:99], %0" : "+v"(a2[0][2]) :  : ); SB();
  asm volatile("ds_read_b64_tr_b16 v[192:193], %0 offset:12288" :  : "v"(aW[1][0][2]) : "memory"); SB();
  asm volatile("buffer_load_dwordx4 %0, %1, %2 offen lds" :  : "v"(voK[1]), "s"(rK), "s"(0u) : "memory"); SB();
  asm volatile("v_mfma_f32_16x16x32_bf16 %0, v[168:171], a[100:103], %0" : "+v"(a2[1][2]) :  : ); SB();
  asm volatile("ds_read_b64_tr_b16 v[194:195], %0 offset:13056" :  : "v"(aW[1][0][2]) : "memory"); SB();
  asm volatile("s_add_u32 m0, %0, 108544" :  : "s"(mA) : "memory", "scc"); SB();
  asm volatile("v_mfma_f32_16x16x32_bf16 %0, v[168:171], %1, %0" : "+v"(c2[2]) : "v"(ones) : ); SB();
  asm volatile("buffer_load_dwordx4 %0, %1, %2 offen lds" :  : "v"(voK[2]), "s"(rK), "s"(0u) : "memory"); SB();
  asm volatile("s_nop 3" : "+v"(rs), "+v"(gr) :  : ); SB();
  {
    const float mean = rs[0] * (1.0f / 640.0f);
    const float dgv = (l15 & 2) ? ((l15 & 1) ? gr[3] : gr[2]) : ((l15 & 1) ? gr[1] : gr[0]);
    float sq = red32(red16(g4 == q4 ? dgv : 0.f)) - rs[0] * mean;
    if (__builtin_expect(__builtin_amdgcn_ballot_w64(!(sq >= 1e-3f * rs[0] * mean)) != 0, 0)) {
      const f32x2 mm = {mean, mean};
      f32x2 q2 = {0.f, 0.f};
      const u32x4* xp = (const u32x4*)(x + (size_t)(rb * 64 + w * 16 + l15) * 1280 + g4 * 320);
#pragma unroll 1
      for (int c = 0; c < 20; ++c) {
        u32x4 xq;
        asm volatile("global_load_dwordx4 %0, %1, off\n\ts_waitcnt vmcnt(0)" : "=&v"(xq) : "v"(xp + c) : "memory");
#pragma unroll
        for (int e = 0; e < 4; ++e) { const f32x2 d = unpk(xq[e]) - mm; q2 = __builtin_elementwise_fma(d, d, q2); }
      }
      sq = red32(red16(q2[0] + q2[1]));
    }
    xm[0] = rbf(mean);
    xd[0] = rbf(__builtin_amdgcn_sqrtf(rbf(rbf(fmaxf(sq, 0.f) * (1.0f / 640.0f)) + 1e-5f)));
    SB();
  }
  asm volatile("ds_write_b64 %0, %1 offset:0" :  : "v"(xw), "v"(((f32x2){xm[0], xd[0]})) : "memory"); SB();
  asm volatile("s_waitcnt lgkmcnt(11)\n\tv_mfma_f32_16x16x32_bf16 %0, v[172:175], a[104:107], %0" : "+v"(a2[0][0]) :  : ); SB();
  asm volatile("s_add_u32 m0, %0, 109568" :  : "s"(mA) : "memory", "scc"); SB();
  asm volatile("s_mov_b32 exec_lo, 0xff\n\ts_mov_b32 exec_hi, 0\n\tbuffer_load_dwordx4 %0, %1, %2 offen lds\n\ts_mov_b64 exec, -1" :  : "v"(voK[3]), "s"(rK), "s"(0u) : "memory"); SB();
  asm volatile("v_mfma_f32_16x16x32_bf16 %0, v[172:175], a[108:111], %0" : "+v"(a2[1][0]) :  : ); SB();
  asm volatile("s_add_u32 m0, %0, 110592" :  : "s"(mA) : "memory", "scc"); SB();
  asm volatile("s_waitcnt lgkmcnt(9)\n\tv_mfma_f32_16x16x32_bf16 %0, v[176:179], a[104:107], %0" : "+v"(a2[0][1]) :  : ); SB();
  asm volatile("ds_read_b64_tr_b16 v[160:161], %0 offset:12288" :  : "v"(aW[1][1][0]) : "memory"); SB();
  asm volatile("buffer_load_dwordx4 %0, %1, %2 offen lds" :  : "v"(voV[0]), "s"(rV), "s"(0u) : "memory"); SB();
  asm volatile("v_mfma_f32_16x16x32_bf16 %0, v[176:179], a[108:111], %0" : "+v"(a2[1][1]) :  : ); SB();
  asm volatile("ds_read_b64_tr_b16 v[162:163], %0 offset:13056" :  : "v"(aW[1][1][0]) : "memory"); SB();
  asm volatile("s_add_u32 m0, %0, 111616" :  : "s"(mA) : "memory", "scc"); SB();
  asm volatile("s_waitcnt lgkmcnt(9)\n\tv_mfma_f32_16x16x32_bf16 %0, v[180:183], a[104:107], %0" : "+v"(a2[0][2]) :  : ); SB();
  asm volatile("ds_read_b64_tr_b16 v[164:165], %0 offset:12288" :  : "v"(aW[1][1][1]) : "memory"); SB();
  asm volatile("buffer_load_dwordx4 %0, %1, %2 offen lds" :  : "v"(voV[1]), "s"(rV), "s"(0u) : "memory"); SB();
  asm volatile("v_mfma_f32_16x16x32_bf16 %0, v[180:183], a[108:111], %0" : "+v"(a2[1][2]) :  : ); SB();
  asm volatile("ds_read_b64_tr_b16 v[166:167], %0 offset:13056" :  : "v"(aW[1][1][1]) : "memory"); SB();
  asm volatile("s_waitcnt lgkmcnt(9)\n\tv_mfma_f32_16x16x32_bf16 %0, v[184:187], a[112:115], %0" : "+v"(a2[0][0]) :  : ); SB();
  asm volatile("ds_read_b64_tr_b16 v[168:169], %0 offset:12288" :  : "v"(aW[1][1][2]) : "memory"); SB();
  asm volatile("s_add_u32 m0, %0, 112640" :  : "s"(mA) : "memory", "scc"); SB();
  asm volatile("v_mfma_f32_16x16x32_bf16 %0, v[184:187], a[116:119], %0" : "+v"(a2[1][0]) :  : ); SB();
  asm volatile("ds_read_b64_tr_b16 v[170:171], %0 offset:13056" :  : "v"(aW[1][1][2]) : "memory"); SB();
  asm volatile("s_mov_b32 exec_lo, -1\n\ts_mov_b32 exec_hi, 0\n\tbuffer_load_dwordx4 %0, %1, %2 offen lds\n\ts_mov_b64 exec, -1" :  : "v"(voV[2]), "s"(rV), "s"(0u) : "memory"); SB();
  asm volatile("v_mfma_f32_16x16x32_bf16 %0, v[184:187], %1, %0" : "+v"(c2[0]) : "v"(ones) : ); SB();
  asm volatile("s_waitcnt vmcnt(14) lgkmcnt(6)\n\ts_barrier" :  :  : "memory"); SB();
  asm volatile("ds_read_b64_tr_b16 v[172:173], %0 offset:0" :  : "v"(aW[0][0][0]) : "memory"); SB();
  asm volatile("ds_read_b64 %0, %1 offset:0" : "=&v"(xo) : "v"(xr_) : "memory"); SB();
  asm volatile("v_mfma_f32_16x16x32_bf16 %0, v[188:191], a[112:115], %0" : "+v"(a2[0][1]) :  : ); SB();
  asm volatile("ds_read_b64_tr_b16 v[174:175], %0 offset:768" :  : "v"(aW[0][0][0]) : "memory"); SB();
  asm volatile("s_add_u32 m0, %0, 109824" :  : "s"(mA) : "memory", "scc"); SB();
  asm volatile("v_mfma_f32_16x16x32_bf16 %0, v[188:191], a[116:119], %0" : "+v"(a2[1][1]) :  : ); SB();
  asm volatile("ds_read_b64_tr_b16 v[176:177], %0 offset:0" :  : "v"(aW[0][0][1]) : "memory"); SB();
  asm volatile("s_mov_b32 exec_lo, -1\n\ts_mov_b32 exec_hi, 0xff\n\tbuffer_load_dwordx4 %0, %1, %2 offen lds\n\ts_mov_b64 exec, -1" :  : "v"(voH), "s"(rS), "s"(0u) : "memory"); SB();
  asm volatile("v_mfma_f32_16x16x32_bf16 %0, v[188:191], %1, %0" : "+v"(c2[1]) : "v"(ones) : ); SB();
  asm volatile("ds_read_b64_tr_b16 v[178:179], %0 offset:768" :  : "v"(aW[0][0][1]) : "memory"); SB();
  asm volatile("v_mfma_f32_16x16x32_bf16 %0, v[192:195], a[112:115], %0" : "+v"(a2[0][2]) :  : ); SB();
  asm volatile("ds_read_b64_tr_b16 v[180:181], %0 offset:0" :  : "v"(aW[0][0][2]) : "memory"); SB();
  asm volatile("s_add_u32 m0, %0, 113664" :  : "s"(mA) : "memory", "scc"); SB();
  asm volatile("v_mfma_f32_16x16x32_bf16 %0, v[192:195], a[116:119], %0" : "+v"(a2[1][2]) :  : ); SB();
  asm volatile("ds_read_b64_tr_b16 v[182:183], %0 offset:768" :  : "v"(aW[0][0][2]) : "memory"); SB();
  asm volatile("buffer_load_dwordx4 %0, %1, %2 offen lds" :  : "v"(voO), "s"(rO), "s"(0u) : "memory"); SB();
  asm volatile("v_mfma_f32_16x16x32_bf16 %0, v[192:195], %1, %0" : "+v"(c2[2]) : "v"(ones) : ); SB();
  asm volatile("s_waitcnt lgkmcnt(11)\n\tv_mfma_f32_16x16x32_bf16 %0, v[160:163], a[120:123], %0" : "+v"(a2[0][0]) :  : ); SB();
  asm volatile("v_mfma_f32_16x16x32_bf16 %0, v[160:163], a[124:127], %0" : "+v"(a2[1][0]) :  : ); SB();
  asm volatile("s_waitcnt lgkmcnt(9)\n\tv_mfma_f32_16x16x32_bf16 %0, v[164:167], a[120:123], %0" : "+v"(a2[0][1]) :  : ); SB();
  asm volatile("ds_read_b64_tr_b16 v[184:185], %0 offset:0" :  : "v"(aW[0][1][0]) : "memory"); SB();
  asm volatile("v_mfma_f32_16x16x32_bf16 %0, v[164:167], a[124:127], %0" : "+v"(a2[1][1]) :  : ); SB();
  asm volatile("ds_read_b64_tr_b16 v[186:187], %0 offset:768" :  : "v"(aW[0][1][0]) : "memory"); SB();
  asm volatile("s_waitcnt lgkmcnt(9)\n\tv_mfma_f32_16x16x32_bf16 %0, v[168:171], a[120:123], %0" : "+v"(a2[0][2]) :  : ); SB();
  asm volatile("ds_read_b64_tr_b16 v[188:189], %0 offset:0" :  : "v"(aW[0][1][1]) : "memory"); SB();
  asm volatile("v_mfma_f32_16x16x32_bf16 %0, v[168:171], a[124:127], %0" : "+v"(a2[1][2]) :  : ); SB();
  asm volatile("ds_read_b64_tr_b16 v[190:191], %0 offset:768" :  : "v"(aW[0][1][1]) : "memory"); SB();
  asm volatile("s_waitcnt lgkmcnt(8)\n\tv_mfma_f32_16x16x32_bf16 %0, v[172:175], a[128:131], %0" : "+v"(a2[0][0]) :  : ); SB();
  asm volatile("ds_read_b64_tr_b16 v[192:193], %0 offset:0" :  : "v"(aW[0][1][2]) : "memory"); SB();
  asm volatile("v_mfma_f32_16x16x32_bf16 %0, v[172:175], a[132:135], %0" : "+v"(a2[1][0]) :  : ); SB();
  asm volatile("ds_read_b64_tr_b16 v[194:195], %0 offset:768" :  : "v"(aW[0][1][2]) : "memory"); SB();
  asm volatile("v_mfma_f32_16x16x32_bf16 %0, v[172:175], %1, %0" : "+v"(c2[0]) : "v"(ones) : ); SB();
  asm volatile("s_waitcnt vmcnt(13)\n\ts_barrier" :  :  : "memory"); SB();
  asm volatile("ds_read_b64_tr_b16 v[160:161], %0 offset:12288" :  : "v"(aW[0][0][0]) : "memory"); SB();
  asm volatile("s_waitcnt lgkmcnt(9)\n\tv_mfma_f32_16x16x32_bf16 %0, v[176:179], a[128:131], %0" : "+v"(a2[0][1]) :  : ); SB();
  asm volatile("ds_read_b64_tr_b16 v[162:163], %0 offset:13056" :  : "v"(aW[0][0][0]) : "memory"); SB();
  asm volatile("buffer_store_dwordx4 %1, %0, %2, %3 offen offset:0 sc1\n\ts_nop 1" :  : "v"(vH_), "v"(hole4), "s"(rWS), "s"(0u) : "memory"); SB();
  asm volatile("v_mfma_f32_16x16x32_bf16 %0, v[176:179], a[132:135], %0" : "+v"(a2[1][1]) :  : ); SB();
  asm volatile("ds_read_b64_tr_b16 v[164:165], %0 offset:12288" :  : "v"(aW[0][0][1]) : "memory"); SB();
  asm volatile("v_mfma_f32_16x16x32_bf16 %0, v[176:179], %1, %0" : "+v"(c2[1]) : "v"(ones) : ); SB();
  asm volatile("ds_read_b64_tr_b16 v[166:167], %0 offset:13056" :  : "v"(aW[0][0][1]) : "memory"); SB();
  asm volatile("s_waitcnt lgkmcnt(10)\n\tv_mfma_f32_16x16x32_bf16 %0, v[180:183], a[128:131], %0" : "+v"(a2[0][2]) :  : ); SB();
  asm volatile("ds_read_b64_tr_b16 v[168:169], %0 offset:12288" :  : "v"(aW[0][0][2]) : "memory"); SB();
  asm volatile("v_mfma_f32_16x16x32_bf16 %0, v[180:183], a[132:135], %0" : "+v"(a2[1][2]) :  : ); SB();
  asm volatile("ds_read_b64_tr_b16 v[170:171], %0 offset:13056" :  : "v"(aW[0][0][2]) : "memory"); SB();
  asm volatile("v_mfma_f32_16x16x32_bf16 %0, v[180:183], %1, %0" : "+v"(c2[2]) : "v"(ones) : ); SB();
  asm volatile("" : "+v"(xo) :  : "memory"); SB();
  xm[1] = xo[0]; xd[1] = xo[1]; kset(); SB();
  asm volatile("s_waitcnt lgkmcnt(10)\n\tv_mfma_f32_16x16x32_bf16 %0, v[184:187], a[136:139], %0" : "+v"(a2[0][0]) :  : ); SB();
  asm volatile("buffer_store_dwordx4 %1, %0, %2, %3 offen offset:1024 sc1\n\ts_nop 1" :  : "v"(vH_), "v"(hole4), "s"(rWS), "s"(0u) : "memory"); SB();
  asm volatile("v_mfma_f32_16x16x32_bf16 %0, v[184:187], a[140:143], %0" : "+v"(a2[1][0]) :  : ); SB();
  asm volatile("s_waitcnt lgkmcnt(8)\n\tv_mfma_f32_16x16x32_bf16 %0, v[188:191], a[136:139], %0" : "+v"(a2[0][1]) :  : ); SB();
  asm volatile("ds_read_b64_tr_b16 v[172:173], %0 offset:12288" :  : "v"(aW[0][1][0]) : "memory"); SB();
  asm volatile("v_mfma_f32_16x16x32_bf16 %0, v[188:191], a[140:143], %0" : "+v"(a2[1][1]) :  : ); SB();
  asm volatile("ds_read_b64_tr_b16 v[174:175], %0 offset:13056" :  : "v"(aW[0][1][0]) : "memory"); SB();
  asm volatile("s_waitcnt lgkmcnt(8)\n\tv_mfma_f32_16x16x32_bf16 %0, v[192:195], a[136:139], %0" : "+v"(a2[0][2]) :  : ); SB();
  asm volatile("ds_read_b64_tr_b16 v[176:177], %0 offset:12288" :  : "v"(aW[0][1][1]) : "memory"); SB();
  asm volatile("buffer_store_dwordx4 %1, %0, %2, %3 offen offset:2048 sc1\n\ts_nop 1" :  : "v"(vH_), "v"(hole4), "s"(rWS), "s"(0u) : "memory"); SB();
  asm volatile("v_mfma_f32_16x16x32_bf16 %0, v[192:195], a[140:143], %0" : "+v"(a2[1][2]) :  : ); SB();
  asm volatile("ds_read_b64_tr_b16 v[178:179], %0 offset:13056" :  : "v"(aW[0][1][1]) : "memory"); SB();
  asm volatile("s_waitcnt lgkmcnt(8)\n\tv_mfma_f32_16x16x32_bf16 %0, v[160:163], a[144:147], %0" : "+v"(a2[0][0]) :  : ); SB();
  asm volatile("ds_read_b64_tr_b16 v[180:181], %0 offset:12288" :  : "v"(aW[0][1][2]) : "memory"); SB();
  asm volatile("v_mfma_f32_16x16x32_bf16 %0, v[160:163], a[148:151], %0" : "+v"(a2[1][0]) :  : ); SB();
  asm volatile("ds_read_b64_tr_b16 v[182:183], %0 offset:13056" :  : "v"(aW[0][1][2]) : "memory"); SB();
  asm volatile("v_mfma_f32_16x16x32_bf16 %0, v[160:163], %1, %0" : "+v"(c2[0]) : "v"(ones) : ); SB();
  asm volatile("s_waitcnt lgkmcnt(8)\n\tv_mfma_f32_16x16x32_bf16 %0, v[164:167], a[144:147], %0" : "+v"(a2[0][1]) :  : ); SB();
  asm volatile("v_mfma_f32_16x16x32_bf16 %0, v[164:167], a[148:151], %0" : "+v"(a2[1][1]) :  : ); SB();
  asm volatile("v_mfma_f32_16x16x32_bf16 %0, v[164:167], %1, %0" : "+v"(c2[1]) : "v"(ones) : ); SB();
  asm volatile("s_waitcnt lgkmcnt(6)\n\tv_mfma_f32_16x16x32_bf16 %0, v[168:171], a[144:147], %0" : "+v"(a2[0][2]) :  : ); SB();
  asm volatile("v_mfma_f32_16x16x32_bf16 %0, v[168:171], a[148:151], %0" : "+v"(a2[1][2]) :  : ); SB();
  asm volatile("v_mfma_f32_16x16x32_bf16 %0, v[168:171], %1, %0" : "+v"(c2[2]) : "v"(ones) : ); SB();
  asm volatile("s_waitcnt lgkmcnt(4)\n\tv_mfma_f32_16x16x32_bf16 %0, v[172:175], a[152:155], %0" : "+v"(a2[0][0]) :  : ); SB();
  asm volatile("v_mfma_f32_16x16x32_bf16 %0, v[172:175], a[156:159], %0" : "+v"(a2[1][0]) :  : ); SB();
  asm volatile("ds_write_b128 %0, %1 offset:0" :  : "v"(ca), "v"(c2[0]) : "memory"); SB();
  asm volatile("s_waitcnt lgkmcnt(3)\n\tv_mfma_f32_16x16x32_bf16 %0, v[176:179], a[152:155], %0" : "+v"(a2[0][1]) :  : ); SB();
  asm volatile("ds_write_b128 %0, %1 offset:64" :  : "v"(ca), "v"(c2[1]) : "memory"); SB();
  asm volatile("v_mfma_f32_16x16x32_bf16 %0, v[176:179], a[156:159], %0" : "+v"(a2[1][1]) :  : ); SB();
  asm volatile("ds_write_b128 %0, %1 offset:128" :  : "v"(ca), "v"(c2[2]) : "memory"); SB();
  asm volatile("s_waitcnt lgkmcnt(3)\n\tv_mfma_f32_16x16x32_bf16 %0, v[180:183], a[152:155], %0" : "+v"(a2[0][2]) :  : ); SB();
  asm volatile("s_add_u32 m0, %0, 36864" :  : "s"(mW) : "memory", "scc"); SB();
  asm volatile("s_nop 0\n\tbuffer_load_dwordx4 %0, %1, %2 offen lds" :  : "v"(voB[0]), "s"(rW1), "s"(kw_[1]) : "memory"); SB();
  asm volatile("v_mfma_f32_16x16x32_bf16 %0, v[180:183], a[156:159], %0" : "+v"(a2[1][2]) :  : ); SB();
  ST(3);
  asm volatile("s_add_u32 m0, %0, 37888" :  : "s"(mW) : "memory", "scc"); SB();
  asm volatile("s_nop 0\n\tbuffer_load_dwordx4 %0, %1, %2 offen lds" :  : "v"(voB[1]), "s"(rW1), "s"(kw_[1]) : "memory"); SB();
  asm volatile("s_add_u32 m0, %0, 38912" :  : "s"(mW) : "memory", "scc"); SB();
  asm volatile("s_nop 0\n\tbuffer_load_dwordx4 %0, %1, %2 offen lds" :  : "v"(voB[2]), "s"(rW1), "s"(kw_[1]) : "memory"); SB();
  asm volatile("s_add_u32 m0, %0, 49152" :  : "s"(mW) : "memory", "scc"); SB();
  asm volatile("s_nop 0\n\tbuffer_load_dwordx4 %0, %1, %2 offen lds" :  : "v"(voB[0]), "s"(rW1), "s"(kw_[2]) : "memory"); SB();
  asm volatile("s_add_u32 m0, %0, 50176" :  : "s"(mW) : "memory", "scc"); SB();
  asm volatile("s_nop 0\n\tbuffer_load_dwordx4 %0, %1, %2 offen lds" :  : "v"(voB[1]), "s"(rW1), "s"(kw_[2]) : "memory"); SB();
  asm volatile("s_add_u32 m0, %0, 51200" :  : "s"(mW) : "memory", "scc"); SB();
  asm volatile("s_nop 0\n\tbuffer_load_dwordx4 %0, %1, %2 offen lds" :  : "v"(voB[2]), "s"(rW1), "s"(kw_[2]) : "memory"); SB();
  asm volatile("s_add_u32 m0, %0, 61440" :  : "s"(mW) : "memory", "scc"); SB();
  asm volatile("s_nop 0\n\tbuffer_load_dwordx4 %0, %1, %2 offen lds" :  : "v"(voB[0]), "s"(rW1), "s"(kw_[3]) : "memory"); SB();
  asm volatile("s_add_u32 m0, %0, 62464" :  : "s"(mW) : "memory", "scc"); SB();
  asm volatile("s_nop 0\n\tbuffer_load_dwordx4 %0, %1, %2 offen lds" :  : "v"(voB[1]), "s"(rW1), "s"(kw_[3]) : "memory"); SB();
  asm volatile("s_add_u32 m0, %0, 63488" :  : "s"(mW) : "memory", "scc"); SB();
  asm volatile("s_nop 0\n\tbuffer_load_dwordx4 %0, %1, %2 offen lds" :  : "v"(voB[2]), "s"(rW1), "s"(kw_[3]) : "memory"); SB();
  asm volatile("s_add_u32 m0, %0, 73728" :  : "s"(mW) : "memory", "scc"); SB();
  asm volatile("s_nop 0\n\tbuffer_load_dwordx4 %0, %1, %2 offen lds" :  : "v"(voB[0]), "s"(rW1), "s"(kw_[4]) : "memory"); SB();
  asm volatile("s_add_u32 m0, %0, 74752" :  : "s"(mW) : "memory", "scc"); SB();
  asm volatile("s_nop 0\n\tbuffer_load_dwordx4 %0, %1, %2 offen lds" :  : "v"(voB[1]), "s"(rW1), "s"(kw_[4]) : "memory"); SB();
  asm volatile("s_waitcnt vmcnt(14)" :  :  : "memory"); SB();
  ST(2);
  asm volatile("ds_read_b64_tr_b16 %0, %1 offset:0" : "=&v"(kp[0]) : "v"(ka) : "memory"); SB();
  asm volatile("ds_read_b64_tr_b16 %0, %1 offset:640" : "=&v"(kc[0]) : "v"(ka) : "memory"); SB();
  asm volatile("ds_read_b64_tr_b16 %0, %1 offset:4096" : "=&v"(vv[0]) : "v"(ka) : "memory"); SB();
  asm volatile("ds_read_b64 %0, %1 offset:0" : "=&v"(sw[0]) : "v"(sa) : "memory"); SB();
  asm volatile("ds_read_b64_tr_b16 %0, %1 offset:32" : "=&v"(kp[1]) : "v"(ka) : "memory"); SB();
  asm volatile("ds_read_b64_tr_b16 %0, %1 offset:672" : "=&v"(kc[1]) : "v"(ka) : "memory"); SB();
  asm volatile("ds_read_b64_tr_b16 %0, %1 offset:4128" : "=&v"(vv[1]) : "v"(ka) : "memory"); SB();
  asm volatile("ds_read_b64 %0, %1 offset:128" : "=&v"(sw[1]) : "v"(sa) : "memory"); SB();
  asm volatile("ds_read_b64_tr_b16 %0, %1 offset:64" : "=&v"(kp[2]) : "v"(ka) : "memory"); SB();
  asm volatile("ds_read_b64_tr_b16 %0, %1 offset:704" : "=&v"(kc[2]) : "v"(ka) : "memory"); SB();
  asm volatile("ds_read_b64_tr_b16 %0, %1 offset:4160" : "=&v"(vv[2]) : "v"(ka) : "memory"); SB();
  asm volatile("ds_read_b64 %0, %1 offset:256" : "=&v"(sw[2]) : "v"(sa) : "memory"); SB();
  asm volatile("ds_read_b64_tr_b16 %0, %1 offset:96" : "=&v"(kp[3]) : "v"(ka) : "memory"); SB();
  asm volatile("ds_read_b64_tr_b16 %0, %1 offset:736" : "=&v"(kc[3]) : "v"(ka) : "memory"); SB();
  asm volatile("ds_read_b64_tr_b16 %0, %1 offset:4192" : "=&v"(vv[3]) : "v"(ka) : "memory"); SB();
  asm volatile("ds_read_b64 %0, %1 offset:384" : "=&v"(sw[3]) : "v"(sa) : "memory"); SB();
  asm volatile("ds_read_b64_tr_b16 %0, %1 offset:128" : "=&v"(kp[4]) : "v"(ka) : "memory"); SB();
  asm volatile("ds_read_b64_tr_b16 %0, %1 offset:768" : "=&v"(kc[4]) : "v"(ka) : "memory"); SB();
  asm volatile("ds_read_b64_tr_b16 %0, %1 offset:4224" : "=&v"(vv[4]) : "v"(ka) : "memory"); SB();
  asm volatile("ds_read_b64 %0, %1 offset:512" : "=&v"(sw[4]) : "v"(sa) : "memory"); SB();
  asm volatile("ds_read_b64_tr_b16 %0, %1 offset:0" : "=&v"(wt[0]) : "v"(oa) : "memory"); SB();
  asm volatile("ds_read_b64_tr_b16 %0, %1 offset:512" : "=&v"(wt[1]) : "v"(oa) : "memory"); SB();
  asm volatile("s_add_u32 m0, %0, 75776" :  : "s"(mW) : "memory", "scc"); SB();
  asm volatile("s_nop 0\n\tbuffer_load_dwordx4 %0, %1, %2 offen lds" :  : "v"(voB[2]), "s"(rW1), "s"(kw_[4]) : "memory"); SB();
  asm volatile("s_waitcnt lgkmcnt(15)" : "+v"(kp[0]), "+v"(kc[0]), "+v"(vv[0]), "+v"(sw[0]) :  : "memory"); SB();
  {
    const unsigned w10 = shr16(sw[0][0], sw[0][0]), w32 = shr16(sw[0][1], sw[0][1]);
    const unsigned s0 = shr16(kp[0][1], kp[0][0]), s1 = shr16(kc[0][0], kp[0][1]), s2 = shr16(kc[0][1], kc[0][0]);
    const float r0 = dot2(s1, w10, dot2(s0, w32, 0.f)), r1 = dot2(kc[0][0], w10, dot2(kp[0][1], w32, 0.f));
    const float r2 = dot2(s2, w10, dot2(s1, w32, 0.f)), r3 = dot2(kc[0][1], w10, dot2(kc[0][0], w32, 0.f));
    ko[0] = (u32x2){pk2((f32x2){r0, r1} * unpk(vv[0][0])), pk2((f32x2){r2, r3} * unpk(vv[0][1]))};
    asm volatile("" : "+v"(ko[0]));
  }
  SB();
  asm volatile("s_add_u32 m0, %0, 86016" :  : "s"(mW) : "memory", "scc"); SB();
  asm volatile("s_nop 0\n\tbuffer_load_dwordx4 %0, %1, %2 offen lds" :  : "v"(voB[0]), "s"(rW1), "s"(kw_[5]) : "memory"); SB();
  asm volatile("ds_write_b64 %0, %1 offset:0" :  : "v"(kw), "v"(ko[0]) : "memory"); SB();
  asm volatile("s_waitcnt lgkmcnt(15)" : "+v"(kp[1]), "+v"(kc[1]), "+v"(vv[1]), "+v"(sw[1]) :  : "memory"); SB();
  {
    const unsigned w10 = shr16(sw[1][0], sw[1][0]), w32 = shr16(sw[1][1], sw[1][1]);
    const unsigned s0 = shr16(kp[1][1], kp[1][0]), s1 = shr16(kc[1][0], kp[1][1]), s2 = shr16(kc[1][1], kc[1][0]);
    const float r0 = dot2(s1, w10, dot2(s0, w32, 0.f)), r1 = dot2(kc[1][0], w10, dot2(kp[1][1], w32, 0.f));
    const float r2 = dot2(s2, w10, dot2(s1, w32, 0.f)), r3 = dot2(kc[1][1], w10, dot2(kc[1][0], w32, 0.f));
    ko[1] = (u32x2){pk2((f32x2){r0, r1} * unpk(vv[1][0])), pk2((f32x2){r2, r3} * unpk(vv[1][1]))};
    asm volatile("" : "+v"(ko[1]));
  }
  SB();
  asm volatile("ds_write_b64 %0, %1 offset:2304" :  : "v"(kw2), "v"(ko[1]) : "memory"); SB();
  asm volatile("s_waitcnt lgkmcnt(12)" : "+v"(kp[2]), "+v"(kc[2]), "+v"(vv[2]), "+v"(sw[2]) :  : "memory"); SB();
  {
    const unsigned w10 = shr16(sw[2][0], sw[2][0]), w32 = shr16(sw[2][1], sw[2][1]);
    const unsigned s0 = shr16(kp[2][1], kp[2][0]), s1 = shr16(kc[2][0], kp[2][1]), s2 = shr16(kc[2][1], kc[2][0]);
    const float r0 = dot2(s1, w10, dot2(s0, w32, 0.f)), r1 = dot2(kc[2][0], w10, dot2(kp[2][1], w32, 0.f));
    const float r2 = dot2(s2, w10, dot2(s1, w32, 0.f)), r3 = dot2(kc[2][1], w10, dot2(kc[2][0], w32, 0.f));
    ko[2] = (u32x2){pk2((f32x2){r0, r1} * unpk(vv[2][0])), pk2((f32x2){r2, r3} * unpk(vv[2][1]))};
    asm volatile("" : "+v"(ko[2]));
  }
  SB();
  asm volatile("s_add_u32 m0, %0, 87040" :  : "s"(mW) : "memory", "scc"); SB();
  asm volatile("s_nop 0\n\tbuffer_load_dwordx4 %0, %1, %2 offen lds" :  : "v"(voB[1]), "s"(rW1), "s"(kw_[5]) : "memory"); SB();
  asm volatile("ds_write_b64 %0, %1 offset:17536" :  : "v"(kw), "v"(ko[2]) : "memory"); SB();
  asm volatile("s_waitcnt lgkmcnt(9)" : "+v"(kp[3]), "+v"(kc[3]), "+v"(vv[3]), "+v"(sw[3]) :  : "memory"); SB();
  {
    const unsigned w10 = shr16(sw[3][0], sw[3][0]), w32 = shr16(sw[3][1], sw[3][1]);
    const unsigned s0 = shr16(kp[3][1], kp[3][0]), s1 = shr16(kc[3][0], kp[3][1]), s2 = shr16(kc[3][1], kc[3][0]);
    const float r0 = dot2(s1, w10, dot2(s0, w32, 0.f)), r1 = dot2(kc[3][0], w10, dot2(kp[3][1], w32, 0.f));
    const float r2 = dot2(s2, w10, dot2(s1, w32, 0.f)), r3 = dot2(kc[3][1], w10, dot2(kc[3][0], w32, 0.f));
    ko[3] = (u32x2){pk2((f32x2){r0, r1} * unpk(vv[3][0])), pk2((f32x2){r2, r3} * unpk(vv[3][1]))};
    asm volatile("" : "+v"(ko[3]));
  }
  SB();
  asm volatile("ds_write_b64 %0, %1 offset:32768" :  : "v"(kw), "v"(ko[3]) : "memory"); SB();
  asm volatile("s_waitcnt lgkmcnt(6)" : "+v"(kp[4]), "+v"(kc[4]), "+v"(vv[4]), "+v"(sw[4]) :  : "memory"); SB();
  {
    const unsigned w10 = shr16(sw[4][0], sw[4][0]), w32 = shr16(sw[4][1], sw[4][1]);
    const unsigned s0 = shr16(kp[4][1], kp[4][0]), s1 = shr16(kc[4][0], kp[4][1]), s2 = shr16(kc[4][1], kc[4][0]);
    const float r0 = dot2(s1, w10, dot2(s0, w32, 0.f)), r1 = dot2(kc[4][0], w10, dot2(kp[4][1], w32, 0.f));
    const float r2 = dot2(s2, w10, dot2(s1, w32, 0.f)), r3 = dot2(kc[4][1], w10, dot2(kc[4][0], w32, 0.f));
    ko[4] = (u32x2){pk2((f32x2){r0, r1} * unpk(vv[4][0])), pk2((f32x2){r2, r3} * unpk(vv[4][1]))};
    asm volatile("" : "+v"(ko[4]));
  }
  SB();
  asm volatile("s_add_u32 m0, %0, 88064" :  : "s"(mW) : "memory", "scc"); SB();
  asm volatile("s_nop 0\n\tbuffer_load_dwordx4 %0, %1, %2 offen lds" :  : "v"(voB[2]), "s"(rW1), "s"(kw_[5]) : "memory"); SB();
  asm volatile("ds_write_b64 %0, %1 offset:35072" :  : "v"(kw2), "v"(ko[4]) : "memory"); SB();
  asm volatile("s_waitcnt lgkmcnt(0)\n\ts_barrier" :  :  : "memory"); SB();
  asm volatile("ds_read_b128 %0, %1 offset:0" : "=&v"(co[0]) : "v"(cr) : "memory"); SB();
  asm volatile("ds_read_b128 %0, %1 offset:64" : "=&v"(co[1]) : "v"(cr) : "memory"); SB();
  asm volatile("ds_read_b128 %0, %1 offset:128" : "=&v"(co[2]) : "v"(cr) : "memory"); SB();
  asm volatile("ds_read_b128 %0, %1 offset:0" : "=&v"(kq[0]) : "v"(kr) : "memory"); SB();
  asm volatile("ds_read_b128 %0, %1 offset:1152" : "=&v"(kq[1]) : "v"(kr) : "memory"); SB();
  asm volatile("ds_read_b128 %0, %1 offset:2304" : "=&v"(kq[2]) : "v"(kr) : "memory"); SB();
  asm volatile("s_waitcnt lgkmcnt(0)" : "+v"(kq[0]), "+v"(kq[1]), "+v"(kq[2]), "+v"(wt[0]), "+v"(wt[1]) :  : "memory"); SB();
  asm volatile("buffer_store_dwordx4 %1, %0, %2, %3 offen offset:0 sc1\n\ts_nop 1" :  : "v"(vKV), "v"(kq[0]), "s"(rWS), "s"(0u) : "memory"); SB();
  asm volatile("buffer_store_dwordx4 %1, %0, %2, %3 offen offset:0 sc1\n\ts_nop 1" :  : "v"(vKV), "v"(kq[1]), "s"(rWS), "s"(w3s + 32768u) : "memory"); SB();
  asm volatile("buffer_store_dwordx4 %1, %0, %2, %3 offen offset:0 sc1\n\ts_nop 1" :  : "v"(vKV), "v"(kq[2]), "s"(rWS), "s"(w3s + 65536u) : "memory"); SB();
  asm volatile("buffer_store_dwordx4 %1, %0, %2, %3 offen offset:0 sc1\n\ts_nop 1" :  : "v"(vWO), "v"(((u32x4){wt[0][0], wt[0][1], wt[1][0], wt[1][1]})), "s"(rWS), "s"(0u) : "memory"); SB();
  asm volatile("v_readfirstlane_b32 %0, v252\n\tv_readfirstlane_b32 %1, v253\n\ts_nop 0\n\ts_add_u32 %0, %0, 1\n\ts_addc_u32 %1, %1, 0" : "=&s"(tg0), "=&s"(tg1) :  : "memory", "scc"); SB();
  ST(5);
  const unsigned long long tg = ((unsigned long long)tg1 << 32) | tg0;
  asm volatile("s_add_u32 m0, %0, 0" :  : "s"(mW) : "memory", "scc"); SB();
  asm volatile("s_nop 0\n\tbuffer_load_dwordx4 %0, %1, %2 offen lds" :  : "v"(voB[0]), "s"(rW1), "s"(kw_[6]) : "memory"); SB();
  asm volatile("" : "+v"(a2[0][0]), "+v"(a2[0][1]), "+v"(a2[0][2]), "+v"(a2[1][0]), "+v"(a2[1][1]), "+v"(a2[1][2]), "+v"(co[0]), "+v"(co[1]), "+v"(co[2]) :  : "memory"); SB();
  c2[0] += co[0]; c2[1] += co[1]; c2[2] += co[2];
  auto act = [&](const f32x4& a, const f32x4& cs, int i, int h) {
    const f32x2 av = {a[2 * h], a[2 * h + 1]}, cv = {cs[2 * h], cs[2 * h + 1]};
    return gelu2(__builtin_elementwise_fma((f32x2){-xm[i], -xm[i]}, cv, av), k1[i], k2[i], xd[i]);
  };
  f32x2 gv[2][3][2];
  float sv[2][2], pv[2];
  gv[0][0][0] = act(a2[0][0], c2[0], 0, 0); gv[0][0][1] = act(a2[0][0], c2[0], 0, 1);
  asm volatile("" : "+v"(gv[0][0][0]), "+v"(gv[0][0][1]));
  SB();
  asm volatile("s_add_u32 m0, %0, 1024" :  : "s"(mW) : "memory", "scc"); SB();
  asm volatile("s_nop 0\n\tbuffer_load_dwordx4 %0, %1, %2 offen lds" :  : "v"(voB[1]), "s"(rW1), "s"(kw_[6]) : "memory"); SB();
  asm volatile("s_add_u32 m0, %0, 2048" :  : "s"(mW) : "memory", "scc"); SB();
  asm volatile("s_nop 0\n\tbuffer_load_dwordx4 %0, %1, %2 offen lds" :  : "v"(voB[2]), "s"(rW1), "s"(kw_[6]) : "memory"); SB();
  gv[0][1][0] = act(a2[0][1], c2[1], 0, 0); gv[0][1][1] = act(a2[0][1], c2[1], 0, 1);
  asm volatile("" : "+v"(gv[0][1][0]), "+v"(gv[0][1][1]));
  SB();
  asm volatile("s_add_u32 m0, %0, 12288" :  : "s"(mW) : "memory", "scc"); SB();
  asm volatile("s_nop 0\n\tbuffer_load_dwordx4 %0, %1, %2 offen lds" :  : "v"(voB[0]), "s"(rW1), "s"(kw_[7]) : "memory"); SB();
  gv[0][2][0] = act(a2[0][2], c2[2], 0, 0); gv[0][2][1] = act(a2[0][2], c2[2], 0, 1);
  asm volatile("" : "+v"(gv[0][2][0]), "+v"(gv[0][2][1]));
  SB();
  asm volatile("s_add_u32 m0, %0, 13312" :  : "s"(mW) : "memory", "scc"); SB();
  asm volatile("s_nop 0\n\tbuffer_load_dwordx4 %0, %1, %2 offen lds" :  : "v"(voB[1]), "s"(rW1), "s"(kw_[7]) : "memory"); SB();
  asm volatile("s_add_u32 m0, %0, 14336" :  : "s"(mW) : "memory", "scc"); SB();
  asm volatile("s_nop 0\n\tbuffer_load_dwordx4 %0, %1, %2 offen lds" :  : "v"(voB[2]), "s"(rW1), "s"(kw_[7]) : "memory"); SB();
  gv[1][0][0] = act(a2[1][0], c2[0], 1, 0); gv[1][0][1] = act(a2[1][0], c2[0], 1, 1);
  asm volatile("" : "+v"(gv[1][0][0]), "+v"(gv[1][0][1]));
  SB();
  gv[1][1][0] = act(a2[1][1], c2[1], 1, 0); gv[1][1][1] = act(a2[1][1], c2[1], 1, 1);
  asm volatile("" : "+v"(gv[1][1][0]), "+v"(gv[1][1][1]));
  SB();
  gv[1][2][0] = act(a2[1][2], c2[2], 1, 0); gv[1][2][1] = act(a2[1][2], c2[2], 1, 1);
  asm volatile("" : "+v"(gv[1][2][0]), "+v"(gv[1][2][1]));
  SB();
#pragma unroll
  for (int i = 0; i < 2; ++i) {
    const unsigned p0 = __float_as_uint(gv[i][0][0][0]);
    const auto pa = __builtin_amdgcn_permlane16_swap(p0, p0, false, false);
    const auto pb = __builtin_amdgcn_permlane32_swap(pa[0], pa[0], false, false);
    pv[i] = __uint_as_float(pb[0]);
    const f32x2 pp = {pv[i], pv[i]};
    f32x2 s1 = gv[i][0][0] - pp, s2 = s1 * s1;
#pragma unroll
    for (int n = 1; n < 6; ++n) { const f32x2 d = gv[i][n >> 1][n & 1] - pp; s1 += d; s2 = __builtin_elementwise_fma(d, d, s2); }
    sv[i][0] = s1[0] + s1[1]; sv[i][1] = s2[0] + s2[1];
  }
  {
    const float t1 = red16(sel32(sv[0][0], sv[1][0])), t2 = red16(sel32(sv[0][1], sv[1][1]));
    const float dl = t1 * (1.0f / 48.0f);
    const u32x4 so = {__float_as_uint((lane < 32 ? pv[0] : pv[1]) + dl), __float_as_uint(fmaxf(t2 - t1 * dl, 0.f)), tg0, tg1};
    SB();
  asm volatile("s_waitcnt vmcnt(25)" :  :  : "memory"); SB();
  asm volatile("s_mov_b32 exec_lo, 0xffff\n\ts_mov_b32 exec_hi, 0xffff\n\tbuffer_store_dwordx4 %1, %0, %2, %3 offen offset:0 sc1\n\ts_mov_b64 exec, -1\n\ts_nop 0" :  : "v"(vo), "v"(so), "s"(rWS), "s"(0u) : "memory"); SB();
  }
  ST(6);
  { unsigned lane = lane0, w = w0, rb = rb0, cb = cb0;
  asm volatile("" : "+v"(lane)); asm volatile("" : "+s"(w), "+s"(rb), "+s"(cb)); SB();
  const unsigned wm = w >> 1, wn = w & 1, g4 = lane >> 4, l15 = lane & 15, q4 = l15 >> 2, p4 = l15 & 3;
  const unsigned nch = 3 - (rb >> 4), ch0 = cb * 80 + rb * 2 + min(rb, 16u);
  const unsigned l16 = lane * 16;
  const unsigned vZ = W_Z2 + ((rb * 2 + wm) * 48 + cb * 6 + wn * 3) * 1024 + l16;
  const unsigned vH = W_HH + ((rb * 4 + w) * 24 + cb * 3) * 1024 + l16;
  const unsigned vY = W_YY + ch0 * 4096 + w * 1024 + l16;
  const unsigned c2o = (nch - 2) * 2, c2s = 8192u + ((rb >> 4) << 30), w3s = (w + 1) >> 2 << 30;
  (void)wm; (void)wn; (void)g4; (void)q4; (void)p4; (void)vZ; (void)vH; (void)vY; (void)c2o; (void)c2s; (void)w3s;
  unsigned zp[2][3][2];
#pragma unroll
  for (int n = 0; n < 12; ++n) { zp[n / 6][(n >> 1) % 3][n & 1] = pk2(gv[n / 6][(n >> 1) % 3][n & 1]); asm volatile("" : "+v"(zp[n / 6][(n >> 1) % 3][n & 1])); }
  SB();
  asm volatile("s_waitcnt vmcnt(23) lgkmcnt(0)\n\ts_barrier" :  :  : "memory"); SB();
  asm volatile("ds_read_b64_tr_b16 v[184:185], %0 offset:24576" :  : "v"(aW[0][0][0]) : "memory"); SB();
  asm volatile("ds_read_b64_tr_b16 v[186:187], %0 offset:25344" :  : "v"(aW[0][0][0]) : "memory"); SB();
  asm volatile("ds_read_b64_tr_b16 v[188:189], %0 offset:24576" :  : "v"(aW[0][0][1]) : "memory"); SB();
  asm volatile("ds_read_b64_tr_b16 v[190:191], %0 offset:25344" :  : "v"(aW[0][0][1]) : "memory"); SB();
  asm volatile("ds_read_b64_tr_b16 v[192:193], %0 offset:24576" :  : "v"(aW[0][0][2]) : "memory"); SB();
  asm volatile("ds_read_b64_tr_b16 v[194:195], %0 offset:25344" :  : "v"(aW[0][0][2]) : "memory"); SB();
  u32x2 zt[3][2];
  const unsigned zwa[2] = {lb + 98304 + 8192 + w * 16384 + (wn * 16 + l15) * 104 + g4 * 8, lb + 98304 + 8192 + w * 16384 + ((wn ^ 1) * 16 + l15) * 104 + g4 * 8};
  const unsigned zra = lb + 98304 + 8192 + w * 16384 + (8 * g4 + q4) * 104 + 8 * p4;
  const unsigned vS = W_ST + (rb * 2 + wm) * 8192 + l16;
  const auto rF = __builtin_amdgcn_make_buffer_rsrc((void*)ssm_k, 0, S_ * D_ * 2, 0x00020000);
  const unsigned vF = ch0 * 4096 + w * 1024 + l16, mU = lb + w * 1024;
  const unsigned vFK = W_FK + cb * 1024 + w * 256 + (lane & 31) * 8;
  unsigned dD[3], bs;
  const unsigned hpo = w ? ch0 * 4096 + w * 1024 - 4 : 0x40000000u;
  ST(7);
  SB();
  asm volatile("s_add_u32 m0, %0, 98304" :  : "s"(mA) : "memory", "scc"); SB();
  asm volatile("s_nop 0\n\tbuffer_load_dwordx4 %0, %1, %2 offen lds" :  : "v"(voS), "s"(rA), "s"(ao[0]) : "memory"); SB();
  asm volatile("s_add_u32 m0, %0, 99328" :  : "s"(mA) : "memory", "scc"); SB();
  asm volatile("s_nop 0\n\tbuffer_load_dwordx4 %0, %1, %2 offen lds" :  : "v"(voS), "s"(rA), "s"(ao[1]) : "memory"); SB();
  asm volatile("s_waitcnt lgkmcnt(4)\n\tv_mfma_f32_16x16x32_bf16 %0, v[184:187], a[0:3], 0" : "=&v"(a1[0][0]) :  : ); SB();
  asm volatile("ds_read_b64_tr_b16 v[160:161], %0 offset:24576" :  : "v"(aW[0][1][0]) : "memory"); SB();
  asm volatile("buffer_load_dwordx4 v[228:231], %0, %1, %2 offen" :  : "v"(vF), "s"(rF), "s"(0u) : "memory"); SB();
  asm volatile("v_mfma_f32_16x16x32_bf16 %0, v[184:187], a[4:7], 0" : "=&v"(a1[1][0]) :  : ); SB();
  asm volatile("ds_read_b64_tr_b16 v[162:163], %0 offset:25344" :  : "v"(aW[0][1][0]) : "memory"); SB();
  asm volatile("buffer_load_dword v242, off, %0, %1" :  : "s"(rF), "s"(hpo + 0u) : "memory"); SB();
  asm volatile("v_mfma_f32_16x16x32_bf16 %0, v[184:187], %1, 0" : "=&v"(c1[0]) : "v"(ones) : ); SB();
  asm volatile("ds_read_b64_tr_b16 v[164:165], %0 offset:24576" :  : "v"(aW[0][1][1]) : "memory"); SB();
  asm volatile("s_waitcnt lgkmcnt(5)\n\tv_mfma_f32_16x16x32_bf16 %0, v[188:191], a[0:3], 0" : "=&v"(a1[0][1]) :  : ); SB();
  asm volatile("ds_read_b64_tr_b16 v[166:167], %0 offset:25344" :  : "v"(aW[0][1][1]) : "memory"); SB();
  asm volatile("global_load_ushort %0, %1, %2" : "=&v"(dD[0]) : "v"(0u), "s"(ssm_D + (ch0 + 0) * 2) : "memory"); SB();
  asm volatile("v_mfma_f32_16x16x32_bf16 %0, v[188:191], a[4:7], 0" : "=&v"(a1[1][1]) :  : ); SB();
  asm volatile("ds_read_b64_tr_b16 v[168:169], %0 offset:24576" :  : "v"(aW[0][1][2]) : "memory"); SB();
  asm volatile("buffer_load_dwordx4 v[232:235], %0, %1, %2 offen" :  : "v"(vF), "s"(rF), "s"(4096u) : "memory"); SB();
  asm volatile("v_mfma_f32_16x16x32_bf16 %0, v[188:191], %1, 0" : "=&v"(c1[1]) : "v"(ones) : ); SB();
  asm volatile("ds_read_b64_tr_b16 v[170:171], %0 offset:25344" :  : "v"(aW[0][1][2]) : "memory"); SB();
  asm volatile("s_waitcnt lgkmcnt(6)\n\tv_mfma_f32_16x16x32_bf16 %0, v[192:195], a[0:3], 0" : "=&v"(a1[0][2]) :  : ); SB();
  asm volatile("ds_read_b64_tr_b16 v[172:173], %0 offset:36864" :  : "v"(aW[0][0][0]) : "memory"); SB();
  asm volatile("v_mfma_f32_16x16x32_bf16 %0, v[192:195], a[4:7], 0" : "=&v"(a1[1][2]) :  : ); SB();
  asm volatile("ds_read_b64_tr_b16 v[174:175], %0 offset:37632" :  : "v"(aW[0][0][0]) : "memory"); SB();
  asm volatile("buffer_load_dword v243, off, %0, %1" :  : "s"(rF), "s"(hpo + 4096u) : "memory"); SB();
  asm volatile("v_mfma_f32_16x16x32_bf16 %0, v[192:195], %1, 0" : "=&v"(c1[2]) : "v"(ones) : ); SB();
  asm volatile("ds_read_b64_tr_b16 v[176:177], %0 offset:36864" :  : "v"(aW[0][0][1]) : "memory"); SB();
  asm volatile("s_waitcnt lgkmcnt(7)\n\tv_mfma_f32_16x16x32_bf16 %0, v[160:163], a[8:11], %0" : "+v"(a1[0][0]) :  : ); SB();
  asm volatile("ds_read_b64_tr_b16 v[178:179], %0 offset:37632" :  : "v"(aW[0][0][1]) : "memory"); SB();
  asm volatile("v_mfma_f32_16x16x32_bf16 %0, v[160:163], a[12:15], %0" : "+v"(a1[1][0]) :  : ); SB();
  asm volatile("ds_read_b64_tr_b16 v[180:181], %0 offset:36864" :  : "v"(aW[0][0][2]) : "memory"); SB();
  asm volatile("s_waitcnt lgkmcnt(7)\n\tv_mfma_f32_16x16x32_bf16 %0, v[164:167], a[8:11], %0" : "+v"(a1[0][1]) :  : ); SB();
  asm volatile("ds_read_b64_tr_b16 v[182:183], %0 offset:37632" :  : "v"(aW[0][0][2]) : "memory"); SB();
  asm volatile("v_mfma_f32_16x16x32_bf16 %0, v[164:167], a[12:15], %0" : "+v"(a1[1][1]) :  : ); SB();
  asm volatile("ds_read_b64_tr_b16 v[184:185], %0 offset:36864" :  : "v"(aW[0][1][0]) : "memory"); SB();
  asm volatile("s_waitcnt lgkmcnt(7)\n\tv_mfma_f32_16x16x32_bf16 %0, v[168:171], a[8:11], %0" : "+v"(a1[0][2]) :  : ); SB();
  asm volatile("ds_read_b64_tr_b16 v[186:187], %0 offset:37632" :  : "v"(aW[0][1][0]) : "memory"); SB();
  asm volatile("v_mfma_f32_16x16x32_bf16 %0, v[168:171], a[12:15], %0" : "+v"(a1[1][2]) :  : ); SB();
  asm volatile("ds_read_b64_tr_b16 v[188:189], %0 offset:36864" :  : "v"(aW[0][1][1]) : "memory"); SB();
  asm volatile("s_waitcnt lgkmcnt(7)\n\tv_mfma_f32_16x16x32_bf16 %0, v[172:175], a[16:19], %0" : "+v"(a1[0][0]) :  : ); SB();
  asm volatile("ds_read_b64_tr_b16 v[190:191], %0 offset:37632" :  : "v"(aW[0][1][1]) : "memory"); SB();
  asm volatile("v_mfma_f32_16x16x32_bf16 %0, v[172:175], a[20:23], %0" : "+v"(a1[1][0]) :  : ); SB();
  asm volatile("ds_read_b64_tr_b16 v[192:193], %0 offset:36864" :  : "v"(aW[0][1][2]) : "memory"); SB();
  asm volatile("v_mfma_f32_16x16x32_bf16 %0, v[172:175], %1, %0" : "+v"(c1[0]) : "v"(ones) : ); SB();
  asm volatile("ds_read_b64_tr_b16 v[194:195], %0 offset:37632" :  : "v"(aW[0][1][2]) : "memory"); SB();
  asm volatile("s_waitcnt lgkmcnt(8)\n\tv_mfma_f32_16x16x32_bf16 %0, v[176:179], a[16:19], %0" : "+v"(a1[0][1]) :  : ); SB();
  asm volatile("s_waitcnt vmcnt(24)\n\ts_barrier" :  :  : "memory"); SB();
  asm volatile("ds_read_b64_tr_b16 v[160:161], %0 offset:0" :  : "v"(aW[1][0][0]) : "memory"); SB();
  asm volatile("v_mfma_f32_16x16x32_bf16 %0, v[176:179], a[20:23], %0" : "+v"(a1[1][1]) :  : ); SB();
  asm volatile("ds_read_b64_tr_b16 v[162:163], %0 offset:768" :  : "v"(aW[1][0][0]) : "memory"); SB();
  asm volatile("v_mfma_f32_16x16x32_bf16 %0, v[176:179], %1, %0" : "+v"(c1[1]) : "v"(ones) : ); SB();
  asm volatile("ds_read_b64_tr_b16 v[164:165], %0 offset:0" :  : "v"(aW[1][0][1]) : "memory"); SB();
  asm volatile("s_waitcnt vmcnt(14)\n\ts_mov_b64 exec, 1\n\tbuffer_store_dwordx2 %0, off, %1, %2 sc1\n\ts_mov_b64 exec, -1" :  : "v"(((u32x2){tg0, tg1})), "s"(rWS), "s"(W_FK + ((cb * 32 + rb) * 4 + w) * 8) : "memory"); SB();
  asm volatile("s_waitcnt lgkmcnt(9)\n\tv_mfma_f32_16x16x32_bf16 %0, v[180:183], a[16:19], %0" : "+v"(a1[0][2]) :  : ); SB();
  asm volatile("ds_read_b64_tr_b16 v[166:167], %0 offset:768" :  : "v"(aW[1][0][1]) : "memory"); SB();
  asm volatile("v_mfma_f32_16x16x32_bf16 %0, v[180:183], a[20:23], %0" : "+v"(a1[1][2]) :  : ); SB();
  asm volatile("ds_read_b64_tr_b16 v[168:169], %0 offset:0" :  : "v"(aW[1][0][2]) : "memory"); SB();
  asm volatile("v_mfma_f32_16x16x32_bf16 %0, v[180:183], %1, %0" : "+v"(c1[2]) : "v"(ones) : ); SB();
  asm volatile("ds_read_b64_tr_b16 v[170:171], %0 offset:768" :  : "v"(aW[1][0][2]) : "memory"); SB();
  asm volatile("s_waitcnt lgkmcnt(10)\n\tv_mfma_f32_16x16x32_bf16 %0, v[184:187], a[24:27], %0" : "+v"(a1[0][0]) :  : ); SB();
  asm volatile("ds_write_b64 %0, %1 offset:0" :  : "v"(zwa[0]), "v"(((u32x2){zp[0][0][0], zp[0][0][1]})) : "memory"); SB();
  asm volatile("v_mfma_f32_16x16x32_bf16 %0, v[184:187], a[28:31], %0" : "+v"(a1[1][0]) :  : ); SB();
  asm volatile("ds_write_b64 %0, %1 offset:32" :  : "v"(zwa[0]), "v"(((u32x2){zp[0][1][0], zp[0][1][1]})) : "memory"); SB();
  asm volatile("s_waitcnt lgkmcnt(10)\n\tv_mfma_f32_16x16x32_bf16 %0, v[188:191], a[24:27], %0" : "+v"(a1[0][1]) :  : ); SB();
  asm volatile("ds_read_b64_tr_b16 v[172:173], %0 offset:0" :  : "v"(aW[1][1][0]) : "memory"); SB();
  asm volatile("s_add_u32 m0, %0, 24576" :  : "s"(mW) : "memory", "scc"); SB();
  asm volatile("v_mfma_f32_16x16x32_bf16 %0, v[188:191], a[28:31], %0" : "+v"(a1[1][1]) :  : ); SB();
  asm volatile("ds_read_b64_tr_b16 v[174:175], %0 offset:768" :  : "v"(aW[1][1][0]) : "memory"); SB();
  asm volatile("s_waitcnt lgkmcnt(10)\n\tv_mfma_f32_16x16x32_bf16 %0, v[192:195], a[24:27], %0" : "+v"(a1[0][2]) :  : ); SB();
  asm volatile("ds_read_b64_tr_b16 v[176:177], %0 offset:0" :  : "v"(aW[1][1][1]) : "memory"); SB();
  asm volatile("buffer_load_dwordx4 %0, %1, %2 offen lds" :  : "v"(voB[0]), "s"(rW1), "s"(kw_[8]) : "memory"); SB();
  asm volatile("v_mfma_f32_16x16x32_bf16 %0, v[192:195], a[28:31], %0" : "+v"(a1[1][2]) :  : ); SB();
  asm volatile("ds_read_b64_tr_b16 v[178:179], %0 offset:768" :  : "v"(aW[1][1][1]) : "memory"); SB();
  asm volatile("s_waitcnt lgkmcnt(10)\n\tv_mfma_f32_16x16x32_bf16 %0, v[160:163], a[32:35], %0" : "+v"(a1[0][0]) :  : ); SB();
  asm volatile("ds_read_b64_tr_b16 v[180:181], %0 offset:0" :  : "v"(aW[1][1][2]) : "memory"); SB();
  asm volatile("v_mfma_f32_16x16x32_bf16 %0, v[160:163], a[36:39], %0" : "+v"(a1[1][0]) :  : ); SB();
  asm volatile("ds_read_b64_tr_b16 v[182:183], %0 offset:768" :  : "v"(aW[1][1][2]) : "memory"); SB();
  asm volatile("v_mfma_f32_16x16x32_bf16 %0, v[160:163], %1, %0" : "+v"(c1[0]) : "v"(ones) : ); SB();
  asm volatile("ds_read_b64_tr_b16 v[184:185], %0 offset:12288" :  : "v"(aW[1][0][0]) : "memory"); SB();
  asm volatile("s_add_u32 m0, %0, 25600" :  : "s"(mW) : "memory", "scc"); SB();
  asm volatile("s_waitcnt lgkmcnt(11)\n\tv_mfma_f32_16x16x32_bf16 %0, v[164:167], a[32:35], %0" : "+v"(a1[0][1]) :  : ); SB();
  asm volatile("ds_read_b64_tr_b16 v[186:187], %0 offset:13056" :  : "v"(aW[1][0][0]) : "memory"); SB();
  asm volatile("buffer_load_dwordx4 %0, %1, %2 offen lds" :  : "v"(voB[1]), "s"(rW1), "s"(kw_[8]) : "memory"); SB();
  asm volatile("v_mfma_f32_16x16x32_bf16 %0, v[164:167], a[36:39], %0" : "+v"(a1[1][1]) :  : ); SB();
  asm volatile("ds_read_b64_tr_b16 v[188:189], %0 offset:12288" :  : "v"(aW[1][0][1]) : "memory"); SB();
  asm volatile("v_mfma_f32_16x16x32_bf16 %0, v[164:167], %1, %0" : "+v"(c1[1]) : "v"(ones) : ); SB();
  asm volatile("ds_read_b64_tr_b16 v[190:191], %0 offset:13056" :  : "v"(aW[1][0][1]) : "memory"); SB();
  asm volatile("s_waitcnt lgkmcnt(12)\n\tv_mfma_f32_16x16x32_bf16 %0, v[168:171], a[32:35], %0" : "+v"(a1[0][2]) :  : ); SB();
  asm volatile("ds_read_b64_tr_b16 v[192:193], %0 offset:12288" :  : "v"(aW[1][0][2]) : "memory"); SB();
  asm volatile("v_mfma_f32_16x16x32_bf16 %0, v[168:171], a[36:39], %0" : "+v"(a1[1][2]) :  : ); SB();
  asm volatile("ds_read_b64_tr_b16 v[194:195], %0 offset:13056" :  : "v"(aW[1][0][2]) : "memory"); SB();
  asm volatile("v_mfma_f32_16x16x32_bf16 %0, v[168:171], %1, %0" : "+v"(c1[2]) : "v"(ones) : ); SB();
  asm volatile("ds_write_b64 %0, %1 offset:64" :  : "v"(zwa[0]), "v"(((u32x2){zp[0][2][0], zp[0][2][1]})) : "memory"); SB();
  asm volatile("s_waitcnt lgkmcnt(11)\n\tv_mfma_f32_16x16x32_bf16 %0, v[172:175], a[40:43], %0" : "+v"(a1[0][0]) :  : ); SB();
  asm volatile("ds_write_b64 %0, %1 offset:0" :  : "v"(zwa[1]), "v"(((u32x2){zp[1][0][0], zp[1][0][1]})) : "memory"); SB();
  asm volatile("s_add_u32 m0, %0, 26624" :  : "s"(mW) : "memory", "scc"); SB();
  asm volatile("v_mfma_f32_16x16x32_bf16 %0, v[172:175], a[44:47], %0" : "+v"(a1[1][0]) :  : ); SB();
  asm volatile("ds_write_b64 %0, %1 offset:32" :  : "v"(zwa[1]), "v"(((u32x2){zp[1][1][0], zp[1][1][1]})) : "memory"); SB();
  asm volatile("s_waitcnt lgkmcnt(11)\n\tv_mfma_f32_16x16x32_bf16 %0, v[176:179], a[40:43], %0" : "+v"(a1[0][1]) :  : ); SB();
  asm volatile("ds_read_b64_tr_b16 v[160:161], %0 offset:12288" :  : "v"(aW[1][1][0]) : "memory"); SB();
  asm volatile("buffer_load_dwordx4 %0, %1, %2 offen lds" :  : "v"(voB[2]), "s"(rW1), "s"(kw_[8]) : "memory"); SB();
  asm volatile("v_mfma_f32_16x16x32_bf16 %0, v[176:179], a[44:47], %0" : "+v"(a1[1][1]) :  : ); SB();
  asm volatile("ds_read_b64_tr_b16 v[162:163], %0 offset:13056" :  : "v"(aW[1][1][0]) : "memory"); SB();
  asm volatile("s_waitcnt lgkmcnt(11)\n\tv_mfma_f32_16x16x32_bf16 %0, v[180:183], a[40:43], %0" : "+v"(a1[0][2]) :  : ); SB();
  asm volatile("ds_read_b64_tr_b16 v[164:165], %0 offset:12288" :  : "v"(aW[1][1][1]) : "memory"); SB();
  asm volatile("v_mfma_f32_16x16x32_bf16 %0, v[180:183], a[44:47], %0" : "+v"(a1[1][2]) :  : ); SB();
  asm volatile("ds_read_b64_tr_b16 v[166:167], %0 offset:13056" :  : "v"(aW[1][1][1]) : "memory"); SB();
  asm volatile("s_waitcnt lgkmcnt(11)\n\tv_mfma_f32_16x16x32_bf16 %0, v[184:187], a[48:51], %0" : "+v"(a1[0][0]) :  : ); SB();
  asm volatile("ds_read_b64_tr_b16 v[168:169], %0 offset:12288" :  : "v"(aW[1][1][2]) : "memory"); SB();
  asm volatile("global_load_ushort %0, %1, %2" : "=&v"(dD[1]) : "v"(0u), "s"(ssm_D + (ch0 + 1) * 2) : "memory"); SB();
  asm volatile("v_mfma_f32_16x16x32_bf16 %0, v[184:187], a[52:55], %0" : "+v"(a1[1][0]) :  : ); SB();
  asm volatile("ds_read_b64_tr_b16 v[170:171], %0 offset:13056" :  : "v"(aW[1][1][2]) : "memory"); SB();
  asm volatile("v_mfma_f32_16x16x32_bf16 %0, v[184:187], %1, %0" : "+v"(c1[0]) : "v"(ones) : ); SB();
  asm volatile("s_barrier" :  :  : "memory"); SB();
  asm volatile("ds_read_b64_tr_b16 v[172:173], %0 offset:24576" :  : "v"(aW[1][0][0]) : "memory"); SB();
  asm volatile("s_waitcnt lgkmcnt(12)\n\tv_mfma_f32_16x16x32_bf16 %0, v[188:191], a[48:51], %0" : "+v"(a1[0][1]) :  : ); SB();
  asm volatile("ds_read_b64_tr_b16 v[174:175], %0 offset:25344" :  : "v"(aW[1][0][0]) : "memory"); SB();
  asm volatile("s_add_u32 m0, %0, 36864" :  : "s"(mW) : "memory", "scc"); SB();
  asm volatile("v_mfma_f32_16x16x32_bf16 %0, v[188:191], a[52:55], %0" : "+v"(a1[1][1]) :  : ); SB();
  asm volatile("ds_read_b64_tr_b16 v[176:177], %0 offset:24576" :  : "v"(aW[1][0][1]) : "memory"); SB();
  asm volatile("buffer_load_dwordx4 %0, %1, %2 offen lds" :  : "v"(voB[0]), "s"(rW1), "s"(kw_[9]) : "memory"); SB();
  asm volatile("v_mfma_f32_16x16x32_bf16 %0, v[188:191], %1, %0" : "+v"(c1[1]) : "v"(ones) : ); SB();
  asm volatile("ds_read_b64_tr_b16 v[178:179], %0 offset:25344" :  : "v"(aW[1][0][1]) : "memory"); SB();
  asm volatile("s_waitcnt lgkmcnt(13)\n\tv_mfma_f32_16x16x32_bf16 %0, v[192:195], a[48:51], %0" : "+v"(a1[0][2]) :  : ); SB();
  asm volatile("ds_read_b64_tr_b16 v[180:181], %0 offset:24576" :  : "v"(aW[1][0][2]) : "memory"); SB();
  asm volatile("v_mfma_f32_16x16x32_bf16 %0, v[192:195], a[52:55], %0" : "+v"(a1[1][2]) :  : ); SB();
  asm volatile("ds_read_b64_tr_b16 v[182:183], %0 offset:25344" :  : "v"(aW[1][0][2]) : "memory"); SB();
  asm volatile("v_mfma_f32_16x16x32_bf16 %0, v[192:195], %1, %0" : "+v"(c1[2]) : "v"(ones) : ); SB();
  asm volatile("ds_write_b64 %0, %1 offset:64" :  : "v"(zwa[1]), "v"(((u32x2){zp[1][2][0], zp[1][2][1]})) : "memory"); SB();
  asm volatile("s_waitcnt lgkmcnt(11)\n\tv_mfma_f32_16x16x32_bf16 %0, v[160:163], a[56:59], %0" : "+v"(a1[0][0]) :  : ); SB();
  asm volatile("ds_read_b64_tr_b16 %0, %1 offset:0" : "=&v"(zt[0][0]) : "v"(zra) : "memory"); SB();
  asm volatile("ds_read_b64_tr_b16 %0, %1 offset:416" : "=&v"(zt[0][1]) : "v"(zra) : "memory"); SB();
  asm volatile("v_mfma_f32_16x16x32_bf16 %0, v[160:163], a[60:63], %0" : "+v"(a1[1][0]) :  : ); SB();
  asm volatile("ds_read_b64_tr_b16 %0, %1 offset:32" : "=&v"(zt[1][0]) : "v"(zra) : "memory"); SB();
  asm volatile("s_add_u32 m0, %0, 37888" :  : "s"(mW) : "memory", "scc"); SB();
  asm volatile("s_waitcnt lgkmcnt(12)\n\tv_mfma_f32_16x16x32_bf16 %0, v[164:167], a[56:59], %0" : "+v"(a1[0][1]) :  : ); SB();
  asm volatile("ds_read_b64_tr_b16 v[184:185], %0 offset:24576" :  : "v"(aW[1][1][0]) : "memory"); SB();
  asm volatile("ds_read_b64_tr_b16 %0, %1 offset:448" : "=&v"(zt[1][1]) : "v"(zra) : "memory"); SB();
  asm volatile("v_mfma_f32_16x16x32_bf16 %0, v[164:167], a[60:63], %0" : "+v"(a1[1][1]) :  : ); SB();
  asm volatile("ds_read_b64_tr_b16 v[186:187], %0 offset:25344" :  : "v"(aW[1][1][0]) : "memory"); SB();
  asm volatile("s_waitcnt lgkmcnt(13)\n\tv_mfma_f32_16x16x32_bf16 %0, v[168:171], a[56:59], %0" : "+v"(a1[0][2]) :  : ); SB();
  asm volatile("ds_read_b64_tr_b16 v[188:189], %0 offset:24576" :  : "v"(aW[1][1][1]) : "memory"); SB();
  asm volatile("ds_read_b64_tr_b16 %0, %1 offset:64" : "=&v"(zt[2][0]) : "v"(zra) : "memory"); SB();
  asm volatile("v_mfma_f32_16x16x32_bf16 %0, v[168:171], a[60:63], %0" : "+v"(a1[1][2]) :  : ); SB();
  asm volatile("ds_read_b64_tr_b16 v[190:191], %0 offset:25344" :  : "v"(aW[1][1][1]) : "memory"); SB();
  asm volatile("s_waitcnt lgkmcnt(14)\n\tv_mfma_f32_16x16x32_bf16 %0, v[172:175], a[64:67], %0" : "+v"(a1[0][0]) :  : ); SB();
  asm volatile("ds_read_b64_tr_b16 v[192:193], %0 offset:24576" :  : "v"(aW[1][1][2]) : "memory"); SB();
  asm volatile("ds_read_b64_tr_b16 %0, %1 offset:480" : "=&v"(zt[2][1]) : "v"(zra) : "memory"); SB();
  asm volatile("v_mfma_f32_16x16x32_bf16 %0, v[172:175], a[68:71], %0" : "+v"(a1[1][0]) :  : ); SB();
  asm volatile("ds_read_b64_tr_b16 v[194:195], %0 offset:25344" :  : "v"(aW[1][1][2]) : "memory"); SB();
  asm volatile("buffer_load_dwordx4 %0, %1, %2 offen lds" :  : "v"(voB[1]), "s"(rW1), "s"(kw_[9]) : "memory"); SB();
  asm volatile("v_mfma_f32_16x16x32_bf16 %0, v[172:175], %1, %0" : "+v"(c1[0]) : "v"(ones) : ); SB();
  asm volatile("ds_read_b64_tr_b16 v[160:161], %0 offset:36864" :  : "v"(aW[1][0][0]) : "memory"); SB();
  asm volatile("s_add_u32 m0, %0, 38912" :  : "s"(mW) : "memory", "scc"); SB();
  asm volatile("s_waitcnt lgkmcnt(15)\n\tv_mfma_f32_16x16x32_bf16 %0, v[176:179], a[64:67], %0" : "+v"(a1[0][1]) :  : ); SB();
  asm volatile("ds_read_b64_tr_b16 v[162:163], %0 offset:37632" :  : "v"(aW[1][0][0]) : "memory"); SB();
  asm volatile("buffer_load_dwordx4 %0, %1, %2 offen lds" :  : "v"(voB[2]), "s"(rW1), "s"(kw_[9]) : "memory"); SB();
  asm volatile("v_mfma_f32_16x16x32_bf16 %0, v[176:179], a[68:71], %0" : "+v"(a1[1][1]) :  : ); SB();
  asm volatile("ds_read_b64_tr_b16 v[164:165], %0 offset:36864" :  : "v"(aW[1][0][1]) : "memory"); SB();
  asm volatile("buffer_load_dwordx4 v[236:239], %0, %1, %2 offen" :  : "v"(vF), "s"(rF), "s"(c2s) : "memory"); SB();
  asm volatile("v_mfma_f32_16x16x32_bf16 %0, v[176:179], %1, %0" : "+v"(c1[1]) : "v"(ones) : ); SB();
  asm volatile("ds_read_b64_tr_b16 v[166:167], %0 offset:37632" :  : "v"(aW[1][0][1]) : "memory"); SB();
  asm volatile("s_waitcnt lgkmcnt(15)\n\tv_mfma_f32_16x16x32_bf16 %0, v[180:183], a[64:67], %0" : "+v"(a1[0][2]) :  : ); SB();
  asm volatile("ds_read_b64_tr_b16 v[168:169], %0 offset:36864" :  : "v"(aW[1][0][2]) : "memory"); SB();
  asm volatile("s_waitcnt vmcnt(14)" :  :  : "memory"); SB();
  asm volatile("v_mfma_f32_16x16x32_bf16 %0, v[180:183], a[68:71], %0" : "+v"(a1[1][2]) :  : ); SB();
  asm volatile("ds_read_b64_tr_b16 v[170:171], %0 offset:37632" :  : "v"(aW[1][0][2]) : "memory"); SB();
  asm volatile("ds_read_b128 a[160:163], %0 offset:0" :  : "v"(aA[0]) : "memory"); SB();
  asm volatile("v_mfma_f32_16x16x32_bf16 %0, v[180:183], %1, %0" : "+v"(c1[2]) : "v"(ones) : ); SB();
  asm volatile("s_waitcnt lgkmcnt(13)\n\tv_mfma_f32_16x16x32_bf16 %0, v[184:187], a[72:75], %0" : "+v"(a1[0][0]) :  : ); SB();
  asm volatile("v_mfma_f32_16x16x32_bf16 %0, v[184:187], a[76:79], %0" : "+v"(a1[1][0]) :  : ); SB();
  asm volatile("s_waitcnt lgkmcnt(10)\n\tv_mfma_f32_16x16x32_bf16 %0, v[188:191], a[72:75], %0" : "+v"(a1[0][1]) :  : ); SB();
  asm volatile("ds_read_b64_tr_b16 v[172:173], %0 offset:36864" :  : "v"(aW[1][1][0]) : "memory"); SB();
  asm volatile("ds_read_b128 a[176:179], %0 offset:0" :  : "v"(aA[1]) : "memory"); SB();
  asm volatile("v_mfma_f32_16x16x32_bf16 %0, v[188:191], a[76:79], %0" : "+v"(a1[1][1]) :  : ); SB();
  asm volatile("ds_read_b64_tr_b16 v[174:175], %0 offset:37632" :  : "v"(aW[1][1][0]) : "memory"); SB();
  asm volatile("s_waitcnt lgkmcnt(10)\n\tv_mfma_f32_16x16x32_bf16 %0, v[192:195], a[72:75], %0" : "+v"(a1[0][2]) :  : ); SB();
  asm volatile("ds_read_b64_tr_b16 v[176:177], %0 offset:36864" :  : "v"(aW[1][1][1]) : "memory"); SB();
  asm volatile("buffer_load_dword v244, off, %0, %1" :  : "s"(rF), "s"(hpo + c2s) : "memory"); SB();
  asm volatile("v_mfma_f32_16x16x32_bf16 %0, v[192:195], a[76:79], %0" : "+v"(a1[1][2]) :  : ); SB();
  asm volatile("ds_read_b64_tr_b16 v[178:179], %0 offset:37632" :  : "v"(aW[1][1][1]) : "memory"); SB();
  asm volatile("s_waitcnt lgkmcnt(10)\n\tv_mfma_f32_16x16x32_bf16 %0, v[160:163], a[80:83], %0" : "+v"(a1[0][0]) :  : ); SB();
  asm volatile("ds_read_b64_tr_b16 v[180:181], %0 offset:36864" :  : "v"(aW[1][1][2]) : "memory"); SB();
  asm volatile("global_load_ushort %0, %1, %2" : "=&v"(dD[2]) : "v"(0u), "s"(ssm_D + (ch0 + c2o) * 2) : "memory"); SB();
  asm volatile("v_mfma_f32_16x16x32_bf16 %0, v[160:163], a[84:87], %0" : "+v"(a1[1][0]) :  : ); SB();
  asm volatile("ds_read_b64_tr_b16 v[182:183], %0 offset:37632" :  : "v"(aW[1][1][2]) : "memory"); SB();
  asm volatile("global_load_ushort %0, %1, %2" : "=&v"(bs) : "v"((rb * 64 + w * 16 + l15) * 2), "s"(b_sp) : "memory"); SB();
  asm volatile("v_mfma_f32_16x16x32_bf16 %0, v[160:163], %1, %0" : "+v"(c1[0]) : "v"(ones) : ); SB();
  asm volatile("s_barrier" :  :  : "memory"); SB();
  asm volatile("ds_read_b64_tr_b16 v[184:185], %0 offset:0" :  : "v"(aW[0][0][0]) : "memory"); SB();
  asm volatile("s_add_u32 m0, %0, 100352" :  : "s"(mA) : "memory", "scc"); SB();
  asm volatile("s_waitcnt lgkmcnt(11)\n\tv_mfma_f32_16x16x32_bf16 %0, v[164:167], a[80:83], %0" : "+v"(a1[0][1]) :  : ); SB();
  asm volatile("ds_read_b64_tr_b16 v[186:187], %0 offset:768" :  : "v"(aW[0][0][0]) : "memory"); SB();
  asm volatile("buffer_load_dwordx4 %0, %1, %2 offen lds" :  : "v"(voS), "s"(rA), "s"(ao[2]) : "memory"); SB();
  asm volatile("v_mfma_f32_16x16x32_bf16 %0, v[164:167], a[84:87], %0" : "+v"(a1[1][1]) :  : ); SB();
  asm volatile("ds_read_b64_tr_b16 v[188:189], %0 offset:0" :  : "v"(aW[0][0][1]) : "memory"); SB();
  asm volatile("v_mfma_f32_16x16x32_bf16 %0, v[164:167], %1, %0" : "+v"(c1[1]) : "v"(ones) : ); SB();
  asm volatile("ds_read_b64_tr_b16 v[190:191], %0 offset:768" :  : "v"(aW[0][0][1]) : "memory"); SB();
  asm volatile("s_waitcnt lgkmcnt(12)\n\tv_mfma_f32_16x16x32_bf16 %0, v[168:171], a[80:83], %0" : "+v"(a1[0][2]) :  : ); SB();
  asm volatile("ds_read_b64_tr_b16 v[192:193], %0 offset:0" :  : "v"(aW[0][0][2]) : "memory"); SB();
  asm volatile("s_add_u32 m0, %0, 101376" :  : "s"(mA) : "memory", "scc"); SB();
  asm volatile("v_mfma_f32_16x16x32_bf16 %0, v[168:171], a[84:87], %0" : "+v"(a1[1][2]) :  : ); SB();
  asm volatile("ds_read_b64_tr_b16 v[194:195], %0 offset:768" :  : "v"(aW[0][0][2]) : "memory"); SB();
  asm volatile("buffer_load_dwordx4 %0, %1, %2 offen lds" :  : "v"(voS), "s"(rA), "s"(ao[3]) : "memory"); SB();
  asm volatile("v_mfma_f32_16x16x32_bf16 %0, v[168:171], %1, %0" : "+v"(c1[2]) : "v"(ones) : ); SB();
  asm volatile("s_waitcnt lgkmcnt(10)\n\tv_mfma_f32_16x16x32_bf16 %0, v[172:175], a[88:91], %0" : "+v"(a1[0][0]) :  : ); SB();
  asm volatile("" : "+v"(zt[0][0]), "+v"(zt[0][1]), "+v"(zt[1][0]), "+v"(zt[1][1]), "+v"(zt[2][0]), "+v"(zt[2][1]) :  : "memory"); SB();
  asm volatile("buffer_load_dwordx4 v[196:199], %0, %1, %2 offen offset:0 sc1" :  : "v"(vS), "s"(rWS), "s"(0u) : "memory"); SB();
  asm volatile("buffer_load_dwordx4 v[200:203], %0, %1, %2 offen offset:1024 sc1" :  : "v"(vS), "s"(rWS), "s"(0u) : "memory"); SB();
  asm volatile("v_mfma_f32_16x16x32_bf16 %0, v[172:175], a[92:95], %0" : "+v"(a1[1][0]) :  : ); SB();
  asm volatile("buffer_load_dwordx4 v[204:207], %0, %1, %2 offen offset:2048 sc1" :  : "v"(vS), "s"(rWS), "s"(0u) : "memory"); SB();
  asm volatile("buffer_load_dwordx4 v[208:211], %0, %1, %2 offen offset:3072 sc1" :  : "v"(vS), "s"(rWS), "s"(0u) : "memory"); SB();
  asm volatile("s_waitcnt lgkmcnt(8)\n\tv_mfma_f32_16x16x32_bf16 %0, v[176:179], a[88:91], %0" : "+v"(a1[0][1]) :  : ); SB();
  asm volatile("ds_read_b64_tr_b16 v[160:161], %0 offset:0" :  : "v"(aW[0][1][0]) : "memory"); SB();
  asm volatile("buffer_load_dwordx4 v[212:215], %0, %1, %2 offen offset:0 sc1" :  : "v"(vS), "s"(rWS), "s"(4096u) : "memory"); SB();
  asm volatile("v_mfma_f32_16x16x32_bf16 %0, v[176:179], a[92:95], %0" : "+v"(a1[1][1]) :  : ); SB();
  asm volatile("ds_read_b64_tr_b16 v[162:163], %0 offset:768" :  : "v"(aW[0][1][0]) : "memory"); SB();
  asm volatile("s_waitcnt lgkmcnt(8)\n\tv_mfma_f32_16x16x32_bf16 %0, v[180:183], a[88:91], %0" : "+v"(a1[0][2]) :  : ); SB();
  asm volatile("ds_read_b64_tr_b16 v[164:165], %0 offset:0" :  : "v"(aW[0][1][1]) : "memory"); SB();
  asm volatile("buffer_load_dwordx4 v[216:219], %0, %1, %2 offen offset:1024 sc1" :  : "v"(vS), "s"(rWS), "s"(4096u) : "memory"); SB();
  asm volatile("v_mfma_f32_16x16x32_bf16 %0, v[180:183], a[92:95], %0" : "+v"(a1[1][2]) :  : ); SB();
  asm volatile("ds_read_b64_tr_b16 v[166:167], %0 offset:768" :  : "v"(aW[0][1][1]) : "memory"); SB();
  asm volatile("s_waitcnt lgkmcnt(8)\n\tv_mfma_f32_16x16x32_bf16 %0, v[184:187], a[96:99], %0" : "+v"(a1[0][0]) :  : ); SB();
  asm volatile("ds_read_b64_tr_b16 v[168:169], %0 offset:0" :  : "v"(aW[0][1][2]) : "memory"); SB();
  asm volatile("buffer_load_dwordx4 v[220:223], %0, %1, %2 offen offset:2048 sc1" :  : "v"(vS), "s"(rWS), "s"(4096u) : "memory"); SB();
  asm volatile("v_mfma_f32_16x16x32_bf16 %0, v[184:187], a[100:103], %0" : "+v"(a1[1][0]) :  : ); SB();
  asm volatile("ds_read_b64_tr_b16 v[170:171], %0 offset:768" :  : "v"(aW[0][1][2]) : "memory"); SB();
  asm volatile("buffer_load_dwordx4 v[224:227], %0, %1, %2 offen offset:3072 sc1" :  : "v"(vS), "s"(rWS), "s"(4096u) : "memory"); SB();
  asm volatile("v_mfma_f32_16x16x32_bf16 %0, v[184:187], %1, %0" : "+v"(c1[0]) : "v"(ones) : ); SB();
  asm volatile("ds_read_b64_tr_b16 v[172:173], %0 offset:12288" :  : "v"(aW[0][0][0]) : "memory"); SB();
  asm volatile("s_waitcnt lgkmcnt(9)\n\tv_mfma_f32_16x16x32_bf16 %0, v[188:191], a[96:99], %0" : "+v"(a1[0][1]) :  : ); SB();
  asm volatile("ds_read_b64_tr_b16 v[174:175], %0 offset:13056" :  : "v"(aW[0][0][0]) : "memory"); SB();
  asm volatile("v_mfma_f32_16x16x32_bf16 %0, v[188:191], a[100:103], %0" : "+v"(a1[1][1]) :  : ); SB();
  asm volatile("ds_read_b64_tr_b16 v[176:177], %0 offset:12288" :  : "v"(aW[0][0][1]) : "memory"); SB();
  asm volatile("v_mfma_f32_16x16x32_bf16 %0, v[188:191], %1, %0" : "+v"(c1[1]) : "v"(ones) : ); SB();
  asm volatile("ds_read_b64_tr_b16 v[178:179], %0 offset:13056" :  : "v"(aW[0][0][1]) : "memory"); SB();
  asm volatile("s_waitcnt lgkmcnt(10)\n\tv_mfma_f32_16x16x32_bf16 %0, v[192:195], a[96:99], %0" : "+v"(a1[0][2]) :  : ); SB();
  asm volatile("ds_read_b64_tr_b16 v[180:181], %0 offset:12288" :  : "v"(aW[0][0][2]) : "memory"); SB();
  asm volatile("v_mfma_f32_16x16x32_bf16 %0, v[192:195], a[100:103], %0" : "+v"(a1[1][2]) :  : ); SB();
  asm volatile("ds_read_b64_tr_b16 v[182:183], %0 offset:13056" :  : "v"(aW[0][0][2]) : "memory"); SB();
  asm volatile("v_mfma_f32_16x16x32_bf16 %0, v[192:195], %1, %0" : "+v"(c1[2]) : "v"(ones) : ); SB();
  asm volatile("s_waitcnt lgkmcnt(10)\n\tv_mfma_f32_16x16x32_bf16 %0, v[160:163], a[104:107], %0" : "+v"(a1[0][0]) :  : ); SB();
  asm volatile("v_mfma_f32_16x16x32_bf16 %0, v[160:163], a[108:111], %0" : "+v"(a1[1][0]) :  : ); SB();
  asm volatile("s_waitcnt lgkmcnt(8)\n\tv_mfma_f32_16x16x32_bf16 %0, v[164:167], a[104:107], %0" : "+v"(a1[0][1]) :  : ); SB();
  asm volatile("ds_read_b64_tr_b16 v[184:185], %0 offset:12288" :  : "v"(aW[0][1][0]) : "memory"); SB();
  asm volatile("v_mfma_f32_16x16x32_bf16 %0, v[164:167], a[108:111], %0" : "+v"(a1[1][1]) :  : ); SB();
  asm volatile("ds_read_b64_tr_b16 v[186:187], %0 offset:13056" :  : "v"(aW[0][1][0]) : "memory"); SB();
  asm volatile("s_waitcnt lgkmcnt(8)\n\tv_mfma_f32_16x16x32_bf16 %0, v[168:171], a[104:107], %0" : "+v"(a1[0][2]) :  : ); SB();
  asm volatile("ds_read_b64_tr_b16 v[188:189], %0 offset:12288" :  : "v"(aW[0][1][1]) : "memory"); SB();
  asm volatile("v_mfma_f32_16x16x32_bf16 %0, v[168:171], a[108:111], %0" : "+v"(a1[1][2]) :  : ); SB();
  asm volatile("ds_read_b64_tr_b16 v[190:191], %0 offset:13056" :  : "v"(aW[0][1][1]) : "memory"); SB();
  ST(8);
  asm volatile("s_add_u32 m0, %0, 102400" :  : "s"(mA) : "memory", "scc"); SB();
  asm volatile("s_nop 0\n\tbuffer_load_dwordx4 %0, %1, %2 offen lds" :  : "v"(voS), "s"(rA), "s"(ao[4]) : "memory"); SB();
  asm volatile("s_add_u32 m0, %0, 103424" :  : "s"(mA) : "memory", "scc"); SB();
  asm volatile("s_nop 0\n\tbuffer_load_dwordx4 %0, %1, %2 offen lds" :  : "v"(voS), "s"(rA), "s"(ao[5]) : "memory"); SB();
  asm volatile("s_add_u32 m0, %0, 104448" :  : "s"(mA) : "memory", "scc"); SB();
  asm volatile("s_nop 0\n\tbuffer_load_dwordx4 %0, %1, %2 offen lds" :  : "v"(voS), "s"(rA), "s"(ao[6]) : "memory"); SB();
  asm volatile("buffer_load_dwordx2 %0, %1, %2, 0 offen sc1" : "=&v"(fk) : "v"(vFK), "s"(rWS) : "memory"); SB();
  asm volatile("s_add_u32 m0, %0, 105472" :  : "s"(mA) : "memory", "scc"); SB();
  asm volatile("s_nop 0\n\tbuffer_load_dwordx4 %0, %1, %2 offen lds" :  : "v"(voS), "s"(rA), "s"(ao[7]) : "memory"); SB();
  asm volatile("s_add_u32 m0, %0, 106368" :  : "s"(mA) : "memory", "scc"); SB();
  asm volatile("s_nop 0\n\tbuffer_load_dwordx4 %0, %1, %2 offen offset:128 lds" :  : "v"(voS), "s"(rA), "s"(ao[0]) : "memory"); SB();
  asm volatile("s_waitcnt vmcnt(14)" :  :  : "memory"); SB();
  asm volatile("ds_read_b128 a[164:167], %0 offset:2048" :  : "v"(aA[0]) : "memory"); SB();
  asm volatile("ds_read_b128 a[180:183], %0 offset:2048" :  : "v"(aA[1]) : "memory"); SB();
  asm volatile("s_add_u32 m0, %0, 107392" :  : "s"(mA) : "memory", "scc"); SB();
  asm volatile("s_nop 0\n\tbuffer_load_dwordx4 %0, %1, %2 offen offset:128 lds" :  : "v"(voS), "s"(rA), "s"(ao[1]) : "memory"); SB();
  asm volatile("s_add_u32 m0, %0, 108416" :  : "s"(mA) : "memory", "scc"); SB();
  asm volatile("s_nop 0\n\tbuffer_load_dwordx4 %0, %1, %2 offen offset:128 lds" :  : "v"(voS), "s"(rA), "s"(ao[2]) : "memory"); SB();
  asm volatile("s_add_u32 m0, %0, 109440" :  : "s"(mA) : "memory", "scc"); SB();
  asm volatile("s_nop 0\n\tbuffer_load_dwordx4 %0, %1, %2 offen offset:128 lds" :  : "v"(voS), "s"(rA), "s"(ao[3]) : "memory"); SB();
  asm volatile("s_add_u32 m0, %0, 110464" :  : "s"(mA) : "memory", "scc"); SB();
  asm volatile("s_nop 0\n\tbuffer_load_dwordx4 %0, %1, %2 offen offset:128 lds" :  : "v"(voS), "s"(rA), "s"(ao[4]) : "memory"); SB();
  asm volatile("s_add_u32 m0, %0, 111488" :  : "s"(mA) : "memory", "scc"); SB();
  asm volatile("s_nop 0\n\tbuffer_load_dwordx4 %0, %1, %2 offen offset:128 lds" :  : "v"(voS), "s"(rA), "s"(ao[5]) : "memory"); SB();
  asm volatile("s_add_u32 m0, %0, 112512" :  : "s"(mA) : "memory", "scc"); SB();
  asm volatile("s_nop 0\n\tbuffer_load_dwordx4 %0, %1, %2 offen offset:128 lds" :  : "v"(voS), "s"(rA), "s"(ao[6]) : "memory"); SB();
  asm volatile("s_add_u32 m0, %0, 113536" :  : "s"(mA) : "memory", "scc"); SB();
  asm volatile("s_nop 0\n\tbuffer_load_dwordx4 %0, %1, %2 offen offset:128 lds" :  : "v"(voS), "s"(rA), "s"(ao[7]) : "memory"); SB();
  asm volatile("s_waitcnt vmcnt(13)\n\t.Lc_%=:\n\tv_cmpx_eq_u64 vcc, %1, v[198:199]\n\tv_cmpx_eq_u64 vcc, %1, v[202:203]\n\tv_cmpx_eq_u64 vcc, %1, v[206:207]\n\tv_cmpx_eq_u64 vcc, %1, v[210:211]\n\tv_cmpx_eq_u64 vcc, %1, v[214:215]\n\tv_cmpx_eq_u64 vcc, %1, v[218:219]\n\tv_cmpx_eq_u64 vcc, %1, v[222:223]\n\tv_cmpx_eq_u64 vcc, %1, v[226:227]\n\ts_cmp_eq_u64 vcc, -1\n\ts_mov_b64 exec, -1\n\ts_cbranch_scc0 .Lr_%=\n\t.Ld_%=:\n\t.subsection 1\n\t.Lr_%=:\n\tv_add_u32 %0, -1, %0\n\ts_nop 1\n\tv_readfirstlane_b32 vcc_hi, %0\n\ts_nop 3\n\ts_cmp_lt_i32 vcc_hi, 1\n\ts_cbranch_scc1 .Ld_%=\n\ts_sleep 2\n\ts_movk_i32 vcc_lo, 0x1000\n\tbuffer_load_dwordx4 v[196:199], %2, %3, 0 offen offset:0 sc1\n\tbuffer_load_dwordx4 v[200:203], %2, %3, 0 offen offset:1024 sc1\n\tbuffer_load_dwordx4 v[204:207], %2, %3, 0 offen offset:2048 sc1\n\tbuffer_load_dwordx4 v[208:211], %2, %3, 0 offen offset:3072 sc1\n\tbuffer_load_dwordx4 v[212:215], %2, %3, vcc_lo offen offset:0 sc1\n\tbuffer_load_dwordx4 v[216:219], %2, %3, vcc_lo offen offset:1024 sc1\n\tbuffer_load_dwordx4 v[220:223], %2, %3, vcc_lo offen offset:2048 sc1\n\tbuffer_load_dwordx4 v[224:227], %2, %3, vcc_lo offen offset:3072 sc1\n\ts_waitcnt vmcnt(0)\n\ts_branch .Lc_%=\n\t.subsection 0" : "+v"(tmo) : "s"(tg), "v"(vS), "s"(rWS) : "memory", "vcc", "scc"); SB();
  ST(9);
  {
    f32x2 s4[4], dm[4];
  asm volatile("v_pk_add_f32 %0, v[196:197], v[200:201]\n\tv_pk_add_f32 %1, v[204:205], v[208:209]\n\tv_pk_add_f32 %2, v[212:213], v[216:217]\n\tv_pk_add_f32 %3, v[220:221], v[224:225]" : "=&v"(s4[0]), "=&v"(s4[1]), "=&v"(s4[2]), "=&v"(s4[3]) :  : ); SB();
    const f32x2 sm = (s4[0] + s4[1]) + (s4[2] + s4[3]);
    const float mean = (sm[0] + dppf<0x128>(sm[0])) * (1.0f / 16.0f);
  asm volatile("v_sub_f32 %0, v196, %8\n\tv_sub_f32 %1, v200, %8\n\tv_sub_f32 %2, v204, %8\n\tv_sub_f32 %3, v208, %8\n\tv_sub_f32 %4, v212, %8\n\tv_sub_f32 %5, v216, %8\n\tv_sub_f32 %6, v220, %8\n\tv_sub_f32 %7, v224, %8" : "=&v"(dm[0][0]), "=&v"(dm[0][1]), "=&v"(dm[1][0]), "=&v"(dm[1][1]), "=&v"(dm[2][0]), "=&v"(dm[2][1]), "=&v"(dm[3][0]), "=&v"(dm[3][1]) : "v"(mean) : ); SB();
    const f32x2 dq = __builtin_elementwise_fma(dm[0], dm[0], dm[1] * dm[1]) + __builtin_elementwise_fma(dm[2], dm[2], dm[3] * dm[3]);
    float qs = __builtin_fmaf(dq[0] + dq[1], 48.0f, sm[1]);
    qs += dppf<0x128>(qs);
    const float r = __builtin_amdgcn_rcpf(rbf(__builtin_amdgcn_sqrtf(rbf(rbf(qs * (1.0f / 768.0f)) + 1e-5f))));
    const float mr = -rbf(mean) * r;
    const f32x2 re[4] = {{dppf<0x150>(r), dppf<0x151>(r)}, {dppf<0x152>(r), dppf<0x153>(r)}, {dppf<0x154>(r), dppf<0x155>(r)}, {dppf<0x156>(r), dppf<0x157>(r)}};
    const f32x2 me[4] = {{dppf<0x150>(mr), dppf<0x151>(mr)}, {dppf<0x152>(mr), dppf<0x153>(mr)}, {dppf<0x154>(mr), dppf<0x155>(mr)}, {dppf<0x156>(mr), dppf<0x157>(mr)}};
    u32x4 zo[3];
#pragma unroll
    for (int j = 0; j < 3; ++j)
#pragma unroll
      for (int e = 0; e < 4; ++e) zo[j][e] = pk2(__builtin_elementwise_fma(unpk(zt[j][e >> 1][e & 1]), re[e], me[e]));
    SB();
  asm volatile("buffer_store_dwordx4 %1, %0, %2, %3 offen offset:0 sc1\n\ts_nop 1" :  : "v"(vZ), "v"(zo[0]), "s"(rWS), "s"(0u) : "memory"); SB();
  asm volatile("buffer_store_dwordx4 %1, %0, %2, %3 offen offset:1024 sc1\n\ts_nop 1" :  : "v"(vZ), "v"(zo[1]), "s"(rWS), "s"(0u) : "memory"); SB();
  asm volatile("buffer_store_dwordx4 %1, %0, %2, %3 offen offset:2048 sc1\n\ts_nop 1" :  : "v"(vZ), "v"(zo[2]), "s"(rWS), "s"(0u) : "memory"); SB();
  }
  ST(10);
  asm volatile("s_waitcnt lgkmcnt(10)\n\tv_mfma_f32_16x16x32_bf16 %0, v[172:175], a[112:115], %0" : "+v"(a1[0][0]) :  : ); SB();
  asm volatile("ds_read_b64_tr_b16 v[192:193], %0 offset:12288" :  : "v"(aW[0][1][2]) : "memory"); SB();
  asm volatile("s_waitcnt vmcnt(14)" :  :  : "memory"); SB();
  asm volatile("v_mfma_f32_16x16x32_bf16 %0, v[172:175], a[116:119], %0" : "+v"(a1[1][0]) :  : ); SB();
  asm volatile("ds_read_b64_tr_b16 v[194:195], %0 offset:13056" :  : "v"(aW[0][1][2]) : "memory"); SB();
  asm volatile("ds_read_b128 a[168:171], %0 offset:4096" :  : "v"(aA[0]) : "memory"); SB();
  asm volatile("v_mfma_f32_16x16x32_bf16 %0, v[172:175], %1, %0" : "+v"(c1[0]) : "v"(ones) : ); SB();
  asm volatile("s_barrier" :  :  : "memory"); SB();
  asm volatile("ds_read_b64_tr_b16 v[160:161], %0 offset:24576" :  : "v"(aW[0][0][0]) : "memory"); SB();
  asm volatile("ds_read_b128 a[184:187], %0 offset:4096" :  : "v"(aA[1]) : "memory"); SB();
  asm volatile("s_waitcnt lgkmcnt(13)\n\tv_mfma_f32_16x16x32_bf16 %0, v[176:179], a[112:115], %0" : "+v"(a1[0][1]) :  : ); SB();
  asm volatile("ds_read_b64_tr_b16 v[162:163], %0 offset:25344" :  : "v"(aW[0][0][0]) : "memory"); SB();
  asm volatile("s_waitcnt vmcnt(11)" :  :  : "memory"); SB();
  asm volatile("v_mfma_f32_16x16x32_bf16 %0, v[176:179], a[116:119], %0" : "+v"(a1[1][1]) :  : ); SB();
  asm volatile("ds_read_b64_tr_b16 v[164:165], %0 offset:24576" :  : "v"(aW[0][0][1]) : "memory"); SB();
  asm volatile("ds_read_b128 a[172:175], %0 offset:6144" :  : "v"(aA[0]) : "memory"); SB();
  asm volatile("v_mfma_f32_16x16x32_bf16 %0, v[176:179], %1, %0" : "+v"(c1[1]) : "v"(ones) : ); SB();
  asm volatile("ds_read_b64_tr_b16 v[166:167], %0 offset:25344" :  : "v"(aW[0][0][1]) : "memory"); SB();
  asm volatile("ds_read_b128 a[188:191], %0 offset:6144" :  : "v"(aA[1]) : "memory"); SB();
  asm volatile("s_waitcnt lgkmcnt(15)\n\tv_mfma_f32_16x16x32_bf16 %0, v[180:183], a[112:115], %0" : "+v"(a1[0][2]) :  : ); SB();
  asm volatile("ds_read_b64_tr_b16 v[168:169], %0 offset:24576" :  : "v"(aW[0][0][2]) : "memory"); SB();
  asm volatile("s_waitcnt vmcnt(9)" :  :  : "memory"); SB();
  asm volatile("v_mfma_f32_16x16x32_bf16 %0, v[180:183], a[116:119], %0" : "+v"(a1[1][2]) :  : ); SB();
  asm volatile("ds_read_b64_tr_b16 v[170:171], %0 offset:25344" :  : "v"(aW[0][0][2]) : "memory"); SB();
  asm volatile("ds_read_b128 a[192:195], %0 offset:8192" :  : "v"(aA[0]) : "memory"); SB();
  asm volatile("v_mfma_f32_16x16x32_bf16 %0, v[180:183], %1, %0" : "+v"(c1[2]) : "v"(ones) : ); SB();
  asm volatile("ds_read_b128 a[208:211], %0 offset:8192" :  : "v"(aA[1]) : "memory"); SB();
  asm volatile("s_waitcnt vmcnt(7)" :  :  : "memory"); SB();
  asm volatile("s_waitcnt lgkmcnt(15)\n\tv_mfma_f32_16x16x32_bf16 %0, v[184:187], a[120:123], %0" : "+v"(a1[0][0]) :  : ); SB();
  asm volatile("ds_read_b128 a[196:199], %0 offset:10240" :  : "v"(aA[0]) : "memory"); SB();
  asm volatile("ds_read_b128 a[212:215], %0 offset:10240" :  : "v"(aA[1]) : "memory"); SB();
  asm volatile("v_mfma_f32_16x16x32_bf16 %0, v[184:187], a[124:127], %0" : "+v"(a1[1][0]) :  : ); SB();
  asm volatile("s_waitcnt vmcnt(5)" :  :  : "memory"); SB();
  asm volatile("ds_read_b128 a[200:203], %0 offset:12288" :  : "v"(aA[0]) : "memory"); SB();
  asm volatile("v_mfma_f32_16x16x32_bf16 %0, v[188:191], a[120:123], %0" : "+v"(a1[0][1]) :  : ); SB();
  asm volatile("ds_read_b64_tr_b16 v[172:173], %0 offset:24576" :  : "v"(aW[0][1][0]) : "memory"); SB();
  asm volatile("ds_read_b128 a[216:219], %0 offset:12288" :  : "v"(aA[1]) : "memory"); SB();
  asm volatile("v_mfma_f32_16x16x32_bf16 %0, v[188:191], a[124:127], %0" : "+v"(a1[1][1]) :  : ); SB();
  asm volatile("ds_read_b64_tr_b16 v[174:175], %0 offset:25344" :  : "v"(aW[0][1][0]) : "memory"); SB();
  asm volatile(".Lc_%=:\n\tv_cmp_ne_u64 vcc, %2, %1\n\ts_cbranch_vccnz .Lr_%=\n\t.Ld_%=:\n\t.subsection 1\n\t.Lr_%=:\n\tv_add_u32 %0, -1, %0\n\ts_nop 1\n\tv_readfirstlane_b32 vcc_hi, %0\n\ts_nop 3\n\ts_cmp_lt_i32 vcc_hi, 1\n\ts_cbranch_scc1 .Ld_%=\n\ts_sleep 2\n\tbuffer_load_dwordx2 %1, %3, %4, 0 offen sc1\n\ts_waitcnt vmcnt(0)\n\ts_branch .Lc_%=\n\t.subsection 0" : "+v"(tmo), "+v"(fk) : "s"(tg), "v"(vFK), "s"(rWS) : "memory", "vcc", "scc"); SB();
  asm volatile("s_add_u32 m0, %0, 49664" :  : "s"(mU) : "memory", "scc"); SB();
  asm volatile("s_waitcnt lgkmcnt(15)\n\tv_mfma_f32_16x16x32_bf16 %0, v[192:195], a[120:123], %0" : "+v"(a1[0][2]) :  : ); SB();
  asm volatile("ds_read_b64_tr_b16 v[176:177], %0 offset:24576" :  : "v"(aW[0][1][1]) : "memory"); SB();
  asm volatile("buffer_load_dwordx4 %0, %1, %2 offen sc1 lds" :  : "v"(vF), "s"(rWS), "s"(W_KV + 0u) : "memory"); SB();
  asm volatile("v_mfma_f32_16x16x32_bf16 %0, v[192:195], a[124:127], %0" : "+v"(a1[1][2]) :  : ); SB();
  asm volatile("ds_read_b64_tr_b16 v[178:179], %0 offset:25344" :  : "v"(aW[0][1][1]) : "memory"); SB();
  asm volatile("s_waitcnt vmcnt(4)" :  :  : "memory"); SB();
  asm volatile("s_waitcnt lgkmcnt(15)\n\tv_mfma_f32_16x16x32_bf16 %0, v[160:163], a[128:131], %0" : "+v"(a1[0][0]) :  : ); SB();
  asm volatile("ds_read_b64_tr_b16 v[180:181], %0 offset:24576" :  : "v"(aW[0][1][2]) : "memory"); SB();
  asm volatile("ds_read_b128 a[204:207], %0 offset:14336" :  : "v"(aA[0]) : "memory"); SB();
  asm volatile("v_mfma_f32_16x16x32_bf16 %0, v[160:163], a[132:135], %0" : "+v"(a1[1][0]) :  : ); SB();
  asm volatile("ds_read_b64_tr_b16 v[182:183], %0 offset:25344" :  : "v"(aW[0][1][2]) : "memory"); SB();
  asm volatile("ds_read_b128 a[220:223], %0 offset:14336" :  : "v"(aA[1]) : "memory"); SB();
  asm volatile("v_mfma_f32_16x16x32_bf16 %0, v[160:163], %1, %0" : "+v"(c1[0]) : "v"(ones) : ); SB();
  asm volatile("ds_read_b64_tr_b16 v[184:185], %0 offset:36864" :  : "v"(aW[0][0][0]) : "memory"); SB();
  asm volatile("s_add_u32 m0, %0, 62592" :  : "s"(mU) : "memory", "scc"); SB();
  asm volatile("s_waitcnt lgkmcnt(15)\n\tv_mfma_f32_16x16x32_bf16 %0, v[164:167], a[128:131], %0" : "+v"(a1[0][1]) :  : ); SB();
  asm volatile("ds_read_b64_tr_b16 v[186:187], %0 offset:37632" :  : "v"(aW[0][0][0]) : "memory"); SB();
  asm volatile("buffer_load_dwordx4 %0, %1, %2 offen sc1 lds" :  : "v"(vF), "s"(rWS), "s"(W_KV + 4096u) : "memory"); SB();
  asm volatile("v_mfma_f32_16x16x32_bf16 %0, v[164:167], a[132:135], %0" : "+v"(a1[1][1]) :  : ); SB();
  asm volatile("ds_read_b64_tr_b16 v[188:189], %0 offset:36864" :  : "v"(aW[0][0][1]) : "memory"); SB();
  asm volatile("s_add_u32 m0, %0, 75520" :  : "s"(mU) : "memory", "scc"); SB();
  asm volatile("v_mfma_f32_16x16x32_bf16 %0, v[164:167], %1, %0" : "+v"(c1[1]) : "v"(ones) : ); SB();
  asm volatile("ds_read_b64_tr_b16 v[190:191], %0 offset:37632" :  : "v"(aW[0][0][1]) : "memory"); SB();
  asm volatile("buffer_load_dwordx4 %0, %1, %2 offen sc1 lds" :  : "v"(vF), "s"(rWS), "s"(W_KV + c2s) : "memory"); SB();
  asm volatile("v_mfma_f32_16x16x32_bf16 %0, v[168:171], a[128:131], %0" : "+v"(a1[0][2]) :  : ); SB();
  asm volatile("ds_read_b64_tr_b16 v[192:193], %0 offset:36864" :  : "v"(aW[0][0][2]) : "memory"); SB();
  asm volatile("s_add_u32 m0, %0, 98048" :  : "s"(mA) : "memory", "scc"); SB();
  asm volatile("v_mfma_f32_16x16x32_bf16 %0, v[168:171], a[132:135], %0" : "+v"(a1[1][2]) :  : ); SB();
  asm volatile("ds_read_b64_tr_b16 v[194:195], %0 offset:37632" :  : "v"(aW[0][0][2]) : "memory"); SB();
  asm volatile("buffer_load_dwordx4 %0, %1, %2 offen offset:256 lds" :  : "v"(voS), "s"(rA), "s"(ao[0]) : "memory"); SB();
  asm volatile("v_mfma_f32_16x16x32_bf16 %0, v[168:171], %1, %0" : "+v"(c1[2]) : "v"(ones) : ); SB();
  asm volatile("s_add_u32 m0, %0, 99072" :  : "s"(mA) : "memory", "scc"); SB();
  asm volatile("s_waitcnt lgkmcnt(12)\n\tv_mfma_f32_16x16x32_bf16 %0, v[172:175], a[136:139], %0" : "+v"(a1[0][0]) :  : ); SB();
  asm volatile("buffer_load_dwordx4 %0, %1, %2 offen offset:256 lds" :  : "v"(voS), "s"(rA), "s"(ao[1]) : "memory"); SB();
  asm volatile("s_add_u32 m0, %0, 100096" :  : "s"(mA) : "memory", "scc"); SB();
  asm volatile("v_mfma_f32_16x16x32_bf16 %0, v[172:175], a[140:143], %0" : "+v"(a1[1][0]) :  : ); SB();
  asm volatile("buffer_load_dwordx4 %0, %1, %2 offen offset:256 lds" :  : "v"(voS), "s"(rA), "s"(ao[2]) : "memory"); SB();
  asm volatile("s_add_u32 m0, %0, 101120" :  : "s"(mA) : "memory", "scc"); SB();
  asm volatile("s_waitcnt lgkmcnt(10)\n\tv_mfma_f32_16x16x32_bf16 %0, v[176:179], a[136:139], %0" : "+v"(a1[0][1]) :  : ); SB();
  asm volatile("ds_read_b64_tr_b16 v[160:161], %0 offset:36864" :  : "v"(aW[0][1][0]) : "memory"); SB();
  asm volatile("buffer_load_dwordx4 %0, %1, %2 offen offset:256 lds" :  : "v"(voS), "s"(rA), "s"(ao[3]) : "memory"); SB();
  asm volatile("v_mfma_f32_16x16x32_bf16 %0, v[176:179], a[140:143], %0" : "+v"(a1[1][1]) :  : ); SB();
  asm volatile("ds_read_b64_tr_b16 v[162:163], %0 offset:37632" :  : "v"(aW[0][1][0]) : "memory"); SB();
  asm volatile("s_add_u32 m0, %0, 102144" :  : "s"(mA) : "memory", "scc"); SB();
  asm volatile("s_waitcnt lgkmcnt(9)\n\tv_mfma_f32_16x16x32_bf16 %0, v[180:183], a[136:139], %0" : "+v"(a1[0][2]) :  : ); SB();
  asm volatile("ds_read_b64_tr_b16 v[164:165], %0 offset:36864" :  : "v"(aW[0][1][1]) : "memory"); SB();
  asm volatile("buffer_load_dwordx4 %0, %1, %2 offen offset:256 lds" :  : "v"(voS), "s"(rA), "s"(ao[4]) : "memory"); SB();
  asm volatile("v_mfma_f32_16x16x32_bf16 %0, v[180:183], a[140:143], %0" : "+v"(a1[1][2]) :  : ); SB();
  asm volatile("ds_read_b64_tr_b16 v[166:167], %0 offset:37632" :  : "v"(aW[0][1][1]) : "memory"); SB();
  asm volatile("s_add_u32 m0, %0, 103168" :  : "s"(mA) : "memory", "scc"); SB();
  asm volatile("s_waitcnt lgkmcnt(8)\n\tv_mfma_f32_16x16x32_bf16 %0, v[184:187], a[144:147], %0" : "+v"(a1[0][0]) :  : ); SB();
  asm volatile("ds_read_b64_tr_b16 v[168:169], %0 offset:36864" :  : "v"(aW[0][1][2]) : "memory"); SB();
  asm volatile("buffer_load_dwordx4 %0, %1, %2 offen offset:256 lds" :  : "v"(voS), "s"(rA), "s"(ao[5]) : "memory"); SB();
  asm volatile("v_mfma_f32_16x16x32_bf16 %0, v[184:187], a[148:151], %0" : "+v"(a1[1][0]) :  : ); SB();
  asm volatile("ds_read_b64_tr_b16 v[170:171], %0 offset:37632" :  : "v"(aW[0][1][2]) : "memory"); SB();
  asm volatile("s_add_u32 m0, %0, 104192" :  : "s"(mA) : "memory", "scc"); SB();
  asm volatile("v_mfma_f32_16x16x32_bf16 %0, v[184:187], %1, %0" : "+v"(c1[0]) : "v"(ones) : ); SB();
  asm volatile("buffer_load_dwordx4 %0, %1, %2 offen offset:256 lds" :  : "v"(voS), "s"(rA), "s"(ao[6]) : "memory"); SB();
  asm volatile("s_add_u32 m0, %0, 105216" :  : "s"(mA) : "memory", "scc"); SB();
  asm volatile("s_waitcnt lgkmcnt(8)\n\tv_mfma_f32_16x16x32_bf16 %0, v[188:191], a[144:147], %0" : "+v"(a1[0][1]) :  : ); SB();
  asm volatile("buffer_load_dwordx4 %0, %1, %2 offen offset:256 lds" :  : "v"(voS), "s"(rA), "s"(ao[7]) : "memory"); SB();
  asm volatile("s_add_u32 m0, %0, 106112" :  : "s"(mA) : "memory", "scc"); SB();
  asm volatile("v_mfma_f32_16x16x32_bf16 %0, v[188:191], a[148:151], %0" : "+v"(a1[1][1]) :  : ); SB();
  asm volatile("buffer_load_dwordx4 %0, %1, %2 offen offset:384 lds" :  : "v"(voS), "s"(rA), "s"(ao[0]) : "memory"); SB();
  asm volatile("v_mfma_f32_16x16x32_bf16 %0, v[188:191], %1, %0" : "+v"(c1[1]) : "v"(ones) : ); SB();
  asm volatile("s_waitcnt lgkmcnt(6)\n\tv_mfma_f32_16x16x32_bf16 %0, v[192:195], a[144:147], %0" : "+v"(a1[0][2]) :  : ); SB();
  asm volatile("v_mfma_f32_16x16x32_bf16 %0, v[192:195], a[148:151], %0" : "+v"(a1[1][2]) :  : ); SB();
  asm volatile("v_mfma_f32_16x16x32_bf16 %0, v[192:195], %1, %0" : "+v"(c1[2]) : "v"(ones) : ); SB();
  asm volatile("s_waitcnt lgkmcnt(4)\n\tv_mfma_f32_16x16x32_bf16 %0, v[160:163], a[152:155], %0" : "+v"(a1[0][0]) :  : ); SB();
  asm volatile("v_mfma_f32_16x16x32_bf16 %0, v[160:163], a[156:159], %0" : "+v"(a1[1][0]) :  : ); SB();
  asm volatile("s_waitcnt lgkmcnt(2)\n\tv_mfma_f32_16x16x32_bf16 %0, v[164:167], a[152:155], %0" : "+v"(a1[0][1]) :  : ); SB();
  asm volatile("v_mfma_f32_16x16x32_bf16 %0, v[164:167], a[156:159], %0" : "+v"(a1[1][1]) :  : ); SB();
  asm volatile("s_waitcnt lgkmcnt(0)\n\tv_mfma_f32_16x16x32_bf16 %0, v[168:171], a[152:155], %0" : "+v"(a1[0][2]) :  : ); SB();
  asm volatile("v_mfma_f32_16x16x32_bf16 %0, v[168:171], a[156:159], %0" : "+v"(a1[1][2]) :  : ); SB();
  ST(11);
  asm volatile("s_nop 7" : "+v"(a1[0][0]), "+v"(a1[0][1]), "+v"(a1[0][2]), "+v"(a1[1][0]), "+v"(a1[1][1]), "+v"(a1[1][2]), "+v"(c1[0]), "+v"(c1[1]), "+v"(c1[2]) :  : ); SB();
  ca = lb + 87936 + w * 192 + g4 * 16; cr = lb + 87936 + (w ^ 2) * 192 + g4 * 16;
  asm volatile("ds_write_b128 %0, %1 offset:0" :  : "v"(ca), "v"(c1[0]) : "memory"); SB();
  asm volatile("ds_write_b128 %0, %1 offset:64" :  : "v"(ca), "v"(c1[1]) : "memory"); SB();
  asm volatile("ds_write_b128 %0, %1 offset:128" :  : "v"(ca), "v"(c1[2]) : "memory"); SB();
  asm volatile("" : "+v"(dD[0]), "+v"(dD[1]), "+v"(dD[2]), "+v"(bs) :  : "memory"); SB();
  {
    const unsigned ha = lb + 49152 + 4608 + (2040 - 8 * (w * 64 + lane)) * 2;
    const unsigned za = lb + 49152 + min(w, 2u) * 12928 + (lane < 32 ? lane * 16 : 4608 + ((lane >> 1) & 1) * 4160 + 4096 + (lane & 1) * 16);
    u32x4 sw[3], mx[3];
  asm volatile("ds_write_b128 %0, %1 offset:0" :  : "v"(za), "v"(((u32x4){0u, 0u, 0u, 0u})) : "memory"); SB();
  asm volatile("v_mov_b32_dpp v242, v231 wave_shr:1 row_mask:0xf bank_mask:0xf\n\tv_alignbit_b32 %0, v231, v231, 16\n\tv_alignbit_b32 %1, v230, v230, 16\n\tv_alignbit_b32 %2, v229, v229, 16\n\tv_alignbit_b32 %3, v228, v228, 16\n\tv_bfi_b32 %4, %8, v231, v230\n\tv_bfi_b32 %5, %8, v230, v229\n\tv_bfi_b32 %6, %8, v229, v228\n\tv_bfi_b32 %7, %8, v228, v242" : "=&v"(sw[0][0]), "=&v"(sw[0][1]), "=&v"(sw[0][2]), "=&v"(sw[0][3]), "=&v"(mx[0][0]), "=&v"(mx[0][1]), "=&v"(mx[0][2]), "=&v"(mx[0][3]) : "s"(0xFFFFu) : ); SB();
  asm volatile("ds_write_b128 %0, %1 offset:0" :  : "v"(ha), "v"(sw[0]) : "memory"); SB();
  asm volatile("ds_write_b128 %0, %1 offset:4160" :  : "v"(ha), "v"(mx[0]) : "memory"); SB();
  asm volatile("s_add_u32 m0, %0, 107136" :  : "s"(mA) : "memory", "scc"); SB();
  asm volatile("s_nop 0\n\tbuffer_load_dwordx4 %0, %1, %2 offen offset:384 lds" :  : "v"(voS), "s"(rA), "s"(ao[1]) : "memory"); SB();
  asm volatile("v_mov_b32_dpp v243, v235 wave_shr:1 row_mask:0xf bank_mask:0xf\n\tv_alignbit_b32 %0, v235, v235, 16\n\tv_alignbit_b32 %1, v234, v234, 16\n\tv_alignbit_b32 %2, v233, v233, 16\n\tv_alignbit_b32 %3, v232, v232, 16\n\tv_bfi_b32 %4, %8, v235, v234\n\tv_bfi_b32 %5, %8, v234, v233\n\tv_bfi_b32 %6, %8, v233, v232\n\tv_bfi_b32 %7, %8, v232, v243" : "=&v"(sw[1][0]), "=&v"(sw[1][1]), "=&v"(sw[1][2]), "=&v"(sw[1][3]), "=&v"(mx[1][0]), "=&v"(mx[1][1]), "=&v"(mx[1][2]), "=&v"(mx[1][3]) : "s"(0xFFFFu) : ); SB();
  asm volatile("ds_write_b128 %0, %1 offset:12928" :  : "v"(ha), "v"(sw[1]) : "memory"); SB();
  asm volatile("ds_write_b128 %0, %1 offset:17088" :  : "v"(ha), "v"(mx[1]) : "memory"); SB();
  asm volatile("s_add_u32 m0, %0, 108160" :  : "s"(mA) : "memory", "scc"); SB();
  asm volatile("s_nop 0\n\tbuffer_load_dwordx4 %0, %1, %2 offen offset:384 lds" :  : "v"(voS), "s"(rA), "s"(ao[2]) : "memory"); SB();
  asm volatile("v_mov_b32_dpp v244, v239 wave_shr:1 row_mask:0xf bank_mask:0xf\n\tv_alignbit_b32 %0, v239, v239, 16\n\tv_alignbit_b32 %1, v238, v238, 16\n\tv_alignbit_b32 %2, v237, v237, 16\n\tv_alignbit_b32 %3, v236, v236, 16\n\tv_bfi_b32 %4, %8, v239, v238\n\tv_bfi_b32 %5, %8, v238, v237\n\tv_bfi_b32 %6, %8, v237, v236\n\tv_bfi_b32 %7, %8, v236, v244" : "=&v"(sw[2][0]), "=&v"(sw[2][1]), "=&v"(sw[2][2]), "=&v"(sw[2][3]), "=&v"(mx[2][0]), "=&v"(mx[2][1]), "=&v"(mx[2][2]), "=&v"(mx[2][3]) : "s"(0xFFFFu) : ); SB();
  asm volatile("ds_write_b128 %0, %1 offset:25856" :  : "v"(ha), "v"(sw[2]) : "memory"); SB();
  asm volatile("ds_write_b128 %0, %1 offset:30016" :  : "v"(ha), "v"(mx[2]) : "memory"); SB();
  }
  ST(12);
  asm volatile("s_waitcnt lgkmcnt(0)\n\ts_barrier" :  :  : "memory"); SB();
  asm volatile("ds_read_b128 %0, %1 offset:0" : "=&v"(co[0]) : "v"(cr) : "memory"); SB();
  asm volatile("ds_read_b128 %0, %1 offset:64" : "=&v"(co[1]) : "v"(cr) : "memory"); SB();
  asm volatile("ds_read_b128 %0, %1 offset:128" : "=&v"(co[2]) : "v"(cr) : "memory"); SB();
  asm volatile("s_waitcnt vmcnt(14)\n\ts_mov_b64 exec, 1\n\tbuffer_store_dwordx2 %0, off, %1, %2 sc1\n\ts_mov_b64 exec, -1" :  : "v"(((u32x2){tg0, tg1})), "s"(rWS), "s"(W_FZ + ((cb * 32 + rb) * 4 + w) * 8) : "memory"); SB();
  asm volatile("s_add_u32 m0, %0, 109184" :  : "s"(mA) : "memory", "scc"); SB();
  asm volatile("s_nop 0\n\tbuffer_load_dwordx4 %0, %1, %2 offen offset:384 lds" :  : "v"(voS), "s"(rA), "s"(ao[3]) : "memory"); SB();
  asm volatile("s_add_u32 m0, %0, 110208" :  : "s"(mA) : "memory", "scc"); SB();
  asm volatile("s_nop 0\n\tbuffer_load_dwordx4 %0, %1, %2 offen offset:384 lds" :  : "v"(voS), "s"(rA), "s"(ao[4]) : "memory"); SB();
  asm volatile("s_waitcnt lgkmcnt(0)" : "+v"(co[0]), "+v"(co[1]), "+v"(co[2]) :  : "memory"); SB();
#pragma unroll
  for (int j = 0; j < 3; ++j) {
    const f32x4 cs = c1[j] + co[j];
#pragma unroll
    for (int i = 0; i < 2; ++i) { a1[i][j] = __builtin_elementwise_fma((f32x4){-xm[i], -xm[i], -xm[i], -xm[i]}, cs, a1[i][j]); asm volatile("" : "+v"(a1[i][j])); }
  }
  const unsigned ew = lb + 32768 + w * 3072 + l16, er = lb + 32768 + (w ^ 1) * 3072 + l16;
  SB();
  asm volatile("s_waitcnt vmcnt(12)" :  :  : "memory"); SB();
  asm volatile("ds_read_b128 a[224:227], %0 offset:0" :  : "v"(aA[0]) : "memory"); SB();
  asm volatile("ds_read_b128 a[240:243], %0 offset:0" :  : "v"(aA[1]) : "memory"); SB();
  asm volatile("s_add_u32 m0, %0, 111232" :  : "s"(mA) : "memory", "scc"); SB();
  asm volatile("s_nop 0\n\tbuffer_load_dwordx4 %0, %1, %2 offen offset:384 lds" :  : "v"(voS), "s"(rA), "s"(ao[5]) : "memory"); SB();
  asm volatile("ds_write_b128 %0, %1 offset:0" :  : "v"(ew), "v"(a1[1][0]) : "memory"); SB();
  asm volatile("ds_write_b128 %0, %1 offset:1024" :  : "v"(ew), "v"(a1[1][1]) : "memory"); SB();
  asm volatile("ds_write_b128 %0, %1 offset:2048" :  : "v"(ew), "v"(a1[1][2]) : "memory"); SB();
  ST(13);
  { unsigned lane = lane0, w = w0, rb = rb0, cb = cb0;
  asm volatile("" : "+v"(lane)); asm volatile("" : "+s"(w), "+s"(rb), "+s"(cb)); SB();
  const unsigned wm = w >> 1, wn = w & 1, g4 = lane >> 4, l15 = lane & 15, q4 = l15 >> 2, p4 = l15 & 3;
  const unsigned nch = 3 - (rb >> 4), ch0 = cb * 80 + rb * 2 + min(rb, 16u);
  const unsigned l16 = lane * 16;
  const unsigned vZ = W_Z2 + ((rb * 2 + wm) * 48 + cb * 6 + wn * 3) * 1024 + l16;
  const unsigned vH = W_HH + ((rb * 4 + w) * 24 + cb * 3) * 1024 + l16;
  const unsigned vY = W_YY + ch0 * 4096 + w * 1024 + l16;
  const unsigned c2o = (nch - 2) * 2, c2s = 8192u + ((rb >> 4) << 30), w3s = (w + 1) >> 2 << 30;
  (void)wm; (void)wn; (void)g4; (void)q4; (void)p4; (void)vZ; (void)vH; (void)vY; (void)c2o; (void)c2s; (void)w3s;
  f32x4 ac[8];
  unsigned aa[4], bb;
  u32x2 cA[2][8][2];
  unsigned cu = w * 2 * nch, cn = cu;
  int dl;
  {
    const unsigned c = (l15 + 1) & 1;
    const unsigned o = lb + 49152 + (cu >> 3) * 12928 - (cu & 7) * 64;
#pragma unroll
    for (int k = 0; k < 4; ++k) aa[k] = o + 4608 + c * 4160 + (2031 - l15 + 8 * g4 - c) * 2 - 512 - 1024 * k;
    bb = o + 512 + 32 * l15 - 32 + 16 * g4;
  }
  unsigned da = lb + 0 + g4 * 256 + ((l15 ^ ((g4 >> 1) << 3)) << 4);
  unsigned vB = W_Z2 + (w * 768 + cb * 6) * 1024 + l16;
  unsigned vFZ = W_FZ + cb * 1024 + w * 256 + (lane & 31) * 8;
  asm volatile("" : "+v"(aa[0]), "+v"(aa[1]), "+v"(aa[2]), "+v"(aa[3]), "+v"(bb), "+v"(da), "+v"(vB), "+v"(vFZ));
  SB();
  asm volatile("s_waitcnt vmcnt(11)" :  :  : "memory"); SB();
  asm volatile("ds_read_b128 a[228:231], %0 offset:2048" :  : "v"(aA[0]) : "memory"); SB();
  asm volatile("ds_read_b128 a[244:247], %0 offset:2048" :  : "v"(aA[1]) : "memory"); SB();
  asm volatile("s_waitcnt vmcnt(9)" :  :  : "memory"); SB();
  asm volatile("ds_read_b128 a[232:235], %0 offset:4096" :  : "v"(aA[0]) : "memory"); SB();
  asm volatile("ds_read_b128 a[248:251], %0 offset:4096" :  : "v"(aA[1]) : "memory"); SB();
  asm volatile("s_waitcnt lgkmcnt(0)\n\ts_barrier" :  :  : "memory"); SB();
  ST(14);
  asm volatile("ds_read_b128 %0, %1 offset:0" : "=&v"(a1[1][0]) : "v"(er) : "memory"); SB();
  asm volatile("ds_read_b128 %0, %1 offset:1024" : "=&v"(a1[1][1]) : "v"(er) : "memory"); SB();
  asm volatile("ds_read_b128 %0, %1 offset:2048" : "=&v"(a1[1][2]) : "v"(er) : "memory"); SB();
  asm volatile("ds_read2_b32 %0, %1 offset0:128 offset1:129" : "=&v"(cA[0][0][0]) : "v"(aa[0]) : "memory"); SB();
  asm volatile("ds_read2_b32 %0, %1 offset0:130 offset1:131" : "=&v"(cA[0][0][1]) : "v"(aa[0]) : "memory"); SB();
  asm volatile("ds_read_b128 a[124:127], %0 offset:3584" :  : "v"(bb) : "memory"); SB();
  asm volatile("ds_read_b128 a[120:123], %0 offset:3072" :  : "v"(bb) : "memory"); SB();
  asm volatile("ds_read_b128 a[116:119], %0 offset:2560" :  : "v"(bb) : "memory"); SB();
  asm volatile("ds_read_b128 a[112:115], %0 offset:2048" :  : "v"(bb) : "memory"); SB();
  asm volatile("ds_read2_b32 %0, %1 offset0:0 offset1:1" : "=&v"(cA[0][1][0]) : "v"(aa[0]) : "memory"); SB();
  asm volatile("ds_read2_b32 %0, %1 offset0:2 offset1:3" : "=&v"(cA[0][1][1]) : "v"(aa[0]) : "memory"); SB();
  asm volatile("ds_read_b128 a[108:111], %0 offset:1536" :  : "v"(bb) : "memory"); SB();
  asm volatile("ds_read2_b32 %0, %1 offset0:128 offset1:129" : "=&v"(cA[0][2][0]) : "v"(aa[1]) : "memory"); SB();
  asm volatile("ds_read2_b32 %0, %1 offset0:130 offset1:131" : "=&v"(cA[0][2][1]) : "v"(aa[1]) : "memory"); SB();
  asm volatile("ds_read2_b32 %0, %1 offset0:0 offset1:1" : "=&v"(cA[0][3][0]) : "v"(aa[1]) : "memory"); SB();
  asm volatile("ds_read2_b32 %0, %1 offset0:2 offset1:3" : "=&v"(cA[0][3][1]) : "v"(aa[1]) : "memory"); SB();
  asm volatile("ds_read_b128 a[104:107], %0 offset:1024" :  : "v"(bb) : "memory"); SB();
  asm volatile("ds_read2_b32 %0, %1 offset0:128 offset1:129" : "=&v"(cA[0][4][0]) : "v"(aa[2]) : "memory"); SB();
  asm volatile("ds_read2_b32 %0, %1 offset0:130 offset1:131" : "=&v"(cA[0][4][1]) : "v"(aa[2]) : "memory"); SB();
  asm volatile("ds_read2_b32 %0, %1 offset0:0 offset1:1" : "=&v"(cA[0][5][0]) : "v"(aa[2]) : "memory"); SB();
  asm volatile("ds_read2_b32 %0, %1 offset0:2 offset1:3" : "=&v"(cA[0][5][1]) : "v"(aa[2]) : "memory"); SB();
  asm volatile("ds_read_b128 a[100:103], %0 offset:512" :  : "v"(bb) : "memory"); SB();
  asm volatile("ds_read2_b32 %0, %1 offset0:128 offset1:129" : "=&v"(cA[0][6][0]) : "v"(aa[3]) : "memory"); SB();
  asm volatile("ds_read2_b32 %0, %1 offset0:130 offset1:131" : "=&v"(cA[0][6][1]) : "v"(aa[3]) : "memory"); SB();
  asm volatile("ds_read2_b32 %0, %1 offset0:0 offset1:1" : "=&v"(cA[0][7][0]) : "v"(aa[3]) : "memory"); SB();
  asm volatile("ds_read2_b32 %0, %1 offset0:2 offset1:3" : "=&v"(cA[0][7][1]) : "v"(aa[3]) : "memory"); SB();
  asm volatile("ds_read_b128 a[96:99], %0 offset:0" :  : "v"(bb) : "memory"); SB();
  ++cn; dl = (cn & 7) ? -64 : 13376; asm volatile("" : "+s"(dl)); SB();
  asm volatile("v_add_u32 %0, %1, %0" : "+v"(aa[0]) : "s"(dl) : ); SB();
  asm volatile("v_add_u32 %0, %1, %0" : "+v"(aa[1]) : "s"(dl) : ); SB();
  asm volatile("v_add_u32 %0, %1, %0" : "+v"(aa[2]) : "s"(dl) : ); SB();
  asm volatile("v_add_u32 %0, %1, %0" : "+v"(aa[3]) : "s"(dl) : ); SB();
  asm volatile("v_add_u32 %0, %1, %0" : "+v"(bb) : "s"(dl) : ); SB();
  asm volatile("s_waitcnt vmcnt(7)" :  :  : "memory"); SB();
  asm volatile("ds_read_b128 a[236:239], %0 offset:6144" :  : "v"(aA[0]) : "memory"); SB();
  asm volatile("ds_read_b128 a[252:255], %0 offset:6144" :  : "v"(aA[1]) : "memory"); SB();
  asm volatile("s_waitcnt vmcnt(5)" :  :  : "memory"); SB();
  asm volatile("ds_read_b128 a[0:3], %0 offset:8192" :  : "v"(aA[0]) : "memory"); SB();
  asm volatile("ds_read_b128 a[16:19], %0 offset:8192" :  : "v"(aA[1]) : "memory"); SB();
  asm volatile("s_waitcnt lgkmcnt(15)" : "+v"(a1[1][0]), "+v"(a1[1][1]), "+v"(a1[1][2]) :  : "memory"); SB();
  if (wn) {
  asm volatile("v_swap_b32 %0, %4\n\tv_swap_b32 %1, %5\n\tv_swap_b32 %2, %6\n\tv_swap_b32 %3, %7" : "+v"(a1[0][0][0]), "+v"(a1[0][0][1]), "+v"(a1[0][0][2]), "+v"(a1[0][0][3]), "+v"(a1[1][0][0]), "+v"(a1[1][0][1]), "+v"(a1[1][0][2]), "+v"(a1[1][0][3]) :  : ); SB();
  asm volatile("v_swap_b32 %0, %4\n\tv_swap_b32 %1, %5\n\tv_swap_b32 %2, %6\n\tv_swap_b32 %3, %7" : "+v"(a1[0][1][0]), "+v"(a1[0][1][1]), "+v"(a1[0][1][2]), "+v"(a1[0][1][3]), "+v"(a1[1][1][0]), "+v"(a1[1][1][1]), "+v"(a1[1][1][2]), "+v"(a1[1][1][3]) :  : ); SB();
  asm volatile("v_swap_b32 %0, %4\n\tv_swap_b32 %1, %5\n\tv_swap_b32 %2, %6\n\tv_swap_b32 %3, %7" : "+v"(a1[0][2][0]), "+v"(a1[0][2][1]), "+v"(a1[0][2][2]), "+v"(a1[0][2][3]), "+v"(a1[1][2][0]), "+v"(a1[1][2][1]), "+v"(a1[1][2][2]), "+v"(a1[1][2][3]) :  : ); SB();
  }
  asm volatile("v_mfma_f32_16x16x32_bf16 %0, %1, a[124:127], 0" : "=&v"(ac[7]) : "v"(((u32x4){cA[0][0][0][0], cA[0][0][0][1], cA[0][0][1][0], cA[0][0][1][1]})) : ); SB();
  asm volatile("ds_read2_b32 %0, %1 offset0:128 offset1:129" : "=&v"(cA[1][0][0]) : "v"(aa[0]) : "memory"); SB();
  asm volatile("s_waitcnt vmcnt(2)" :  :  : "memory"); SB();
  asm volatile("v_mfma_f32_16x16x32_bf16 %0, %1, a[120:123], 0" : "=&v"(ac[6]) : "v"(((u32x4){cA[0][0][0][0], cA[0][0][0][1], cA[0][0][1][0], cA[0][0][1][1]})) : ); SB();
  asm volatile("ds_read2_b32 %0, %1 offset0:130 offset1:131" : "=&v"(cA[1][0][1]) : "v"(aa[0]) : "memory"); SB();
  asm volatile("ds_read_b128 a[4:7], %0 offset:10240" :  : "v"(aA[0]) : "memory"); SB();
  asm volatile("v_mfma_f32_16x16x32_bf16 %0, %1, a[116:119], 0" : "=&v"(ac[5]) : "v"(((u32x4){cA[0][0][0][0], cA[0][0][0][1], cA[0][0][1][0], cA[0][0][1][1]})) : ); SB();
  asm volatile("ds_read_b128 a[156:159], %0 offset:3584" :  : "v"(bb) : "memory"); SB();
  asm volatile("ds_read_b128 a[20:23], %0 offset:10240" :  : "v"(aA[1]) : "memory"); SB();
  asm volatile("v_mfma_f32_16x16x32_bf16 %0, %1, a[112:115], 0" : "=&v"(ac[4]) : "v"(((u32x4){cA[0][0][0][0], cA[0][0][0][1], cA[0][0][1][0], cA[0][0][1][1]})) : ); SB();
  asm volatile("ds_read_b128 a[152:155], %0 offset:3072" :  : "v"(bb) : "memory"); SB();
  asm volatile("s_add_u32 m0, %0, 112256" :  : "s"(mA) : "memory", "scc"); SB();
  asm volatile("v_mfma_f32_16x16x32_bf16 %0, %1, a[120:123], %0" : "+v"(ac[7]) : "v"(((u32x4){cA[0][1][0][0], cA[0][1][0][1], cA[0][1][1][0], cA[0][1][1][1]})) : ); SB();
  asm volatile("buffer_load_dwordx4 %0, %1, %2 offen offset:384 lds" :  : "v"(voS), "s"(rA), "s"(ao[6]) : "memory"); SB();
  asm volatile("s_add_u32 m0, %0, 113280" :  : "s"(mA) : "memory", "scc"); SB();
  asm volatile("v_mfma_f32_16x16x32_bf16 %0, %1, a[116:119], %0" : "+v"(ac[6]) : "v"(((u32x4){cA[0][1][0][0], cA[0][1][0][1], cA[0][1][1][0], cA[0][1][1][1]})) : ); SB();
  asm volatile("ds_read_b128 a[148:151], %0 offset:2560" :  : "v"(bb) : "memory"); SB();
  asm volatile("buffer_load_dwordx4 %0, %1, %2 offen offset:384 lds" :  : "v"(voS), "s"(rA), "s"(ao[7]) : "memory"); SB();
  asm volatile("v_mfma_f32_16x16x32_bf16 %0, %1, a[112:115], %0" : "+v"(ac[5]) : "v"(((u32x4){cA[0][1][0][0], cA[0][1][0][1], cA[0][1][1][0], cA[0][1][1][1]})) : ); SB();
  asm volatile("ds_read_b128 a[144:147], %0 offset:2048" :  : "v"(bb) : "memory"); SB();
  asm volatile("s_waitcnt vmcnt(2)" :  :  : "memory"); SB();
  asm volatile("v_mfma_f32_16x16x32_bf16 %0, %1, a[108:111], %0" : "+v"(ac[4]) : "v"(((u32x4){cA[0][1][0][0], cA[0][1][0][1], cA[0][1][1][0], cA[0][1][1][1]})) : ); SB();
  asm volatile("ds_read2_b32 %0, %1 offset0:0 offset1:1" : "=&v"(cA[1][1][0]) : "v"(aa[0]) : "memory"); SB();
  asm volatile("ds_read_b128 a[8:11], %0 offset:12288" :  : "v"(aA[0]) : "memory"); SB();
  asm volatile("v_mfma_f32_16x16x32_bf16 %0, %1, a[116:119], %0" : "+v"(ac[7]) : "v"(((u32x4){cA[0][2][0][0], cA[0][2][0][1], cA[0][2][1][0], cA[0][2][1][1]})) : ); SB();
  asm volatile("ds_read2_b32 %0, %1 offset0:2 offset1:3" : "=&v"(cA[1][1][1]) : "v"(aa[0]) : "memory"); SB();
  asm volatile("ds_read_b128 a[24:27], %0 offset:12288" :  : "v"(aA[1]) : "memory"); SB();
  asm volatile("v_mfma_f32_16x16x32_bf16 %0, %1, a[112:115], %0" : "+v"(ac[6]) : "v"(((u32x4){cA[0][2][0][0], cA[0][2][0][1], cA[0][2][1][0], cA[0][2][1][1]})) : ); SB();
  asm volatile("s_add_u32 m0, %0, 97792" :  : "s"(mA) : "memory", "scc"); SB();
  asm volatile("v_mfma_f32_16x16x32_bf16 %0, %1, a[108:111], %0" : "+v"(ac[5]) : "v"(((u32x4){cA[0][2][0][0], cA[0][2][0][1], cA[0][2][1][0], cA[0][2][1][1]})) : ); SB();
  asm volatile("ds_read_b128 a[140:143], %0 offset:1536" :  : "v"(bb) : "memory"); SB();
  asm volatile("buffer_load_dwordx4 %0, %1, %2 offen offset:512 lds" :  : "v"(voS), "s"(rA), "s"(ao[0]) : "memory"); SB();
  asm volatile("v_mfma_f32_16x16x32_bf16 %0, %1, a[108:111], 0" : "=&v"(ac[3]) : "v"(((u32x4){cA[0][0][0][0], cA[0][0][0][1], cA[0][0][1][0], cA[0][0][1][1]})) : ); SB();
  asm volatile("ds_read2_b32 %0, %1 offset0:128 offset1:129" : "=&v"(cA[1][2][0]) : "v"(aa[1]) : "memory"); SB();
  asm volatile("s_add_u32 m0, %0, 98816" :  : "s"(mA) : "memory", "scc"); SB();
  asm volatile("v_mfma_f32_16x16x32_bf16 %0, %1, a[112:115], %0" : "+v"(ac[7]) : "v"(((u32x4){cA[0][3][0][0], cA[0][3][0][1], cA[0][3][1][0], cA[0][3][1][1]})) : ); SB();
  asm volatile("ds_read2_b32 %0, %1 offset0:130 offset1:131" : "=&v"(cA[1][2][1]) : "v"(aa[1]) : "memory"); SB();
  asm volatile("buffer_load_dwordx4 %0, %1, %2 offen offset:512 lds" :  : "v"(voS), "s"(rA), "s"(ao[1]) : "memory"); SB();
  asm volatile("v_mfma_f32_16x16x32_bf16 %0, %1, a[108:111], %0" : "+v"(ac[6]) : "v"(((u32x4){cA[0][3][0][0], cA[0][3][0][1], cA[0][3][1][0], cA[0][3][1][1]})) : ); SB();
  asm volatile("ds_read2_b32 %0, %1 offset0:0 offset1:1" : "=&v"(cA[1][3][0]) : "v"(aa[1]) : "memory"); SB();
  asm volatile("s_add_u32 m0, %0, 99840" :  : "s"(mA) : "memory", "scc"); SB();
  asm volatile("s_waitcnt lgkmcnt(15)\n\tv_mfma_f32_16x16x32_bf16 %0, %1, a[104:107], %0" : "+v"(ac[5]) : "v"(((u32x4){cA[0][3][0][0], cA[0][3][0][1], cA[0][3][1][0], cA[0][3][1][1]})) : ); SB();
  asm volatile("buffer_load_dwordx4 %0, %1, %2 offen offset:512 lds" :  : "v"(voS), "s"(rA), "s"(ao[2]) : "memory"); SB();
  asm volatile("s_add_u32 m0, %0, 100864" :  : "s"(mA) : "memory", "scc"); SB();
  asm volatile("v_mfma_f32_16x16x32_bf16 %0, %1, a[104:107], %0" : "+v"(ac[4]) : "v"(((u32x4){cA[0][2][0][0], cA[0][2][0][1], cA[0][2][1][0], cA[0][2][1][1]})) : ); SB();
  asm volatile("ds_read2_b32 %0, %1 offset0:2 offset1:3" : "=&v"(cA[1][3][1]) : "v"(aa[1]) : "memory"); SB();
  asm volatile("buffer_load_dwordx4 %0, %1, %2 offen offset:512 lds" :  : "v"(voS), "s"(rA), "s"(ao[3]) : "memory"); SB();
  asm volatile("v_mfma_f32_16x16x32_bf16 %0, %1, a[108:111], %0" : "+v"(ac[7]) : "v"(((u32x4){cA[0][4][0][0], cA[0][4][0][1], cA[0][4][1][0], cA[0][4][1][1]})) : ); SB();
  asm volatile("ds_read_b128 a[136:139], %0 offset:1024" :  : "v"(bb) : "memory"); SB();
  asm volatile("s_add_u32 m0, %0, 101888" :  : "s"(mA) : "memory", "scc"); SB();
  asm volatile("v_mfma_f32_16x16x32_bf16 %0, %1, a[104:107], %0" : "+v"(ac[6]) : "v"(((u32x4){cA[0][4][0][0], cA[0][4][0][1], cA[0][4][1][0], cA[0][4][1][1]})) : ); SB();
  asm volatile("ds_read2_b32 %0, %1 offset0:128 offset1:129" : "=&v"(cA[1][4][0]) : "v"(aa[2]) : "memory"); SB();
  asm volatile("buffer_load_dwordx4 %0, %1, %2 offen offset:512 lds" :  : "v"(voS), "s"(rA), "s"(ao[4]) : "memory"); SB();
  asm volatile("v_mfma_f32_16x16x32_bf16 %0, %1, a[104:107], %0" : "+v"(ac[3]) : "v"(((u32x4){cA[0][1][0][0], cA[0][1][0][1], cA[0][1][1][0], cA[0][1][1][1]})) : ); SB();
  asm volatile("ds_read2_b32 %0, %1 offset0:130 offset1:131" : "=&v"(cA[1][4][1]) : "v"(aa[2]) : "memory"); SB();
  asm volatile("s_add_u32 m0, %0, 102912" :  : "s"(mA) : "memory", "scc"); SB();
  asm volatile("v_mfma_f32_16x16x32_bf16 %0, %1, a[104:107], 0" : "=&v"(ac[2]) : "v"(((u32x4){cA[0][0][0][0], cA[0][0][0][1], cA[0][0][1][0], cA[0][0][1][1]})) : ); SB();
  asm volatile("buffer_load_dwordx4 %0, %1, %2 offen offset:512 lds" :  : "v"(voS), "s"(rA), "s"(ao[5]) : "memory"); SB();
  asm volatile("s_add_u32 m0, %0, 103936" :  : "s"(mA) : "memory", "scc"); SB();
  asm volatile("v_mfma_f32_16x16x32_bf16 %0, %1, a[104:107], %0" : "+v"(ac[7]) : "v"(((u32x4){cA[0][5][0][0], cA[0][5][0][1], cA[0][5][1][0], cA[0][5][1][1]})) : ); SB();
  asm volatile("ds_read2_b32 %0, %1 offset0:0 offset1:1" : "=&v"(cA[1][5][0]) : "v"(aa[2]) : "memory"); SB();
  asm volatile("buffer_load_dwordx4 %0, %1, %2 offen offset:512 lds" :  : "v"(voS), "s"(rA), "s"(ao[6]) : "memory"); SB();
  asm volatile("v_mfma_f32_16x16x32_bf16 %0, %1, a[100:103], %0" : "+v"(ac[6]) : "v"(((u32x4){cA[0][5][0][0], cA[0][5][0][1], cA[0][5][1][0], cA[0][5][1][1]})) : ); SB();
  asm volatile("ds_read2_b32 %0, %1 offset0:2 offset1:3" : "=&v"(cA[1][5][1]) : "v"(aa[2]) : "memory"); SB();
  asm volatile("s_add_u32 m0, %0, 104960" :  : "s"(mA) : "memory", "scc"); SB();
  asm volatile("v_mfma_f32_16x16x32_bf16 %0, %1, a[100:103], %0" : "+v"(ac[5]) : "v"(((u32x4){cA[0][4][0][0], cA[0][4][0][1], cA[0][4][1][0], cA[0][4][1][1]})) : ); SB();
  asm volatile("ds_read_b128 a[132:135], %0 offset:512" :  : "v"(bb) : "memory"); SB();
  asm volatile("buffer_load_dwordx4 %0, %1, %2 offen offset:512 lds" :  : "v"(voS), "s"(rA), "s"(ao[7]) : "memory"); SB();
  asm volatile("v_mfma_f32_16x16x32_bf16 %0, %1, a[100:103], %0" : "+v"(ac[4]) : "v"(((u32x4){cA[0][3][0][0], cA[0][3][0][1], cA[0][3][1][0], cA[0][3][1][1]})) : ); SB();
  asm volatile("ds_read2_b32 %0, %1 offset0:128 offset1:129" : "=&v"(cA[1][6][0]) : "v"(aa[3]) : "memory"); SB();
  asm volatile("s_add_u32 m0, %0, 105856" :  : "s"(mA) : "memory", "scc"); SB();
  asm volatile("v_mfma_f32_16x16x32_bf16 %0, %1, a[100:103], %0" : "+v"(ac[7]) : "v"(((u32x4){cA[0][6][0][0], cA[0][6][0][1], cA[0][6][1][0], cA[0][6][1][1]})) : ); SB();
  asm volatile("buffer_load_dwordx4 %0, %1, %2 offen offset:640 lds" :  : "v"(voS), "s"(rA), "s"(ao[0]) : "memory"); SB();
  asm volatile("s_add_u32 m0, %0, 106880" :  : "s"(mA) : "memory", "scc"); SB();
  asm volatile("v_mfma_f32_16x16x32_bf16 %0, %1, a[100:103], %0" : "+v"(ac[3]) : "v"(((u32x4){cA[0][2][0][0], cA[0][2][0][1], cA[0][2][1][0], cA[0][2][1][1]})) : ); SB();
  asm volatile("ds_read2_b32 %0, %1 offset0:130 offset1:131" : "=&v"(cA[1][6][1]) : "v"(aa[3]) : "memory"); SB();
  asm volatile("buffer_load_dwordx4 %0, %1, %2 offen offset:640 lds" :  : "v"(voS), "s"(rA), "s"(ao[1]) : "memory"); SB();
  asm volatile("v_mfma_f32_16x16x32_bf16 %0, %1, a[100:103], %0" : "+v"(ac[2]) : "v"(((u32x4){cA[0][1][0][0], cA[0][1][0][1], cA[0][1][1][0], cA[0][1][1][1]})) : ); SB();
  asm volatile("ds_read2_b32 %0, %1 offset0:0 offset1:1" : "=&v"(cA[1][7][0]) : "v"(aa[3]) : "memory"); SB();
  asm volatile("s_waitcnt lgkmcnt(15)" :  :  : "memory"); SB();
  asm volatile("v_mfma_f32_16x16x32_bf16 %0, %1, a[100:103], 0" : "=&v"(ac[1]) : "v"(((u32x4){cA[0][0][0][0], cA[0][0][0][1], cA[0][0][1][0], cA[0][0][1][1]})) : ); SB();
  asm volatile("ds_read2_b32 %0, %1 offset0:2 offset1:3" : "=&v"(cA[1][7][1]) : "v"(aa[3]) : "memory"); SB();
  asm volatile("s_add_u32 m0, %0, 107904" :  : "s"(mA) : "memory", "scc"); SB();
  asm volatile("v_mfma_f32_16x16x32_bf16 %0, %1, a[96:99], %0" : "+v"(ac[7]) : "v"(((u32x4){cA[0][7][0][0], cA[0][7][0][1], cA[0][7][1][0], cA[0][7][1][1]})) : ); SB();
  asm volatile("ds_read_b128 a[128:131], %0 offset:0" :  : "v"(bb) : "memory"); SB();
  asm volatile("v_mfma_f32_16x16x32_bf16 %0, %1, a[96:99], %0" : "+v"(ac[6]) : "v"(((u32x4){cA[0][6][0][0], cA[0][6][0][1], cA[0][6][1][0], cA[0][6][1][1]})) : ); SB();
  asm volatile("buffer_load_dwordx4 %0, %1, %2 offen offset:640 lds" :  : "v"(voS), "s"(rA), "s"(ao[2]) : "memory"); SB();
  asm volatile("s_add_u32 m0, %0, 108928" :  : "s"(mA) : "memory", "scc"); SB();
  asm volatile("v_mfma_f32_16x16x32_bf16 %0, %1, a[96:99], %0" : "+v"(ac[5]) : "v"(((u32x4){cA[0][5][0][0], cA[0][5][0][1], cA[0][5][1][0], cA[0][5][1][1]})) : ); SB();
  ++cn; dl = (cn & 7) ? -64 : 13376; asm volatile("" : "+s"(dl)); SB();
  asm volatile("buffer_load_dwordx4 %0, %1, %2 offen offset:640 lds" :  : "v"(voS), "s"(rA), "s"(ao[3]) : "memory"); SB();
  asm volatile("s_waitcnt lgkmcnt(15)" :  :  : "memory"); SB();
  asm volatile("v_mfma_f32_16x16x32_bf16 %0, %1, a[96:99], %0" : "+v"(ac[4]) : "v"(((u32x4){cA[0][4][0][0], cA[0][4][0][1], cA[0][4][1][0], cA[0][4][1][1]})) : ); SB();
  asm volatile("s_add_u32 m0, %0, 109952" :  : "s"(mA) : "memory", "scc"); SB();
  asm volatile("v_add_u32 %0, %1, %0" : "+v"(aa[0]) : "s"(dl) : ); SB();
  asm volatile("v_mfma_f32_16x16x32_bf16 %0, %1, a[96:99], %0" : "+v"(ac[3]) : "v"(((u32x4){cA[0][3][0][0], cA[0][3][0][1], cA[0][3][1][0], cA[0][3][1][1]})) : ); SB();
  asm volatile("buffer_load_dwordx4 %0, %1, %2 offen offset:640 lds" :  : "v"(voS), "s"(rA), "s"(ao[4]) : "memory"); SB();
  asm volatile("v_add_u32 %0, %1, %0" : "+v"(aa[1]) : "s"(dl) : ); SB();
  asm volatile("v_mfma_f32_16x16x32_bf16 %0, %1, a[96:99], %0" : "+v"(ac[2]) : "v"(((u32x4){cA[0][2][0][0], cA[0][2][0][1], cA[0][2][1][0], cA[0][2][1][1]})) : ); SB();
  asm volatile("v_add_u32 %0, %1, %0" : "+v"(aa[2]) : "s"(dl) : ); SB();
  asm volatile("v_mfma_f32_16x16x32_bf16 %0, %1, a[96:99], %0" : "+v"(ac[1]) : "v"(((u32x4){cA[0][1][0][0], cA[0][1][0][1], cA[0][1][1][0], cA[0][1][1][1]})) : ); SB();
  asm volatile("v_add_u32 %0, %1, %0" : "+v"(aa[3]) : "s"(dl) : ); SB();
  asm volatile("v_mfma_f32_16x16x32_bf16 %0, %1, a[96:99], 0" : "=&v"(ac[0]) : "v"(((u32x4){cA[0][0][0][0], cA[0][0][0][1], cA[0][0][1][0], cA[0][0][1][1]})) : ); SB();
  asm volatile("v_add_u32 %0, %1, %0" : "+v"(bb) : "s"(dl) : ); SB();
  asm volatile("v_mfma_f32_16x16x32_bf16 %0, %1, a[156:159], %0" : "+v"(ac[7]) : "v"(((u32x4){cA[1][0][0][0], cA[1][0][0][1], cA[1][0][1][0], cA[1][0][1][1]})) : ); SB();
  asm volatile("ds_read2_b32 %0, %1 offset0:128 offset1:129" : "=&v"(cA[0][0][0]) : "v"(aa[0]) : "memory"); SB();
  asm volatile("v_mfma_f32_16x16x32_bf16 %0, %1, a[152:155], %0" : "+v"(ac[6]) : "v"(((u32x4){cA[1][0][0][0], cA[1][0][0][1], cA[1][0][1][0], cA[1][0][1][1]})) : ); SB();
  asm volatile("ds_read2_b32 %0, %1 offset0:130 offset1:131" : "=&v"(cA[0][0][1]) : "v"(aa[0]) : "memory"); SB();
  asm volatile("v_mfma_f32_16x16x32_bf16 %0, %1, a[148:151], %0" : "+v"(ac[5]) : "v"(((u32x4){cA[1][0][0][0], cA[1][0][0][1], cA[1][0][1][0], cA[1][0][1][1]})) : ); SB();
  asm volatile("ds_read_b128 a[124:127], %0 offset:3584" :  : "v"(bb) : "memory"); SB();
  asm volatile("v_mfma_f32_16x16x32_bf16 %0, %1, a[144:147], %0" : "+v"(ac[4]) : "v"(((u32x4){cA[1][0][0][0], cA[1][0][0][1], cA[1][0][1][0], cA[1][0][1][1]})) : ); SB();
  asm volatile("ds_read_b128 a[120:123], %0 offset:3072" :  : "v"(bb) : "memory"); SB();
  asm volatile("v_mfma_f32_16x16x32_bf16 %0, %1, a[152:155], %0" : "+v"(ac[7]) : "v"(((u32x4){cA[1][1][0][0], cA[1][1][0][1], cA[1][1][1][0], cA[1][1][1][1]})) : ); SB();
  asm volatile("v_mfma_f32_16x16x32_bf16 %0, %1, a[148:151], %0" : "+v"(ac[6]) : "v"(((u32x4){cA[1][1][0][0], cA[1][1][0][1], cA[1][1][1][0], cA[1][1][1][1]})) : ); SB();
  asm volatile("ds_read_b128 a[116:119], %0 offset:2560" :  : "v"(bb) : "memory"); SB();
  asm volatile("v_mfma_f32_16x16x32_bf16 %0, %1, a[144:147], %0" : "+v"(ac[5]) : "v"(((u32x4){cA[1][1][0][0], cA[1][1][0][1], cA[1][1][1][0], cA[1][1][1][1]})) : ); SB();
  asm volatile("ds_read_b128 a[112:115], %0 offset:2048" :  : "v"(bb) : "memory"); SB();
  asm volatile("v_mfma_f32_16x16x32_bf16 %0, %1, a[140:143], %0" : "+v"(ac[4]) : "v"(((u32x4){cA[1][1][0][0], cA[1][1][0][1], cA[1][1][1][0], cA[1][1][1][1]})) : ); SB();
  asm volatile("ds_read2_b32 %0, %1 offset0:0 offset1:1" : "=&v"(cA[0][1][0]) : "v"(aa[0]) : "memory"); SB();
  asm volatile("s_waitcnt lgkmcnt(15)\n\tv_mfma_f32_16x16x32_bf16 %0, %1, a[148:151], %0" : "+v"(ac[7]) : "v"(((u32x4){cA[1][2][0][0], cA[1][2][0][1], cA[1][2][1][0], cA[1][2][1][1]})) : ); SB();
  asm volatile("ds_read2_b32 %0, %1 offset0:2 offset1:3" : "=&v"(cA[0][1][1]) : "v"(aa[0]) : "memory"); SB();
  asm volatile("v_mfma_f32_16x16x32_bf16 %0, %1, a[144:147], %0" : "+v"(ac[6]) : "v"(((u32x4){cA[1][2][0][0], cA[1][2][0][1], cA[1][2][1][0], cA[1][2][1][1]})) : ); SB();
  asm volatile("v_mfma_f32_16x16x32_bf16 %0, %1, a[140:143], %0" : "+v"(ac[5]) : "v"(((u32x4){cA[1][2][0][0], cA[1][2][0][1], cA[1][2][1][0], cA[1][2][1][1]})) : ); SB();
  asm volatile("ds_read_b128 a[108:111], %0 offset:1536" :  : "v"(bb) : "memory"); SB();
  asm volatile("v_mfma_f32_16x16x32_bf16 %0, %1, a[140:143], %0" : "+v"(ac[3]) : "v"(((u32x4){cA[1][0][0][0], cA[1][0][0][1], cA[1][0][1][0], cA[1][0][1][1]})) : ); SB();
  asm volatile("ds_read2_b32 %0, %1 offset0:128 offset1:129" : "=&v"(cA[0][2][0]) : "v"(aa[1]) : "memory"); SB();
  asm volatile("v_mfma_f32_16x16x32_bf16 %0, %1, a[144:147], %0" : "+v"(ac[7]) : "v"(((u32x4){cA[1][3][0][0], cA[1][3][0][1], cA[1][3][1][0], cA[1][3][1][1]})) : ); SB();
  asm volatile("ds_read2_b32 %0, %1 offset0:130 offset1:131" : "=&v"(cA[0][2][1]) : "v"(aa[1]) : "memory"); SB();
  asm volatile("v_mfma_f32_16x16x32_bf16 %0, %1, a[140:143], %0" : "+v"(ac[6]) : "v"(((u32x4){cA[1][3][0][0], cA[1][3][0][1], cA[1][3][1][0], cA[1][3][1][1]})) : ); SB();
  asm volatile("ds_read2_b32 %0, %1 offset0:0 offset1:1" : "=&v"(cA[0][3][0]) : "v"(aa[1]) : "memory"); SB();
  asm volatile("v_mfma_f32_16x16x32_bf16 %0, %1, a[136:139], %0" : "+v"(ac[5]) : "v"(((u32x4){cA[1][3][0][0], cA[1][3][0][1], cA[1][3][1][0], cA[1][3][1][1]})) : ); SB();
  asm volatile("v_mfma_f32_16x16x32_bf16 %0, %1, a[136:139], %0" : "+v"(ac[4]) : "v"(((u32x4){cA[1][2][0][0], cA[1][2][0][1], cA[1][2][1][0], cA[1][2][1][1]})) : ); SB();
  asm volatile("ds_read2_b32 %0, %1 offset0:2 offset1:3" : "=&v"(cA[0][3][1]) : "v"(aa[1]) : "memory"); SB();
  asm volatile("v_mfma_f32_16x16x32_bf16 %0, %1, a[140:143], %0" : "+v"(ac[7]) : "v"(((u32x4){cA[1][4][0][0], cA[1][4][0][1], cA[1][4][1][0], cA[1][4][1][1]})) : ); SB();
  asm volatile("ds_read_b128 a[104:107], %0 offset:1024" :  : "v"(bb) : "memory"); SB();
  asm volatile("v_mfma_f32_16x16x32_bf16 %0, %1, a[136:139], %0" : "+v"(ac[6]) : "v"(((u32x4){cA[1][4][0][0], cA[1][4][0][1], cA[1][4][1][0], cA[1][4][1][1]})) : ); SB();
  asm volatile("ds_read2_b32 %0, %1 offset0:128 offset1:129" : "=&v"(cA[0][4][0]) : "v"(aa[2]) : "memory"); SB();
  asm volatile("v_mfma_f32_16x16x32_bf16 %0, %1, a[136:139], %0" : "+v"(ac[3]) : "v"(((u32x4){cA[1][1][0][0], cA[1][1][0][1], cA[1][1][1][0], cA[1][1][1][1]})) : ); SB();
  asm volatile("ds_read2_b32 %0, %1 offset0:130 offset1:131" : "=&v"(cA[0][4][1]) : "v"(aa[2]) : "memory"); SB();
  asm volatile("v_mfma_f32_16x16x32_bf16 %0, %1, a[136:139], %0" : "+v"(ac[2]) : "v"(((u32x4){cA[1][0][0][0], cA[1][0][0][1], cA[1][0][1][0], cA[1][0][1][1]})) : ); SB();
  asm volatile("s_waitcnt lgkmcnt(15)\n\tv_mfma_f32_16x16x32_bf16 %0, %1, a[136:139], %0" : "+v"(ac[7]) : "v"(((u32x4){cA[1][5][0][0], cA[1][5][0][1], cA[1][5][1][0], cA[1][5][1][1]})) : ); SB();
  asm volatile("ds_read2_b32 %0, %1 offset0:0 offset1:1" : "=&v"(cA[0][5][0]) : "v"(aa[2]) : "memory"); SB();
  asm volatile("v_mfma_f32_16x16x32_bf16 %0, %1, a[132:135], %0" : "+v"(ac[6]) : "v"(((u32x4){cA[1][5][0][0], cA[1][5][0][1], cA[1][5][1][0], cA[1][5][1][1]})) : ); SB();
  asm volatile("ds_read2_b32 %0, %1 offset0:2 offset1:3" : "=&v"(cA[0][5][1]) : "v"(aa[2]) : "memory"); SB();
  asm volatile("v_mfma_f32_16x16x32_bf16 %0, %1, a[132:135], %0" : "+v"(ac[5]) : "v"(((u32x4){cA[1][4][0][0], cA[1][4][0][1], cA[1][4][1][0], cA[1][4][1][1]})) : ); SB();
  asm volatile("ds_read_b128 a[100:103], %0 offset:512" :  : "v"(bb) : "memory"); SB();
  asm volatile("v_mfma_f32_16x16x32_bf16 %0, %1, a[132:135], %0" : "+v"(ac[4]) : "v"(((u32x4){cA[1][3][0][0], cA[1][3][0][1], cA[1][3][1][0], cA[1][3][1][1]})) : ); SB();
  asm volatile("ds_read2_b32 %0, %1 offset0:128 offset1:129" : "=&v"(cA[0][6][0]) : "v"(aa[3]) : "memory"); SB();
  asm volatile("v_mfma_f32_16x16x32_bf16 %0, %1, a[132:135], %0" : "+v"(ac[7]) : "v"(((u32x4){cA[1][6][0][0], cA[1][6][0][1], cA[1][6][1][0], cA[1][6][1][1]})) : ); SB();
  asm volatile("v_mfma_f32_16x16x32_bf16 %0, %1, a[132:135], %0" : "+v"(ac[3]) : "v"(((u32x4){cA[1][2][0][0], cA[1][2][0][1], cA[1][2][1][0], cA[1][2][1][1]})) : ); SB();
  asm volatile("ds_read2_b32 %0, %1 offset0:130 offset1:131" : "=&v"(cA[0][6][1]) : "v"(aa[3]) : "memory"); SB();
  asm volatile("v_mfma_f32_16x16x32_bf16 %0, %1, a[132:135], %0" : "+v"(ac[2]) : "v"(((u32x4){cA[1][1][0][0], cA[1][1][0][1], cA[1][1][1][0], cA[1][1][1][1]})) : ); SB();
  asm volatile("ds_read2_b32 %0, %1 offset0:0 offset1:1" : "=&v"(cA[0][7][0]) : "v"(aa[3]) : "memory"); SB();
  asm volatile("v_mfma_f32_16x16x32_bf16 %0, %1, a[132:135], %0" : "+v"(ac[1]) : "v"(((u32x4){cA[1][0][0][0], cA[1][0][0][1], cA[1][0][1][0], cA[1][0][1][1]})) : ); SB();
  asm volatile("ds_read2_b32 %0, %1 offset0:2 offset1:3" : "=&v"(cA[0][7][1]) : "v"(aa[3]) : "memory"); SB();
  asm volatile("v_mfma_f32_16x16x32_bf16 %0, %1, a[128:131], %0" : "+v"(ac[7]) : "v"(((u32x4){cA[1][7][0][0], cA[1][7][0][1], cA[1][7][1][0], cA[1][7][1][1]})) : ); SB();
  asm volatile("ds_read_b128 a[96:99], %0 offset:0" :  : "v"(bb) : "memory"); SB();
  asm volatile("v_mfma_f32_16x16x32_bf16 %0, %1, a[128:131], %0" : "+v"(ac[6]) : "v"(((u32x4){cA[1][6][0][0], cA[1][6][0][1], cA[1][6][1][0], cA[1][6][1][1]})) : ); SB();
  asm volatile("buffer_load_dwordx2 %0, %1, %2, 0 offen sc1" : "=&v"(fz) : "v"(vFZ), "s"(rWS) : "memory"); SB();
  asm volatile("v_mfma_f32_16x16x32_bf16 %0, %1, a[128:131], %0" : "+v"(ac[5]) : "v"(((u32x4){cA[1][5][0][0], cA[1][5][0][1], cA[1][5][1][0], cA[1][5][1][1]})) : ); SB();
  ++cn; dl = (cn & 7) ? -64 : 13376; asm volatile("" : "+s"(dl)); SB();
  asm volatile("s_waitcnt vmcnt(14)" :  :  : "memory"); SB();
  asm volatile("ds_read_b128 a[12:15], %0 offset:14336" :  : "v"(aA[0]) : "memory"); SB();
  asm volatile("v_mfma_f32_16x16x32_bf16 %0, %1, a[128:131], %0" : "+v"(ac[4]) : "v"(((u32x4){cA[1][4][0][0], cA[1][4][0][1], cA[1][4][1][0], cA[1][4][1][1]})) : ); SB();
  asm volatile("ds_read_b128 a[28:31], %0 offset:14336" :  : "v"(aA[1]) : "memory"); SB();
  asm volatile("s_add_u32 m0, %0, 110976" :  : "s"(mA) : "memory", "scc"); SB();
  asm volatile("v_mfma_f32_16x16x32_bf16 %0, %1, a[128:131], %0" : "+v"(ac[3]) : "v"(((u32x4){cA[1][3][0][0], cA[1][3][0][1], cA[1][3][1][0], cA[1][3][1][1]})) : ); SB();
  asm volatile("buffer_load_dwordx4 %0, %1, %2 offen offset:640 lds" :  : "v"(voS), "s"(rA), "s"(ao[5]) : "memory"); SB();
  asm volatile("v_add_u32 %0, %1, %0" : "+v"(aa[0]) : "s"(dl) : ); SB();
  asm volatile("v_mfma_f32_16x16x32_bf16 %0, %1, a[128:131], %0" : "+v"(ac[2]) : "v"(((u32x4){cA[1][2][0][0], cA[1][2][0][1], cA[1][2][1][0], cA[1][2][1][1]})) : ); SB();
  asm volatile("v_add_u32 %0, %1, %0" : "+v"(aa[1]) : "s"(dl) : ); SB();
  asm volatile("v_mfma_f32_16x16x32_bf16 %0, %1, a[128:131], %0" : "+v"(ac[1]) : "v"(((u32x4){cA[1][1][0][0], cA[1][1][0][1], cA[1][1][1][0], cA[1][1][1][1]})) : ); SB();
  asm volatile("v_add_u32 %0, %1, %0" : "+v"(aa[2]) : "s"(dl) : ); SB();
  asm volatile("v_mfma_f32_16x16x32_bf16 %0, %1, a[128:131], %0" : "+v"(ac[0]) : "v"(((u32x4){cA[1][0][0][0], cA[1][0][0][1], cA[1][0][1][0], cA[1][0][1][1]})) : ); SB();
  asm volatile("v_add_u32 %0, %1, %0" : "+v"(aa[3]) : "s"(dl) : ); SB();
  asm volatile("v_add_u32 %0, %1, %0" : "+v"(bb) : "s"(dl) : ); SB();
  cu += 2;
  if ((cu & 7) == 0 && nch == 3) {
  asm volatile("s_nop 7" : "+v"(ac[0]), "+v"(ac[1]), "+v"(ac[2]), "+v"(ac[3]), "+v"(ac[4]), "+v"(ac[5]), "+v"(ac[6]), "+v"(ac[7]) :  : ); SB();
    const unsigned dd = da + (((cu - 1) >> 3) * 2 + 1) * 8192;
  asm volatile("ds_write_b128 %0, %1 offset:0" :  : "v"(dd), "v"(ac[0]) : "memory"); SB();
  asm volatile("ds_write_b128 %0, %1 offset:1024" :  : "v"(dd), "v"(ac[1]) : "memory"); SB();
  asm volatile("ds_write_b128 %0, %1 offset:2048" :  : "v"(dd), "v"(ac[2]) : "memory"); SB();
  asm volatile("ds_write_b128 %0, %1 offset:3072" :  : "v"(dd), "v"(ac[3]) : "memory"); SB();
  asm volatile("ds_write_b128 %0, %1 offset:4096" :  : "v"(dd), "v"(ac[4]) : "memory"); SB();
  asm volatile("ds_write_b128 %0, %1 offset:5120" :  : "v"(dd), "v"(ac[5]) : "memory"); SB();
  asm volatile("ds_write_b128 %0, %1 offset:6144" :  : "v"(dd), "v"(ac[6]) : "memory"); SB();
  asm volatile("ds_write_b128 %0, %1 offset:7168" :  : "v"(dd), "v"(ac[7]) : "memory"); SB();
#pragma unroll
  for (int t = 0; t < 8; ++t) ac[t] = (f32x4){0.f, 0.f, 0.f, 0.f};
  asm volatile("s_nop 1" : "+v"(ac[0]), "+v"(ac[1]), "+v"(ac[2]), "+v"(ac[3]), "+v"(ac[4]), "+v"(ac[5]), "+v"(ac[6]), "+v"(ac[7]) :  : ); SB();
  }
  asm volatile("s_waitcnt lgkmcnt(15)\n\tv_mfma_f32_16x16x32_bf16 %0, %1, a[124:127], %0" : "+v"(ac[7]) : "v"(((u32x4){cA[0][0][0][0], cA[0][0][0][1], cA[0][0][1][0], cA[0][0][1][1]})) : ); SB();
  asm volatile("ds_read2_b32 %0, %1 offset0:128 offset1:129" : "=&v"(cA[1][0][0]) : "v"(aa[0]) : "memory"); SB();
  asm volatile("s_waitcnt vmcnt(13)" :  :  : "memory"); SB();
  asm volatile("v_mfma_f32_16x16x32_bf16 %0, %1, a[120:123], %0" : "+v"(ac[6]) : "v"(((u32x4){cA[0][0][0][0], cA[0][0][0][1], cA[0][0][1][0], cA[0][0][1][1]})) : ); SB();
  asm volatile("ds_read2_b32 %0, %1 offset0:130 offset1:131" : "=&v"(cA[1][0][1]) : "v"(aa[0]) : "memory"); SB();
  asm volatile("v_mfma_f32_16x16x32_bf16 %0, %1, a[116:119], %0" : "+v"(ac[5]) : "v"(((u32x4){cA[0][0][0][0], cA[0][0][0][1], cA[0][0][1][0], cA[0][0][1][1]})) : ); SB();
  asm volatile("ds_read_b128 a[156:159], %0 offset:3584" :  : "v"(bb) : "memory"); SB();
  asm volatile("ds_read_b128 a[32:35], %0 offset:0" :  : "v"(aA[0]) : "memory"); SB();
  asm volatile("v_mfma_f32_16x16x32_bf16 %0, %1, a[112:115], %0" : "+v"(ac[4]) : "v"(((u32x4){cA[0][0][0][0], cA[0][0][0][1], cA[0][0][1][0], cA[0][0][1][1]})) : ); SB();
  asm volatile("ds_read_b128 a[152:155], %0 offset:3072" :  : "v"(bb) : "memory"); SB();
  asm volatile("ds_read_b128 a[48:51], %0 offset:0" :  : "v"(aA[1]) : "memory"); SB();
  asm volatile("v_mfma_f32_16x16x32_bf16 %0, %1, a[120:123], %0" : "+v"(ac[7]) : "v"(((u32x4){cA[0][1][0][0], cA[0][1][0][1], cA[0][1][1][0], cA[0][1][1][1]})) : ); SB();
  asm volatile("s_waitcnt vmcnt(11)" :  :  : "memory"); SB();
  asm volatile("ds_read_b128 a[36:39], %0 offset:2048" :  : "v"(aA[0]) : "memory"); SB();
  asm volatile("v_mfma_f32_16x16x32_bf16 %0, %1, a[116:119], %0" : "+v"(ac[6]) : "v"(((u32x4){cA[0][1][0][0], cA[0][1][0][1], cA[0][1][1][0], cA[0][1][1][1]})) : ); SB();
  asm volatile("ds_read_b128 a[148:151], %0 offset:2560" :  : "v"(bb) : "memory"); SB();
  asm volatile("ds_read_b128 a[52:55], %0 offset:2048" :  : "v"(aA[1]) : "memory"); SB();
  asm volatile("v_mfma_f32_16x16x32_bf16 %0, %1, a[112:115], %0" : "+v"(ac[5]) : "v"(((u32x4){cA[0][1][0][0], cA[0][1][0][1], cA[0][1][1][0], cA[0][1][1][1]})) : ); SB();
  asm volatile("ds_read_b128 a[144:147], %0 offset:2048" :  : "v"(bb) : "memory"); SB();
  asm volatile("s_waitcnt lgkmcnt(10)" :  :  : "memory"); SB();
  asm volatile("s_add_u32 m0, %0, 112000" :  : "s"(mA) : "memory", "scc"); SB();
  asm volatile("v_mfma_f32_16x16x32_bf16 %0, %1, a[108:111], %0" : "+v"(ac[4]) : "v"(((u32x4){cA[0][1][0][0], cA[0][1][0][1], cA[0][1][1][0], cA[0][1][1][1]})) : ); SB();
  asm volatile("ds_read2_b32 %0, %1 offset0:0 offset1:1" : "=&v"(cA[1][1][0]) : "v"(aa[0]) : "memory"); SB();
  asm volatile("buffer_load_dwordx4 %0, %1, %2 offen offset:640 lds" :  : "v"(voS), "s"(rA), "s"(ao[6]) : "memory"); SB();
  asm volatile("v_mfma_f32_16x16x32_bf16 %0, %1, a[116:119], %0" : "+v"(ac[7]) : "v"(((u32x4){cA[0][2][0][0], cA[0][2][0][1], cA[0][2][1][0], cA[0][2][1][1]})) : ); SB();
  asm volatile("ds_read2_b32 %0, %1 offset0:2 offset1:3" : "=&v"(cA[1][1][1]) : "v"(aa[0]) : "memory"); SB();
  asm volatile("s_add_u32 m0, %0, 113024" :  : "s"(mA) : "memory", "scc"); SB();
  asm volatile("v_mfma_f32_16x16x32_bf16 %0, %1, a[112:115], %0" : "+v"(ac[6]) : "v"(((u32x4){cA[0][2][0][0], cA[0][2][0][1], cA[0][2][1][0], cA[0][2][1][1]})) : ); SB();
  asm volatile("buffer_load_dwordx4 %0, %1, %2 offen offset:640 lds" :  : "v"(voS), "s"(rA), "s"(ao[7]) : "memory"); SB();
  asm volatile("v_mfma_f32_16x16x32_bf16 %0, %1, a[108:111], %0" : "+v"(ac[5]) : "v"(((u32x4){cA[0][2][0][0], cA[0][2][0][1], cA[0][2][1][0], cA[0][2][1][1]})) : ); SB();
  asm volatile("ds_read_b128 a[140:143], %0 offset:1536" :  : "v"(bb) : "memory"); SB();
  asm volatile("v_mfma_f32_16x16x32_bf16 %0, %1, a[108:111], %0" : "+v"(ac[3]) : "v"(((u32x4){cA[0][0][0][0], cA[0][0][0][1], cA[0][0][1][0], cA[0][0][1][1]})) : ); SB();
  asm volatile("ds_read2_b32 %0, %1 offset0:128 offset1:129" : "=&v"(cA[1][2][0]) : "v"(aa[1]) : "memory"); SB();
  asm volatile("s_waitcnt vmcnt(11)" :  :  : "memory"); SB();
  asm volatile("v_mfma_f32_16x16x32_bf16 %0, %1, a[112:115], %0" : "+v"(ac[7]) : "v"(((u32x4){cA[0][3][0][0], cA[0][3][0][1], cA[0][3][1][0], cA[0][3][1][1]})) : ); SB();
  asm volatile("ds_read2_b32 %0, %1 offset0:130 offset1:131" : "=&v"(cA[1][2][1]) : "v"(aa[1]) : "memory"); SB();
  asm volatile("ds_read_b128 a[40:43], %0 offset:4096" :  : "v"(aA[0]) : "memory"); SB();
  asm volatile("v_mfma_f32_16x16x32_bf16 %0, %1, a[108:111], %0" : "+v"(ac[6]) : "v"(((u32x4){cA[0][3][0][0], cA[0][3][0][1], cA[0][3][1][0], cA[0][3][1][1]})) : ); SB();
  asm volatile("ds_read2_b32 %0, %1 offset0:0 offset1:1" : "=&v"(cA[1][3][0]) : "v"(aa[1]) : "memory"); SB();
  asm volatile("ds_read_b128 a[56:59], %0 offset:4096" :  : "v"(aA[1]) : "memory"); SB();
  asm volatile("v_mfma_f32_16x16x32_bf16 %0, %1, a[104:107], %0" : "+v"(ac[5]) : "v"(((u32x4){cA[0][3][0][0], cA[0][3][0][1], cA[0][3][1][0], cA[0][3][1][1]})) : ); SB();
  asm volatile("s_waitcnt lgkmcnt(12)" :  :  : "memory"); SB();
  asm volatile("s_add_u32 m0, %0, 97536" :  : "s"(mA) : "memory", "scc"); SB();
  asm volatile("v_mfma_f32_16x16x32_bf16 %0, %1, a[104:107], %0" : "+v"(ac[4]) : "v"(((u32x4){cA[0][2][0][0], cA[0][2][0][1], cA[0][2][1][0], cA[0][2][1][1]})) : ); SB();
  asm volatile("ds_read2_b32 %0, %1 offset0:2 offset1:3" : "=&v"(cA[1][3][1]) : "v"(aa[1]) : "memory"); SB();
  asm volatile("buffer_load_dwordx4 %0, %1, %2 offen offset:768 lds" :  : "v"(voS), "s"(rA), "s"(ao[0]) : "memory"); SB();
  asm volatile("v_mfma_f32_16x16x32_bf16 %0, %1, a[108:111], %0" : "+v"(ac[7]) : "v"(((u32x4){cA[0][4][0][0], cA[0][4][0][1], cA[0][4][1][0], cA[0][4][1][1]})) : ); SB();
  asm volatile("ds_read_b128 a[136:139], %0 offset:1024" :  : "v"(bb) : "memory"); SB();
  asm volatile("s_add_u32 m0, %0, 98560" :  : "s"(mA) : "memory", "scc"); SB();
  asm volatile("v_mfma_f32_16x16x32_bf16 %0, %1, a[104:107], %0" : "+v"(ac[6]) : "v"(((u32x4){cA[0][4][0][0], cA[0][4][0][1], cA[0][4][1][0], cA[0][4][1][1]})) : ); SB();
  asm volatile("ds_read2_b32 %0, %1 offset0:128 offset1:129" : "=&v"(cA[1][4][0]) : "v"(aa[2]) : "memory"); SB();
  asm volatile("buffer_load_dwordx4 %0, %1, %2 offen offset:768 lds" :  : "v"(voS), "s"(rA), "s"(ao[1]) : "memory"); SB();
  asm volatile("v_mfma_f32_16x16x32_bf16 %0, %1, a[104:107], %0" : "+v"(ac[3]) : "v"(((u32x4){cA[0][1][0][0], cA[0][1][0][1], cA[0][1][1][0], cA[0][1][1][1]})) : ); SB();
  asm volatile("ds_read2_b32 %0, %1 offset0:130 offset1:131" : "=&v"(cA[1][4][1]) : "v"(aa[2]) : "memory"); SB();
  asm volatile("s_waitcnt lgkmcnt(13)" :  :  : "memory"); SB();
  asm volatile("v_mfma_f32_16x16x32_bf16 %0, %1, a[104:107], %0" : "+v"(ac[2]) : "v"(((u32x4){cA[0][0][0][0], cA[0][0][0][1], cA[0][0][1][0], cA[0][0][1][1]})) : ); SB();
  asm volatile("s_add_u32 m0, %0, 99584" :  : "s"(mA) : "memory", "scc"); SB();
  asm volatile("v_mfma_f32_16x16x32_bf16 %0, %1, a[104:107], %0" : "+v"(ac[7]) : "v"(((u32x4){cA[0][5][0][0], cA[0][5][0][1], cA[0][5][1][0], cA[0][5][1][1]})) : ); SB();
  asm volatile("ds_read2_b32 %0, %1 offset0:0 offset1:1" : "=&v"(cA[1][5][0]) : "v"(aa[2]) : "memory"); SB();
  asm volatile("buffer_load_dwordx4 %0, %1, %2 offen offset:768 lds" :  : "v"(voS), "s"(rA), "s"(ao[2]) : "memory"); SB();
  asm volatile("v_mfma_f32_16x16x32_bf16 %0, %1, a[100:103], %0" : "+v"(ac[6]) : "v"(((u32x4){cA[0][5][0][0], cA[0][5][0][1], cA[0][5][1][0], cA[0][5][1][1]})) : ); SB();
  asm volatile("ds_read2_b32 %0, %1 offset0:2 offset1:3" : "=&v"(cA[1][5][1]) : "v"(aa[2]) : "memory"); SB();
  asm volatile("s_waitcnt vmcnt(12)" :  :  : "memory"); SB();
  asm volatile("v_mfma_f32_16x16x32_bf16 %0, %1, a[100:103], %0" : "+v"(ac[5]) : "v"(((u32x4){cA[0][4][0][0], cA[0][4][0][1], cA[0][4][1][0], cA[0][4][1][1]})) : ); SB();
  asm volatile("ds_read_b128 a[132:135], %0 offset:512" :  : "v"(bb) : "memory"); SB();
  asm volatile("v_mfma_f32_16x16x32_bf16 %0, %1, a[100:103], %0" : "+v"(ac[4]) : "v"(((u32x4){cA[0][3][0][0], cA[0][3][0][1], cA[0][3][1][0], cA[0][3][1][1]})) : ); SB();
  asm volatile("ds_read2_b32 %0, %1 offset0:128 offset1:129" : "=&v"(cA[1][6][0]) : "v"(aa[3]) : "memory"); SB();
  asm volatile("v_mfma_f32_16x16x32_bf16 %0, %1, a[100:103], %0" : "+v"(ac[7]) : "v"(((u32x4){cA[0][6][0][0], cA[0][6][0][1], cA[0][6][1][0], cA[0][6][1][1]})) : ); SB();
  asm volatile("ds_read_b128 a[44:47], %0 offset:6144" :  : "v"(aA[0]) : "memory"); SB();
  asm volatile("v_mfma_f32_16x16x32_bf16 %0, %1, a[100:103], %0" : "+v"(ac[3]) : "v"(((u32x4){cA[0][2][0][0], cA[0][2][0][1], cA[0][2][1][0], cA[0][2][1][1]})) : ); SB();
  asm volatile("ds_read2_b32 %0, %1 offset0:130 offset1:131" : "=&v"(cA[1][6][1]) : "v"(aa[3]) : "memory"); SB();
  asm volatile("ds_read_b128 a[60:63], %0 offset:6144" :  : "v"(aA[1]) : "memory"); SB();
  asm volatile("v_mfma_f32_16x16x32_bf16 %0, %1, a[100:103], %0" : "+v"(ac[2]) : "v"(((u32x4){cA[0][1][0][0], cA[0][1][0][1], cA[0][1][1][0], cA[0][1][1][1]})) : ); SB();
  asm volatile("ds_read2_b32 %0, %1 offset0:0 offset1:1" : "=&v"(cA[1][7][0]) : "v"(aa[3]) : "memory"); SB();
  asm volatile("s_add_u32 m0, %0, 100608" :  : "s"(mA) : "memory", "scc"); SB();
  asm volatile("v_mfma_f32_16x16x32_bf16 %0, %1, a[100:103], %0" : "+v"(ac[1]) : "v"(((u32x4){cA[0][0][0][0], cA[0][0][0][1], cA[0][0][1][0], cA[0][0][1][1]})) : ); SB();
  asm volatile("ds_read2_b32 %0, %1 offset0:2 offset1:3" : "=&v"(cA[1][7][1]) : "v"(aa[3]) : "memory"); SB();
  asm volatile("buffer_load_dwordx4 %0, %1, %2 offen offset:768 lds" :  : "v"(voS), "s"(rA), "s"(ao[3]) : "memory"); SB();
  asm volatile("v_mfma_f32_16x16x32_bf16 %0, %1, a[96:99], %0" : "+v"(ac[7]) : "v"(((u32x4){cA[0][7][0][0], cA[0][7][0][1], cA[0][7][1][0], cA[0][7][1][1]})) : ); SB();
  asm volatile("ds_read_b128 a[128:131], %0 offset:0" :  : "v"(bb) : "memory"); SB();
  asm volatile("s_waitcnt vmcnt(11)" :  :  : "memory"); SB();
  asm volatile("v_mfma_f32_16x16x32_bf16 %0, %1, a[96:99], %0" : "+v"(ac[6]) : "v"(((u32x4){cA[0][6][0][0], cA[0][6][0][1], cA[0][6][1][0], cA[0][6][1][1]})) : ); SB();
  asm volatile("ds_read_b128 a[64:67], %0 offset:8192" :  : "v"(aA[0]) : "memory"); SB();
  asm volatile("v_mfma_f32_16x16x32_bf16 %0, %1, a[96:99], %0" : "+v"(ac[5]) : "v"(((u32x4){cA[0][5][0][0], cA[0][5][0][1], cA[0][5][1][0], cA[0][5][1][1]})) : ); SB();
  ++cn; dl = nch == 2 ? 0 : (cn & 7) ? -64 : 13376; asm volatile("" : "+s"(dl)); SB();
  asm volatile("ds_read_b128 a[80:83], %0 offset:8192" :  : "v"(aA[1]) : "memory"); SB();
  asm volatile("s_waitcnt lgkmcnt(15)" :  :  : "memory"); SB();
  asm volatile("v_mfma_f32_16x16x32_bf16 %0, %1, a[96:99], %0" : "+v"(ac[4]) : "v"(((u32x4){cA[0][4][0][0], cA[0][4][0][1], cA[0][4][1][0], cA[0][4][1][1]})) : ); SB();
  asm volatile("s_add_u32 m0, %0, 101632" :  : "s"(mA) : "memory", "scc"); SB();
  asm volatile("v_add_u32 %0, %1, %0" : "+v"(aa[0]) : "s"(dl) : ); SB();
  asm volatile("v_mfma_f32_16x16x32_bf16 %0, %1, a[96:99], %0" : "+v"(ac[3]) : "v"(((u32x4){cA[0][3][0][0], cA[0][3][0][1], cA[0][3][1][0], cA[0][3][1][1]})) : ); SB();
  asm volatile("buffer_load_dwordx4 %0, %1, %2 offen offset:768 lds" :  : "v"(voS), "s"(rA), "s"(ao[4]) : "memory"); SB();
  asm volatile("s_add_u32 m0, %0, 102656" :  : "s"(mA) : "memory", "scc"); SB();
  asm volatile("v_mfma_f32_16x16x32_bf16 %0, %1, a[96:99], %0" : "+v"(ac[2]) : "v"(((u32x4){cA[0][2][0][0], cA[0][2][0][1], cA[0][2][1][0], cA[0][2][1][1]})) : ); SB();
  asm volatile("buffer_load_dwordx4 %0, %1, %2 offen offset:768 lds" :  : "v"(voS), "s"(rA), "s"(ao[5]) : "memory"); SB();
  asm volatile("s_waitcnt lgkmcnt(5)" :  :  : "memory"); SB();
  asm volatile("v_mfma_f32_16x16x32_bf16 %0, %1, a[96:99], %0" : "+v"(ac[1]) : "v"(((u32x4){cA[0][1][0][0], cA[0][1][0][1], cA[0][1][1][0], cA[0][1][1][1]})) : ); SB();
  asm volatile("s_add_u32 m0, %0, 103680" :  : "s"(mA) : "memory", "scc"); SB();
  asm volatile("v_add_u32 %0, %1, %0" : "+v"(aa[1]) : "s"(dl) : ); SB();
  asm volatile("v_mfma_f32_16x16x32_bf16 %0, %1, a[96:99], %0" : "+v"(ac[0]) : "v"(((u32x4){cA[0][0][0][0], cA[0][0][0][1], cA[0][0][1][0], cA[0][0][1][1]})) : ); SB();
  asm volatile("buffer_load_dwordx4 %0, %1, %2 offen offset:768 lds" :  : "v"(voS), "s"(rA), "s"(ao[6]) : "memory"); SB();
  asm volatile("s_add_u32 m0, %0, 104704" :  : "s"(mA) : "memory", "scc"); SB();
  asm volatile("v_add_u32 %0, %1, %0" : "+v"(aa[2]) : "s"(dl) : ); SB();
  asm volatile("v_add_u32 %0, %1, %0" : "+v"(aa[3]) : "s"(dl) : ); SB();
  asm volatile("v_add_u32 %0, %1, %0" : "+v"(bb) : "s"(dl) : ); SB();
  asm volatile("v_mfma_f32_16x16x32_bf16 %0, %1, a[156:159], %0" : "+v"(ac[7]) : "v"(((u32x4){cA[1][0][0][0], cA[1][0][0][1], cA[1][0][1][0], cA[1][0][1][1]})) : ); SB();
  asm volatile("ds_read2_b32 %0, %1 offset0:128 offset1:129" : "=&v"(cA[0][0][0]) : "v"(aa[0]) : "memory"); SB();
  asm volatile("v_mfma_f32_16x16x32_bf16 %0, %1, a[152:155], %0" : "+v"(ac[6]) : "v"(((u32x4){cA[1][0][0][0], cA[1][0][0][1], cA[1][0][1][0], cA[1][0][1][1]})) : ); SB();
  asm volatile("ds_read2_b32 %0, %1 offset0:130 offset1:131" : "=&v"(cA[0][0][1]) : "v"(aa[0]) : "memory"); SB();
  asm volatile("v_mfma_f32_16x16x32_bf16 %0, %1, a[148:151], %0" : "+v"(ac[5]) : "v"(((u32x4){cA[1][0][0][0], cA[1][0][0][1], cA[1][0][1][0], cA[1][0][1][1]})) : ); SB();
  asm volatile("ds_read_b128 a[124:127], %0 offset:3584" :  : "v"(bb) : "memory"); SB();
  asm volatile("v_mfma_f32_16x16x32_bf16 %0, %1, a[144:147], %0" : "+v"(ac[4]) : "v"(((u32x4){cA[1][0][0][0], cA[1][0][0][1], cA[1][0][1][0], cA[1][0][1][1]})) : ); SB();
  asm volatile("ds_read_b128 a[120:123], %0 offset:3072" :  : "v"(bb) : "memory"); SB();
  asm volatile("v_mfma_f32_16x16x32_bf16 %0, %1, a[152:155], %0" : "+v"(ac[7]) : "v"(((u32x4){cA[1][1][0][0], cA[1][1][0][1], cA[1][1][1][0], cA[1][1][1][1]})) : ); SB();
  asm volatile("buffer_load_dwordx4 %0, %1, %2 offen offset:768 lds" :  : "v"(voS), "s"(rA), "s"(ao[7]) : "memory"); SB();
  asm volatile("s_waitcnt vmcnt(13)" :  :  : "memory"); SB();
  asm volatile("v_mfma_f32_16x16x32_bf16 %0, %1, a[148:151], %0" : "+v"(ac[6]) : "v"(((u32x4){cA[1][1][0][0], cA[1][1][0][1], cA[1][1][1][0], cA[1][1][1][1]})) : ); SB();
  asm volatile("ds_read_b128 a[116:119], %0 offset:2560" :  : "v"(bb) : "memory"); SB();
  asm volatile("v_mfma_f32_16x16x32_bf16 %0, %1, a[144:147], %0" : "+v"(ac[5]) : "v"(((u32x4){cA[1][1][0][0], cA[1][1][0][1], cA[1][1][1][0], cA[1][1][1][1]})) : ); SB();
  asm volatile("ds_read_b128 a[112:115], %0 offset:2048" :  : "v"(bb) : "memory"); SB();
  asm volatile("v_mfma_f32_16x16x32_bf16 %0, %1, a[140:143], %0" : "+v"(ac[4]) : "v"(((u32x4){cA[1][1][0][0], cA[1][1][0][1], cA[1][1][1][0], cA[1][1][1][1]})) : ); SB();
  asm volatile("ds_read2_b32 %0, %1 offset0:0 offset1:1" : "=&v"(cA[0][1][0]) : "v"(aa[0]) : "memory"); SB();
  asm volatile("v_mfma_f32_16x16x32_bf16 %0, %1, a[148:151], %0" : "+v"(ac[7]) : "v"(((u32x4){cA[1][2][0][0], cA[1][2][0][1], cA[1][2][1][0], cA[1][2][1][1]})) : ); SB();
  asm volatile("ds_read2_b32 %0, %1 offset0:2 offset1:3" : "=&v"(cA[0][1][1]) : "v"(aa[0]) : "memory"); SB();
  asm volatile("ds_read_b128 a[68:71], %0 offset:10240" :  : "v"(aA[0]) : "memory"); SB();
  asm volatile("v_mfma_f32_16x16x32_bf16 %0, %1, a[144:147], %0" : "+v"(ac[6]) : "v"(((u32x4){cA[1][2][0][0], cA[1][2][0][1], cA[1][2][1][0], cA[1][2][1][1]})) : ); SB();
  asm volatile("ds_read_b128 a[84:87], %0 offset:10240" :  : "v"(aA[1]) : "memory"); SB();
  asm volatile("s_waitcnt vmcnt(11)\n\t.Lc_%=:\n\tv_cmp_ne_u64 vcc, %2, %1\n\ts_cbranch_vccnz .Lr_%=\n\t.Ld_%=:\n\t.subsection 1\n\t.Lr_%=:\n\tv_add_u32 %0, -1, %0\n\ts_nop 1\n\tv_readfirstlane_b32 vcc_hi, %0\n\ts_nop 3\n\ts_cmp_lt_i32 vcc_hi, 1\n\ts_cbranch_scc1 .Ld_%=\n\ts_sleep 2\n\tbuffer_load_dwordx2 %1, %3, %4, 0 offen sc1\n\ts_waitcnt vmcnt(0)\n\ts_branch .Lc_%=\n\t.subsection 0" : "+v"(tmo), "+v"(fz) : "s"(tg), "v"(vFZ), "s"(rWS) : "memory", "vcc", "scc"); SB();
  asm volatile("v_mfma_f32_16x16x32_bf16 %0, %1, a[140:143], %0" : "+v"(ac[5]) : "v"(((u32x4){cA[1][2][0][0], cA[1][2][0][1], cA[1][2][1][0], cA[1][2][1][1]})) : ); SB();
  asm volatile("ds_read_b128 a[108:111], %0 offset:1536" :  : "v"(bb) : "memory"); SB();
  asm volatile("buffer_load_dwordx4 v[160:163], %0, %1, %2 offen offset:0 sc1" :  : "v"(vB), "s"(rWS), "s"(0u) : "memory"); SB();
  asm volatile("v_mfma_f32_16x16x32_bf16 %0, %1, a[140:143], %0" : "+v"(ac[3]) : "v"(((u32x4){cA[1][0][0][0], cA[1][0][0][1], cA[1][0][1][0], cA[1][0][1][1]})) : ); SB();
  asm volatile("ds_read2_b32 %0, %1 offset0:128 offset1:129" : "=&v"(cA[0][2][0]) : "v"(aa[1]) : "memory"); SB();
  asm volatile("s_waitcnt vmcnt(11)" :  :  : "memory"); SB();
  asm volatile("v_mfma_f32_16x16x32_bf16 %0, %1, a[144:147], %0" : "+v"(ac[7]) : "v"(((u32x4){cA[1][3][0][0], cA[1][3][0][1], cA[1][3][1][0], cA[1][3][1][1]})) : ); SB();
  asm volatile("ds_read2_b32 %0, %1 offset0:130 offset1:131" : "=&v"(cA[0][2][1]) : "v"(aa[1]) : "memory"); SB();
  asm volatile("ds_read_b128 a[72:75], %0 offset:12288" :  : "v"(aA[0]) : "memory"); SB();
  asm volatile("v_mfma_f32_16x16x32_bf16 %0, %1, a[140:143], %0" : "+v"(ac[6]) : "v"(((u32x4){cA[1][3][0][0], cA[1][3][0][1], cA[1][3][1][0], cA[1][3][1][1]})) : ); SB();
  asm volatile("ds_read2_b32 %0, %1 offset0:0 offset1:1" : "=&v"(cA[0][3][0]) : "v"(aa[1]) : "memory"); SB();
  asm volatile("ds_read_b128 a[88:91], %0 offset:12288" :  : "v"(aA[1]) : "memory"); SB();
  asm volatile("v_mfma_f32_16x16x32_bf16 %0, %1, a[136:139], %0" : "+v"(ac[5]) : "v"(((u32x4){cA[1][3][0][0], cA[1][3][0][1], cA[1][3][1][0], cA[1][3][1][1]})) : ); SB();
  asm volatile("buffer_load_dwordx4 v[164:167], %0, %1, %2 offen offset:1024 sc1" :  : "v"(vB), "s"(rWS), "s"(0u) : "memory"); SB();
  asm volatile("v_mfma_f32_16x16x32_bf16 %0, %1, a[136:139], %0" : "+v"(ac[4]) : "v"(((u32x4){cA[1][2][0][0], cA[1][2][0][1], cA[1][2][1][0], cA[1][2][1][1]})) : ); SB();
  asm volatile("ds_read2_b32 %0, %1 offset0:2 offset1:3" : "=&v"(cA[0][3][1]) : "v"(aa[1]) : "memory"); SB();
  asm volatile("v_mfma_f32_16x16x32_bf16 %0, %1, a[140:143], %0" : "+v"(ac[7]) : "v"(((u32x4){cA[1][4][0][0], cA[1][4][0][1], cA[1][4][1][0], cA[1][4][1][1]})) : ); SB();
  asm volatile("ds_read_b128 a[104:107], %0 offset:1024" :  : "v"(bb) : "memory"); SB();
  asm volatile("v_mfma_f32_16x16x32_bf16 %0, %1, a[136:139], %0" : "+v"(ac[6]) : "v"(((u32x4){cA[1][4][0][0], cA[1][4][0][1], cA[1][4][1][0], cA[1][4][1][1]})) : ); SB();
  asm volatile("ds_read2_b32 %0, %1 offset0:128 offset1:129" : "=&v"(cA[0][4][0]) : "v"(aa[2]) : "memory"); SB();
  asm volatile("v_mfma_f32_16x16x32_bf16 %0, %1, a[136:139], %0" : "+v"(ac[3]) : "v"(((u32x4){cA[1][1][0][0], cA[1][1][0][1], cA[1][1][1][0], cA[1][1][1][1]})) : ); SB();
  asm volatile("ds_read2_b32 %0, %1 offset0:130 offset1:131" : "=&v"(cA[0][4][1]) : "v"(aa[2]) : "memory"); SB();
  asm volatile("v_mfma_f32_16x16x32_bf16 %0, %1, a[136:139], %0" : "+v"(ac[2]) : "v"(((u32x4){cA[1][0][0][0], cA[1][0][0][1], cA[1][0][1][0], cA[1][0][1][1]})) : ); SB();
  asm volatile("buffer_load_dwordx4 v[168:171], %0, %1, %2 offen offset:0 sc1" :  : "v"(vB), "s"(rWS), "s"(49152u) : "memory"); SB();
  asm volatile("v_mfma_f32_16x16x32_bf16 %0, %1, a[136:139], %0" : "+v"(ac[7]) : "v"(((u32x4){cA[1][5][0][0], cA[1][5][0][1], cA[1][5][1][0], cA[1][5][1][1]})) : ); SB();
  asm volatile("ds_read2_b32 %0, %1 offset0:0 offset1:1" : "=&v"(cA[0][5][0]) : "v"(aa[2]) : "memory"); SB();
  asm volatile("v_mfma_f32_16x16x32_bf16 %0, %1, a[132:135], %0" : "+v"(ac[6]) : "v"(((u32x4){cA[1][5][0][0], cA[1][5][0][1], cA[1][5][1][0], cA[1][5][1][1]})) : ); SB();
  asm volatile("ds_read2_b32 %0, %1 offset0:2 offset1:3" : "=&v"(cA[0][5][1]) : "v"(aa[2]) : "memory"); SB();
  asm volatile("v_mfma_f32_16x16x32_bf16 %0, %1, a[132:135], %0" : "+v"(ac[5]) : "v"(((u32x4){cA[1][4][0][0], cA[1][4][0][1], cA[1][4][1][0], cA[1][4][1][1]})) : ); SB();
  asm volatile("ds_read_b128 a[100:103], %0 offset:512" :  : "v"(bb) : "memory"); SB();
  asm volatile("v_mfma_f32_16x16x32_bf16 %0, %1, a[132:135], %0" : "+v"(ac[4]) : "v"(((u32x4){cA[1][3][0][0], cA[1][3][0][1], cA[1][3][1][0], cA[1][3][1][1]})) : ); SB();
  asm volatile("ds_read2_b32 %0, %1 offset0:128 offset1:129" : "=&v"(cA[0][6][0]) : "v"(aa[3]) : "memory"); SB();
  asm volatile("v_mfma_f32_16x16x32_bf16 %0, %1, a[132:135], %0" : "+v"(ac[7]) : "v"(((u32x4){cA[1][6][0][0], cA[1][6][0][1], cA[1][6][1][0], cA[1][6][1][1]})) : ); SB();
  asm volatile("buffer_load_dwordx4 v[172:175], %0, %1, %2 offen offset:1024 sc1" :  : "v"(vB), "s"(rWS), "s"(49152u) : "memory"); SB();
  asm volatile("v_mfma_f32_16x16x32_bf16 %0, %1, a[132:135], %0" : "+v"(ac[3]) : "v"(((u32x4){cA[1][2][0][0], cA[1][2][0][1], cA[1][2][1][0], cA[1][2][1][1]})) : ); SB();
  asm volatile("ds_read2_b32 %0, %1 offset0:130 offset1:131" : "=&v"(cA[0][6][1]) : "v"(aa[3]) : "memory"); SB();
  asm volatile("v_mfma_f32_16x16x32_bf16 %0, %1, a[132:135], %0" : "+v"(ac[2]) : "v"(((u32x4){cA[1][1][0][0], cA[1][1][0][1], cA[1][1][1][0], cA[1][1][1][1]})) : ); SB();
  asm volatile("ds_read2_b32 %0, %1 offset0:0 offset1:1" : "=&v"(cA[0][7][0]) : "v"(aa[3]) : "memory"); SB();
  asm volatile("v_mfma_f32_16x16x32_bf16 %0, %1, a[132:135], %0" : "+v"(ac[1]) : "v"(((u32x4){cA[1][0][0][0], cA[1][0][0][1], cA[1][0][1][0], cA[1][0][1][1]})) : ); SB();
  asm volatile("ds_read2_b32 %0, %1 offset0:2 offset1:3" : "=&v"(cA[0][7][1]) : "v"(aa[3]) : "memory"); SB();
  asm volatile("s_waitcnt lgkmcnt(15)\n\tv_mfma_f32_16x16x32_bf16 %0, %1, a[128:131], %0" : "+v"(ac[7]) : "v"(((u32x4){cA[1][7][0][0], cA[1][7][0][1], cA[1][7][1][0], cA[1][7][1][1]})) : ); SB();
  asm volatile("ds_read_b128 a[96:99], %0 offset:0" :  : "v"(bb) : "memory"); SB();
  asm volatile("v_mfma_f32_16x16x32_bf16 %0, %1, a[128:131], %0" : "+v"(ac[6]) : "v"(((u32x4){cA[1][6][0][0], cA[1][6][0][1], cA[1][6][1][0], cA[1][6][1][1]})) : ); SB();
  asm volatile("buffer_load_dwordx4 v[176:179], %0, %1, %2 offen offset:0 sc1" :  : "v"(vB), "s"(rWS), "s"(98304u) : "memory"); SB();
  asm volatile("v_mfma_f32_16x16x32_bf16 %0, %1, a[128:131], %0" : "+v"(ac[5]) : "v"(((u32x4){cA[1][5][0][0], cA[1][5][0][1], cA[1][5][1][0], cA[1][5][1][1]})) : ); SB();
  ++cn; dl = (cn & 7) ? -64 : 13376; asm volatile("" : "+s"(dl)); SB();
  asm volatile("buffer_load_dwordx4 v[180:183], %0, %1, %2 offen offset:1024 sc1" :  : "v"(vB), "s"(rWS), "s"(98304u) : "memory"); SB();
  asm volatile("v_add_u32 %0, %1, %0" : "+v"(aa[0]) : "s"(dl) : ); SB();
  asm volatile("v_mfma_f32_16x16x32_bf16 %0, %1, a[128:131], %0" : "+v"(ac[4]) : "v"(((u32x4){cA[1][4][0][0], cA[1][4][0][1], cA[1][4][1][0], cA[1][4][1][1]})) : ); SB();
  asm volatile("s_waitcnt vmcnt(14)" :  :  : "memory"); SB();
  asm volatile("ds_read_b128 a[76:79], %0 offset:14336" :  : "v"(aA[0]) : "memory"); SB();
  asm volatile("v_mfma_f32_16x16x32_bf16 %0, %1, a[128:131], %0" : "+v"(ac[3]) : "v"(((u32x4){cA[1][3][0][0], cA[1][3][0][1], cA[1][3][1][0], cA[1][3][1][1]})) : ); SB();
  asm volatile("ds_read_b128 a[92:95], %0 offset:14336" :  : "v"(aA[1]) : "memory"); SB();
  asm volatile("v_add_u32 %0, %1, %0" : "+v"(aa[1]) : "s"(dl) : ); SB();
  asm volatile("v_mfma_f32_16x16x32_bf16 %0, %1, a[128:131], %0" : "+v"(ac[2]) : "v"(((u32x4){cA[1][2][0][0], cA[1][2][0][1], cA[1][2][1][0], cA[1][2][1][1]})) : ); SB();
  asm volatile("buffer_load_dwordx4 v[184:187], %0, %1, %2 offen offset:0 sc1" :  : "v"(vB), "s"(rWS), "s"(147456u) : "memory"); SB();
  asm volatile("v_add_u32 %0, %1, %0" : "+v"(aa[2]) : "s"(dl) : ); SB();
  asm volatile("v_mfma_f32_16x16x32_bf16 %0, %1, a[128:131], %0" : "+v"(ac[1]) : "v"(((u32x4){cA[1][1][0][0], cA[1][1][0][1], cA[1][1][1][0], cA[1][1][1][1]})) : ); SB();
  asm volatile("v_add_u32 %0, %1, %0" : "+v"(aa[3]) : "s"(dl) : ); SB();
  asm volatile("v_mfma_f32_16x16x32_bf16 %0, %1, a[128:131], %0" : "+v"(ac[0]) : "v"(((u32x4){cA[1][0][0][0], cA[1][0][0][1], cA[1][0][1][0], cA[1][0][1][1]})) : ); SB();
  asm volatile("v_add_u32 %0, %1, %0" : "+v"(bb) : "s"(dl) : ); SB();
  cu += 2;
  if ((cu & 7) == 0 && nch == 3) {
  asm volatile("s_nop 7" : "+v"(ac[0]), "+v"(ac[1]), "+v"(ac[2]), "+v"(ac[3]), "+v"(ac[4]), "+v"(ac[5]), "+v"(ac[6]), "+v"(ac[7]) :  : ); SB();
    const unsigned dd = da + (((cu - 1) >> 3) * 2 + 1) * 8192;
  asm volatile("ds_write_b128 %0, %1 offset:0" :  : "v"(dd), "v"(ac[0]) : "memory"); SB();
  asm volatile("ds_write_b128 %0, %1 offset:1024" :  : "v"(dd), "v"(ac[1]) : "memory"); SB();
  asm volatile("ds_write_b128 %0, %1 offset:2048" :  : "v"(dd), "v"(ac[2]) : "memory"); SB();
  asm volatile("ds_write_b128 %0, %1 offset:3072" :  : "v"(dd), "v"(ac[3]) : "memory"); SB();
  asm volatile("ds_write_b128 %0, %1 offset:4096" :  : "v"(dd), "v"(ac[4]) : "memory"); SB();
  asm volatile("ds_write_b128 %0, %1 offset:5120" :  : "v"(dd), "v"(ac[5]) : "memory"); SB();
  asm volatile("ds_write_b128 %0, %1 offset:6144" :  : "v"(dd), "v"(ac[6]) : "memory"); SB();
  asm volatile("ds_write_b128 %0, %1 offset:7168" :  : "v"(dd), "v"(ac[7]) : "memory"); SB();
#pragma unroll
  for (int t = 0; t < 8; ++t) ac[t] = (f32x4){0.f, 0.f, 0.f, 0.f};
  asm volatile("s_nop 1" : "+v"(ac[0]), "+v"(ac[1]), "+v"(ac[2]), "+v"(ac[3]), "+v"(ac[4]), "+v"(ac[5]), "+v"(ac[6]), "+v"(ac[7]) :  : ); SB();
  }
  asm volatile("s_waitcnt lgkmcnt(0)" :  :  : "memory"); SB();
  if (nch == 3) {
  asm volatile("v_mfma_f32_16x16x32_bf16 %0, %1, a[124:127], %0" : "+v"(ac[7]) : "v"(((u32x4){cA[0][0][0][0], cA[0][0][0][1], cA[0][0][1][0], cA[0][0][1][1]})) : ); SB();
  asm volatile("ds_read2_b32 %0, %1 offset0:128 offset1:129" : "=&v"(cA[1][0][0]) : "v"(aa[0]) : "memory"); SB();
  asm volatile("v_mfma_f32_16x16x32_bf16 %0, %1, a[120:123], %0" : "+v"(ac[6]) : "v"(((u32x4){cA[0][0][0][0], cA[0][0][0][1], cA[0][0][1][0], cA[0][0][1][1]})) : ); SB();
  asm volatile("ds_read2_b32 %0, %1 offset0:130 offset1:131" : "=&v"(cA[1][0][1]) : "v"(aa[0]) : "memory"); SB();
  asm volatile("v_mfma_f32_16x16x32_bf16 %0, %1, a[116:119], %0" : "+v"(ac[5]) : "v"(((u32x4){cA[0][0][0][0], cA[0][0][0][1], cA[0][0][1][0], cA[0][0][1][1]})) : ); SB();
  asm volatile("ds_read_b128 a[156:159], %0 offset:3584" :  : "v"(bb) : "memory"); SB();
  asm volatile("v_mfma_f32_16x16x32_bf16 %0, %1, a[112:115], %0" : "+v"(ac[4]) : "v"(((u32x4){cA[0][0][0][0], cA[0][0][0][1], cA[0][0][1][0], cA[0][0][1][1]})) : ); SB();
  asm volatile("ds_read_b128 a[152:155], %0 offset:3072" :  : "v"(bb) : "memory"); SB();
  asm volatile("v_mfma_f32_16x16x32_bf16 %0, %1, a[120:123], %0" : "+v"(ac[7]) : "v"(((u32x4){cA[0][1][0][0], cA[0][1][0][1], cA[0][1][1][0], cA[0][1][1][1]})) : ); SB();
  asm volatile("buffer_load_dwordx4 v[188:191], %0, %1, %2 offen offset:1024 sc1" :  : "v"(vB), "s"(rWS), "s"(147456u) : "memory"); SB();
  asm volatile("v_mfma_f32_16x16x32_bf16 %0, %1, a[116:119], %0" : "+v"(ac[6]) : "v"(((u32x4){cA[0][1][0][0], cA[0][1][0][1], cA[0][1][1][0], cA[0][1][1][1]})) : ); SB();
  asm volatile("ds_read_b128 a[148:151], %0 offset:2560" :  : "v"(bb) : "memory"); SB();
  asm volatile("v_mfma_f32_16x16x32_bf16 %0, %1, a[112:115], %0" : "+v"(ac[5]) : "v"(((u32x4){cA[0][1][0][0], cA[0][1][0][1], cA[0][1][1][0], cA[0][1][1][1]})) : ); SB();
  asm volatile("ds_read_b128 a[144:147], %0 offset:2048" :  : "v"(bb) : "memory"); SB();
  asm volatile("v_mfma_f32_16x16x32_bf16 %0, %1, a[108:111], %0" : "+v"(ac[4]) : "v"(((u32x4){cA[0][1][0][0], cA[0][1][0][1], cA[0][1][1][0], cA[0][1][1][1]})) : ); SB();
  asm volatile("ds_read2_b32 %0, %1 offset0:0 offset1:1" : "=&v"(cA[1][1][0]) : "v"(aa[0]) : "memory"); SB();
  asm volatile("v_mfma_f32_16x16x32_bf16 %0, %1, a[116:119], %0" : "+v"(ac[7]) : "v"(((u32x4){cA[0][2][0][0], cA[0][2][0][1], cA[0][2][1][0], cA[0][2][1][1]})) : ); SB();
  asm volatile("ds_read2_b32 %0, %1 offset0:2 offset1:3" : "=&v"(cA[1][1][1]) : "v"(aa[0]) : "memory"); SB();
  asm volatile("v_mfma_f32_16x16x32_bf16 %0, %1, a[112:115], %0" : "+v"(ac[6]) : "v"(((u32x4){cA[0][2][0][0], cA[0][2][0][1], cA[0][2][1][0], cA[0][2][1][1]})) : ); SB();
  asm volatile("buffer_load_dwordx4 v[192:195], %0, %1, %2 offen offset:0 sc1" :  : "v"(vB), "s"(rWS), "s"(196608u) : "memory"); SB();
  asm volatile("v_mfma_f32_16x16x32_bf16 %0, %1, a[108:111], %0" : "+v"(ac[5]) : "v"(((u32x4){cA[0][2][0][0], cA[0][2][0][1], cA[0][2][1][0], cA[0][2][1][1]})) : ); SB();
  asm volatile("ds_read_b128 a[140:143], %0 offset:1536" :  : "v"(bb) : "memory"); SB();
  asm volatile("v_mfma_f32_16x16x32_bf16 %0, %1, a[108:111], %0" : "+v"(ac[3]) : "v"(((u32x4){cA[0][0][0][0], cA[0][0][0][1], cA[0][0][1][0], cA[0][0][1][1]})) : ); SB();
  asm volatile("ds_read2_b32 %0, %1 offset0:128 offset1:129" : "=&v"(cA[1][2][0]) : "v"(aa[1]) : "memory"); SB();
  asm volatile("v_mfma_f32_16x16x32_bf16 %0, %1, a[112:115], %0" : "+v"(ac[7]) : "v"(((u32x4){cA[0][3][0][0], cA[0][3][0][1], cA[0][3][1][0], cA[0][3][1][1]})) : ); SB();
  asm volatile("ds_read2_b32 %0, %1 offset0:130 offset1:131" : "=&v"(cA[1][2][1]) : "v"(aa[1]) : "memory"); SB();
  asm volatile("v_mfma_f32_16x16x32_bf16 %0, %1, a[108:111], %0" : "+v"(ac[6]) : "v"(((u32x4){cA[0][3][0][0], cA[0][3][0][1], cA[0][3][1][0], cA[0][3][1][1]})) : ); SB();
  asm volatile("ds_read2_b32 %0, %1 offset0:0 offset1:1" : "=&v"(cA[1][3][0]) : "v"(aa[1]) : "memory"); SB();
  asm volatile("v_mfma_f32_16x16x32_bf16 %0, %1, a[104:107], %0" : "+v"(ac[5]) : "v"(((u32x4){cA[0][3][0][0], cA[0][3][0][1], cA[0][3][1][0], cA[0][3][1][1]})) : ); SB();
  asm volatile("buffer_load_dwordx4 v[196:199], %0, %1, %2 offen offset:1024 sc1" :  : "v"(vB), "s"(rWS), "s"(196608u) : "memory"); SB();
  asm volatile("v_mfma_f32_16x16x32_bf16 %0, %1, a[104:107], %0" : "+v"(ac[4]) : "v"(((u32x4){cA[0][2][0][0], cA[0][2][0][1], cA[0][2][1][0], cA[0][2][1][1]})) : ); SB();
  asm volatile("ds_read2_b32 %0, %1 offset0:2 offset1:3" : "=&v"(cA[1][3][1]) : "v"(aa[1]) : "memory"); SB();
  asm volatile("v_mfma_f32_16x16x32_bf16 %0, %1, a[108:111], %0" : "+v"(ac[7]) : "v"(((u32x4){cA[0][4][0][0], cA[0][4][0][1], cA[0][4][1][0], cA[0][4][1][1]})) : ); SB();
  asm volatile("ds_read_b128 a[136:139], %0 offset:1024" :  : "v"(bb) : "memory"); SB();
  asm volatile("v_mfma_f32_16x16x32_bf16 %0, %1, a[104:107], %0" : "+v"(ac[6]) : "v"(((u32x4){cA[0][4][0][0], cA[0][4][0][1], cA[0][4][1][0], cA[0][4][1][1]})) : ); SB();
  asm volatile("ds_read2_b32 %0, %1 offset0:128 offset1:129" : "=&v"(cA[1][4][0]) : "v"(aa[2]) : "memory"); SB();
  asm volatile("v_mfma_f32_16x16x32_bf16 %0, %1, a[104:107], %0" : "+v"(ac[3]) : "v"(((u32x4){cA[0][1][0][0], cA[0][1][0][1], cA[0][1][1][0], cA[0][1][1][1]})) : ); SB();
  asm volatile("ds_read2_b32 %0, %1 offset0:130 offset1:131" : "=&v"(cA[1][4][1]) : "v"(aa[2]) : "memory"); SB();
  asm volatile("buffer_load_dwordx4 v[200:203], %0, %1, %2 offen offset:0 sc1" :  : "v"(vB), "s"(rWS), "s"(245760u) : "memory"); SB();
  asm volatile("v_mfma_f32_16x16x32_bf16 %0, %1, a[104:107], %0" : "+v"(ac[2]) : "v"(((u32x4){cA[0][0][0][0], cA[0][0][0][1], cA[0][0][1][0], cA[0][0][1][1]})) : ); SB();
  asm volatile("buffer_load_dwordx4 v[204:207], %0, %1, %2 offen offset:1024 sc1" :  : "v"(vB), "s"(rWS), "s"(245760u) : "memory"); SB();
  asm volatile("v_mfma_f32_16x16x32_bf16 %0, %1, a[104:107], %0" : "+v"(ac[7]) : "v"(((u32x4){cA[0][5][0][0], cA[0][5][0][1], cA[0][5][1][0], cA[0][5][1][1]})) : ); SB();
  asm volatile("ds_read2_b32 %0, %1 offset0:0 offset1:1" : "=&v"(cA[1][5][0]) : "v"(aa[2]) : "memory"); SB();
  asm volatile("v_mfma_f32_16x16x32_bf16 %0, %1, a[100:103], %0" : "+v"(ac[6]) : "v"(((u32x4){cA[0][5][0][0], cA[0][5][0][1], cA[0][5][1][0], cA[0][5][1][1]})) : ); SB();
  asm volatile("ds_read2_b32 %0, %1 offset0:2 offset1:3" : "=&v"(cA[1][5][1]) : "v"(aa[2]) : "memory"); SB();
  asm volatile("v_mfma_f32_16x16x32_bf16 %0, %1, a[100:103], %0" : "+v"(ac[5]) : "v"(((u32x4){cA[0][4][0][0], cA[0][4][0][1], cA[0][4][1][0], cA[0][4][1][1]})) : ); SB();
  asm volatile("ds_read_b128 a[132:135], %0 offset:512" :  : "v"(bb) : "memory"); SB();
  asm volatile("v_mfma_f32_16x16x32_bf16 %0, %1, a[100:103], %0" : "+v"(ac[4]) : "v"(((u32x4){cA[0][3][0][0], cA[0][3][0][1], cA[0][3][1][0], cA[0][3][1][1]})) : ); SB();
  asm volatile("ds_read2_b32 %0, %1 offset0:128 offset1:129" : "=&v"(cA[1][6][0]) : "v"(aa[3]) : "memory"); SB();
  asm volatile("v_mfma_f32_16x16x32_bf16 %0, %1, a[100:103], %0" : "+v"(ac[7]) : "v"(((u32x4){cA[0][6][0][0], cA[0][6][0][1], cA[0][6][1][0], cA[0][6][1][1]})) : ); SB();
  asm volatile("buffer_load_dwordx4 v[208:211], %0, %1, %2 offen offset:0 sc1" :  : "v"(vB), "s"(rWS), "s"(294912u) : "memory"); SB();
  asm volatile("v_mfma_f32_16x16x32_bf16 %0, %1, a[100:103], %0" : "+v"(ac[3]) : "v"(((u32x4){cA[0][2][0][0], cA[0][2][0][1], cA[0][2][1][0], cA[0][2][1][1]})) : ); SB();
  asm volatile("ds_read2_b32 %0, %1 offset0:130 offset1:131" : "=&v"(cA[1][6][1]) : "v"(aa[3]) : "memory"); SB();
  asm volatile("v_mfma_f32_16x16x32_bf16 %0, %1, a[100:103], %0" : "+v"(ac[2]) : "v"(((u32x4){cA[0][1][0][0], cA[0][1][0][1], cA[0][1][1][0], cA[0][1][1][1]})) : ); SB();
  asm volatile("ds_read2_b32 %0, %1 offset0:0 offset1:1" : "=&v"(cA[1][7][0]) : "v"(aa[3]) : "memory"); SB();
  asm volatile("v_mfma_f32_16x16x32_bf16 %0, %1, a[100:103], %0" : "+v"(ac[1]) : "v"(((u32x4){cA[0][0][0][0], cA[0][0][0][1], cA[0][0][1][0], cA[0][0][1][1]})) : ); SB();
  asm volatile("ds_read2_b32 %0, %1 offset0:2 offset1:3" : "=&v"(cA[1][7][1]) : "v"(aa[3]) : "memory"); SB();
  asm volatile("v_mfma_f32_16x16x32_bf16 %0, %1, a[96:99], %0" : "+v"(ac[7]) : "v"(((u32x4){cA[0][7][0][0], cA[0][7][0][1], cA[0][7][1][0], cA[0][7][1][1]})) : ); SB();
  asm volatile("ds_read_b128 a[128:131], %0 offset:0" :  : "v"(bb) : "memory"); SB();
  asm volatile("v_mfma_f32_16x16x32_bf16 %0, %1, a[96:99], %0" : "+v"(ac[6]) : "v"(((u32x4){cA[0][6][0][0], cA[0][6][0][1], cA[0][6][1][0], cA[0][6][1][1]})) : ); SB();
  asm volatile("buffer_load_dwordx4 v[212:215], %0, %1, %2 offen offset:1024 sc1" :  : "v"(vB), "s"(rWS), "s"(294912u) : "memory"); SB();
  asm volatile("v_mfma_f32_16x16x32_bf16 %0, %1, a[96:99], %0" : "+v"(ac[5]) : "v"(((u32x4){cA[0][5][0][0], cA[0][5][0][1], cA[0][5][1][0], cA[0][5][1][1]})) : ); SB();
  asm volatile("buffer_load_dwordx4 v[216:219], %0, %1, %2 offen offset:0 sc1" :  : "v"(vB), "s"(rWS), "s"(344064u) : "memory"); SB();
  asm volatile("v_mfma_f32_16x16x32_bf16 %0, %1, a[96:99], %0" : "+v"(ac[4]) : "v"(((u32x4){cA[0][4][0][0], cA[0][4][0][1], cA[0][4][1][0], cA[0][4][1][1]})) : ); SB();
  asm volatile("buffer_load_dwordx4 v[220:223], %0, %1, %2 offen offset:1024 sc1" :  : "v"(vB), "s"(rWS), "s"(344064u) : "memory"); SB();
  asm volatile("v_mfma_f32_16x16x32_bf16 %0, %1, a[96:99], %0" : "+v"(ac[3]) : "v"(((u32x4){cA[0][3][0][0], cA[0][3][0][1], cA[0][3][1][0], cA[0][3][1][1]})) : ); SB();
  asm volatile("v_mfma_f32_16x16x32_bf16 %0, %1, a[96:99], %0" : "+v"(ac[2]) : "v"(((u32x4){cA[0][2][0][0], cA[0][2][0][1], cA[0][2][1][0], cA[0][2][1][1]})) : ); SB();
  asm volatile("buffer_load_dwordx4 v[224:227], %0, %1, %2 offen offset:0 sc1" :  : "v"(vB), "s"(rWS), "s"(393216u) : "memory"); SB();
  asm volatile("v_mfma_f32_16x16x32_bf16 %0, %1, a[96:99], %0" : "+v"(ac[1]) : "v"(((u32x4){cA[0][1][0][0], cA[0][1][0][1], cA[0][1][1][0], cA[0][1][1][1]})) : ); SB();
  asm volatile("v_mfma_f32_16x16x32_bf16 %0, %1, a[96:99], %0" : "+v"(ac[0]) : "v"(((u32x4){cA[0][0][0][0], cA[0][0][0][1], cA[0][0][1][0], cA[0][0][1][1]})) : ); SB();
  asm volatile("s_waitcnt lgkmcnt(15)\n\tv_mfma_f32_16x16x32_bf16 %0, %1, a[156:159], %0" : "+v"(ac[7]) : "v"(((u32x4){cA[1][0][0][0], cA[1][0][0][1], cA[1][0][1][0], cA[1][0][1][1]})) : ); SB();
  asm volatile("v_mfma_f32_16x16x32_bf16 %0, %1, a[152:155], %0" : "+v"(ac[6]) : "v"(((u32x4){cA[1][0][0][0], cA[1][0][0][1], cA[1][0][1][0], cA[1][0][1][1]})) : ); SB();
  asm volatile("v_mfma_f32_16x16x32_bf16 %0, %1, a[148:151], %0" : "+v"(ac[5]) : "v"(((u32x4){cA[1][0][0][0], cA[1][0][0][1], cA[1][0][1][0], cA[1][0][1][1]})) : ); SB();
  asm volatile("buffer_load_dwordx4 v[228:231], %0, %1, %2 offen offset:1024 sc1" :  : "v"(vB), "s"(rWS), "s"(393216u) : "memory"); SB();
  asm volatile("v_mfma_f32_16x16x32_bf16 %0, %1, a[144:147], %0" : "+v"(ac[4]) : "v"(((u32x4){cA[1][0][0][0], cA[1][0][0][1], cA[1][0][1][0], cA[1][0][1][1]})) : ); SB();
  asm volatile("v_mfma_f32_16x16x32_bf16 %0, %1, a[152:155], %0" : "+v"(ac[7]) : "v"(((u32x4){cA[1][1][0][0], cA[1][1][0][1], cA[1][1][1][0], cA[1][1][1][1]})) : ); SB();
  asm volatile("v_mfma_f32_16x16x32_bf16 %0, %1, a[148:151], %0" : "+v"(ac[6]) : "v"(((u32x4){cA[1][1][0][0], cA[1][1][0][1], cA[1][1][1][0], cA[1][1][1][1]})) : ); SB();
  asm volatile("v_mfma_f32_16x16x32_bf16 %0, %1, a[144:147], %0" : "+v"(ac[5]) : "v"(((u32x4){cA[1][1][0][0], cA[1][1][0][1], cA[1][1][1][0], cA[1][1][1][1]})) : ); SB();
  asm volatile("buffer_load_dwordx4 v[232:235], %0, %1, %2 offen offset:0 sc1" :  : "v"(vB), "s"(rWS), "s"(442368u) : "memory"); SB();
  asm volatile("v_mfma_f32_16x16x32_bf16 %0, %1, a[140:143], %0" : "+v"(ac[4]) : "v"(((u32x4){cA[1][1][0][0], cA[1][1][0][1], cA[1][1][1][0], cA[1][1][1][1]})) : ); SB();
  asm volatile("s_waitcnt lgkmcnt(13)\n\tv_mfma_f32_16x16x32_bf16 %0, %1, a[148:151], %0" : "+v"(ac[7]) : "v"(((u32x4){cA[1][2][0][0], cA[1][2][0][1], cA[1][2][1][0], cA[1][2][1][1]})) : ); SB();
  asm volatile("v_mfma_f32_16x16x32_bf16 %0, %1, a[144:147], %0" : "+v"(ac[6]) : "v"(((u32x4){cA[1][2][0][0], cA[1][2][0][1], cA[1][2][1][0], cA[1][2][1][1]})) : ); SB();
  asm volatile("v_mfma_f32_16x16x32_bf16 %0, %1, a[140:143], %0" : "+v"(ac[5]) : "v"(((u32x4){cA[1][2][0][0], cA[1][2][0][1], cA[1][2][1][0], cA[1][2][1][1]})) : ); SB();
  asm volatile("v_mfma_f32_16x16x32_bf16 %0, %1, a[140:143], %0" : "+v"(ac[3]) : "v"(((u32x4){cA[1][0][0][0], cA[1][0][0][1], cA[1][0][1][0], cA[1][0][1][1]})) : ); SB();
  asm volatile("buffer_load_dwordx4 v[236:239], %0, %1, %2 offen offset:1024 sc1" :  : "v"(vB), "s"(rWS), "s"(442368u) : "memory"); SB();
  asm volatile("s_waitcnt lgkmcnt(11)\n\tv_mfma_f32_16x16x32_bf16 %0, %1, a[144:147], %0" : "+v"(ac[7]) : "v"(((u32x4){cA[1][3][0][0], cA[1][3][0][1], cA[1][3][1][0], cA[1][3][1][1]})) : ); SB();
  asm volatile("v_mfma_f32_16x16x32_bf16 %0, %1, a[140:143], %0" : "+v"(ac[6]) : "v"(((u32x4){cA[1][3][0][0], cA[1][3][0][1], cA[1][3][1][0], cA[1][3][1][1]})) : ); SB();
  asm volatile("s_waitcnt lgkmcnt(10)\n\tv_mfma_f32_16x16x32_bf16 %0, %1, a[136:139], %0" : "+v"(ac[5]) : "v"(((u32x4){cA[1][3][0][0], cA[1][3][0][1], cA[1][3][1][0], cA[1][3][1][1]})) : ); SB();
  asm volatile("v_mfma_f32_16x16x32_bf16 %0, %1, a[136:139], %0" : "+v"(ac[4]) : "v"(((u32x4){cA[1][2][0][0], cA[1][2][0][1], cA[1][2][1][0], cA[1][2][1][1]})) : ); SB();
  asm volatile("s_waitcnt lgkmcnt(8)\n\tv_mfma_f32_16x16x32_bf16 %0, %1, a[140:143], %0" : "+v"(ac[7]) : "v"(((u32x4){cA[1][4][0][0], cA[1][4][0][1], cA[1][4][1][0], cA[1][4][1][1]})) : ); SB();
  asm volatile("buffer_load_dwordx4 v[240:243], %0, %1, %2 offen offset:0 sc1" :  : "v"(vB), "s"(rWS), "s"(491520u) : "memory"); SB();
  asm volatile("v_mfma_f32_16x16x32_bf16 %0, %1, a[136:139], %0" : "+v"(ac[6]) : "v"(((u32x4){cA[1][4][0][0], cA[1][4][0][1], cA[1][4][1][0], cA[1][4][1][1]})) : ); SB();
  asm volatile("v_mfma_f32_16x16x32_bf16 %0, %1, a[136:139], %0" : "+v"(ac[3]) : "v"(((u32x4){cA[1][1][0][0], cA[1][1][0][1], cA[1][1][1][0], cA[1][1][1][1]})) : ); SB();
  asm volatile("v_mfma_f32_16x16x32_bf16 %0, %1, a[136:139], %0" : "+v"(ac[2]) : "v"(((u32x4){cA[1][0][0][0], cA[1][0][0][1], cA[1][0][1][0], cA[1][0][1][1]})) : ); SB();
  asm volatile("s_waitcnt lgkmcnt(6)\n\tv_mfma_f32_16x16x32_bf16 %0, %1, a[136:139], %0" : "+v"(ac[7]) : "v"(((u32x4){cA[1][5][0][0], cA[1][5][0][1], cA[1][5][1][0], cA[1][5][1][1]})) : ); SB();
  asm volatile("s_waitcnt lgkmcnt(5)\n\tv_mfma_f32_16x16x32_bf16 %0, %1, a[132:135], %0" : "+v"(ac[6]) : "v"(((u32x4){cA[1][5][0][0], cA[1][5][0][1], cA[1][5][1][0], cA[1][5][1][1]})) : ); SB();
  asm volatile("buffer_load_dwordx4 v[244:247], %0, %1, %2 offen offset:1024 sc1" :  : "v"(vB), "s"(rWS), "s"(491520u) : "memory"); SB();
  asm volatile("v_mfma_f32_16x16x32_bf16 %0, %1, a[132:135], %0" : "+v"(ac[5]) : "v"(((u32x4){cA[1][4][0][0], cA[1][4][0][1], cA[1][4][1][0], cA[1][4][1][1]})) : ); SB();
  asm volatile("v_mfma_f32_16x16x32_bf16 %0, %1, a[132:135], %0" : "+v"(ac[4]) : "v"(((u32x4){cA[1][3][0][0], cA[1][3][0][1], cA[1][3][1][0], cA[1][3][1][1]})) : ); SB();
  asm volatile("s_waitcnt lgkmcnt(3)\n\tv_mfma_f32_16x16x32_bf16 %0, %1, a[132:135], %0" : "+v"(ac[7]) : "v"(((u32x4){cA[1][6][0][0], cA[1][6][0][1], cA[1][6][1][0], cA[1][6][1][1]})) : ); SB();
  asm volatile("v_mfma_f32_16x16x32_bf16 %0, %1, a[132:135], %0" : "+v"(ac[3]) : "v"(((u32x4){cA[1][2][0][0], cA[1][2][0][1], cA[1][2][1][0], cA[1][2][1][1]})) : ); SB();
  asm volatile("v_mfma_f32_16x16x32_bf16 %0, %1, a[132:135], %0" : "+v"(ac[2]) : "v"(((u32x4){cA[1][1][0][0], cA[1][1][0][1], cA[1][1][1][0], cA[1][1][1][1]})) : ); SB();
  asm volatile("v_mfma_f32_16x16x32_bf16 %0, %1, a[132:135], %0" : "+v"(ac[1]) : "v"(((u32x4){cA[1][0][0][0], cA[1][0][0][1], cA[1][0][1][0], cA[1][0][1][1]})) : ); SB();
  asm volatile("s_waitcnt lgkmcnt(0)\n\tv_mfma_f32_16x16x32_bf16 %0, %1, a[128:131], %0" : "+v"(ac[7]) : "v"(((u32x4){cA[1][7][0][0], cA[1][7][0][1], cA[1][7][1][0], cA[1][7][1][1]})) : ); SB();
  asm volatile("v_mfma_f32_16x16x32_bf16 %0, %1, a[128:131], %0" : "+v"(ac[6]) : "v"(((u32x4){cA[1][6][0][0], cA[1][6][0][1], cA[1][6][1][0], cA[1][6][1][1]})) : ); SB();
  asm volatile("buffer_load_dwordx4 v[248:251], %0, %1, %2 offen offset:0 sc1" :  : "v"(vB), "s"(rWS), "s"(540672u) : "memory"); SB();
  asm volatile("v_mfma_f32_16x16x32_bf16 %0, %1, a[128:131], %0" : "+v"(ac[5]) : "v"(((u32x4){cA[1][5][0][0], cA[1][5][0][1], cA[1][5][1][0], cA[1][5][1][1]})) : ); SB();
  asm volatile("v_mfma_f32_16x16x32_bf16 %0, %1, a[128:131], %0" : "+v"(ac[4]) : "v"(((u32x4){cA[1][4][0][0], cA[1][4][0][1], cA[1][4][1][0], cA[1][4][1][1]})) : ); SB();
  asm volatile("v_mfma_f32_16x16x32_bf16 %0, %1, a[128:131], %0" : "+v"(ac[3]) : "v"(((u32x4){cA[1][3][0][0], cA[1][3][0][1], cA[1][3][1][0], cA[1][3][1][1]})) : ); SB();
  asm volatile("v_mfma_f32_16x16x32_bf16 %0, %1, a[128:131], %0" : "+v"(ac[2]) : "v"(((u32x4){cA[1][2][0][0], cA[1][2][0][1], cA[1][2][1][0], cA[1][2][1][1]})) : ); SB();
  asm volatile("v_mfma_f32_16x16x32_bf16 %0, %1, a[128:131], %0" : "+v"(ac[1]) : "v"(((u32x4){cA[1][1][0][0], cA[1][1][0][1], cA[1][1][1][0], cA[1][1][1][1]})) : ); SB();
  asm volatile("buffer_load_dwordx4 v[252:255], %0, %1, %2 offen offset:1024 sc1" :  : "v"(vB), "s"(rWS), "s"(540672u) : "memory"); SB();
  asm volatile("v_mfma_f32_16x16x32_bf16 %0, %1, a[128:131], %0" : "+v"(ac[0]) : "v"(((u32x4){cA[1][0][0][0], cA[1][0][0][1], cA[1][0][1][0], cA[1][0][1][1]})) : ); SB();
  cu += 2;
  asm volatile("s_waitcnt lgkmcnt(0)" :  :  : "memory"); SB();
  asm volatile("s_nop 7" : "+v"(ac[0]), "+v"(ac[1]), "+v"(ac[2]), "+v"(ac[3]), "+v"(ac[4]), "+v"(ac[5]), "+v"(ac[6]), "+v"(ac[7]) :  : ); SB();
  } else {
  asm volatile("; ELSE_BEGIN"); SB();
  asm volatile("s_waitcnt lgkmcnt(0)" ::: "memory"); SB();
  asm volatile("buffer_load_dwordx4 v[188:191], %0, %1, %2 offen offset:1024 sc1" :  : "v"(vB), "s"(rWS), "s"(147456u) : "memory"); SB();
  asm volatile("s_waitcnt lgkmcnt(0)" ::: "memory"); SB();
  asm volatile("buffer_load_dwordx4 v[192:195], %0, %1, %2 offen offset:0 sc1" :  : "v"(vB), "s"(rWS), "s"(196608u) : "memory"); SB();
  asm volatile("s_waitcnt lgkmcnt(0)" ::: "memory"); SB();
  asm volatile("buffer_load_dwordx4 v[196:199], %0, %1, %2 offen offset:1024 sc1" :  : "v"(vB), "s"(rWS), "s"(196608u) : "memory"); SB();
  asm volatile("s_waitcnt lgkmcnt(0)" ::: "memory"); SB();
  asm volatile("buffer_load_dwordx4 v[200:203], %0, %1, %2 offen offset:0 sc1" :  : "v"(vB), "s"(rWS), "s"(245760u) : "memory"); SB();
  asm volatile("s_waitcnt lgkmcnt(0)" ::: "memory"); SB();
  asm volatile("buffer_load_dwordx4 v[204:207], %0, %1, %2 offen offset:1024 sc1" :  : "v"(vB), "s"(rWS), "s"(245760u) : "memory"); SB();
  asm volatile("s_waitcnt lgkmcnt(0)" ::: "memory"); SB();
  asm volatile("buffer_load_dwordx4 v[208:211], %0, %1, %2 offen offset:0 sc1" :  : "v"(vB), "s"(rWS), "s"(294912u) : "memory"); SB();
  asm volatile("s_waitcnt lgkmcnt(0)" ::: "memory"); SB();
  asm volatile("buffer_load_dwordx4 v[212:215], %0, %1, %2 offen offset:1024 sc1" :  : "v"(vB), "s"(rWS), "s"(294912u) : "memory"); SB();
  asm volatile("s_waitcnt lgkmcnt(0)" ::: "memory"); SB();
  asm volatile("buffer_load_dwordx4 v[216:219], %0, %1, %2 offen offset:0 sc1" :  : "v"(vB), "s"(rWS), "s"(344064u) : "memory"); SB();
  asm volatile("s_waitcnt lgkmcnt(0)" ::: "memory"); SB();
  asm volatile("buffer_load_dwordx4 v[220:223], %0, %1, %2 offen offset:1024 sc1" :  : "v"(vB), "s"(rWS), "s"(344064u) : "memory"); SB();
  asm volatile("s_waitcnt lgkmcnt(0)" ::: "memory"); SB();
  asm volatile("buffer_load_dwordx4 v[224:227], %0, %1, %2 offen offset:0 sc1" :  : "v"(vB), "s"(rWS), "s"(393216u) : "memory"); SB();
  asm volatile("s_waitcnt lgkmcnt(0)" ::: "memory"); SB();
  asm volatile("buffer_load_dwordx4 v[228:231], %0, %1, %2 offen offset:1024 sc1" :  : "v"(vB), "s"(rWS), "s"(393216u) : "memory"); SB();
  asm volatile("s_waitcnt lgkmcnt(0)" ::: "memory"); SB();
  asm volatile("buffer_load_dwordx4 v[232:235], %0, %1, %2 offen offset:0 sc1" :  : "v"(vB), "s"(rWS), "s"(442368u) : "memory"); SB();
  asm volatile("s_waitcnt lgkmcnt(0)" ::: "memory"); SB();
  asm volatile("buffer_load_dwordx4 v[236:239], %0, %1, %2 offen offset:1024 sc1" :  : "v"(vB), "s"(rWS), "s"(442368u) : "memory"); SB();
  asm volatile("s_waitcnt lgkmcnt(0)" ::: "memory"); SB();
  asm volatile("buffer_load_dwordx4 v[240:243], %0, %1, %2 offen offset:0 sc1" :  : "v"(vB), "s"(rWS), "s"(491520u) : "memory"); SB();
  asm volatile("s_waitcnt lgkmcnt(0)" ::: "memory"); SB();
  asm volatile("buffer_load_dwordx4 v[244:247], %0, %1, %2 offen offset:1024 sc1" :  : "v"(vB), "s"(rWS), "s"(491520u) : "memory"); SB();
  asm volatile("s_waitcnt lgkmcnt(0)" ::: "memory"); SB();
  asm volatile("buffer_load_dwordx4 v[248:251], %0, %1, %2 offen offset:0 sc1" :  : "v"(vB), "s"(rWS), "s"(540672u) : "memory"); SB();
  asm volatile("s_waitcnt lgkmcnt(0)" ::: "memory"); SB();
  asm volatile("buffer_load_dwordx4 v[252:255], %0, %1, %2 offen offset:1024 sc1" :  : "v"(vB), "s"(rWS), "s"(540672u) : "memory"); SB();
  asm volatile("s_waitcnt lgkmcnt(0) ; ELSE_END" ::: "memory"); SB();
  }
  ST(15);
  { unsigned lane = lane0, w = w0, rb = rb0, cb = cb0;
  asm volatile("" : "+v"(lane)); asm volatile("" : "+s"(w), "+s"(rb), "+s"(cb)); SB();
  const unsigned wm = w >> 1, wn = w & 1, g4 = lane >> 4, l15 = lane & 15, q4 = l15 >> 2, p4 = l15 & 3;
  const unsigned nch = 3 - (rb >> 4), ch0 = cb * 80 + rb * 2 + min(rb, 16u);
  const unsigned l16 = lane * 16;
  const unsigned vZ = W_Z2 + ((rb * 2 + wm) * 48 + cb * 6 + wn * 3) * 1024 + l16;
  const unsigned vH = W_HH + ((rb * 4 + w) * 24 + cb * 3) * 1024 + l16;
  const unsigned vY = W_YY + ch0 * 4096 + w * 1024 + l16;
  const unsigned c2o = (nch - 2) * 2, c2s = 8192u + ((rb >> 4) << 30), w3s = (w + 1) >> 2 << 30;
  (void)wm; (void)wn; (void)g4; (void)q4; (void)p4; (void)vZ; (void)vH; (void)vY; (void)c2o; (void)c2s; (void)w3s;
  f32x4 g[2][4][2], pr[6], yp[4], bs4;
  unsigned dd = da + (((cu - 1) >> 3) * 2 + ((cu & 7) == 0)) * 8192;
  unsigned vY_ = vY, vH_ = vH, pw2 = lb + 122880 + w * 6144 + l16, pe[3];
#pragma unroll
  for (int i = 0; i < 3; ++i) pe[i] = lb + 122880 + (w ^ (i + 1)) * 6144 + l16;
  unsigned ydr = lb + 0 + (w * 2 + (lane >> 5)) * 1024 + (lane & 1) * 512 + ((((lane >> 1) & 15) ^ ((lane & 1) << 3)) << 4), ykr = lb + 49152 + 512 + w * 1024 + l16;
  u32x4 ykv, yo, ho;
  float gt[4], ge[4], Dv;
  unsigned vtmp;
  unsigned vO = W_WO + (cb * 120 + w * 10) * 1024 + l16, aO = lb + wn * 20480 + l16;
  unsigned vFY = W_FY + cb * 1024 + (rb >> 3) * 256 + (lane & 31) * 8;
  unsigned vYr = W_YY + (cb * 80 + w * 24 + (lane >> 3)) * 4096 + rb * 128 + (((lane & 7) ^ ((lane >> 3) & 6)) << 4);
  unsigned vHr[2] = {W_HH + ((rb * 4 + (w ^ 1)) * 24 + wn * 12) * 1024 + l16, W_HH + ((rb * 4 + w) * 24 + wn * 12) * 1024 + l16};
  const unsigned mO = lb + w * 10240, mY = lb + w * 3072;
  asm volatile("" : "+v"(xd[0]));
  { const float r = __builtin_amdgcn_rcpf(xd[0]); k1[0] = -2.3022082f * r; k2[0] = (-0.10294324f * r) * (r * r); }
  bs4 = (f32x4){lo16(bs), lo16(bs), lo16(bs), lo16(bs)};
  asm volatile("" : "+v"(dd), "+v"(pw2), "+v"(ydr), "+v"(ykr), "+v"(vY_), "+v"(vH_), "+v"(pe[0]), "+v"(pe[1]), "+v"(pe[2]), "+v"(k1[0]), "+v"(k2[0]), "+v"(bs4),
               "+v"(vO), "+v"(aO), "+v"(vFY), "+v"(vYr), "+v"(vHr[0]), "+v"(vHr[1]));
  SB();
  asm volatile("s_waitcnt vmcnt(30)" :  :  : "memory"); SB();
  asm volatile("ds_read_b128 a[96:99], %0 offset:0" :  : "v"(aA[0]) : "memory"); SB();
  asm volatile("ds_read_b128 a[112:115], %0 offset:0" :  : "v"(aA[1]) : "memory"); SB();
  asm volatile("s_waitcnt vmcnt(28)" :  :  : "memory"); SB();
  asm volatile("ds_read_b128 a[100:103], %0 offset:2048" :  : "v"(aA[0]) : "memory"); SB();
  asm volatile("ds_read_b128 a[116:119], %0 offset:2048" :  : "v"(aA[1]) : "memory"); SB();
  asm volatile("s_waitcnt vmcnt(26)" :  :  : "memory"); SB();
  asm volatile("ds_read_b128 a[104:107], %0 offset:4096" :  : "v"(aA[0]) : "memory"); SB();
  asm volatile("ds_read_b128 a[120:123], %0 offset:4096" :  : "v"(aA[1]) : "memory"); SB();
  asm volatile("s_waitcnt vmcnt(24)" :  :  : "memory"); SB();
  asm volatile("ds_read_b128 a[108:111], %0 offset:6144" :  : "v"(aA[0]) : "memory"); SB();
  asm volatile("ds_read_b128 a[124:127], %0 offset:6144" :  : "v"(aA[1]) : "memory"); SB();
  asm volatile("s_add_u32 m0, %0, 105600" :  : "s"(mA) : "memory", "scc"); SB();
  asm volatile("s_nop 0\n\tbuffer_load_dwordx4 %0, %1, %2 offen offset:896 lds" :  : "v"(voS), "s"(rA), "s"(ao[0]) : "memory"); SB();
  asm volatile("s_add_u32 m0, %0, 106624" :  : "s"(mA) : "memory", "scc"); SB();
  asm volatile("s_nop 0\n\tbuffer_load_dwordx4 %0, %1, %2 offen offset:896 lds" :  : "v"(voS), "s"(rA), "s"(ao[1]) : "memory"); SB();
  asm volatile("s_add_u32 m0, %0, 107648" :  : "s"(mA) : "memory", "scc"); SB();
  asm volatile("s_nop 0\n\tbuffer_load_dwordx4 %0, %1, %2 offen offset:896 lds" :  : "v"(voS), "s"(rA), "s"(ao[2]) : "memory"); SB();
  asm volatile("s_add_u32 m0, %0, 108672" :  : "s"(mA) : "memory", "scc"); SB();
  asm volatile("s_nop 0\n\tbuffer_load_dwordx4 %0, %1, %2 offen offset:896 lds" :  : "v"(voS), "s"(rA), "s"(ao[3]) : "memory"); SB();
  asm volatile("s_add_u32 m0, %0, 109696" :  : "s"(mA) : "memory", "scc"); SB();
  asm volatile("s_nop 0\n\tbuffer_load_dwordx4 %0, %1, %2 offen offset:896 lds" :  : "v"(voS), "s"(rA), "s"(ao[4]) : "memory"); SB();
  ST(16);
  asm volatile("s_waitcnt vmcnt(27)" :  :  : "memory"); SB();
  asm volatile("v_mfma_f32_16x16x32_bf16 %0, v[160:163], a[160:163], %1" : "=&v"(g[0][0][0]) : "v"(bs4) : ); SB();
  asm volatile("s_barrier" :  :  : "memory"); SB();
  asm volatile("s_add_u32 m0, %0, 110720" :  : "s"(mA) : "memory", "scc"); SB();
  asm volatile("v_mul_f32 %0, %1, %1" : "=v"(gt[0]) : "v"(a1[0][0][0]) : ); SB();
  asm volatile("v_mfma_f32_16x16x32_bf16 %0, v[160:163], a[164:167], 0" : "=&v"(g[0][1][0]) :  : ); SB();
  asm volatile("ds_write_b128 %0, %1 offset:0" :  : "v"(dd), "v"(ac[0]) : "memory"); SB();
  asm volatile("v_mfma_f32_16x16x32_bf16 %0, v[160:163], a[168:171], 0" : "=&v"(g[0][2][0]) :  : ); SB();
  asm volatile("buffer_load_dwordx4 %0, %1, %2 offen offset:896 lds" :  : "v"(voS), "s"(rA), "s"(ao[5]) : "memory"); SB();
  asm volatile("s_add_u32 m0, %0, 111744" :  : "s"(mA) : "memory", "scc"); SB();
  asm volatile("v_mfma_f32_16x16x32_bf16 %0, v[160:163], a[172:175], 0" : "=&v"(g[0][3][0]) :  : ); SB();
  asm volatile("buffer_load_dwordx4 %0, %1, %2 offen offset:896 lds" :  : "v"(voS), "s"(rA), "s"(ao[6]) : "memory"); SB();
  asm volatile("v_mul_f32 %0, %1, %1" : "=v"(gt[1]) : "v"(a1[0][0][1]) : ); SB();
  asm volatile("v_mfma_f32_16x16x32_bf16 %0, v[164:167], a[160:163], %1" : "=&v"(g[0][0][1]) : "v"(bs4) : ); SB();
  asm volatile("ds_write_b128 %0, %1 offset:1024" :  : "v"(dd), "v"(ac[1]) : "memory"); SB();
  asm volatile("v_mfma_f32_16x16x32_bf16 %0, v[164:167], a[164:167], 0" : "=&v"(g[0][1][1]) :  : ); SB();
  asm volatile("v_mul_f32 %0, %1, %1" : "=v"(gt[2]) : "v"(a1[0][0][2]) : ); SB();
  asm volatile("v_mfma_f32_16x16x32_bf16 %0, v[164:167], a[168:171], 0" : "=&v"(g[0][2][1]) :  : ); SB();
  asm volatile("s_add_u32 m0, %0, 112768" :  : "s"(mA) : "memory", "scc"); SB();
  asm volatile("v_mul_f32 %0, %1, %1" : "=v"(gt[3]) : "v"(a1[0][0][3]) : ); SB();
  asm volatile("v_mfma_f32_16x16x32_bf16 %0, v[164:167], a[172:175], 0" : "=&v"(g[0][3][1]) :  : ); SB();
  asm volatile("ds_write_b128 %0, %1 offset:2048" :  : "v"(dd), "v"(ac[2]) : "memory"); SB();
  asm volatile("s_waitcnt vmcnt(27)" :  :  : "memory"); SB();
  asm volatile("v_mfma_f32_16x16x32_bf16 %0, v[168:171], a[176:179], %0" : "+v"(g[0][0][0]) :  : ); SB();
  asm volatile("buffer_load_dwordx4 %0, %1, %2 offen offset:896 lds" :  : "v"(voS), "s"(rA), "s"(ao[7]) : "memory"); SB();
  asm volatile("v_fma_f32 %0, %0, %1, %2" : "+v"(gt[0]) : "v"(k2[0]), "v"(k1[0]) : ); SB();
  asm volatile("v_mfma_f32_16x16x32_bf16 %0, v[168:171], a[180:183], %0" : "+v"(g[0][1][0]) :  : ); SB();
  asm volatile("v_fma_f32 %0, %0, %1, %2" : "+v"(gt[1]) : "v"(k2[0]), "v"(k1[0]) : ); SB();
  asm volatile("v_mfma_f32_16x16x32_bf16 %0, v[168:171], a[184:187], %0" : "+v"(g[0][2][0]) :  : ); SB();
  asm volatile("ds_write_b128 %0, %1 offset:3072" :  : "v"(dd), "v"(ac[3]) : "memory"); SB();
  asm volatile("v_mfma_f32_16x16x32_bf16 %0, v[168:171], a[188:191], %0" : "+v"(g[0][3][0]) :  : ); SB();
  asm volatile("buffer_load_dwordx4 v[160:163], %0, %1, %2 offen offset:0 sc1" :  : "v"(vB), "s"(rWS), "s"(589824u) : "memory"); SB();
  asm volatile("v_fma_f32 %0, %0, %1, %2" : "+v"(gt[2]) : "v"(k2[0]), "v"(k1[0]) : ); SB();
  asm volatile("v_mfma_f32_16x16x32_bf16 %0, v[172:175], a[176:179], %0" : "+v"(g[0][0][1]) :  : ); SB();
  asm volatile("v_fma_f32 %0, %0, %1, %2" : "+v"(gt[3]) : "v"(k2[0]), "v"(k1[0]) : ); SB();
  asm volatile("v_mfma_f32_16x16x32_bf16 %0, v[172:175], a[180:183], %0" : "+v"(g[0][1][1]) :  : ); SB();
  asm volatile("ds_write_b128 %0, %1 offset:4096" :  : "v"(dd), "v"(ac[4]) : "memory"); SB();
  asm volatile("v_mfma_f32_16x16x32_bf16 %0, v[172:175], a[184:187], %0" : "+v"(g[0][2][1]) :  : ); SB();
  asm volatile("buffer_load_dwordx4 v[164:167], %0, %1, %2 offen offset:1024 sc1" :  : "v"(vB), "s"(rWS), "s"(589824u) : "memory"); SB();
  asm volatile("v_mul_f32 %0, %0, %1" : "+v"(gt[0]) : "v"(a1[0][0][0]) : ); SB();
  asm volatile("v_mfma_f32_16x16x32_bf16 %0, v[172:175], a[188:191], %0" : "+v"(g[0][3][1]) :  : ); SB();
  asm volatile("v_mul_f32 %0, %0, %1" : "+v"(gt[1]) : "v"(a1[0][0][1]) : ); SB();
  asm volatile("s_waitcnt vmcnt(28)" :  :  : "memory"); SB();
  asm volatile("v_mfma_f32_16x16x32_bf16 %0, v[176:179], a[192:195], %0" : "+v"(g[0][0][0]) :  : ); SB();
  asm volatile("ds_write_b128 %0, %1 offset:5120" :  : "v"(dd), "v"(ac[5]) : "memory"); SB();
  asm volatile("v_mfma_f32_16x16x32_bf16 %0, v[176:179], a[196:199], %0" : "+v"(g[0][1][0]) :  : ); SB();
  asm volatile("buffer_load_dwordx4 v[168:171], %0, %1, %2 offen offset:0 sc1" :  : "v"(vB), "s"(rWS), "s"(638976u) : "memory"); SB();
  asm volatile("v_mul_f32 %0, %0, %1" : "+v"(gt[2]) : "v"(a1[0][0][2]) : ); SB();
  asm volatile("v_mfma_f32_16x16x32_bf16 %0, v[176:179], a[200:203], %0" : "+v"(g[0][2][0]) :  : ); SB();
  asm volatile("v_mul_f32 %0, %0, %1" : "+v"(gt[3]) : "v"(a1[0][0][3]) : ); SB();
  asm volatile("v_mfma_f32_16x16x32_bf16 %0, v[176:179], a[204:207], %0" : "+v"(g[0][3][0]) :  : ); SB();
  asm volatile("ds_write_b128 %0, %1 offset:6144" :  : "v"(dd), "v"(ac[6]) : "memory"); SB();
  asm volatile("v_mfma_f32_16x16x32_bf16 %0, v[180:183], a[192:195], %0" : "+v"(g[0][0][1]) :  : ); SB();
  asm volatile("buffer_load_dwordx4 v[172:175], %0, %1, %2 offen offset:1024 sc1" :  : "v"(vB), "s"(rWS), "s"(638976u) : "memory"); SB();
  asm volatile("v_mfma_f32_16x16x32_bf16 %0, v[180:183], a[196:199], %0" : "+v"(g[0][1][1]) :  : ); SB();
  asm volatile("v_exp_f32 %0, %1" : "=v"(ge[0]) : "v"(gt[0]) : ); SB();
  asm volatile("v_mfma_f32_16x16x32_bf16 %0, v[180:183], a[200:203], %0" : "+v"(g[0][2][1]) :  : ); SB();
  asm volatile("ds_write_b128 %0, %1 offset:7168" :  : "v"(dd), "v"(ac[7]) : "memory"); SB();
  asm volatile("v_mfma_f32_16x16x32_bf16 %0, v[180:183], a[204:207], %0" : "+v"(g[0][3][1]) :  : ); SB();
  asm volatile("v_exp_f32 %0, %1" : "=v"(ge[1]) : "v"(gt[1]) : ); SB();
  asm volatile("s_waitcnt vmcnt(28)" :  :  : "memory"); SB();
  asm volatile("v_mfma_f32_16x16x32_bf16 %0, v[184:187], a[208:211], %0" : "+v"(g[0][0][0]) :  : ); SB();
  asm volatile("buffer_load_dwordx4 v[176:179], %0, %1, %2 offen offset:0 sc1" :  : "v"(vB), "s"(rWS), "s"(688128u) : "memory"); SB();
  asm volatile("v_mfma_f32_16x16x32_bf16 %0, v[184:187], a[212:215], %0" : "+v"(g[0][1][0]) :  : ); SB();
  asm volatile("v_exp_f32 %0, %1" : "=v"(ge[2]) : "v"(gt[2]) : ); SB();
  asm volatile("v_mfma_f32_16x16x32_bf16 %0, v[184:187], a[216:219], %0" : "+v"(g[0][2][0]) :  : ); SB();
  asm volatile("s_waitcnt lgkmcnt(0)\n\ts_barrier" :  :  : "memory"); SB();
  asm volatile("ds_read_b128 %0, %1 offset:0" : "=&v"(yp[0]) : "v"(ydr) : "memory"); SB();
  asm volatile("v_mfma_f32_16x16x32_bf16 %0, v[184:187], a[220:223], %0" : "+v"(g[0][3][0]) :  : ); SB();
  asm volatile("ds_read_b128 %0, %1 offset:256" : "=&v"(yp[1]) : "v"(ydr) : "memory"); SB();
  asm volatile("v_mfma_f32_16x16x32_bf16 %0, v[188:191], a[208:211], %0" : "+v"(g[0][0][1]) :  : ); SB();
  asm volatile("ds_read_b128 %0, %1 offset:8192" : "=&v"(yp[2]) : "v"(ydr) : "memory"); SB();
  asm volatile("v_mfma_f32_16x16x32_bf16 %0, v[188:191], a[212:215], %0" : "+v"(g[0][1][1]) :  : ); SB();
  asm volatile("ds_read_b128 %0, %1 offset:8448" : "=&v"(yp[3]) : "v"(ydr) : "memory"); SB();
  asm volatile("v_mfma_f32_16x16x32_bf16 %0, v[188:191], a[216:219], %0" : "+v"(g[0][2][1]) :  : ); SB();
  asm volatile("ds_read_b128 %0, %1 offset:0" : "=&v"(ykv) : "v"(ykr) : "memory"); SB();
  asm volatile("v_lshlrev_b32 %0, 16, %1" : "=v"(Dv) : "v"(dD[0]) : ); SB();
  asm volatile("v_mfma_f32_16x16x32_bf16 %0, v[188:191], a[220:223], %0" : "+v"(g[0][3][1]) :  : ); SB();
  asm volatile("buffer_load_dwordx4 v[180:183], %0, %1, %2 offen offset:1024 sc1" :  : "v"(vB), "s"(rWS), "s"(688128u) : "memory"); SB();
  asm volatile("s_waitcnt vmcnt(28)" :  :  : "memory"); SB();
  asm volatile("v_mfma_f32_16x16x32_bf16 %0, v[192:195], a[224:227], %0" : "+v"(g[0][0][0]) :  : ); SB();
  asm volatile("v_exp_f32 %0, %1" : "=v"(ge[3]) : "v"(gt[3]) : ); SB();
  asm volatile("v_mfma_f32_16x16x32_bf16 %0, v[192:195], a[228:231], %0" : "+v"(g[0][1][0]) :  : ); SB();
  asm volatile("v_fma_f32 %0, %0, %1, %1" : "+v"(ge[0]) : "v"(xd[0]) : ); SB();
  asm volatile("v_mfma_f32_16x16x32_bf16 %0, v[192:195], a[232:235], %0" : "+v"(g[0][2][0]) :  : ); SB();
  asm volatile("buffer_load_dwordx4 v[184:187], %0, %1, %2 offen offset:0 sc1" :  : "v"(vB), "s"(rWS), "s"(737280u) : "memory"); SB();
  asm volatile("v_fma_f32 %0, %0, %1, %1" : "+v"(ge[1]) : "v"(xd[0]) : ); SB();
  asm volatile("v_mfma_f32_16x16x32_bf16 %0, v[192:195], a[236:239], %0" : "+v"(g[0][3][0]) :  : ); SB();
  asm volatile("s_waitcnt lgkmcnt(0)" : "+v"(yp[0]), "+v"(yp[1]), "+v"(yp[2]), "+v"(yp[3]), "+v"(ykv) :  : "memory"); SB();
  asm volatile("s_waitcnt vmcnt(13)" :  :  : "memory"); SB();
  asm volatile("v_add_f32 %0, %0, %1" : "+v"(yp[0][0]) : "v"(yp[2][0]) : ); SB();
  asm volatile("v_mfma_f32_16x16x32_bf16 %0, v[196:199], a[224:227], %0" : "+v"(g[0][0][1]) :  : ); SB();
  asm volatile("ds_read_b128 a[128:131], %0 offset:8192" :  : "v"(aA[0]) : "memory"); SB();
  asm volatile("v_add_f32 %0, %0, %1" : "+v"(yp[0][1]) : "v"(yp[2][1]) : ); SB();
  asm volatile("v_mfma_f32_16x16x32_bf16 %0, v[196:199], a[228:231], %0" : "+v"(g[0][1][1]) :  : ); SB();
  asm volatile("ds_read_b128 a[144:147], %0 offset:8192" :  : "v"(aA[1]) : "memory"); SB();
  asm volatile("v_add_f32 %0, %0, %1" : "+v"(yp[0][2]) : "v"(yp[2][2]) : ); SB();
  asm volatile("v_mfma_f32_16x16x32_bf16 %0, v[196:199], a[232:235], %0" : "+v"(g[0][2][1]) :  : ); SB();
  asm volatile("buffer_load_dwordx4 v[188:191], %0, %1, %2 offen offset:1024 sc1" :  : "v"(vB), "s"(rWS), "s"(737280u) : "memory"); SB();
  asm volatile("v_add_f32 %0, %0, %1" : "+v"(yp[0][3]) : "v"(yp[2][3]) : ); SB();
  asm volatile("v_mfma_f32_16x16x32_bf16 %0, v[196:199], a[236:239], %0" : "+v"(g[0][3][1]) :  : ); SB();
  asm volatile("v_add_f32 %0, %0, %1" : "+v"(yp[1][0]) : "v"(yp[3][0]) : ); SB();
  asm volatile("v_mfma_f32_16x16x32_bf16 %0, v[200:203], a[240:243], %0" : "+v"(g[0][0][0]) :  : ); SB();
  asm volatile("buffer_load_dwordx4 v[192:195], %0, %1, %2 offen offset:0 sc1" :  : "v"(vB), "s"(rWS), "s"(2048u) : "memory"); SB();
  asm volatile("v_add_f32 %0, %0, %1" : "+v"(yp[1][1]) : "v"(yp[3][1]) : ); SB();
  asm volatile("v_mfma_f32_16x16x32_bf16 %0, v[200:203], a[244:247], %0" : "+v"(g[0][1][0]) :  : ); SB();
  asm volatile("v_add_f32 %0, %0, %1" : "+v"(yp[1][2]) : "v"(yp[3][2]) : ); SB();
  asm volatile("v_mfma_f32_16x16x32_bf16 %0, v[200:203], a[248:251], %0" : "+v"(g[0][2][0]) :  : ); SB();
  asm volatile("v_add_f32 %0, %0, %1" : "+v"(yp[1][3]) : "v"(yp[3][3]) : ); SB();
  asm volatile("v_mfma_f32_16x16x32_bf16 %0, v[200:203], a[252:255], %0" : "+v"(g[0][3][0]) :  : ); SB();
  asm volatile("v_lshlrev_b32 %0, 16, %1" : "=v"(yp[2][0]) : "v"(ykv[0]) : ); SB();
  asm volatile("v_mfma_f32_16x16x32_bf16 %0, v[204:207], a[240:243], %0" : "+v"(g[0][0][1]) :  : ); SB();
  asm volatile("s_waitcnt vmcnt(13)" :  :  : "memory"); SB();
  asm volatile("ds_read_b128 a[132:135], %0 offset:10240" :  : "v"(aA[0]) : "memory"); SB();
  asm volatile("v_mfma_f32_16x16x32_bf16 %0, v[204:207], a[244:247], %0" : "+v"(g[0][1][1]) :  : ); SB();
  asm volatile("ds_read_b128 a[148:151], %0 offset:10240" :  : "v"(aA[1]) : "memory"); SB();
  asm volatile("v_and_b32 %0, 0xffff0000, %1" : "=v"(yp[2][1]) : "v"(ykv[0]) : ); SB();
  asm volatile("v_mfma_f32_16x16x32_bf16 %0, v[204:207], a[248:251], %0" : "+v"(g[0][2][1]) :  : ); SB();
  asm volatile("buffer_load_dwordx4 v[196:199], %0, %1, %2 offen offset:1024 sc1" :  : "v"(vB), "s"(rWS), "s"(2048u) : "memory"); SB();
  asm volatile("v_lshlrev_b32 %0, 16, %1" : "=v"(yp[2][2]) : "v"(ykv[1]) : ); SB();
  asm volatile("v_mfma_f32_16x16x32_bf16 %0, v[204:207], a[252:255], %0" : "+v"(g[0][3][1]) :  : ); SB();
  asm volatile("v_and_b32 %0, 0xffff0000, %1" : "=v"(yp[2][3]) : "v"(ykv[1]) : ); SB();
  ST(25);
  asm volatile("v_mfma_f32_16x16x32_bf16 %0, v[208:211], a[0:3], %0" : "+v"(g[0][0][0]) :  : ); SB();
  asm volatile("v_lshlrev_b32 %0, 16, %1" : "=v"(yp[3][0]) : "v"(ykv[2]) : ); SB();
  asm volatile("v_mfma_f32_16x16x32_bf16 %0, v[208:211], a[4:7], %0" : "+v"(g[0][1][0]) :  : ); SB();
  asm volatile("buffer_load_dwordx4 v[200:203], %0, %1, %2 offen offset:0 sc1" :  : "v"(vB), "s"(rWS), "s"(51200u) : "memory"); SB();
  asm volatile("v_and_b32 %0, 0xffff0000, %1" : "=v"(yp[3][1]) : "v"(ykv[2]) : ); SB();
  asm volatile("v_mfma_f32_16x16x32_bf16 %0, v[208:211], a[8:11], %0" : "+v"(g[0][2][0]) :  : ); SB();
  asm volatile("v_lshlrev_b32 %0, 16, %1" : "=v"(yp[3][2]) : "v"(ykv[3]) : ); SB();
  asm volatile("v_mfma_f32_16x16x32_bf16 %0, v[208:211], a[12:15], %0" : "+v"(g[0][3][0]) :  : ); SB();
  asm volatile("v_and_b32 %0, 0xffff0000, %1" : "=v"(yp[3][3]) : "v"(ykv[3]) : ); SB();
  asm volatile("v_mfma_f32_16x16x32_bf16 %0, v[212:215], a[0:3], %0" : "+v"(g[0][0][1]) :  : ); SB();
  asm volatile("v_fmac_f32 %0, %1, %2" : "+v"(yp[0][0]) : "v"(yp[2][0]), "v"(Dv) : ); SB();
  asm volatile("v_mfma_f32_16x16x32_bf16 %0, v[212:215], a[4:7], %0" : "+v"(g[0][1][1]) :  : ); SB();
  asm volatile("v_fmac_f32 %0, %1, %2" : "+v"(yp[0][1]) : "v"(yp[2][1]), "v"(Dv) : ); SB();
  asm volatile("v_mfma_f32_16x16x32_bf16 %0, v[212:215], a[8:11], %0" : "+v"(g[0][2][1]) :  : ); SB();
  asm volatile("s_waitcnt vmcnt(13)" :  :  : "memory"); SB();
  asm volatile("ds_read_b128 a[136:139], %0 offset:12288" :  : "v"(aA[0]) : "memory"); SB();
  asm volatile("v_mfma_f32_16x16x32_bf16 %0, v[212:215], a[12:15], %0" : "+v"(g[0][3][1]) :  : ); SB();
  asm volatile("ds_read_b128 a[152:155], %0 offset:12288" :  : "v"(aA[1]) : "memory"); SB();
  asm volatile("v_mfma_f32_16x16x32_bf16 %0, v[216:219], a[16:19], %0" : "+v"(g[0][0][0]) :  : ); SB();
  asm volatile("buffer_load_dwordx4 v[204:207], %0, %1, %2 offen offset:1024 sc1" :  : "v"(vB), "s"(rWS), "s"(51200u) : "memory"); SB();
  asm volatile("v_fmac_f32 %0, %1, %2" : "+v"(yp[0][2]) : "v"(yp[2][2]), "v"(Dv) : ); SB();
  asm volatile("v_mfma_f32_16x16x32_bf16 %0, v[216:219], a[20:23], %0" : "+v"(g[0][1][0]) :  : ); SB();
  asm volatile("v_fmac_f32 %0, %1, %2" : "+v"(yp[0][3]) : "v"(yp[2][3]), "v"(Dv) : ); SB();
  asm volatile("v_mfma_f32_16x16x32_bf16 %0, v[216:219], a[24:27], %0" : "+v"(g[0][2][0]) :  : ); SB();
  asm volatile("buffer_load_dwordx4 v[208:211], %0, %1, %2 offen offset:0 sc1" :  : "v"(vB), "s"(rWS), "s"(100352u) : "memory"); SB();
  asm volatile("v_fmac_f32 %0, %1, %2" : "+v"(yp[1][0]) : "v"(yp[3][0]), "v"(Dv) : ); SB();
  asm volatile("v_mfma_f32_16x16x32_bf16 %0, v[216:219], a[28:31], %0" : "+v"(g[0][3][0]) :  : ); SB();
  asm volatile("v_fmac_f32 %0, %1, %2" : "+v"(yp[1][1]) : "v"(yp[3][1]), "v"(Dv) : ); SB();
  asm volatile("v_mfma_f32_16x16x32_bf16 %0, v[220:223], a[16:19], %0" : "+v"(g[0][0][1]) :  : ); SB();
  asm volatile("v_fmac_f32 %0, %1, %2" : "+v"(yp[1][2]) : "v"(yp[3][2]), "v"(Dv) : ); SB();
  asm volatile("v_mfma_f32_16x16x32_bf16 %0, v[220:223], a[20:23], %0" : "+v"(g[0][1][1]) :  : ); SB();
  asm volatile("v_fmac_f32 %0, %1, %2" : "+v"(yp[1][3]) : "v"(yp[3][3]), "v"(Dv) : ); SB();
  asm volatile("v_mfma_f32_16x16x32_bf16 %0, v[220:223], a[24:27], %0" : "+v"(g[0][2][1]) :  : ); SB();
  asm volatile("v_cvt_pk_bf16_f32 %0, %1, %2" : "=v"(yo[0]) : "v"(yp[0][0]), "v"(yp[0][1]) : ); SB();
  asm volatile("v_mfma_f32_16x16x32_bf16 %0, v[220:223], a[28:31], %0" : "+v"(g[0][3][1]) :  : ); SB();
  asm volatile("s_waitcnt vmcnt(13)" :  :  : "memory"); SB();
  asm volatile("v_cvt_pk_bf16_f32 %0, %1, %2" : "=v"(yo[1]) : "v"(yp[0][2]), "v"(yp[0][3]) : ); SB();
  asm volatile("v_mfma_f32_16x16x32_bf16 %0, v[224:227], a[32:35], %0" : "+v"(g[0][0][0]) :  : ); SB();
  asm volatile("ds_read_b128 a[140:143], %0 offset:14336" :  : "v"(aA[0]) : "memory"); SB();
  asm volatile("v_cvt_pk_bf16_f32 %0, %1, %2" : "=v"(yo[2]) : "v"(yp[1][0]), "v"(yp[1][1]) : ); SB();
  asm volatile("v_mfma_f32_16x16x32_bf16 %0, v[224:227], a[36:39], %0" : "+v"(g[0][1][0]) :  : ); SB();
  asm volatile("ds_read_b128 a[156:159], %0 offset:14336" :  : "v"(aA[1]) : "memory"); SB();
  asm volatile("v_cvt_pk_bf16_f32 %0, %1, %2" : "=v"(yo[3]) : "v"(yp[1][2]), "v"(yp[1][3]) : ); SB();
  asm volatile("v_mfma_f32_16x16x32_bf16 %0, v[224:227], a[40:43], %0" : "+v"(g[0][2][0]) :  : ); SB();
  asm volatile("buffer_store_dwordx4 %1, %0, %2, %3 offen offset:0 sc1\n\ts_nop 1" :  : "v"(vY_), "v"(yo), "s"(rWS), "s"(0u) : "memory"); SB();
  asm volatile("v_mfma_f32_16x16x32_bf16 %0, v[224:227], a[44:47], %0" : "+v"(g[0][3][0]) :  : ); SB();
  asm volatile("ds_read_b128 %0, %1 offset:16384" : "=&v"(yp[0]) : "v"(ydr) : "memory"); SB();
  asm volatile("v_fma_f32 %0, %0, %1, %1" : "+v"(ge[2]) : "v"(xd[0]) : ); SB();
  asm volatile("v_mfma_f32_16x16x32_bf16 %0, v[228:231], a[32:35], %0" : "+v"(g[0][0][1]) :  : ); SB();
  asm volatile("ds_read_b128 %0, %1 offset:16640" : "=&v"(yp[1]) : "v"(ydr) : "memory"); SB();
  asm volatile("v_fma_f32 %0, %0, %1, %1" : "+v"(ge[3]) : "v"(xd[0]) : ); SB();
  asm volatile("v_mfma_f32_16x16x32_bf16 %0, v[228:231], a[36:39], %0" : "+v"(g[0][1][1]) :  : ); SB();
  asm volatile("ds_read_b128 %0, %1 offset:24576" : "=&v"(yp[2]) : "v"(ydr) : "memory"); SB();
  asm volatile("v_mfma_f32_16x16x32_bf16 %0, v[228:231], a[40:43], %0" : "+v"(g[0][2][1]) :  : ); SB();
  asm volatile("ds_read_b128 %0, %1 offset:24832" : "=&v"(yp[3]) : "v"(ydr) : "memory"); SB();
  asm volatile("v_mfma_f32_16x16x32_bf16 %0, v[228:231], a[44:47], %0" : "+v"(g[0][3][1]) :  : ); SB();
  asm volatile("ds_read_b128 %0, %1 offset:12928" : "=&v"(ykv) : "v"(ykr) : "memory"); SB();
  asm volatile("v_mfma_f32_16x16x32_bf16 %0, v[232:235], a[48:51], %0" : "+v"(g[0][0][0]) :  : ); SB();
  asm volatile("buffer_load_dwordx4 v[212:215], %0, %1, %2 offen offset:1024 sc1" :  : "v"(vB), "s"(rWS), "s"(100352u) : "memory"); SB();
  asm volatile("v_lshlrev_b32 %0, 16, %1" : "=v"(Dv) : "v"(dD[1]) : ); SB();
  asm volatile("v_mfma_f32_16x16x32_bf16 %0, v[232:235], a[52:55], %0" : "+v"(g[0][1][0]) :  : ); SB();
  asm volatile("buffer_load_dwordx4 v[216:219], %0, %1, %2 offen offset:0 sc1" :  : "v"(vB), "s"(rWS), "s"(149504u) : "memory"); SB();
  asm volatile("v_mfma_f32_16x16x32_bf16 %0, v[232:235], a[56:59], %0" : "+v"(g[0][2][0]) :  : ); SB();
  asm volatile("v_rcp_f32 %0, %1" : "=v"(gt[0]) : "v"(ge[0]) : ); SB();
  asm volatile("v_mfma_f32_16x16x32_bf16 %0, v[232:235], a[60:63], %0" : "+v"(g[0][3][0]) :  : ); SB();
  asm volatile("v_rcp_f32 %0, %1" : "=v"(gt[1]) : "v"(ge[1]) : ); SB();
  asm volatile("v_mfma_f32_16x16x32_bf16 %0, v[236:239], a[48:51], %0" : "+v"(g[0][0][1]) :  : ); SB();
  asm volatile("v_rcp_f32 %0, %1" : "=v"(gt[2]) : "v"(ge[2]) : ); SB();
  asm volatile("v_mfma_f32_16x16x32_bf16 %0, v[236:239], a[52:55], %0" : "+v"(g[0][1][1]) :  : ); SB();
  asm volatile("s_waitcnt lgkmcnt(0)" : "+v"(yp[0]), "+v"(yp[1]), "+v"(yp[2]), "+v"(yp[3]), "+v"(ykv) :  : "memory"); SB();
  asm volatile("buffer_load_dwordx4 v[220:223], %0, %1, %2 offen offset:1024 sc1" :  : "v"(vB), "s"(rWS), "s"(149504u) : "memory"); SB();
  asm volatile("v_mfma_f32_16x16x32_bf16 %0, v[236:239], a[56:59], %0" : "+v"(g[0][2][1]) :  : ); SB();
  asm volatile("v_add_f32 %0, %0, %1" : "+v"(yp[0][0]) : "v"(yp[2][0]) : ); SB();
  asm volatile("v_mfma_f32_16x16x32_bf16 %0, v[236:239], a[60:63], %0" : "+v"(g[0][3][1]) :  : ); SB();
  asm volatile("v_add_f32 %0, %0, %1" : "+v"(yp[0][1]) : "v"(yp[2][1]) : ); SB();
  asm volatile("v_mfma_f32_16x16x32_bf16 %0, v[240:243], a[64:67], %0" : "+v"(g[0][0][0]) :  : ); SB();
  asm volatile("buffer_load_dwordx4 v[224:227], %0, %1, %2 offen offset:0 sc1" :  : "v"(vB), "s"(rWS), "s"(198656u) : "memory"); SB();
  asm volatile("v_add_f32 %0, %0, %1" : "+v"(yp[0][2]) : "v"(yp[2][2]) : ); SB();
  asm volatile("v_mfma_f32_16x16x32_bf16 %0, v[240:243], a[68:71], %0" : "+v"(g[0][1][0]) :  : ); SB();
  asm volatile("v_add_f32 %0, %0, %1" : "+v"(yp[0][3]) : "v"(yp[2][3]) : ); SB();
  asm volatile("v_mfma_f32_16x16x32_bf16 %0, v[240:243], a[72:75], %0" : "+v"(g[0][2][0]) :  : ); SB();
  asm volatile("v_add_f32 %0, %0, %1" : "+v"(yp[1][0]) : "v"(yp[3][0]) : ); SB();
  asm volatile("v_mfma_f32_16x16x32_bf16 %0, v[240:243], a[76:79], %0" : "+v"(g[0][3][0]) :  : ); SB();
  asm volatile("v_add_f32 %0, %0, %1" : "+v"(yp[1][1]) : "v"(yp[3][1]) : ); SB();
  asm volatile("v_mfma_f32_16x16x32_bf16 %0, v[244:247], a[64:67], %0" : "+v"(g[0][0][1]) :  : ); SB();
  asm volatile("buffer_load_dwordx4 v[228:231], %0, %1, %2 offen offset:1024 sc1" :  : "v"(vB), "s"(rWS), "s"(198656u) : "memory"); SB();
  asm volatile("v_add_f32 %0, %0, %1" : "+v"(yp[1][2]) : "v"(yp[3][2]) : ); SB();
  asm volatile("v_mfma_f32_16x16x32_bf16 %0, v[244:247], a[68:71], %0" : "+v"(g[0][1][1]) :  : ); SB();
  asm volatile("v_add_f32 %0, %0, %1" : "+v"(yp[1][3]) : "v"(yp[3][3]) : ); SB();
  asm volatile("v_mfma_f32_16x16x32_bf16 %0, v[244:247], a[72:75], %0" : "+v"(g[0][2][1]) :  : ); SB();
  asm volatile("v_lshlrev_b32 %0, 16, %1" : "=v"(yp[2][0]) : "v"(ykv[0]) : ); SB();
  asm volatile("v_mfma_f32_16x16x32_bf16 %0, v[244:247], a[76:79], %0" : "+v"(g[0][3][1]) :  : ); SB();
  asm volatile("v_and_b32 %0, 0xffff0000, %1" : "=v"(yp[2][1]) : "v"(ykv[0]) : ); SB();
  asm volatile("v_mfma_f32_16x16x32_bf16 %0, v[248:251], a[80:83], %0" : "+v"(g[0][0][0]) :  : ); SB();
  asm volatile("v_lshlrev_b32 %0, 16, %1" : "=v"(yp[2][2]) : "v"(ykv[1]) : ); SB();
  asm volatile("v_mfma_f32_16x16x32_bf16 %0, v[248:251], a[84:87], %0" : "+v"(g[0][1][0]) :  : ); SB();
  asm volatile("buffer_load_dwordx4 v[232:235], %0, %1, %2 offen offset:0 sc1" :  : "v"(vB), "s"(rWS), "s"(247808u) : "memory"); SB();
  asm volatile("v_and_b32 %0, 0xffff0000, %1" : "=v"(yp[2][3]) : "v"(ykv[1]) : ); SB();
  asm volatile("v_mfma_f32_16x16x32_bf16 %0, v[248:251], a[88:91], %0" : "+v"(g[0][2][0]) :  : ); SB();
  asm volatile("v_lshlrev_b32 %0, 16, %1" : "=v"(yp[3][0]) : "v"(ykv[2]) : ); SB();
  asm volatile("v_mfma_f32_16x16x32_bf16 %0, v[248:251], a[92:95], %0" : "+v"(g[0][3][0]) :  : ); SB();
  asm volatile("v_and_b32 %0, 0xffff0000, %1" : "=v"(yp[3][1]) : "v"(ykv[2]) : ); SB();
  asm volatile("v_mfma_f32_16x16x32_bf16 %0, v[252:255], a[80:83], %0" : "+v"(g[0][0][1]) :  : ); SB();
  asm volatile("v_lshlrev_b32 %0, 16, %1" : "=v"(yp[3][2]) : "v"(ykv[3]) : ); SB();
  asm volatile("v_mfma_f32_16x16x32_bf16 %0, v[252:255], a[84:87], %0" : "+v"(g[0][1][1]) :  : ); SB();
  asm volatile("v_and_b32 %0, 0xffff0000, %1" : "=v"(yp[3][3]) : "v"(ykv[3]) : ); SB();
  asm volatile("v_mfma_f32_16x16x32_bf16 %0, v[252:255], a[88:91], %0" : "+v"(g[0][2][1]) :  : ); SB();
  asm volatile("buffer_load_dwordx4 v[236:239], %0, %1, %2 offen offset:1024 sc1" :  : "v"(vB), "s"(rWS), "s"(247808u) : "memory"); SB();
  asm volatile("v_fmac_f32 %0, %1, %2" : "+v"(yp[0][0]) : "v"(yp[2][0]), "v"(Dv) : ); SB();
  asm volatile("v_mfma_f32_16x16x32_bf16 %0, v[252:255], a[92:95], %0" : "+v"(g[0][3][1]) :  : ); SB();
  asm volatile("v_fmac_f32 %0, %1, %2" : "+v"(yp[0][1]) : "v"(yp[2][1]), "v"(Dv) : ); SB();
  ST(26);
  asm volatile("s_waitcnt vmcnt(19)" :  :  : "memory"); SB();
  asm volatile("v_mfma_f32_16x16x32_bf16 %0, v[160:163], a[96:99], %0" : "+v"(g[0][0][0]) :  : ); SB();
  asm volatile("v_fmac_f32 %0, %1, %2" : "+v"(yp[0][2]) : "v"(yp[2][2]), "v"(Dv) : ); SB();
  asm volatile("v_mfma_f32_16x16x32_bf16 %0, v[160:163], a[100:103], %0" : "+v"(g[0][1][0]) :  : ); SB();
  asm volatile("v_fmac_f32 %0, %1, %2" : "+v"(yp[0][3]) : "v"(yp[2][3]), "v"(Dv) : ); SB();
  asm volatile("v_mfma_f32_16x16x32_bf16 %0, v[160:163], a[104:107], %0" : "+v"(g[0][2][0]) :  : ); SB();
  asm volatile("buffer_load_dwordx4 v[240:243], %0, %1, %2 offen offset:0 sc1" :  : "v"(vB), "s"(rWS), "s"(296960u) : "memory"); SB();
  asm volatile("v_fmac_f32 %0, %1, %2" : "+v"(yp[1][0]) : "v"(yp[3][0]), "v"(Dv) : ); SB();
  asm volatile("v_mfma_f32_16x16x32_bf16 %0, v[160:163], a[108:111], %0" : "+v"(g[0][3][0]) :  : ); SB();
  asm volatile("v_fmac_f32 %0, %1, %2" : "+v"(yp[1][1]) : "v"(yp[3][1]), "v"(Dv) : ); SB();
  asm volatile("v_mfma_f32_16x16x32_bf16 %0, v[164:167], a[96:99], %0" : "+v"(g[0][0][1]) :  : ); SB();
  asm volatile("v_fmac_f32 %0, %1, %2" : "+v"(yp[1][2]) : "v"(yp[3][2]), "v"(Dv) : ); SB();
  asm volatile("v_mfma_f32_16x16x32_bf16 %0, v[164:167], a[100:103], %0" : "+v"(g[0][1][1]) :  : ); SB();
  asm volatile("v_fmac_f32 %0, %1, %2" : "+v"(yp[1][3]) : "v"(yp[3][3]), "v"(Dv) : ); SB();
  asm volatile("v_mfma_f32_16x16x32_bf16 %0, v[164:167], a[104:107], %0" : "+v"(g[0][2][1]) :  : ); SB();
  asm volatile("v_cvt_pk_bf16_f32 %0, %1, %2" : "=v"(yo[0]) : "v"(yp[0][0]), "v"(yp[0][1]) : ); SB();
  asm volatile("v_mfma_f32_16x16x32_bf16 %0, v[164:167], a[108:111], %0" : "+v"(g[0][3][1]) :  : ); SB();
  asm volatile("buffer_load_dwordx4 v[244:247], %0, %1, %2 offen offset:1024 sc1" :  : "v"(vB), "s"(rWS), "s"(296960u) : "memory"); SB();
  asm volatile("s_waitcnt vmcnt(19)" :  :  : "memory"); SB();
  asm volatile("v_mfma_f32_16x16x32_bf16 %0, v[168:171], a[112:115], %0" : "+v"(g[0][0][0]) :  : ); SB();
  asm volatile("v_cvt_pk_bf16_f32 %0, %1, %2" : "=v"(yo[1]) : "v"(yp[0][2]), "v"(yp[0][3]) : ); SB();
  asm volatile("v_mfma_f32_16x16x32_bf16 %0, v[168:171], a[116:119], %0" : "+v"(g[0][1][0]) :  : ); SB();
  asm volatile("v_cvt_pk_bf16_f32 %0, %1, %2" : "=v"(yo[2]) : "v"(yp[1][0]), "v"(yp[1][1]) : ); SB();
  asm volatile("v_mfma_f32_16x16x32_bf16 %0, v[168:171], a[120:123], %0" : "+v"(g[0][2][0]) :  : ); SB();
  asm volatile("v_cvt_pk_bf16_f32 %0, %1, %2" : "=v"(yo[3]) : "v"(yp[1][2]), "v"(yp[1][3]) : ); SB();
  asm volatile("v_mfma_f32_16x16x32_bf16 %0, v[168:171], a[124:127], %0" : "+v"(g[0][3][0]) :  : ); SB();
  asm volatile("buffer_store_dwordx4 %1, %0, %2, %3 offen offset:0 sc1\n\ts_nop 1" :  : "v"(vY_), "v"(yo), "s"(rWS), "s"(4096u) : "memory"); SB();
  asm volatile("v_mfma_f32_16x16x32_bf16 %0, v[172:175], a[112:115], %0" : "+v"(g[0][0][1]) :  : ); SB();
  asm volatile("ds_read_b128 %0, %1 offset:32768" : "=&v"(yp[0]) : "v"(ydr) : "memory"); SB();
  asm volatile("v_mfma_f32_16x16x32_bf16 %0, v[172:175], a[116:119], %0" : "+v"(g[0][1][1]) :  : ); SB();
  asm volatile("ds_read_b128 %0, %1 offset:33024" : "=&v"(yp[1]) : "v"(ydr) : "memory"); SB();
  asm volatile("v_mfma_f32_16x16x32_bf16 %0, v[172:175], a[120:123], %0" : "+v"(g[0][2][1]) :  : ); SB();
  asm volatile("ds_read_b128 %0, %1 offset:40960" : "=&v"(yp[2]) : "v"(ydr) : "memory"); SB();
  asm volatile("v_mfma_f32_16x16x32_bf16 %0, v[172:175], a[124:127], %0" : "+v"(g[0][3][1]) :  : ); SB();
  asm volatile("ds_read_b128 %0, %1 offset:41216" : "=&v"(yp[3]) : "v"(ydr) : "memory"); SB();
  asm volatile("s_waitcnt vmcnt(18)" :  :  : "memory"); SB();
  asm volatile("v_mfma_f32_16x16x32_bf16 %0, v[176:179], a[128:131], %0" : "+v"(g[0][0][0]) :  : ); SB();
  asm volatile("ds_read_b128 %0, %1 offset:25856" : "=&v"(ykv) : "v"(ykr) : "memory"); SB();
  asm volatile("v_lshlrev_b32 %0, 16, %1" : "=v"(Dv) : "v"(dD[2]) : ); SB();
  asm volatile("v_mfma_f32_16x16x32_bf16 %0, v[176:179], a[132:135], %0" : "+v"(g[0][1][0]) :  : ); SB();
  asm volatile("buffer_load_dwordx4 v[248:251], %0, %1, %2 offen offset:0 sc1" :  : "v"(vB), "s"(rWS), "s"(346112u) : "memory"); SB();
  asm volatile("v_mfma_f32_16x16x32_bf16 %0, v[176:179], a[136:139], %0" : "+v"(g[0][2][0]) :  : ); SB();
  asm volatile("v_rcp_f32 %0, %1" : "=v"(gt[3]) : "v"(ge[3]) : ); SB();
  asm volatile("v_mfma_f32_16x16x32_bf16 %0, v[176:179], a[140:143], %0" : "+v"(g[0][3][0]) :  : ); SB();
  asm volatile("v_mul_f32 %0, %0, %1" : "+v"(a1[0][0][0]) : "v"(gt[0]) : ); SB();
  asm volatile("v_mfma_f32_16x16x32_bf16 %0, v[180:183], a[128:131], %0" : "+v"(g[0][0][1]) :  : ); SB();
  asm volatile("v_mul_f32 %0, %0, %1" : "+v"(a1[0][0][1]) : "v"(gt[1]) : ); SB();
  asm volatile("v_mfma_f32_16x16x32_bf16 %0, v[180:183], a[132:135], %0" : "+v"(g[0][1][1]) :  : ); SB();
  asm volatile("s_waitcnt lgkmcnt(0)" : "+v"(yp[0]), "+v"(yp[1]), "+v"(yp[2]), "+v"(yp[3]), "+v"(ykv) :  : "memory"); SB();
  asm volatile("buffer_load_dwordx4 v[252:255], %0, %1, %2 offen offset:1024 sc1" :  : "v"(vB), "s"(rWS), "s"(346112u) : "memory"); SB();
  asm volatile("v_mfma_f32_16x16x32_bf16 %0, v[180:183], a[136:139], %0" : "+v"(g[0][2][1]) :  : ); SB();
  asm volatile("v_add_f32 %0, %0, %1" : "+v"(yp[0][0]) : "v"(yp[2][0]) : ); SB();
  asm volatile("v_mfma_f32_16x16x32_bf16 %0, v[180:183], a[140:143], %0" : "+v"(g[0][3][1]) :  : ); SB();
  asm volatile("buffer_load_dwordx4 v[160:163], %0, %1, %2 offen offset:0 sc1" :  : "v"(vB), "s"(rWS), "s"(395264u) : "memory"); SB();
  asm volatile("s_waitcnt vmcnt(19)" :  :  : "memory"); SB();
  asm volatile("v_mfma_f32_16x16x32_bf16 %0, v[184:187], a[144:147], %0" : "+v"(g[0][0][0]) :  : ); SB();
  asm volatile("v_add_f32 %0, %0, %1" : "+v"(yp[0][1]) : "v"(yp[2][1]) : ); SB();
  asm volatile("v_mfma_f32_16x16x32_bf16 %0, v[184:187], a[148:151], %0" : "+v"(g[0][1][0]) :  : ); SB();
  asm volatile("v_add_f32 %0, %0, %1" : "+v"(yp[0][2]) : "v"(yp[2][2]) : ); SB();
  asm volatile("v_mfma_f32_16x16x32_bf16 %0, v[184:187], a[152:155], %0" : "+v"(g[0][2][0]) :  : ); SB();
  asm volatile("v_add_f32 %0, %0, %1" : "+v"(yp[0][3]) : "v"(yp[2][3]) : ); SB();
  asm volatile("v_mfma_f32_16x16x32_bf16 %0, v[184:187], a[156:159], %0" : "+v"(g[0][3][0]) :  : ); SB();
  asm volatile("v_add_f32 %0, %0, %1" : "+v"(yp[1][0]) : "v"(yp[3][0]) : ); SB();
  asm volatile("v_mfma_f32_16x16x32_bf16 %0, v[188:191], a[144:147], %0" : "+v"(g[0][0][1]) :  : ); SB();
  asm volatile("v_add_f32 %0, %0, %1" : "+v"(yp[1][1]) : "v"(yp[3][1]) : ); SB();
  asm volatile("v_mfma_f32_16x16x32_bf16 %0, v[188:191], a[148:151], %0" : "+v"(g[0][1][1]) :  : ); SB();
  asm volatile("v_add_f32 %0, %0, %1" : "+v"(yp[1][2]) : "v"(yp[3][2]) : ); SB();
  asm volatile("v_mfma_f32_16x16x32_bf16 %0, v[188:191], a[152:155], %0" : "+v"(g[0][2][1]) :  : ); SB();
  asm volatile("v_add_f32 %0, %0, %1" : "+v"(yp[1][3]) : "v"(yp[3][3]) : ); SB();
  asm volatile("v_mfma_f32_16x16x32_bf16 %0, v[188:191], a[156:159], %0" : "+v"(g[0][3][1]) :  : ); SB();
  asm volatile("v_lshlrev_b32 %0, 16, %1" : "=v"(yp[2][0]) : "v"(ykv[0]) : ); SB();
  ST(27);
  asm volatile("s_waitcnt vmcnt(17)" :  :  : "memory"); SB();
  asm volatile("v_mfma_f32_16x16x32_bf16 %0, v[192:195], a[160:163], %1" : "=&v"(g[1][0][0]) : "v"(bs4) : ); SB();
  asm volatile("s_barrier" :  :  : "memory"); SB();
  asm volatile("v_and_b32 %0, 0xffff0000, %1" : "=v"(yp[2][1]) : "v"(ykv[0]) : ); SB();
  asm volatile("v_mfma_f32_16x16x32_bf16 %0, v[192:195], a[164:167], 0" : "=&v"(g[1][1][0]) :  : ); SB();
  asm volatile("v_lshlrev_b32 %0, 16, %1" : "=v"(yp[2][2]) : "v"(ykv[1]) : ); SB();
  asm volatile("v_mfma_f32_16x16x32_bf16 %0, v[192:195], a[168:171], 0" : "=&v"(g[1][2][0]) :  : ); SB();
  asm volatile("ds_write_b128 %0, %1 offset:0" :  : "v"(pw2), "v"(g[0][1][0]) : "memory"); SB();
  asm volatile("v_mfma_f32_16x16x32_bf16 %0, v[192:195], a[172:175], 0" : "=&v"(g[1][3][0]) :  : ); SB();
  asm volatile("v_and_b32 %0, 0xffff0000, %1" : "=v"(yp[2][3]) : "v"(ykv[1]) : ); SB();
  asm volatile("v_mfma_f32_16x16x32_bf16 %0, v[196:199], a[160:163], %1" : "=&v"(g[1][0][1]) : "v"(bs4) : ); SB();
  asm volatile("buffer_load_dwordx4 v[164:167], %0, %1, %2 offen offset:1024 sc1" :  : "v"(vB), "s"(rWS), "s"(395264u) : "memory"); SB();
  asm volatile("v_lshlrev_b32 %0, 16, %1" : "=v"(yp[3][0]) : "v"(ykv[2]) : ); SB();
  asm volatile("v_mfma_f32_16x16x32_bf16 %0, v[196:199], a[164:167], 0" : "=&v"(g[1][1][1]) :  : ); SB();
  asm volatile("ds_write_b128 %0, %1 offset:1024" :  : "v"(pw2), "v"(g[0][1][1]) : "memory"); SB();
  asm volatile("v_mfma_f32_16x16x32_bf16 %0, v[196:199], a[168:171], 0" : "=&v"(g[1][2][1]) :  : ); SB();
  asm volatile("v_and_b32 %0, 0xffff0000, %1" : "=v"(yp[3][1]) : "v"(ykv[2]) : ); SB();
  asm volatile("v_mfma_f32_16x16x32_bf16 %0, v[196:199], a[172:175], 0" : "=&v"(g[1][3][1]) :  : ); SB();
  asm volatile("v_lshlrev_b32 %0, 16, %1" : "=v"(yp[3][2]) : "v"(ykv[3]) : ); SB();
  asm volatile("s_waitcnt vmcnt(16)" :  :  : "memory"); SB();
  asm volatile("v_mfma_f32_16x16x32_bf16 %0, v[200:203], a[176:179], %0" : "+v"(g[1][0][0]) :  : ); SB();
  asm volatile("ds_write_b128 %0, %1 offset:2048" :  : "v"(pw2), "v"(g[0][2][0]) : "memory"); SB();
  asm volatile("v_mfma_f32_16x16x32_bf16 %0, v[200:203], a[180:183], %0" : "+v"(g[1][1][0]) :  : ); SB();
  asm volatile("s_add_u32 m0, %0, 0" :  : "s"(mO) : "memory", "scc"); SB();
  asm volatile("v_and_b32 %0, 0xffff0000, %1" : "=v"(yp[3][3]) : "v"(ykv[3]) : ); SB();
  asm volatile("v_mfma_f32_16x16x32_bf16 %0, v[200:203], a[184:187], %0" : "+v"(g[1][2][0]) :  : ); SB();
  asm volatile("buffer_load_dwordx4 %0, %1, %2 offen sc1 lds" :  : "v"(vO), "s"(rWS), "s"(0u) : "memory"); SB();
  asm volatile("v_fmac_f32 %0, %1, %2" : "+v"(yp[0][0]) : "v"(yp[2][0]), "v"(Dv) : ); SB();
  asm volatile("v_mfma_f32_16x16x32_bf16 %0, v[200:203], a[188:191], %0" : "+v"(g[1][3][0]) :  : ); SB();
  asm volatile("ds_write_b128 %0, %1 offset:3072" :  : "v"(pw2), "v"(g[0][2][1]) : "memory"); SB();
  asm volatile("v_mfma_f32_16x16x32_bf16 %0, v[204:207], a[176:179], %0" : "+v"(g[1][0][1]) :  : ); SB();
  asm volatile("s_add_u32 m0, %0, 0" :  : "s"(mO) : "memory", "scc"); SB();
  asm volatile("v_fmac_f32 %0, %1, %2" : "+v"(yp[0][1]) : "v"(yp[2][1]), "v"(Dv) : ); SB();
  asm volatile("v_mfma_f32_16x16x32_bf16 %0, v[204:207], a[180:183], %0" : "+v"(g[1][1][1]) :  : ); SB();
  asm volatile("buffer_load_dwordx4 %0, %1, %2 offen offset:1024 sc1 lds" :  : "v"(vO), "s"(rWS), "s"(0u) : "memory"); SB();
  asm volatile("v_fmac_f32 %0, %1, %2" : "+v"(yp[0][2]) : "v"(yp[2][2]), "v"(Dv) : ); SB();
  asm volatile("v_mfma_f32_16x16x32_bf16 %0, v[204:207], a[184:187], %0" : "+v"(g[1][2][1]) :  : ); SB();
  asm volatile("ds_write_b128 %0, %1 offset:4096" :  : "v"(pw2), "v"(g[0][3][0]) : "memory"); SB();
  asm volatile("v_mfma_f32_16x16x32_bf16 %0, v[204:207], a[188:191], %0" : "+v"(g[1][3][1]) :  : ); SB();
  asm volatile("s_add_u32 m0, %0, 0" :  : "s"(mO) : "memory", "scc"); SB();
  asm volatile("v_fmac_f32 %0, %1, %2" : "+v"(yp[0][3]) : "v"(yp[2][3]), "v"(Dv) : ); SB();
  asm volatile("s_waitcnt vmcnt(15)" :  :  : "memory"); SB();
  asm volatile("v_mfma_f32_16x16x32_bf16 %0, v[208:211], a[192:195], %0" : "+v"(g[1][0][0]) :  : ); SB();
  asm volatile("buffer_load_dwordx4 %0, %1, %2 offen offset:2048 sc1 lds" :  : "v"(vO), "s"(rWS), "s"(0u) : "memory"); SB();
  asm volatile("v_fmac_f32 %0, %1, %2" : "+v"(yp[1][0]) : "v"(yp[3][0]), "v"(Dv) : ); SB();
  asm volatile("v_mfma_f32_16x16x32_bf16 %0, v[208:211], a[196:199], %0" : "+v"(g[1][1][0]) :  : ); SB();
  asm volatile("ds_write_b128 %0, %1 offset:5120" :  : "v"(pw2), "v"(g[0][3][1]) : "memory"); SB();
  asm volatile("v_mfma_f32_16x16x32_bf16 %0, v[208:211], a[200:203], %0" : "+v"(g[1][2][0]) :  : ); SB();
  asm volatile("s_add_u32 m0, %0, 0" :  : "s"(mO) : "memory", "scc"); SB();
  asm volatile("v_fmac_f32 %0, %1, %2" : "+v"(yp[1][1]) : "v"(yp[3][1]), "v"(Dv) : ); SB();
  asm volatile("v_mfma_f32_16x16x32_bf16 %0, v[208:211], a[204:207], %0" : "+v"(g[1][3][0]) :  : ); SB();
  asm volatile("buffer_load_dwordx4 %0, %1, %2 offen offset:3072 sc1 lds" :  : "v"(vO), "s"(rWS), "s"(0u) : "memory"); SB();
  asm volatile("v_fmac_f32 %0, %1, %2" : "+v"(yp[1][2]) : "v"(yp[3][2]), "v"(Dv) : ); SB();
  asm volatile("v_mfma_f32_16x16x32_bf16 %0, v[212:215], a[192:195], %0" : "+v"(g[1][0][1]) :  : ); SB();
  asm volatile("s_waitcnt lgkmcnt(0)\n\ts_barrier" :  :  : "memory"); SB();
  asm volatile("ds_read_b128 %0, %1 offset:0" : "=&v"(pr[0]) : "v"(pe[0]) : "memory"); SB();
  asm volatile("v_mfma_f32_16x16x32_bf16 %0, v[212:215], a[196:199], %0" : "+v"(g[1][1][1]) :  : ); SB();
  asm volatile("ds_read_b128 %0, %1 offset:1024" : "=&v"(pr[1]) : "v"(pe[0]) : "memory"); SB();
  asm volatile("s_add_u32 m0, %0, 4096" :  : "s"(mO) : "memory", "scc"); SB();
  asm volatile("v_mfma_f32_16x16x32_bf16 %0, v[212:215], a[200:203], %0" : "+v"(g[1][2][1]) :  : ); SB();
  asm volatile("ds_read_b128 %0, %1 offset:2048" : "=&v"(pr[2]) : "v"(pe[1]) : "memory"); SB();
  asm volatile("v_fmac_f32 %0, %1, %2" : "+v"(yp[1][3]) : "v"(yp[3][3]), "v"(Dv) : ); SB();
  asm volatile("v_mfma_f32_16x16x32_bf16 %0, v[212:215], a[204:207], %0" : "+v"(g[1][3][1]) :  : ); SB();
  asm volatile("ds_read_b128 %0, %1 offset:3072" : "=&v"(pr[3]) : "v"(pe[1]) : "memory"); SB();
  asm volatile("s_waitcnt vmcnt(15)" :  :  : "memory"); SB();
  asm volatile("v_mfma_f32_16x16x32_bf16 %0, v[216:219], a[208:211], %0" : "+v"(g[1][0][0]) :  : ); SB();
  asm volatile("ds_read_b128 %0, %1 offset:4096" : "=&v"(pr[4]) : "v"(pe[2]) : "memory"); SB();
  asm volatile("v_cvt_pk_bf16_f32 %0, %1, %2" : "=v"(yo[0]) : "v"(yp[0][0]), "v"(yp[0][1]) : ); SB();
  asm volatile("v_mfma_f32_16x16x32_bf16 %0, v[216:219], a[212:215], %0" : "+v"(g[1][1][0]) :  : ); SB();
  asm volatile("ds_read_b128 %0, %1 offset:5120" : "=&v"(pr[5]) : "v"(pe[2]) : "memory"); SB();
  asm volatile("v_cvt_pk_bf16_f32 %0, %1, %2" : "=v"(yo[1]) : "v"(yp[0][2]), "v"(yp[0][3]) : ); SB();
  asm volatile("v_mfma_f32_16x16x32_bf16 %0, v[216:219], a[216:219], %0" : "+v"(g[1][2][0]) :  : ); SB();
  asm volatile("buffer_load_dwordx4 %0, %1, %2 offen sc1 lds" :  : "v"(vO), "s"(rWS), "s"(4096u) : "memory"); SB();
  asm volatile("s_add_u32 m0, %0, 4096" :  : "s"(mO) : "memory", "scc"); SB();
  asm volatile("v_mfma_f32_16x16x32_bf16 %0, v[216:219], a[220:223], %0" : "+v"(g[1][3][0]) :  : ); SB();
  asm volatile("buffer_load_dwordx4 %0, %1, %2 offen offset:1024 sc1 lds" :  : "v"(vO), "s"(rWS), "s"(4096u) : "memory"); SB();
  asm volatile("s_add_u32 m0, %0, 4096" :  : "s"(mO) : "memory", "scc"); SB();
  asm volatile("v_mfma_f32_16x16x32_bf16 %0, v[220:223], a[208:211], %0" : "+v"(g[1][0][1]) :  : ); SB();
  asm volatile("buffer_load_dwordx4 %0, %1, %2 offen offset:2048 sc1 lds" :  : "v"(vO), "s"(rWS), "s"(4096u) : "memory"); SB();
  asm volatile("s_add_u32 m0, %0, 4096" :  : "s"(mO) : "memory", "scc"); SB();
  asm volatile("v_mfma_f32_16x16x32_bf16 %0, v[220:223], a[212:215], %0" : "+v"(g[1][1][1]) :  : ); SB();
  asm volatile("buffer_load_dwordx4 %0, %1, %2 offen offset:3072 sc1 lds" :  : "v"(vO), "s"(rWS), "s"(4096u) : "memory"); SB();
  asm volatile("v_cvt_pk_bf16_f32 %0, %1, %2" : "=v"(yo[2]) : "v"(yp[1][0]), "v"(yp[1][1]) : ); SB();
  asm volatile("v_mfma_f32_16x16x32_bf16 %0, v[220:223], a[216:219], %0" : "+v"(g[1][2][1]) :  : ); SB();
  asm volatile("v_cvt_pk_bf16_f32 %0, %1, %2" : "=v"(yo[3]) : "v"(yp[1][2]), "v"(yp[1][3]) : ); SB();
  asm volatile("v_mfma_f32_16x16x32_bf16 %0, v[220:223], a[220:223], %0" : "+v"(g[1][3][1]) :  : ); SB();
  asm volatile("s_waitcnt lgkmcnt(0)" : "+v"(pr[0]), "+v"(pr[1]), "+v"(pr[2]), "+v"(pr[3]), "+v"(pr[4]), "+v"(pr[5]) :  : "memory"); SB();
  asm volatile("s_add_u32 m0, %0, 8192" :  : "s"(mO) : "memory", "scc"); SB();
  asm volatile("s_waitcnt vmcnt(17)" :  :  : "memory"); SB();
  asm volatile("v_mfma_f32_16x16x32_bf16 %0, v[224:227], a[224:227], %0" : "+v"(g[1][0][0]) :  : ); SB();
  asm volatile("buffer_store_dwordx4 %1, %0, %2, %3 offen offset:0 sc1\n\ts_nop 1" :  : "v"(vY_), "v"(yo), "s"(rWS), "s"(c2s) : "memory"); SB();
  asm volatile("v_mfma_f32_16x16x32_bf16 %0, v[224:227], a[228:231], %0" : "+v"(g[1][1][0]) :  : ); SB();
  asm volatile("buffer_load_dwordx4 %0, %1, %2 offen sc1 lds" :  : "v"(vO), "s"(rWS), "s"(8192u) : "memory"); SB();
  asm volatile("v_add_f32 %0, %0, %1" : "+v"(pr[0][0]) : "v"(g[0][0][0][0]) : ); SB();
  asm volatile("v_mfma_f32_16x16x32_bf16 %0, v[224:227], a[232:235], %0" : "+v"(g[1][2][0]) :  : ); SB();
  asm volatile("v_add_f32 %0, %0, %1" : "+v"(pr[0][1]) : "v"(g[0][0][0][1]) : ); SB();
  asm volatile("v_mfma_f32_16x16x32_bf16 %0, v[224:227], a[236:239], %0" : "+v"(g[1][3][0]) :  : ); SB();
  asm volatile("v_add_f32 %0, %0, %1" : "+v"(pr[0][2]) : "v"(g[0][0][0][2]) : ); SB();
  asm volatile("v_mfma_f32_16x16x32_bf16 %0, v[228:231], a[224:227], %0" : "+v"(g[1][0][1]) :  : ); SB();
  asm volatile("v_add_f32 %0, %0, %1" : "+v"(pr[0][3]) : "v"(g[0][0][0][3]) : ); SB();
  asm volatile("v_mfma_f32_16x16x32_bf16 %0, v[228:231], a[228:231], %0" : "+v"(g[1][1][1]) :  : ); SB();
  asm volatile("v_add_f32 %0, %0, %1" : "+v"(pr[1][0]) : "v"(g[0][0][1][0]) : ); SB();
  asm volatile("v_mfma_f32_16x16x32_bf16 %0, v[228:231], a[232:235], %0" : "+v"(g[1][2][1]) :  : ); SB();
  asm volatile("v_add_f32 %0, %0, %1" : "+v"(pr[1][1]) : "v"(g[0][0][1][1]) : ); SB();
  asm volatile("v_mfma_f32_16x16x32_bf16 %0, v[228:231], a[236:239], %0" : "+v"(g[1][3][1]) :  : ); SB();
  asm volatile("v_add_f32 %0, %0, %1" : "+v"(pr[1][2]) : "v"(g[0][0][1][2]) : ); SB();
  asm volatile("s_waitcnt vmcnt(17)" :  :  : "memory"); SB();
  asm volatile("v_mfma_f32_16x16x32_bf16 %0, v[232:235], a[240:243], %0" : "+v"(g[1][0][0]) :  : ); SB();
  asm volatile("v_add_f32 %0, %0, %1" : "+v"(pr[1][3]) : "v"(g[0][0][1][3]) : ); SB();
  asm volatile("v_mfma_f32_16x16x32_bf16 %0, v[232:235], a[244:247], %0" : "+v"(g[1][1][0]) :  : ); SB();
  asm volatile("v_add_f32 %0, %0, %1" : "+v"(pr[2][0]) : "v"(pr[4][0]) : ); SB();
  asm volatile("v_mfma_f32_16x16x32_bf16 %0, v[232:235], a[248:251], %0" : "+v"(g[1][2][0]) :  : ); SB();
  asm volatile("v_add_f32 %0, %0, %1" : "+v"(pr[2][1]) : "v"(pr[4][1]) : ); SB();
  asm volatile("v_mfma_f32_16x16x32_bf16 %0, v[232:235], a[252:255], %0" : "+v"(g[1][3][0]) :  : ); SB();
  asm volatile("v_add_f32 %0, %0, %1" : "+v"(pr[2][2]) : "v"(pr[4][2]) : ); SB();
  asm volatile("v_mfma_f32_16x16x32_bf16 %0, v[236:239], a[240:243], %0" : "+v"(g[1][0][1]) :  : ); SB();
  asm volatile("v_add_f32 %0, %0, %1" : "+v"(pr[2][3]) : "v"(pr[4][3]) : ); SB();
  asm volatile("v_mfma_f32_16x16x32_bf16 %0, v[236:239], a[244:247], %0" : "+v"(g[1][1][1]) :  : ); SB();
  asm volatile("v_add_f32 %0, %0, %1" : "+v"(pr[3][0]) : "v"(pr[5][0]) : ); SB();
  asm volatile("v_mfma_f32_16x16x32_bf16 %0, v[236:239], a[248:251], %0" : "+v"(g[1][2][1]) :  : ); SB();
  asm volatile("v_add_f32 %0, %0, %1" : "+v"(pr[3][1]) : "v"(pr[5][1]) : ); SB();
  asm volatile("v_mfma_f32_16x16x32_bf16 %0, v[236:239], a[252:255], %0" : "+v"(g[1][3][1]) :  : ); SB();
  asm volatile("v_add_f32 %0, %0, %1" : "+v"(pr[3][2]) : "v"(pr[5][2]) : ); SB();
  asm volatile("s_waitcnt vmcnt(15)" :  :  : "memory"); SB();
  asm volatile("v_mfma_f32_16x16x32_bf16 %0, v[240:243], a[0:3], %0" : "+v"(g[1][0][0]) :  : ); SB();
  asm volatile("v_add_f32 %0, %0, %1" : "+v"(pr[3][3]) : "v"(pr[5][3]) : ); SB();
  asm volatile("v_mfma_f32_16x16x32_bf16 %0, v[240:243], a[4:7], %0" : "+v"(g[1][1][0]) :  : ); SB();
  asm volatile("v_add_f32 %0, %0, %1" : "+v"(pr[0][0]) : "v"(pr[2][0]) : ); SB();
  asm volatile("v_mfma_f32_16x16x32_bf16 %0, v[240:243], a[8:11], %0" : "+v"(g[1][2][0]) :  : ); SB();
  asm volatile("v_add_f32 %0, %0, %1" : "+v"(pr[0][1]) : "v"(pr[2][1]) : ); SB();
  asm volatile("v_mfma_f32_16x16x32_bf16 %0, v[240:243], a[12:15], %0" : "+v"(g[1][3][0]) :  : ); SB();
  asm volatile("v_add_f32 %0, %0, %1" : "+v"(pr[0][2]) : "v"(pr[2][2]) : ); SB();
  asm volatile("v_mfma_f32_16x16x32_bf16 %0, v[244:247], a[0:3], %0" : "+v"(g[1][0][1]) :  : ); SB();
  asm volatile("s_add_u32 m0, %0, 8192" :  : "s"(mO) : "memory", "scc"); SB();
  asm volatile("v_add_f32 %0, %0, %1" : "+v"(pr[0][3]) : "v"(pr[2][3]) : ); SB();
  asm volatile("v_mfma_f32_16x16x32_bf16 %0, v[244:247], a[4:7], %0" : "+v"(g[1][1][1]) :  : ); SB();
  asm volatile("buffer_load_dwordx4 %0, %1, %2 offen offset:1024 sc1 lds" :  : "v"(vO), "s"(rWS), "s"(8192u) : "memory"); SB();
  asm volatile("v_add_f32 %0, %0, %1" : "+v"(pr[1][0]) : "v"(pr[3][0]) : ); SB();
  asm volatile("v_mfma_f32_16x16x32_bf16 %0, v[244:247], a[8:11], %0" : "+v"(g[1][2][1]) :  : ); SB();
  asm volatile("v_add_f32 %0, %0, %1" : "+v"(pr[1][1]) : "v"(pr[3][1]) : ); SB();
  asm volatile("v_mfma_f32_16x16x32_bf16 %0, v[244:247], a[12:15], %0" : "+v"(g[1][3][1]) :  : ); SB();
  asm volatile("v_add_f32 %0, %0, %1" : "+v"(pr[1][2]) : "v"(pr[3][2]) : ); SB();
  asm volatile("s_waitcnt vmcnt(13)" :  :  : "memory"); SB();
  asm volatile("v_mfma_f32_16x16x32_bf16 %0, v[248:251], a[16:19], %0" : "+v"(g[1][0][0]) :  : ); SB();
  asm volatile("buffer_load_dwordx4 v[168:171], %0, %1, %2 offen offset:0 sc1" :  : "v"(vB), "s"(rWS), "s"(444416u) : "memory"); SB();
  asm volatile("v_add_f32 %0, %0, %1" : "+v"(pr[1][3]) : "v"(pr[3][3]) : ); SB();
  asm volatile("v_mfma_f32_16x16x32_bf16 %0, v[248:251], a[20:23], %0" : "+v"(g[1][1][0]) :  : ); SB();
  asm volatile("v_mul_f32 %0, %0, %1" : "+v"(a1[0][0][2]) : "v"(gt[2]) : ); SB();
  asm volatile("v_mul_f32 %0, %0, %1" : "+v"(a1[0][0][3]) : "v"(gt[3]) : ); SB();
  asm volatile("v_mul_f32 %0, %1, %1" : "=v"(gt[0]) : "v"(a1[0][1][0]) : ); SB();
  asm volatile("v_mul_f32 %0, %1, %1" : "=v"(gt[1]) : "v"(a1[0][1][1]) : ); SB();
  asm volatile("v_mul_f32 %0, %1, %1" : "=v"(gt[2]) : "v"(a1[0][1][2]) : ); SB();
  asm volatile("v_mul_f32 %0, %1, %1" : "=v"(gt[3]) : "v"(a1[0][1][3]) : ); SB();
  asm volatile("v_fma_f32 %0, %0, %1, %2" : "+v"(gt[0]) : "v"(k2[0]), "v"(k1[0]) : ); SB();
  asm volatile("v_fma_f32 %0, %0, %1, %2" : "+v"(gt[1]) : "v"(k2[0]), "v"(k1[0]) : ); SB();
  asm volatile("v_fma_f32 %0, %0, %1, %2" : "+v"(gt[2]) : "v"(k2[0]), "v"(k1[0]) : ); SB();
  asm volatile("v_fma_f32 %0, %0, %1, %2" : "+v"(gt[3]) : "v"(k2[0]), "v"(k1[0]) : ); SB();
  asm volatile("v_mul_f32 %0, %0, %1" : "+v"(gt[0]) : "v"(a1[0][1][0]) : ); SB();
  asm volatile("v_mul_f32 %0, %0, %1" : "+v"(gt[1]) : "v"(a1[0][1][1]) : ); SB();
  asm volatile("v_mul_f32 %0, %0, %1" : "+v"(gt[2]) : "v"(a1[0][1][2]) : ); SB();
  asm volatile("v_mul_f32 %0, %0, %1" : "+v"(gt[3]) : "v"(a1[0][1][3]) : ); SB();
  asm volatile("v_exp_f32 %0, %1" : "=v"(ge[0]) : "v"(gt[0]) : ); SB();
  asm volatile("v_exp_f32 %0, %1" : "=v"(ge[1]) : "v"(gt[1]) : ); SB();
  asm volatile("v_exp_f32 %0, %1" : "=v"(ge[2]) : "v"(gt[2]) : ); SB();
  asm volatile("v_exp_f32 %0, %1" : "=v"(ge[3]) : "v"(gt[3]) : ); SB();
  asm volatile("v_fma_f32 %0, %0, %1, %1" : "+v"(ge[0]) : "v"(xd[0]) : ); SB();
  asm volatile("v_fma_f32 %0, %0, %1, %1" : "+v"(ge[1]) : "v"(xd[0]) : ); SB();
  asm volatile("v_fma_f32 %0, %0, %1, %1" : "+v"(ge[2]) : "v"(xd[0]) : ); SB();
  asm volatile("v_fma_f32 %0, %0, %1, %1" : "+v"(ge[3]) : "v"(xd[0]) : ); SB();
  asm volatile("v_rcp_f32 %0, %1" : "=v"(gt[0]) : "v"(ge[0]) : ); SB();
  asm volatile("v_rcp_f32 %0, %1" : "=v"(gt[1]) : "v"(ge[1]) : ); SB();
  asm volatile("v_rcp_f32 %0, %1" : "=v"(gt[2]) : "v"(ge[2]) : ); SB();
  asm volatile("v_rcp_f32 %0, %1" : "=v"(gt[3]) : "v"(ge[3]) : ); SB();
  asm volatile("v_mul_f32 %0, %0, %1" : "+v"(a1[0][1][0]) : "v"(gt[0]) : ); SB();
  asm volatile("v_mul_f32 %0, %0, %1" : "+v"(a1[0][1][1]) : "v"(gt[1]) : ); SB();
  asm volatile("v_mul_f32 %0, %0, %1" : "+v"(a1[0][1][2]) : "v"(gt[2]) : ); SB();
  asm volatile("v_mul_f32 %0, %0, %1" : "+v"(a1[0][1][3]) : "v"(gt[3]) : ); SB();
  asm volatile("buffer_load_dwordx4 v[172:175], %0, %1, %2 offen offset:1024 sc1" :  : "v"(vB), "s"(rWS), "s"(444416u) : "memory"); SB();
  asm volatile("v_mul_f32 %0, %0, %1" : "+v"(pr[0][0]) : "v"(a1[0][0][0]) : ); SB();
  asm volatile("v_mfma_f32_16x16x32_bf16 %0, v[248:251], a[24:27], %0" : "+v"(g[1][2][0]) :  : ); SB();
  asm volatile("buffer_load_dwordx4 v[176:179], %0, %1, %2 offen offset:0 sc1" :  : "v"(vB), "s"(rWS), "s"(493568u) : "memory"); SB();
  asm volatile("v_mul_f32 %0, %0, %1" : "+v"(pr[0][1]) : "v"(a1[0][0][1]) : ); SB();
  asm volatile("v_mfma_f32_16x16x32_bf16 %0, v[248:251], a[28:31], %0" : "+v"(g[1][3][0]) :  : ); SB();
  asm volatile("v_mul_f32 %0, %0, %1" : "+v"(pr[0][2]) : "v"(a1[0][0][2]) : ); SB();
  asm volatile("v_mfma_f32_16x16x32_bf16 %0, v[252:255], a[16:19], %0" : "+v"(g[1][0][1]) :  : ); SB();
  asm volatile("buffer_load_dwordx4 v[180:183], %0, %1, %2 offen offset:1024 sc1" :  : "v"(vB), "s"(rWS), "s"(493568u) : "memory"); SB();
  asm volatile("v_mul_f32 %0, %0, %1" : "+v"(pr[0][3]) : "v"(a1[0][0][3]) : ); SB();
  asm volatile("v_mfma_f32_16x16x32_bf16 %0, v[252:255], a[20:23], %0" : "+v"(g[1][1][1]) :  : ); SB();
  asm volatile("v_mul_f32 %0, %0, %1" : "+v"(pr[1][0]) : "v"(a1[0][1][0]) : ); SB();
  asm volatile("v_mfma_f32_16x16x32_bf16 %0, v[252:255], a[24:27], %0" : "+v"(g[1][2][1]) :  : ); SB();
  asm volatile("v_mul_f32 %0, %0, %1" : "+v"(pr[1][1]) : "v"(a1[0][1][1]) : ); SB();
  asm volatile("v_mfma_f32_16x16x32_bf16 %0, v[252:255], a[28:31], %0" : "+v"(g[1][3][1]) :  : ); SB();
  asm volatile("v_mul_f32 %0, %0, %1" : "+v"(pr[1][2]) : "v"(a1[0][1][2]) : ); SB();
  ST(28);
  asm volatile("s_waitcnt vmcnt(15)" :  :  : "memory"); SB();
  asm volatile("v_mfma_f32_16x16x32_bf16 %0, v[160:163], a[32:35], %0" : "+v"(g[1][0][0]) :  : ); SB();
  asm volatile("buffer_load_dwordx4 v[184:187], %0, %1, %2 offen offset:0 sc1" :  : "v"(vB), "s"(rWS), "s"(542720u) : "memory"); SB();
  asm volatile("v_mul_f32 %0, %0, %1" : "+v"(pr[1][3]) : "v"(a1[0][1][3]) : ); SB();
  asm volatile("v_mfma_f32_16x16x32_bf16 %0, v[160:163], a[36:39], %0" : "+v"(g[1][1][0]) :  : ); SB();
  asm volatile("v_cvt_pk_bf16_f32 %0, %1, %2" : "=v"(ho[0]) : "v"(pr[0][0]), "v"(pr[0][1]) : ); SB();
  asm volatile("v_mfma_f32_16x16x32_bf16 %0, v[160:163], a[40:43], %0" : "+v"(g[1][2][0]) :  : ); SB();
  asm volatile("v_cvt_pk_bf16_f32 %0, %1, %2" : "=v"(ho[1]) : "v"(pr[0][2]), "v"(pr[0][3]) : ); SB();
  asm volatile("v_mfma_f32_16x16x32_bf16 %0, v[160:163], a[44:47], %0" : "+v"(g[1][3][0]) :  : ); SB();
  asm volatile("v_cvt_pk_bf16_f32 %0, %1, %2" : "=v"(ho[2]) : "v"(pr[1][0]), "v"(pr[1][1]) : ); SB();
  asm volatile("v_mfma_f32_16x16x32_bf16 %0, v[164:167], a[32:35], %0" : "+v"(g[1][0][1]) :  : ); SB();
  asm volatile("v_cvt_pk_bf16_f32 %0, %1, %2" : "=v"(ho[3]) : "v"(pr[1][2]), "v"(pr[1][3]) : ); SB();
  asm volatile("v_mfma_f32_16x16x32_bf16 %0, v[164:167], a[36:39], %0" : "+v"(g[1][1][1]) :  : ); SB();
  asm volatile("buffer_load_dwordx4 v[188:191], %0, %1, %2 offen offset:1024 sc1" :  : "v"(vB), "s"(rWS), "s"(542720u) : "memory"); SB();
  asm volatile("v_min_u32 %0, -2, %0" : "+v"(ho[3]) :  : ); SB();
  asm volatile("v_mfma_f32_16x16x32_bf16 %0, v[164:167], a[40:43], %0" : "+v"(g[1][2][1]) :  : ); SB();
  asm volatile("buffer_store_dwordx4 %1, %0, %2, %3 offen offset:0 sc1\n\ts_nop 1" :  : "v"(vH_), "v"(ho), "s"(rWS), "s"(0u) : "memory"); SB();
  asm volatile("v_mfma_f32_16x16x32_bf16 %0, v[164:167], a[44:47], %0" : "+v"(g[1][3][1]) :  : ); SB();
  asm volatile("v_mul_f32 %0, %1, %1" : "=v"(gt[0]) : "v"(a1[0][2][0]) : ); SB();
  asm volatile("buffer_load_dwordx4 v[192:195], %0, %1, %2 offen offset:0 sc1" :  : "v"(vB), "s"(rWS), "s"(591872u) : "memory"); SB();
  asm volatile("buffer_load_dwordx4 v[196:199], %0, %1, %2 offen offset:1024 sc1" :  : "v"(vB), "s"(rWS), "s"(591872u) : "memory"); SB();
  asm volatile("buffer_load_dwordx4 v[200:203], %0, %1, %2 offen offset:0 sc1" :  : "v"(vB), "s"(rWS), "s"(641024u) : "memory"); SB();
  asm volatile("buffer_load_dwordx4 v[204:207], %0, %1, %2 offen offset:1024 sc1" :  : "v"(vB), "s"(rWS), "s"(641024u) : "memory"); SB();
  asm volatile("s_waitcnt vmcnt(13)\n\ts_mov_b64 exec, 1\n\tbuffer_store_dwordx2 %0, off, %1, %2 sc1\n\ts_mov_b64 exec, -1" :  : "v"(((u32x2){tg0, tg1})), "s"(rWS), "s"(W_FY + ((cb * 4 + w) * 32 + rb) * 8) : "memory"); SB();
  asm volatile("buffer_load_dwordx4 v[208:211], %0, %1, %2 offen offset:0 sc1" :  : "v"(vB), "s"(rWS), "s"(690176u) : "memory"); SB();
  asm volatile("buffer_load_dwordx4 v[212:215], %0, %1, %2 offen offset:1024 sc1" :  : "v"(vB), "s"(rWS), "s"(690176u) : "memory"); SB();
  asm volatile("s_waitcnt vmcnt(12)" :  :  : "memory"); SB();
  asm volatile("v_mfma_f32_16x16x32_bf16 %0, v[168:171], a[48:51], %0" : "+v"(g[1][0][0]) :  : ); SB();
  asm volatile("buffer_load_dwordx4 v[216:219], %0, %1, %2 offen offset:0 sc1" :  : "v"(vB), "s"(rWS), "s"(739328u) : "memory"); SB();
  asm volatile("v_mul_f32 %0, %1, %1" : "=v"(gt[1]) : "v"(a1[0][2][1]) : ); SB();
  asm volatile("v_mfma_f32_16x16x32_bf16 %0, v[168:171], a[52:55], %0" : "+v"(g[1][1][0]) :  : ); SB();
  asm volatile("buffer_load_dwordx4 v[220:223], %0, %1, %2 offen offset:1024 sc1" :  : "v"(vB), "s"(rWS), "s"(739328u) : "memory"); SB();
  asm volatile("v_mul_f32 %0, %1, %1" : "=v"(gt[2]) : "v"(a1[0][2][2]) : ); SB();
  asm volatile("v_mfma_f32_16x16x32_bf16 %0, v[168:171], a[56:59], %0" : "+v"(g[1][2][0]) :  : ); SB();
  asm volatile("v_mul_f32 %0, %1, %1" : "=v"(gt[3]) : "v"(a1[0][2][3]) : ); SB();
  asm volatile("v_mfma_f32_16x16x32_bf16 %0, v[168:171], a[60:63], %0" : "+v"(g[1][3][0]) :  : ); SB();
  asm volatile("v_fma_f32 %0, %0, %1, %2" : "+v"(gt[0]) : "v"(k2[0]), "v"(k1[0]) : ); SB();
  asm volatile("v_mfma_f32_16x16x32_bf16 %0, v[172:175], a[48:51], %0" : "+v"(g[1][0][1]) :  : ); SB();
  asm volatile("v_fma_f32 %0, %0, %1, %2" : "+v"(gt[1]) : "v"(k2[0]), "v"(k1[0]) : ); SB();
  asm volatile("v_mfma_f32_16x16x32_bf16 %0, v[172:175], a[52:55], %0" : "+v"(g[1][1][1]) :  : ); SB();
  asm volatile("buffer_load_dwordx4 v[224:227], %0, %1, %2 offen offset:0 sc1" :  : "v"(vB), "s"(rWS), "s"(4096u) : "memory"); SB();
  asm volatile("v_fma_f32 %0, %0, %1, %2" : "+v"(gt[2]) : "v"(k2[0]), "v"(k1[0]) : ); SB();
  asm volatile("v_mfma_f32_16x16x32_bf16 %0, v[172:175], a[56:59], %0" : "+v"(g[1][2][1]) :  : ); SB();
  asm volatile("v_fma_f32 %0, %0, %1, %2" : "+v"(gt[3]) : "v"(k2[0]), "v"(k1[0]) : ); SB();
  asm volatile("v_mfma_f32_16x16x32_bf16 %0, v[172:175], a[60:63], %0" : "+v"(g[1][3][1]) :  : ); SB();
  asm volatile("v_mul_f32 %0, %0, %1" : "+v"(gt[0]) : "v"(a1[0][2][0]) : ); SB();
  asm volatile("s_waitcnt vmcnt(13)" :  :  : "memory"); SB();
  asm volatile("v_mfma_f32_16x16x32_bf16 %0, v[176:179], a[64:67], %0" : "+v"(g[1][0][0]) :  : ); SB();
  asm volatile("buffer_load_dwordx4 v[228:231], %0, %1, %2 offen offset:1024 sc1" :  : "v"(vB), "s"(rWS), "s"(4096u) : "memory"); SB();
  asm volatile("v_mul_f32 %0, %0, %1" : "+v"(gt[1]) : "v"(a1[0][2][1]) : ); SB();
  asm volatile("v_mfma_f32_16x16x32_bf16 %0, v[176:179], a[68:71], %0" : "+v"(g[1][1][0]) :  : ); SB();
  asm volatile("v_mul_f32 %0, %0, %1" : "+v"(gt[2]) : "v"(a1[0][2][2]) : ); SB();
  asm volatile("v_mfma_f32_16x16x32_bf16 %0, v[176:179], a[72:75], %0" : "+v"(g[1][2][0]) :  : ); SB();
  asm volatile("v_mul_f32 %0, %0, %1" : "+v"(gt[3]) : "v"(a1[0][2][3]) : ); SB();
  asm volatile("v_mfma_f32_16x16x32_bf16 %0, v[176:179], a[76:79], %0" : "+v"(g[1][3][0]) :  : ); SB();
  asm volatile("v_exp_f32 %0, %1" : "=v"(ge[0]) : "v"(gt[0]) : ); SB();
  asm volatile("v_mfma_f32_16x16x32_bf16 %0, v[180:183], a[64:67], %0" : "+v"(g[1][0][1]) :  : ); SB();
  asm volatile("v_exp_f32 %0, %1" : "=v"(ge[1]) : "v"(gt[1]) : ); SB();
  asm volatile("v_mfma_f32_16x16x32_bf16 %0, v[180:183], a[68:71], %0" : "+v"(g[1][1][1]) :  : ); SB();
  asm volatile("buffer_load_dwordx4 v[232:235], %0, %1, %2 offen offset:0 sc1" :  : "v"(vB), "s"(rWS), "s"(53248u) : "memory"); SB();
  asm volatile("v_mfma_f32_16x16x32_bf16 %0, v[180:183], a[72:75], %0" : "+v"(g[1][2][1]) :  : ); SB();
  asm volatile("v_exp_f32 %0, %1" : "=v"(ge[2]) : "v"(gt[2]) : ); SB();
  asm volatile("v_mfma_f32_16x16x32_bf16 %0, v[180:183], a[76:79], %0" : "+v"(g[1][3][1]) :  : ); SB();
  asm volatile("v_exp_f32 %0, %1" : "=v"(ge[3]) : "v"(gt[3]) : ); SB();
  asm volatile("s_waitcnt vmcnt(13)" :  :  : "memory"); SB();
  asm volatile("v_mfma_f32_16x16x32_bf16 %0, v[184:187], a[80:83], %0" : "+v"(g[1][0][0]) :  : ); SB();
  asm volatile("buffer_load_dwordx4 v[236:239], %0, %1, %2 offen offset:1024 sc1" :  : "v"(vB), "s"(rWS), "s"(53248u) : "memory"); SB();
  asm volatile("v_fma_f32 %0, %0, %1, %1" : "+v"(ge[0]) : "v"(xd[0]) : ); SB();
  asm volatile("v_mfma_f32_16x16x32_bf16 %0, v[184:187], a[84:87], %0" : "+v"(g[1][1][0]) :  : ); SB();
  asm volatile("v_fma_f32 %0, %0, %1, %1" : "+v"(ge[1]) : "v"(xd[0]) : ); SB();
  asm volatile("v_mfma_f32_16x16x32_bf16 %0, v[184:187], a[88:91], %0" : "+v"(g[1][2][0]) :  : ); SB();
  asm volatile("v_fma_f32 %0, %0, %1, %1" : "+v"(ge[2]) : "v"(xd[0]) : ); SB();
  asm volatile("v_mfma_f32_16x16x32_bf16 %0, v[184:187], a[92:95], %0" : "+v"(g[1][3][0]) :  : ); SB();
  asm volatile("v_fma_f32 %0, %0, %1, %1" : "+v"(ge[3]) : "v"(xd[0]) : ); SB();
  asm volatile("v_mfma_f32_16x16x32_bf16 %0, v[188:191], a[80:83], %0" : "+v"(g[1][0][1]) :  : ); SB();
  asm volatile("v_rcp_f32 %0, %1" : "=v"(gt[0]) : "v"(ge[0]) : ); SB();
  asm volatile("v_mfma_f32_16x16x32_bf16 %0, v[188:191], a[84:87], %0" : "+v"(g[1][1][1]) :  : ); SB();
  asm volatile("v_rcp_f32 %0, %1" : "=v"(gt[1]) : "v"(ge[1]) : ); SB();
  asm volatile("v_mfma_f32_16x16x32_bf16 %0, v[188:191], a[88:91], %0" : "+v"(g[1][2][1]) :  : ); SB();
  asm volatile("v_rcp_f32 %0, %1" : "=v"(gt[2]) : "v"(ge[2]) : ); SB();
  asm volatile("v_mfma_f32_16x16x32_bf16 %0, v[188:191], a[92:95], %0" : "+v"(g[1][3][1]) :  : ); SB();
  asm volatile("v_rcp_f32 %0, %1" : "=v"(gt[3]) : "v"(ge[3]) : ); SB();
  asm volatile("buffer_load_dwordx4 v[240:243], %0, %1, %2 offen offset:0 sc1" :  : "v"(vB), "s"(rWS), "s"(102400u) : "memory"); SB();
  asm volatile("s_waitcnt vmcnt(12)" :  :  : "memory"); SB();
  asm volatile("v_mfma_f32_16x16x32_bf16 %0, v[192:195], a[96:99], %0" : "+v"(g[1][0][0]) :  : ); SB();
  asm volatile("buffer_load_dwordx4 v[244:247], %0, %1, %2 offen offset:1024 sc1" :  : "v"(vB), "s"(rWS), "s"(102400u) : "memory"); SB();
  asm volatile("s_add_u32 m0, %0, 40960" :  : "s"(mO) : "memory", "scc"); SB();
  asm volatile("v_mfma_f32_16x16x32_bf16 %0, v[192:195], a[100:103], %0" : "+v"(g[1][1][0]) :  : ); SB();
  asm volatile("buffer_load_dwordx4 %0, %1, %2 offen sc1 lds" :  : "v"(vO), "s"(rWS), "s"(40960u) : "memory"); SB();
  asm volatile("v_mul_f32 %0, %0, %1" : "+v"(a1[0][2][0]) : "v"(gt[0]) : ); SB();
  asm volatile("v_mfma_f32_16x16x32_bf16 %0, v[192:195], a[104:107], %0" : "+v"(g[1][2][0]) :  : ); SB();
  asm volatile("v_mul_f32 %0, %0, %1" : "+v"(a1[0][2][1]) : "v"(gt[1]) : ); SB();
  asm volatile("v_mfma_f32_16x16x32_bf16 %0, v[192:195], a[108:111], %0" : "+v"(g[1][3][0]) :  : ); SB();
  asm volatile("v_mul_f32 %0, %0, %1" : "+v"(a1[0][2][2]) : "v"(gt[2]) : ); SB();
  asm volatile("v_mfma_f32_16x16x32_bf16 %0, v[196:199], a[96:99], %0" : "+v"(g[1][0][1]) :  : ); SB();
  asm volatile("v_mul_f32 %0, %0, %1" : "+v"(a1[0][2][3]) : "v"(gt[3]) : ); SB();
  asm volatile("v_mfma_f32_16x16x32_bf16 %0, v[196:199], a[100:103], %0" : "+v"(g[1][1][1]) :  : ); SB();
  asm volatile("s_add_u32 m0, %0, 40960" :  : "s"(mO) : "memory", "scc"); SB();
  asm volatile("v_mul_f32 %0, %1, %1" : "=v"(gt[0]) : "v"(a1[1][0][0]) : ); SB();
  asm volatile("v_mfma_f32_16x16x32_bf16 %0, v[196:199], a[104:107], %0" : "+v"(g[1][2][1]) :  : ); SB();
  asm volatile("buffer_load_dwordx4 %0, %1, %2 offen offset:1024 sc1 lds" :  : "v"(vO), "s"(rWS), "s"(40960u) : "memory"); SB();
  asm volatile("v_mul_f32 %0, %1, %1" : "=v"(gt[1]) : "v"(a1[1][0][1]) : ); SB();
  asm volatile("v_mfma_f32_16x16x32_bf16 %0, v[196:199], a[108:111], %0" : "+v"(g[1][3][1]) :  : ); SB();
  asm volatile("v_mul_f32 %0, %1, %1" : "=v"(gt[2]) : "v"(a1[1][0][2]) : ); SB();
  asm volatile("s_waitcnt vmcnt(13)" :  :  : "memory"); SB();
  asm volatile("v_mfma_f32_16x16x32_bf16 %0, v[200:203], a[112:115], %0" : "+v"(g[1][0][0]) :  : ); SB();
  asm volatile("s_add_u32 m0, %0, 40960" :  : "s"(mO) : "memory", "scc"); SB();
  asm volatile("v_mul_f32 %0, %1, %1" : "=v"(gt[3]) : "v"(a1[1][0][3]) : ); SB();
  asm volatile("v_mfma_f32_16x16x32_bf16 %0, v[200:203], a[116:119], %0" : "+v"(g[1][1][0]) :  : ); SB();
  asm volatile("buffer_load_dwordx4 %0, %1, %2 offen offset:2048 sc1 lds" :  : "v"(vO), "s"(rWS), "s"(40960u) : "memory"); SB();
  asm volatile("v_fma_f32 %0, %0, %1, %2" : "+v"(gt[0]) : "v"(k2[0]), "v"(k1[0]) : ); SB();
  asm volatile("v_mfma_f32_16x16x32_bf16 %0, v[200:203], a[120:123], %0" : "+v"(g[1][2][0]) :  : ); SB();
  asm volatile("v_fma_f32 %0, %0, %1, %2" : "+v"(gt[1]) : "v"(k2[0]), "v"(k1[0]) : ); SB();
  asm volatile("v_mfma_f32_16x16x32_bf16 %0, v[200:203], a[124:127], %0" : "+v"(g[1][3][0]) :  : ); SB();
  asm volatile("v_fma_f32 %0, %0, %1, %2" : "+v"(gt[2]) : "v"(k2[0]), "v"(k1[0]) : ); SB();
  asm volatile("v_mfma_f32_16x16x32_bf16 %0, v[204:207], a[112:115], %0" : "+v"(g[1][0][1]) :  : ); SB();
  asm volatile("s_add_u32 m0, %0, 40960" :  : "s"(mO) : "memory", "scc"); SB();
  asm volatile("v_fma_f32 %0, %0, %1, %2" : "+v"(gt[3]) : "v"(k2[0]), "v"(k1[0]) : ); SB();
  asm volatile("v_mfma_f32_16x16x32_bf16 %0, v[204:207], a[116:119], %0" : "+v"(g[1][1][1]) :  : ); SB();
  asm volatile("buffer_load_dwordx4 %0, %1, %2 offen offset:3072 sc1 lds" :  : "v"(vO), "s"(rWS), "s"(40960u) : "memory"); SB();
  asm volatile("v_mul_f32 %0, %0, %1" : "+v"(gt[0]) : "v"(a1[1][0][0]) : ); SB();
  asm volatile("v_mfma_f32_16x16x32_bf16 %0, v[204:207], a[120:123], %0" : "+v"(g[1][2][1]) :  : ); SB();
  asm volatile("v_mul_f32 %0, %0, %1" : "+v"(gt[1]) : "v"(a1[1][0][1]) : ); SB();
  asm volatile("v_mfma_f32_16x16x32_bf16 %0, v[204:207], a[124:127], %0" : "+v"(g[1][3][1]) :  : ); SB();
  asm volatile("v_mul_f32 %0, %0, %1" : "+v"(gt[2]) : "v"(a1[1][0][2]) : ); SB();
  asm volatile("s_waitcnt vmcnt(12)" :  :  : "memory"); SB();
  asm volatile("v_mfma_f32_16x16x32_bf16 %0, v[208:211], a[128:131], %0" : "+v"(g[1][0][0]) :  : ); SB();
  asm volatile("s_add_u32 m0, %0, 45056" :  : "s"(mO) : "memory", "scc"); SB();
  asm volatile("v_mul_f32 %0, %0, %1" : "+v"(gt[3]) : "v"(a1[1][0][3]) : ); SB();
  asm volatile("v_mfma_f32_16x16x32_bf16 %0, v[208:211], a[132:135], %0" : "+v"(g[1][1][0]) :  : ); SB();
  asm volatile("buffer_load_dwordx4 %0, %1, %2 offen sc1 lds" :  : "v"(vO), "s"(rWS), "s"(45056u) : "memory"); SB();
  asm volatile("s_add_u32 m0, %0, 45056" :  : "s"(mO) : "memory", "scc"); SB();
  asm volatile("v_mfma_f32_16x16x32_bf16 %0, v[208:211], a[136:139], %0" : "+v"(g[1][2][0]) :  : ); SB();
  asm volatile("buffer_load_dwordx4 %0, %1, %2 offen offset:1024 sc1 lds" :  : "v"(vO), "s"(rWS), "s"(45056u) : "memory"); SB();
  asm volatile("v_mfma_f32_16x16x32_bf16 %0, v[208:211], a[140:143], %0" : "+v"(g[1][3][0]) :  : ); SB();
  asm volatile("v_exp_f32 %0, %1" : "=v"(ge[0]) : "v"(gt[0]) : ); SB();
  asm volatile("v_mfma_f32_16x16x32_bf16 %0, v[212:215], a[128:131], %0" : "+v"(g[1][0][1]) :  : ); SB();
  asm volatile("v_exp_f32 %0, %1" : "=v"(ge[1]) : "v"(gt[1]) : ); SB();
  asm volatile("v_mfma_f32_16x16x32_bf16 %0, v[212:215], a[132:135], %0" : "+v"(g[1][1][1]) :  : ); SB();
  asm volatile("s_add_u32 m0, %0, 45056" :  : "s"(mO) : "memory", "scc"); SB();
  asm volatile("v_exp_f32 %0, %1" : "=v"(ge[2]) : "v"(gt[2]) : ); SB();
  asm volatile("v_mfma_f32_16x16x32_bf16 %0, v[212:215], a[136:139], %0" : "+v"(g[1][2][1]) :  : ); SB();
  asm volatile("buffer_load_dwordx4 %0, %1, %2 offen offset:2048 sc1 lds" :  : "v"(vO), "s"(rWS), "s"(45056u) : "memory"); SB();
  asm volatile("v_mfma_f32_16x16x32_bf16 %0, v[212:215], a[140:143], %0" : "+v"(g[1][3][1]) :  : ); SB();
  asm volatile("v_exp_f32 %0, %1" : "=v"(ge[3]) : "v"(gt[3]) : ); SB();
  asm volatile("s_waitcnt vmcnt(13)" :  :  : "memory"); SB();
  asm volatile("v_mfma_f32_16x16x32_bf16 %0, v[216:219], a[144:147], %0" : "+v"(g[1][0][0]) :  : ); SB();
  asm volatile("s_add_u32 m0, %0, 45056" :  : "s"(mO) : "memory", "scc"); SB();
  asm volatile("v_fma_f32 %0, %0, %1, %1" : "+v"(ge[0]) : "v"(xd[0]) : ); SB();
  asm volatile("v_mfma_f32_16x16x32_bf16 %0, v[216:219], a[148:151], %0" : "+v"(g[1][1][0]) :  : ); SB();
  asm volatile("buffer_load_dwordx4 %0, %1, %2 offen offset:3072 sc1 lds" :  : "v"(vO), "s"(rWS), "s"(45056u) : "memory"); SB();
  asm volatile("v_fma_f32 %0, %0, %1, %1" : "+v"(ge[1]) : "v"(xd[0]) : ); SB();
  asm volatile("v_mfma_f32_16x16x32_bf16 %0, v[216:219], a[152:155], %0" : "+v"(g[1][2][0]) :  : ); SB();
  asm volatile("v_fma_f32 %0, %0, %1, %1" : "+v"(ge[2]) : "v"(xd[0]) : ); SB();
  asm volatile("v_mfma_f32_16x16x32_bf16 %0, v[216:219], a[156:159], %0" : "+v"(g[1][3][0]) :  : ); SB();
  asm volatile("v_fma_f32 %0, %0, %1, %1" : "+v"(ge[3]) : "v"(xd[0]) : ); SB();
  asm volatile("v_mfma_f32_16x16x32_bf16 %0, v[220:223], a[144:147], %0" : "+v"(g[1][0][1]) :  : ); SB();
  asm volatile("v_rcp_f32 %0, %1" : "=v"(gt[0]) : "v"(ge[0]) : ); SB();
  asm volatile("v_mfma_f32_16x16x32_bf16 %0, v[220:223], a[148:151], %0" : "+v"(g[1][1][1]) :  : ); SB();
  asm volatile("s_add_u32 m0, %0, 49152" :  : "s"(mO) : "memory", "scc"); SB();
  asm volatile("v_rcp_f32 %0, %1" : "=v"(gt[1]) : "v"(ge[1]) : ); SB();
  asm volatile("v_mfma_f32_16x16x32_bf16 %0, v[220:223], a[152:155], %0" : "+v"(g[1][2][1]) :  : ); SB();
  asm volatile("buffer_load_dwordx4 %0, %1, %2 offen sc1 lds" :  : "v"(vO), "s"(rWS), "s"(49152u) : "memory"); SB();
  asm volatile("v_mfma_f32_16x16x32_bf16 %0, v[220:223], a[156:159], %0" : "+v"(g[1][3][1]) :  : ); SB();
  asm volatile("v_rcp_f32 %0, %1" : "=v"(gt[2]) : "v"(ge[2]) : ); SB();
  ST(29);
  asm volatile("s_waitcnt vmcnt(13)" :  :  : "memory"); SB();
  asm volatile("v_mfma_f32_16x16x32_bf16 %0, v[224:227], a[160:163], %1" : "=&v"(g[0][0][0]) : "v"(bs4) : ); SB();
  asm volatile("s_barrier" :  :  : "memory"); SB();
  asm volatile("s_add_u32 m0, %0, 49152" :  : "s"(mO) : "memory", "scc"); SB();
  asm volatile("v_mfma_f32_16x16x32_bf16 %0, v[224:227], a[164:167], 0" : "=&v"(g[0][1][0]) :  : ); SB();
  asm volatile("buffer_load_dwordx4 %0, %1, %2 offen offset:1024 sc1 lds" :  : "v"(vO), "s"(rWS), "s"(49152u) : "memory"); SB();
  asm volatile("v_mfma_f32_16x16x32_bf16 %0, v[224:227], a[168:171], 0" : "=&v"(g[0][2][0]) :  : ); SB();
  asm volatile("ds_write_b128 %0, %1 offset:0" :  : "v"(pw2), "v"(g[1][1][0]) : "memory"); SB();
  asm volatile("v_mfma_f32_16x16x32_bf16 %0, v[224:227], a[172:175], 0" : "=&v"(g[0][3][0]) :  : ); SB();
  asm volatile("buffer_load_dwordx4 v[248:251], %0, %1, %2 offen offset:0 sc1" :  : "v"(vB), "s"(rWS), "s"(151552u) : "memory"); SB();
  asm volatile("v_mfma_f32_16x16x32_bf16 %0, v[228:231], a[160:163], %1" : "=&v"(g[0][0][1]) : "v"(bs4) : ); SB();
  asm volatile("v_rcp_f32 %0, %1" : "=v"(gt[3]) : "v"(ge[3]) : ); SB();
  asm volatile("v_mfma_f32_16x16x32_bf16 %0, v[228:231], a[164:167], 0" : "=&v"(g[0][1][1]) :  : ); SB();
  asm volatile("ds_write_b128 %0, %1 offset:1024" :  : "v"(pw2), "v"(g[1][1][1]) : "memory"); SB();
  asm volatile("v_mfma_f32_16x16x32_bf16 %0, v[228:231], a[168:171], 0" : "=&v"(g[0][2][1]) :  : ); SB();
  asm volatile("buffer_load_dwordx4 v[252:255], %0, %1, %2 offen offset:1024 sc1" :  : "v"(vB), "s"(rWS), "s"(151552u) : "memory"); SB();
  asm volatile("v_mul_f32 %0, %0, %1" : "+v"(a1[1][0][0]) : "v"(gt[0]) : ); SB();
  asm volatile("v_mfma_f32_16x16x32_bf16 %0, v[228:231], a[172:175], 0" : "=&v"(g[0][3][1]) :  : ); SB();
  asm volatile("v_mul_f32 %0, %0, %1" : "+v"(a1[1][0][1]) : "v"(gt[1]) : ); SB();
  asm volatile("s_waitcnt vmcnt(14)" :  :  : "memory"); SB();
  asm volatile("v_mfma_f32_16x16x32_bf16 %0, v[232:235], a[176:179], %0" : "+v"(g[0][0][0]) :  : ); SB();
  asm volatile("ds_write_b128 %0, %1 offset:2048" :  : "v"(pw2), "v"(g[1][2][0]) : "memory"); SB();
  asm volatile("v_mfma_f32_16x16x32_bf16 %0, v[232:235], a[180:183], %0" : "+v"(g[0][1][0]) :  : ); SB();
  asm volatile("v_mul_f32 %0, %0, %1" : "+v"(a1[1][0][2]) : "v"(gt[2]) : ); SB();
  asm volatile("v_mfma_f32_16x16x32_bf16 %0, v[232:235], a[184:187], %0" : "+v"(g[0][2][0]) :  : ); SB();
  asm volatile("buffer_load_dwordx4 v[192:195], %0, %1, %2 offen offset:0 sc1" :  : "v"(vB), "s"(rWS), "s"(200704u) : "memory"); SB();
  asm volatile("v_mul_f32 %0, %0, %1" : "+v"(a1[1][0][3]) : "v"(gt[3]) : ); SB();
  asm volatile("v_mfma_f32_16x16x32_bf16 %0, v[232:235], a[188:191], %0" : "+v"(g[0][3][0]) :  : ); SB();
  asm volatile("ds_write_b128 %0, %1 offset:3072" :  : "v"(pw2), "v"(g[1][2][1]) : "memory"); SB();
  asm volatile("v_mfma_f32_16x16x32_bf16 %0, v[236:239], a[176:179], %0" : "+v"(g[0][0][1]) :  : ); SB();
  asm volatile("v_mul_f32 %0, %1, %1" : "=v"(gt[0]) : "v"(a1[1][1][0]) : ); SB();
  asm volatile("v_mfma_f32_16x16x32_bf16 %0, v[236:239], a[180:183], %0" : "+v"(g[0][1][1]) :  : ); SB();
  asm volatile("buffer_load_dwordx4 v[196:199], %0, %1, %2 offen offset:1024 sc1" :  : "v"(vB), "s"(rWS), "s"(200704u) : "memory"); SB();
  asm volatile("v_mul_f32 %0, %1, %1" : "=v"(gt[1]) : "v"(a1[1][1][1]) : ); SB();
  asm volatile("v_mfma_f32_16x16x32_bf16 %0, v[236:239], a[184:187], %0" : "+v"(g[0][2][1]) :  : ); SB();
  asm volatile("ds_write_b128 %0, %1 offset:4096" :  : "v"(pw2), "v"(g[1][3][0]) : "memory"); SB();
  asm volatile("v_mfma_f32_16x16x32_bf16 %0, v[236:239], a[188:191], %0" : "+v"(g[0][3][1]) :  : ); SB();
  asm volatile("v_mul_f32 %0, %1, %1" : "=v"(gt[2]) : "v"(a1[1][1][2]) : ); SB();
  asm volatile("s_waitcnt vmcnt(14)" :  :  : "memory"); SB();
  asm volatile("v_mfma_f32_16x16x32_bf16 %0, v[240:243], a[192:195], %0" : "+v"(g[0][0][0]) :  : ); SB();
  asm volatile("v_mul_f32 %0, %1, %1" : "=v"(gt[3]) : "v"(a1[1][1][3]) : ); SB();
  asm volatile("v_mfma_f32_16x16x32_bf16 %0, v[240:243], a[196:199], %0" : "+v"(g[0][1][0]) :  : ); SB();
  asm volatile("ds_write_b128 %0, %1 offset:5120" :  : "v"(pw2), "v"(g[1][3][1]) : "memory"); SB();
  asm volatile("v_mfma_f32_16x16x32_bf16 %0, v[240:243], a[200:203], %0" : "+v"(g[0][2][0]) :  : ); SB();
  asm volatile("buffer_load_dwordx4 v[200:203], %0, %1, %2 offen offset:0 sc1" :  : "v"(vB), "s"(rWS), "s"(249856u) : "memory"); SB();
  asm volatile("v_fma_f32 %0, %0, %1, %2" : "+v"(gt[0]) : "v"(k2[0]), "v"(k1[0]) : ); SB();
  asm volatile("v_mfma_f32_16x16x32_bf16 %0, v[240:243], a[204:207], %0" : "+v"(g[0][3][0]) :  : ); SB();
  asm volatile("v_fma_f32 %0, %0, %1, %2" : "+v"(gt[1]) : "v"(k2[0]), "v"(k1[0]) : ); SB();
  asm volatile("v_mfma_f32_16x16x32_bf16 %0, v[244:247], a[192:195], %0" : "+v"(g[0][0][1]) :  : ); SB();
  asm volatile("s_waitcnt lgkmcnt(0)\n\ts_barrier" :  :  : "memory"); SB();
  asm volatile("ds_read_b128 %0, %1 offset:0" : "=&v"(pr[0]) : "v"(pe[0]) : "memory"); SB();
  asm volatile("v_mfma_f32_16x16x32_bf16 %0, v[244:247], a[196:199], %0" : "+v"(g[0][1][1]) :  : ); SB();
  asm volatile("ds_read_b128 %0, %1 offset:1024" : "=&v"(pr[1]) : "v"(pe[0]) : "memory"); SB();
  asm volatile("v_fma_f32 %0, %0, %1, %2" : "+v"(gt[2]) : "v"(k2[0]), "v"(k1[0]) : ); SB();
  asm volatile("v_mfma_f32_16x16x32_bf16 %0, v[244:247], a[200:203], %0" : "+v"(g[0][2][1]) :  : ); SB();
  asm volatile("ds_read_b128 %0, %1 offset:2048" : "=&v"(pr[2]) : "v"(pe[1]) : "memory"); SB();
  asm volatile("v_fma_f32 %0, %0, %1, %2" : "+v"(gt[3]) : "v"(k2[0]), "v"(k1[0]) : ); SB();
  asm volatile("v_mfma_f32_16x16x32_bf16 %0, v[244:247], a[204:207], %0" : "+v"(g[0][3][1]) :  : ); SB();
  asm volatile("ds_read_b128 %0, %1 offset:3072" : "=&v"(pr[3]) : "v"(pe[1]) : "memory"); SB();
  asm volatile("buffer_load_dwordx4 v[204:207], %0, %1, %2 offen offset:1024 sc1" :  : "v"(vB), "s"(rWS), "s"(249856u) : "memory"); SB();
  asm volatile("buffer_load_dwordx4 v[208:211], %0, %1, %2 offen offset:0 sc1" :  : "v"(vB), "s"(rWS), "s"(299008u) : "memory"); SB();
  asm volatile("buffer_load_dwordx4 v[212:215], %0, %1, %2 offen offset:1024 sc1" :  : "v"(vB), "s"(rWS), "s"(299008u) : "memory"); SB();
  asm volatile("buffer_load_dwordx4 v[216:219], %0, %1, %2 offen offset:0 sc1" :  : "v"(vB), "s"(rWS), "s"(348160u) : "memory"); SB();
  asm volatile("buffer_load_dwordx4 v[220:223], %0, %1, %2 offen offset:1024 sc1" :  : "v"(vB), "s"(rWS), "s"(348160u) : "memory"); SB();
  asm volatile("buffer_load_dwordx4 v[224:227], %0, %1, %2 offen offset:0 sc1" :  : "v"(vB), "s"(rWS), "s"(397312u) : "memory"); SB();
  asm volatile("buffer_load_dwordx4 v[228:231], %0, %1, %2 offen offset:1024 sc1" :  : "v"(vB), "s"(rWS), "s"(397312u) : "memory"); SB();
  asm volatile("buffer_load_dwordx4 v[232:235], %0, %1, %2 offen offset:0 sc1" :  : "v"(vB), "s"(rWS), "s"(446464u) : "memory"); SB();
  asm volatile("buffer_load_dwordx4 v[236:239], %0, %1, %2 offen offset:1024 sc1" :  : "v"(vB), "s"(rWS), "s"(446464u) : "memory"); SB();
  asm volatile("s_waitcnt vmcnt(12)" :  :  : "memory"); SB();
  asm volatile("v_mfma_f32_16x16x32_bf16 %0, v[248:251], a[208:211], %0" : "+v"(g[0][0][0]) :  : ); SB();
  asm volatile("ds_read_b128 %0, %1 offset:4096" : "=&v"(pr[4]) : "v"(pe[2]) : "memory"); SB();
  asm volatile("v_mul_f32 %0, %0, %1" : "+v"(gt[0]) : "v"(a1[1][1][0]) : ); SB();
  asm volatile("v_mfma_f32_16x16x32_bf16 %0, v[248:251], a[212:215], %0" : "+v"(g[0][1][0]) :  : ); SB();
  asm volatile("ds_read_b128 %0, %1 offset:5120" : "=&v"(pr[5]) : "v"(pe[2]) : "memory"); SB();
  asm volatile("v_mul_f32 %0, %0, %1" : "+v"(gt[1]) : "v"(a1[1][1][1]) : ); SB();
  asm volatile("v_mfma_f32_16x16x32_bf16 %0, v[248:251], a[216:219], %0" : "+v"(g[0][2][0]) :  : ); SB();
  asm volatile("buffer_load_dwordx4 v[240:243], %0, %1, %2 offen offset:0 sc1" :  : "v"(vB), "s"(rWS), "s"(495616u) : "memory"); SB();
  asm volatile("v_mul_f32 %0, %0, %1" : "+v"(gt[2]) : "v"(a1[1][1][2]) : ); SB();
  asm volatile("v_mfma_f32_16x16x32_bf16 %0, v[248:251], a[220:223], %0" : "+v"(g[0][3][0]) :  : ); SB();
  asm volatile("buffer_load_dwordx4 v[244:247], %0, %1, %2 offen offset:1024 sc1" :  : "v"(vB), "s"(rWS), "s"(495616u) : "memory"); SB();
  asm volatile("v_mul_f32 %0, %0, %1" : "+v"(gt[3]) : "v"(a1[1][1][3]) : ); SB();
  asm volatile("v_mfma_f32_16x16x32_bf16 %0, v[252:255], a[208:211], %0" : "+v"(g[0][0][1]) :  : ); SB();
  asm volatile("v_exp_f32 %0, %1" : "=v"(ge[0]) : "v"(gt[0]) : ); SB();
  asm volatile("v_mfma_f32_16x16x32_bf16 %0, v[252:255], a[212:215], %0" : "+v"(g[0][1][1]) :  : ); SB();
  asm volatile("s_add_u32 m0, %0, 81920" :  : "s"(mO) : "memory", "scc"); SB();
  asm volatile("v_exp_f32 %0, %1" : "=v"(ge[1]) : "v"(gt[1]) : ); SB();
  asm volatile("v_mfma_f32_16x16x32_bf16 %0, v[252:255], a[216:219], %0" : "+v"(g[0][2][1]) :  : ); SB();
  asm volatile("buffer_load_dwordx4 %0, %1, %2 offen sc1 lds" :  : "v"(vO), "s"(rWS), "s"(81920u) : "memory"); SB();
  asm volatile("v_mfma_f32_16x16x32_bf16 %0, v[252:255], a[220:223], %0" : "+v"(g[0][3][1]) :  : ); SB();
  asm volatile("s_waitcnt lgkmcnt(0)" : "+v"(pr[0]), "+v"(pr[1]), "+v"(pr[2]), "+v"(pr[3]), "+v"(pr[4]), "+v"(pr[5]) :  : "memory"); SB();
  asm volatile("s_add_u32 m0, %0, 81920" :  : "s"(mO) : "memory", "scc"); SB();
  asm volatile("s_waitcnt vmcnt(13)" :  :  : "memory"); SB();
  asm volatile("v_mfma_f32_16x16x32_bf16 %0, v[192:195], a[224:227], %0" : "+v"(g[0][0][0]) :  : ); SB();
  asm volatile("buffer_load_dwordx4 %0, %1, %2 offen offset:1024 sc1 lds" :  : "v"(vO), "s"(rWS), "s"(81920u) : "memory"); SB();
  asm volatile("v_add_f32 %0, %0, %1" : "+v"(pr[0][0]) : "v"(g[1][0][0][0]) : ); SB();
  asm volatile("v_mfma_f32_16x16x32_bf16 %0, v[192:195], a[228:231], %0" : "+v"(g[0][1][0]) :  : ); SB();
  asm volatile("v_add_f32 %0, %0, %1" : "+v"(pr[0][1]) : "v"(g[1][0][0][1]) : ); SB();
  asm volatile("v_mfma_f32_16x16x32_bf16 %0, v[192:195], a[232:235], %0" : "+v"(g[0][2][0]) :  : ); SB();
  asm volatile("v_add_f32 %0, %0, %1" : "+v"(pr[0][2]) : "v"(g[1][0][0][2]) : ); SB();
  asm volatile("v_mfma_f32_16x16x32_bf16 %0, v[192:195], a[236:239], %0" : "+v"(g[0][3][0]) :  : ); SB();
  asm volatile("v_add_f32 %0, %0, %1" : "+v"(pr[0][3]) : "v"(g[1][0][0][3]) : ); SB();
  asm volatile("v_mfma_f32_16x16x32_bf16 %0, v[196:199], a[224:227], %0" : "+v"(g[0][0][1]) :  : ); SB();
  asm volatile("buffer_load_dwordx4 v[248:251], %0, %1, %2 offen offset:0 sc1" :  : "v"(vB), "s"(rWS), "s"(544768u) : "memory"); SB();
  asm volatile("v_add_f32 %0, %0, %1" : "+v"(pr[1][0]) : "v"(g[1][0][1][0]) : ); SB();
  asm volatile("v_mfma_f32_16x16x32_bf16 %0, v[196:199], a[228:231], %0" : "+v"(g[0][1][1]) :  : ); SB();
  asm volatile("v_add_f32 %0, %0, %1" : "+v"(pr[1][1]) : "v"(g[1][0][1][1]) : ); SB();
  asm volatile("v_mfma_f32_16x16x32_bf16 %0, v[196:199], a[232:235], %0" : "+v"(g[0][2][1]) :  : ); SB();
  asm volatile("v_add_f32 %0, %0, %1" : "+v"(pr[1][2]) : "v"(g[1][0][1][2]) : ); SB();
  asm volatile("v_mfma_f32_16x16x32_bf16 %0, v[196:199], a[236:239], %0" : "+v"(g[0][3][1]) :  : ); SB();
  asm volatile("v_add_f32 %0, %0, %1" : "+v"(pr[1][3]) : "v"(g[1][0][1][3]) : ); SB();
  asm volatile("s_waitcnt vmcnt(13)" :  :  : "memory"); SB();
  asm volatile("v_mfma_f32_16x16x32_bf16 %0, v[200:203], a[240:243], %0" : "+v"(g[0][0][0]) :  : ); SB();
  asm volatile("buffer_load_dwordx4 v[252:255], %0, %1, %2 offen offset:1024 sc1" :  : "v"(vB), "s"(rWS), "s"(544768u) : "memory"); SB();
  asm volatile("v_add_f32 %0, %0, %1" : "+v"(pr[2][0]) : "v"(pr[4][0]) : ); SB();
  asm volatile("v_mfma_f32_16x16x32_bf16 %0, v[200:203], a[244:247], %0" : "+v"(g[0][1][0]) :  : ); SB();
  asm volatile("v_add_f32 %0, %0, %1" : "+v"(pr[2][1]) : "v"(pr[4][1]) : ); SB();
  asm volatile("v_mfma_f32_16x16x32_bf16 %0, v[200:203], a[248:251], %0" : "+v"(g[0][2][0]) :  : ); SB();
  asm volatile("v_add_f32 %0, %0, %1" : "+v"(pr[2][2]) : "v"(pr[4][2]) : ); SB();
  asm volatile("v_mfma_f32_16x16x32_bf16 %0, v[200:203], a[252:255], %0" : "+v"(g[0][3][0]) :  : ); SB();
  asm volatile("v_add_f32 %0, %0, %1" : "+v"(pr[2][3]) : "v"(pr[4][3]) : ); SB();
  asm volatile("v_mfma_f32_16x16x32_bf16 %0, v[204:207], a[240:243], %0" : "+v"(g[0][0][1]) :  : ); SB();
  asm volatile("v_add_f32 %0, %0, %1" : "+v"(pr[3][0]) : "v"(pr[5][0]) : ); SB();
  asm volatile("v_mfma_f32_16x16x32_bf16 %0, v[204:207], a[244:247], %0" : "+v"(g[0][1][1]) :  : ); SB();
  asm volatile("buffer_load_dwordx4 v[192:195], %0, %1, %2 offen offset:0 sc1" :  : "v"(vB), "s"(rWS), "s"(593920u) : "memory"); SB();
  asm volatile("v_add_f32 %0, %0, %1" : "+v"(pr[3][1]) : "v"(pr[5][1]) : ); SB();
  asm volatile("v_mfma_f32_16x16x32_bf16 %0, v[204:207], a[248:251], %0" : "+v"(g[0][2][1]) :  : ); SB();
  asm volatile("v_add_f32 %0, %0, %1" : "+v"(pr[3][2]) : "v"(pr[5][2]) : ); SB();
  asm volatile("v_mfma_f32_16x16x32_bf16 %0, v[204:207], a[252:255], %0" : "+v"(g[0][3][1]) :  : ); SB();
  asm volatile("v_add_f32 %0, %0, %1" : "+v"(pr[3][3]) : "v"(pr[5][3]) : ); SB();
  asm volatile("s_waitcnt vmcnt(13)" :  :  : "memory"); SB();
  asm volatile("v_mfma_f32_16x16x32_bf16 %0, v[208:211], a[0:3], %0" : "+v"(g[0][0][0]) :  : ); SB();
  asm volatile("buffer_load_dwordx4 v[196:199], %0, %1, %2 offen offset:1024 sc1" :  : "v"(vB), "s"(rWS), "s"(593920u) : "memory"); SB();
  asm volatile("v_add_f32 %0, %0, %1" : "+v"(pr[0][0]) : "v"(pr[2][0]) : ); SB();
  asm volatile("v_mfma_f32_16x16x32_bf16 %0, v[208:211], a[4:7], %0" : "+v"(g[0][1][0]) :  : ); SB();
  asm volatile("v_add_f32 %0, %0, %1" : "+v"(pr[0][1]) : "v"(pr[2][1]) : ); SB();
  asm volatile("v_mfma_f32_16x16x32_bf16 %0, v[208:211], a[8:11], %0" : "+v"(g[0][2][0]) :  : ); SB();
  asm volatile("v_add_f32 %0, %0, %1" : "+v"(pr[0][2]) : "v"(pr[2][2]) : ); SB();
  asm volatile("v_mfma_f32_16x16x32_bf16 %0, v[208:211], a[12:15], %0" : "+v"(g[0][3][0]) :  : ); SB();
  asm volatile("v_add_f32 %0, %0, %1" : "+v"(pr[0][3]) : "v"(pr[2][3]) : ); SB();
  asm volatile("v_mfma_f32_16x16x32_bf16 %0, v[212:215], a[0:3], %0" : "+v"(g[0][0][1]) :  : ); SB();
  asm volatile("v_add_f32 %0, %0, %1" : "+v"(pr[1][0]) : "v"(pr[3][0]) : ); SB();
  asm volatile("v_mfma_f32_16x16x32_bf16 %0, v[212:215], a[4:7], %0" : "+v"(g[0][1][1]) :  : ); SB();
  asm volatile("buffer_load_dwordx2 %0, %1, %2, 0 offen sc1" : "=&v"(fy) : "v"(vFY), "s"(rWS) : "memory"); SB();
  asm volatile("v_add_f32 %0, %0, %1" : "+v"(pr[1][1]) : "v"(pr[3][1]) : ); SB();
  asm volatile("v_mfma_f32_16x16x32_bf16 %0, v[212:215], a[8:11], %0" : "+v"(g[0][2][1]) :  : ); SB();
  asm volatile("v_add_f32 %0, %0, %1" : "+v"(pr[1][2]) : "v"(pr[3][2]) : ); SB();
  asm volatile("v_mfma_f32_16x16x32_bf16 %0, v[212:215], a[12:15], %0" : "+v"(g[0][3][1]) :  : ); SB();
  asm volatile("v_add_f32 %0, %0, %1" : "+v"(pr[1][3]) : "v"(pr[3][3]) : ); SB();
  asm volatile("s_waitcnt vmcnt(13)" :  :  : "memory"); SB();
  asm volatile("v_mfma_f32_16x16x32_bf16 %0, v[216:219], a[16:19], %0" : "+v"(g[0][0][0]) :  : ); SB();
  asm volatile("s_add_u32 m0, %0, 81920" :  : "s"(mO) : "memory", "scc"); SB();
  asm volatile("v_mul_f32 %0, %0, %1" : "+v"(pr[0][0]) : "v"(a1[0][2][0]) : ); SB();
  asm volatile("v_mfma_f32_16x16x32_bf16 %0, v[216:219], a[20:23], %0" : "+v"(g[0][1][0]) :  : ); SB();
  asm volatile("buffer_load_dwordx4 %0, %1, %2 offen offset:2048 sc1 lds" :  : "v"(vO), "s"(rWS), "s"(81920u) : "memory"); SB();
  asm volatile("v_mul_f32 %0, %0, %1" : "+v"(pr[0][1]) : "v"(a1[0][2][1]) : ); SB();
  asm volatile("v_mfma_f32_16x16x32_bf16 %0, v[216:219], a[24:27], %0" : "+v"(g[0][2][0]) :  : ); SB();
  asm volatile("v_mul_f32 %0, %0, %1" : "+v"(pr[0][2]) : "v"(a1[0][2][2]) : ); SB();
  asm volatile("v_mfma_f32_16x16x32_bf16 %0, v[216:219], a[28:31], %0" : "+v"(g[0][3][0]) :  : ); SB();
  asm volatile("v_mul_f32 %0, %0, %1" : "+v"(pr[0][3]) : "v"(a1[0][2][3]) : ); SB();
  asm volatile("v_mfma_f32_16x16x32_bf16 %0, v[220:223], a[16:19], %0" : "+v"(g[0][0][1]) :  : ); SB();
  asm volatile("v_mul_f32 %0, %0, %1" : "+v"(pr[1][0]) : "v"(a1[1][0][0]) : ); SB();
  asm volatile("v_mfma_f32_16x16x32_bf16 %0, v[220:223], a[20:23], %0" : "+v"(g[0][1][1]) :  : ); SB();
  asm volatile("s_add_u32 m0, %0, 81920" :  : "s"(mO) : "memory", "scc"); SB();
  asm volatile("v_mul_f32 %0, %0, %1" : "+v"(pr[1][1]) : "v"(a1[1][0][1]) : ); SB();
  asm volatile("v_mfma_f32_16x16x32_bf16 %0, v[220:223], a[24:27], %0" : "+v"(g[0][2][1]) :  : ); SB();
  asm volatile("buffer_load_dwordx4 %0, %1, %2 offen offset:3072 sc1 lds" :  : "v"(vO), "s"(rWS), "s"(81920u) : "memory"); SB();
  asm volatile("v_mul_f32 %0, %0, %1" : "+v"(pr[1][2]) : "v"(a1[1][0][2]) : ); SB();
  asm volatile("v_mfma_f32_16x16x32_bf16 %0, v[220:223], a[28:31], %0" : "+v"(g[0][3][1]) :  : ); SB();
  asm volatile("v_mul_f32 %0, %0, %1" : "+v"(pr[1][3]) : "v"(a1[1][0][3]) : ); SB();
  ST(30);
  asm volatile("s_barrier" :  :  : "memory"); SB();
  asm volatile("s_waitcnt vmcnt(13)" :  :  : "memory"); SB();
  asm volatile("v_mfma_f32_16x16x32_bf16 %0, v[224:227], a[32:35], %0" : "+v"(g[0][0][0]) :  : ); SB();
  asm volatile("ds_read_b128 a[160:163], %0 offset:0" :  : "v"(aO) : "memory"); SB();
  asm volatile("s_add_u32 m0, %0, 86016" :  : "s"(mO) : "memory", "scc"); SB();
  asm volatile("v_mfma_f32_16x16x32_bf16 %0, v[224:227], a[36:39], %0" : "+v"(g[0][1][0]) :  : ); SB();
  asm volatile("ds_read_b128 a[164:167], %0 offset:1024" :  : "v"(aO) : "memory"); SB();
  asm volatile("v_cvt_pk_bf16_f32 %0, %1, %2" : "=v"(ho[0]) : "v"(pr[0][0]), "v"(pr[0][1]) : ); SB();
  asm volatile("v_mfma_f32_16x16x32_bf16 %0, v[224:227], a[40:43], %0" : "+v"(g[0][2][0]) :  : ); SB();
  asm volatile("ds_read_b128 a[168:171], %0 offset:2048" :  : "v"(aO) : "memory"); SB();
  asm volatile("v_cvt_pk_bf16_f32 %0, %1, %2" : "=v"(ho[1]) : "v"(pr[0][2]), "v"(pr[0][3]) : ); SB();
  asm volatile("v_mfma_f32_16x16x32_bf16 %0, v[224:227], a[44:47], %0" : "+v"(g[0][3][0]) :  : ); SB();
  asm volatile("ds_read_b128 a[172:175], %0 offset:3072" :  : "v"(aO) : "memory"); SB();
  asm volatile("v_cvt_pk_bf16_f32 %0, %1, %2" : "=v"(ho[2]) : "v"(pr[1][0]), "v"(pr[1][1]) : ); SB();
  asm volatile("v_mfma_f32_16x16x32_bf16 %0, v[228:231], a[32:35], %0" : "+v"(g[0][0][1]) :  : ); SB();
  asm volatile("ds_read_b128 a[176:179], %0 offset:4096" :  : "v"(aO) : "memory"); SB();
  asm volatile("v_cvt_pk_bf16_f32 %0, %1, %2" : "=v"(ho[3]) : "v"(pr[1][2]), "v"(pr[1][3]) : ); SB();
  asm volatile("v_mfma_f32_16x16x32_bf16 %0, v[228:231], a[36:39], %0" : "+v"(g[0][1][1]) :  : ); SB();
  asm volatile("ds_read_b128 a[180:183], %0 offset:5120" :  : "v"(aO) : "memory"); SB();
  asm volatile("v_min_u32 %0, -2, %0" : "+v"(ho[3]) :  : ); SB();
  asm volatile("v_mfma_f32_16x16x32_bf16 %0, v[228:231], a[40:43], %0" : "+v"(g[0][2][1]) :  : ); SB();
  asm volatile("buffer_store_dwordx4 %1, %0, %2, %3 offen offset:1024 sc1\n\ts_nop 1" :  : "v"(vH_), "v"(ho), "s"(rWS), "s"(0u) : "memory"); SB();
  asm volatile("v_mfma_f32_16x16x32_bf16 %0, v[228:231], a[44:47], %0" : "+v"(g[0][3][1]) :  : ); SB();
  asm volatile("ds_read_b128 a[184:187], %0 offset:6144" :  : "v"(aO) : "memory"); SB();
  asm volatile("s_waitcnt vmcnt(12)" :  :  : "memory"); SB();
  asm volatile("v_mfma_f32_16x16x32_bf16 %0, v[232:235], a[48:51], %0" : "+v"(g[0][0][0]) :  : ); SB();
  asm volatile("ds_read_b128 a[188:191], %0 offset:7168" :  : "v"(aO) : "memory"); SB();
  asm volatile("v_mfma_f32_16x16x32_bf16 %0, v[232:235], a[52:55], %0" : "+v"(g[0][1][0]) :  : ); SB();
  asm volatile("ds_read_b128 a[192:195], %0 offset:8192" :  : "v"(aO) : "memory"); SB();
  asm volatile("v_mfma_f32_16x16x32_bf16 %0, v[232:235], a[56:59], %0" : "+v"(g[0][2][0]) :  : ); SB();
  asm volatile("ds_read_b128 a[196:199], %0 offset:9216" :  : "v"(aO) : "memory"); SB();
  asm volatile("v_mfma_f32_16x16x32_bf16 %0, v[232:235], a[60:63], %0" : "+v"(g[0][3][0]) :  : ); SB();
  asm volatile("ds_read_b128 a[200:203], %0 offset:10240" :  : "v"(aO) : "memory"); SB();
  asm volatile("v_mfma_f32_16x16x32_bf16 %0, v[236:239], a[48:51], %0" : "+v"(g[0][0][1]) :  : ); SB();
  asm volatile("ds_read_b128 a[204:207], %0 offset:11264" :  : "v"(aO) : "memory"); SB();
  asm volatile("v_mfma_f32_16x16x32_bf16 %0, v[236:239], a[52:55], %0" : "+v"(g[0][1][1]) :  : ); SB();
  asm volatile("ds_read_b128 a[208:211], %0 offset:12288" :  : "v"(aO) : "memory"); SB();
  asm volatile("v_mfma_f32_16x16x32_bf16 %0, v[236:239], a[56:59], %0" : "+v"(g[0][2][1]) :  : ); SB();
  asm volatile("ds_read_b128 a[212:215], %0 offset:13312" :  : "v"(aO) : "memory"); SB();
  asm volatile("v_mfma_f32_16x16x32_bf16 %0, v[236:239], a[60:63], %0" : "+v"(g[0][3][1]) :  : ); SB();
  asm volatile("ds_read_b128 a[216:219], %0 offset:14336" :  : "v"(aO) : "memory"); SB();
  asm volatile("s_waitcnt vmcnt(10)" :  :  : "memory"); SB();
  asm volatile("v_mfma_f32_16x16x32_bf16 %0, v[240:243], a[64:67], %0" : "+v"(g[0][0][0]) :  : ); SB();
  asm volatile("buffer_load_dwordx4 %0, %1, %2 offen sc1 lds" :  : "v"(vO), "s"(rWS), "s"(86016u) : "memory"); SB();
  asm volatile("s_add_u32 m0, %0, 86016" :  : "s"(mO) : "memory", "scc"); SB();
  asm volatile("v_mfma_f32_16x16x32_bf16 %0, v[240:243], a[68:71], %0" : "+v"(g[0][1][0]) :  : ); SB();
  asm volatile("ds_read_b128 a[220:223], %0 offset:15360" :  : "v"(aO) : "memory"); SB();
  asm volatile("v_mfma_f32_16x16x32_bf16 %0, v[240:243], a[72:75], %0" : "+v"(g[0][2][0]) :  : ); SB();
  asm volatile("ds_read_b128 a[224:227], %0 offset:16384" :  : "v"(aO) : "memory"); SB();
  asm volatile("v_mfma_f32_16x16x32_bf16 %0, v[240:243], a[76:79], %0" : "+v"(g[0][3][0]) :  : ); SB();
  asm volatile("ds_read_b128 a[228:231], %0 offset:17408" :  : "v"(aO) : "memory"); SB();
  asm volatile("v_mfma_f32_16x16x32_bf16 %0, v[244:247], a[64:67], %0" : "+v"(g[0][0][1]) :  : ); SB();
  asm volatile("ds_read_b128 a[232:235], %0 offset:18432" :  : "v"(aO) : "memory"); SB();
  asm volatile("v_mfma_f32_16x16x32_bf16 %0, v[244:247], a[68:71], %0" : "+v"(g[0][1][1]) :  : ); SB();
  asm volatile("buffer_load_dwordx4 %0, %1, %2 offen offset:1024 sc1 lds" :  : "v"(vO), "s"(rWS), "s"(86016u) : "memory"); SB();
  asm volatile("s_add_u32 m0, %0, 86016" :  : "s"(mO) : "memory", "scc"); SB();
  asm volatile("v_mfma_f32_16x16x32_bf16 %0, v[244:247], a[72:75], %0" : "+v"(g[0][2][1]) :  : ); SB();
  asm volatile("ds_read_b128 a[236:239], %0 offset:19456" :  : "v"(aO) : "memory"); SB();
  asm volatile("v_mfma_f32_16x16x32_bf16 %0, v[244:247], a[76:79], %0" : "+v"(g[0][3][1]) :  : ); SB();
  asm volatile("buffer_load_dwordx4 %0, %1, %2 offen offset:2048 sc1 lds" :  : "v"(vO), "s"(rWS), "s"(86016u) : "memory"); SB();
  asm volatile("s_add_u32 m0, %0, 86016" :  : "s"(mO) : "memory", "scc"); SB();
  asm volatile("s_nop 0\n\tbuffer_load_dwordx4 %0, %1, %2 offen offset:3072 sc1 lds" :  : "v"(vO), "s"(rWS), "s"(86016u) : "memory"); SB();
  asm volatile("s_add_u32 m0, %0, 90112" :  : "s"(mO) : "memory", "scc"); SB();
  asm volatile("s_nop 0\n\tbuffer_load_dwordx4 %0, %1, %2 offen sc1 lds" :  : "v"(vO), "s"(rWS), "s"(90112u) : "memory"); SB();
  asm volatile("s_add_u32 m0, %0, 90112" :  : "s"(mO) : "memory", "scc"); SB();
  asm volatile("s_nop 0\n\tbuffer_load_dwordx4 %0, %1, %2 offen offset:1024 sc1 lds" :  : "v"(vO), "s"(rWS), "s"(90112u) : "memory"); SB();
  asm volatile("s_waitcnt vmcnt(12)" :  :  : "memory"); SB();
  asm volatile("v_mfma_f32_16x16x32_bf16 %0, v[248:251], a[80:83], %0" : "+v"(g[0][0][0]) :  : ); SB();
  asm volatile("buffer_load_dwordx4 v[200:203], %0, %1, %2 offen offset:0 sc1" :  : "v"(vB), "s"(rWS), "s"(643072u) : "memory"); SB();
  asm volatile("v_mfma_f32_16x16x32_bf16 %0, v[248:251], a[84:87], %0" : "+v"(g[0][1][0]) :  : ); SB();
  asm volatile("buffer_load_dwordx4 v[204:207], %0, %1, %2 offen offset:1024 sc1" :  : "v"(vB), "s"(rWS), "s"(643072u) : "memory"); SB();
  asm volatile("v_mfma_f32_16x16x32_bf16 %0, v[248:251], a[88:91], %0" : "+v"(g[0][2][0]) :  : ); SB();
  asm volatile("v_exp_f32 %0, %1" : "=v"(ge[2]) : "v"(gt[2]) : ); SB();
  asm volatile("v_mfma_f32_16x16x32_bf16 %0, v[248:251], a[92:95], %0" : "+v"(g[0][3][0]) :  : ); SB();
  asm volatile("v_exp_f32 %0, %1" : "=v"(ge[3]) : "v"(gt[3]) : ); SB();
  asm volatile("v_mfma_f32_16x16x32_bf16 %0, v[252:255], a[80:83], %0" : "+v"(g[0][0][1]) :  : ); SB();
  asm volatile("v_fma_f32 %0, %0, %1, %1" : "+v"(ge[0]) : "v"(xd[0]) : ); SB();
  asm volatile("v_mfma_f32_16x16x32_bf16 %0, v[252:255], a[84:87], %0" : "+v"(g[0][1][1]) :  : ); SB();
  asm volatile("buffer_load_dwordx4 v[208:211], %0, %1, %2 offen offset:0 sc1" :  : "v"(vB), "s"(rWS), "s"(692224u) : "memory"); SB();
  asm volatile("v_fma_f32 %0, %0, %1, %1" : "+v"(ge[1]) : "v"(xd[0]) : ); SB();
  asm volatile("v_mfma_f32_16x16x32_bf16 %0, v[252:255], a[88:91], %0" : "+v"(g[0][2][1]) :  : ); SB();
  asm volatile("v_fma_f32 %0, %0, %1, %1" : "+v"(ge[2]) : "v"(xd[0]) : ); SB();
  asm volatile("v_mfma_f32_16x16x32_bf16 %0, v[252:255], a[92:95], %0" : "+v"(g[0][3][1]) :  : ); SB();
  asm volatile("v_fma_f32 %0, %0, %1, %1" : "+v"(ge[3]) : "v"(xd[0]) : ); SB();
  ST(31);
  asm volatile("s_waitcnt vmcnt(13)" :  :  : "memory"); SB();
  asm volatile("v_mfma_f32_16x16x32_bf16 %0, v[192:195], a[96:99], %0" : "+v"(g[0][0][0]) :  : ); SB();
  asm volatile("buffer_load_dwordx4 v[212:215], %0, %1, %2 offen offset:1024 sc1" :  : "v"(vB), "s"(rWS), "s"(692224u) : "memory"); SB();
  asm volatile("v_mfma_f32_16x16x32_bf16 %0, v[192:195], a[100:103], %0" : "+v"(g[0][1][0]) :  : ); SB();
  asm volatile("v_rcp_f32 %0, %1" : "=v"(gt[0]) : "v"(ge[0]) : ); SB();
  asm volatile("v_mfma_f32_16x16x32_bf16 %0, v[192:195], a[104:107], %0" : "+v"(g[0][2][0]) :  : ); SB();
  asm volatile("v_rcp_f32 %0, %1" : "=v"(gt[1]) : "v"(ge[1]) : ); SB();
  asm volatile("v_mfma_f32_16x16x32_bf16 %0, v[192:195], a[108:111], %0" : "+v"(g[0][3][0]) :  : ); SB();
  asm volatile("v_rcp_f32 %0, %1" : "=v"(gt[2]) : "v"(ge[2]) : ); SB();
  asm volatile("v_mfma_f32_16x16x32_bf16 %0, v[196:199], a[96:99], %0" : "+v"(g[0][0][1]) :  : ); SB();
  asm volatile("s_waitcnt vmcnt(13)\n\t.Lc_%=:\n\tv_cmp_ne_u64 vcc, %2, %1\n\ts_cbranch_vccnz .Lr_%=\n\t.Ld_%=:\n\t.subsection 1\n\t.Lr_%=:\n\tv_add_u32 %0, -1, %0\n\ts_nop 1\n\tv_readfirstlane_b32 vcc_hi, %0\n\ts_nop 3\n\ts_cmp_lt_i32 vcc_hi, 1\n\ts_cbranch_scc1 .Ld_%=\n\ts_sleep 2\n\tbuffer_load_dwordx2 %1, %3, %4, 0 offen sc1\n\ts_waitcnt vmcnt(0)\n\ts_branch .Lc_%=\n\t.subsection 0" : "+v"(tmo), "+v"(fy) : "s"(tg), "v"(vFY), "s"(rWS) : "memory", "vcc", "scc"); SB();
  asm volatile("buffer_load_dwordx4 v[216:219], %0, %1, %2 offen offset:0 sc1" :  : "v"(vB), "s"(rWS), "s"(741376u) : "memory"); SB();
  asm volatile("v_mfma_f32_16x16x32_bf16 %0, v[196:199], a[100:103], %0" : "+v"(g[0][1][1]) :  : ); SB();
  asm volatile("v_rcp_f32 %0, %1" : "=v"(gt[3]) : "v"(ge[3]) : ); SB();
  asm volatile("v_mfma_f32_16x16x32_bf16 %0, v[196:199], a[104:107], %0" : "+v"(g[0][2][1]) :  : ); SB();
  asm volatile("v_mul_f32 %0, %0, %1" : "+v"(a1[1][1][0]) : "v"(gt[0]) : ); SB();
  asm volatile("v_mfma_f32_16x16x32_bf16 %0, v[196:199], a[108:111], %0" : "+v"(g[0][3][1]) :  : ); SB();
  asm volatile("v_mul_f32 %0, %0, %1" : "+v"(a1[1][1][1]) : "v"(gt[1]) : ); SB();
  asm volatile("buffer_load_dwordx4 v[220:223], %0, %1, %2 offen offset:1024 sc1" :  : "v"(vB), "s"(rWS), "s"(741376u) : "memory"); SB();
  asm volatile("buffer_load_dwordx4 v[160:163], %0, %1, %2 offen offset:0 sc1" :  : "v"(vHr[0]), "s"(rWS), "s"(0u) : "memory"); SB();
  asm volatile("buffer_load_dwordx4 v[164:167], %0, %1, %2 offen offset:0 sc1" :  : "v"(vHr[0]), "s"(rWS), "s"(3072u) : "memory"); SB();
  asm volatile("buffer_load_dwordx4 v[168:171], %0, %1, %2 offen offset:0 sc1" :  : "v"(vHr[0]), "s"(rWS), "s"(6144u) : "memory"); SB();
  asm volatile("buffer_load_dwordx4 v[172:175], %0, %1, %2 offen offset:0 sc1" :  : "v"(vHr[0]), "s"(rWS), "s"(9216u) : "memory"); SB();
  asm volatile("buffer_load_dwordx4 v[176:179], %0, %1, %2 offen offset:0 sc1" :  : "v"(vHr[1]), "s"(rWS), "s"(0u) : "memory"); SB();
  asm volatile("buffer_load_dwordx4 v[180:183], %0, %1, %2 offen offset:0 sc1" :  : "v"(vHr[1]), "s"(rWS), "s"(3072u) : "memory"); SB();
  asm volatile("buffer_load_dwordx4 v[184:187], %0, %1, %2 offen offset:0 sc1" :  : "v"(vHr[1]), "s"(rWS), "s"(6144u) : "memory"); SB();
  asm volatile("buffer_load_dwordx4 v[188:191], %0, %1, %2 offen offset:0 sc1" :  : "v"(vHr[1]), "s"(rWS), "s"(9216u) : "memory"); SB();
  asm volatile("s_waitcnt vmcnt(12)" :  :  : "memory"); SB();
  asm volatile("v_mfma_f32_16x16x32_bf16 %0, v[200:203], a[112:115], %0" : "+v"(g[0][0][0]) :  : ); SB();
  asm volatile("s_add_u32 m0, %0, 147456" :  : "s"(mY) : "memory", "scc"); SB();
  asm volatile("v_mul_f32 %0, %0, %1" : "+v"(a1[1][1][2]) : "v"(gt[2]) : ); SB();
  asm volatile("v_mfma_f32_16x16x32_bf16 %0, v[200:203], a[116:119], %0" : "+v"(g[0][1][0]) :  : ); SB();
  asm volatile("buffer_load_dwordx4 %0, %1, %2 offen sc1 lds" :  : "v"(vYr), "s"(rWS), "s"(0u) : "memory"); SB();
  asm volatile("s_add_u32 m0, %0, 148480" :  : "s"(mY) : "memory", "scc"); SB();
  asm volatile("v_mfma_f32_16x16x32_bf16 %0, v[200:203], a[120:123], %0" : "+v"(g[0][2][0]) :  : ); SB();
  asm volatile("buffer_load_dwordx4 %0, %1, %2 offen sc1 lds" :  : "v"(vYr), "s"(rWS), "s"(w3s + 32768u) : "memory"); SB();
  asm volatile("v_mul_f32 %0, %0, %1" : "+v"(a1[1][1][3]) : "v"(gt[3]) : ); SB();
  asm volatile("v_mfma_f32_16x16x32_bf16 %0, v[200:203], a[124:127], %0" : "+v"(g[0][3][0]) :  : ); SB();
  asm volatile("v_mul_f32 %0, %1, %1" : "=v"(gt[0]) : "v"(a1[1][2][0]) : ); SB();
  asm volatile("v_mfma_f32_16x16x32_bf16 %0, v[204:207], a[112:115], %0" : "+v"(g[0][0][1]) :  : ); SB();
  asm volatile("v_mul_f32 %0, %1, %1" : "=v"(gt[1]) : "v"(a1[1][2][1]) : ); SB();
  asm volatile("v_mfma_f32_16x16x32_bf16 %0, v[204:207], a[116:119], %0" : "+v"(g[0][1][1]) :  : ); SB();
  asm volatile("s_add_u32 m0, %0, 149504" :  : "s"(mY) : "memory", "scc"); SB();
  asm volatile("v_mul_f32 %0, %1, %1" : "=v"(gt[2]) : "v"(a1[1][2][2]) : ); SB();
  asm volatile("v_mfma_f32_16x16x32_bf16 %0, v[204:207], a[120:123], %0" : "+v"(g[0][2][1]) :  : ); SB();
  asm volatile("buffer_load_dwordx4 %0, %1, %2 offen sc1 lds" :  : "v"(vYr), "s"(rWS), "s"(w3s + 65536u) : "memory"); SB();
  asm volatile("v_mul_f32 %0, %1, %1" : "=v"(gt[3]) : "v"(a1[1][2][3]) : ); SB();
  asm volatile("v_mfma_f32_16x16x32_bf16 %0, v[204:207], a[124:127], %0" : "+v"(g[0][3][1]) :  : ); SB();
  asm volatile("v_fma_f32 %0, %0, %1, %2" : "+v"(gt[0]) : "v"(k2[0]), "v"(k1[0]) : ); SB();
  asm volatile("s_barrier" :  :  : "memory"); SB();
  asm volatile("s_waitcnt vmcnt(13)" :  :  : "memory"); SB();
  asm volatile("v_mfma_f32_16x16x32_bf16 %0, v[208:211], a[128:131], %0" : "+v"(g[0][0][0]) :  : ); SB();
  asm volatile("buffer_load_dwordx4 v[224:227], %0, %1, %2 offen offset:1024 sc1" :  : "v"(vHr[0]), "s"(rWS), "s"(0u) : "memory"); SB();
  asm volatile("v_fma_f32 %0, %0, %1, %2" : "+v"(gt[1]) : "v"(k2[0]), "v"(k1[0]) : ); SB();
  asm volatile("v_mfma_f32_16x16x32_bf16 %0, v[208:211], a[132:135], %0" : "+v"(g[0][1][0]) :  : ); SB();
  asm volatile("v_fma_f32 %0, %0, %1, %2" : "+v"(gt[2]) : "v"(k2[0]), "v"(k1[0]) : ); SB();
  asm volatile("v_mfma_f32_16x16x32_bf16 %0, v[208:211], a[136:139], %0" : "+v"(g[0][2][0]) :  : ); SB();
  asm volatile("v_fma_f32 %0, %0, %1, %2" : "+v"(gt[3]) : "v"(k2[0]), "v"(k1[0]) : ); SB();
  asm volatile("v_mfma_f32_16x16x32_bf16 %0, v[208:211], a[140:143], %0" : "+v"(g[0][3][0]) :  : ); SB();
  asm volatile("v_mul_f32 %0, %0, %1" : "+v"(gt[0]) : "v"(a1[1][2][0]) : ); SB();
  asm volatile("v_mfma_f32_16x16x32_bf16 %0, v[212:215], a[128:131], %0" : "+v"(g[0][0][1]) :  : ); SB();
  asm volatile("buffer_load_dwordx4 v[228:231], %0, %1, %2 offen offset:1024 sc1" :  : "v"(vHr[0]), "s"(rWS), "s"(3072u) : "memory"); SB();
  asm volatile("v_mul_f32 %0, %0, %1" : "+v"(gt[1]) : "v"(a1[1][2][1]) : ); SB();
  asm volatile("v_mfma_f32_16x16x32_bf16 %0, v[212:215], a[132:135], %0" : "+v"(g[0][1][1]) :  : ); SB();
  asm volatile("v_mul_f32 %0, %0, %1" : "+v"(gt[2]) : "v"(a1[1][2][2]) : ); SB();
  asm volatile("v_mfma_f32_16x16x32_bf16 %0, v[212:215], a[136:139], %0" : "+v"(g[0][2][1]) :  : ); SB();
  asm volatile("v_mul_f32 %0, %0, %1" : "+v"(gt[3]) : "v"(a1[1][2][3]) : ); SB();
  asm volatile("v_mfma_f32_16x16x32_bf16 %0, v[212:215], a[140:143], %0" : "+v"(g[0][3][1]) :  : ); SB();
  asm volatile("v_exp_f32 %0, %1" : "=v"(ge[0]) : "v"(gt[0]) : ); SB();
  asm volatile("s_waitcnt vmcnt(13)" :  :  : "memory"); SB();
  asm volatile("v_mfma_f32_16x16x32_bf16 %0, v[216:219], a[148:151], %0" : "+v"(g[0][1][0]) :  : ); SB();
  asm volatile("buffer_load_dwordx4 v[232:235], %0, %1, %2 offen offset:1024 sc1" :  : "v"(vHr[0]), "s"(rWS), "s"(6144u) : "memory"); SB();
  asm volatile("v_mfma_f32_16x16x32_bf16 %0, v[220:223], a[148:151], %0" : "+v"(g[0][1][1]) :  : ); SB();
  asm volatile("v_exp_f32 %0, %1" : "=v"(ge[1]) : "v"(gt[1]) : ); SB();
  asm volatile("v_mfma_f32_16x16x32_bf16 %0, v[216:219], a[152:155], %0" : "+v"(g[0][2][0]) :  : ); SB();
  asm volatile("v_exp_f32 %0, %1" : "=v"(ge[2]) : "v"(gt[2]) : ); SB();
  asm volatile("v_mfma_f32_16x16x32_bf16 %0, v[220:223], a[152:155], %0" : "+v"(g[0][2][1]) :  : ); SB();
  asm volatile("v_exp_f32 %0, %1" : "=v"(ge[3]) : "v"(gt[3]) : ); SB();
  asm volatile("v_mfma_f32_16x16x32_bf16 %0, v[216:219], a[156:159], %0" : "+v"(g[0][3][0]) :  : ); SB();
  asm volatile("v_fma_f32 %0, %0, %1, %1" : "+v"(ge[0]) : "v"(xd[0]) : ); SB();
  asm volatile("v_mfma_f32_16x16x32_bf16 %0, v[220:223], a[156:159], %0" : "+v"(g[0][3][1]) :  : ); SB();
  asm volatile("buffer_load_dwordx4 v[236:239], %0, %1, %2 offen offset:1024 sc1" :  : "v"(vHr[0]), "s"(rWS), "s"(9216u) : "memory"); SB();
  asm volatile("v_fma_f32 %0, %0, %1, %1" : "+v"(ge[1]) : "v"(xd[0]) : ); SB();
  asm volatile("v_mfma_f32_16x16x32_bf16 %0, v[216:219], a[144:147], %0" : "+v"(g[0][0][0]) :  : ); SB();
  asm volatile("v_fma_f32 %0, %0, %1, %1" : "+v"(ge[2]) : "v"(xd[0]) : ); SB();
  asm volatile("v_mfma_f32_16x16x32_bf16 %0, v[220:223], a[144:147], %0" : "+v"(g[0][0][1]) :  : ); SB();
  asm volatile("v_fma_f32 %0, %0, %1, %1" : "+v"(ge[3]) : "v"(xd[0]) : ); SB();
  asm volatile("s_nop 1" : "+v"(g[0][1][0]) :  : ); SB();
  asm volatile("ds_write_b128 %0, %1 offset:0" :  : "v"(pw2), "v"(g[0][1][0]) : "memory"); SB();
  asm volatile("ds_write_b128 %0, %1 offset:1024" :  : "v"(pw2), "v"(g[0][1][1]) : "memory"); SB();
  asm volatile("ds_write_b128 %0, %1 offset:2048" :  : "v"(pw2), "v"(g[0][2][0]) : "memory"); SB();
  asm volatile("ds_write_b128 %0, %1 offset:3072" :  : "v"(pw2), "v"(g[0][2][1]) : "memory"); SB();
  asm volatile("ds_write_b128 %0, %1 offset:4096" :  : "v"(pw2), "v"(g[0][3][0]) : "memory"); SB();
  asm volatile("ds_write_b128 %0, %1 offset:5120" :  : "v"(pw2), "v"(g[0][3][1]) : "memory"); SB();
  asm volatile("v_rcp_f32 %0, %1" : "=v"(gt[0]) : "v"(ge[0]) : ); SB();
  asm volatile("v_rcp_f32 %0, %1" : "=v"(gt[1]) : "v"(ge[1]) : ); SB();
  asm volatile("v_rcp_f32 %0, %1" : "=v"(gt[2]) : "v"(ge[2]) : ); SB();
  asm volatile("v_rcp_f32 %0, %1" : "=v"(gt[3]) : "v"(ge[3]) : ); SB();
  asm volatile("v_mul_f32 %0, %0, %1" : "+v"(a1[1][2][0]) : "v"(gt[0]) : ); SB();
  asm volatile("v_mul_f32 %0, %0, %1" : "+v"(a1[1][2][1]) : "v"(gt[1]) : ); SB();
  asm volatile("v_mul_f32 %0, %0, %1" : "+v"(a1[1][2][2]) : "v"(gt[2]) : ); SB();
  asm volatile("v_mul_f32 %0, %0, %1" : "+v"(a1[1][2][3]) : "v"(gt[3]) : ); SB();
  ST(17);
  asm volatile("s_waitcnt lgkmcnt(0)\n\ts_barrier" :  :  : "memory"); SB();
  asm volatile("ds_read_b128 %0, %1 offset:0" : "=&v"(pr[0]) : "v"(pe[0]) : "memory"); SB();
  asm volatile("ds_read_b128 %0, %1 offset:1024" : "=&v"(pr[1]) : "v"(pe[0]) : "memory"); SB();
  asm volatile("ds_read_b128 %0, %1 offset:2048" : "=&v"(pr[2]) : "v"(pe[1]) : "memory"); SB();
  asm volatile("ds_read_b128 %0, %1 offset:3072" : "=&v"(pr[3]) : "v"(pe[1]) : "memory"); SB();
  asm volatile("ds_read_b128 %0, %1 offset:4096" : "=&v"(pr[4]) : "v"(pe[2]) : "memory"); SB();
  asm volatile("ds_read_b128 %0, %1 offset:5120" : "=&v"(pr[5]) : "v"(pe[2]) : "memory"); SB();
  asm volatile("s_waitcnt lgkmcnt(0)" : "+v"(pr[0]), "+v"(pr[1]), "+v"(pr[2]), "+v"(pr[3]), "+v"(pr[4]), "+v"(pr[5]), "+v"(g[0][0][0]), "+v"(g[0][0][1]) :  : "memory"); SB();
  {
    const f32x4 h0 = ((g[0][0][0] + pr[0]) + (pr[2] + pr[4])) * a1[1][1], h1 = ((g[0][0][1] + pr[1]) + (pr[3] + pr[5])) * a1[1][2];
    ho = (u32x4){pack2(h0[0], h0[1]), pack2(h0[2], h0[3]), pack2(h1[0], h1[1]), nohole(pack2(h1[2], h1[3]))};
    SB();
  }
  asm volatile("buffer_store_dwordx4 %1, %0, %2, %3 offen offset:2048 sc1\n\ts_nop 1" :  : "v"(vH_), "v"(ho), "s"(rWS), "s"(0u) : "memory"); SB();
  if ((rb | cb | w) == 0) asm volatile("buffer_store_dwordx2 %0, off, %1, 0 sc1" :: "v"(((u32x2){tg0, tg1})), "s"(rWS) : "memory");
  SB();
  f32x4 o3[2][5], pq[5];
  f32x2 xf[5][2], sv[5][2];
  ST(18);
  ST(19);
  asm volatile("s_waitcnt vmcnt(12)\n\t.Lc_%=:\n\tv_max3_u32 %1, v163, v167, v171\n\tv_max_u32 %1, %1, v175\n\tv_cmp_eq_u32 vcc, -1, %1\n\ts_cbranch_vccnz .Lr_%=\n\t.Ld_%=:\n\t.subsection 1\n\t.Lr_%=:\n\tv_add_u32 %0, -1, %0\n\ts_nop 1\n\tv_readfirstlane_b32 vcc_hi, %0\n\ts_nop 3\n\ts_cmp_lt_i32 vcc_hi, 1\n\ts_cbranch_scc1 .Ld_%=\n\ts_sleep 2\n\ts_mov_b32 vcc_lo, 0\n\tbuffer_load_dwordx4 v[160:163], %2, %3, vcc_lo offen offset:0 sc1\n\ts_mov_b32 vcc_lo, 3072\n\tbuffer_load_dwordx4 v[164:167], %2, %3, vcc_lo offen offset:0 sc1\n\ts_mov_b32 vcc_lo, 6144\n\tbuffer_load_dwordx4 v[168:171], %2, %3, vcc_lo offen offset:0 sc1\n\ts_mov_b32 vcc_lo, 9216\n\tbuffer_load_dwordx4 v[172:175], %2, %3, vcc_lo offen offset:0 sc1\n\ts_waitcnt vmcnt(0)\n\ts_branch .Lc_%=\n\t.subsection 0" : "+v"(tmo), "=&v"(vtmp) : "v"(vHr[0]), "s"(rWS) : "memory", "vcc", "scc"); SB();
  asm volatile("s_barrier" :  :  : "memory"); SB();
  asm volatile("v_mfma_f32_16x16x32_bf16 %0, a[160:163], v[160:163], 0" : "=&v"(o3[0][0]) :  : ); SB();
  asm volatile("buffer_load_dwordx4 v[240:243], %0, %1, %2 offen offset:1024 sc1" :  : "v"(vHr[1]), "s"(rWS), "s"(0u) : "memory"); SB();
  asm volatile("v_mfma_f32_16x16x32_bf16 %0, a[164:167], v[160:163], 0" : "=&v"(o3[0][1]) :  : ); SB();
  asm volatile("buffer_load_dwordx4 v[244:247], %0, %1, %2 offen offset:1024 sc1" :  : "v"(vHr[1]), "s"(rWS), "s"(3072u) : "memory"); SB();
  asm volatile("v_mfma_f32_16x16x32_bf16 %0, a[168:171], v[160:163], 0" : "=&v"(o3[0][2]) :  : ); SB();
  asm volatile("buffer_load_dwordx4 v[248:251], %0, %1, %2 offen offset:1024 sc1" :  : "v"(vHr[1]), "s"(rWS), "s"(6144u) : "memory"); SB();
  asm volatile("v_mfma_f32_16x16x32_bf16 %0, a[172:175], v[160:163], 0" : "=&v"(o3[0][3]) :  : ); SB();
  asm volatile("buffer_load_dwordx4 v[252:255], %0, %1, %2 offen offset:1024 sc1" :  : "v"(vHr[1]), "s"(rWS), "s"(9216u) : "memory"); SB();
  asm volatile("v_mfma_f32_16x16x32_bf16 %0, a[176:179], v[160:163], 0" : "=&v"(o3[0][4]) :  : ); SB();
  asm volatile("ds_read_b128 a[240:243], %0 offset:40960" :  : "v"(aO) : "memory"); SB();
  asm volatile("v_mfma_f32_16x16x32_bf16 %0, a[180:183], v[164:167], %0" : "+v"(o3[0][0]) :  : ); SB();
  asm volatile("ds_read_b128 a[244:247], %0 offset:41984" :  : "v"(aO) : "memory"); SB();
  asm volatile("v_mfma_f32_16x16x32_bf16 %0, a[184:187], v[164:167], %0" : "+v"(o3[0][1]) :  : ); SB();
  asm volatile("ds_read_b128 a[248:251], %0 offset:43008" :  : "v"(aO) : "memory"); SB();
  asm volatile("v_mfma_f32_16x16x32_bf16 %0, a[188:191], v[164:167], %0" : "+v"(o3[0][2]) :  : ); SB();
  asm volatile("ds_read_b128 a[252:255], %0 offset:44032" :  : "v"(aO) : "memory"); SB();
  asm volatile("v_mfma_f32_16x16x32_bf16 %0, a[192:195], v[164:167], %0" : "+v"(o3[0][3]) :  : ); SB();
  asm volatile("ds_read_b128 a[0:3], %0 offset:45056" :  : "v"(aO) : "memory"); SB();
  asm volatile("v_mfma_f32_16x16x32_bf16 %0, a[196:199], v[164:167], %0" : "+v"(o3[0][4]) :  : ); SB();
  asm volatile("ds_read_b128 a[4:7], %0 offset:46080" :  : "v"(aO) : "memory"); SB();
  asm volatile("v_mfma_f32_16x16x32_bf16 %0, a[200:203], v[168:171], %0" : "+v"(o3[0][0]) :  : ); SB();
  asm volatile("ds_read_b128 a[8:11], %0 offset:47104" :  : "v"(aO) : "memory"); SB();
  asm volatile("v_mfma_f32_16x16x32_bf16 %0, a[204:207], v[168:171], %0" : "+v"(o3[0][1]) :  : ); SB();
  asm volatile("ds_read_b128 a[12:15], %0 offset:48128" :  : "v"(aO) : "memory"); SB();
  asm volatile("v_mfma_f32_16x16x32_bf16 %0, a[208:211], v[168:171], %0" : "+v"(o3[0][2]) :  : ); SB();
  asm volatile("ds_read_b128 a[16:19], %0 offset:49152" :  : "v"(aO) : "memory"); SB();
  asm volatile("v_mfma_f32_16x16x32_bf16 %0, a[212:215], v[168:171], %0" : "+v"(o3[0][3]) :  : ); SB();
  asm volatile("ds_read_b128 a[20:23], %0 offset:50176" :  : "v"(aO) : "memory"); SB();
  asm volatile("v_mfma_f32_16x16x32_bf16 %0, a[216:219], v[168:171], %0" : "+v"(o3[0][4]) :  : ); SB();
  asm volatile("ds_read_b128 a[24:27], %0 offset:51200" :  : "v"(aO) : "memory"); SB();
  asm volatile("v_mfma_f32_16x16x32_bf16 %0, a[220:223], v[172:175], %0" : "+v"(o3[0][0]) :  : ); SB();
  asm volatile("ds_read_b128 a[28:31], %0 offset:52224" :  : "v"(aO) : "memory"); SB();
  asm volatile("v_mfma_f32_16x16x32_bf16 %0, a[224:227], v[172:175], %0" : "+v"(o3[0][1]) :  : ); SB();
  asm volatile("s_waitcnt vmcnt(12)\n\t.Lc_%=:\n\tv_max3_u32 %1, v179, v183, v187\n\tv_max_u32 %1, %1, v191\n\tv_cmp_eq_u32 vcc, -1, %1\n\ts_cbranch_vccnz .Lr_%=\n\t.Ld_%=:\n\t.subsection 1\n\t.Lr_%=:\n\tv_add_u32 %0, -1, %0\n\ts_nop 1\n\tv_readfirstlane_b32 vcc_hi, %0\n\ts_nop 3\n\ts_cmp_lt_i32 vcc_hi, 1\n\ts_cbranch_scc1 .Ld_%=\n\ts_sleep 2\n\ts_mov_b32 vcc_lo, 0\n\tbuffer_load_dwordx4 v[176:179], %2, %3, vcc_lo offen offset:0 sc1\n\ts_mov_b32 vcc_lo, 3072\n\tbuffer_load_dwordx4 v[180:183], %2, %3, vcc_lo offen offset:0 sc1\n\ts_mov_b32 vcc_lo, 6144\n\tbuffer_load_dwordx4 v[184:187], %2, %3, vcc_lo offen offset:0 sc1\n\ts_mov_b32 vcc_lo, 9216\n\tbuffer_load_dwordx4 v[188:191], %2, %3, vcc_lo offen offset:0 sc1\n\ts_waitcnt vmcnt(0)\n\ts_branch .Lc_%=\n\t.subsection 0" : "+v"(tmo), "=&v"(vtmp) : "v"(vHr[1]), "s"(rWS) : "memory", "vcc", "scc"); SB();
  asm volatile("v_mfma_f32_16x16x32_bf16 %0, a[228:231], v[172:175], %0" : "+v"(o3[0][2]) :  : ); SB();
  asm volatile("ds_read_b128 a[32:35], %0 offset:53248" :  : "v"(aO) : "memory"); SB();
  asm volatile("v_mfma_f32_16x16x32_bf16 %0, a[232:235], v[172:175], %0" : "+v"(o3[0][3]) :  : ); SB();
  asm volatile("ds_read_b128 a[36:39], %0 offset:54272" :  : "v"(aO) : "memory"); SB();
  asm volatile("v_mfma_f32_16x16x32_bf16 %0, a[236:239], v[172:175], %0" : "+v"(o3[0][4]) :  : ); SB();
  asm volatile("ds_read_b128 a[40:43], %0 offset:55296" :  : "v"(aO) : "memory"); SB();
  asm volatile("v_mfma_f32_16x16x32_bf16 %0, a[160:163], v[176:179], 0" : "=&v"(o3[1][0]) :  : ); SB();
  asm volatile("ds_read_b128 a[44:47], %0 offset:56320" :  : "v"(aO) : "memory"); SB();
  asm volatile("v_mfma_f32_16x16x32_bf16 %0, a[164:167], v[176:179], 0" : "=&v"(o3[1][1]) :  : ); SB();
  asm volatile("ds_read_b128 a[48:51], %0 offset:57344" :  : "v"(aO) : "memory"); SB();
  asm volatile("v_mfma_f32_16x16x32_bf16 %0, a[168:171], v[176:179], 0" : "=&v"(o3[1][2]) :  : ); SB();
  asm volatile("ds_read_b128 a[52:55], %0 offset:58368" :  : "v"(aO) : "memory"); SB();
  asm volatile("v_mfma_f32_16x16x32_bf16 %0, a[172:175], v[176:179], 0" : "=&v"(o3[1][3]) :  : ); SB();
  asm volatile("ds_read_b128 a[56:59], %0 offset:59392" :  : "v"(aO) : "memory"); SB();
  asm volatile("v_mfma_f32_16x16x32_bf16 %0, a[176:179], v[176:179], 0" : "=&v"(o3[1][4]) :  : ); SB();
  asm volatile("ds_read_b128 a[60:63], %0 offset:60416" :  : "v"(aO) : "memory"); SB();
  asm volatile("v_mfma_f32_16x16x32_bf16 %0, a[180:183], v[180:183], %0" : "+v"(o3[1][0]) :  : ); SB();
  asm volatile("v_mfma_f32_16x16x32_bf16 %0, a[184:187], v[180:183], %0" : "+v"(o3[1][1]) :  : ); SB();
  asm volatile("v_mfma_f32_16x16x32_bf16 %0, a[188:191], v[180:183], %0" : "+v"(o3[1][2]) :  : ); SB();
  asm volatile("v_mfma_f32_16x16x32_bf16 %0, a[192:195], v[180:183], %0" : "+v"(o3[1][3]) :  : ); SB();
  asm volatile("v_mfma_f32_16x16x32_bf16 %0, a[196:199], v[180:183], %0" : "+v"(o3[1][4]) :  : ); SB();
  asm volatile("v_mfma_f32_16x16x32_bf16 %0, a[200:203], v[184:187], %0" : "+v"(o3[1][0]) :  : ); SB();
  asm volatile("v_mfma_f32_16x16x32_bf16 %0, a[204:207], v[184:187], %0" : "+v"(o3[1][1]) :  : ); SB();
  asm volatile("v_mfma_f32_16x16x32_bf16 %0, a[208:211], v[184:187], %0" : "+v"(o3[1][2]) :  : ); SB();
  asm volatile("v_mfma_f32_16x16x32_bf16 %0, a[212:215], v[184:187], %0" : "+v"(o3[1][3]) :  : ); SB();
  asm volatile("v_mfma_f32_16x16x32_bf16 %0, a[216:219], v[184:187], %0" : "+v"(o3[1][4]) :  : ); SB();
  asm volatile("v_mfma_f32_16x16x32_bf16 %0, a[220:223], v[188:191], %0" : "+v"(o3[1][0]) :  : ); SB();
  asm volatile("v_mfma_f32_16x16x32_bf16 %0, a[224:227], v[188:191], %0" : "+v"(o3[1][1]) :  : ); SB();
  asm volatile("v_mfma_f32_16x16x32_bf16 %0, a[228:231], v[188:191], %0" : "+v"(o3[1][2]) :  : ); SB();
  asm volatile("v_mfma_f32_16x16x32_bf16 %0, a[232:235], v[188:191], %0" : "+v"(o3[1][3]) :  : ); SB();
  asm volatile("v_mfma_f32_16x16x32_bf16 %0, a[236:239], v[188:191], %0" : "+v"(o3[1][4]) :  : ); SB();
  ST(20);
  asm volatile("s_waitcnt vmcnt(5)\n\t.Lc_%=:\n\tv_max3_u32 %1, v227, v231, v235\n\tv_max_u32 %1, %1, v239\n\tv_cmp_eq_u32 vcc, -1, %1\n\ts_cbranch_vccnz .Lr_%=\n\t.Ld_%=:\n\t.subsection 1\n\t.Lr_%=:\n\tv_add_u32 %0, -1, %0\n\ts_nop 1\n\tv_readfirstlane_b32 vcc_hi, %0\n\ts_nop 3\n\ts_cmp_lt_i32 vcc_hi, 1\n\ts_cbranch_scc1 .Ld_%=\n\ts_sleep 2\n\ts_mov_b32 vcc_lo, 0\n\tbuffer_load_dwordx4 v[224:227], %2, %3, vcc_lo offen offset:1024 sc1\n\ts_mov_b32 vcc_lo, 3072\n\tbuffer_load_dwordx4 v[228:231], %2, %3, vcc_lo offen offset:1024 sc1\n\ts_mov_b32 vcc_lo, 6144\n\tbuffer_load_dwordx4 v[232:235], %2, %3, vcc_lo offen offset:1024 sc1\n\ts_mov_b32 vcc_lo, 9216\n\tbuffer_load_dwordx4 v[236:239], %2, %3, vcc_lo offen offset:1024 sc1\n\ts_waitcnt vmcnt(0)\n\ts_branch .Lc_%=\n\t.subsection 0" : "+v"(tmo), "=&v"(vtmp) : "v"(vHr[0]), "s"(rWS) : "memory", "vcc", "scc"); SB();
  asm volatile("s_barrier" :  :  : "memory"); SB();
  asm volatile("s_waitcnt lgkmcnt(15)\n\tv_mfma_f32_16x16x32_bf16 %0, a[240:243], v[224:227], %0" : "+v"(o3[0][0]) :  : ); SB();
  asm volatile("ds_read_b128 a[64:67], %0 offset:0" :  : "v"((aO + 81920)) : "memory"); SB();
  asm volatile("v_mfma_f32_16x16x32_bf16 %0, a[244:247], v[224:227], %0" : "+v"(o3[0][1]) :  : ); SB();
  asm volatile("ds_read_b128 a[68:71], %0 offset:1024" :  : "v"((aO + 81920)) : "memory"); SB();
  asm volatile("v_mfma_f32_16x16x32_bf16 %0, a[248:251], v[224:227], %0" : "+v"(o3[0][2]) :  : ); SB();
  asm volatile("ds_read_b128 a[72:75], %0 offset:2048" :  : "v"((aO + 81920)) : "memory"); SB();
  asm volatile("v_mfma_f32_16x16x32_bf16 %0, a[252:255], v[224:227], %0" : "+v"(o3[0][3]) :  : ); SB();
  asm volatile("ds_read_b128 a[76:79], %0 offset:3072" :  : "v"((aO + 81920)) : "memory"); SB();
  asm volatile("v_mfma_f32_16x16x32_bf16 %0, a[0:3], v[224:227], %0" : "+v"(o3[0][4]) :  : ); SB();
  asm volatile("ds_read_b128 a[80:83], %0 offset:4096" :  : "v"((aO + 81920)) : "memory"); SB();
  asm volatile("s_waitcnt lgkmcnt(15)\n\tv_mfma_f32_16x16x32_bf16 %0, a[4:7], v[228:231], %0" : "+v"(o3[0][0]) :  : ); SB();
  asm volatile("ds_read_b128 a[84:87], %0 offset:5120" :  : "v"((aO + 81920)) : "memory"); SB();
  asm volatile("v_mfma_f32_16x16x32_bf16 %0, a[8:11], v[228:231], %0" : "+v"(o3[0][1]) :  : ); SB();
  asm volatile("ds_read_b128 a[88:91], %0 offset:6144" :  : "v"((aO + 81920)) : "memory"); SB();
  asm volatile("v_mfma_f32_16x16x32_bf16 %0, a[12:15], v[228:231], %0" : "+v"(o3[0][2]) :  : ); SB();
  asm volatile("ds_read_b128 a[92:95], %0 offset:7168" :  : "v"((aO + 81920)) : "memory"); SB();
  asm volatile("v_mfma_f32_16x16x32_bf16 %0, a[16:19], v[228:231], %0" : "+v"(o3[0][3]) :  : ); SB();
  asm volatile("ds_read_b128 a[96:99], %0 offset:8192" :  : "v"((aO + 81920)) : "memory"); SB();
  asm volatile("v_mfma_f32_16x16x32_bf16 %0, a[20:23], v[228:231], %0" : "+v"(o3[0][4]) :  : ); SB();
  asm volatile("ds_read_b128 a[100:103], %0 offset:9216" :  : "v"((aO + 81920)) : "memory"); SB();
  asm volatile("s_waitcnt lgkmcnt(15)\n\tv_mfma_f32_16x16x32_bf16 %0, a[24:27], v[232:235], %0" : "+v"(o3[0][0]) :  : ); SB();
  asm volatile("ds_read_b128 a[104:107], %0 offset:10240" :  : "v"((aO + 81920)) : "memory"); SB();
  asm volatile("v_mfma_f32_16x16x32_bf16 %0, a[28:31], v[232:235], %0" : "+v"(o3[0][1]) :  : ); SB();
  asm volatile("ds_read_b128 a[108:111], %0 offset:11264" :  : "v"((aO + 81920)) : "memory"); SB();
  asm volatile("v_mfma_f32_16x16x32_bf16 %0, a[32:35], v[232:235], %0" : "+v"(o3[0][2]) :  : ); SB();
  asm volatile("ds_read_b128 a[112:115], %0 offset:12288" :  : "v"((aO + 81920)) : "memory"); SB();
  asm volatile("v_mfma_f32_16x16x32_bf16 %0, a[36:39], v[232:235], %0" : "+v"(o3[0][3]) :  : ); SB();
  asm volatile("ds_read_b128 a[116:119], %0 offset:13312" :  : "v"((aO + 81920)) : "memory"); SB();
  asm volatile("v_mfma_f32_16x16x32_bf16 %0, a[40:43], v[232:235], %0" : "+v"(o3[0][4]) :  : ); SB();
  asm volatile("ds_read_b128 a[120:123], %0 offset:14336" :  : "v"((aO + 81920)) : "memory"); SB();
  asm volatile("s_waitcnt lgkmcnt(15)\n\tv_mfma_f32_16x16x32_bf16 %0, a[44:47], v[236:239], %0" : "+v"(o3[0][0]) :  : ); SB();
  asm volatile("ds_read_b128 a[124:127], %0 offset:15360" :  : "v"((aO + 81920)) : "memory"); SB();
  asm volatile("v_mfma_f32_16x16x32_bf16 %0, a[48:51], v[236:239], %0" : "+v"(o3[0][1]) :  : ); SB();
  asm volatile("s_waitcnt vmcnt(0)\n\t.Lc_%=:\n\tv_max3_u32 %1, v243, v247, v251\n\tv_max_u32 %1, %1, v255\n\tv_cmp_eq_u32 vcc, -1, %1\n\ts_cbranch_vccnz .Lr_%=\n\t.Ld_%=:\n\t.subsection 1\n\t.Lr_%=:\n\tv_add_u32 %0, -1, %0\n\ts_nop 1\n\tv_readfirstlane_b32 vcc_hi, %0\n\ts_nop 3\n\ts_cmp_lt_i32 vcc_hi, 1\n\ts_cbranch_scc1 .Ld_%=\n\ts_sleep 2\n\ts_mov_b32 vcc_lo, 0\n\tbuffer_load_dwordx4 v[240:243], %2, %3, vcc_lo offen offset:1024 sc1\n\ts_mov_b32 vcc_lo, 3072\n\tbuffer_load_dwordx4 v[244:247], %2, %3, vcc_lo offen offset:1024 sc1\n\ts_mov_b32 vcc_lo, 6144\n\tbuffer_load_dwordx4 v[248:251], %2, %3, vcc_lo offen offset:1024 sc1\n\ts_mov_b32 vcc_lo, 9216\n\tbuffer_load_dwordx4 v[252:255], %2, %3, vcc_lo offen offset:1024 sc1\n\ts_waitcnt vmcnt(0)\n\ts_branch .Lc_%=\n\t.subsection 0" : "+v"(tmo), "=&v"(vtmp) : "v"(vHr[1]), "s"(rWS) : "memory", "vcc", "scc"); SB();
  asm volatile("v_mfma_f32_16x16x32_bf16 %0, a[52:55], v[236:239], %0" : "+v"(o3[0][2]) :  : ); SB();
  asm volatile("ds_read_b128 a[128:131], %0 offset:16384" :  : "v"((aO + 81920)) : "memory"); SB();
  asm volatile("v_mfma_f32_16x16x32_bf16 %0, a[56:59], v[236:239], %0" : "+v"(o3[0][3]) :  : ); SB();
  asm volatile("ds_read_b128 a[132:135], %0 offset:17408" :  : "v"((aO + 81920)) : "memory"); SB();
  asm volatile("v_mfma_f32_16x16x32_bf16 %0, a[60:63], v[236:239], %0" : "+v"(o3[0][4]) :  : ); SB();
  asm volatile("ds_read_b128 a[136:139], %0 offset:18432" :  : "v"((aO + 81920)) : "memory"); SB();
  asm volatile("v_mfma_f32_16x16x32_bf16 %0, a[240:243], v[240:243], %0" : "+v"(o3[1][0]) :  : ); SB();
  asm volatile("ds_read_b128 a[140:143], %0 offset:19456" :  : "v"((aO + 81920)) : "memory"); SB();
  asm volatile("v_mfma_f32_16x16x32_bf16 %0, a[244:247], v[240:243], %0" : "+v"(o3[1][1]) :  : ); SB();
  asm volatile("v_mfma_f32_16x16x32_bf16 %0, a[248:251], v[240:243], %0" : "+v"(o3[1][2]) :  : ); SB();
  asm volatile("v_mfma_f32_16x16x32_bf16 %0, a[252:255], v[240:243], %0" : "+v"(o3[1][3]) :  : ); SB();
  asm volatile("v_mfma_f32_16x16x32_bf16 %0, a[0:3], v[240:243], %0" : "+v"(o3[1][4]) :  : ); SB();
  asm volatile("v_mfma_f32_16x16x32_bf16 %0, a[4:7], v[244:247], %0" : "+v"(o3[1][0]) :  : ); SB();
  asm volatile("v_mfma_f32_16x16x32_bf16 %0, a[8:11], v[244:247], %0" : "+v"(o3[1][1]) :  : ); SB();
  asm volatile("v_mfma_f32_16x16x32_bf16 %0, a[12:15], v[244:247], %0" : "+v"(o3[1][2]) :  : ); SB();
  asm volatile("buffer_load_dwordx4 v[192:195], %0, %1, %2 offen offset:2048 sc1" :  : "v"(vHr[0]), "s"(rWS), "s"(0u) : "memory"); SB();
  asm volatile("v_mfma_f32_16x16x32_bf16 %0, a[16:19], v[244:247], %0" : "+v"(o3[1][3]) :  : ); SB();
  asm volatile("buffer_load_dwordx4 v[196:199], %0, %1, %2 offen offset:2048 sc1" :  : "v"(vHr[0]), "s"(rWS), "s"(3072u) : "memory"); SB();
  asm volatile("v_mfma_f32_16x16x32_bf16 %0, a[20:23], v[244:247], %0" : "+v"(o3[1][4]) :  : ); SB();
  asm volatile("buffer_load_dwordx4 v[200:203], %0, %1, %2 offen offset:2048 sc1" :  : "v"(vHr[0]), "s"(rWS), "s"(6144u) : "memory"); SB();
  asm volatile("v_mfma_f32_16x16x32_bf16 %0, a[24:27], v[248:251], %0" : "+v"(o3[1][0]) :  : ); SB();
  asm volatile("buffer_load_dwordx4 v[204:207], %0, %1, %2 offen offset:2048 sc1" :  : "v"(vHr[0]), "s"(rWS), "s"(9216u) : "memory"); SB();
  asm volatile("v_mfma_f32_16x16x32_bf16 %0, a[28:31], v[248:251], %0" : "+v"(o3[1][1]) :  : ); SB();
  asm volatile("buffer_load_dwordx4 v[208:211], %0, %1, %2 offen offset:2048 sc1" :  : "v"(vHr[1]), "s"(rWS), "s"(0u) : "memory"); SB();
  asm volatile("v_mfma_f32_16x16x32_bf16 %0, a[32:35], v[248:251], %0" : "+v"(o3[1][2]) :  : ); SB();
  asm volatile("buffer_load_dwordx4 v[212:215], %0, %1, %2 offen offset:2048 sc1" :  : "v"(vHr[1]), "s"(rWS), "s"(3072u) : "memory"); SB();
  asm volatile("v_mfma_f32_16x16x32_bf16 %0, a[36:39], v[248:251], %0" : "+v"(o3[1][3]) :  : ); SB();
  asm volatile("buffer_load_dwordx4 v[216:219], %0, %1, %2 offen offset:2048 sc1" :  : "v"(vHr[1]), "s"(rWS), "s"(6144u) : "memory"); SB();
  asm volatile("v_mfma_f32_16x16x32_bf16 %0, a[40:43], v[248:251], %0" : "+v"(o3[1][4]) :  : ); SB();
  asm volatile("buffer_load_dwordx4 v[220:223], %0, %1, %2 offen offset:2048 sc1" :  : "v"(vHr[1]), "s"(rWS), "s"(9216u) : "memory"); SB();
  asm volatile("v_mfma_f32_16x16x32_bf16 %0, a[44:47], v[252:255], %0" : "+v"(o3[1][0]) :  : ); SB();
  asm volatile("v_mfma_f32_16x16x32_bf16 %0, a[48:51], v[252:255], %0" : "+v"(o3[1][1]) :  : ); SB();
  asm volatile("v_mfma_f32_16x16x32_bf16 %0, a[52:55], v[252:255], %0" : "+v"(o3[1][2]) :  : ); SB();
  asm volatile("v_mfma_f32_16x16x32_bf16 %0, a[56:59], v[252:255], %0" : "+v"(o3[1][3]) :  : ); SB();
  asm volatile("v_mfma_f32_16x16x32_bf16 %0, a[60:63], v[252:255], %0" : "+v"(o3[1][4]) :  : ); SB();
  ST(21);
  {
    const unsigned ya = lb + 147456 + (4 * g4 + q4) * 128 + ((w ^ ((2 * g4 + (q4 >> 1)) & 3)) << 5) + 8 * p4;
    u32x2 yr[5];
  asm volatile("ds_read_b64_tr_b16 %0, %1 offset:0" : "=&v"(yr[0]) : "v"(ya) : "memory"); SB();
  asm volatile("ds_read_b64_tr_b16 %0, %1 offset:2048" : "=&v"(yr[1]) : "v"(ya) : "memory"); SB();
  asm volatile("ds_read_b64_tr_b16 %0, %1 offset:4096" : "=&v"(yr[2]) : "v"(ya) : "memory"); SB();
  asm volatile("ds_read_b64_tr_b16 %0, %1 offset:6144" : "=&v"(yr[3]) : "v"(ya) : "memory"); SB();
  asm volatile("ds_read_b64_tr_b16 %0, %1 offset:8192" : "=&v"(yr[4]) : "v"(ya) : "memory"); SB();
  asm volatile("s_waitcnt lgkmcnt(0)" : "+v"(yr[0]), "+v"(yr[1]), "+v"(yr[2]), "+v"(yr[3]), "+v"(yr[4]) :  : "memory"); SB();
    asm volatile("" : "+v"(xm[0]), "+v"(xd[0]));
    const float rd = __builtin_amdgcn_rcpf(xd[0]), mr = -xm[0] * rd;
#pragma unroll
    for (int h = 0; h < 2; ++h) {
      xf[0][h] = unpk(xv[0][h]);
      sv[0][h] = __builtin_elementwise_fma(xf[0][h], (f32x2){rd, rd}, (f32x2){mr, mr}) * unpk(yr[0][h]);
      asm volatile("" : "+v"(sv[0][h]), "+v"(xf[0][h]));
    }
  SB();
#pragma unroll
    for (int h = 0; h < 2; ++h) {
      xf[1][h] = unpk(xv[1][h]);
      sv[1][h] = __builtin_elementwise_fma(xf[1][h], (f32x2){rd, rd}, (f32x2){mr, mr}) * unpk(yr[1][h]);
      asm volatile("" : "+v"(sv[1][h]), "+v"(xf[1][h]));
    }
  SB();
#pragma unroll
    for (int h = 0; h < 2; ++h) {
      xf[2][h] = unpk(xv[2][h]);
      sv[2][h] = __builtin_elementwise_fma(xf[2][h], (f32x2){rd, rd}, (f32x2){mr, mr}) * unpk(yr[2][h]);
      asm volatile("" : "+v"(sv[2][h]), "+v"(xf[2][h]));
    }
  SB();
#pragma unroll
    for (int h = 0; h < 2; ++h) {
      xf[3][h] = unpk(xv[3][h]);
      sv[3][h] = __builtin_elementwise_fma(xf[3][h], (f32x2){rd, rd}, (f32x2){mr, mr}) * unpk(yr[3][h]);
      asm volatile("" : "+v"(sv[3][h]), "+v"(xf[3][h]));
    }
  SB();
#pragma unroll
    for (int h = 0; h < 2; ++h) {
      xf[4][h] = unpk(xv[4][h]);
      sv[4][h] = __builtin_elementwise_fma(xf[4][h], (f32x2){rd, rd}, (f32x2){mr, mr}) * unpk(yr[4][h]);
      asm volatile("" : "+v"(sv[4][h]), "+v"(xf[4][h]));
    }
  SB();
  }
  asm volatile("s_waitcnt vmcnt(4)\n\t.Lc_%=:\n\tv_max3_u32 %1, v195, v199, v203\n\tv_max_u32 %1, %1, v207\n\tv_cmp_eq_u32 vcc, -1, %1\n\ts_cbranch_vccnz .Lr_%=\n\t.Ld_%=:\n\t.subsection 1\n\t.Lr_%=:\n\tv_add_u32 %0, -1, %0\n\ts_nop 1\n\tv_readfirstlane_b32 vcc_hi, %0\n\ts_nop 3\n\ts_cmp_lt_i32 vcc_hi, 1\n\ts_cbranch_scc1 .Ld_%=\n\ts_sleep 2\n\ts_mov_b32 vcc_lo, 0\n\tbuffer_load_dwordx4 v[192:195], %2, %3, vcc_lo offen offset:2048 sc1\n\ts_mov_b32 vcc_lo, 3072\n\tbuffer_load_dwordx4 v[196:199], %2, %3, vcc_lo offen offset:2048 sc1\n\ts_mov_b32 vcc_lo, 6144\n\tbuffer_load_dwordx4 v[200:203], %2, %3, vcc_lo offen offset:2048 sc1\n\ts_mov_b32 vcc_lo, 9216\n\tbuffer_load_dwordx4 v[204:207], %2, %3, vcc_lo offen offset:2048 sc1\n\ts_waitcnt vmcnt(0)\n\ts_branch .Lc_%=\n\t.subsection 0" : "+v"(tmo), "=&v"(vtmp) : "v"(vHr[0]), "s"(rWS) : "memory", "vcc", "scc"); SB();
  asm volatile("v_mfma_f32_16x16x32_bf16 %0, a[64:67], v[192:195], %0" : "+v"(o3[0][0]) :  : ); SB();
  asm volatile("v_mfma_f32_16x16x32_bf16 %0, a[68:71], v[192:195], %0" : "+v"(o3[0][1]) :  : ); SB();
  asm volatile("v_mfma_f32_16x16x32_bf16 %0, a[72:75], v[192:195], %0" : "+v"(o3[0][2]) :  : ); SB();
  asm volatile("v_mfma_f32_16x16x32_bf16 %0, a[76:79], v[192:195], %0" : "+v"(o3[0][3]) :  : ); SB();
  asm volatile("v_mfma_f32_16x16x32_bf16 %0, a[80:83], v[192:195], %0" : "+v"(o3[0][4]) :  : ); SB();
  asm volatile("v_mfma_f32_16x16x32_bf16 %0, a[84:87], v[196:199], %0" : "+v"(o3[0][0]) :  : ); SB();
  asm volatile("v_mfma_f32_16x16x32_bf16 %0, a[88:91], v[196:199], %0" : "+v"(o3[0][1]) :  : ); SB();
  asm volatile("v_mfma_f32_16x16x32_bf16 %0, a[92:95], v[196:199], %0" : "+v"(o3[0][2]) :  : ); SB();
  asm volatile("v_mfma_f32_16x16x32_bf16 %0, a[96:99], v[196:199], %0" : "+v"(o3[0][3]) :  : ); SB();
  asm volatile("v_mfma_f32_16x16x32_bf16 %0, a[100:103], v[196:199], %0" : "+v"(o3[0][4]) :  : ); SB();
  asm volatile("v_mfma_f32_16x16x32_bf16 %0, a[104:107], v[200:203], %0" : "+v"(o3[0][0]) :  : ); SB();
  asm volatile("v_mfma_f32_16x16x32_bf16 %0, a[108:111], v[200:203], %0" : "+v"(o3[0][1]) :  : ); SB();
  asm volatile("v_mfma_f32_16x16x32_bf16 %0, a[112:115], v[200:203], %0" : "+v"(o3[0][2]) :  : ); SB();
  asm volatile("v_mfma_f32_16x16x32_bf16 %0, a[116:119], v[200:203], %0" : "+v"(o3[0][3]) :  : ); SB();
  asm volatile("v_mfma_f32_16x16x32_bf16 %0, a[120:123], v[200:203], %0" : "+v"(o3[0][4]) :  : ); SB();
  asm volatile("v_mfma_f32_16x16x32_bf16 %0, a[124:127], v[204:207], %0" : "+v"(o3[0][0]) :  : ); SB();
  asm volatile("v_mfma_f32_16x16x32_bf16 %0, a[128:131], v[204:207], %0" : "+v"(o3[0][1]) :  : ); SB();
  asm volatile("s_waitcnt vmcnt(0)\n\t.Lc_%=:\n\tv_max3_u32 %1, v211, v215, v219\n\tv_max_u32 %1, %1, v223\n\tv_cmp_eq_u32 vcc, -1, %1\n\ts_cbranch_vccnz .Lr_%=\n\t.Ld_%=:\n\t.subsection 1\n\t.Lr_%=:\n\tv_add_u32 %0, -1, %0\n\ts_nop 1\n\tv_readfirstlane_b32 vcc_hi, %0\n\ts_nop 3\n\ts_cmp_lt_i32 vcc_hi, 1\n\ts_cbranch_scc1 .Ld_%=\n\ts_sleep 2\n\ts_mov_b32 vcc_lo, 0\n\tbuffer_load_dwordx4 v[208:211], %2, %3, vcc_lo offen offset:2048 sc1\n\ts_mov_b32 vcc_lo, 3072\n\tbuffer_load_dwordx4 v[212:215], %2, %3, vcc_lo offen offset:2048 sc1\n\ts_mov_b32 vcc_lo, 6144\n\tbuffer_load_dwordx4 v[216:219], %2, %3, vcc_lo offen offset:2048 sc1\n\ts_mov_b32 vcc_lo, 9216\n\tbuffer_load_dwordx4 v[220:223], %2, %3, vcc_lo offen offset:2048 sc1\n\ts_waitcnt vmcnt(0)\n\ts_branch .Lc_%=\n\t.subsection 0" : "+v"(tmo), "=&v"(vtmp) : "v"(vHr[1]), "s"(rWS) : "memory", "vcc", "scc"); SB();
  asm volatile("v_mfma_f32_16x16x32_bf16 %0, a[132:135], v[204:207], %0" : "+v"(o3[0][2]) :  : ); SB();
  asm volatile("v_mfma_f32_16x16x32_bf16 %0, a[136:139], v[204:207], %0" : "+v"(o3[0][3]) :  : ); SB();
  asm volatile("v_mfma_f32_16x16x32_bf16 %0, a[140:143], v[204:207], %0" : "+v"(o3[0][4]) :  : ); SB();
  asm volatile("v_mfma_f32_16x16x32_bf16 %0, a[64:67], v[208:211], %0" : "+v"(o3[1][0]) :  : ); SB();
  asm volatile("v_mfma_f32_16x16x32_bf16 %0, a[68:71], v[208:211], %0" : "+v"(o3[1][1]) :  : ); SB();
  asm volatile("ds_write_b128 %0, %1 offset:0" :  : "v"(pw2), "v"(o3[0][0]) : "memory"); SB();
  asm volatile("v_mfma_f32_16x16x32_bf16 %0, a[72:75], v[208:211], %0" : "+v"(o3[1][2]) :  : ); SB();
  asm volatile("v_mfma_f32_16x16x32_bf16 %0, a[76:79], v[208:211], %0" : "+v"(o3[1][3]) :  : ); SB();
  asm volatile("ds_write_b128 %0, %1 offset:1024" :  : "v"(pw2), "v"(o3[0][1]) : "memory"); SB();
  asm volatile("v_mfma_f32_16x16x32_bf16 %0, a[80:83], v[208:211], %0" : "+v"(o3[1][4]) :  : ); SB();
  asm volatile("v_mfma_f32_16x16x32_bf16 %0, a[84:87], v[212:215], %0" : "+v"(o3[1][0]) :  : ); SB();
  asm volatile("ds_write_b128 %0, %1 offset:2048" :  : "v"(pw2), "v"(o3[0][2]) : "memory"); SB();
  asm volatile("v_mfma_f32_16x16x32_bf16 %0, a[88:91], v[212:215], %0" : "+v"(o3[1][1]) :  : ); SB();
  asm volatile("v_mfma_f32_16x16x32_bf16 %0, a[92:95], v[212:215], %0" : "+v"(o3[1][2]) :  : ); SB();
  asm volatile("ds_write_b128 %0, %1 offset:3072" :  : "v"(pw2), "v"(o3[0][3]) : "memory"); SB();
  asm volatile("v_mfma_f32_16x16x32_bf16 %0, a[96:99], v[212:215], %0" : "+v"(o3[1][3]) :  : ); SB();
  asm volatile("v_mfma_f32_16x16x32_bf16 %0, a[100:103], v[212:215], %0" : "+v"(o3[1][4]) :  : ); SB();
  asm volatile("ds_write_b128 %0, %1 offset:4096" :  : "v"(pw2), "v"(o3[0][4]) : "memory"); SB();
  asm volatile("v_mfma_f32_16x16x32_bf16 %0, a[104:107], v[216:219], %0" : "+v"(o3[1][0]) :  : ); SB();
  asm volatile("v_mfma_f32_16x16x32_bf16 %0, a[108:111], v[216:219], %0" : "+v"(o3[1][1]) :  : ); SB();
  asm volatile("s_waitcnt lgkmcnt(0)\n\ts_barrier" :  :  : "memory"); SB();
  asm volatile("ds_read_b128 %0, %1 offset:0" : "=&v"(pq[0]) : "v"(pe[0]) : "memory"); SB();
  asm volatile("v_mfma_f32_16x16x32_bf16 %0, a[112:115], v[216:219], %0" : "+v"(o3[1][2]) :  : ); SB();
  asm volatile("ds_read_b128 %0, %1 offset:1024" : "=&v"(pq[1]) : "v"(pe[0]) : "memory"); SB();
  asm volatile("v_mfma_f32_16x16x32_bf16 %0, a[116:119], v[216:219], %0" : "+v"(o3[1][3]) :  : ); SB();
  asm volatile("ds_read_b128 %0, %1 offset:2048" : "=&v"(pq[2]) : "v"(pe[0]) : "memory"); SB();
  asm volatile("v_mfma_f32_16x16x32_bf16 %0, a[120:123], v[216:219], %0" : "+v"(o3[1][4]) :  : ); SB();
  asm volatile("ds_read_b128 %0, %1 offset:3072" : "=&v"(pq[3]) : "v"(pe[0]) : "memory"); SB();
  asm volatile("v_mfma_f32_16x16x32_bf16 %0, a[124:127], v[220:223], %0" : "+v"(o3[1][0]) :  : ); SB();
  asm volatile("ds_read_b128 %0, %1 offset:4096" : "=&v"(pq[4]) : "v"(pe[0]) : "memory"); SB();
  asm volatile("v_mfma_f32_16x16x32_bf16 %0, a[128:131], v[220:223], %0" : "+v"(o3[1][1]) :  : ); SB();
  asm volatile("v_mfma_f32_16x16x32_bf16 %0, a[132:135], v[220:223], %0" : "+v"(o3[1][2]) :  : ); SB();
  asm volatile("v_mfma_f32_16x16x32_bf16 %0, a[136:139], v[220:223], %0" : "+v"(o3[1][3]) :  : ); SB();
  asm volatile("v_mfma_f32_16x16x32_bf16 %0, a[140:143], v[220:223], %0" : "+v"(o3[1][4]) :  : ); SB();
  ST(22);
  asm volatile("s_waitcnt lgkmcnt(0)\n\ts_nop 7" : "+v"(o3[1][0]), "+v"(o3[1][1]), "+v"(o3[1][2]), "+v"(o3[1][3]), "+v"(o3[1][4]), "+v"(pq[0]), "+v"(pq[1]), "+v"(pq[2]), "+v"(pq[3]), "+v"(pq[4]) :  : "memory"); SB();
  {
    unsigned pk[5][2];
#pragma unroll
    for (int j = 0; j < 5; ++j) {
      const f32x4 o4 = o3[1][j] + pq[j];
#pragma unroll
      for (int h = 0; h < 2; ++h) {
        const f32x2 o = {o4[2 * h], o4[2 * h + 1]};
        const f32x2 a = o * (f32x2){-1.4426950f, -1.4426950f};
        const f32x2 e = (f32x2){__builtin_amdgcn_exp2f(a[0]), __builtin_amdgcn_exp2f(a[1])} + (f32x2){1.0f, 1.0f};
        const f32x2 r = {__builtin_amdgcn_rcpf(e[0]), __builtin_amdgcn_rcpf(e[1])};
        pk[j][h] = pk2(__builtin_elementwise_fma(o * r, sv[j][h], xf[j][h]));
      }
    }
    GAS char* po = (GAS char*)out + (rb * 64 + w * 16 + l15) * 1280 + cb * 160;
    const unsigned dq = 32 * (g4 & 1) + 16 * (g4 >> 1);
#pragma unroll
    for (int q = 0; q < 2; ++q) {
      const auto s0 = __builtin_amdgcn_permlane16_swap(pk[2 * q][0], pk[2 * q + 1][0], false, false);
      const auto s1 = __builtin_amdgcn_permlane16_swap(pk[2 * q][1], pk[2 * q + 1][1], false, false);
      *(GAS u32x4*)(po + 64 * q + dq) = (u32x4){s0[0], s1[0], s0[1], s1[1]};
    }
    *(GAS u32x2*)(po + 128 + 8 * g4) = (u32x2){pk[4][0], pk[4][1]};
  }
  ST(23);
  ST_OUT();
  }}}}
}

extern "C" __global__ void k_end() { asm volatile(".subsection 2\n.Lcode_end:\n\ts_endpgm\n\t.subsection 0"); }

extern "C" void kernel_launch(void* const* d_in, const int* in_sizes, int n_in,
                              void* d_out, int out_size, void* d_ws, size_t ws_size,
                              hipStream_t stream) {
  (void)hipFuncSetAttribute((const void*)k_all, hipFuncAttributeMaxDynamicSharedMemorySize, 163840);
  k_all<<<256, 256, 163840, stream>>>((const char*)d_in[0], (const char*)d_in[1], (const char*)d_in[2], (const char*)d_in[3], (const char*)d_in[4],
                                      (const char*)d_in[5], (const char*)d_in[7], (const char*)d_in[8], (const char*)d_in[9], (const char*)d_in[10],
                                      (char*)d_out, (char*)d_ws);
}
```

```cpp
#include <hip/hip_runtime.h>
#include <stdint.h>

typedef unsigned short u16;
typedef __attribute__((ext_vector_type(4))) float f32x4;
typedef __attribute__((ext_vector_type(2))) float f32x2;
typedef __attribute__((ext_vector_type(4))) unsigned int u32x4;
typedef __attribute__((ext_vector_type(2))) unsigned int u32x2;
typedef __attribute__((ext_vector_type(2))) __bf16 bf16x2n;
typedef __attribute__((ext_vector_type(8))) unsigned long long u64x8;
typedef __attribute__((ext_vector_type(4))) unsigned long long u64x4;
#define LDS_AS __attribute__((address_space(3)))
#define GAS __attribute__((address_space(1)))
#define SB() __builtin_amdgcn_sched_barrier(0)
#define DI __device__ __forceinline__

#define SGA
#define ST_DECL do {} while (0)
#define ST(i) do {} while (0)
#define ST_OUT() do {} while (0)

constexpr int S_ = 2048, D_ = 640;
constexpr unsigned HOLE = 0xFFFFFFFFu;
constexpr int W_ST = 4096, W_FZ = 768 << 10, W_FK = W_FZ + 8192, W_FY = W_FK + 8192, W_FH = W_FY + 8192, W_KV = 1 << 20, W_Z2 = 4 << 20, W_YY = 8 << 20, W_HH = 11 << 20, W_WO = 14 << 20;

#define wm (w >> 1)
#define wn (w & 1)
#define g4 (lane >> 4)
#define l15 (lane & 15)
#define q4 ((lane >> 2) & 3)
#define p4 (lane & 3)
#define l16 (lane * 16)
#define nch (3 - (rb >> 4))
#define ch0 (cb * 80 + rb * 2 + min(rb, 16u))
#define c2o ((nch - 2) * 2)
#define c2s (8192u + ((rb >> 4) << 30))
#define w3s ((w + 1) >> 2 << 30)
#define vZ (W_Z2 + ((rb * 2 + wm) * 64 + cb * 6 + wn * 3) * 1024 + l16)
#define vH (W_HH + ((rb * 4 + w) * 24 + cb * 3) * 1024 + l16)

DI unsigned pack2(float lo, float hi) {
  f32x2 v = {lo, hi};
  return __builtin_bit_cast(unsigned, __builtin_convertvector(v, bf16x2n));
}
DI float lo16(unsigned w) { return __uint_as_float(w << 16); }
DI float hi16(unsigned w) { return __uint_as_float(w & 0xFFFF0000u); }
DI float rbf(float f) { return lo16(pack2(f, f)); }
DI f32x2 unpk(unsigned w) { f32x2 r = {lo16(w), hi16(w)}; return r; }
DI unsigned pk2(f32x2 v) { return pack2(v[0], v[1]); }
DI unsigned nohole(unsigned a) { return a < HOLE - 1 ? a : HOLE - 1; }
template <int CTRL> DI float dppf(float v) {
  return __builtin_bit_cast(float, __builtin_amdgcn_update_dpp(0, __builtin_bit_cast(int, v), CTRL, 0xF, 0xF, true));
}
DI f32x2 gelu2(f32x2 t, float k1, float k2, float den) {
  const f32x2 a = t * __builtin_elementwise_fma(t * t, (f32x2){k2, k2}, (f32x2){k1, k1});
  f32x2 e = {__builtin_amdgcn_exp2f(a[0]), __builtin_amdgcn_exp2f(a[1])};
  e = __builtin_elementwise_fma(e, (f32x2){den, den}, (f32x2){den, den});
  const f32x2 r = {__builtin_amdgcn_rcpf(e[0]), __builtin_amdgcn_rcpf(e[1])};
  return t * r;
}
DI float red16(float v) {
  const auto a = __builtin_amdgcn_permlane16_swap(__float_as_uint(v), __float_as_uint(v), false, false);
  return __uint_as_float(a[0]) + __uint_as_float(a[1]);
}
DI float red32(float v) {
  const auto c = __builtin_amdgcn_permlane32_swap(__float_as_uint(v), __float_as_uint(v), false, false);
  return __uint_as_float(c[0]) + __uint_as_float(c[1]);
}
DI float sel32(float a, float b) {
  const auto c = __builtin_amdgcn_permlane32_swap(__float_as_uint(a), __float_as_uint(b), false, false);
  return __uint_as_float(c[0]) + __uint_as_float(c[1]);
}
DI float dot2(unsigned a, unsigned b, float c) {
  return __builtin_amdgcn_fdot2_f32_bf16(__builtin_bit_cast(bf16x2n, a), __builtin_bit_cast(bf16x2n, b), c, false);
}
DI unsigned shr16(unsigned hi, unsigned lo) { return __builtin_amdgcn_alignbit(hi, lo, 16); }
DI unsigned lds_addr(const void* p) { return (unsigned)(uintptr_t)(LDS_AS const char*)p; }

__global__ __launch_bounds__(256, 1) __attribute__((amdgpu_num_vgpr(160))) SGA void k_all(
    const char*, const char*, const char*, const char*, const char*, const char*, const char*, const char*, const char*, const char*, char*, char*) {
  extern __shared__ __attribute__((aligned(16))) char smem[];
  ST_DECL;
  ST(0);
  u64x8 ka0; u64x4 ka1;
  asm volatile("s_load_dwordx16 %0, %2, 0x0\n\ts_load_dwordx8 %1, %2, 0x40" : "=&s"(ka0), "=&s"(ka1) : "s"(__builtin_amdgcn_kernarg_segment_ptr()) : "memory"); SB();
  const unsigned tid = threadIdx.x, lane0 = tid & 63, w0 = __builtin_amdgcn_readfirstlane(tid >> 6);
  const unsigned b = blockIdx.x, xcd = b & 7, idx = b >> 3;
  const unsigned rb0 = 8 * (xcd >> 1) + (idx >> 2), cb0 = 4 * (xcd & 1) + (idx & 3);
  const unsigned lb = lds_addr(smem);
  const unsigned lane = lane0, w = w0, rb = rb0, cb = cb0;
  {
    unsigned long long pc; unsigned clen;
    asm volatile("s_getpc_b64 %0\n.Lpc_here_%=:\n\ts_mov_b32 %1, .Lcode_end-.Lpc_here_%=" : "=s"(pc), "=s"(clen));
    const unsigned o = min((((lane & 3) * 4 + w) * 32 + idx) * 128, clen - 4u) & ~3u;
    asm volatile("s_mov_b64 exec, 15\n\tglobal_load_dword v254, %0, %1\n\ts_mov_b64 exec, -1" :: "v"(o), "s"(pc) : "memory");
    SB();
  }
  unsigned voA = (rb * 64 + w * 16 + (lane >> 3)) * 1280 + (((lane & 7) ^ (lane >> 3)) << 4), voB[3];
#pragma unroll
  for (int i = 0; i < 3; ++i) { const unsigned o = i * 64 + lane, r = (o * 171) >> 11, c = o - r * 12; voB[i] = (w * 16 + r) * 3072 + cb * 192 + ((c ^ ((r >> 3) << 1)) << 4) - i * 1024; }
  unsigned mX = lb + w * 2048, mW = lb + w * 3072, mA = lb + w * 8192;
  const unsigned rot = ((rb ^ cb) & 3);
  unsigned kx[10], kw_[10], voA2 = voA + 9216;
  auto kofs = [&](int s) {
    asm volatile("s_xor_b32 %0, %2, %3\n\ts_mul_i32 %1, %0, 0x30000\n\ts_lshl_b32 %0, %0, 7" : "=&s"(kx[s]), "=&s"(kw_[s]) : "s"(s < 8 ? rot : rot & 1), "i"(s) : "scc");
  };
  kofs(0); kofs(1); kofs(2);
  asm volatile("" : "+v"(voA), "+v"(voA2), "+v"(voB[0]), "+v"(voB[1]), "+v"(voB[2]));
  asm volatile("" : "+s"(mX), "+s"(mW), "+s"(mA)); SB();
  asm volatile("s_waitcnt lgkmcnt(0)" : "+s"(ka0), "+s"(ka1) :: "memory"); SB();
  const char *x = (const char*)ka0[0], *kin = (const char*)ka0[1], *vin = (const char*)ka0[2], *shift_k = (const char*)ka0[3], *ssm_k = (const char*)ka0[4];
  const char *ssm_D = (const char*)ka0[5], *w_in = (const char*)ka0[6], *w_sp = (const char*)ka0[7], *b_sp = (const char*)ka1[0], *w_out = (const char*)ka1[1];
  char *out = (char*)ka1[2], *ws = (char*)ka1[3];
  const auto rX = __builtin_amdgcn_make_buffer_rsrc((void*)x, 0, S_ * D_ * 2, 0x00020000);
  const auto rW = __builtin_amdgcn_make_buffer_rsrc((void*)(w_in + 1536), 0, 640 * 3072 - 1536, 0x00020000);
  asm volatile("; named registers" :  :  : "v255", "a255"); SB();
  asm volatile("s_add_u32 m0, %0, 81920" :  : "s"(mX) : "memory", "scc"); SB();
  asm volatile("s_nop 0\n\tbuffer_load_dwordx4 %0, %1, %2 offen lds" :  : "v"(voA), "s"(rX), "s"(kx[0]) : "memory"); SB();
  asm volatile("buffer_load_dwordx4 %0, %1, %2 offen offset:1024 lds" :  : "v"(voA2), "s"(rX), "s"(kx[0]) : "memory"); SB();
  asm volatile("s_add_u32 m0, %0, 0" :  : "s"(mW) : "memory", "scc"); SB();
  asm volatile("s_nop 0\n\tbuffer_load_dwordx4 %0, %1, %2 offen lds" :  : "v"(voB[0]), "s"(rW), "s"(kw_[0]) : "memory"); SB();
  asm volatile("buffer_load_dwordx4 %0, %1, %2 offen offset:1024 lds" :  : "v"(voB[1]), "s"(rW), "s"(kw_[0]) : "memory"); SB();
  asm volatile("buffer_load_dwordx4 %0, %1, %2 offen offset:2048 lds" :  : "v"(voB[2]), "s"(rW), "s"(kw_[0]) : "memory"); SB();
  asm volatile("s_add_u32 m0, %0, 90112" :  : "s"(mX) : "memory", "scc"); SB();
  asm volatile("s_nop 0\n\tbuffer_load_dwordx4 %0, %1, %2 offen lds" :  : "v"(voA), "s"(rX), "s"(kx[1]) : "memory"); SB();
  asm volatile("buffer_load_dwordx4 %0, %1, %2 offen offset:1024 lds" :  : "v"(voA2), "s"(rX), "s"(kx[1]) : "memory"); SB();
  asm volatile("s_add_u32 m0, %0, 12288" :  : "s"(mW) : "memory", "scc"); SB();
  asm volatile("s_nop 0\n\tbuffer_load_dwordx4 %0, %1, %2 offen lds" :  : "v"(voB[0]), "s"(rW), "s"(kw_[1]) : "memory"); SB();
  asm volatile("buffer_load_dwordx4 %0, %1, %2 offen offset:1024 lds" :  : "v"(voB[1]), "s"(rW), "s"(kw_[1]) : "memory"); SB();
  asm volatile("buffer_load_dwordx4 %0, %1, %2 offen offset:2048 lds" :  : "v"(voB[2]), "s"(rW), "s"(kw_[1]) : "memory"); SB();
  kofs(3); kofs(4); kofs(5); kofs(6); kofs(7); kofs(8); kofs(9);
  const auto rWS = __builtin_amdgcn_make_buffer_rsrc((void*)ws, 0, 32 << 20, 0x00020000);
  const auto rK = __builtin_amdgcn_make_buffer_rsrc((void*)kin, 0, S_ * D_ * 2, 0x00020000);
  const auto rV = __builtin_amdgcn_make_buffer_rsrc((void*)vin, 0, S_ * D_ * 2, 0x00020000);
  const auto rS = __builtin_amdgcn_make_buffer_rsrc((void*)(shift_k - 3328), 0, D_ * 8 + 3328, 0x00020000);
  const auto rO = __builtin_amdgcn_make_buffer_rsrc((void*)(w_out - 3072), 0, 768 * 1280 + 3072, 0x00020000);
  const auto rW1 = __builtin_amdgcn_make_buffer_rsrc((void*)w_in, 0, 640 * 3072, 0x00020000);
  { unsigned lane = lane0, w = w0, rb = rb0, cb = cb0;
  asm volatile("" : "+v"(lane)); asm volatile("" : "+s"(w), "+s"(rb), "+s"(cb)); SB();

  const auto rA = __builtin_amdgcn_make_buffer_rsrc((void*)(w_sp - 3072), 0, S_ * S_ * 2 + 3072, 0x00020000);
  unsigned ao[8];
  unsigned voK[4], voV[3], voH = cb * 640 + l16;
#pragma unroll
  for (int n = 0; n < 4; ++n) { const unsigned o = n * 64 + lane, r = (o * 205) >> 11; voK[n] = (rb * 64 + w * 16 - 4) * 1280 + cb * 160 + r * 1120 + lane * 16; }
#pragma unroll
  for (int n = 0; n < 3; ++n) voV[n] = voK[n] + 5120;
  const unsigned wof = (rb >> 3) * 30 + min((rb & 7) * 4 + w, 29u), wok = (wof * 205) >> 10;
  unsigned voO = (lane >> 1) * 1280 + (lane & 1) * 16 + wok * 40960 + (wof - 5 * wok) * 32 + cb * 160;
  unsigned ka = lb + 98304 + w * 8192 + (4 * p4 + q4) * 160 + 8 * g4, oa = lb + 98304 + 7168 + w * 8192 + (4 * g4 + q4) * 32 + 8 * p4;
  unsigned sa = lb + 98304 + 3328 + w * 8192 + (4 * g4 + p4) * 8;
  unsigned kw = lb + 12288 + (4 * g4 + p4) * 144 + w * 32 + q4 * 8, kr = lb + 12288 + (w * 24 + (lane >> 3)) * 144 + (lane & 7) * 16;
  unsigned vKV = W_KV + (cb * 80 + w * 24 + (lane >> 3)) * 4096 + rb * 128 + (lane & 7) * 16, vWO = W_WO + (cb * 120 + wof) * 1024 + l16;
  asm volatile("" : "+v"(ka), "+v"(oa), "+v"(sa), "+v"(kw), "+v"(kr), "+v"(vKV), "+v"(vWO), "+v"(voK[0]), "+v"(voK[1]), "+v"(voK[2]), "+v"(voK[3]),
               "+v"(voV[0]), "+v"(voV[1]), "+v"(voV[2]), "+v"(voH), "+v"(voO));
  u32x2 kp[5], kc[5], vv[5], sw[5], wt[2], ko[5];
  u32x4 kq[3];
  unsigned aX[2][2], aW[2][3];
#pragma unroll
  for (int n = 0; n < 4; ++n) aX[n >> 1][n & 1] = lb + 98304 + (wm * 32 + ((n & 1) ^ wn) * 16 + l15) * 128 + ((((n >> 1) * 4 + g4) ^ (l15 & 7)) << 4);
#pragma unroll
  for (int j = 0; j < 3; ++j) {
    aW[0][j] = lb + (8 * g4 + q4) * 192 + (((wn * 3 + j) ^ (g4 & 1)) << 5) + 8 * p4;
    aW[1][j] = aW[0][j] + 49152;
  }
  u32x4 ones = {0x3F803F80u, 0x3F803F80u, 0x3F803F80u, 0x3F803F80u};
  f32x4 a2[2][3], a1[2][3], rs, gr, c2[3], c1[3];
  unsigned voS = (rb * 64 + (lane >> 3)) * 4096 + w * 1024 + (cb & 1) * 128 + (((lane & 7) ^ (lane >> 3)) << 4), aA[2];
#pragma unroll
  for (int k = 0; k < 2; ++k) aA[k] = lb + 98304 + w * 8192 + l15 * 128 + (((k * 4 + g4) ^ (l15 & 7)) << 4);
  unsigned c2s_ = c2s, xw = lb + 98304 + 6656 + w * 8192 + l15 * 16, xr_ = lb + 98304 + 6656 + (w ^ 1) * 8192 + l15 * 16;
  u32x2 zt[3][2];
  unsigned zwa[2] = {lb + 98304 + w * 8192 + (wn * 16 + l15) * 104 + g4 * 8, lb + 98304 + w * 8192 + ((wn ^ 1) * 16 + l15) * 104 + g4 * 8};
  unsigned zra = lb + 98304 + w * 8192 + (8 * g4 + q4) * 104 + 8 * p4;
  asm volatile("" : "+v"(zwa[0]), "+v"(zwa[1]), "+v"(zra));
  unsigned vo = W_ST + ((rb * 2 + wm) * 8 + cb) * 1024 + wn * 128 + ((lane >> 5) ^ wn) * 512 + (l15 >> 3) * 256 + (l15 & 7) * 16;
  asm volatile("" : "+v"(ones),
               "+v"(voS), "+v"(aA[0]), "+v"(aA[1]), "+s"(c2s_), "+v"(xw), "+v"(xr_), "+v"(vo));
  asm volatile("" : "+v"(aX[0][0]), "+v"(aX[0][1]), "+v"(aX[1][0]), "+v"(aX[1][1]), "+v"(aW[0][0]), "+v"(aW[0][1]), "+v"(aW[0][2]), "+v"(aW[1][0]), "+v"(aW[1][1]), "+v"(aW[1][2]));
  int tmo = (1 << 18);
  u32x2 fk, fz, fh, fi, fy;
  unsigned tg0, tg1;
  float xm[2], xd[2], k1[2], k2[2];
  f32x4 xs[2];
  float xt[4];
  const auto rF = __builtin_amdgcn_make_buffer_rsrc((void*)ssm_k, 0, S_ * D_ * 2, 0x00020000);
  unsigned vF = ch0 * 4096 + w * 1024 + l16;
  asm volatile("" : "+v"(vF));
  unsigned hpo[3], vD = ch0 * 2, vD2 = vD + c2o * 2 - 4, vBs = (rb * 64 + w * 16 + l15) * 2;
  hpo[0] = w ? ch0 * 4096 + w * 1024 - 4 : 0x40000000u; hpo[1] = hpo[0] + 4096; hpo[2] = hpo[0] + c2s;
  asm volatile("" : "+s"(hpo[0]), "+s"(hpo[1]), "+s"(hpo[2]), "+v"(vD), "+v"(vD2), "+v"(vBs));
  unsigned vS = W_ST + (rb * 2 + wm) * 8192 + l16, vFZ = W_FZ + cb * 1024 + w * 256 + (lane & 31) * 8, vZ_ = vZ;
  unsigned vB = W_Z2 + (w * 1024 + (cb & 1) * 128 + cb * 6) * 1024 + l16;
  unsigned ha = lb + 59520 + 4608 + (2040 - 8 * (w * 64 + lane)) * 2;
  unsigned za = lb + 59520 + min(w, 2u) * 12928 + (lane < 32 ? lane * 16 : 4608 + ((lane >> 1) & 1) * 4160 + 4096 + (lane & 1) * 16);
  unsigned ew[2] = {lb + 32768 + w * 6144 + wn * 3072 + l16, lb + 32768 + (w ^ 1) * 6144 + wn * 3072 + l16};
  unsigned aa[4], bb;
  {
    const unsigned c = (l15 + 1) & 1;
    const unsigned o = lb + 59520 + 2 * 12928 - w * 128 - 192;
#pragma unroll
    for (int k = 0; k < 4; ++k) aa[k] = o + 4608 + c * 4160 + (2031 - l15 + 8 * g4 - c) * 2 - 512 - 1024 * k;
    bb = o + 512 + 32 * l15 - 32 + 16 * g4;
  }
  unsigned dd = lb + 0 + w * 8192 + g4 * 256 + ((l15 ^ ((g4 >> 1) << 3)) << 4);
  asm volatile("" : "+v"(aa[0]), "+v"(aa[1]), "+v"(aa[2]), "+v"(aa[3]), "+v"(bb), "+v"(dd), "+v"(vS), "+v"(vFZ), "+v"(vZ_), "+v"(vB), "+v"(ha), "+v"(za), "+v"(ew[0]), "+v"(ew[1]));
  unsigned mU, uso, w3k[2], sFK, sFZ;
  float dD[3], bs;
  u32x4 im[6], zero4;
  ST(1);
  SB();
  asm volatile("s_add_u32 m0, %0, 98304" :  : "s"(mX) : "memory", "scc"); SB();
  asm volatile("s_nop 0\n\tbuffer_load_dwordx4 %0, %1, %2 offen lds" :  : "v"(voA), "s"(rX), "s"(kx[2]) : "memory"); SB();
  asm volatile("buffer_load_dwordx4 %0, %1, %2 offen offset:1024 lds" :  : "v"(voA2), "s"(rX), "s"(kx[2]) : "memory"); SB();
  asm volatile("buffer_load_dwordx2 v[252:253], off, %0, 0 sc1" :  : "s"(rWS) : "memory"); SB();
  asm volatile("buffer_load_dword v242, off, %0, %1" :  : "s"(rF), "s"(hpo[0]) : "memory"); SB();
  asm volatile("global_load_ushort v245, %0, %1 offset:0" :  : "v"(vD), "s"(ssm_D) : "memory"); SB();
  asm volatile("buffer_load_dword v243, off, %0, %1" :  : "s"(rF), "s"(hpo[1]) : "memory"); SB();
  asm volatile("global_load_ushort v246, %0, %1 offset:2" :  : "v"(vD), "s"(ssm_D) : "memory"); SB();
  asm volatile("buffer_load_dword v244, off, %0, %1" :  : "s"(rF), "s"(hpo[2]) : "memory"); SB();
  asm volatile("global_load_ushort v247, %0, %1 offset:4" :  : "v"(vD2), "s"(ssm_D) : "memory"); SB();
  asm volatile("global_load_ushort v248, %0, %1" :  : "v"(vBs), "s"(b_sp) : "memory"); SB();
  asm volatile("s_add_u32 m0, %0, 106496" :  : "s"(mX) : "memory", "scc"); SB();
  asm volatile("s_nop 0\n\tbuffer_load_dwordx4 %0, %1, %2 offen lds" :  : "v"(voA), "s"(rX), "s"(kx[3]) : "memory"); SB();
  asm volatile("buffer_load_dwordx4 %0, %1, %2 offen offset:1024 lds" :  : "v"(voA2), "s"(rX), "s"(kx[3]) : "memory"); SB();
  asm volatile("s_add_u32 m0, %0, 24576" :  : "s"(mW) : "memory", "scc"); SB();
  asm volatile("s_nop 0\n\tbuffer_load_dwordx4 %0, %1, %2 offen lds" :  : "v"(voB[0]), "s"(rW), "s"(kw_[2]) : "memory"); SB();
  asm volatile("s_waitcnt vmcnt(18) lgkmcnt(0)\n\ts_barrier" :  :  : "memory"); SB();
  ST(4);
  asm volatile("ds_read_b128 a[0:3], %0 offset:0" :  : "v"((aX[0][0] - 16384)) : "memory"); SB();
  asm volatile("ds_read_b128 a[4:7], %0 offset:0" :  : "v"((aX[0][1] - 16384)) : "memory"); SB();
  asm volatile("ds_read_b64_tr_b16 v[160:161], %0 offset:0" :  : "v"(aW[0][0]) : "memory"); SB();
  asm volatile("ds_read_b64_tr_b16 v[162:163], %0 offset:768" :  : "v"(aW[0][0]) : "memory"); SB();
  asm volatile("ds_read_b64_tr_b16 v[164:165], %0 offset:0" :  : "v"(aW[0][1]) : "memory"); SB();
  asm volatile("ds_read_b64_tr_b16 v[166:167], %0 offset:768" :  : "v"(aW[0][1]) : "memory"); SB();
  asm volatile("ds_read_b64_tr_b16 v[168:169], %0 offset:0" :  : "v"(aW[0][2]) : "memory"); SB();
  asm volatile("ds_read_b64_tr_b16 v[170:171], %0 offset:768" :  : "v"(aW[0][2]) : "memory"); SB();
  asm volatile("ds_read_b128 a[8:11], %0 offset:0" :  : "v"((aX[1][0] - 16384)) : "memory"); SB();
  asm volatile("ds_read_b128 a[12:15], %0 offset:0" :  : "v"((aX[1][1] - 16384)) : "memory"); SB();
  asm volatile("s_waitcnt lgkmcnt(6)\n\tv_mfma_f32_16x16x32_bf16 %0, v[160:163], a[0:3], 0" : "=&v"(a2[0][0]) :  : ); SB();
  asm volatile("ds_read_b64_tr_b16 v[172:173], %0 offset:6144" :  : "v"(aW[0][0]) : "memory"); SB();
  asm volatile("buffer_load_dwordx4 %0, %1, %2 offen offset:1024 lds" :  : "v"(voB[1]), "s"(rW), "s"(kw_[2]) : "memory"); SB();
  asm volatile("v_mfma_f32_16x16x32_bf16 %0, v[160:163], a[4:7], 0" : "=&v"(a2[1][0]) :  : ); SB();
  asm volatile("ds_read_b64_tr_b16 v[174:175], %0 offset:6912" :  : "v"(aW[0][0]) : "memory"); SB();
  asm volatile("buffer_load_dwordx4 %0, %1, %2 offen offset:2048 lds" :  : "v"(voB[2]), "s"(rW), "s"(kw_[2]) : "memory"); SB();
  asm volatile("v_mfma_f32_16x16x32_bf16 %0, v[160:163], %1, 0" : "=&v"(c2[0]) : "v"(ones) : ); SB();
  asm volatile("ds_read_b64_tr_b16 v[176:177], %0 offset:6144" :  : "v"(aW[0][1]) : "memory"); SB();
  asm volatile("s_waitcnt lgkmcnt(5)\n\tv_mfma_f32_16x16x32_bf16 %0, v[164:167], a[0:3], 0" : "=&v"(a2[0][1]) :  : ); SB();
  asm volatile("ds_read_b64_tr_b16 v[178:179], %0 offset:6912" :  : "v"(aW[0][1]) : "memory"); SB();
  asm volatile("s_add_u32 m0, %0, 114688" :  : "s"(mX) : "memory", "scc"); SB();
  asm volatile("v_mfma_f32_16x16x32_bf16 %0, v[164:167], a[4:7], 0" : "=&v"(a2[1][1]) :  : ); SB();
  asm volatile("ds_read_b64_tr_b16 v[180:181], %0 offset:6144" :  : "v"(aW[0][2]) : "memory"); SB();
  asm volatile("buffer_load_dwordx4 %0, %1, %2 offen lds" :  : "v"(voA), "s"(rX), "s"(kx[4]) : "memory"); SB();
  asm volatile("v_mfma_f32_16x16x32_bf16 %0, v[164:167], %1, 0" : "=&v"(c2[1]) : "v"(ones) : ); SB();
  asm volatile("ds_read_b64_tr_b16 v[182:183], %0 offset:6912" :  : "v"(aW[0][2]) : "memory"); SB();
  asm volatile("v_mfma_f32_16x16x32_bf16 %0, v[168:171], a[0:3], 0" : "=&v"(a2[0][2]) :  : ); SB();
  asm volatile("buffer_load_dwordx4 %0, %1, %2 offen offset:1024 lds" :  : "v"(voA2), "s"(rX), "s"(kx[4]) : "memory"); SB();
  asm volatile("v_mfma_f32_16x16x32_bf16 %0, v[168:171], a[4:7], 0" : "=&v"(a2[1][2]) :  : ); SB();
  asm volatile("s_waitcnt vmcnt(17)\n\ts_barrier" :  :  : "memory"); SB();
  asm volatile("ds_read_b64_tr_b16 v[184:185], %0 offset:12288" :  : "v"(aW[0][0]) : "memory"); SB();
  asm volatile("s_add_u32 m0, %0, 122880" :  : "s"(mX) : "memory", "scc"); SB();
  asm volatile("v_mfma_f32_16x16x32_bf16 %0, v[168:171], %1, 0" : "=&v"(c2[2]) : "v"(ones) : ); SB();
  asm volatile("ds_read_b64_tr_b16 v[186:187], %0 offset:13056" :  : "v"(aW[0][0]) : "memory"); SB();
  asm volatile("v_mfma_f32_16x16x32_bf16 %0, %1, a[0:3], 0" : "=&v"(rs) : "v"(ones) : ); SB();
  asm volatile("ds_read_b64_tr_b16 v[188:189], %0 offset:12288" :  : "v"(aW[0][1]) : "memory"); SB();
  asm volatile("v_mfma_f32_16x16x32_bf16 %0, a[0:3], a[0:3], 0" : "=&v"(gr) :  : ); SB();
  asm volatile("ds_read_b64_tr_b16 v[190:191], %0 offset:13056" :  : "v"(aW[0][1]) : "memory"); SB();
  asm volatile("s_waitcnt lgkmcnt(4)\n\tv_mfma_f32_16x16x32_bf16 %0, v[172:175], a[8:11], %0" : "+v"(a2[0][0]) :  : ); SB();
  asm volatile("ds_read_b64_tr_b16 v[192:193], %0 offset:12288" :  : "v"(aW[0][2]) : "memory"); SB();
  asm volatile("v_mfma_f32_16x16x32_bf16 %0, v[172:175], a[12:15], %0" : "+v"(a2[1][0]) :  : ); SB();
  asm volatile("ds_read_b128 a[16:19], %0 offset:8192" :  : "v"((aX[0][0] - 16384)) : "memory"); SB();
  asm volatile("ds_read_b64_tr_b16 v[194:195], %0 offset:13056" :  : "v"(aW[0][2]) : "memory"); SB();
  asm volatile("v_mfma_f32_16x16x32_bf16 %0, v[172:175], %1, %0" : "+v"(c2[0]) : "v"(ones) : ); SB();
  asm volatile("ds_read_b128 a[20:23], %0 offset:8192" :  : "v"((aX[0][1] - 16384)) : "memory"); SB();
  asm volatile("ds_read_b128 a[24:27], %0 offset:8192" :  : "v"((aX[1][0] - 16384)) : "memory"); SB();
  asm volatile("v_mfma_f32_16x16x32_bf16 %0, v[176:179], a[8:11], %0" : "+v"(a2[0][1]) :  : ); SB();
  asm volatile("ds_read_b128 a[28:31], %0 offset:8192" :  : "v"((aX[1][1] - 16384)) : "memory"); SB();
  asm volatile("v_mfma_f32_16x16x32_bf16 %0, v[176:179], a[12:15], %0" : "+v"(a2[1][1]) :  : ); SB();
  asm volatile("buffer_load_dwordx4 %0, %1, %2 offen lds" :  : "v"(voA), "s"(rX), "s"(kx[5]) : "memory"); SB();
  asm volatile("v_mfma_f32_16x16x32_bf16 %0, v[176:179], %1, %0" : "+v"(c2[1]) : "v"(ones) : ); SB();
  asm volatile("buffer_load_dwordx4 %0, %1, %2 offen offset:1024 lds" :  : "v"(voA2), "s"(rX), "s"(kx[5]) : "memory"); SB();
  asm volatile("s_add_u32 m0, %0, 36864" :  : "s"(mW) : "memory", "scc"); SB();
  asm volatile("v_mfma_f32_16x16x32_bf16 %0, v[180:183], a[8:11], %0" : "+v"(a2[0][2]) :  : ); SB();
  asm volatile("buffer_load_dwordx4 %0, %1, %2 offen lds" :  : "v"(voB[0]), "s"(rW), "s"(kw_[3]) : "memory"); SB();
  asm volatile("v_mfma_f32_16x16x32_bf16 %0, v[180:183], a[12:15], %0" : "+v"(a2[1][2]) :  : ); SB();
  asm volatile("ds_read_b64_tr_b16 v[160:161], %0 offset:18432" :  : "v"(aW[0][0]) : "memory"); SB();
  asm volatile("v_mfma_f32_16x16x32_bf16 %0, v[180:183], %1, %0" : "+v"(c2[2]) : "v"(ones) : ); SB();
  asm volatile("ds_read_b64_tr_b16 v[162:163], %0 offset:19200" :  : "v"(aW[0][0]) : "memory"); SB();
  asm volatile("v_mfma_f32_16x16x32_bf16 %0, %1, a[8:11], %0" : "+v"(rs) : "v"(ones) : ); SB();
  asm volatile("ds_read_b64_tr_b16 v[164:165], %0 offset:18432" :  : "v"(aW[0][1]) : "memory"); SB();
  asm volatile("v_mfma_f32_16x16x32_bf16 %0, a[8:11], a[8:11], %0" : "+v"(gr) :  : ); SB();
  asm volatile("ds_read_b64_tr_b16 v[166:167], %0 offset:19200" :  : "v"(aW[0][1]) : "memory"); SB();
  asm volatile("s_waitcnt lgkmcnt(6)\n\tv_mfma_f32_16x16x32_bf16 %0, v[184:187], a[16:19], %0" : "+v"(a2[0][0]) :  : ); SB();
  asm volatile("ds_read_b64_tr_b16 v[168:169], %0 offset:18432" :  : "v"(aW[0][2]) : "memory"); SB();
  asm volatile("v_mfma_f32_16x16x32_bf16 %0, v[184:187], a[20:23], %0" : "+v"(a2[1][0]) :  : ); SB();
  asm volatile("ds_read_b64_tr_b16 v[170:171], %0 offset:19200" :  : "v"(aW[0][2]) : "memory"); SB();
  asm volatile("v_mfma_f32_16x16x32_bf16 %0, v[184:187], %1, %0" : "+v"(c2[0]) : "v"(ones) : ); SB();
  asm volatile("buffer_load_dwordx4 %0, %1, %2 offen offset:1024 lds" :  : "v"(voB[1]), "s"(rW), "s"(kw_[3]) : "memory"); SB();
  asm volatile("v_mfma_f32_16x16x32_bf16 %0, v[188:191], a[16:19], %0" : "+v"(a2[0][1]) :  : ); SB();
  asm volatile("buffer_load_dwordx4 %0, %1, %2 offen offset:2048 lds" :  : "v"(voB[2]), "s"(rW), "s"(kw_[3]) : "memory"); SB();
  asm volatile("v_mfma_f32_16x16x32_bf16 %0, v[188:191], a[20:23], %0" : "+v"(a2[1][1]) :  : ); SB();
  asm volatile("v_mfma_f32_16x16x32_bf16 %0, v[188:191], %1, %0" : "+v"(c2[1]) : "v"(ones) : ); SB();
  asm volatile("v_mfma_f32_16x16x32_bf16 %0, v[192:195], a[16:19], %0" : "+v"(a2[0][2]) :  : ); SB();
  asm volatile("v_mfma_f32_16x16x32_bf16 %0, v[192:195], a[20:23], %0" : "+v"(a2[1][2]) :  : ); SB();
  asm volatile("s_add_u32 m0, %0, 131072" :  : "s"(mX) : "memory", "scc"); SB();
  asm volatile("s_nop 0\n\tbuffer_load_dwordx4 %0, %1, %2 offen lds" :  : "v"(voA), "s"(rX), "s"(kx[6]) : "memory"); SB();
  asm volatile("buffer_load_dwordx4 %0, %1, %2 offen offset:1024 lds" :  : "v"(voA2), "s"(rX), "s"(kx[6]) : "memory"); SB();
  asm volatile("s_add_u32 m0, %0, 139264" :  : "s"(mX) : "memory", "scc"); SB();
  asm volatile("s_nop 0\n\tbuffer_load_dwordx4 %0, %1, %2 offen lds" :  : "v"(voA), "s"(rX), "s"(kx[7]) : "memory"); SB();
  asm volatile("s_waitcnt vmcnt(10)\n\ts_barrier" :  :  : "memory"); SB();
  asm volatile("ds_read_b64_tr_b16 v[172:173], %0 offset:24576" :  : "v"(aW[0][0]) : "memory"); SB();
  asm volatile("v_mfma_f32_16x16x32_bf16 %0, v[192:195], %1, %0" : "+v"(c2[2]) : "v"(ones) : ); SB();
  asm volatile("ds_read_b64_tr_b16 v[174:175], %0 offset:25344" :  : "v"(aW[0][0]) : "memory"); SB();
  asm volatile("v_mfma_f32_16x16x32_bf16 %0, %1, a[16:19], %0" : "+v"(rs) : "v"(ones) : ); SB();
  asm volatile("ds_read_b64_tr_b16 v[176:177], %0 offset:24576" :  : "v"(aW[0][1]) : "memory"); SB();
  asm volatile("v_mfma_f32_16x16x32_bf16 %0, a[16:19], a[16:19], %0" : "+v"(gr) :  : ); SB();
  asm volatile("ds_read_b64_tr_b16 v[178:179], %0 offset:25344" :  : "v"(aW[0][1]) : "memory"); SB();
  asm volatile("s_waitcnt lgkmcnt(4)\n\tv_mfma_f32_16x16x32_bf16 %0, v[160:163], a[24:27], %0" : "+v"(a2[0][0]) :  : ); SB();
  asm volatile("ds_read_b64_tr_b16 v[180:181], %0 offset:24576" :  : "v"(aW[0][2]) : "memory"); SB();
  asm volatile("v_mfma_f32_16x16x32_bf16 %0, v[160:163], a[28:31], %0" : "+v"(a2[1][0]) :  : ); SB();
  asm volatile("ds_read_b128 a[32:35], %0 offset:0" :  : "v"(aX[0][0]) : "memory"); SB();
  asm volatile("ds_read_b64_tr_b16 v[182:183], %0 offset:25344" :  : "v"(aW[0][2]) : "memory"); SB();
  asm volatile("buffer_load_dwordx4 %0, %1, %2 offen offset:1024 lds" :  : "v"(voA2), "s"(rX), "s"(kx[7]) : "memory"); SB();
  asm volatile("v_mfma_f32_16x16x32_bf16 %0, v[160:163], %1, %0" : "+v"(c2[0]) : "v"(ones) : ); SB();
  asm volatile("ds_read_b128 a[36:39], %0 offset:0" :  : "v"(aX[0][1]) : "memory"); SB();
  asm volatile("ds_read_b128 a[40:43], %0 offset:0" :  : "v"(aX[1][0]) : "memory"); SB();
  asm volatile("s_add_u32 m0, %0, 49152" :  : "s"(mW) : "memory", "scc"); SB();
  asm volatile("v_mfma_f32_16x16x32_bf16 %0, v[164:167], a[24:27], %0" : "+v"(a2[0][1]) :  : ); SB();
  asm volatile("ds_read_b128 a[44:47], %0 offset:0" :  : "v"(aX[1][1]) : "memory"); SB();
  asm volatile("v_mfma_f32_16x16x32_bf16 %0, v[164:167], a[28:31], %0" : "+v"(a2[1][1]) :  : ); SB();
  asm volatile("ds_read_b128 a[48:51], %0 offset:8192" :  : "v"(aX[0][0]) : "memory"); SB();
  asm volatile("v_mfma_f32_16x16x32_bf16 %0, v[164:167], %1, %0" : "+v"(c2[1]) : "v"(ones) : ); SB();
  asm volatile("ds_read_b128 a[56:59], %0 offset:8192" :  : "v"(aX[1][0]) : "memory"); SB();
  asm volatile("v_mfma_f32_16x16x32_bf16 %0, v[168:171], a[24:27], %0" : "+v"(a2[0][2]) :  : ); SB();
  asm volatile("ds_read_b128 a[52:55], %0 offset:8192" :  : "v"(aX[0][1]) : "memory"); SB();
  asm volatile("v_mfma_f32_16x16x32_bf16 %0, v[168:171], a[28:31], %0" : "+v"(a2[1][2]) :  : ); SB();
  asm volatile("ds_read_b128 a[60:63], %0 offset:8192" :  : "v"(aX[1][1]) : "memory"); SB();
  asm volatile("v_mfma_f32_16x16x32_bf16 %0, v[168:171], %1, %0" : "+v"(c2[2]) : "v"(ones) : ); SB();
  asm volatile("buffer_load_dwordx4 %0, %1, %2 offen lds" :  : "v"(voB[0]), "s"(rW), "s"(kw_[4]) : "memory"); SB();
  asm volatile("v_mfma_f32_16x16x32_bf16 %0, %1, a[24:27], %0" : "+v"(rs) : "v"(ones) : ); SB();
  asm volatile("ds_read_b64_tr_b16 v[184:185], %0 offset:30720" :  : "v"(aW[0][0]) : "memory"); SB();
  asm volatile("v_mfma_f32_16x16x32_bf16 %0, a[24:27], a[24:27], %0" : "+v"(gr) :  : ); SB();
  asm volatile("ds_read_b64_tr_b16 v[186:187], %0 offset:31488" :  : "v"(aW[0][0]) : "memory"); SB();
  asm volatile("s_waitcnt lgkmcnt(8)\n\tv_mfma_f32_16x16x32_bf16 %0, v[172:175], a[32:35], %0" : "+v"(a2[0][0]) :  : ); SB();
  asm volatile("ds_read_b64_tr_b16 v[188:189], %0 offset:30720" :  : "v"(aW[0][1]) : "memory"); SB();
  asm volatile("v_mfma_f32_16x16x32_bf16 %0, v[172:175], a[36:39], %0" : "+v"(a2[1][0]) :  : ); SB();
  asm volatile("ds_read_b64_tr_b16 v[190:191], %0 offset:31488" :  : "v"(aW[0][1]) : "memory"); SB();
  asm volatile("buffer_load_dwordx4 %0, %1, %2 offen offset:1024 lds" :  : "v"(voB[1]), "s"(rW), "s"(kw_[4]) : "memory"); SB();
  asm volatile("v_mfma_f32_16x16x32_bf16 %0, v[172:175], %1, %0" : "+v"(c2[0]) : "v"(ones) : ); SB();
  asm volatile("ds_read_b64_tr_b16 v[192:193], %0 offset:30720" :  : "v"(aW[0][2]) : "memory"); SB();
  asm volatile("v_mfma_f32_16x16x32_bf16 %0, v[176:179], a[32:35], %0" : "+v"(a2[0][1]) :  : ); SB();
  asm volatile("ds_read_b64_tr_b16 v[194:195], %0 offset:31488" :  : "v"(aW[0][2]) : "memory"); SB();
  asm volatile("v_mfma_f32_16x16x32_bf16 %0, v[176:179], a[36:39], %0" : "+v"(a2[1][1]) :  : ); SB();
  asm volatile("buffer_load_dwordx4 %0, %1, %2 offen offset:2048 lds" :  : "v"(voB[2]), "s"(rW), "s"(kw_[4]) : "memory"); SB();
  asm volatile("s_add_u32 m0, %0, 147456" :  : "s"(mX) : "memory", "scc"); SB();
  asm volatile("v_mfma_f32_16x16x32_bf16 %0, v[176:179], %1, %0" : "+v"(c2[1]) : "v"(ones) : ); SB();
  asm volatile("buffer_load_dwordx4 %0, %1, %2 offen lds" :  : "v"(voA), "s"(rX), "s"(kx[8]) : "memory"); SB();
  asm volatile("v_mfma_f32_16x16x32_bf16 %0, v[180:183], a[32:35], %0" : "+v"(a2[0][2]) :  : ); SB();
  asm volatile("buffer_load_dwordx4 %0, %1, %2 offen offset:1024 lds" :  : "v"(voA2), "s"(rX), "s"(kx[8]) : "memory"); SB();
  asm volatile("v_mfma_f32_16x16x32_bf16 %0, v[180:183], a[36:39], %0" : "+v"(a2[1][2]) :  : ); SB();
  asm volatile("v_mfma_f32_16x16x32_bf16 %0, v[180:183], %1, %0" : "+v"(c2[2]) : "v"(ones) : ); SB();
  asm volatile("s_add_u32 m0, %0, 155648" :  : "s"(mX) : "memory", "scc"); SB();
  asm volatile("v_mfma_f32_16x16x32_bf16 %0, %1, a[32:35], %0" : "+v"(rs) : "v"(ones) : ); SB();
  asm volatile("buffer_load_dwordx4 %0, %1, %2 offen lds" :  : "v"(voA), "s"(rX), "s"(kx[9]) : "memory"); SB();
  asm volatile("v_mfma_f32_16x16x32_bf16 %0, a[32:35], a[32:35], %0" : "+v"(gr) :  : ); SB();
  asm volatile("s_waitcnt lgkmcnt(2)\n\tv_mfma_f32_16x16x32_bf16 %0, %1, a[40:43], %0" : "+v"(rs) : "v"(ones) : ); SB();
  asm volatile("s_waitcnt vmcnt(10)\n\ts_barrier" :  :  : "memory"); SB();
  asm volatile("ds_read_b64_tr_b16 v[160:161], %0 offset:36864" :  : "v"(aW[0][0]) : "memory"); SB();
  asm volatile("v_mfma_f32_16x16x32_bf16 %0, a[40:43], a[40:43], %0" : "+v"(gr) :  : ); SB();
  asm volatile("ds_read_b64_tr_b16 v[162:163], %0 offset:37632" :  : "v"(aW[0][0]) : "memory"); SB();
  asm volatile("v_mfma_f32_16x16x32_bf16 %0, v[184:187], a[40:43], %0" : "+v"(a2[0][0]) :  : ); SB();
  asm volatile("ds_read_b64_tr_b16 v[164:165], %0 offset:36864" :  : "v"(aW[0][1]) : "memory"); SB();
  asm volatile("v_mfma_f32_16x16x32_bf16 %0, v[184:187], a[44:47], %0" : "+v"(a2[1][0]) :  : ); SB();
  asm volatile("ds_read_b64_tr_b16 v[166:167], %0 offset:37632" :  : "v"(aW[0][1]) : "memory"); SB();
  asm volatile("v_mfma_f32_16x16x32_bf16 %0, v[184:187], %1, %0" : "+v"(c2[0]) : "v"(ones) : ); SB();
  asm volatile("ds_read_b64_tr_b16 v[168:169], %0 offset:36864" :  : "v"(aW[0][2]) : "memory"); SB();
  asm volatile("v_mfma_f32_16x16x32_bf16 %0, v[188:191], a[40:43], %0" : "+v"(a2[0][1]) :  : ); SB();
  asm volatile("ds_read_b64_tr_b16 v[170:171], %0 offset:37632" :  : "v"(aW[0][2]) : "memory"); SB();
  asm volatile("v_mfma_f32_16x16x32_bf16 %0, v[188:191], a[44:47], %0" : "+v"(a2[1][1]) :  : ); SB();
  asm volatile("ds_read_b128 a[64:67], %0 offset:16384" :  : "v"(aX[0][0]) : "memory"); SB();
  asm volatile("v_mfma_f32_16x16x32_bf16 %0, v[188:191], %1, %0" : "+v"(c2[1]) : "v"(ones) : ); SB();
  asm volatile("ds_read_b128 a[72:75], %0 offset:16384" :  : "v"(aX[1][0]) : "memory"); SB();
  asm volatile("s_waitcnt lgkmcnt(8)\n\tv_mfma_f32_16x16x32_bf16 %0, v[192:195], a[40:43], %0" : "+v"(a2[0][2]) :  : ); SB();
  asm volatile("ds_read_b128 a[80:83], %0 offset:24576" :  : "v"(aX[0][0]) : "memory"); SB();
  asm volatile("v_mfma_f32_16x16x32_bf16 %0, v[192:195], a[44:47], %0" : "+v"(a2[1][2]) :  : ); SB();
  asm volatile("ds_read_b128 a[88:91], %0 offset:24576" :  : "v"(aX[1][0]) : "memory"); SB();
  asm volatile("v_mfma_f32_16x16x32_bf16 %0, v[192:195], %1, %0" : "+v"(c2[2]) : "v"(ones) : ); SB();
  asm volatile("ds_read_b128 a[68:71], %0 offset:16384" :  : "v"(aX[0][1]) : "memory"); SB();
  asm volatile("v_mfma_f32_16x16x32_bf16 %0, %1, a[48:51], %0" : "+v"(rs) : "v"(ones) : ); SB();
  asm volatile("ds_read_b128 a[76:79], %0 offset:16384" :  : "v"(aX[1][1]) : "memory"); SB();
  asm volatile("v_mfma_f32_16x16x32_bf16 %0, a[48:51], a[48:51], %0" : "+v"(gr) :  : ); SB();
  asm volatile("buffer_load_dwordx4 %0, %1, %2 offen offset:1024 lds" :  : "v"(voA2), "s"(rX), "s"(kx[9]) : "memory"); SB();
  asm volatile("s_add_u32 m0, %0, 61440" :  : "s"(mW) : "memory", "scc"); SB();
  asm volatile("v_mfma_f32_16x16x32_bf16 %0, %1, a[56:59], %0" : "+v"(rs) : "v"(ones) : ); SB();
  asm volatile("ds_read_b64_tr_b16 v[172:173], %0 offset:43008" :  : "v"(aW[0][0]) : "memory"); SB();
  asm volatile("v_mfma_f32_16x16x32_bf16 %0, a[56:59], a[56:59], %0" : "+v"(gr) :  : ); SB();
  asm volatile("ds_read_b64_tr_b16 v[174:175], %0 offset:43776" :  : "v"(aW[0][0]) : "memory"); SB();
  asm volatile("s_waitcnt lgkmcnt(8)\n\tv_mfma_f32_16x16x32_bf16 %0, v[160:163], a[48:51], %0" : "+v"(a2[0][0]) :  : ); SB();
  asm volatile("ds_read_b64_tr_b16 v[176:177], %0 offset:43008" :  : "v"(aW[0][1]) : "memory"); SB();
  asm volatile("v_mfma_f32_16x16x32_bf16 %0, v[160:163], a[52:55], %0" : "+v"(a2[1][0]) :  : ); SB();
  asm volatile("ds_read_b64_tr_b16 v[178:179], %0 offset:43776" :  : "v"(aW[0][1]) : "memory"); SB();
  asm volatile("buffer_load_dwordx4 %0, %1, %2 offen lds" :  : "v"(voB[0]), "s"(rW), "s"(kw_[5]) : "memory"); SB();
  asm volatile("v_mfma_f32_16x16x32_bf16 %0, v[160:163], %1, %0" : "+v"(c2[0]) : "v"(ones) : ); SB();
  asm volatile("ds_read_b64_tr_b16 v[180:181], %0 offset:43008" :  : "v"(aW[0][2]) : "memory"); SB();
  asm volatile("v_mfma_f32_16x16x32_bf16 %0, v[164:167], a[48:51], %0" : "+v"(a2[0][1]) :  : ); SB();
  asm volatile("ds_read_b64_tr_b16 v[182:183], %0 offset:43776" :  : "v"(aW[0][2]) : "memory"); SB();
  asm volatile("v_mfma_f32_16x16x32_bf16 %0, v[164:167], a[52:55], %0" : "+v"(a2[1][1]) :  : ); SB();
  asm volatile("ds_read_b128 a[84:87], %0 offset:24576" :  : "v"(aX[0][1]) : "memory"); SB();
  asm volatile("v_mfma_f32_16x16x32_bf16 %0, v[164:167], %1, %0" : "+v"(c2[1]) : "v"(ones) : ); SB();
  asm volatile("ds_read_b128 a[92:95], %0 offset:24576" :  : "v"(aX[1][1]) : "memory"); SB();
  asm volatile("v_mfma_f32_16x16x32_bf16 %0, v[168:171], a[48:51], %0" : "+v"(a2[0][2]) :  : ); SB();
  asm volatile("buffer_load_dwordx4 %0, %1, %2 offen offset:1024 lds" :  : "v"(voB[1]), "s"(rW), "s"(kw_[5]) : "memory"); SB();
  asm volatile("v_mfma_f32_16x16x32_bf16 %0, v[168:171], a[52:55], %0" : "+v"(a2[1][2]) :  : ); SB();
  asm volatile("buffer_load_dwordx4 %0, %1, %2 offen offset:2048 lds" :  : "v"(voB[2]), "s"(rW), "s"(kw_[5]) : "memory"); SB();
  asm volatile("s_add_u32 m0, %0, 73728" :  : "s"(mW) : "memory", "scc"); SB();
  asm volatile("v_mfma_f32_16x16x32_bf16 %0, v[168:171], %1, %0" : "+v"(c2[2]) : "v"(ones) : ); SB();
  asm volatile("buffer_load_dwordx4 %0, %1, %2 offen lds" :  : "v"(voB[0]), "s"(rW), "s"(kw_[6]) : "memory"); SB();
  asm volatile("s_waitcnt lgkmcnt(5)\n\tv_mfma_f32_16x16x32_bf16 %0, %1, a[64:67], %0" : "+v"(rs) : "v"(ones) : ); SB();
  asm volatile("v_mfma_f32_16x16x32_bf16 %0, a[64:67], a[64:67], %0" : "+v"(gr) :  : ); SB();
  asm volatile("v_mfma_f32_16x16x32_bf16 %0, %1, a[72:75], %0" : "+v"(rs) : "v"(ones) : ); SB();
  asm volatile("buffer_load_dwordx4 %0, %1, %2 offen offset:1024 lds" :  : "v"(voB[1]), "s"(rW), "s"(kw_[6]) : "memory"); SB();
  asm volatile("buffer_load_dwordx4 %0, %1, %2 offen offset:2048 lds" :  : "v"(voB[2]), "s"(rW), "s"(kw_[6]) : "memory"); SB();
  asm volatile("s_waitcnt vmcnt(10)\n\ts_barrier" :  :  : "memory"); SB();
  asm volatile("ds_read_b64_tr_b16 v[184:185], %0 offset:0" :  : "v"(aW[1][0]) : "memory"); SB();
  asm volatile("s_add_u32 m0, %0, 86016" :  : "s"(mW) : "memory", "scc"); SB();
  asm volatile("v_mfma_f32_16x16x32_bf16 %0, a[72:75], a[72:75], %0" : "+v"(gr) :  : ); SB();
  asm volatile("ds_read_b64_tr_b16 v[186:187], %0 offset:768" :  : "v"(aW[1][0]) : "memory"); SB();
  asm volatile("v_mfma_f32_16x16x32_bf16 %0, v[172:175], a[56:59], %0" : "+v"(a2[0][0]) :  : ); SB();
  asm volatile("ds_read_b64_tr_b16 v[188:189], %0 offset:0" :  : "v"(aW[1][1]) : "memory"); SB();
  asm volatile("v_mfma_f32_16x16x32_bf16 %0, v[172:175], a[60:63], %0" : "+v"(a2[1][0]) :  : ); SB();
  asm volatile("ds_read_b64_tr_b16 v[190:191], %0 offset:768" :  : "v"(aW[1][1]) : "memory"); SB();
  asm volatile("v_mfma_f32_16x16x32_bf16 %0, v[172:175], %1, %0" : "+v"(c2[0]) : "v"(ones) : ); SB();
  asm volatile("ds_read_b64_tr_b16 v[192:193], %0 offset:0" :  : "v"(aW[1][2]) : "memory"); SB();
  asm volatile("s_waitcnt lgkmcnt(5)\n\tv_mfma_f32_16x16x32_bf16 %0, v[176:179], a[56:59], %0" : "+v"(a2[0][1]) :  : ); SB();
  asm volatile("ds_read_b64_tr_b16 v[194:195], %0 offset:768" :  : "v"(aW[1][2]) : "memory"); SB();
  asm volatile("buffer_load_dwordx4 %0, %1, %2 offen lds" :  : "v"(voB[0]), "s"(rW), "s"(kw_[7]) : "memory"); SB();
  asm volatile("v_mfma_f32_16x16x32_bf16 %0, v[176:179], a[60:63], %0" : "+v"(a2[1][1]) :  : ); SB();
  asm volatile("ds_read_b128 a[96:99], %0 offset:32768" :  : "v"(aX[0][0]) : "memory"); SB();
  asm volatile("v_mfma_f32_16x16x32_bf16 %0, v[176:179], %1, %0" : "+v"(c2[1]) : "v"(ones) : ); SB();
  asm volatile("ds_read_b128 a[104:107], %0 offset:32768" :  : "v"(aX[1][0]) : "memory"); SB();
  asm volatile("v_mfma_f32_16x16x32_bf16 %0, v[180:183], a[56:59], %0" : "+v"(a2[0][2]) :  : ); SB();
  asm volatile("ds_read_b128 a[112:115], %0 offset:40960" :  : "v"(aX[0][0]) : "memory"); SB();
  asm volatile("v_mfma_f32_16x16x32_bf16 %0, v[180:183], a[60:63], %0" : "+v"(a2[1][2]) :  : ); SB();
  asm volatile("buffer_load_dwordx4 %0, %1, %2 offen offset:1024 lds" :  : "v"(voB[1]), "s"(rW), "s"(kw_[7]) : "memory"); SB();
  asm volatile("v_mfma_f32_16x16x32_bf16 %0, v[180:183], %1, %0" : "+v"(c2[2]) : "v"(ones) : ); SB();
  asm volatile("ds_read_b128 a[120:123], %0 offset:40960" :  : "v"(aX[1][0]) : "memory"); SB();
  asm volatile("v_mfma_f32_16x16x32_bf16 %0, %1, a[80:83], %0" : "+v"(rs) : "v"(ones) : ); SB();
  asm volatile("ds_read_b128 a[100:103], %0 offset:32768" :  : "v"(aX[0][1]) : "memory"); SB();
  asm volatile("v_mfma_f32_16x16x32_bf16 %0, a[80:83], a[80:83], %0" : "+v"(gr) :  : ); SB();
  asm volatile("ds_read_b128 a[108:111], %0 offset:32768" :  : "v"(aX[1][1]) : "memory"); SB();
  asm volatile("v_mfma_f32_16x16x32_bf16 %0, %1, a[88:91], %0" : "+v"(rs) : "v"(ones) : ); SB();
  asm volatile("ds_read_b64_tr_b16 v[160:161], %0 offset:6144" :  : "v"(aW[1][0]) : "memory"); SB();
  asm volatile("v_mfma_f32_16x16x32_bf16 %0, a[88:91], a[88:91], %0" : "+v"(gr) :  : ); SB();
  asm volatile("ds_read_b64_tr_b16 v[162:163], %0 offset:6912" :  : "v"(aW[1][0]) : "memory"); SB();
  asm volatile("s_waitcnt lgkmcnt(8)\n\tv_mfma_f32_16x16x32_bf16 %0, v[184:187], a[64:67], %0" : "+v"(a2[0][0]) :  : ); SB();
  asm volatile("ds_read_b64_tr_b16 v[164:165], %0 offset:6144" :  : "v"(aW[1][1]) : "memory"); SB();
  asm volatile("v_mfma_f32_16x16x32_bf16 %0, v[184:187], a[68:71], %0" : "+v"(a2[1][0]) :  : ); SB();
  asm volatile("ds_read_b64_tr_b16 v[166:167], %0 offset:6912" :  : "v"(aW[1][1]) : "memory"); SB();
  asm volatile("v_mfma_f32_16x16x32_bf16 %0, v[184:187], %1, %0" : "+v"(c2[0]) : "v"(ones) : ); SB();
  asm volatile("ds_read_b64_tr_b16 v[168:169], %0 offset:6144" :  : "v"(aW[1][2]) : "memory"); SB();
  asm volatile("v_mfma_f32_16x16x32_bf16 %0, v[188:191], a[64:67], %0" : "+v"(a2[0][1]) :  : ); SB();
  asm volatile("ds_read_b64_tr_b16 v[170:171], %0 offset:6912" :  : "v"(aW[1][2]) : "memory"); SB();
  asm volatile("v_mfma_f32_16x16x32_bf16 %0, v[188:191], a[68:71], %0" : "+v"(a2[1][1]) :  : ); SB();
  asm volatile("ds_read_b128 a[116:119], %0 offset:40960" :  : "v"(aX[0][1]) : "memory"); SB();
  asm volatile("v_mfma_f32_16x16x32_bf16 %0, v[188:191], %1, %0" : "+v"(c2[1]) : "v"(ones) : ); SB();
  asm volatile("ds_read_b128 a[124:127], %0 offset:40960" :  : "v"(aX[1][1]) : "memory"); SB();
  asm volatile("v_mfma_f32_16x16x32_bf16 %0, v[192:195], a[64:67], %0" : "+v"(a2[0][2]) :  : ); SB();
  asm volatile("buffer_load_dwordx4 %0, %1, %2 offen offset:2048 lds" :  : "v"(voB[2]), "s"(rW), "s"(kw_[7]) : "memory"); SB();
  asm volatile("s_add_u32 m0, %0, 0" :  : "s"(mW) : "memory", "scc"); SB();
  asm volatile("v_mfma_f32_16x16x32_bf16 %0, v[192:195], a[68:71], %0" : "+v"(a2[1][2]) :  : ); SB();
  asm volatile("buffer_load_dwordx4 %0, %1, %2 offen lds" :  : "v"(voB[0]), "s"(rW), "s"(kw_[8]) : "memory"); SB();
  asm volatile("v_mfma_f32_16x16x32_bf16 %0, v[192:195], %1, %0" : "+v"(c2[2]) : "v"(ones) : ); SB();
  asm volatile("buffer_load_dwordx4 %0, %1, %2 offen offset:1024 lds" :  : "v"(voB[1]), "s"(rW), "s"(kw_[8]) : "memory"); SB();
  asm volatile("s_waitcnt lgkmcnt(5)\n\tv_mfma_f32_16x16x32_bf16 %0, %1, a[96:99], %0" : "+v"(rs) : "v"(ones) : ); SB();
  asm volatile("buffer_load_dwordx4 %0, %1, %2 offen offset:2048 lds" :  : "v"(voB[2]), "s"(rW), "s"(kw_[8]) : "memory"); SB();
  asm volatile("v_mfma_f32_16x16x32_bf16 %0, a[96:99], a[96:99], %0" : "+v"(gr) :  : ); SB();
  asm volatile("v_mfma_f32_16x16x32_bf16 %0, %1, a[104:107], %0" : "+v"(rs) : "v"(ones) : ); SB();
  asm volatile("s_add_u32 m0, %0, 12288" :  : "s"(mW) : "memory", "scc"); SB();
  asm volatile("s_nop 0\n\tbuffer_load_dwordx4 %0, %1, %2 offen lds" :  : "v"(voB[0]), "s"(rW), "s"(kw_[9]) : "memory"); SB();
  asm volatile("s_waitcnt vmcnt(10)\n\ts_barrier" :  :  : "memory"); SB();
  asm volatile("ds_read_b64_tr_b16 v[172:173], %0 offset:12288" :  : "v"(aW[1][0]) : "memory"); SB();
  asm volatile("v_mfma_f32_16x16x32_bf16 %0, a[104:107], a[104:107], %0" : "+v"(gr) :  : ); SB();
  asm volatile("ds_read_b64_tr_b16 v[174:175], %0 offset:13056" :  : "v"(aW[1][0]) : "memory"); SB();
  asm volatile("v_mfma_f32_16x16x32_bf16 %0, v[160:163], a[72:75], %0" : "+v"(a2[0][0]) :  : ); SB();
  asm volatile("ds_read_b64_tr_b16 v[176:177], %0 offset:12288" :  : "v"(aW[1][1]) : "memory"); SB();
  asm volatile("v_mfma_f32_16x16x32_bf16 %0, v[160:163], a[76:79], %0" : "+v"(a2[1][0]) :  : ); SB();
  asm volatile("ds_read_b64_tr_b16 v[178:179], %0 offset:13056" :  : "v"(aW[1][1]) : "memory"); SB();
  asm volatile("v_mfma_f32_16x16x32_bf16 %0, v[160:163], %1, %0" : "+v"(c2[0]) : "v"(ones) : ); SB();
  asm volatile("ds_read_b64_tr_b16 v[180:181], %0 offset:12288" :  : "v"(aW[1][2]) : "memory"); SB();
  asm volatile("s_waitcnt lgkmcnt(5)\n\tv_mfma_f32_16x16x32_bf16 %0, v[164:167], a[72:75], %0" : "+v"(a2[0][1]) :  : ); SB();
  asm volatile("ds_read_b64_tr_b16 v[182:183], %0 offset:13056" :  : "v"(aW[1][2]) : "memory"); SB();
  asm volatile("v_mfma_f32_16x16x32_bf16 %0, v[164:167], a[76:79], %0" : "+v"(a2[1][1]) :  : ); SB();
  asm volatile("ds_read_b128 a[128:131], %0 offset:49152" :  : "v"(aX[0][0]) : "memory"); SB();
  asm volatile("v_mfma_f32_16x16x32_bf16 %0, v[164:167], %1, %0" : "+v"(c2[1]) : "v"(ones) : ); SB();
  asm volatile("ds_read_b128 a[136:139], %0 offset:49152" :  : "v"(aX[1][0]) : "memory"); SB();
  asm volatile("v_mfma_f32_16x16x32_bf16 %0, v[168:171], a[72:75], %0" : "+v"(a2[0][2]) :  : ); SB();
  asm volatile("ds_read_b128 a[144:147], %0 offset:57344" :  : "v"(aX[0][0]) : "memory"); SB();
  asm volatile("v_mfma_f32_16x16x32_bf16 %0, v[168:171], a[76:79], %0" : "+v"(a2[1][2]) :  : ); SB();
  asm volatile("ds_read_b128 a[152:155], %0 offset:57344" :  : "v"(aX[1][0]) : "memory"); SB();
  asm volatile("v_mfma_f32_16x16x32_bf16 %0, v[168:171], %1, %0" : "+v"(c2[2]) : "v"(ones) : ); SB();
  asm volatile("ds_read_b128 a[132:135], %0 offset:49152" :  : "v"(aX[0][1]) : "memory"); SB();
  asm volatile("v_mfma_f32_16x16x32_bf16 %0, %1, a[112:115], %0" : "+v"(rs) : "v"(ones) : ); SB();
  asm volatile("ds_read_b128 a[140:143], %0 offset:49152" :  : "v"(aX[1][1]) : "memory"); SB();
  asm volatile("v_mfma_f32_16x16x32_bf16 %0, a[112:115], a[112:115], %0" : "+v"(gr) :  : ); SB();
  asm volatile("ds_read_b128 a[148:151], %0 offset:57344" :  : "v"(aX[0][1]) : "memory"); SB();
  asm volatile("v_mfma_f32_16x16x32_bf16 %0, %1, a[120:123], %0" : "+v"(rs) : "v"(ones) : ); SB();
  asm volatile("ds_read_b64_tr_b16 v[184:185], %0 offset:18432" :  : "v"(aW[1][0]) : "memory"); SB();
  asm volatile("v_mfma_f32_16x16x32_bf16 %0, a[120:123], a[120:123], %0" : "+v"(gr) :  : ); SB();
  asm volatile("ds_read_b64_tr_b16 v[186:187], %0 offset:19200" :  : "v"(aW[1][0]) : "memory"); SB();
  asm volatile("s_waitcnt lgkmcnt(9)\n\tv_mfma_f32_16x16x32_bf16 %0, v[172:175], a[80:83], %0" : "+v"(a2[0][0]) :  : ); SB();
  asm volatile("ds_read_b64_tr_b16 v[188:189], %0 offset:18432" :  : "v"(aW[1][1]) : "memory"); SB();
  asm volatile("v_mfma_f32_16x16x32_bf16 %0, v[172:175], a[84:87], %0" : "+v"(a2[1][0]) :  : ); SB();
  asm volatile("ds_read_b64_tr_b16 v[190:191], %0 offset:19200" :  : "v"(aW[1][1]) : "memory"); SB();
  asm volatile("v_mfma_f32_16x16x32_bf16 %0, v[172:175], %1, %0" : "+v"(c2[0]) : "v"(ones) : ); SB();
  asm volatile("ds_read_b64_tr_b16 v[192:193], %0 offset:18432" :  : "v"(aW[1][2]) : "memory"); SB();
  asm volatile("v_mfma_f32_16x16x32_bf16 %0, v[176:179], a[80:83], %0" : "+v"(a2[0][1]) :  : ); SB();
  asm volatile("ds_read_b64_tr_b16 v[194:195], %0 offset:19200" :  : "v"(aW[1][2]) : "memory"); SB();
  asm volatile("v_mfma_f32_16x16x32_bf16 %0, v[176:179], a[84:87], %0" : "+v"(a2[1][1]) :  : ); SB();
  asm volatile("ds_read_b128 a[156:159], %0 offset:57344" :  : "v"(aX[1][1]) : "memory"); SB();
  asm volatile("v_mfma_f32_16x16x32_bf16 %0, v[176:179], %1, %0" : "+v"(c2[1]) : "v"(ones) : ); SB();
  ao[0] = (w ^ 0) * 65536 + 3072; asm volatile("" : "+s"(ao[0])); SB();
  ao[1] = ao[0] + 31744; asm volatile("" : "+s"(ao[1])); SB();
  asm volatile("v_mfma_f32_16x16x32_bf16 %0, v[180:183], a[80:83], %0" : "+v"(a2[0][2]) :  : ); SB();
  ao[2] = (w ^ 1) * 65536 + 1024; asm volatile("" : "+s"(ao[2])); SB();
  ao[3] = ao[2] + 31744; asm volatile("" : "+s"(ao[3])); SB();
  asm volatile("v_mfma_f32_16x16x32_bf16 %0, v[180:183], a[84:87], %0" : "+v"(a2[1][2]) :  : ); SB();
  asm volatile("buffer_load_dwordx4 %0, %1, %2 offen offset:1024 lds" :  : "v"(voB[1]), "s"(rW), "s"(kw_[9]) : "memory"); SB();
  asm volatile("v_mfma_f32_16x16x32_bf16 %0, v[180:183], %1, %0" : "+v"(c2[2]) : "v"(ones) : ); SB();
  ao[4] = (w ^ 2) * 65536 + 3072; asm volatile("" : "+s"(ao[4])); SB();
  asm volatile("s_waitcnt lgkmcnt(5)\n\tv_mfma_f32_16x16x32_bf16 %0, %1, a[128:131], %0" : "+v"(rs) : "v"(ones) : ); SB();
  ao[5] = ao[4] + 31744; asm volatile("" : "+s"(ao[5])); SB();
  ao[6] = (w ^ 3) * 65536 + 1024; asm volatile("" : "+s"(ao[6])); SB();
  asm volatile("v_mfma_f32_16x16x32_bf16 %0, a[128:131], a[128:131], %0" : "+v"(gr) :  : ); SB();
  ao[7] = ao[6] + 31744; asm volatile("" : "+s"(ao[7])); SB();
  asm volatile("buffer_load_dwordx4 %0, %1, %2 offen offset:2048 lds" :  : "v"(voB[2]), "s"(rW), "s"(kw_[9]) : "memory"); SB();
  asm volatile("v_mfma_f32_16x16x32_bf16 %0, %1, a[136:139], %0" : "+v"(rs) : "v"(ones) : ); SB();
  asm volatile("s_waitcnt vmcnt(9)\n\ts_barrier" :  :  : "memory"); SB();
  asm volatile("ds_read_b64_tr_b16 v[160:161], %0 offset:24576" :  : "v"(aW[1][0]) : "memory"); SB();
  asm volatile("s_add_u32 m0, %0, 98304" :  : "s"(mA) : "memory", "scc"); SB();
  asm volatile("v_mfma_f32_16x16x32_bf16 %0, a[136:139], a[136:139], %0" : "+v"(gr) :  : ); SB();
  asm volatile("ds_read_b64_tr_b16 v[162:163], %0 offset:25344" :  : "v"(aW[1][0]) : "memory"); SB();
  asm volatile("v_mfma_f32_16x16x32_bf16 %0, v[184:187], a[88:91], %0" : "+v"(a2[0][0]) :  : ); SB();
  asm volatile("ds_read_b64_tr_b16 v[164:165], %0 offset:24576" :  : "v"(aW[1][1]) : "memory"); SB();
  asm volatile("v_mfma_f32_16x16x32_bf16 %0, v[184:187], a[92:95], %0" : "+v"(a2[1][0]) :  : ); SB();
  asm volatile("ds_read_b64_tr_b16 v[166:167], %0 offset:25344" :  : "v"(aW[1][1]) : "memory"); SB();
  asm volatile("v_mfma_f32_16x16x32_bf16 %0, v[184:187], %1, %0" : "+v"(c2[0]) : "v"(ones) : ); SB();
  asm volatile("ds_read_b64_tr_b16 v[168:169], %0 offset:24576" :  : "v"(aW[1][2]) : "memory"); SB();
  asm volatile("s_waitcnt lgkmcnt(5)\n\tv_mfma_f32_16x16x32_bf16 %0, v[188:191], a[88:91], %0" : "+v"(a2[0][1]) :  : ); SB();
  asm volatile("ds_read_b64_tr_b16 v[170:171], %0 offset:25344" :  : "v"(aW[1][2]) : "memory"); SB();
  asm volatile("v_mfma_f32_16x16x32_bf16 %0, v[188:191], a[92:95], %0" : "+v"(a2[1][1]) :  : ); SB();
  asm volatile("buffer_load_dwordx4 %0, %1, %2 offen lds" :  : "v"(voK[0]), "s"(rK), "s"(0u) : "memory"); SB();
  asm volatile("v_mfma_f32_16x16x32_bf16 %0, v[188:191], %1, %0" : "+v"(c2[1]) : "v"(ones) : ); SB();
  asm volatile("buffer_load_dwordx4 %0, %1, %2 offen offset:1024 lds" :  : "v"(voK[1]), "s"(rK), "s"(0u) : "memory"); SB();
  asm volatile("v_mfma_f32_16x16x32_bf16 %0, v[192:195], a[88:91], %0" : "+v"(a2[0][2]) :  : ); SB();
  asm volatile("buffer_load_dwordx4 %0, %1, %2 offen offset:2048 lds" :  : "v"(voK[2]), "s"(rK), "s"(0u) : "memory"); SB();
  asm volatile("v_mfma_f32_16x16x32_bf16 %0, v[192:195], a[92:95], %0" : "+v"(a2[1][2]) :  : ); SB();
  asm volatile("ds_read_b64_tr_b16 v[172:173], %0 offset:30720" :  : "v"(aW[1][0]) : "memory"); SB();
  asm volatile("v_mfma_f32_16x16x32_bf16 %0, v[192:195], %1, %0" : "+v"(c2[2]) : "v"(ones) : ); SB();
  asm volatile("ds_read_b64_tr_b16 v[174:175], %0 offset:31488" :  : "v"(aW[1][0]) : "memory"); SB();
  asm volatile("v_mfma_f32_16x16x32_bf16 %0, %1, a[144:147], %0" : "+v"(rs) : "v"(ones) : ); SB();
  asm volatile("ds_read_b64_tr_b16 v[176:177], %0 offset:30720" :  : "v"(aW[1][1]) : "memory"); SB();
  asm volatile("v_mfma_f32_16x16x32_bf16 %0, a[144:147], a[144:147], %0" : "+v"(gr) :  : ); SB();
  asm volatile("ds_read_b64_tr_b16 v[178:179], %0 offset:31488" :  : "v"(aW[1][1]) : "memory"); SB();
  asm volatile("v_mfma_f32_16x16x32_bf16 %0, %1, a[152:155], %0" : "+v"(rs) : "v"(ones) : ); SB();
  asm volatile("ds_read_b64_tr_b16 v[180:181], %0 offset:30720" :  : "v"(aW[1][2]) : "memory"); SB();
  asm volatile("v_mfma_f32_16x16x32_bf16 %0, a[152:155], a[152:155], %0" : "+v"(gr) :  : ); SB();
  asm volatile("ds_read_b64_tr_b16 v[182:183], %0 offset:31488" :  : "v"(aW[1][2]) : "memory"); SB();
  asm volatile("s_waitcnt lgkmcnt(6)\n\tv_mfma_f32_16x16x32_bf16 %0, v[160:163], a[96:99], %0" : "+v"(a2[0][0]) :  : ); SB();
  asm volatile("s_mov_b32 exec_lo, 0xff\n\ts_mov_b32 exec_hi, 0\n\tbuffer_load_dwordx4 %0, %1, %2 offen offset:3072 lds\n\ts_mov_b64 exec, -1" :  : "v"(voK[3]), "s"(rK), "s"(0u) : "memory"); SB();
  asm volatile("v_mfma_f32_16x16x32_bf16 %0, v[160:163], a[100:103], %0" : "+v"(a2[1][0]) :  : ); SB();
  asm volatile("v_mfma_f32_16x16x32_bf16 %0, v[160:163], %1, %0" : "+v"(c2[0]) : "v"(ones) : ); SB();
  asm volatile("v_mfma_f32_16x16x32_bf16 %0, v[164:167], a[96:99], %0" : "+v"(a2[0][1]) :  : ); SB();
  asm volatile("s_mov_b32 exec_lo, -1\n\ts_mov_b32 exec_hi, 0xff\n\tbuffer_load_dwordx4 %0, %1, %2 offen offset:3328 lds\n\ts_mov_b64 exec, -1" :  : "v"(voH), "s"(rS), "s"(0u) : "memory"); SB();
  asm volatile("s_waitcnt vmcnt(11)\n\ts_barrier" :  :  : "memory"); SB();
  asm volatile("ds_read_b64_tr_b16 v[184:185], %0 offset:36864" :  : "v"(aW[1][0]) : "memory"); SB();
  asm volatile("s_add_u32 m0, %0, 102400" :  : "s"(mA) : "memory", "scc"); SB();
  asm volatile("v_mfma_f32_16x16x32_bf16 %0, v[164:167], a[100:103], %0" : "+v"(a2[1][1]) :  : ); SB();
  asm volatile("ds_read_b64_tr_b16 v[186:187], %0 offset:37632" :  : "v"(aW[1][0]) : "memory"); SB();
  asm volatile("v_mfma_f32_16x16x32_bf16 %0, v[164:167], %1, %0" : "+v"(c2[1]) : "v"(ones) : ); SB();
  asm volatile("ds_read_b64_tr_b16 v[188:189], %0 offset:36864" :  : "v"(aW[1][1]) : "memory"); SB();
  asm volatile("v_mfma_f32_16x16x32_bf16 %0, v[168:171], a[96:99], %0" : "+v"(a2[0][2]) :  : ); SB();
  asm volatile("ds_read_b64_tr_b16 v[190:191], %0 offset:37632" :  : "v"(aW[1][1]) : "memory"); SB();
  asm volatile("v_mfma_f32_16x16x32_bf16 %0, v[168:171], a[100:103], %0" : "+v"(a2[1][2]) :  : ); SB();
  asm volatile("ds_read_b64_tr_b16 v[192:193], %0 offset:36864" :  : "v"(aW[1][2]) : "memory"); SB();
  asm volatile("v_mfma_f32_16x16x32_bf16 %0, v[168:171], %1, %0" : "+v"(c2[2]) : "v"(ones) : ); SB();
  asm volatile("ds_read_b64_tr_b16 v[194:195], %0 offset:37632" :  : "v"(aW[1][2]) : "memory"); SB();
  asm volatile("s_waitcnt lgkmcnt(6)\n\tv_mfma_f32_16x16x32_bf16 %0, v[172:175], a[104:107], %0" : "+v"(a2[0][0]) :  : ); SB();
  asm volatile("buffer_load_dwordx4 %0, %1, %2 offen lds" :  : "v"(voV[0]), "s"(rV), "s"(0u) : "memory"); SB();
  asm volatile("v_cndmask_b32 %0, %1, %2, %3" : "=v"(xt[0]) : "v"(gr[0]), "v"(gr[1]), "s"(0xAAAAAAAAAAAAAAAAull) : ); SB();
  asm volatile("v_mfma_f32_16x16x32_bf16 %0, v[172:175], a[108:111], %0" : "+v"(a2[1][0]) :  : ); SB();
  asm volatile("buffer_load_dwordx4 %0, %1, %2 offen offset:1024 lds" :  : "v"(voV[1]), "s"(rV), "s"(0u) : "memory"); SB();
  asm volatile("v_cndmask_b32 %0, %1, %2, %3" : "=v"(xt[1]) : "v"(gr[2]), "v"(gr[3]), "s"(0xAAAAAAAAAAAAAAAAull) : ); SB();
  asm volatile("v_mfma_f32_16x16x32_bf16 %0, v[172:175], %1, %0" : "+v"(c2[0]) : "v"(ones) : ); SB();
  asm volatile("s_mov_b32 exec_lo, -1\n\ts_mov_b32 exec_hi, 0\n\tbuffer_load_dwordx4 %0, %1, %2 offen offset:2048 lds\n\ts_mov_b64 exec, -1" :  : "v"(voV[2]), "s"(rV), "s"(0u) : "memory"); SB();
  asm volatile("v_mul_f32 %0, 0x3acccccd, %1" : "=v"(xs[0][0]) : "v"(rs[0]) : ); SB();
  asm volatile("v_mfma_f32_16x16x32_bf16 %0, v[176:179], a[104:107], %0" : "+v"(a2[0][1]) :  : ); SB();
  asm volatile("ds_read_b64_tr_b16 v[160:161], %0 offset:43008" :  : "v"(aW[1][0]) : "memory"); SB();
  asm volatile("v_cndmask_b32 %0, %0, %1, %2" : "+v"(xt[0]) : "v"(xt[1]), "s"(0xCCCCCCCCCCCCCCCCull) : ); SB();
  asm volatile("v_mfma_f32_16x16x32_bf16 %0, v[176:179], a[108:111], %0" : "+v"(a2[1][1]) :  : ); SB();
  asm volatile("ds_read_b64_tr_b16 v[162:163], %0 offset:43776" :  : "v"(aW[1][0]) : "memory"); SB();
  asm volatile("v_mul_f32 %0, 0x3a83126f, %1" : "=v"(xt[1]) : "v"(rs[0]) : ); SB();
  asm volatile("v_mfma_f32_16x16x32_bf16 %0, v[176:179], %1, %0" : "+v"(c2[1]) : "v"(ones) : ); SB();
  asm volatile("ds_read_b64_tr_b16 v[164:165], %0 offset:43008" :  : "v"(aW[1][1]) : "memory"); SB();
  asm volatile("v_fma_f32 %0, -%1, %2, %3" : "=v"(xs[0][1]) : "v"(rs[0]), "v"(xs[0][0]), "v"(xt[0]) : ); SB();
  asm volatile("v_mfma_f32_16x16x32_bf16 %0, v[180:183], a[104:107], %0" : "+v"(a2[0][2]) :  : ); SB();
  asm volatile("ds_read_b64_tr_b16 v[166:167], %0 offset:43776" :  : "v"(aW[1][1]) : "memory"); SB();
  asm volatile("v_mul_f32 %0, %0, %1" : "+v"(xt[1]) : "v"(xs[0][0]) : ); SB();
  asm volatile("v_mfma_f32_16x16x32_bf16 %0, v[180:183], a[108:111], %0" : "+v"(a2[1][2]) :  : ); SB();
  asm volatile("ds_read_b64_tr_b16 v[168:169], %0 offset:43008" :  : "v"(aW[1][2]) : "memory"); SB();
  asm volatile("v_mfma_f32_16x16x32_bf16 %0, v[180:183], %1, %0" : "+v"(c2[2]) : "v"(ones) : ); SB();
  asm volatile("ds_read_b64_tr_b16 v[170:171], %0 offset:43776" :  : "v"(aW[1][2]) : "memory"); SB();
  asm volatile("s_waitcnt lgkmcnt(6)\n\tv_mfma_f32_16x16x32_bf16 %0, v[184:187], a[112:115], %0" : "+v"(a2[0][0]) :  : ); SB();
  if (__builtin_expect((__builtin_amdgcn_ballot_w64(!(xs[0][1] >= xt[1])) & 0xF0000F0000F0000Full) != 0, 0)) {
    const f32x2 mm = {xs[0][0], xs[0][0]};
    f32x2 q2 = {0.f, 0.f};
    const u32x4* xp = (const u32x4*)(x + (size_t)(rb * 64 + w * 16 + l15) * 1280 + g4 * 320);
#pragma unroll 1
    for (int c = 0; c < 20; ++c) {
      u32x4 xq;
      asm volatile("global_load_dwordx4 %0, %1, off\n\ts_waitcnt vmcnt(0)" : "=&v"(xq) : "v"(xp + c) : "memory");
#pragma unroll
      for (int e = 0; e < 4; ++e) { const f32x2 d = unpk(xq[e]) - mm; q2 = __builtin_elementwise_fma(d, d, q2); }
    }
    xs[0][1] = red32(red16(q2[0] + q2[1]));
  }
  SB();
  asm volatile("v_mfma_f32_16x16x32_bf16 %0, v[184:187], a[116:119], %0" : "+v"(a2[1][0]) :  : ); SB();
  asm volatile("buffer_load_dwordx4 %0, %1, %2 offen offset:3072 lds" :  : "v"(voO), "s"(rO), "s"(0u) : "memory"); SB();
  asm volatile("v_max_f32 %0, 0, %0" : "+v"(xs[0][1]) :  : ); SB();
  asm volatile("v_mfma_f32_16x16x32_bf16 %0, v[184:187], %1, %0" : "+v"(c2[0]) : "v"(ones) : ); SB();
  asm volatile("buffer_load_dwordx4 v[228:231], %0, %1, %2 offen" :  : "v"(vF), "s"(rF), "s"(0u) : "memory"); SB();
  asm volatile("v_mul_f32 %0, 0x3acccccd, %0" : "+v"(xs[0][1]) :  : ); SB();
  asm volatile("v_mfma_f32_16x16x32_bf16 %0, v[188:191], a[112:115], %0" : "+v"(a2[0][1]) :  : ); SB();
  asm volatile("s_waitcnt vmcnt(13)\n\ts_barrier" :  :  : "memory"); SB();
  asm volatile("ds_read_b64_tr_b16 v[172:173], %0 offset:0" :  : "v"(aW[0][0]) : "memory"); SB();
  asm volatile("v_cvt_pk_bf16_f32 %0, %0, %0" : "+v"(xs[0][1]) :  : ); SB();
  asm volatile("v_mfma_f32_16x16x32_bf16 %0, v[188:191], a[116:119], %0" : "+v"(a2[1][1]) :  : ); SB();
  asm volatile("ds_read_b64_tr_b16 v[174:175], %0 offset:768" :  : "v"(aW[0][0]) : "memory"); SB();
  asm volatile("v_lshlrev_b32 %0, 16, %0" : "+v"(xs[0][1]) :  : ); SB();
  asm volatile("v_mfma_f32_16x16x32_bf16 %0, v[188:191], %1, %0" : "+v"(c2[1]) : "v"(ones) : ); SB();
  asm volatile("ds_read_b64_tr_b16 v[176:177], %0 offset:0" :  : "v"(aW[0][1]) : "memory"); SB();
  asm volatile("v_add_f32 %0, 0x3727c5ac, %0" : "+v"(xs[0][1]) :  : ); SB();
  asm volatile("v_mfma_f32_16x16x32_bf16 %0, v[192:195], a[112:115], %0" : "+v"(a2[0][2]) :  : ); SB();
  asm volatile("ds_read_b64_tr_b16 v[178:179], %0 offset:768" :  : "v"(aW[0][1]) : "memory"); SB();
  asm volatile("v_cvt_pk_bf16_f32 %0, %0, %0" : "+v"(xs[0][1]) :  : ); SB();
  asm volatile("v_mfma_f32_16x16x32_bf16 %0, v[192:195], a[116:119], %0" : "+v"(a2[1][2]) :  : ); SB();
  asm volatile("ds_read_b64_tr_b16 v[180:181], %0 offset:0" :  : "v"(aW[0][2]) : "memory"); SB();
  asm volatile("v_lshlrev_b32 %0, 16, %0" : "+v"(xs[0][1]) :  : ); SB();
  asm volatile("v_mfma_f32_16x16x32_bf16 %0, v[192:195], %1, %0" : "+v"(c2[2]) : "v"(ones) : ); SB();
  asm volatile("ds_read_b64_tr_b16 v[182:183], %0 offset:768" :  : "v"(aW[0][2]) : "memory"); SB();
  asm volatile("s_waitcnt lgkmcnt(6)\n\tv_mfma_f32_16x16x32_bf16 %0, v[160:163], a[120:123], %0" : "+v"(a2[0][0]) :  : ); SB();
  asm volatile("v_sqrt_f32 %0, %0" : "+v"(xs[0][1]) :  : ); SB();
  asm volatile("v_mfma_f32_16x16x32_bf16 %0, v[160:163], a[124:127], %0" : "+v"(a2[1][0]) :  : ); SB();
  asm volatile("v_cvt_pk_bf16_f32 %0, %0, %0" : "+v"(xs[0][0]) :  : ); SB();
  asm volatile("v_mfma_f32_16x16x32_bf16 %0, v[160:163], %1, %0" : "+v"(c2[0]) : "v"(ones) : ); SB();
  asm volatile("v_lshlrev_b32 %0, 16, %0" : "+v"(xs[0][0]) :  : ); SB();
  asm volatile("v_mfma_f32_16x16x32_bf16 %0, v[164:167], a[120:123], %0" : "+v"(a2[0][1]) :  : ); SB();
  asm volatile("ds_read_b64_tr_b16 v[184:185], %0 offset:6144" :  : "v"(aW[0][0]) : "memory"); SB();
  asm volatile("v_cvt_pk_bf16_f32 %0, %0, %0" : "+v"(xs[0][1]) :  : ); SB();
  asm volatile("v_mfma_f32_16x16x32_bf16 %0, v[164:167], a[124:127], %0" : "+v"(a2[1][1]) :  : ); SB();
  asm volatile("ds_read_b64_tr_b16 v[186:187], %0 offset:6912" :  : "v"(aW[0][0]) : "memory"); SB();
  asm volatile("v_lshlrev_b32 %0, 16, %0" : "+v"(xs[0][1]) :  : ); SB();
  asm volatile("v_mfma_f32_16x16x32_bf16 %0, v[164:167], %1, %0" : "+v"(c2[1]) : "v"(ones) : ); SB();
  asm volatile("ds_read_b64_tr_b16 v[188:189], %0 offset:6144" :  : "v"(aW[0][1]) : "memory"); SB();
  asm volatile("v_mfma_f32_16x16x32_bf16 %0, v[168:171], a[120:123], %0" : "+v"(a2[0][2]) :  : ); SB();
  asm volatile("ds_read_b64_tr_b16 v[190:191], %0 offset:6912" :  : "v"(aW[0][1]) : "memory"); SB();
  asm volatile("v_mfma_f32_16x16x32_bf16 %0, v[168:171], a[124:127], %0" : "+v"(a2[1][2]) :  : ); SB();
  asm volatile("ds_read_b64_tr_b16 v[192:193], %0 offset:6144" :  : "v"(aW[0][2]) : "memory"); SB();
  asm volatile("v_mfma_f32_16x16x32_bf16 %0, v[168:171], %1, %0" : "+v"(c2[2]) : "v"(ones) : ); SB();
  asm volatile("ds_read_b64_tr_b16 v[194:195], %0 offset:6912" :  : "v"(aW[0][2]) : "memory"); SB();
  asm volatile("s_waitcnt lgkmcnt(6)\n\tv_mfma_f32_16x16x32_bf16 %0, v[172:175], a[128:131], %0" : "+v"(a2[0][0]) :  : ); SB();
  asm volatile("buffer_load_dwordx4 v[232:235], %0, %1, %2 offen" :  : "v"(vF), "s"(rF), "s"(4096u) : "memory"); SB();
  asm volatile("v_mfma_f32_16x16x32_bf16 %0, v[172:175], a[132:135], %0" : "+v"(a2[1][0]) :  : ); SB();
  asm volatile("v_rcp_f32 %0, %1" : "=v"(xt[0]) : "v"(xs[0][1]) : ); SB();
  asm volatile("v_mfma_f32_16x16x32_bf16 %0, v[172:175], %1, %0" : "+v"(c2[0]) : "v"(ones) : ); SB();
  asm volatile("buffer_load_dwordx4 v[236:239], %0, %1, %2 offen" :  : "v"(vF), "s"(rF), "s"(c2s_) : "memory"); SB();
  asm volatile("s_nop 0\n\tv_mul_f32 %0, 0xc0135761, %1" : "=v"(xs[0][2]) : "v"(xt[0]) : ); SB();
  asm volatile("v_mfma_f32_16x16x32_bf16 %0, v[176:179], a[128:131], %0" : "+v"(a2[0][1]) :  : ); SB();
  asm volatile("v_mul_f32 %0, %1, %1" : "=v"(xt[1]) : "v"(xt[0]) : ); SB();
  asm volatile("v_mul_f32 %0, 0xbdd2d3e8, %0" : "+v"(xt[0]) :  : ); SB();
  asm volatile("v_mul_f32 %0, %1, %2" : "=v"(xs[0][3]) : "v"(xt[0]), "v"(xt[1]) : ); SB();
  asm volatile("s_mov_b32 exec_lo, 0xf0000f\n\ts_mov_b32 exec_hi, 0xf0000f00\n\tds_write_b128 %0, %1\n\ts_mov_b64 exec, -1" :  : "v"(xw), "v"(xs[0]) : "memory"); SB();
  asm volatile("s_waitcnt vmcnt(12) lgkmcnt(0)\n\ts_barrier" :  :  : "memory"); SB();
  asm volatile("ds_read_b64_tr_b16 v[160:161], %0 offset:12288" :  : "v"(aW[0][0]) : "memory"); SB();
  asm volatile("v_mfma_f32_16x16x32_bf16 %0, v[176:179], a[132:135], %0" : "+v"(a2[1][1]) :  : ); SB();
  asm volatile("ds_read_b64_tr_b16 v[162:163], %0 offset:13056" :  : "v"(aW[0][0]) : "memory"); SB();
  asm volatile("s_add_u32 m0, %0, 24576" :  : "s"(mW) : "memory", "scc"); SB();
  asm volatile("v_mfma_f32_16x16x32_bf16 %0, v[176:179], %1, %0" : "+v"(c2[1]) : "v"(ones) : ); SB();
  asm volatile("ds_read_b64_tr_b16 v[164:165], %0 offset:12288" :  : "v"(aW[0][1]) : "memory"); SB();
  asm volatile("v_mfma_f32_16x16x32_bf16 %0, v[180:183], a[128:131], %0" : "+v"(a2[0][2]) :  : ); SB();
  asm volatile("ds_read_b64_tr_b16 v[166:167], %0 offset:13056" :  : "v"(aW[0][1]) : "memory"); SB();
  asm volatile("v_mfma_f32_16x16x32_bf16 %0, v[180:183], a[132:135], %0" : "+v"(a2[1][2]) :  : ); SB();
  asm volatile("ds_read_b64_tr_b16 v[168:169], %0 offset:12288" :  : "v"(aW[0][2]) : "memory"); SB();
  asm volatile("v_mfma_f32_16x16x32_bf16 %0, v[180:183], %1, %0" : "+v"(c2[2]) : "v"(ones) : ); SB();
  asm volatile("ds_read_b64_tr_b16 v[170:171], %0 offset:13056" :  : "v"(aW[0][2]) : "memory"); SB();
  asm volatile("v_mfma_f32_16x16x32_bf16 %0, v[184:187], a[136:139], %0" : "+v"(a2[0][0]) :  : ); SB();
  asm volatile("ds_read_b128 %0, %1 offset:0" : "=&v"(xs[0]) : "v"(xw) : "memory"); SB();
  asm volatile("v_mfma_f32_16x16x32_bf16 %0, v[184:187], a[140:143], %0" : "+v"(a2[1][0]) :  : ); SB();
  asm volatile("ds_read_b128 %0, %1 offset:0" : "=&v"(xs[1]) : "v"(xr_) : "memory"); SB();
  asm volatile("v_mfma_f32_16x16x32_bf16 %0, v[184:187], %1, %0" : "+v"(c2[0]) : "v"(ones) : ); SB();
  asm volatile("v_readfirstlane_b32 %0, v252\n\tv_readfirstlane_b32 %1, v253\n\ts_nop 0\n\ts_add_u32 %0, %0, 1\n\ts_addc_u32 %1, %1, 0" : "=&s"(tg0), "=&s"(tg1) :  : "memory", "scc"); SB();
  asm volatile("v_mfma_f32_16x16x32_bf16 %0, v[188:191], a[136:139], %0" : "+v"(a2[0][1]) :  : ); SB();
  asm volatile("ds_read_b64_tr_b16 v[172:173], %0 offset:18432" :  : "v"(aW[0][0]) : "memory"); SB();
  asm volatile("v_mfma_f32_16x16x32_bf16 %0, v[188:191], a[140:143], %0" : "+v"(a2[1][1]) :  : ); SB();
  asm volatile("ds_read_b64_tr_b16 v[174:175], %0 offset:19200" :  : "v"(aW[0][0]) : "memory"); SB();
  asm volatile("v_mfma_f32_16x16x32_bf16 %0, v[188:191], %1, %0" : "+v"(c2[1]) : "v"(ones) : ); SB();
  asm volatile("ds_read_b64_tr_b16 v[176:177], %0 offset:18432" :  : "v"(aW[0][1]) : "memory"); SB();
  asm volatile("v_mfma_f32_16x16x32_bf16 %0, v[192:195], a[136:139], %0" : "+v"(a2[0][2]) :  : ); SB();
  asm volatile("ds_read_b64_tr_b16 v[178:179], %0 offset:19200" :  : "v"(aW[0][1]) : "memory"); SB();
  asm volatile("v_mfma_f32_16x16x32_bf16 %0, v[192:195], a[140:143], %0" : "+v"(a2[1][2]) :  : ); SB();
  asm volatile("ds_read_b64_tr_b16 v[180:181], %0 offset:18432" :  : "v"(aW[0][2]) : "memory"); SB();
  asm volatile("v_mfma_f32_16x16x32_bf16 %0, v[192:195], %1, %0" : "+v"(c2[2]) : "v"(ones) : ); SB();
  asm volatile("ds_read_b64_tr_b16 v[182:183], %0 offset:19200" :  : "v"(aW[0][2]) : "memory"); SB();
  asm volatile("s_waitcnt lgkmcnt(8)\n\tv_mfma_f32_16x16x32_bf16 %0, v[160:163], a[144:147], %0" : "+v"(a2[0][0]) :  : ); SB();
  { unsigned lane = lane0, w = w0, rb = rb0, cb = cb0;
  asm volatile("" : "+v"(lane)); asm volatile("" : "+s"(w), "+s"(rb), "+s"(cb)); SB();

  asm volatile("v_mfma_f32_16x16x32_bf16 %0, v[160:163], a[148:151], %0" : "+v"(a2[1][0]) :  : ); SB();
  w3k[0] = w3s + 32768; asm volatile("" : "+s"(w3k[0])); SB();
  asm volatile("v_mfma_f32_16x16x32_bf16 %0, v[160:163], %1, %0" : "+v"(c2[0]) : "v"(ones) : ); SB();
  asm volatile("buffer_load_dwordx4 %0, %1, %2 offen lds" :  : "v"(voB[0]), "s"(rW1), "s"(kw_[0]) : "memory"); SB();
  w3k[1] = w3s + 65536; asm volatile("" : "+s"(w3k[1])); SB();
  asm volatile("v_mfma_f32_16x16x32_bf16 %0, v[164:167], a[144:147], %0" : "+v"(a2[0][1]) :  : ); SB();
  sFZ = W_FZ + ((cb * 32 + rb) * 4 + w) * 8; asm volatile("" : "+s"(sFZ)); SB();
  asm volatile("v_mfma_f32_16x16x32_bf16 %0, v[164:167], a[148:151], %0" : "+v"(a2[1][1]) :  : ); SB();
  sFK = sFZ + 8192; asm volatile("" : "+s"(sFK)); SB();
  mU = lb + w * 1024; asm volatile("" : "+s"(mU)); SB();
  asm volatile("v_mfma_f32_16x16x32_bf16 %0, v[164:167], %1, %0" : "+v"(c2[1]) : "v"(ones) : ); SB();
  uso = W_KV + c2s; asm volatile("" : "+s"(uso)); SB();
  asm volatile("v_mfma_f32_16x16x32_bf16 %0, v[168:171], a[144:147], %0" : "+v"(a2[0][2]) :  : ); SB();
  asm volatile("buffer_load_dwordx4 %0, %1, %2 offen offset:1024 lds" :  : "v"(voB[1]), "s"(rW1), "s"(kw_[0]) : "memory"); SB();
  asm volatile("v_mfma_f32_16x16x32_bf16 %0, v[168:171], a[148:151], %0" : "+v"(a2[1][2]) :  : ); SB();
  asm volatile("buffer_load_dwordx4 %0, %1, %2 offen offset:2048 lds" :  : "v"(voB[2]), "s"(rW1), "s"(kw_[0]) : "memory"); SB();
  asm volatile("s_add_u32 m0, %0, 36864" :  : "s"(mW) : "memory", "scc"); SB();
  asm volatile("v_mfma_f32_16x16x32_bf16 %0, v[168:171], %1, %0" : "+v"(c2[2]) : "v"(ones) : ); SB();
  asm volatile("buffer_load_dwordx4 %0, %1, %2 offen lds" :  : "v"(voB[0]), "s"(rW1), "s"(kw_[1]) : "memory"); SB();
  asm volatile("s_waitcnt lgkmcnt(0)\n\tv_mfma_f32_16x16x32_bf16 %0, v[172:175], a[152:155], %0" : "+v"(a2[0][0]) :  : ); SB();
  asm volatile("buffer_load_dwordx4 %0, %1, %2 offen offset:1024 lds" :  : "v"(voB[1]), "s"(rW1), "s"(kw_[1]) : "memory"); SB();
  asm volatile("v_mfma_f32_16x16x32_bf16 %0, v[172:175], a[156:159], %0" : "+v"(a2[1][0]) :  : ); SB();
  asm volatile("v_mfma_f32_16x16x32_bf16 %0, v[172:175], %1, %0" : "+v"(c2[0]) : "v"(ones) : ); SB();
  asm volatile("v_mfma_f32_16x16x32_bf16 %0, v[176:179], a[152:155], %0" : "+v"(a2[0][1]) :  : ); SB();
  asm volatile("v_mfma_f32_16x16x32_bf16 %0, v[176:179], a[156:159], %0" : "+v"(a2[1][1]) :  : ); SB();
  asm volatile("v_mfma_f32_16x16x32_bf16 %0, v[176:179], %1, %0" : "+v"(c2[1]) : "v"(ones) : ); SB();
  asm volatile("v_mfma_f32_16x16x32_bf16 %0, v[180:183], a[152:155], %0" : "+v"(a2[0][2]) :  : ); SB();
  asm volatile("v_mfma_f32_16x16x32_bf16 %0, v[180:183], a[156:159], %0" : "+v"(a2[1][2]) :  : ); SB();
  asm volatile("v_mfma_f32_16x16x32_bf16 %0, v[180:183], %1, %0" : "+v"(c2[2]) : "v"(ones) : ); SB();
  ST(3);
  ST(5);
  const unsigned long long tg = ((unsigned long long)tg1 << 32) | tg0;
  asm volatile("" : "+v"(xs[0]), "+v"(xs[1]) :  : "memory"); SB();
  asm volatile("s_nop 3" : "+v"(a2[0][0]), "+v"(a2[0][1]), "+v"(a2[0][2]), "+v"(a2[1][0]), "+v"(a2[1][1]), "+v"(a2[1][2]), "+v"(c2[0]), "+v"(c2[1]), "+v"(c2[2]) :  : ); SB();
  for (int i = 0; i < 2; ++i) { xm[i] = xs[i][0]; xd[i] = xs[i][1]; k1[i] = xs[i][2]; k2[i] = xs[i][3]; }
  auto act = [&](const f32x4& a, const f32x4& cs, int i, int h) {
    const f32x2 av = {a[2 * h], a[2 * h + 1]}, cv = {cs[2 * h], cs[2 * h + 1]};
    return gelu2(__builtin_elementwise_fma((f32x2){-xm[i], -xm[i]}, cv, av), k1[i], k2[i], xd[i]);
  };
  f32x2 gv[2][3][2];
  float sv[2][2], pv[2];
  gv[0][0][0] = act(a2[0][0], c2[0], 0, 0); gv[0][0][1] = act(a2[0][0], c2[0], 0, 1);
  asm volatile("" : "+v"(gv[0][0][0]), "+v"(gv[0][0][1]));
  SB();
  asm volatile("buffer_load_dwordx4 %0, %1, %2 offen offset:2048 lds" :  : "v"(voB[2]), "s"(rW1), "s"(kw_[1]) : "memory"); SB();
  asm volatile("s_add_u32 m0, %0, 49152" :  : "s"(mW) : "memory", "scc"); SB();
  asm volatile("s_nop 0\n\tbuffer_load_dwordx4 %0, %1, %2 offen lds" :  : "v"(voB[0]), "s"(rW1), "s"(kw_[2]) : "memory"); SB();
  asm volatile("buffer_load_dwordx4 %0, %1, %2 offen offset:1024 lds" :  : "v"(voB[1]), "s"(rW1), "s"(kw_[2]) : "memory"); SB();
  gv[0][1][0] = act(a2[0][1], c2[1], 0, 0); gv[0][1][1] = act(a2[0][1], c2[1], 0, 1);
  asm volatile("" : "+v"(gv[0][1][0]), "+v"(gv[0][1][1]));
  SB();
  asm volatile("buffer_load_dwordx4 %0, %1, %2 offen offset:2048 lds" :  : "v"(voB[2]), "s"(rW1), "s"(kw_[2]) : "memory"); SB();
  asm volatile("s_add_u32 m0, %0, 61440" :  : "s"(mW) : "memory", "scc"); SB();
  asm volatile("s_nop 0\n\tbuffer_load_dwordx4 %0, %1, %2 offen lds" :  : "v"(voB[0]), "s"(rW1), "s"(kw_[3]) : "memory"); SB();
  asm volatile("buffer_load_dwordx4 %0, %1, %2 offen offset:1024 lds" :  : "v"(voB[1]), "s"(rW1), "s"(kw_[3]) : "memory"); SB();
  gv[0][2][0] = act(a2[0][2], c2[2], 0, 0); gv[0][2][1] = act(a2[0][2], c2[2], 0, 1);
  asm volatile("" : "+v"(gv[0][2][0]), "+v"(gv[0][2][1]));
  SB();
  asm volatile("buffer_load_dwordx4 %0, %1, %2 offen offset:2048 lds" :  : "v"(voB[2]), "s"(rW1), "s"(kw_[3]) : "memory"); SB();
  asm volatile("s_add_u32 m0, %0, 73728" :  : "s"(mW) : "memory", "scc"); SB();
  asm volatile("s_nop 0\n\tbuffer_load_dwordx4 %0, %1, %2 offen lds" :  : "v"(voB[0]), "s"(rW1), "s"(kw_[4]) : "memory"); SB();
  asm volatile("buffer_load_dwordx4 %0, %1, %2 offen offset:1024 lds" :  : "v"(voB[1]), "s"(rW1), "s"(kw_[4]) : "memory"); SB();
  gv[1][0][0] = act(a2[1][0], c2[0], 1, 0); gv[1][0][1] = act(a2[1][0], c2[0], 1, 1);
  asm volatile("" : "+v"(gv[1][0][0]), "+v"(gv[1][0][1]));
  SB();
  asm volatile("buffer_load_dwordx4 %0, %1, %2 offen offset:2048 lds" :  : "v"(voB[2]), "s"(rW1), "s"(kw_[4]) : "memory"); SB();
  gv[1][1][0] = act(a2[1][1], c2[1], 1, 0); gv[1][1][1] = act(a2[1][1], c2[1], 1, 1);
  asm volatile("" : "+v"(gv[1][1][0]), "+v"(gv[1][1][1]));
  SB();
  gv[1][2][0] = act(a2[1][2], c2[2], 1, 0); gv[1][2][1] = act(a2[1][2], c2[2], 1, 1);
  asm volatile("" : "+v"(gv[1][2][0]), "+v"(gv[1][2][1]));
  SB();
  asm volatile("s_add_u32 m0, %0, 86016" :  : "s"(mW) : "memory", "scc"); SB();
  asm volatile("s_nop 0\n\tbuffer_load_dwordx4 %0, %1, %2 offen lds" :  : "v"(voB[0]), "s"(rW1), "s"(kw_[5]) : "memory"); SB();
#pragma unroll
  for (int i = 0; i < 2; ++i) {
    const unsigned p0 = __float_as_uint(gv[i][0][0][0]);
    const auto pa = __builtin_amdgcn_permlane16_swap(p0, p0, false, false);
    const auto pb = __builtin_amdgcn_permlane32_swap(pa[0], pa[0], false, false);
    pv[i] = __uint_as_float(pb[0]);
    const f32x2 pp = {pv[i], pv[i]};
    f32x2 s1 = gv[i][0][0] - pp, s2 = s1 * s1;
#pragma unroll
    for (int n = 1; n < 6; ++n) { const f32x2 d = gv[i][n >> 1][n & 1] - pp; s1 += d; s2 = __builtin_elementwise_fma(d, d, s2); }
    sv[i][0] = s1[0] + s1[1]; sv[i][1] = s2[0] + s2[1];
  }
  {
    const float t1 = red16(sel32(sv[0][0], sv[1][0])), t2 = red16(sel32(sv[0][1], sv[1][1]));
    const float dl = t1 * (1.0f / 48.0f);
    const u32x4 so = {__float_as_uint((lane < 32 ? pv[0] : pv[1]) + dl), __float_as_uint(fmaxf(t2 - t1 * dl, 0.f)), tg0, tg1};
    SB();
  asm volatile("s_mov_b32 exec_lo, 0xffff\n\ts_mov_b32 exec_hi, 0xffff\n\tbuffer_store_dwordx4 %1, %0, %2, %3 offen offset:0 sc1\n\ts_mov_b64 exec, -1\n\ts_nop 0" :  : "v"(vo), "v"(so), "s"(rWS), "s"(0u) : "memory"); SB();
  }
  ST(6);
  unsigned zp[2][3][2];
#pragma unroll
  for (int n = 0; n < 12; ++n) { zp[n / 6][(n >> 1) % 3][n & 1] = pk2(gv[n / 6][(n >> 1) % 3][n & 1]); asm volatile("" : "+v"(zp[n / 6][(n >> 1) % 3][n & 1])); }
  SB();
  asm volatile("buffer_load_dwordx4 %0, %1, %2 offen offset:1024 lds" :  : "v"(voB[1]), "s"(rW1), "s"(kw_[5]) : "memory"); SB();
  asm volatile("buffer_load_dwordx4 %0, %1, %2 offen offset:2048 lds" :  : "v"(voB[2]), "s"(rW1), "s"(kw_[5]) : "memory"); SB();
  asm volatile("s_add_u32 m0, %0, 0" :  : "s"(mW) : "memory", "scc"); SB();
  asm volatile("s_nop 0\n\tbuffer_load_dwordx4 %0, %1, %2 offen lds" :  : "v"(voB[0]), "s"(rW1), "s"(kw_[6]) : "memory"); SB();
  asm volatile("s_waitcnt vmcnt(23)\n\ts_barrier" :  :  : "memory"); SB();
  ST(2);
  asm volatile("ds_read_b64_tr_b16 %0, %1 offset:0" : "=&v"(wt[0]) : "v"(oa) : "memory"); SB();
  asm volatile("ds_read_b64_tr_b16 %0, %1 offset:512" : "=&v"(wt[1]) : "v"(oa) : "memory"); SB();
  asm volatile("ds_read_b64_tr_b16 %0, %1 offset:0" : "=&v"(kp[0]) : "v"(ka) : "memory"); SB();
  asm volatile("ds_read_b64_tr_b16 %0, %1 offset:640" : "=&v"(kc[0]) : "v"(ka) : "memory"); SB();
  asm volatile("ds_read_b64_tr_b16 %0, %1 offset:4096" : "=&v"(vv[0]) : "v"(ka) : "memory"); SB();
  asm volatile("ds_read_b64 %0, %1 offset:0" : "=&v"(sw[0]) : "v"(sa) : "memory"); SB();
  asm volatile("ds_read_b64_tr_b16 %0, %1 offset:32" : "=&v"(kp[1]) : "v"(ka) : "memory"); SB();
  asm volatile("ds_read_b64_tr_b16 %0, %1 offset:672" : "=&v"(kc[1]) : "v"(ka) : "memory"); SB();
  asm volatile("ds_read_b64_tr_b16 %0, %1 offset:4128" : "=&v"(vv[1]) : "v"(ka) : "memory"); SB();
  asm volatile("ds_read_b64 %0, %1 offset:128" : "=&v"(sw[1]) : "v"(sa) : "memory"); SB();
  asm volatile("ds_read_b64_tr_b16 %0, %1 offset:64" : "=&v"(kp[2]) : "v"(ka) : "memory"); SB();
  asm volatile("ds_read_b64_tr_b16 %0, %1 offset:704" : "=&v"(kc[2]) : "v"(ka) : "memory"); SB();
  asm volatile("ds_read_b64_tr_b16 %0, %1 offset:4160" : "=&v"(vv[2]) : "v"(ka) : "memory"); SB();
  asm volatile("ds_read_b64 %0, %1 offset:256" : "=&v"(sw[2]) : "v"(sa) : "memory"); SB();
  asm volatile("ds_read_b64_tr_b16 %0, %1 offset:96" : "=&v"(kp[3]) : "v"(ka) : "memory"); SB();
  asm volatile("ds_read_b64_tr_b16 %0, %1 offset:736" : "=&v"(kc[3]) : "v"(ka) : "memory"); SB();
  asm volatile("ds_read_b64_tr_b16 %0, %1 offset:4192" : "=&v"(vv[3]) : "v"(ka) : "memory"); SB();
  asm volatile("ds_read_b64 %0, %1 offset:384" : "=&v"(sw[3]) : "v"(sa) : "memory"); SB();
  asm volatile("ds_read_b64_tr_b16 %0, %1 offset:128" : "=&v"(kp[4]) : "v"(ka) : "memory"); SB();
  asm volatile("ds_read_b64_tr_b16 %0, %1 offset:768" : "=&v"(kc[4]) : "v"(ka) : "memory"); SB();
  asm volatile("ds_read_b64_tr_b16 %0, %1 offset:4224" : "=&v"(vv[4]) : "v"(ka) : "memory"); SB();
  asm volatile("ds_read_b64 %0, %1 offset:512" : "=&v"(sw[4]) : "v"(sa) : "memory"); SB();
  asm volatile("ds_write_b64 %0, %1 offset:0" :  : "v"(zwa[0]), "v"(((u32x2){zp[0][0][0], zp[0][0][1]})) : "memory"); SB();
  asm volatile("ds_write_b64 %0, %1 offset:32" :  : "v"(zwa[0]), "v"(((u32x2){zp[0][1][0], zp[0][1][1]})) : "memory"); SB();
  asm volatile("ds_write_b64 %0, %1 offset:64" :  : "v"(zwa[0]), "v"(((u32x2){zp[0][2][0], zp[0][2][1]})) : "memory"); SB();
  asm volatile("ds_write_b64 %0, %1 offset:0" :  : "v"(zwa[1]), "v"(((u32x2){zp[1][0][0], zp[1][0][1]})) : "memory"); SB();
  asm volatile("ds_write_b64 %0, %1 offset:32" :  : "v"(zwa[1]), "v"(((u32x2){zp[1][1][0], zp[1][1][1]})) : "memory"); SB();
  asm volatile("ds_write_b64 %0, %1 offset:64" :  : "v"(zwa[1]), "v"(((u32x2){zp[1][2][0], zp[1][2][1]})) : "memory"); SB();
  asm volatile("ds_read_b64_tr_b16 %0, %1 offset:0" : "=&v"(zt[0][0]) : "v"(zra) : "memory"); SB();
  asm volatile("ds_read_b64_tr_b16 %0, %1 offset:416" : "=&v"(zt[0][1]) : "v"(zra) : "memory"); SB();
  asm volatile("ds_read_b64_tr_b16 %0, %1 offset:32" : "=&v"(zt[1][0]) : "v"(zra) : "memory"); SB();
  asm volatile("ds_read_b64_tr_b16 %0, %1 offset:448" : "=&v"(zt[1][1]) : "v"(zra) : "memory"); SB();
  asm volatile("ds_read_b64_tr_b16 %0, %1 offset:64" : "=&v"(zt[2][0]) : "v"(zra) : "memory"); SB();
  asm volatile("ds_read_b64_tr_b16 %0, %1 offset:480" : "=&v"(zt[2][1]) : "v"(zra) : "memory"); SB();
  asm volatile("buffer_load_dwordx4 %0, %1, %2 offen offset:1024 lds" :  : "v"(voB[1]), "s"(rW1), "s"(kw_[6]) : "memory"); SB();
  asm volatile("buffer_load_dwordx4 %0, %1, %2 offen offset:2048 lds" :  : "v"(voB[2]), "s"(rW1), "s"(kw_[6]) : "memory"); SB();
  asm volatile("s_add_u32 m0, %0, 131072" :  : "s"(mA) : "memory", "scc"); SB();
  asm volatile("s_nop 0\n\tbuffer_load_dwordx4 %0, %1, %2 offen lds" :  : "v"(voS), "s"(rA), "s"(ao[0]) : "memory"); SB();
  asm volatile("s_waitcnt lgkmcnt(11)" : "+v"(kp[0]), "+v"(kc[0]), "+v"(vv[0]), "+v"(sw[0]), "+v"(wt[0]), "+v"(wt[1]) :  : "memory"); SB();
  {
    const unsigned w10 = shr16(sw[0][0], sw[0][0]), w32 = shr16(sw[0][1], sw[0][1]);
    const unsigned s0 = shr16(kp[0][1], kp[0][0]), s1 = shr16(kc[0][0], kp[0][1]), s2 = shr16(kc[0][1], kc[0][0]);
    const float r0 = dot2(s1, w10, dot2(s0, w32, 0.f)), r1 = dot2(kc[0][0], w10, dot2(kp[0][1], w32, 0.f));
    const float r2 = dot2(s2, w10, dot2(s1, w32, 0.f)), r3 = dot2(kc[0][1], w10, dot2(kc[0][0], w32, 0.f));
    ko[0] = (u32x2){pk2((f32x2){r0, r1} * unpk(vv[0][0])), pk2((f32x2){r2, r3} * unpk(vv[0][1]))};
    asm volatile("" : "+v"(ko[0]));
  }
  SB();
  asm volatile("buffer_load_dwordx4 %0, %1, %2 offen offset:1024 lds" :  : "v"(voS), "s"(rA), "s"(ao[1]) : "memory"); SB();
  asm volatile("buffer_load_dwordx4 %0, %1, %2 offen offset:2048 lds" :  : "v"(voS), "s"(rA), "s"(ao[2]) : "memory"); SB();
  asm volatile("buffer_load_dwordx4 %0, %1, %2 offen offset:3072 lds" :  : "v"(voS), "s"(rA), "s"(ao[3]) : "memory"); SB();
  asm volatile("ds_write_b64 %0, %1 offset:0" :  : "v"(kw), "v"(ko[0]) : "memory"); SB();
  asm volatile("" : "+v"(kp[1]), "+v"(kc[1]), "+v"(vv[1]), "+v"(sw[1]) :  : "memory"); SB();
  {
    const unsigned w10 = shr16(sw[1][0], sw[1][0]), w32 = shr16(sw[1][1], sw[1][1]);
    const unsigned s0 = shr16(kp[1][1], kp[1][0]), s1 = shr16(kc[1][0], kp[1][1]), s2 = shr16(kc[1][1], kc[1][0]);
    const float r0 = dot2(s1, w10, dot2(s0, w32, 0.f)), r1 = dot2(kc[1][0], w10, dot2(kp[1][1], w32, 0.f));
    const float r2 = dot2(s2, w10, dot2(s1, w32, 0.f)), r3 = dot2(kc[1][1], w10, dot2(kc[1][0], w32, 0.f));
    ko[1] = (u32x2){pk2((f32x2){r0, r1} * unpk(vv[1][0])), pk2((f32x2){r2, r3} * unpk(vv[1][1]))};
    asm volatile("" : "+v"(ko[1]));
  }
  SB();
  asm volatile("s_add_u32 m0, %0, 135168" :  : "s"(mA) : "memory", "scc"); SB();
  asm volatile("s_nop 0\n\tbuffer_load_dwordx4 %0, %1, %2 offen lds" :  : "v"(voS), "s"(rA), "s"(ao[4]) : "memory"); SB();
  asm volatile("buffer_load_dwordx4 %0, %1, %2 offen offset:1024 lds" :  : "v"(voS), "s"(rA), "s"(ao[5]) : "memory"); SB();
  asm volatile("buffer_load_dwordx4 %0, %1, %2 offen offset:2048 lds" :  : "v"(voS), "s"(rA), "s"(ao[6]) : "memory"); SB();
  asm volatile("ds_write_b64 %0, %1 offset:2304" :  : "v"(kw), "v"(ko[1]) : "memory"); SB();
  asm volatile("" : "+v"(kp[2]), "+v"(kc[2]), "+v"(vv[2]), "+v"(sw[2]) :  : "memory"); SB();
  asm volatile("s_waitcnt lgkmcnt(1)" : "+v"(zt[0][0]), "+v"(zt[0][1]), "+v"(zt[1][0]), "+v"(zt[1][1]), "+v"(zt[2][0]), "+v"(zt[2][1]) :  : "memory"); SB();
  {
    const unsigned w10 = shr16(sw[2][0], sw[2][0]), w32 = shr16(sw[2][1], sw[2][1]);
    const unsigned s0 = shr16(kp[2][1], kp[2][0]), s1 = shr16(kc[2][0], kp[2][1]), s2 = shr16(kc[2][1], kc[2][0]);
    const float r0 = dot2(s1, w10, dot2(s0, w32, 0.f)), r1 = dot2(kc[2][0], w10, dot2(kp[2][1], w32, 0.f));
    const float r2 = dot2(s2, w10, dot2(s1, w32, 0.f)), r3 = dot2(kc[2][1], w10, dot2(kc[2][0], w32, 0.f));
    ko[2] = (u32x2){pk2((f32x2){r0, r1} * unpk(vv[2][0])), pk2((f32x2){r2, r3} * unpk(vv[2][1]))};
    asm volatile("" : "+v"(ko[2]));
  }
  SB();
  asm volatile("buffer_load_dwordx4 %0, %1, %2 offen offset:3072 lds" :  : "v"(voS), "s"(rA), "s"(ao[7]) : "memory"); SB();
  asm volatile("v_xor_b32 %0, 128, %0" : "+v"(voS) :  : "memory"); SB();
  asm volatile("ds_write_b64 %0, %1 offset:4608" :  : "v"(kw), "v"(ko[2]) : "memory"); SB();
  asm volatile("" : "+v"(kp[3]), "+v"(kc[3]), "+v"(vv[3]), "+v"(sw[3]) :  : "memory"); SB();
  {
    const unsigned w10 = shr16(sw[3][0], sw[3][0]), w32 = shr16(sw[3][1], sw[3][1]);
    const unsigned s0 = shr16(kp[3][1], kp[3][0]), s1 = shr16(kc[3][0], kp[3][1]), s2 = shr16(kc[3][1], kc[3][0]);
    const float r0 = dot2(s1, w10, dot2(s0, w32, 0.f)), r1 = dot2(kc[3][0], w10, dot2(kp[3][1], w32, 0.f));
    const float r2 = dot2(s2, w10, dot2(s1, w32, 0.f)), r3 = dot2(kc[3][1], w10, dot2(kc[3][0], w32, 0.f));
    ko[3] = (u32x2){pk2((f32x2){r0, r1} * unpk(vv[3][0])), pk2((f32x2){r2, r3} * unpk(vv[3][1]))};
    asm volatile("" : "+v"(ko[3]));
  }
  SB();
  asm volatile("s_add_u32 m0, %0, 98304" :  : "s"(mA) : "memory", "scc"); SB();
  asm volatile("s_nop 0\n\tbuffer_load_dwordx4 %0, %1, %2 offen lds" :  : "v"(voS), "s"(rA), "s"(ao[0]) : "memory"); SB();
  asm volatile("buffer_load_dwordx4 %0, %1, %2 offen offset:1024 lds" :  : "v"(voS), "s"(rA), "s"(ao[1]) : "memory"); SB();
  asm volatile("ds_write_b64 %0, %1 offset:6912" :  : "v"(kw), "v"(ko[3]) : "memory"); SB();
  asm volatile("" : "+v"(kp[4]), "+v"(kc[4]), "+v"(vv[4]), "+v"(sw[4]) :  : "memory"); SB();
  {
    const unsigned w10 = shr16(sw[4][0], sw[4][0]), w32 = shr16(sw[4][1], sw[4][1]);
    const unsigned s0 = shr16(kp[4][1], kp[4][0]), s1 = shr16(kc[4][0], kp[4][1]), s2 = shr16(kc[4][1], kc[4][0]);
    const float r0 = dot2(s1, w10, dot2(s0, w32, 0.f)), r1 = dot2(kc[4][0], w10, dot2(kp[4][1], w32, 0.f));
    const float r2 = dot2(s2, w10, dot2(s1, w32, 0.f)), r3 = dot2(kc[4][1], w10, dot2(kc[4][0], w32, 0.f));
    ko[4] = (u32x2){pk2((f32x2){r0, r1} * unpk(vv[4][0])), pk2((f32x2){r2, r3} * unpk(vv[4][1]))};
    asm volatile("" : "+v"(ko[4]));
  }
  SB();
  asm volatile("ds_write_b64 %0, %1 offset:9216" :  : "v"(kw), "v"(ko[4]) : "memory"); SB();
  ST(16);
  asm volatile("s_waitcnt vmcnt(26) lgkmcnt(0)\n\ts_barrier" :  :  : "memory"); SB();
  asm volatile("ds_read_b128 %0, %1 offset:0" : "=&v"(kq[0]) : "v"(kr) : "memory"); SB();
  asm volatile("ds_read_b128 %0, %1 offset:1152" : "=&v"(kq[1]) : "v"(kr) : "memory"); SB();
  asm volatile("ds_read_b128 %0, %1 offset:2304" : "=&v"(kq[2]) : "v"(kr) : "memory"); SB();
  asm volatile("ds_read_b64_tr_b16 v[184:185], %0 offset:24576" :  : "v"(aW[0][0]) : "memory"); SB();
  asm volatile("ds_read_b64_tr_b16 v[186:187], %0 offset:25344" :  : "v"(aW[0][0]) : "memory"); SB();
  asm volatile("ds_read_b64_tr_b16 v[188:189], %0 offset:24576" :  : "v"(aW[0][1]) : "memory"); SB();
  asm volatile("ds_read_b64_tr_b16 v[190:191], %0 offset:25344" :  : "v"(aW[0][1]) : "memory"); SB();
  asm volatile("ds_read_b64_tr_b16 v[192:193], %0 offset:24576" :  : "v"(aW[0][2]) : "memory"); SB();
  asm volatile("ds_read_b64_tr_b16 v[194:195], %0 offset:25344" :  : "v"(aW[0][2]) : "memory"); SB();
  ST(7);
  asm volatile("s_waitcnt lgkmcnt(6)" : "+v"(kq[0]), "+v"(kq[1]), "+v"(kq[2]) :  : "memory"); SB();
  unsigned kz[3] = {kq[0][3], kq[1][3] | (w + 1) >> 2, kq[2][3] | (w + 1) >> 2};
  asm volatile("" : "+v"(kz[0]), "+v"(kz[1]), "+v"(kz[2])); SB();
  asm volatile("buffer_load_dwordx4 v[196:199], %0, %1, %2 offen offset:0 sc1" :  : "v"(vS), "s"(rWS), "s"(0u) : "memory"); SB();
  asm volatile("buffer_load_dwordx4 v[200:203], %0, %1, %2 offen offset:1024 sc1" :  : "v"(vS), "s"(rWS), "s"(0u) : "memory"); SB();
  asm volatile("buffer_load_dwordx4 v[204:207], %0, %1, %2 offen offset:2048 sc1" :  : "v"(vS), "s"(rWS), "s"(0u) : "memory"); SB();
  asm volatile("s_waitcnt vmcnt(11)" :  :  : "memory"); SB();
  asm volatile("s_waitcnt lgkmcnt(0)\n\tv_mfma_f32_16x16x32_bf16 %0, v[184:187], a[0:3], 0" : "=&v"(a1[0][0]) :  : ); SB();
  asm volatile("ds_read_b64_tr_b16 v[160:161], %0 offset:30720" :  : "v"(aW[0][0]) : "memory"); SB();
  asm volatile("v_mfma_f32_16x16x32_bf16 %0, v[184:187], a[4:7], 0" : "=&v"(a1[1][0]) :  : ); SB();
  asm volatile("ds_read_b64_tr_b16 v[162:163], %0 offset:31488" :  : "v"(aW[0][0]) : "memory"); SB();
  asm volatile("v_lshlrev_b32 %0, 16, v245" : "=v"(dD[0]) :  : ); SB();
  asm volatile("v_mfma_f32_16x16x32_bf16 %0, v[184:187], %1, 0" : "=&v"(c1[0]) : "v"(ones) : ); SB();
  asm volatile("ds_read_b64_tr_b16 v[164:165], %0 offset:30720" :  : "v"(aW[0][1]) : "memory"); SB();
  asm volatile("v_lshlrev_b32 %0, 16, v246" : "=v"(dD[1]) :  : ); SB();
  asm volatile("v_mfma_f32_16x16x32_bf16 %0, v[188:191], a[0:3], 0" : "=&v"(a1[0][1]) :  : ); SB();
  asm volatile("ds_read_b64_tr_b16 v[166:167], %0 offset:31488" :  : "v"(aW[0][1]) : "memory"); SB();
  asm volatile("v_lshlrev_b32 %0, 16, v247" : "=v"(dD[2]) :  : ); SB();
  asm volatile("v_mfma_f32_16x16x32_bf16 %0, v[188:191], a[4:7], 0" : "=&v"(a1[1][1]) :  : ); SB();
  asm volatile("ds_read_b64_tr_b16 v[168:169], %0 offset:30720" :  : "v"(aW[0][2]) : "memory"); SB();
  asm volatile("v_lshlrev_b32 %0, 16, v248" : "=v"(bs) :  : ); SB();
  asm volatile("v_mfma_f32_16x16x32_bf16 %0, v[188:191], %1, 0" : "=&v"(c1[1]) : "v"(ones) : ); SB();
  asm volatile("ds_read_b64_tr_b16 v[170:171], %0 offset:31488" :  : "v"(aW[0][2]) : "memory"); SB();
  asm volatile("v_mov_b32_dpp v242, v231 wave_shr:1 row_mask:0xf bank_mask:0xf" :  :  : ); SB();
  asm volatile("v_mfma_f32_16x16x32_bf16 %0, v[192:195], a[0:3], 0" : "=&v"(a1[0][2]) :  : ); SB();
  asm volatile("ds_read_b64_tr_b16 v[172:173], %0 offset:36864" :  : "v"(aW[0][0]) : "memory"); SB();
  asm volatile("v_alignbit_b32 %0, v231, v231, 16" : "=v"(im[0][0]) :  : ); SB();
  asm volatile("v_mfma_f32_16x16x32_bf16 %0, v[192:195], a[4:7], 0" : "=&v"(a1[1][2]) :  : ); SB();
  asm volatile("ds_read_b64_tr_b16 v[174:175], %0 offset:37632" :  : "v"(aW[0][0]) : "memory"); SB();
  asm volatile("v_alignbit_b32 %0, v230, v230, 16" : "=v"(im[0][1]) :  : ); SB();
  asm volatile("v_mfma_f32_16x16x32_bf16 %0, v[192:195], %1, 0" : "=&v"(c1[2]) : "v"(ones) : ); SB();
  asm volatile("ds_read_b64_tr_b16 v[176:177], %0 offset:36864" :  : "v"(aW[0][1]) : "memory"); SB();
  asm volatile("s_waitcnt lgkmcnt(7)\n\tv_mfma_f32_16x16x32_bf16 %0, v[160:163], a[8:11], %0" : "+v"(a1[0][0]) :  : ); SB();
  asm volatile("ds_read_b64_tr_b16 v[178:179], %0 offset:37632" :  : "v"(aW[0][1]) : "memory"); SB();
  asm volatile("v_alignbit_b32 %0, v229, v229, 16" : "=v"(im[0][2]) :  : ); SB();
  asm volatile("v_mfma_f32_16x16x32_bf16 %0, v[160:163], a[12:15], %0" : "+v"(a1[1][0]) :  : ); SB();
  asm volatile("ds_read_b64_tr_b16 v[180:181], %0 offset:36864" :  : "v"(aW[0][2]) : "memory"); SB();
  asm volatile("buffer_load_dwordx4 v[208:211], %0, %1, %2 offen offset:3072 sc1" :  : "v"(vS), "s"(rWS), "s"(0u) : "memory"); SB();
  asm volatile("v_mfma_f32_16x16x32_bf16 %0, v[160:163], %1, %0" : "+v"(c1[0]) : "v"(ones) : ); SB();
  asm volatile("ds_read_b64_tr_b16 v[182:183], %0 offset:37632" :  : "v"(aW[0][2]) : "memory"); SB();
  asm volatile("s_waitcnt lgkmcnt(8)\n\tv_mfma_f32_16x16x32_bf16 %0, v[164:167], a[8:11], %0" : "+v"(a1[0][1]) :  : ); SB();
  asm volatile("ds_read_b64_tr_b16 v[184:185], %0 offset:43008" :  : "v"(aW[0][0]) : "memory"); SB();
  asm volatile("v_alignbit_b32 %0, v228, v228, 16" : "=v"(im[0][3]) :  : ); SB();
  asm volatile("v_mfma_f32_16x16x32_bf16 %0, v[164:167], a[12:15], %0" : "+v"(a1[1][1]) :  : ); SB();
  asm volatile("ds_read_b64_tr_b16 v[186:187], %0 offset:43776" :  : "v"(aW[0][0]) : "memory"); SB();
  asm volatile("buffer_load_dwordx4 v[212:215], %0, %1, %2 offen offset:0 sc1" :  : "v"(vS), "s"(rWS), "s"(4096u) : "memory"); SB();
  asm volatile("v_mfma_f32_16x16x32_bf16 %0, v[164:167], %1, %0" : "+v"(c1[1]) : "v"(ones) : ); SB();
  asm volatile("ds_read_b64_tr_b16 v[188:189], %0 offset:43008" :  : "v"(aW[0][1]) : "memory"); SB();
  asm volatile("s_waitcnt lgkmcnt(8)\n\tv_mfma_f32_16x16x32_bf16 %0, v[168:171], a[8:11], %0" : "+v"(a1[0][2]) :  : ); SB();
  asm volatile("ds_read_b64_tr_b16 v[190:191], %0 offset:43776" :  : "v"(aW[0][1]) : "memory"); SB();
  asm volatile("v_bfi_b32 %0, %1, v231, v230" : "=v"(im[1][0]) : "s"(0xFFFFu) : ); SB();
  asm volatile("v_mfma_f32_16x16x32_bf16 %0, v[168:171], a[12:15], %0" : "+v"(a1[1][2]) :  : ); SB();
  asm volatile("ds_read_b64_tr_b16 v[192:193], %0 offset:43008" :  : "v"(aW[0][2]) : "memory"); SB();
  asm volatile("v_bfi_b32 %0, %1, v230, v229" : "=v"(im[1][1]) : "s"(0xFFFFu) : ); SB();
  asm volatile("v_mfma_f32_16x16x32_bf16 %0, v[168:171], %1, %0" : "+v"(c1[2]) : "v"(ones) : ); SB();
  asm volatile("ds_read_b64_tr_b16 v[194:195], %0 offset:43776" :  : "v"(aW[0][2]) : "memory"); SB();
  asm volatile("buffer_load_dwordx4 v[216:219], %0, %1, %2 offen offset:1024 sc1" :  : "v"(vS), "s"(rWS), "s"(4096u) : "memory"); SB();
  asm volatile("s_waitcnt lgkmcnt(8)\n\tv_mfma_f32_16x16x32_bf16 %0, v[172:175], a[16:19], %0" : "+v"(a1[0][0]) :  : ); SB();
  asm volatile("buffer_load_dwordx4 v[220:223], %0, %1, %2 offen offset:2048 sc1" :  : "v"(vS), "s"(rWS), "s"(4096u) : "memory"); SB();
  asm volatile("v_bfi_b32 %0, %1, v229, v228" : "=v"(im[1][2]) : "s"(0xFFFFu) : ); SB();
  asm volatile("v_mfma_f32_16x16x32_bf16 %0, v[172:175], a[20:23], %0" : "+v"(a1[1][0]) :  : ); SB();
  asm volatile("buffer_load_dwordx4 v[224:227], %0, %1, %2 offen offset:3072 sc1" :  : "v"(vS), "s"(rWS), "s"(4096u) : "memory"); SB();
  asm volatile("v_bfi_b32 %0, %1, v228, v242" : "=v"(im[1][3]) : "s"(0xFFFFu) : ); SB();
  asm volatile("v_mfma_f32_16x16x32_bf16 %0, v[172:175], %1, %0" : "+v"(c1[0]) : "v"(ones) : ); SB();
  asm volatile("buffer_store_dwordx4 %1, %0, %2, %3 offen offset:0 sc1\n\ts_nop 1" :  : "v"(vKV), "v"(kq[0]), "s"(rWS), "s"(0u) : "memory"); SB();
  asm volatile("v_mov_b32_dpp v243, v235 wave_shr:1 row_mask:0xf bank_mask:0xf" :  :  : ); SB();
  asm volatile("v_mfma_f32_16x16x32_bf16 %0, v[176:179], a[16:19], %0" : "+v"(a1[0][1]) :  : ); SB();
  asm volatile("s_barrier" :  :  : "memory"); SB();
  asm volatile("ds_read_b64_tr_b16 v[160:161], %0 offset:0" :  : "v"(aW[1][0]) : "memory"); SB();
  asm volatile("v_alignbit_b32 %0, v235, v235, 16" : "=v"(im[2][0]) :  : ); SB();
  asm volatile("v_mfma_f32_16x16x32_bf16 %0, v[176:179], a[20:23], %0" : "+v"(a1[1][1]) :  : ); SB();
  asm volatile("ds_read_b64_tr_b16 v[162:163], %0 offset:768" :  : "v"(aW[1][0]) : "memory"); SB();
  asm volatile("v_alignbit_b32 %0, v234, v234, 16" : "=v"(im[2][1]) :  : ); SB();
  asm volatile("v_mfma_f32_16x16x32_bf16 %0, v[176:179], %1, %0" : "+v"(c1[1]) : "v"(ones) : ); SB();
  asm volatile("ds_read_b64_tr_b16 v[164:165], %0 offset:0" :  : "v"(aW[1][1]) : "memory"); SB();
  asm volatile("s_waitcnt lgkmcnt(3)\n\tv_mfma_f32_16x16x32_bf16 %0, v[180:183], a[16:19], %0" : "+v"(a1[0][2]) :  : ); SB();
  asm volatile("ds_read_b64_tr_b16 v[166:167], %0 offset:768" :  : "v"(aW[1][1]) : "memory"); SB();
  asm volatile("v_alignbit_b32 %0, v233, v233, 16" : "=v"(im[2][2]) :  : ); SB();
  asm volatile("v_mfma_f32_16x16x32_bf16 %0, v[180:183], a[20:23], %0" : "+v"(a1[1][2]) :  : ); SB();
  asm volatile("ds_read_b64_tr_b16 v[168:169], %0 offset:0" :  : "v"(aW[1][2]) : "memory"); SB();
  asm volatile("v_alignbit_b32 %0, v232, v232, 16" : "=v"(im[2][3]) :  : ); SB();
  asm volatile("v_mfma_f32_16x16x32_bf16 %0, v[180:183], %1, %0" : "+v"(c1[2]) : "v"(ones) : ); SB();
  asm volatile("ds_read_b64_tr_b16 v[170:171], %0 offset:768" :  : "v"(aW[1][2]) : "memory"); SB();
  asm volatile("v_bfi_b32 %0, %1, v235, v234" : "=v"(im[3][0]) : "s"(0xFFFFu) : ); SB();
  asm volatile("v_mfma_f32_16x16x32_bf16 %0, v[184:187], a[24:27], %0" : "+v"(a1[0][0]) :  : ); SB();
  asm volatile("buffer_store_dwordx4 %1, %0, %2, %3 offen offset:0 sc1\n\ts_nop 1" :  : "v"(vKV), "v"(kq[1]), "s"(rWS), "s"(w3k[0]) : "memory"); SB();
  asm volatile("v_bfi_b32 %0, %1, v234, v233" : "=v"(im[3][1]) : "s"(0xFFFFu) : ); SB();
  asm volatile("v_mfma_f32_16x16x32_bf16 %0, v[184:187], a[28:31], %0" : "+v"(a1[1][0]) :  : ); SB();
  asm volatile("buffer_store_dwordx4 %1, %0, %2, %3 offen offset:0 sc1\n\ts_nop 1" :  : "v"(vKV), "v"(kq[2]), "s"(rWS), "s"(w3k[1]) : "memory"); SB();
  asm volatile("v_bfi_b32 %0, %1, v233, v232" : "=v"(im[3][2]) : "s"(0xFFFFu) : ); SB();
  asm volatile("v_mfma_f32_16x16x32_bf16 %0, v[184:187], %1, %0" : "+v"(c1[0]) : "v"(ones) : ); SB();
  asm volatile("buffer_store_dwordx4 %1, %0, %2, %3 offen offset:0 sc1\n\ts_nop 1" :  : "v"(vWO), "v"(((u32x4){wt[0][0], wt[0][1], wt[1][0], wt[1][1]})), "s"(rWS), "s"(0u) : "memory"); SB();
  asm volatile("v_bfi_b32 %0, %1, v232, v243" : "=v"(im[3][3]) : "s"(0xFFFFu) : ); SB();
  asm volatile("v_mfma_f32_16x16x32_bf16 %0, v[188:191], a[24:27], %0" : "+v"(a1[0][1]) :  : ); SB();
  asm volatile("ds_read_b64_tr_b16 v[172:173], %0 offset:6144" :  : "v"(aW[1][0]) : "memory"); SB();
  asm volatile("v_mov_b32_dpp v244, v239 wave_shr:1 row_mask:0xf bank_mask:0xf" :  :  : ); SB();
  asm volatile("v_mfma_f32_16x16x32_bf16 %0, v[188:191], a[28:31], %0" : "+v"(a1[1][1]) :  : ); SB();
  asm volatile("ds_read_b64_tr_b16 v[174:175], %0 offset:6912" :  : "v"(aW[1][0]) : "memory"); SB();
  asm volatile("v_alignbit_b32 %0, v239, v239, 16" : "=v"(im[4][0]) :  : ); SB();
  asm volatile("v_mfma_f32_16x16x32_bf16 %0, v[188:191], %1, %0" : "+v"(c1[1]) : "v"(ones) : ); SB();
  asm volatile("ds_read_b64_tr_b16 v[176:177], %0 offset:6144" :  : "v"(aW[1][1]) : "memory"); SB();
  asm volatile("v_alignbit_b32 %0, v238, v238, 16" : "=v"(im[4][1]) :  : ); SB();
  asm volatile("v_mfma_f32_16x16x32_bf16 %0, v[192:195], a[24:27], %0" : "+v"(a1[0][2]) :  : ); SB();
  asm volatile("ds_read_b64_tr_b16 v[178:179], %0 offset:6912" :  : "v"(aW[1][1]) : "memory"); SB();
  asm volatile("v_alignbit_b32 %0, v237, v237, 16" : "=v"(im[4][2]) :  : ); SB();
  asm volatile("v_mfma_f32_16x16x32_bf16 %0, v[192:195], a[28:31], %0" : "+v"(a1[1][2]) :  : ); SB();
  asm volatile("ds_read_b64_tr_b16 v[180:181], %0 offset:6144" :  : "v"(aW[1][2]) : "memory"); SB();
  asm volatile("v_alignbit_b32 %0, v236, v236, 16" : "=v"(im[4][3]) :  : ); SB();
  asm volatile("v_mfma_f32_16x16x32_bf16 %0, v[192:195], %1, %0" : "+v"(c1[2]) : "v"(ones) : ); SB();
  asm volatile("ds_read_b64_tr_b16 v[182:183], %0 offset:6912" :  : "v"(aW[1][2]) : "memory"); SB();
  asm volatile("s_waitcnt lgkmcnt(6)\n\tv_mfma_f32_16x16x32_bf16 %0, v[160:163], a[32:35], %0" : "+v"(a1[0][0]) :  : ); SB();
  asm volatile("ds_read_b128 a[160:163], %0 offset:32768" :  : "v"(aA[0]) : "memory"); SB();
  asm volatile("v_bfi_b32 %0, %1, v239, v238" : "=v"(im[5][0]) : "s"(0xFFFFu) : ); SB();
  asm volatile("v_mfma_f32_16x16x32_bf16 %0, v[160:163], a[36:39], %0" : "+v"(a1[1][0]) :  : ); SB();
  asm volatile("ds_read_b128 a[176:179], %0 offset:32768" :  : "v"(aA[1]) : "memory"); SB();
  asm volatile("s_waitcnt vmcnt(18)" :  :  : "memory"); SB();
  asm volatile("v_mfma_f32_16x16x32_bf16 %0, v[160:163], %1, %0" : "+v"(c1[0]) : "v"(ones) : ); SB();
  asm volatile("ds_read_b128 a[164:167], %0 offset:34816" :  : "v"(aA[0]) : "memory"); SB();
  asm volatile("v_bfi_b32 %0, %1, v238, v237" : "=v"(im[5][1]) : "s"(0xFFFFu) : ); SB();
  asm volatile("v_mfma_f32_16x16x32_bf16 %0, v[164:167], a[32:35], %0" : "+v"(a1[0][1]) :  : ); SB();
  asm volatile("ds_read_b64_tr_b16 v[184:185], %0 offset:12288" :  : "v"(aW[1][0]) : "memory"); SB();
  asm volatile("v_bfi_b32 %0, %1, v237, v236" : "=v"(im[5][2]) : "s"(0xFFFFu) : ); SB();
  asm volatile("v_mfma_f32_16x16x32_bf16 %0, v[164:167], a[36:39], %0" : "+v"(a1[1][1]) :  : ); SB();
  asm volatile("ds_read_b64_tr_b16 v[186:187], %0 offset:13056" :  : "v"(aW[1][0]) : "memory"); SB();
  asm volatile("v_bfi_b32 %0, %1, v236, v244" : "=v"(im[5][3]) : "s"(0xFFFFu) : ); SB();
  asm volatile("v_mfma_f32_16x16x32_bf16 %0, v[164:167], %1, %0" : "+v"(c1[1]) : "v"(ones) : ); SB();
  asm volatile("ds_read_b64_tr_b16 v[188:189], %0 offset:12288" :  : "v"(aW[1][1]) : "memory"); SB();
  asm volatile("v_mov_b32 %0, 0" : "=v"(zero4[0]) :  : ); SB();
  asm volatile("v_mfma_f32_16x16x32_bf16 %0, v[168:171], a[32:35], %0" : "+v"(a1[0][2]) :  : ); SB();
  asm volatile("ds_read_b64_tr_b16 v[190:191], %0 offset:13056" :  : "v"(aW[1][1]) : "memory"); SB();
  asm volatile("v_mov_b32 %0, 0" : "=v"(zero4[1]) :  : ); SB();
  asm volatile("v_mfma_f32_16x16x32_bf16 %0, v[168:171], a[36:39], %0" : "+v"(a1[1][2]) :  : ); SB();
  asm volatile("ds_read_b64_tr_b16 v[192:193], %0 offset:12288" :  : "v"(aW[1][2]) : "memory"); SB();
  asm volatile("v_mov_b32 %0, 0" : "=v"(zero4[2]) :  : ); SB();
  asm volatile("v_mfma_f32_16x16x32_bf16 %0, v[168:171], %1, %0" : "+v"(c1[2]) : "v"(ones) : ); SB();
  asm volatile("ds_read_b64_tr_b16 v[194:195], %0 offset:13056" :  : "v"(aW[1][2]) : "memory"); SB();
  ST(8);
  asm volatile("ds_read_b128 a[180:183], %0 offset:34816" :  : "v"(aA[1]) : "memory"); SB();
  asm volatile("s_waitcnt vmcnt(16)" :  :  : "memory"); SB();
  asm volatile("ds_read_b128 a[168:171], %0 offset:36864" :  : "v"(aA[0]) : "memory"); SB();
  asm volatile("ds_read_b128 a[184:187], %0 offset:36864" :  : "v"(aA[1]) : "memory"); SB();
  asm volatile("s_waitcnt vmcnt(14)" :  :  : "memory"); SB();
  asm volatile("ds_read_b128 a[172:175], %0 offset:38912" :  : "v"(aA[0]) : "memory"); SB();
  asm volatile("ds_read_b128 a[188:191], %0 offset:38912" :  : "v"(aA[1]) : "memory"); SB();
  asm volatile("s_waitcnt vmcnt(12)" :  :  : "memory"); SB();
  asm volatile("ds_read_b128 a[192:195], %0 offset:0" :  : "v"(aA[0]) : "memory"); SB();
  asm volatile("ds_read_b128 a[208:211], %0 offset:0" :  : "v"(aA[1]) : "memory"); SB();
  asm volatile("s_add_u32 m0, %0, 12288" :  : "s"(mW) : "memory", "scc"); SB();
  asm volatile("s_nop 0\n\tbuffer_load_dwordx4 %0, %1, %2 offen lds" :  : "v"(voB[0]), "s"(rW1), "s"(kw_[7]) : "memory"); SB();
  asm volatile("buffer_load_dwordx4 %0, %1, %2 offen offset:1024 lds" :  : "v"(voB[1]), "s"(rW1), "s"(kw_[7]) : "memory"); SB();
  asm volatile("buffer_load_dwordx4 %0, %1, %2 offen offset:2048 lds" :  : "v"(voB[2]), "s"(rW1), "s"(kw_[7]) : "memory"); SB();
  asm volatile("s_add_u32 m0, %0, 24576" :  : "s"(mW) : "memory", "scc"); SB();
  asm volatile("s_nop 0\n\tbuffer_load_dwordx4 %0, %1, %2 offen lds" :  : "v"(voB[0]), "s"(rW1), "s"(kw_[8]) : "memory"); SB();
  asm volatile("buffer_load_dwordx4 %0, %1, %2 offen offset:1024 lds" :  : "v"(voB[1]), "s"(rW1), "s"(kw_[8]) : "memory"); SB();
  asm volatile("buffer_load_dwordx4 %0, %1, %2 offen offset:2048 lds" :  : "v"(voB[2]), "s"(rW1), "s"(kw_[8]) : "memory"); SB();
  asm volatile("s_waitcnt vmcnt(10)\n\t.Lc_%=:\n\tv_cmpx_eq_u64 vcc, %1, v[198:199]\n\tv_cmpx_eq_u64 vcc, %1, v[202:203]\n\tv_cmpx_eq_u64 vcc, %1, v[206:207]\n\tv_cmpx_eq_u64 vcc, %1, v[210:211]\n\tv_cmpx_eq_u64 vcc, %1, v[214:215]\n\tv_cmpx_eq_u64 vcc, %1, v[218:219]\n\tv_cmpx_eq_u64 vcc, %1, v[222:223]\n\tv_cmpx_eq_u64 vcc, %1, v[226:227]\n\ts_cmp_eq_u64 vcc, -1\n\ts_mov_b64 exec, -1\n\ts_cbranch_scc0 .Lr_%=\n\t.Ld_%=:\n\t.subsection 1\n\t.Lr_%=:\n\tv_add_u32 %0, -1, %0\n\ts_nop 1\n\tv_readfirstlane_b32 vcc_hi, %0\n\ts_nop 3\n\ts_cmp_lt_i32 vcc_hi, 1\n\ts_cbranch_scc1 .Ld_%=\n\ts_sleep 2\n\ts_movk_i32 vcc_lo, 0x1000\n\tbuffer_load_dwordx4 v[196:199], %2, %3, 0 offen offset:0 sc1\n\tbuffer_load_dwordx4 v[200:203], %2, %3, 0 offen offset:1024 sc1\n\tbuffer_load_dwordx4 v[204:207], %2, %3, 0 offen offset:2048 sc1\n\tbuffer_load_dwordx4 v[208:211], %2, %3, 0 offen offset:3072 sc1\n\tbuffer_load_dwordx4 v[212:215], %2, %3, vcc_lo offen offset:0 sc1\n\tbuffer_load_dwordx4 v[216:219], %2, %3, vcc_lo offen offset:1024 sc1\n\tbuffer_load_dwordx4 v[220:223], %2, %3, vcc_lo offen offset:2048 sc1\n\tbuffer_load_dwordx4 v[224:227], %2, %3, vcc_lo offen offset:3072 sc1\n\ts_waitcnt vmcnt(0)\n\ts_branch .Lc_%=\n\t.subsection 0" : "+v"(tmo) : "s"(tg), "v"(vS), "s"(rWS) : "memory", "vcc", "scc"); SB();
  ST(9);
  {
    f32x2 s4[4], dm[4];
  asm volatile("v_pk_add_f32 %0, v[196:197], v[200:201]\n\tv_pk_add_f32 %1, v[204:205], v[208:209]\n\tv_pk_add_f32 %2, v[212:213], v[216:217]\n\tv_pk_add_f32 %3, v[220:221], v[224:225]" : "=&v"(s4[0]), "=&v"(s4[1]), "=&v"(s4[2]), "=&v"(s4[3]) :  : ); SB();
    const f32x2 sm = (s4[0] + s4[1]) + (s4[2] + s4[3]);
    const float mean = (sm[0] + dppf<0x128>(sm[0])) * (1.0f / 16.0f);
  asm volatile("v_sub_f32 %0, v196, %8\n\tv_sub_f32 %1, v200, %8\n\tv_sub_f32 %2, v204, %8\n\tv_sub_f32 %3, v208, %8\n\tv_sub_f32 %4, v212, %8\n\tv_sub_f32 %5, v216, %8\n\tv_sub_f32 %6, v220, %8\n\tv_sub_f32 %7, v224, %8" : "=&v"(dm[0][0]), "=&v"(dm[0][1]), "=&v"(dm[1][0]), "=&v"(dm[1][1]), "=&v"(dm[2][0]), "=&v"(dm[2][1]), "=&v"(dm[3][0]), "=&v"(dm[3][1]) : "v"(mean) : ); SB();
    const f32x2 dq = __builtin_elementwise_fma(dm[0], dm[0], dm[1] * dm[1]) + __builtin_elementwise_fma(dm[2], dm[2], dm[3] * dm[3]);
    float qs = __builtin_fmaf(dq[0] + dq[1], 48.0f, sm[1]);
    qs += dppf<0x128>(qs);
    float r = __builtin_amdgcn_rcpf(rbf(__builtin_amdgcn_sqrtf(rbf(rbf(qs * (1.0f / 768.0f)) + 1e-5f))));
    float mr = -rbf(mean) * r;
    asm volatile("" : "+v"(r), "+v"(mr)); SB();
  asm volatile("s_add_u32 m0, %0, 98304" :  : "s"(mA) : "memory", "scc"); SB();
  asm volatile("s_nop 0\n\tbuffer_load_dwordx4 %0, %1, %2 offen offset:2048 lds" :  : "v"(voS), "s"(rA), "s"(ao[2]) : "memory"); SB();
  asm volatile("buffer_load_dwordx4 %0, %1, %2 offen offset:3072 lds" :  : "v"(voS), "s"(rA), "s"(ao[3]) : "memory"); SB();
  asm volatile("s_add_u32 m0, %0, 102400" :  : "s"(mA) : "memory", "scc"); SB();
  asm volatile("s_nop 0\n\tbuffer_load_dwordx4 %0, %1, %2 offen lds" :  : "v"(voS), "s"(rA), "s"(ao[4]) : "memory"); SB();
    const f32x2 re[4] = {{dppf<0x150>(r), dppf<0x151>(r)}, {dppf<0x152>(r), dppf<0x153>(r)}, {dppf<0x154>(r), dppf<0x155>(r)}, {dppf<0x156>(r), dppf<0x157>(r)}};
    const f32x2 me[4] = {{dppf<0x150>(mr), dppf<0x151>(mr)}, {dppf<0x152>(mr), dppf<0x153>(mr)}, {dppf<0x154>(mr), dppf<0x155>(mr)}, {dppf<0x156>(mr), dppf<0x157>(mr)}};
    u32x4 zo[3];
#pragma unroll
    for (int e = 0; e < 4; ++e) zo[0][e] = pk2(__builtin_elementwise_fma(unpk(zt[0][e >> 1][e & 1]), re[e], me[e]));
    SB();
  asm volatile("buffer_store_dwordx4 %1, %0, %2, %3 offen offset:0 sc1\n\ts_nop 1" :  : "v"(vZ_), "v"(zo[0]), "s"(rWS), "s"(0u) : "memory"); SB();
#pragma unroll
    for (int e = 0; e < 4; ++e) zo[1][e] = pk2(__builtin_elementwise_fma(unpk(zt[1][e >> 1][e & 1]), re[e], me[e]));
    SB();
  asm volatile("buffer_store_dwordx4 %1, %0, %2, %3 offen offset:1024 sc1\n\ts_nop 1" :  : "v"(vZ_), "v"(zo[1]), "s"(rWS), "s"(0u) : "memory"); SB();
#pragma unroll
    for (int e = 0; e < 4; ++e) zo[2][e] = pk2(__builtin_elementwise_fma(unpk(zt[2][e >> 1][e & 1]), re[e], me[e]));
    SB();
  asm volatile("buffer_store_dwordx4 %1, %0, %2, %3 offen offset:2048 sc1\n\ts_nop 1" :  : "v"(vZ_), "v"(zo[2]), "s"(rWS), "s"(0u) : "memory"); SB();
  }
  ST(10);
  asm volatile("s_waitcnt lgkmcnt(0)\n\tv_mfma_f32_16x16x32_bf16 %0, v[172:175], a[40:43], %0" : "+v"(a1[0][0]) :  : ); SB();
  asm volatile("buffer_load_dwordx4 %0, %1, %2 offen offset:1024 lds" :  : "v"(voS), "s"(rA), "s"(ao[5]) : "memory"); SB();
  asm volatile("v_mov_b32 %0, 0" : "=v"(zero4[3]) :  : ); SB();
  asm volatile("v_mfma_f32_16x16x32_bf16 %0, v[172:175], a[44:47], %0" : "+v"(a1[1][0]) :  : ); SB();
  asm volatile("v_mfma_f32_16x16x32_bf16 %0, v[172:175], %1, %0" : "+v"(c1[0]) : "v"(ones) : ); SB();
  asm volatile("v_mfma_f32_16x16x32_bf16 %0, v[176:179], a[40:43], %0" : "+v"(a1[0][1]) :  : ); SB();
  asm volatile("ds_read_b64_tr_b16 v[160:161], %0 offset:18432" :  : "v"(aW[1][0]) : "memory"); SB();
  asm volatile("v_mfma_f32_16x16x32_bf16 %0, v[176:179], a[44:47], %0" : "+v"(a1[1][1]) :  : ); SB();
  asm volatile("ds_read_b64_tr_b16 v[162:163], %0 offset:19200" :  : "v"(aW[1][0]) : "memory"); SB();
  asm volatile("v_mfma_f32_16x16x32_bf16 %0, v[176:179], %1, %0" : "+v"(c1[1]) : "v"(ones) : ); SB();
  asm volatile("ds_read_b64_tr_b16 v[164:165], %0 offset:18432" :  : "v"(aW[1][1]) : "memory"); SB();
  asm volatile("v_mfma_f32_16x16x32_bf16 %0, v[180:183], a[40:43], %0" : "+v"(a1[0][2]) :  : ); SB();
  asm volatile("ds_read_b64_tr_b16 v[166:167], %0 offset:19200" :  : "v"(aW[1][1]) : "memory"); SB();
  asm volatile("s_waitcnt vmcnt(13)\n\ts_mov_b64 exec, 1\n\tbuffer_store_dwordx2 %0, off, %1, %2 sc1\n\ts_mov_b64 exec, -1" :  : "v"(((u32x2){tg0, tg1})), "s"(rWS), "s"(sFK) : "memory"); SB();
  asm volatile("v_mfma_f32_16x16x32_bf16 %0, v[180:183], a[44:47], %0" : "+v"(a1[1][2]) :  : ); SB();
  asm volatile("ds_read_b64_tr_b16 v[168:169], %0 offset:18432" :  : "v"(aW[1][2]) : "memory"); SB();
  asm volatile("v_mfma_f32_16x16x32_bf16 %0, v[180:183], %1, %0" : "+v"(c1[2]) : "v"(ones) : ); SB();
  asm volatile("ds_read_b64_tr_b16 v[170:171], %0 offset:19200" :  : "v"(aW[1][2]) : "memory"); SB();
  asm volatile("v_mfma_f32_16x16x32_bf16 %0, v[184:187], a[48:51], %0" : "+v"(a1[0][0]) :  : ); SB();
  asm volatile("buffer_load_dwordx4 %0, %1, %2 offen offset:2048 lds" :  : "v"(voS), "s"(rA), "s"(ao[6]) : "memory"); SB();
  asm volatile("v_mfma_f32_16x16x32_bf16 %0, v[184:187], a[52:55], %0" : "+v"(a1[1][0]) :  : ); SB();
  asm volatile("buffer_load_dwordx4 %0, %1, %2 offen offset:3072 lds" :  : "v"(voS), "s"(rA), "s"(ao[7]) : "memory"); SB();
  asm volatile("v_mfma_f32_16x16x32_bf16 %0, v[184:187], %1, %0" : "+v"(c1[0]) : "v"(ones) : ); SB();
  asm volatile("v_xor_b32 %0, 384, %0" : "+v"(voS) :  : "memory"); SB();
  asm volatile("v_mfma_f32_16x16x32_bf16 %0, v[188:191], a[48:51], %0" : "+v"(a1[0][1]) :  : ); SB();
  asm volatile("s_barrier" :  :  : "memory"); SB();
  asm volatile("ds_read_b64_tr_b16 v[172:173], %0 offset:24576" :  : "v"(aW[1][0]) : "memory"); SB();
  asm volatile("s_add_u32 m0, %0, 36864" :  : "s"(mW) : "memory", "scc"); SB();
  asm volatile("v_mfma_f32_16x16x32_bf16 %0, v[188:191], a[52:55], %0" : "+v"(a1[1][1]) :  : ); SB();
  asm volatile("ds_read_b64_tr_b16 v[174:175], %0 offset:25344" :  : "v"(aW[1][0]) : "memory"); SB();
  asm volatile("v_mfma_f32_16x16x32_bf16 %0, v[188:191], %1, %0" : "+v"(c1[1]) : "v"(ones) : ); SB();
  asm volatile("ds_read_b64_tr_b16 v[176:177], %0 offset:24576" :  : "v"(aW[1][1]) : "memory"); SB();
  asm volatile("v_mfma_f32_16x16x32_bf16 %0, v[192:195], a[48:51], %0" : "+v"(a1[0][2]) :  : ); SB();
  asm volatile("ds_read_b64_tr_b16 v[178:179], %0 offset:25344" :  : "v"(aW[1][1]) : "memory"); SB();
  asm volatile("v_mfma_f32_16x16x32_bf16 %0, v[192:195], a[52:55], %0" : "+v"(a1[1][2]) :  : ); SB();
  asm volatile("ds_read_b64_tr_b16 v[180:181], %0 offset:24576" :  : "v"(aW[1][2]) : "memory"); SB();
  asm volatile("v_mfma_f32_16x16x32_bf16 %0, v[192:195], %1, %0" : "+v"(c1[2]) : "v"(ones) : ); SB();
  asm volatile("ds_read_b64_tr_b16 v[182:183], %0 offset:25344" :  : "v"(aW[1][2]) : "memory"); SB();
  asm volatile("s_waitcnt lgkmcnt(6)\n\tv_mfma_f32_16x16x32_bf16 %0, v[160:163], a[56:59], %0" : "+v"(a1[0][0]) :  : ); SB();
  asm volatile("buffer_load_dwordx4 %0, %1, %2 offen lds" :  : "v"(voB[0]), "s"(rW1), "s"(kw_[9]) : "memory"); SB();
  asm volatile("v_mfma_f32_16x16x32_bf16 %0, v[160:163], a[60:63], %0" : "+v"(a1[1][0]) :  : ); SB();
  asm volatile("buffer_load_dwordx4 %0, %1, %2 offen offset:1024 lds" :  : "v"(voB[1]), "s"(rW1), "s"(kw_[9]) : "memory"); SB();
  asm volatile("v_mfma_f32_16x16x32_bf16 %0, v[160:163], %1, %0" : "+v"(c1[0]) : "v"(ones) : ); SB();
  asm volatile("v_mfma_f32_16x16x32_bf16 %0, v[164:167], a[56:59], %0" : "+v"(a1[0][1]) :  : ); SB();
  asm volatile("ds_read_b64_tr_b16 v[184:185], %0 offset:30720" :  : "v"(aW[1][0]) : "memory"); SB();
  asm volatile("v_mfma_f32_16x16x32_bf16 %0, v[164:167], a[60:63], %0" : "+v"(a1[1][1]) :  : ); SB();
  asm volatile("ds_read_b64_tr_b16 v[186:187], %0 offset:31488" :  : "v"(aW[1][0]) : "memory"); SB();
  asm volatile("v_mfma_f32_16x16x32_bf16 %0, v[164:167], %1, %0" : "+v"(c1[1]) : "v"(ones) : ); SB();
  asm volatile("ds_read_b64_tr_b16 v[188:189], %0 offset:30720" :  : "v"(aW[1][1]) : "memory"); SB();
  asm volatile("v_mfma_f32_16x16x32_bf16 %0, v[168:171], a[56:59], %0" : "+v"(a1[0][2]) :  : ); SB();
  asm volatile("ds_read_b64_tr_b16 v[190:191], %0 offset:31488" :  : "v"(aW[1][1]) : "memory"); SB();
  asm volatile("v_mfma_f32_16x16x32_bf16 %0, v[168:171], a[60:63], %0" : "+v"(a1[1][2]) :  : ); SB();
  asm volatile("ds_read_b64_tr_b16 v[192:193], %0 offset:30720" :  : "v"(aW[1][2]) : "memory"); SB();
  asm volatile("v_mfma_f32_16x16x32_bf16 %0, v[168:171], %1, %0" : "+v"(c1[2]) : "v"(ones) : ); SB();
  asm volatile("ds_read_b64_tr_b16 v[194:195], %0 offset:31488" :  : "v"(aW[1][2]) : "memory"); SB();
  asm volatile("s_waitcnt lgkmcnt(6)\n\tv_mfma_f32_16x16x32_bf16 %0, v[172:175], a[64:67], %0" : "+v"(a1[0][0]) :  : ); SB();
  asm volatile("buffer_load_dwordx4 %0, %1, %2 offen offset:2048 lds" :  : "v"(voB[2]), "s"(rW1), "s"(kw_[9]) : "memory"); SB();
  asm volatile("s_add_u32 m0, %0, 131072" :  : "s"(mA) : "memory", "scc"); SB();
  asm volatile("v_mfma_f32_16x16x32_bf16 %0, v[172:175], a[68:71], %0" : "+v"(a1[1][0]) :  : ); SB();
  asm volatile("buffer_load_dwordx4 %0, %1, %2 offen lds" :  : "v"(voS), "s"(rA), "s"(ao[0]) : "memory"); SB();
  asm volatile("v_mfma_f32_16x16x32_bf16 %0, v[172:175], %1, %0" : "+v"(c1[0]) : "v"(ones) : ); SB();
  asm volatile("v_mfma_f32_16x16x32_bf16 %0, v[176:179], a[64:67], %0" : "+v"(a1[0][1]) :  : ); SB();
  asm volatile("ds_read_b64_tr_b16 v[160:161], %0 offset:36864" :  : "v"(aW[1][0]) : "memory"); SB();
  asm volatile("v_mfma_f32_16x16x32_bf16 %0, v[176:179], a[68:71], %0" : "+v"(a1[1][1]) :  : ); SB();
  asm volatile("ds_read_b64_tr_b16 v[162:163], %0 offset:37632" :  : "v"(aW[1][0]) : "memory"); SB();
  asm volatile("v_mfma_f32_16x16x32_bf16 %0, v[176:179], %1, %0" : "+v"(c1[1]) : "v"(ones) : ); SB();
  asm volatile("ds_read_b64_tr_b16 v[164:165], %0 offset:36864" :  : "v"(aW[1][1]) : "memory"); SB();
  asm volatile("v_mfma_f32_16x16x32_bf16 %0, v[180:183], a[64:67], %0" : "+v"(a1[0][2]) :  : ); SB();
  asm volatile("ds_read_b64_tr_b16 v[166:167], %0 offset:37632" :  : "v"(aW[1][1]) : "memory"); SB();
  asm volatile("v_mfma_f32_16x16x32_bf16 %0, v[180:183], a[68:71], %0" : "+v"(a1[1][2]) :  : ); SB();
  asm volatile("ds_read_b64_tr_b16 v[168:169], %0 offset:36864" :  : "v"(aW[1][2]) : "memory"); SB();
  asm volatile("v_mfma_f32_16x16x32_bf16 %0, v[180:183], %1, %0" : "+v"(c1[2]) : "v"(ones) : ); SB();
  asm volatile("ds_read_b64_tr_b16 v[170:171], %0 offset:37632" :  : "v"(aW[1][2]) : "memory"); SB();
  asm volatile("s_waitcnt lgkmcnt(6)\n\tv_mfma_f32_16x16x32_bf16 %0, v[184:187], a[72:75], %0" : "+v"(a1[0][0]) :  : ); SB();
  asm volatile("buffer_load_dwordx4 %0, %1, %2 offen offset:1024 lds" :  : "v"(voS), "s"(rA), "s"(ao[1]) : "memory"); SB();
  asm volatile("v_mfma_f32_16x16x32_bf16 %0, v[184:187], a[76:79], %0" : "+v"(a1[1][0]) :  : ); SB();
  asm volatile("buffer_load_dwordx4 %0, %1, %2 offen offset:2048 lds" :  : "v"(voS), "s"(rA), "s"(ao[2]) : "memory"); SB();
  asm volatile("v_mfma_f32_16x16x32_bf16 %0, v[184:187], %1, %0" : "+v"(c1[0]) : "v"(ones) : ); SB();
  asm volatile("v_mfma_f32_16x16x32_bf16 %0, v[188:191], a[72:75], %0" : "+v"(a1[0][1]) :  : ); SB();
  asm volatile("ds_read_b64_tr_b16 v[172:173], %0 offset:43008" :  : "v"(aW[1][0]) : "memory"); SB();
  asm volatile("s_waitcnt vmcnt(14)" :  :  : "memory"); SB();
  asm volatile("v_mfma_f32_16x16x32_bf16 %0, v[188:191], a[76:79], %0" : "+v"(a1[1][1]) :  : ); SB();
  asm volatile("ds_read_b64_tr_b16 v[174:175], %0 offset:43776" :  : "v"(aW[1][0]) : "memory"); SB();
  asm volatile("v_mfma_f32_16x16x32_bf16 %0, v[188:191], %1, %0" : "+v"(c1[1]) : "v"(ones) : ); SB();
  asm volatile("ds_read_b64_tr_b16 v[176:177], %0 offset:43008" :  : "v"(aW[1][1]) : "memory"); SB();
  asm volatile("v_mfma_f32_16x16x32_bf16 %0, v[192:195], a[72:75], %0" : "+v"(a1[0][2]) :  : ); SB();
  asm volatile("ds_read_b64_tr_b16 v[178:179], %0 offset:43776" :  : "v"(aW[1][1]) : "memory"); SB();
  asm volatile("v_mfma_f32_16x16x32_bf16 %0, v[192:195], a[76:79], %0" : "+v"(a1[1][2]) :  : ); SB();
  asm volatile("ds_read_b64_tr_b16 v[180:181], %0 offset:43008" :  : "v"(aW[1][2]) : "memory"); SB();
  asm volatile("v_mfma_f32_16x16x32_bf16 %0, v[192:195], %1, %0" : "+v"(c1[2]) : "v"(ones) : ); SB();
  asm volatile("ds_read_b64_tr_b16 v[182:183], %0 offset:43776" :  : "v"(aW[1][2]) : "memory"); SB();
  asm volatile("s_waitcnt lgkmcnt(6)\n\tv_mfma_f32_16x16x32_bf16 %0, v[160:163], a[80:83], %0" : "+v"(a1[0][0]) :  : ); SB();
  asm volatile("ds_read_b128 a[196:199], %0 offset:2048" :  : "v"(aA[0]) : "memory"); SB();
  asm volatile("v_mfma_f32_16x16x32_bf16 %0, v[160:163], a[84:87], %0" : "+v"(a1[1][0]) :  : ); SB();
  asm volatile("ds_read_b128 a[212:215], %0 offset:2048" :  : "v"(aA[1]) : "memory"); SB();
  asm volatile("v_mfma_f32_16x16x32_bf16 %0, v[160:163], %1, %0" : "+v"(c1[0]) : "v"(ones) : ); SB();
  asm volatile("buffer_load_dwordx4 %0, %1, %2 offen offset:3072 lds" :  : "v"(voS), "s"(rA), "s"(ao[3]) : "memory"); SB();
  asm volatile("s_add_u32 m0, %0, 135168" :  : "s"(mA) : "memory", "scc"); SB();
  asm volatile("v_mfma_f32_16x16x32_bf16 %0, v[164:167], a[80:83], %0" : "+v"(a1[0][1]) :  : ); SB();
  asm volatile("s_barrier" :  :  : "memory"); SB();
  asm volatile("ds_read_b64_tr_b16 v[184:185], %0 offset:0" :  : "v"(aW[0][0]) : "memory"); SB();
  asm volatile("v_mfma_f32_16x16x32_bf16 %0, v[164:167], a[84:87], %0" : "+v"(a1[1][1]) :  : ); SB();
  asm volatile("ds_read_b64_tr_b16 v[186:187], %0 offset:768" :  : "v"(aW[0][0]) : "memory"); SB();
  asm volatile("v_mfma_f32_16x16x32_bf16 %0, v[164:167], %1, %0" : "+v"(c1[1]) : "v"(ones) : ); SB();
  asm volatile("ds_read_b64_tr_b16 v[188:189], %0 offset:0" :  : "v"(aW[0][1]) : "memory"); SB();
  asm volatile("v_mfma_f32_16x16x32_bf16 %0, v[168:171], a[80:83], %0" : "+v"(a1[0][2]) :  : ); SB();
  asm volatile("ds_read_b64_tr_b16 v[190:191], %0 offset:768" :  : "v"(aW[0][1]) : "memory"); SB();
  asm volatile("v_mfma_f32_16x16x32_bf16 %0, v[168:171], a[84:87], %0" : "+v"(a1[1][2]) :  : ); SB();
  asm volatile("ds_read_b64_tr_b16 v[192:193], %0 offset:0" :  : "v"(aW[0][2]) : "memory"); SB();
  asm volatile("v_mfma_f32_16x16x32_bf16 %0, v[168:171], %1, %0" : "+v"(c1[2]) : "v"(ones) : ); SB();
  asm volatile("ds_read_b64_tr_b16 v[194:195], %0 offset:768" :  : "v"(aW[0][2]) : "memory"); SB();
  asm volatile("s_waitcnt lgkmcnt(7)\n\tv_mfma_f32_16x16x32_bf16 %0, v[172:175], a[88:91], %0" : "+v"(a1[0][0]) :  : ); SB();
  asm volatile("buffer_load_dwordx4 %0, %1, %2 offen lds" :  : "v"(voS), "s"(rA), "s"(ao[4]) : "memory"); SB();
  asm volatile("v_mfma_f32_16x16x32_bf16 %0, v[172:175], a[92:95], %0" : "+v"(a1[1][0]) :  : ); SB();
  asm volatile("buffer_load_dwordx4 %0, %1, %2 offen offset:1024 lds" :  : "v"(voS), "s"(rA), "s"(ao[5]) : "memory"); SB();
  asm volatile("v_mfma_f32_16x16x32_bf16 %0, v[172:175], %1, %0" : "+v"(c1[0]) : "v"(ones) : ); SB();
  asm volatile("v_mfma_f32_16x16x32_bf16 %0, v[176:179], a[88:91], %0" : "+v"(a1[0][1]) :  : ); SB();
  asm volatile("ds_read_b64_tr_b16 v[160:161], %0 offset:6144" :  : "v"(aW[0][0]) : "memory"); SB();
  asm volatile("v_mfma_f32_16x16x32_bf16 %0, v[176:179], a[92:95], %0" : "+v"(a1[1][1]) :  : ); SB();
  asm volatile("ds_read_b64_tr_b16 v[162:163], %0 offset:6912" :  : "v"(aW[0][0]) : "memory"); SB();
  asm volatile("v_mfma_f32_16x16x32_bf16 %0, v[176:179], %1, %0" : "+v"(c1[1]) : "v"(ones) : ); SB();
  asm volatile("ds_read_b64_tr_b16 v[164:165], %0 offset:6144" :  : "v"(aW[0][1]) : "memory"); SB();
  asm volatile("v_mfma_f32_16x16x32_bf16 %0, v[180:183], a[88:91], %0" : "+v"(a1[0][2]) :  : ); SB();
  asm volatile("ds_read_b64_tr_b16 v[166:167], %0 offset:6912" :  : "v"(aW[0][1]) : "memory"); SB();
  asm volatile("v_mfma_f32_16x16x32_bf16 %0, v[180:183], a[92:95], %0" : "+v"(a1[1][2]) :  : ); SB();
  asm volatile("ds_read_b64_tr_b16 v[168:169], %0 offset:6144" :  : "v"(aW[0][2]) : "memory"); SB();
  asm volatile("v_mfma_f32_16x16x32_bf16 %0, v[180:183], %1, %0" : "+v"(c1[2]) : "v"(ones) : ); SB();
  asm volatile("ds_read_b64_tr_b16 v[170:171], %0 offset:6912" :  : "v"(aW[0][2]) : "memory"); SB();
  asm volatile("s_waitcnt lgkmcnt(6)\n\tv_mfma_f32_16x16x32_bf16 %0, v[184:187], a[96:99], %0" : "+v"(a1[0][0]) :  : ); SB();
  asm volatile("v_mfma_f32_16x16x32_bf16 %0, v[184:187], a[100:103], %0" : "+v"(a1[1][0]) :  : ); SB();
  asm volatile("v_mfma_f32_16x16x32_bf16 %0, v[184:187], %1, %0" : "+v"(c1[0]) : "v"(ones) : ); SB();
  asm volatile("v_mfma_f32_16x16x32_bf16 %0, v[188:191], a[96:99], %0" : "+v"(a1[0][1]) :  : ); SB();
  asm volatile("ds_read_b64_tr_b16 v[172:173], %0 offset:12288" :  : "v"(aW[0][0]) : "memory"); SB();
  asm volatile("v_mfma_f32_16x16x32_bf16 %0, v[188:191], a[100:103], %0" : "+v"(a1[1][1]) :  : ); SB();
  asm volatile("ds_read_b64_tr_b16 v[174:175], %0 offset:13056" :  : "v"(aW[0][0]) : "memory"); SB();
  asm volatile("v_mfma_f32_16x16x32_bf16 %0, v[188:191], %1, %0" : "+v"(c1[1]) : "v"(ones) : ); SB();
  asm volatile("ds_read_b64_tr_b16 v[176:177], %0 offset:12288" :  : "v"(aW[0][1]) : "memory"); SB();
  asm volatile("v_mfma_f32_16x16x32_bf16 %0, v[192:195], a[96:99], %0" : "+v"(a1[0][2]) :  : ); SB();
  asm volatile("ds_read_b64_tr_b16 v[178:179], %0 offset:13056" :  : "v"(aW[0][1]) : "memory"); SB();
  asm volatile("v_mfma_f32_16x16x32_bf16 %0, v[192:195], a[100:103], %0" : "+v"(a1[1][2]) :  : ); SB();
  asm volatile("ds_read_b64_tr_b16 v[180:181], %0 offset:12288" :  : "v"(aW[0][2]) : "memory"); SB();
  asm volatile("v_mfma_f32_16x16x32_bf16 %0, v[192:195], %1, %0" : "+v"(c1[2]) : "v"(ones) : ); SB();
  asm volatile("ds_read_b64_tr_b16 v[182:183], %0 offset:13056" :  : "v"(aW[0][2]) : "memory"); SB();
  asm volatile("s_waitcnt lgkmcnt(6)\n\tv_mfma_f32_16x16x32_bf16 %0, v[160:163], a[104:107], %0" : "+v"(a1[0][0]) :  : ); SB();
  asm volatile("buffer_load_dwordx4 %0, %1, %2 offen offset:2048 lds" :  : "v"(voS), "s"(rA), "s"(ao[6]) : "memory"); SB();
  asm volatile("v_mfma_f32_16x16x32_bf16 %0, v[160:163], a[108:111], %0" : "+v"(a1[1][0]) :  : ); SB();
  asm volatile("v_mfma_f32_16x16x32_bf16 %0, v[160:163], %1, %0" : "+v"(c1[0]) : "v"(ones) : ); SB();
  asm volatile("v_mfma_f32_16x16x32_bf16 %0, v[164:167], a[104:107], %0" : "+v"(a1[0][1]) :  : ); SB();
  asm volatile("ds_read_b64_tr_b16 v[184:185], %0 offset:18432" :  : "v"(aW[0][0]) : "memory"); SB();
  asm volatile("v_mfma_f32_16x16x32_bf16 %0, v[164:167], a[108:111], %0" : "+v"(a1[1][1]) :  : ); SB();
  asm volatile("ds_read_b64_tr_b16 v[186:187], %0 offset:19200" :  : "v"(aW[0][0]) : "memory"); SB();
  asm volatile("v_mfma_f32_16x16x32_bf16 %0, v[164:167], %1, %0" : "+v"(c1[1]) : "v"(ones) : ); SB();
  asm volatile("ds_read_b64_tr_b16 v[188:189], %0 offset:18432" :  : "v"(aW[0][1]) : "memory"); SB();
  asm volatile("v_mfma_f32_16x16x32_bf16 %0, v[168:171], a[104:107], %0" : "+v"(a1[0][2]) :  : ); SB();
  asm volatile("ds_read_b64_tr_b16 v[190:191], %0 offset:19200" :  : "v"(aW[0][1]) : "memory"); SB();
  asm volatile("v_mfma_f32_16x16x32_bf16 %0, v[168:171], a[108:111], %0" : "+v"(a1[1][2]) :  : ); SB();
  asm volatile("ds_read_b64_tr_b16 v[192:193], %0 offset:18432" :  : "v"(aW[0][2]) : "memory"); SB();
  asm volatile("v_mfma_f32_16x16x32_bf16 %0, v[168:171], %1, %0" : "+v"(c1[2]) : "v"(ones) : ); SB();
  asm volatile("ds_read_b64_tr_b16 v[194:195], %0 offset:19200" :  : "v"(aW[0][2]) : "memory"); SB();
  asm volatile("s_waitcnt lgkmcnt(6)\n\tv_mfma_f32_16x16x32_bf16 %0, v[172:175], a[112:115], %0" : "+v"(a1[0][0]) :  : ); SB();
  asm volatile("v_mfma_f32_16x16x32_bf16 %0, v[172:175], a[116:119], %0" : "+v"(a1[1][0]) :  : ); SB();
  asm volatile("s_waitcnt vmcnt(14)\n\ts_mov_b64 exec, 1\n\tbuffer_store_dwordx2 %0, off, %1, %2 sc1\n\ts_mov_b64 exec, -1" :  : "v"(((u32x2){tg0, tg1})), "s"(rWS), "s"(sFZ) : "memory"); SB();
  asm volatile("buffer_load_dwordx4 %0, %1, %2 offen offset:3072 lds" :  : "v"(voS), "s"(rA), "s"(ao[7]) : "memory"); SB();
  asm volatile("v_mfma_f32_16x16x32_bf16 %0, v[172:175], %1, %0" : "+v"(c1[0]) : "v"(ones) : ); SB();
  asm volatile("v_xor_b32 %0, 128, %0" : "+v"(voS) :  : "memory"); SB();
  asm volatile("s_waitcnt vmcnt(15)" :  :  : "memory"); SB();
  asm volatile("v_mfma_f32_16x16x32_bf16 %0, v[176:179], a[112:115], %0" : "+v"(a1[0][1]) :  : ); SB();
  asm volatile("ds_read_b128 a[200:203], %0 offset:4096" :  : "v"(aA[0]) : "memory"); SB();
  asm volatile("ds_read_b128 a[216:219], %0 offset:4096" :  : "v"(aA[1]) : "memory"); SB();
  asm volatile("s_add_u32 m0, %0, 98304" :  : "s"(mA) : "memory", "scc"); SB();
  asm volatile("s_nop 0\n\tbuffer_load_dwordx4 %0, %1, %2 offen lds" :  : "v"(voS), "s"(rA), "s"(ao[0]) : "memory"); SB();
  asm volatile("buffer_load_dwordx4 %0, %1, %2 offen offset:1024 lds" :  : "v"(voS), "s"(rA), "s"(ao[1]) : "memory"); SB();
  asm volatile("s_waitcnt vmcnt(14)" :  :  : "memory"); SB();
  asm volatile("ds_read_b128 a[204:207], %0 offset:6144" :  : "v"(aA[0]) : "memory"); SB();
  asm volatile("ds_read_b128 a[220:223], %0 offset:6144" :  : "v"(aA[1]) : "memory"); SB();
  asm volatile("buffer_load_dwordx4 %0, %1, %2 offen offset:2048 lds" :  : "v"(voS), "s"(rA), "s"(ao[2]) : "memory"); SB();
  asm volatile("buffer_load_dwordx4 %0, %1, %2 offen offset:3072 lds" :  : "v"(voS), "s"(rA), "s"(ao[3]) : "memory"); SB();
  asm volatile("s_waitcnt lgkmcnt(2)" :  :  : "memory"); SB();
  asm volatile("s_waitcnt vmcnt(13)\n\ts_barrier" :  :  : "memory"); SB();
  asm volatile("ds_read_b64_tr_b16 v[160:161], %0 offset:24576" :  : "v"(aW[0][0]) : "memory"); SB();
  asm volatile("s_add_u32 m0, %0, 102400" :  : "s"(mA) : "memory", "scc"); SB();
  asm volatile("v_mfma_f32_16x16x32_bf16 %0, v[176:179], a[116:119], %0" : "+v"(a1[1][1]) :  : ); SB();
  asm volatile("ds_read_b64_tr_b16 v[162:163], %0 offset:25344" :  : "v"(aW[0][0]) : "memory"); SB();
  asm volatile("v_mfma_f32_16x16x32_bf16 %0, v[176:179], %1, %0" : "+v"(c1[1]) : "v"(ones) : ); SB();
  asm volatile("ds_read_b64_tr_b16 v[164:165], %0 offset:24576" :  : "v"(aW[0][1]) : "memory"); SB();
  asm volatile("v_mfma_f32_16x16x32_bf16 %0, v[180:183], a[112:115], %0" : "+v"(a1[0][2]) :  : ); SB();
  asm volatile("ds_read_b64_tr_b16 v[166:167], %0 offset:25344" :  : "v"(aW[0][1]) : "memory"); SB();
  asm volatile("v_mfma_f32_16x16x32_bf16 %0, v[180:183], a[116:119], %0" : "+v"(a1[1][2]) :  : ); SB();
  asm volatile("ds_read_b64_tr_b16 v[168:169], %0 offset:24576" :  : "v"(aW[0][2]) : "memory"); SB();
  asm volatile("v_mfma_f32_16x16x32_bf16 %0, v[180:183], %1, %0" : "+v"(c1[2]) : "v"(ones) : ); SB();
  asm volatile("ds_read_b64_tr_b16 v[170:171], %0 offset:25344" :  : "v"(aW[0][2]) : "memory"); SB();
  asm volatile("buffer_load_dwordx4 %0, %1, %2 offen lds" :  : "v"(voS), "s"(rA), "s"(ao[4]) : "memory"); SB();
  asm volatile("v_mfma_f32_16x16x32_bf16 %0, v[184:187], a[120:123], %0" : "+v"(a1[0][0]) :  : ); SB();
  asm volatile("s_waitcnt vmcnt(12)" :  :  : "memory"); SB();
  asm volatile("ds_read_b128 a[224:227], %0 offset:32768" :  : "v"(aA[0]) : "memory"); SB();
  asm volatile("v_mfma_f32_16x16x32_bf16 %0, v[184:187], a[124:127], %0" : "+v"(a1[1][0]) :  : ); SB();
  asm volatile("ds_read_b128 a[240:243], %0 offset:32768" :  : "v"(aA[1]) : "memory"); SB();
  asm volatile("v_mfma_f32_16x16x32_bf16 %0, v[184:187], %1, %0" : "+v"(c1[0]) : "v"(ones) : ); SB();
  asm volatile("buffer_load_dwordx4 %0, %1, %2 offen offset:1024 lds" :  : "v"(voS), "s"(rA), "s"(ao[5]) : "memory"); SB();
  asm volatile("s_waitcnt lgkmcnt(8)" :  :  : "memory"); SB();
  asm volatile("v_mfma_f32_16x16x32_bf16 %0, v[188:191], a[120:123], %0" : "+v"(a1[0][1]) :  : ); SB();
  asm volatile("ds_read_b64_tr_b16 v[172:173], %0 offset:30720" :  : "v"(aW[0][0]) : "memory"); SB();
  asm volatile("v_mfma_f32_16x16x32_bf16 %0, v[188:191], a[124:127], %0" : "+v"(a1[1][1]) :  : ); SB();
  asm volatile("ds_read_b64_tr_b16 v[174:175], %0 offset:31488" :  : "v"(aW[0][0]) : "memory"); SB();
  asm volatile("v_mfma_f32_16x16x32_bf16 %0, v[188:191], %1, %0" : "+v"(c1[1]) : "v"(ones) : ); SB();
  asm volatile("ds_read_b64_tr_b16 v[176:177], %0 offset:30720" :  : "v"(aW[0][1]) : "memory"); SB();
  asm volatile("v_mfma_f32_16x16x32_bf16 %0, v[192:195], a[120:123], %0" : "+v"(a1[0][2]) :  : ); SB();
  asm volatile("ds_read_b64_tr_b16 v[178:179], %0 offset:31488" :  : "v"(aW[0][1]) : "memory"); SB();
  asm volatile("s_waitcnt vmcnt(11)" :  :  : "memory"); SB();
  asm volatile("v_mfma_f32_16x16x32_bf16 %0, v[192:195], a[124:127], %0" : "+v"(a1[1][2]) :  : ); SB();
  asm volatile("ds_read_b64_tr_b16 v[180:181], %0 offset:30720" :  : "v"(aW[0][2]) : "memory"); SB();
  asm volatile("ds_read_b128 a[228:231], %0 offset:34816" :  : "v"(aA[0]) : "memory"); SB();
  asm volatile("v_mfma_f32_16x16x32_bf16 %0, v[192:195], %1, %0" : "+v"(c1[2]) : "v"(ones) : ); SB();
  asm volatile("ds_read_b64_tr_b16 v[182:183], %0 offset:31488" :  : "v"(aW[0][2]) : "memory"); SB();
  asm volatile("ds_read_b128 a[244:247], %0 offset:34816" :  : "v"(aA[1]) : "memory"); SB();
  asm volatile("s_waitcnt lgkmcnt(10)\n\tv_mfma_f32_16x16x32_bf16 %0, v[160:163], a[128:131], %0" : "+v"(a1[0][0]) :  : ); SB();
  asm volatile("buffer_load_dwordx4 %0, %1, %2 offen offset:2048 lds" :  : "v"(voS), "s"(rA), "s"(ao[6]) : "memory"); SB();
  asm volatile("v_mfma_f32_16x16x32_bf16 %0, v[160:163], a[132:135], %0" : "+v"(a1[1][0]) :  : ); SB();
  asm volatile("buffer_load_dwordx4 %0, %1, %2 offen offset:3072 lds" :  : "v"(voS), "s"(rA), "s"(ao[7]) : "memory"); SB();
  asm volatile("s_waitcnt lgkmcnt(8)" :  :  : "memory"); SB();
  asm volatile("v_mfma_f32_16x16x32_bf16 %0, v[160:163], %1, %0" : "+v"(c1[0]) : "v"(ones) : ); SB();
  asm volatile("ds_write_b128 %0, %1 offset:25856" :  : "v"(ha), "v"(im[4]) : "memory"); SB();
  asm volatile("v_mfma_f32_16x16x32_bf16 %0, v[164:167], a[128:131], %0" : "+v"(a1[0][1]) :  : ); SB();
  asm volatile("ds_read_b64_tr_b16 v[184:185], %0 offset:36864" :  : "v"(aW[0][0]) : "memory"); SB();
  asm volatile("buffer_load_dwordx2 %0, %1, %2, %3 offen sc1" : "=&v"(fk) : "v"(vFZ), "s"(rWS), "s"(8192u) : "memory"); SB();
  asm volatile("v_mfma_f32_16x16x32_bf16 %0, v[164:167], a[132:135], %0" : "+v"(a1[1][1]) :  : ); SB();
  asm volatile("ds_read_b64_tr_b16 v[186:187], %0 offset:37632" :  : "v"(aW[0][0]) : "memory"); SB();
  asm volatile("s_waitcnt vmcnt(12)" :  :  : "memory"); SB();
  asm volatile("v_mfma_f32_16x16x32_bf16 %0, v[164:167], %1, %0" : "+v"(c1[1]) : "v"(ones) : ); SB();
  asm volatile("ds_read_b64_tr_b16 v[188:189], %0 offset:36864" :  : "v"(aW[0][1]) : "memory"); SB();
  asm volatile("ds_read_b128 a[232:235], %0 offset:36864" :  : "v"(aA[0]) : "memory"); SB();
  asm volatile("v_mfma_f32_16x16x32_bf16 %0, v[168:171], a[128:131], %0" : "+v"(a1[0][2]) :  : ); SB();
  asm volatile("ds_read_b64_tr_b16 v[190:191], %0 offset:37632" :  : "v"(aW[0][1]) : "memory"); SB();
  asm volatile("ds_read_b128 a[248:251], %0 offset:36864" :  : "v"(aA[1]) : "memory"); SB();
  asm volatile("v_mfma_f32_16x16x32_bf16 %0, v[168:171], a[132:135], %0" : "+v"(a1[1][2]) :  : ); SB();
  asm volatile("ds_read_b64_tr_b16 v[192:193], %0 offset:36864" :  : "v"(aW[0][2]) : "memory"); SB();
  asm volatile("v_xor_b32 %0, 896, %0" : "+v"(voS) :  : "memory"); SB();
  asm volatile("v_mfma_f32_16x16x32_bf16 %0, v[168:171], %1, %0" : "+v"(c1[2]) : "v"(ones) : ); SB();
  asm volatile("ds_read_b64_tr_b16 v[194:195], %0 offset:37632" :  : "v"(aW[0][2]) : "memory"); SB();
  asm volatile("s_add_u32 m0, %0, 131072" :  : "s"(mA) : "memory", "scc"); SB();
  asm volatile("s_waitcnt lgkmcnt(9)\n\tv_mfma_f32_16x16x32_bf16 %0, v[172:175], a[136:139], %0" : "+v"(a1[0][0]) :  : ); SB();
  asm volatile("buffer_load_dwordx4 %0, %1, %2 offen lds" :  : "v"(voS), "s"(rA), "s"(ao[0]) : "memory"); SB();
  asm volatile("v_mfma_f32_16x16x32_bf16 %0, v[172:175], a[140:143], %0" : "+v"(a1[1][0]) :  : ); SB();
  asm volatile("buffer_load_dwordx4 %0, %1, %2 offen offset:1024 lds" :  : "v"(voS), "s"(rA), "s"(ao[1]) : "memory"); SB();
  asm volatile("v_mfma_f32_16x16x32_bf16 %0, v[172:175], %1, %0" : "+v"(c1[0]) : "v"(ones) : ); SB();
  asm volatile("ds_write_b128 %0, %1 offset:30016" :  : "v"(ha), "v"(im[5]) : "memory"); SB();
  asm volatile("v_mfma_f32_16x16x32_bf16 %0, v[176:179], a[136:139], %0" : "+v"(a1[0][1]) :  : ); SB();
  asm volatile("ds_read_b64_tr_b16 v[160:161], %0 offset:43008" :  : "v"(aW[0][0]) : "memory"); SB();
  asm volatile("v_mfma_f32_16x16x32_bf16 %0, v[176:179], a[140:143], %0" : "+v"(a1[1][1]) :  : ); SB();
  asm volatile("ds_read_b64_tr_b16 v[162:163], %0 offset:43776" :  : "v"(aW[0][0]) : "memory"); SB();
  asm volatile("s_waitcnt vmcnt(11)" :  :  : "memory"); SB();
  asm volatile("v_mfma_f32_16x16x32_bf16 %0, v[176:179], %1, %0" : "+v"(c1[1]) : "v"(ones) : ); SB();
  asm volatile("ds_read_b64_tr_b16 v[164:165], %0 offset:43008" :  : "v"(aW[0][1]) : "memory"); SB();
  asm volatile("ds_read_b128 a[236:239], %0 offset:38912" :  : "v"(aA[0]) : "memory"); SB();
  asm volatile("v_mfma_f32_16x16x32_bf16 %0, v[180:183], a[136:139], %0" : "+v"(a1[0][2]) :  : ); SB();
  asm volatile("ds_read_b64_tr_b16 v[166:167], %0 offset:43776" :  : "v"(aW[0][1]) : "memory"); SB();
  asm volatile("ds_read_b128 a[252:255], %0 offset:38912" :  : "v"(aA[1]) : "memory"); SB();
  asm volatile("v_mfma_f32_16x16x32_bf16 %0, v[180:183], a[140:143], %0" : "+v"(a1[1][2]) :  : ); SB();
  asm volatile("ds_read_b64_tr_b16 v[168:169], %0 offset:43008" :  : "v"(aW[0][2]) : "memory"); SB();
  asm volatile("buffer_load_dwordx4 %0, %1, %2 offen offset:2048 lds" :  : "v"(voS), "s"(rA), "s"(ao[2]) : "memory"); SB();
  asm volatile("v_mfma_f32_16x16x32_bf16 %0, v[180:183], %1, %0" : "+v"(c1[2]) : "v"(ones) : ); SB();
  asm volatile("ds_read_b64_tr_b16 v[170:171], %0 offset:43776" :  : "v"(aW[0][2]) : "memory"); SB();
  asm volatile("buffer_load_dwordx4 %0, %1, %2 offen offset:3072 lds" :  : "v"(voS), "s"(rA), "s"(ao[3]) : "memory"); SB();
  asm volatile("s_waitcnt lgkmcnt(9)\n\tv_mfma_f32_16x16x32_bf16 %0, v[184:187], a[144:147], %0" : "+v"(a1[0][0]) :  : ); SB();
  asm volatile("ds_write_b128 %0, %1 offset:0" :  : "v"(ha), "v"(im[0]) : "memory"); SB();
  asm volatile("v_mfma_f32_16x16x32_bf16 %0, v[184:187], a[148:151], %0" : "+v"(a1[1][0]) :  : ); SB();
  asm volatile("s_waitcnt vmcnt(11)" :  :  : "memory"); SB();
  asm volatile("ds_read_b128 a[0:3], %0 offset:0" :  : "v"(aA[0]) : "memory"); SB();
  asm volatile("v_mfma_f32_16x16x32_bf16 %0, v[184:187], %1, %0" : "+v"(c1[0]) : "v"(ones) : ); SB();
  asm volatile("ds_read_b128 a[16:19], %0 offset:0" :  : "v"(aA[1]) : "memory"); SB();
  asm volatile("s_add_u32 m0, %0, 135168" :  : "s"(mA) : "memory", "scc"); SB();
  asm volatile("v_mfma_f32_16x16x32_bf16 %0, v[188:191], a[144:147], %0" : "+v"(a1[0][1]) :  : ); SB();
  asm volatile("buffer_load_dwordx4 %0, %1, %2 offen lds" :  : "v"(voS), "s"(rA), "s"(ao[4]) : "memory"); SB();
  asm volatile("v_mfma_f32_16x16x32_bf16 %0, v[188:191], a[148:151], %0" : "+v"(a1[1][1]) :  : ); SB();
  asm volatile("buffer_load_dwordx4 %0, %1, %2 offen offset:1024 lds" :  : "v"(voS), "s"(rA), "s"(ao[5]) : "memory"); SB();
  asm volatile("s_waitcnt lgkmcnt(5)" :  :  : "memory"); SB();
  asm volatile("v_mfma_f32_16x16x32_bf16 %0, v[188:191], %1, %0" : "+v"(c1[1]) : "v"(ones) : ); SB();
  asm volatile("ds_write_b128 %0, %1 offset:4160" :  : "v"(ha), "v"(im[1]) : "memory"); SB();
  asm volatile("v_mfma_f32_16x16x32_bf16 %0, v[192:195], a[144:147], %0" : "+v"(a1[0][2]) :  : ); SB();
  asm volatile("s_waitcnt vmcnt(11)" :  :  : "memory"); SB();
  asm volatile("ds_read_b128 a[4:7], %0 offset:2048" :  : "v"(aA[0]) : "memory"); SB();
  asm volatile("v_mfma_f32_16x16x32_bf16 %0, v[192:195], a[148:151], %0" : "+v"(a1[1][2]) :  : ); SB();
  asm volatile("ds_read_b128 a[20:23], %0 offset:2048" :  : "v"(aA[1]) : "memory"); SB();
  asm volatile("v_mfma_f32_16x16x32_bf16 %0, v[192:195], %1, %0" : "+v"(c1[2]) : "v"(ones) : ); SB();
  asm volatile("buffer_load_dwordx4 %0, %1, %2 offen offset:2048 lds" :  : "v"(voS), "s"(rA), "s"(ao[6]) : "memory"); SB();
  asm volatile("v_mfma_f32_16x16x32_bf16 %0, v[160:163], a[152:155], %0" : "+v"(a1[0][0]) :  : ); SB();
  asm volatile("ds_write_b128 %0, %1 offset:12928" :  : "v"(ha), "v"(im[2]) : "memory"); SB();
  asm volatile("v_mfma_f32_16x16x32_bf16 %0, v[160:163], a[156:159], %0" : "+v"(a1[1][0]) :  : ); SB();
  asm volatile("ds_write_b128 %0, %1 offset:17088" :  : "v"(ha), "v"(im[3]) : "memory"); SB();
  asm volatile("v_mfma_f32_16x16x32_bf16 %0, v[160:163], %1, %0" : "+v"(c1[0]) : "v"(ones) : ); SB();
  asm volatile("ds_write_b128 %0, %1 offset:0" :  : "v"(za), "v"(zero4) : "memory"); SB();
  asm volatile("v_mfma_f32_16x16x32_bf16 %0, v[164:167], a[152:155], %0" : "+v"(a1[0][1]) :  : ); SB();
  asm volatile("buffer_load_dwordx4 %0, %1, %2 offen offset:3072 lds" :  : "v"(voS), "s"(rA), "s"(ao[7]) : "memory"); SB();
  asm volatile("s_waitcnt lgkmcnt(4)" :  :  : "memory"); SB();
  asm volatile("v_mfma_f32_16x16x32_bf16 %0, v[164:167], a[156:159], %0" : "+v"(a1[1][1]) :  : ); SB();
  asm volatile("v_xor_b32 %0, 128, %0" : "+v"(voS) :  : "memory"); SB();
  asm volatile("s_add_u32 m0, %0, 98304" :  : "s"(mA) : "memory", "scc"); SB();
  asm volatile("v_mfma_f32_16x16x32_bf16 %0, v[164:167], %1, %0" : "+v"(c1[1]) : "v"(ones) : ); SB();
  asm volatile("buffer_load_dwordx4 %0, %1, %2 offen lds" :  : "v"(voS), "s"(rA), "s"(ao[0]) : "memory"); SB();
  asm volatile("v_mfma_f32_16x16x32_bf16 %0, v[168:171], a[152:155], %0" : "+v"(a1[0][2]) :  : ); SB();
  asm volatile("buffer_load_dwordx4 %0, %1, %2 offen offset:1024 lds" :  : "v"(voS), "s"(rA), "s"(ao[1]) : "memory"); SB();
  asm volatile("s_waitcnt lgkmcnt(3)" :  :  : "memory"); SB();
  asm volatile("v_mfma_f32_16x16x32_bf16 %0, v[168:171], a[156:159], %0" : "+v"(a1[1][2]) :  : ); SB();
  asm volatile("buffer_load_dwordx4 %0, %1, %2 offen offset:2048 lds" :  : "v"(voS), "s"(rA), "s"(ao[2]) : "memory"); SB();
  asm volatile("v_mfma_f32_16x16x32_bf16 %0, v[168:171], %1, %0" : "+v"(c1[2]) : "v"(ones) : ); SB();
  ST(11);
  f32x4 ac[8];
  u32x2 cA[2][8][2];
  asm volatile("s_waitcnt vmcnt(14)" :  :  : "memory"); SB();
  asm volatile("ds_read_b128 a[8:11], %0 offset:4096" :  : "v"(aA[0]) : "memory"); SB();
  asm volatile("ds_read_b128 a[24:27], %0 offset:4096" :  : "v"(aA[1]) : "memory"); SB();
  asm volatile("buffer_load_dwordx2 %0, %1, %2, %3 offen sc1" : "=&v"(fz) : "v"(vFZ), "s"(rWS), "s"(0u) : "memory"); SB();
  asm volatile("buffer_load_dwordx4 %0, %1, %2 offen offset:3072 lds" :  : "v"(voS), "s"(rA), "s"(ao[3]) : "memory"); SB();
  asm volatile("s_waitcnt vmcnt(14)" :  :  : "memory"); SB();
  asm volatile("ds_read_b128 a[12:15], %0 offset:6144" :  : "v"(aA[0]) : "memory"); SB();
  asm volatile("s_waitcnt vmcnt(13)\n\t.Lc_%=:\n\tv_cmp_ne_u64 vcc, %2, %1\n\ts_cbranch_vccnz .Lr_%=\n\t.Ld_%=:\n\t.subsection 1\n\t.Lr_%=:\n\tv_add_u32 %0, -1, %0\n\ts_nop 1\n\tv_readfirstlane_b32 vcc_hi, %0\n\ts_nop 3\n\ts_cmp_lt_i32 vcc_hi, 1\n\ts_cbranch_scc1 .Ld_%=\n\ts_sleep 2\n\ts_mov_b32 vcc_lo, 8192\n\tbuffer_load_dwordx2 %1, %3, %4, vcc_lo offen sc1\n\ts_waitcnt vmcnt(0)\n\ts_branch .Lc_%=\n\t.subsection 0" : "+v"(tmo), "+v"(fk) : "s"(tg), "v"(vFZ), "s"(rWS) : "memory", "vcc", "scc"); SB();
  asm volatile("s_add_u32 m0, %0, 60032" :  : "s"(mU) : "memory", "scc"); SB();
  asm volatile("s_nop 0\n\tbuffer_load_dwordx4 %0, %1, %2 offen sc1 lds" :  : "v"(vF), "s"(rWS), "s"(W_KV) : "memory"); SB();
  asm volatile("s_add_u32 m0, %0, 72960" :  : "s"(mU) : "memory", "scc"); SB();
  asm volatile("s_nop 0\n\tbuffer_load_dwordx4 %0, %1, %2 offen sc1 lds" :  : "v"(vF), "s"(rWS), "s"(W_KV + 4096) : "memory"); SB();
  asm volatile("s_add_u32 m0, %0, 85888" :  : "s"(mU) : "memory", "scc"); SB();
  asm volatile("s_nop 0\n\tbuffer_load_dwordx4 %0, %1, %2 offen sc1 lds" :  : "v"(vF), "s"(rWS), "s"(uso) : "memory"); SB();
  asm volatile("ds_read_b128 a[28:31], %0 offset:6144" :  : "v"(aA[1]) : "memory"); SB();
  asm volatile("s_waitcnt lgkmcnt(2)" :  :  : "memory"); SB();
  asm volatile("s_waitcnt vmcnt(14)" :  :  : "memory"); SB();
  asm volatile("ds_read_b128 a[32:35], %0 offset:32768" :  : "v"(aA[0]) : "memory"); SB();
  asm volatile("ds_read_b128 a[48:51], %0 offset:32768" :  : "v"(aA[1]) : "memory"); SB();
  asm volatile("s_add_u32 m0, %0, 102400" :  : "s"(mA) : "memory", "scc"); SB();
  asm volatile("s_nop 0\n\tbuffer_load_dwordx4 %0, %1, %2 offen lds" :  : "v"(voS), "s"(rA), "s"(ao[4]) : "memory"); SB();
  asm volatile("buffer_load_dwordx4 %0, %1, %2 offen offset:1024 lds" :  : "v"(voS), "s"(rA), "s"(ao[5]) : "memory"); SB();
  asm volatile("s_waitcnt lgkmcnt(2)" :  :  : "memory"); SB();
  asm volatile("s_waitcnt vmcnt(14)" :  :  : "memory"); SB();
  asm volatile("ds_read_b128 a[36:39], %0 offset:34816" :  : "v"(aA[0]) : "memory"); SB();
  asm volatile("ds_read_b128 a[52:55], %0 offset:34816" :  : "v"(aA[1]) : "memory"); SB();
  asm volatile("buffer_load_dwordx4 %0, %1, %2 offen offset:2048 lds" :  : "v"(voS), "s"(rA), "s"(ao[6]) : "memory"); SB();
  asm volatile("buffer_load_dwordx4 %0, %1, %2 offen offset:3072 lds" :  : "v"(voS), "s"(rA), "s"(ao[7]) : "memory"); SB();
  asm volatile("s_waitcnt lgkmcnt(2)" :  :  : "memory"); SB();
  asm volatile("v_xor_b32 %0, 384, %0" : "+v"(voS) :  : "memory"); SB();
  asm volatile("s_waitcnt vmcnt(14)" :  :  : "memory"); SB();
  asm volatile("ds_read_b128 a[40:43], %0 offset:36864" :  : "v"(aA[0]) : "memory"); SB();
  asm volatile("ds_read_b128 a[56:59], %0 offset:36864" :  : "v"(aA[1]) : "memory"); SB();
  asm volatile("s_add_u32 m0, %0, 131072" :  : "s"(mA) : "memory", "scc"); SB();
  asm volatile("s_nop 0\n\tbuffer_load_dwordx4 %0, %1, %2 offen lds" :  : "v"(voS), "s"(rA), "s"(ao[0]) : "memory"); SB();
  asm volatile("buffer_load_dwordx4 %0, %1, %2 offen offset:1024 lds" :  : "v"(voS), "s"(rA), "s"(ao[1]) : "memory"); SB();
  asm volatile("s_waitcnt lgkmcnt(2)" :  :  : "memory"); SB();
  asm volatile("s_waitcnt vmcnt(14)" :  :  : "memory"); SB();
  asm volatile("ds_read_b128 a[44:47], %0 offset:38912" :  : "v"(aA[0]) : "memory"); SB();
  asm volatile("ds_read_b128 a[60:63], %0 offset:38912" :  : "v"(aA[1]) : "memory"); SB();
  asm volatile("buffer_load_dwordx4 %0, %1, %2 offen offset:2048 lds" :  : "v"(voS), "s"(rA), "s"(ao[2]) : "memory"); SB();
  asm volatile("buffer_load_dwordx4 %0, %1, %2 offen offset:3072 lds" :  : "v"(voS), "s"(rA), "s"(ao[3]) : "memory"); SB();
  asm volatile("s_waitcnt lgkmcnt(2)" :  :  : "memory"); SB();
  asm volatile("s_waitcnt vmcnt(14)" :  :  : "memory"); SB();
  asm volatile("ds_read_b128 a[64:67], %0 offset:0" :  : "v"(aA[0]) : "memory"); SB();
  asm volatile("ds_read_b128 a[80:83], %0 offset:0" :  : "v"(aA[1]) : "memory"); SB();
  asm volatile("s_add_u32 m0, %0, 135168" :  : "s"(mA) : "memory", "scc"); SB();
  asm volatile("s_nop 0\n\tbuffer_load_dwordx4 %0, %1, %2 offen lds" :  : "v"(voS), "s"(rA), "s"(ao[4]) : "memory"); SB();
  asm volatile("buffer_load_dwordx4 %0, %1, %2 offen offset:1024 lds" :  : "v"(voS), "s"(rA), "s"(ao[5]) : "memory"); SB();
  asm volatile("s_waitcnt lgkmcnt(2)" :  :  : "memory"); SB();
  asm volatile("s_waitcnt vmcnt(14)\n\t.Lc_%=:\n\tv_cmp_ne_u64 vcc, %2, %1\n\ts_cbranch_vccnz .Lr_%=\n\t.Ld_%=:\n\t.subsection 1\n\t.Lr_%=:\n\tv_add_u32 %0, -1, %0\n\ts_nop 1\n\tv_readfirstlane_b32 vcc_hi, %0\n\ts_nop 3\n\ts_cmp_lt_i32 vcc_hi, 1\n\ts_cbranch_scc1 .Ld_%=\n\ts_sleep 2\n\ts_mov_b32 vcc_lo, 0\n\tbuffer_load_dwordx2 %1, %3, %4, vcc_lo offen sc1\n\ts_waitcnt vmcnt(0)\n\ts_branch .Lc_%=\n\t.subsection 0" : "+v"(tmo), "+v"(fz) : "s"(tg), "v"(vFZ), "s"(rWS) : "memory", "vcc", "scc"); SB();
  asm volatile("s_waitcnt vmcnt(13)" :  :  : "memory"); SB();
  asm volatile("ds_read_b128 a[68:71], %0 offset:2048" :  : "v"(aA[0]) : "memory"); SB();
  asm volatile("ds_read_b128 a[84:87], %0 offset:2048" :  : "v"(aA[1]) : "memory"); SB();
  asm volatile("buffer_load_dwordx4 v[160:163], %0, %1, %2 offen offset:0 sc1" :  : "v"(vB), "s"(rWS), "s"(0u) : "memory"); SB();
  asm volatile("buffer_load_dwordx4 v[164:167], %0, %1, %2 offen offset:1024 sc1" :  : "v"(vB), "s"(rWS), "s"(0u) : "memory"); SB();
  asm volatile("s_waitcnt vmcnt(12) lgkmcnt(0)\n\ts_barrier" :  :  : "memory"); SB();
  ST(14);
  asm volatile("ds_read2_b32 %0, %1 offset0:176 offset1:177" : "=&v"(cA[0][0][0]) : "v"(aa[0]) : "memory"); SB();
  asm volatile("ds_read2_b32 %0, %1 offset0:178 offset1:179" : "=&v"(cA[0][0][1]) : "v"(aa[0]) : "memory"); SB();
  asm volatile("ds_read_b128 a[124:127], %0 offset:3776" :  : "v"(bb) : "memory"); SB();
  asm volatile("ds_read_b128 a[120:123], %0 offset:3264" :  : "v"(bb) : "memory"); SB();
  asm volatile("ds_read_b128 a[116:119], %0 offset:2752" :  : "v"(bb) : "memory"); SB();
  asm volatile("ds_read_b128 a[112:115], %0 offset:2240" :  : "v"(bb) : "memory"); SB();
  asm volatile("ds_read2_b32 %0, %1 offset0:48 offset1:49" : "=&v"(cA[0][1][0]) : "v"(aa[0]) : "memory"); SB();
  asm volatile("ds_read2_b32 %0, %1 offset0:50 offset1:51" : "=&v"(cA[0][1][1]) : "v"(aa[0]) : "memory"); SB();
  asm volatile("ds_read_b128 a[108:111], %0 offset:1728" :  : "v"(bb) : "memory"); SB();
  asm volatile("ds_read2_b32 %0, %1 offset0:176 offset1:177" : "=&v"(cA[0][2][0]) : "v"(aa[1]) : "memory"); SB();
  asm volatile("ds_read2_b32 %0, %1 offset0:178 offset1:179" : "=&v"(cA[0][2][1]) : "v"(aa[1]) : "memory"); SB();
  asm volatile("ds_read2_b32 %0, %1 offset0:48 offset1:49" : "=&v"(cA[0][3][0]) : "v"(aa[1]) : "memory"); SB();
  asm volatile("ds_read2_b32 %0, %1 offset0:50 offset1:51" : "=&v"(cA[0][3][1]) : "v"(aa[1]) : "memory"); SB();
  asm volatile("ds_read_b128 a[104:107], %0 offset:1216" :  : "v"(bb) : "memory"); SB();
  asm volatile("ds_read2_b32 %0, %1 offset0:176 offset1:177" : "=&v"(cA[0][4][0]) : "v"(aa[2]) : "memory"); SB();
  asm volatile("ds_read2_b32 %0, %1 offset0:178 offset1:179" : "=&v"(cA[0][4][1]) : "v"(aa[2]) : "memory"); SB();
  asm volatile("ds_read2_b32 %0, %1 offset0:48 offset1:49" : "=&v"(cA[0][5][0]) : "v"(aa[2]) : "memory"); SB();
  asm volatile("ds_read2_b32 %0, %1 offset0:50 offset1:51" : "=&v"(cA[0][5][1]) : "v"(aa[2]) : "memory"); SB();
  asm volatile("ds_read_b128 a[100:103], %0 offset:704" :  : "v"(bb) : "memory"); SB();
  asm volatile("ds_read2_b32 %0, %1 offset0:176 offset1:177" : "=&v"(cA[0][6][0]) : "v"(aa[3]) : "memory"); SB();
  asm volatile("ds_read2_b32 %0, %1 offset0:178 offset1:179" : "=&v"(cA[0][6][1]) : "v"(aa[3]) : "memory"); SB();
  asm volatile("ds_read2_b32 %0, %1 offset0:48 offset1:49" : "=&v"(cA[0][7][0]) : "v"(aa[3]) : "memory"); SB();
  asm volatile("ds_read2_b32 %0, %1 offset0:50 offset1:51" : "=&v"(cA[0][7][1]) : "v"(aa[3]) : "memory"); SB();
  asm volatile("ds_read_b128 a[96:99], %0 offset:192" :  : "v"(bb) : "memory"); SB();
  asm volatile("" : "+v"(a1[0][0]), "+v"(a1[0][1]), "+v"(a1[0][2]), "+v"(a1[1][0]), "+v"(a1[1][1]), "+v"(a1[1][2]), "+v"(c1[0]), "+v"(c1[1]), "+v"(c1[2]) :  : ); SB();
#pragma unroll
  for (int n = 0; n < 6; ++n) { a1[n / 3][n % 3] = __builtin_elementwise_fma((f32x4){-xm[n / 3], -xm[n / 3], -xm[n / 3], -xm[n / 3]}, c1[n % 3], a1[n / 3][n % 3]); asm volatile("" : "+v"(a1[n / 3][n % 3])); }
  SB();
  asm volatile("s_waitcnt vmcnt(10)" :  :  : "memory"); SB();
  asm volatile("ds_read_b128 a[72:75], %0 offset:4096" :  : "v"(aA[0]) : "memory"); SB();
  asm volatile("ds_read_b128 a[88:91], %0 offset:4096" :  : "v"(aA[1]) : "memory"); SB();
  asm volatile("s_waitcnt vmcnt(8)" :  :  : "memory"); SB();
  asm volatile("ds_read_b128 a[76:79], %0 offset:6144" :  : "v"(aA[0]) : "memory"); SB();
  asm volatile("ds_read_b128 a[92:95], %0 offset:6144" :  : "v"(aA[1]) : "memory"); SB();
  asm volatile("buffer_load_dwordx4 v[168:171], %0, %1, %2 offen offset:0 sc1" :  : "v"(vB), "s"(rWS), "s"(65536u) : "memory"); SB();
  asm volatile("buffer_load_dwordx4 v[172:175], %0, %1, %2 offen offset:1024 sc1" :  : "v"(vB), "s"(rWS), "s"(65536u) : "memory"); SB();
  asm volatile("v_xor_b32 %0, 131072, %0" : "+v"(vB) :  : "memory"); SB();
  asm volatile("buffer_load_dwordx4 v[176:179], %0, %1, %2 offen offset:0 sc1" :  : "v"(vB), "s"(rWS), "s"(0u) : "memory"); SB();
  if (nch == 3) {
  asm volatile("s_waitcnt lgkmcnt(4)\n\tv_mfma_f32_16x16x32_bf16 %0, %1, a[124:127], 0" : "=&v"(ac[7]) : "v"(((u32x4){cA[0][0][0][0], cA[0][0][0][1], cA[0][0][1][0], cA[0][0][1][1]})) : ); SB();
  asm volatile("ds_read2_b32 %0, %1 offset0:160 offset1:161" : "=&v"(cA[1][0][0]) : "v"(aa[0]) : "memory"); SB();
  asm volatile("buffer_load_dwordx4 v[180:183], %0, %1, %2 offen offset:1024 sc1" :  : "v"(vB), "s"(rWS), "s"(0u) : "memory"); SB();
  asm volatile("v_mfma_f32_16x16x32_bf16 %0, %1, a[120:123], 0" : "=&v"(ac[6]) : "v"(((u32x4){cA[0][0][0][0], cA[0][0][0][1], cA[0][0][1][0], cA[0][0][1][1]})) : ); SB();
  asm volatile("ds_read2_b32 %0, %1 offset0:162 offset1:163" : "=&v"(cA[1][0][1]) : "v"(aa[0]) : "memory"); SB();
  asm volatile("v_mfma_f32_16x16x32_bf16 %0, %1, a[116:119], 0" : "=&v"(ac[5]) : "v"(((u32x4){cA[0][0][0][0], cA[0][0][0][1], cA[0][0][1][0], cA[0][0][1][1]})) : ); SB();
  asm volatile("ds_read_b128 a[156:159], %0 offset:3712" :  : "v"(bb) : "memory"); SB();
  asm volatile("v_mfma_f32_16x16x32_bf16 %0, %1, a[112:115], 0" : "=&v"(ac[4]) : "v"(((u32x4){cA[0][0][0][0], cA[0][0][0][1], cA[0][0][1][0], cA[0][0][1][1]})) : ); SB();
  asm volatile("ds_write_b128 %0, %1 offset:0" :  : "v"(ew[0]), "v"(a1[0][0]) : "memory"); SB();
  asm volatile("v_mfma_f32_16x16x32_bf16 %0, %1, a[120:123], %0" : "+v"(ac[7]) : "v"(((u32x4){cA[0][1][0][0], cA[0][1][0][1], cA[0][1][1][0], cA[0][1][1][1]})) : ); SB();
  asm volatile("ds_read_b128 a[152:155], %0 offset:3200" :  : "v"(bb) : "memory"); SB();
  asm volatile("buffer_load_dwordx4 v[184:187], %0, %1, %2 offen offset:0 sc1" :  : "v"(vB), "s"(rWS), "s"(65536u) : "memory"); SB();
  asm volatile("v_mfma_f32_16x16x32_bf16 %0, %1, a[116:119], %0" : "+v"(ac[6]) : "v"(((u32x4){cA[0][1][0][0], cA[0][1][0][1], cA[0][1][1][0], cA[0][1][1][1]})) : ); SB();
  asm volatile("ds_read_b128 a[128:131], %0 offset:128" :  : "v"(bb) : "memory"); SB();
  asm volatile("buffer_load_dwordx4 v[188:191], %0, %1, %2 offen offset:1024 sc1" :  : "v"(vB), "s"(rWS), "s"(65536u) : "memory"); SB();
  asm volatile("v_mfma_f32_16x16x32_bf16 %0, %1, a[112:115], %0" : "+v"(ac[5]) : "v"(((u32x4){cA[0][1][0][0], cA[0][1][0][1], cA[0][1][1][0], cA[0][1][1][1]})) : ); SB();
  asm volatile("ds_read_b128 a[132:135], %0 offset:640" :  : "v"(bb) : "memory"); SB();
  asm volatile("v_mfma_f32_16x16x32_bf16 %0, %1, a[108:111], %0" : "+v"(ac[4]) : "v"(((u32x4){cA[0][1][0][0], cA[0][1][0][1], cA[0][1][1][0], cA[0][1][1][1]})) : ); SB();
  asm volatile("v_xor_b32 %0, 393216, %0" : "+v"(vB) :  : "memory"); SB();
  asm volatile("v_mfma_f32_16x16x32_bf16 %0, %1, a[116:119], %0" : "+v"(ac[7]) : "v"(((u32x4){cA[0][2][0][0], cA[0][2][0][1], cA[0][2][1][0], cA[0][2][1][1]})) : ); SB();
  asm volatile("ds_read2_b32 %0, %1 offset0:32 offset1:33" : "=&v"(cA[1][1][0]) : "v"(aa[0]) : "memory"); SB();
  asm volatile("buffer_load_dwordx4 v[192:195], %0, %1, %2 offen offset:0 sc1" :  : "v"(vB), "s"(rWS), "s"(0u) : "memory"); SB();
  asm volatile("v_mfma_f32_16x16x32_bf16 %0, %1, a[112:115], %0" : "+v"(ac[6]) : "v"(((u32x4){cA[0][2][0][0], cA[0][2][0][1], cA[0][2][1][0], cA[0][2][1][1]})) : ); SB();
  asm volatile("ds_read2_b32 %0, %1 offset0:34 offset1:35" : "=&v"(cA[1][1][1]) : "v"(aa[0]) : "memory"); SB();
  asm volatile("v_mfma_f32_16x16x32_bf16 %0, %1, a[108:111], %0" : "+v"(ac[5]) : "v"(((u32x4){cA[0][2][0][0], cA[0][2][0][1], cA[0][2][1][0], cA[0][2][1][1]})) : ); SB();
  asm volatile("ds_read_b128 a[148:151], %0 offset:2688" :  : "v"(bb) : "memory"); SB();
  asm volatile("v_mfma_f32_16x16x32_bf16 %0, %1, a[108:111], 0" : "=&v"(ac[3]) : "v"(((u32x4){cA[0][0][0][0], cA[0][0][0][1], cA[0][0][1][0], cA[0][0][1][1]})) : ); SB();
  asm volatile("ds_write_b128 %0, %1 offset:1024" :  : "v"(ew[0]), "v"(a1[0][1]) : "memory"); SB();
  asm volatile("v_mfma_f32_16x16x32_bf16 %0, %1, a[112:115], %0" : "+v"(ac[7]) : "v"(((u32x4){cA[0][3][0][0], cA[0][3][0][1], cA[0][3][1][0], cA[0][3][1][1]})) : ); SB();
  asm volatile("ds_read_b128 a[136:139], %0 offset:1152" :  : "v"(bb) : "memory"); SB();
  asm volatile("buffer_load_dwordx4 v[196:199], %0, %1, %2 offen offset:1024 sc1" :  : "v"(vB), "s"(rWS), "s"(0u) : "memory"); SB();
  asm volatile("v_mfma_f32_16x16x32_bf16 %0, %1, a[108:111], %0" : "+v"(ac[6]) : "v"(((u32x4){cA[0][3][0][0], cA[0][3][0][1], cA[0][3][1][0], cA[0][3][1][1]})) : ); SB();
  asm volatile("ds_read2_b32 %0, %1 offset0:160 offset1:161" : "=&v"(cA[1][2][0]) : "v"(aa[1]) : "memory"); SB();
  asm volatile("v_mfma_f32_16x16x32_bf16 %0, %1, a[104:107], %0" : "+v"(ac[5]) : "v"(((u32x4){cA[0][3][0][0], cA[0][3][0][1], cA[0][3][1][0], cA[0][3][1][1]})) : ); SB();
  asm volatile("ds_read2_b32 %0, %1 offset0:162 offset1:163" : "=&v"(cA[1][2][1]) : "v"(aa[1]) : "memory"); SB();
  asm volatile("buffer_load_dwordx4 v[200:203], %0, %1, %2 offen offset:0 sc1" :  : "v"(vB), "s"(rWS), "s"(65536u) : "memory"); SB();
  asm volatile("v_mfma_f32_16x16x32_bf16 %0, %1, a[104:107], %0" : "+v"(ac[4]) : "v"(((u32x4){cA[0][2][0][0], cA[0][2][0][1], cA[0][2][1][0], cA[0][2][1][1]})) : ); SB();
  asm volatile("ds_write_b128 %0, %1 offset:2048" :  : "v"(ew[0]), "v"(a1[0][2]) : "memory"); SB();
  asm volatile("v_mfma_f32_16x16x32_bf16 %0, %1, a[108:111], %0" : "+v"(ac[7]) : "v"(((u32x4){cA[0][4][0][0], cA[0][4][0][1], cA[0][4][1][0], cA[0][4][1][1]})) : ); SB();
  asm volatile("ds_read_b128 a[140:143], %0 offset:1664" :  : "v"(bb) : "memory"); SB();
  asm volatile("buffer_load_dwordx4 v[204:207], %0, %1, %2 offen offset:1024 sc1" :  : "v"(vB), "s"(rWS), "s"(65536u) : "memory"); SB();
  asm volatile("v_mfma_f32_16x16x32_bf16 %0, %1, a[104:107], %0" : "+v"(ac[6]) : "v"(((u32x4){cA[0][4][0][0], cA[0][4][0][1], cA[0][4][1][0], cA[0][4][1][1]})) : ); SB();
  asm volatile("ds_read_b128 a[144:147], %0 offset:2176" :  : "v"(bb) : "memory"); SB();
  asm volatile("v_mfma_f32_16x16x32_bf16 %0, %1, a[104:107], %0" : "+v"(ac[3]) : "v"(((u32x4){cA[0][1][0][0], cA[0][1][0][1], cA[0][1][1][0], cA[0][1][1][1]})) : ); SB();
  asm volatile("ds_read2_b32 %0, %1 offset0:32 offset1:33" : "=&v"(cA[1][3][0]) : "v"(aa[1]) : "memory"); SB();
  asm volatile("v_mfma_f32_16x16x32_bf16 %0, %1, a[104:107], 0" : "=&v"(ac[2]) : "v"(((u32x4){cA[0][0][0][0], cA[0][0][0][1], cA[0][0][1][0], cA[0][0][1][1]})) : ); SB();
  asm volatile("v_xor_b32 %0, 131072, %0" : "+v"(vB) :  : "memory"); SB();
  asm volatile("v_mfma_f32_16x16x32_bf16 %0, %1, a[104:107], %0" : "+v"(ac[7]) : "v"(((u32x4){cA[0][5][0][0], cA[0][5][0][1], cA[0][5][1][0], cA[0][5][1][1]})) : ); SB();
  asm volatile("ds_read2_b32 %0, %1 offset0:34 offset1:35" : "=&v"(cA[1][3][1]) : "v"(aa[1]) : "memory"); SB();
  asm volatile("buffer_load_dwordx4 v[208:211], %0, %1, %2 offen offset:0 sc1" :  : "v"(vB), "s"(rWS), "s"(0u) : "memory"); SB();
  asm volatile("v_mfma_f32_16x16x32_bf16 %0, %1, a[100:103], %0" : "+v"(ac[6]) : "v"(((u32x4){cA[0][5][0][0], cA[0][5][0][1], cA[0][5][1][0], cA[0][5][1][1]})) : ); SB();
  asm volatile("ds_read2_b32 %0, %1 offset0:160 offset1:161" : "=&v"(cA[1][4][0]) : "v"(aa[2]) : "memory"); SB();
  asm volatile("v_mfma_f32_16x16x32_bf16 %0, %1, a[100:103], %0" : "+v"(ac[5]) : "v"(((u32x4){cA[0][4][0][0], cA[0][4][0][1], cA[0][4][1][0], cA[0][4][1][1]})) : ); SB();
  asm volatile("ds_read2_b32 %0, %1 offset0:162 offset1:163" : "=&v"(cA[1][4][1]) : "v"(aa[2]) : "memory"); SB();
  asm volatile("v_mfma_f32_16x16x32_bf16 %0, %1, a[100:103], %0" : "+v"(ac[4]) : "v"(((u32x4){cA[0][3][0][0], cA[0][3][0][1], cA[0][3][1][0], cA[0][3][1][1]})) : ); SB();
  asm volatile("buffer_load_dwordx4 v[212:215], %0, %1, %2 offen offset:1024 sc1" :  : "v"(vB), "s"(rWS), "s"(0u) : "memory"); SB();
  asm volatile("v_mfma_f32_16x16x32_bf16 %0, %1, a[100:103], %0" : "+v"(ac[7]) : "v"(((u32x4){cA[0][6][0][0], cA[0][6][0][1], cA[0][6][1][0], cA[0][6][1][1]})) : ); SB();
  asm volatile("ds_read2_b32 %0, %1 offset0:32 offset1:33" : "=&v"(cA[1][5][0]) : "v"(aa[2]) : "memory"); SB();
  asm volatile("v_mfma_f32_16x16x32_bf16 %0, %1, a[100:103], %0" : "+v"(ac[3]) : "v"(((u32x4){cA[0][2][0][0], cA[0][2][0][1], cA[0][2][1][0], cA[0][2][1][1]})) : ); SB();
  asm volatile("ds_read2_b32 %0, %1 offset0:34 offset1:35" : "=&v"(cA[1][5][1]) : "v"(aa[2]) : "memory"); SB();
  asm volatile("v_mfma_f32_16x16x32_bf16 %0, %1, a[100:103], %0" : "+v"(ac[2]) : "v"(((u32x4){cA[0][1][0][0], cA[0][1][0][1], cA[0][1][1][0], cA[0][1][1][1]})) : ); SB();
  asm volatile("ds_read2_b32 %0, %1 offset0:160 offset1:161" : "=&v"(cA[1][6][0]) : "v"(aa[3]) : "memory"); SB();
  asm volatile("v_mfma_f32_16x16x32_bf16 %0, %1, a[100:103], 0" : "=&v"(ac[1]) : "v"(((u32x4){cA[0][0][0][0], cA[0][0][0][1], cA[0][0][1][0], cA[0][0][1][1]})) : ); SB();
  asm volatile("buffer_load_dwordx4 v[216:219], %0, %1, %2 offen offset:0 sc1" :  : "v"(vB), "s"(rWS), "s"(65536u) : "memory"); SB();
  asm volatile("v_mfma_f32_16x16x32_bf16 %0, %1, a[96:99], %0" : "+v"(ac[7]) : "v"(((u32x4){cA[0][7][0][0], cA[0][7][0][1], cA[0][7][1][0], cA[0][7][1][1]})) : ); SB();
  asm volatile("ds_read2_b32 %0, %1 offset0:162 offset1:163" : "=&v"(cA[1][6][1]) : "v"(aa[3]) : "memory"); SB();
  asm volatile("v_mfma_f32_16x16x32_bf16 %0, %1, a[96:99], %0" : "+v"(ac[6]) : "v"(((u32x4){cA[0][6][0][0], cA[0][6][0][1], cA[0][6][1][0], cA[0][6][1][1]})) : ); SB();
  asm volatile("ds_read2_b32 %0, %1 offset0:32 offset1:33" : "=&v"(cA[1][7][0]) : "v"(aa[3]) : "memory"); SB();
  asm volatile("v_mfma_f32_16x16x32_bf16 %0, %1, a[96:99], %0" : "+v"(ac[5]) : "v"(((u32x4){cA[0][5][0][0], cA[0][5][0][1], cA[0][5][1][0], cA[0][5][1][1]})) : ); SB();
  asm volatile("ds_read2_b32 %0, %1 offset0:34 offset1:35" : "=&v"(cA[1][7][1]) : "v"(aa[3]) : "memory"); SB();
  asm volatile("v_mfma_f32_16x16x32_bf16 %0, %1, a[96:99], %0" : "+v"(ac[4]) : "v"(((u32x4){cA[0][4][0][0], cA[0][4][0][1], cA[0][4][1][0], cA[0][4][1][1]})) : ); SB();
  asm volatile("buffer_load_dwordx4 v[220:223], %0, %1, %2 offen offset:1024 sc1" :  : "v"(vB), "s"(rWS), "s"(65536u) : "memory"); SB();
  asm volatile("v_mfma_f32_16x16x32_bf16 %0, %1, a[96:99], %0" : "+v"(ac[3]) : "v"(((u32x4){cA[0][3][0][0], cA[0][3][0][1], cA[0][3][1][0], cA[0][3][1][1]})) : ); SB();
  asm volatile("v_xor_b32 %0, 917504, %0" : "+v"(vB) :  : "memory"); SB();
  asm volatile("v_mfma_f32_16x16x32_bf16 %0, %1, a[96:99], %0" : "+v"(ac[2]) : "v"(((u32x4){cA[0][2][0][0], cA[0][2][0][1], cA[0][2][1][0], cA[0][2][1][1]})) : ); SB();
  asm volatile("ds_write_b128 %0, %1 offset:0" :  : "v"(ew[1]), "v"(a1[1][0]) : "memory"); SB();
  asm volatile("v_mfma_f32_16x16x32_bf16 %0, %1, a[96:99], %0" : "+v"(ac[1]) : "v"(((u32x4){cA[0][1][0][0], cA[0][1][0][1], cA[0][1][1][0], cA[0][1][1][1]})) : ); SB();
  asm volatile("ds_write_b128 %0, %1 offset:1024" :  : "v"(ew[1]), "v"(a1[1][1]) : "memory"); SB();
  asm volatile("v_mfma_f32_16x16x32_bf16 %0, %1, a[96:99], 0" : "=&v"(ac[0]) : "v"(((u32x4){cA[0][0][0][0], cA[0][0][0][1], cA[0][0][1][0], cA[0][0][1][1]})) : ); SB();
  asm volatile("ds_write_b128 %0, %1 offset:2048" :  : "v"(ew[1]), "v"(a1[1][2]) : "memory"); SB();
  asm volatile("s_waitcnt lgkmcnt(4)\n\tv_mfma_f32_16x16x32_bf16 %0, %1, a[156:159], %0" : "+v"(ac[7]) : "v"(((u32x4){cA[1][0][0][0], cA[1][0][0][1], cA[1][0][1][0], cA[1][0][1][1]})) : ); SB();
  asm volatile("v_mfma_f32_16x16x32_bf16 %0, %1, a[152:155], %0" : "+v"(ac[6]) : "v"(((u32x4){cA[1][0][0][0], cA[1][0][0][1], cA[1][0][1][0], cA[1][0][1][1]})) : ); SB();
  asm volatile("v_mfma_f32_16x16x32_bf16 %0, %1, a[128:131], %0" : "+v"(ac[0]) : "v"(((u32x4){cA[1][0][0][0], cA[1][0][0][1], cA[1][0][1][0], cA[1][0][1][1]})) : ); SB();
  asm volatile("v_mfma_f32_16x16x32_bf16 %0, %1, a[132:135], %0" : "+v"(ac[1]) : "v"(((u32x4){cA[1][0][0][0], cA[1][0][0][1], cA[1][0][1][0], cA[1][0][1][1]})) : ); SB();
  asm volatile("v_mfma_f32_16x16x32_bf16 %0, %1, a[152:155], %0" : "+v"(ac[7]) : "v"(((u32x4){cA[1][1][0][0], cA[1][1][0][1], cA[1][1][1][0], cA[1][1][1][1]})) : ); SB();
  asm volatile("v_mfma_f32_16x16x32_bf16 %0, %1, a[148:151], %0" : "+v"(ac[6]) : "v"(((u32x4){cA[1][1][0][0], cA[1][1][0][1], cA[1][1][1][0], cA[1][1][1][1]})) : ); SB();
  asm volatile("v_mfma_f32_16x16x32_bf16 %0, %1, a[136:139], %0" : "+v"(ac[2]) : "v"(((u32x4){cA[1][0][0][0], cA[1][0][0][1], cA[1][0][1][0], cA[1][0][1][1]})) : ); SB();
  asm volatile("ds_write_b128 %0, %1 offset:0" :  : "v"(dd), "v"(ac[0]) : "memory"); SB();
  asm volatile("v_mfma_f32_16x16x32_bf16 %0, %1, a[128:131], %0" : "+v"(ac[1]) : "v"(((u32x4){cA[1][1][0][0], cA[1][1][0][1], cA[1][1][1][0], cA[1][1][1][1]})) : ); SB();
  asm volatile("buffer_load_dwordx4 v[224:227], %0, %1, %2 offen offset:0 sc1" :  : "v"(vB), "s"(rWS), "s"(0u) : "memory"); SB();
  asm volatile("v_mfma_f32_16x16x32_bf16 %0, %1, a[148:151], %0" : "+v"(ac[7]) : "v"(((u32x4){cA[1][2][0][0], cA[1][2][0][1], cA[1][2][1][0], cA[1][2][1][1]})) : ); SB();
  asm volatile("v_mfma_f32_16x16x32_bf16 %0, %1, a[140:143], %0" : "+v"(ac[3]) : "v"(((u32x4){cA[1][0][0][0], cA[1][0][0][1], cA[1][0][1][0], cA[1][0][1][1]})) : ); SB();
  asm volatile("buffer_load_dwordx4 v[228:231], %0, %1, %2 offen offset:1024 sc1" :  : "v"(vB), "s"(rWS), "s"(0u) : "memory"); SB();
  asm volatile("v_mfma_f32_16x16x32_bf16 %0, %1, a[132:135], %0" : "+v"(ac[2]) : "v"(((u32x4){cA[1][1][0][0], cA[1][1][0][1], cA[1][1][1][0], cA[1][1][1][1]})) : ); SB();
  asm volatile("v_mfma_f32_16x16x32_bf16 %0, %1, a[144:147], %0" : "+v"(ac[4]) : "v"(((u32x4){cA[1][0][0][0], cA[1][0][0][1], cA[1][0][1][0], cA[1][0][1][1]})) : ); SB();
  asm volatile("ds_write_b128 %0, %1 offset:1024" :  : "v"(dd), "v"(ac[1]) : "memory"); SB();
  asm volatile("v_mfma_f32_16x16x32_bf16 %0, %1, a[144:147], %0" : "+v"(ac[7]) : "v"(((u32x4){cA[1][3][0][0], cA[1][3][0][1], cA[1][3][1][0], cA[1][3][1][1]})) : ); SB();
  asm volatile("buffer_load_dwordx4 v[232:235], %0, %1, %2 offen offset:0 sc1" :  : "v"(vB), "s"(rWS), "s"(65536u) : "memory"); SB();
  asm volatile("v_mfma_f32_16x16x32_bf16 %0, %1, a[136:139], %0" : "+v"(ac[3]) : "v"(((u32x4){cA[1][1][0][0], cA[1][1][0][1], cA[1][1][1][0], cA[1][1][1][1]})) : ); SB();
  asm volatile("v_mfma_f32_16x16x32_bf16 %0, %1, a[128:131], %0" : "+v"(ac[2]) : "v"(((u32x4){cA[1][2][0][0], cA[1][2][0][1], cA[1][2][1][0], cA[1][2][1][1]})) : ); SB();
  asm volatile("v_mfma_f32_16x16x32_bf16 %0, %1, a[140:143], %0" : "+v"(ac[4]) : "v"(((u32x4){cA[1][1][0][0], cA[1][1][0][1], cA[1][1][1][0], cA[1][1][1][1]})) : ); SB();
  asm volatile("buffer_load_dwordx4 v[236:239], %0, %1, %2 offen offset:1024 sc1" :  : "v"(vB), "s"(rWS), "s"(65536u) : "memory"); SB();
  asm volatile("v_mfma_f32_16x16x32_bf16 %0, %1, a[148:151], %0" : "+v"(ac[5]) : "v"(((u32x4){cA[1][0][0][0], cA[1][0][0][1], cA[1][0][1][0], cA[1][0][1][1]})) : ); SB();
  asm volatile("v_xor_b32 %0, 131072, %0" : "+v"(vB) :  : "memory"); SB();
  asm volatile("v_mfma_f32_16x16x32_bf16 %0, %1, a[132:135], %0" : "+v"(ac[3]) : "v"(((u32x4){cA[1][2][0][0], cA[1][2][0][1], cA[1][2][1][0], cA[1][2][1][1]})) : ); SB();
  asm volatile("v_mfma_f32_16x16x32_bf16 %0, %1, a[144:147], %0" : "+v"(ac[6]) : "v"(((u32x4){cA[1][2][0][0], cA[1][2][0][1], cA[1][2][1][0], cA[1][2][1][1]})) : ); SB();
  asm volatile("ds_write_b128 %0, %1 offset:2048" :  : "v"(dd), "v"(ac[2]) : "memory"); SB();
  asm volatile("v_mfma_f32_16x16x32_bf16 %0, %1, a[136:139], %0" : "+v"(ac[4]) : "v"(((u32x4){cA[1][2][0][0], cA[1][2][0][1], cA[1][2][1][0], cA[1][2][1][1]})) : ); SB();
  asm volatile("buffer_load_dwordx4 v[240:243], %0, %1, %2 offen offset:0 sc1" :  : "v"(vB), "s"(rWS), "s"(0u) : "memory"); SB();
  asm volatile("v_mfma_f32_16x16x32_bf16 %0, %1, a[144:147], %0" : "+v"(ac[5]) : "v"(((u32x4){cA[1][1][0][0], cA[1][1][0][1], cA[1][1][1][0], cA[1][1][1][1]})) : ); SB();
  asm volatile("v_mfma_f32_16x16x32_bf16 %0, %1, a[128:131], %0" : "+v"(ac[3]) : "v"(((u32x4){cA[1][3][0][0], cA[1][3][0][1], cA[1][3][1][0], cA[1][3][1][1]})) : ); SB();
  asm volatile("buffer_load_dwordx4 v[244:247], %0, %1, %2 offen offset:1024 sc1" :  : "v"(vB), "s"(rWS), "s"(0u) : "memory"); SB();
  asm volatile("v_mfma_f32_16x16x32_bf16 %0, %1, a[140:143], %0" : "+v"(ac[6]) : "v"(((u32x4){cA[1][3][0][0], cA[1][3][0][1], cA[1][3][1][0], cA[1][3][1][1]})) : ); SB();
  asm volatile("v_mfma_f32_16x16x32_bf16 %0, %1, a[132:135], %0" : "+v"(ac[4]) : "v"(((u32x4){cA[1][3][0][0], cA[1][3][0][1], cA[1][3][1][0], cA[1][3][1][1]})) : ); SB();
  asm volatile("v_mfma_f32_16x16x32_bf16 %0, %1, a[140:143], %0" : "+v"(ac[5]) : "v"(((u32x4){cA[1][2][0][0], cA[1][2][0][1], cA[1][2][1][0], cA[1][2][1][1]})) : ); SB();
  asm volatile("v_mfma_f32_16x16x32_bf16 %0, %1, a[140:143], %0" : "+v"(ac[7]) : "v"(((u32x4){cA[1][4][0][0], cA[1][4][0][1], cA[1][4][1][0], cA[1][4][1][1]})) : ); SB();
  asm volatile("ds_write_b128 %0, %1 offset:3072" :  : "v"(dd), "v"(ac[3]) : "memory"); SB();
  asm volatile("v_mfma_f32_16x16x32_bf16 %0, %1, a[136:139], %0" : "+v"(ac[6]) : "v"(((u32x4){cA[1][4][0][0], cA[1][4][0][1], cA[1][4][1][0], cA[1][4][1][1]})) : ); SB();
  asm volatile("buffer_load_dwordx4 %0, %1, %2 offen offset:2048 lds" :  : "v"(voS), "s"(rA), "s"(ao[6]) : "memory"); SB();
  asm volatile("v_mfma_f32_16x16x32_bf16 %0, %1, a[128:131], %0" : "+v"(ac[4]) : "v"(((u32x4){cA[1][4][0][0], cA[1][4][0][1], cA[1][4][1][0], cA[1][4][1][1]})) : ); SB();
  asm volatile("v_mfma_f32_16x16x32_bf16 %0, %1, a[136:139], %0" : "+v"(ac[5]) : "v"(((u32x4){cA[1][3][0][0], cA[1][3][0][1], cA[1][3][1][0], cA[1][3][1][1]})) : ); SB();
  asm volatile("buffer_load_dwordx4 %0, %1, %2 offen offset:3072 lds" :  : "v"(voS), "s"(rA), "s"(ao[7]) : "memory"); SB();
  asm volatile("v_mfma_f32_16x16x32_bf16 %0, %1, a[136:139], %0" : "+v"(ac[7]) : "v"(((u32x4){cA[1][5][0][0], cA[1][5][0][1], cA[1][5][1][0], cA[1][5][1][1]})) : ); SB();
  asm volatile("v_xor_b32 %0, 128, %0" : "+v"(voS) :  : "memory"); SB();
  asm volatile("v_mfma_f32_16x16x32_bf16 %0, %1, a[132:135], %0" : "+v"(ac[6]) : "v"(((u32x4){cA[1][5][0][0], cA[1][5][0][1], cA[1][5][1][0], cA[1][5][1][1]})) : ); SB();
  asm volatile("v_mfma_f32_16x16x32_bf16 %0, %1, a[132:135], %0" : "+v"(ac[5]) : "v"(((u32x4){cA[1][4][0][0], cA[1][4][0][1], cA[1][4][1][0], cA[1][4][1][1]})) : ); SB();
  asm volatile("ds_write_b128 %0, %1 offset:4096" :  : "v"(dd), "v"(ac[4]) : "memory"); SB();
  asm volatile("v_mfma_f32_16x16x32_bf16 %0, %1, a[132:135], %0" : "+v"(ac[7]) : "v"(((u32x4){cA[1][6][0][0], cA[1][6][0][1], cA[1][6][1][0], cA[1][6][1][1]})) : ); SB();
  asm volatile("buffer_load_dwordx4 v[248:251], %0, %1, %2 offen offset:0 sc1" :  : "v"(vB), "s"(rWS), "s"(65536u) : "memory"); SB();
  asm volatile("v_mfma_f32_16x16x32_bf16 %0, %1, a[128:131], %0" : "+v"(ac[6]) : "v"(((u32x4){cA[1][6][0][0], cA[1][6][0][1], cA[1][6][1][0], cA[1][6][1][1]})) : ); SB();
  asm volatile("v_mfma_f32_16x16x32_bf16 %0, %1, a[128:131], %0" : "+v"(ac[5]) : "v"(((u32x4){cA[1][5][0][0], cA[1][5][0][1], cA[1][5][1][0], cA[1][5][1][1]})) : ); SB();
  asm volatile("buffer_load_dwordx4 v[252:255], %0, %1, %2 offen offset:1024 sc1" :  : "v"(vB), "s"(rWS), "s"(65536u) : "memory"); SB();
  asm volatile("s_waitcnt lgkmcnt(1)\n\tv_mfma_f32_16x16x32_bf16 %0, %1, a[128:131], %0" : "+v"(ac[7]) : "v"(((u32x4){cA[1][7][0][0], cA[1][7][0][1], cA[1][7][1][0], cA[1][7][1][1]})) : ); SB();
  asm volatile("v_xor_b32 %0, 393216, %0" : "+v"(vB) :  : "memory"); SB();
  asm volatile("s_nop 5" : "+v"(ac[5]), "+v"(ac[6]), "+v"(ac[7]) :  : ); SB();
  asm volatile("ds_write_b128 %0, %1 offset:5120" :  : "v"(dd), "v"(ac[5]) : "memory"); SB();
  asm volatile("ds_write_b128 %0, %1 offset:6144" :  : "v"(dd), "v"(ac[6]) : "memory"); SB();
  asm volatile("ds_write_b128 %0, %1 offset:7168" :  : "v"(dd), "v"(ac[7]) : "memory"); SB();
  } else {
  asm volatile("; ELSE_BEGIN"); SB();
  asm volatile("s_waitcnt lgkmcnt(0)" ::: "memory"); SB();
  asm volatile("buffer_load_dwordx4 v[180:183], %0, %1, %2 offen offset:1024 sc1" :  : "v"(vB), "s"(rWS), "s"(0u) : "memory"); SB();
  asm volatile("s_waitcnt lgkmcnt(0)" ::: "memory"); SB();
  asm volatile("ds_write_b128 %0, %1 offset:0" :  : "v"(ew[0]), "v"(a1[0][0]) : "memory"); SB();
  asm volatile("s_waitcnt lgkmcnt(0)" ::: "memory"); SB();
  asm volatile("buffer_load_dwordx4 v[184:187], %0, %1, %2 offen offset:0 sc1" :  : "v"(vB), "s"(rWS), "s"(65536u) : "memory"); SB();
  asm volatile("s_waitcnt lgkmcnt(0)" ::: "memory"); SB();
  asm volatile("buffer_load_dwordx4 v[188:191], %0, %1, %2 offen offset:1024 sc1" :  : "v"(vB), "s"(rWS), "s"(65536u) : "memory"); SB();
  asm volatile("s_waitcnt lgkmcnt(0)" ::: "memory"); SB();
  asm volatile("v_xor_b32 %0, 393216, %0" : "+v"(vB) :  : "memory"); SB();
  asm volatile("s_waitcnt lgkmcnt(0)" ::: "memory"); SB();
  asm volatile("buffer_load_dwordx4 v[192:195], %0, %1, %2 offen offset:0 sc1" :  : "v"(vB), "s"(rWS), "s"(0u) : "memory"); SB();
  asm volatile("s_waitcnt lgkmcnt(0)" ::: "memory"); SB();
  asm volatile("ds_write_b128 %0, %1 offset:1024" :  : "v"(ew[0]), "v"(a1[0][1]) : "memory"); SB();
  asm volatile("s_waitcnt lgkmcnt(0)" ::: "memory"); SB();
  asm volatile("buffer_load_dwordx4 v[196:199], %0, %1, %2 offen offset:1024 sc1" :  : "v"(vB), "s"(rWS), "s"(0u) : "memory"); SB();
  asm volatile("s_waitcnt lgkmcnt(0)" ::: "memory"); SB();
  asm volatile("buffer_load_dwordx4 v[200:203], %0, %1, %2 offen offset:0 sc1" :  : "v"(vB), "s"(rWS), "s"(65536u) : "memory"); SB();
  asm volatile("s_waitcnt lgkmcnt(0)" ::: "memory"); SB();
  asm volatile("ds_write_b128 %0, %1 offset:2048" :  : "v"(ew[0]), "v"(a1[0][2]) : "memory"); SB();
  asm volatile("s_waitcnt lgkmcnt(0)" ::: "memory"); SB();
  asm volatile("buffer_load_dwordx4 v[204:207], %0, %1, %2 offen offset:1024 sc1" :  : "v"(vB), "s"(rWS), "s"(65536u) : "memory"); SB();
  asm volatile("s_waitcnt lgkmcnt(0)" ::: "memory"); SB();
  asm volatile("v_xor_b32 %0, 131072, %0" : "+v"(vB) :  : "memory"); SB();
  asm volatile("s_waitcnt lgkmcnt(0)" ::: "memory"); SB();
  asm volatile("buffer_load_dwordx4 v[208:211], %0, %1, %2 offen offset:0 sc1" :  : "v"(vB), "s"(rWS), "s"(0u) : "memory"); SB();
  asm volatile("s_waitcnt lgkmcnt(0)" ::: "memory"); SB();
  asm volatile("buffer_load_dwordx4 v[212:215], %0, %1, %2 offen offset:1024 sc1" :  : "v"(vB), "s"(rWS), "s"(0u) : "memory"); SB();
  asm volatile("s_waitcnt lgkmcnt(0)" ::: "memory"); SB();
  asm volatile("buffer_load_dwordx4 v[216:219], %0, %1, %2 offen offset:0 sc1" :  : "v"(vB), "s"(rWS), "s"(65536u) : "memory"); SB();
  asm volatile("s_waitcnt lgkmcnt(0)" ::: "memory"); SB();
  asm volatile("buffer_load_dwordx4 v[220:223], %0, %1, %2 offen offset:1024 sc1" :  : "v"(vB), "s"(rWS), "s"(65536u) : "memory"); SB();
  asm volatile("s_waitcnt lgkmcnt(0)" ::: "memory"); SB();
  asm volatile("v_xor_b32 %0, 917504, %0" : "+v"(vB) :  : "memory"); SB();
  asm volatile("s_waitcnt lgkmcnt(0)" ::: "memory"); SB();
  asm volatile("ds_write_b128 %0, %1 offset:0" :  : "v"(ew[1]), "v"(a1[1][0]) : "memory"); SB();
  asm volatile("s_waitcnt lgkmcnt(0)" ::: "memory"); SB();
  asm volatile("ds_write_b128 %0, %1 offset:1024" :  : "v"(ew[1]), "v"(a1[1][1]) : "memory"); SB();
  asm volatile("s_waitcnt lgkmcnt(0)" ::: "memory"); SB();
  asm volatile("ds_write_b128 %0, %1 offset:2048" :  : "v"(ew[1]), "v"(a1[1][2]) : "memory"); SB();
  asm volatile("s_waitcnt lgkmcnt(0)" ::: "memory"); SB();
  asm volatile("buffer_load_dwordx4 v[224:227], %0, %1, %2 offen offset:0 sc1" :  : "v"(vB), "s"(rWS), "s"(0u) : "memory"); SB();
  asm volatile("s_waitcnt lgkmcnt(0)" ::: "memory"); SB();
  asm volatile("buffer_load_dwordx4 v[228:231], %0, %1, %2 offen offset:1024 sc1" :  : "v"(vB), "s"(rWS), "s"(0u) : "memory"); SB();
  asm volatile("s_waitcnt lgkmcnt(0)" ::: "memory"); SB();
  asm volatile("buffer_load_dwordx4 v[232:235], %0, %1, %2 offen offset:0 sc1" :  : "v"(vB), "s"(rWS), "s"(65536u) : "memory"); SB();
  asm volatile("s_waitcnt lgkmcnt(0)" ::: "memory"); SB();
  asm volatile("buffer_load_dwordx4 v[236:239], %0, %1, %2 offen offset:1024 sc1" :  : "v"(vB), "s"(rWS), "s"(65536u) : "memory"); SB();
  asm volatile("s_waitcnt lgkmcnt(0)" ::: "memory"); SB();
  asm volatile("v_xor_b32 %0, 131072, %0" : "+v"(vB) :  : "memory"); SB();
  asm volatile("s_waitcnt lgkmcnt(0)" ::: "memory"); SB();
  asm volatile("buffer_load_dwordx4 v[240:243], %0, %1, %2 offen offset:0 sc1" :  : "v"(vB), "s"(rWS), "s"(0u) : "memory"); SB();
  asm volatile("s_waitcnt lgkmcnt(0)" ::: "memory"); SB();
  asm volatile("buffer_load_dwordx4 v[244:247], %0, %1, %2 offen offset:1024 sc1" :  : "v"(vB), "s"(rWS), "s"(0u) : "memory"); SB();
  asm volatile("s_waitcnt lgkmcnt(0)" ::: "memory"); SB();
  asm volatile("s_waitcnt lgkmcnt(0)" ::: "memory"); SB();
  asm volatile("buffer_load_dwordx4 %0, %1, %2 offen offset:2048 lds" :  : "v"(voS), "s"(rA), "s"(ao[6]) : "memory"); SB();
  asm volatile("s_waitcnt lgkmcnt(0)" ::: "memory"); SB();
  asm volatile("s_waitcnt lgkmcnt(0)" ::: "memory"); SB();
  asm volatile("buffer_load_dwordx4 %0, %1, %2 offen offset:3072 lds" :  : "v"(voS), "s"(rA), "s"(ao[7]) : "memory"); SB();
  asm volatile("s_waitcnt lgkmcnt(0)" ::: "memory"); SB();
  asm volatile("s_waitcnt lgkmcnt(0)" ::: "memory"); SB();
  asm volatile("v_xor_b32 %0, 128, %0" : "+v"(voS) :  : "memory"); SB();
  asm volatile("s_waitcnt lgkmcnt(0)" ::: "memory"); SB();
  asm volatile("buffer_load_dwordx4 v[248:251], %0, %1, %2 offen offset:0 sc1" :  : "v"(vB), "s"(rWS), "s"(65536u) : "memory"); SB();
  asm volatile("s_waitcnt lgkmcnt(0)" ::: "memory"); SB();
  asm volatile("buffer_load_dwordx4 v[252:255], %0, %1, %2 offen offset:1024 sc1" :  : "v"(vB), "s"(rWS), "s"(65536u) : "memory"); SB();
  asm volatile("s_waitcnt lgkmcnt(0)" ::: "memory"); SB();
  asm volatile("v_xor_b32 %0, 393216, %0" : "+v"(vB) :  : "memory"); SB();
  asm volatile("s_waitcnt lgkmcnt(0) ; ELSE_END" ::: "memory"); SB();
  }
  ST(15);
  f32x4 g[2][4][2], pr[6], yq[8], bs4 = {bs, bs, bs, bs};
  u32x4 ho[3], ykv, y2;
  u32x2 xv[5];
  float gt[4], ge[4];
  unsigned vH_, pw2, pe[3], vX, vO, vFH, vFY, vHr, hso[8], ys2, ydr, ykr, er, sFH, sFY;
  int dl;
  asm volatile("" : "+v"(bs4)); SB();
  asm volatile("s_waitcnt vmcnt(24)" :  :  : "memory"); SB();
  asm volatile("v_mfma_f32_16x16x32_bf16 %0, v[160:163], a[160:163], %1" : "=&v"(g[0][0][0]) : "v"(bs4) : ); SB();
  { unsigned lane = lane0, w = w0, rb = rb0, cb = cb0;
  asm volatile("" : "+v"(lane)); asm volatile("" : "+s"(w), "+s"(rb), "+s"(cb)); SB();

  asm volatile("v_mfma_f32_16x16x32_bf16 %0, v[160:163], a[164:167], 0" : "=&v"(g[0][1][0]) :  : ); SB();
  er = lb + 32768 + w * 6144 + l16; asm volatile("" : "+v"(er)); SB();
  asm volatile("v_mfma_f32_16x16x32_bf16 %0, v[160:163], a[168:171], 0" : "=&v"(g[0][2][0]) :  : ); SB();
  asm volatile("s_waitcnt lgkmcnt(0)\n\ts_barrier" :  :  : "memory"); SB();
  asm volatile("ds_read_b128 %0, %1 offset:0" : "=&v"(a1[0][0]) : "v"(er) : "memory"); SB();
  asm volatile("v_mfma_f32_16x16x32_bf16 %0, v[160:163], a[172:175], 0" : "=&v"(g[0][3][0]) :  : ); SB();
  asm volatile("ds_read_b128 a[96:99], %0 offset:32768" :  : "v"(aA[0]) : "memory"); SB();
  asm volatile("v_mfma_f32_16x16x32_bf16 %0, v[164:167], a[160:163], %1" : "=&v"(g[0][0][1]) : "v"(bs4) : ); SB();
  asm volatile("ds_read_b128 a[112:115], %0 offset:32768" :  : "v"(aA[1]) : "memory"); SB();
  asm volatile("v_mfma_f32_16x16x32_bf16 %0, v[164:167], a[164:167], 0" : "=&v"(g[0][1][1]) :  : ); SB();
  asm volatile("ds_read_b128 a[100:103], %0 offset:34816" :  : "v"(aA[0]) : "memory"); SB();
  asm volatile("v_mfma_f32_16x16x32_bf16 %0, v[164:167], a[168:171], 0" : "=&v"(g[0][2][1]) :  : ); SB();
  asm volatile("ds_read_b128 a[116:119], %0 offset:34816" :  : "v"(aA[1]) : "memory"); SB();
  asm volatile("v_mfma_f32_16x16x32_bf16 %0, v[164:167], a[172:175], 0" : "=&v"(g[0][3][1]) :  : ); SB();
  asm volatile("ds_read_b128 a[104:107], %0 offset:36864" :  : "v"(aA[0]) : "memory"); SB();
  ST(25);
  asm volatile("s_waitcnt vmcnt(22)" :  :  : "memory"); SB();
  asm volatile("v_mfma_f32_16x16x32_bf16 %0, v[168:171], a[176:179], %0" : "+v"(g[0][0][0]) :  : ); SB();
  asm volatile("ds_read_b128 a[120:123], %0 offset:36864" :  : "v"(aA[1]) : "memory"); SB();
  asm volatile("v_mfma_f32_16x16x32_bf16 %0, v[168:171], a[180:183], %0" : "+v"(g[0][1][0]) :  : ); SB();
  asm volatile("buffer_load_dwordx4 v[160:163], %0, %1, %2 offen offset:0 sc1" :  : "v"(vB), "s"(rWS), "s"(0u) : "memory"); SB();
  asm volatile("v_mfma_f32_16x16x32_bf16 %0, v[168:171], a[184:187], %0" : "+v"(g[0][2][0]) :  : ); SB();
  asm volatile("buffer_load_dwordx4 v[164:167], %0, %1, %2 offen offset:1024 sc1" :  : "v"(vB), "s"(rWS), "s"(0u) : "memory"); SB();
  asm volatile("s_add_u32 m0, %0, 98304" :  : "s"(mA) : "memory", "scc"); SB();
  asm volatile("v_mfma_f32_16x16x32_bf16 %0, v[168:171], a[188:191], %0" : "+v"(g[0][3][0]) :  : ); SB();
  asm volatile("ds_read_b128 %0, %1 offset:1024" : "=&v"(a1[0][1]) : "v"(er) : "memory"); SB();
  asm volatile("v_mfma_f32_16x16x32_bf16 %0, v[172:175], a[176:179], %0" : "+v"(g[0][0][1]) :  : ); SB();
  asm volatile("ds_read_b128 %0, %1 offset:2048" : "=&v"(a1[0][2]) : "v"(er) : "memory"); SB();
  asm volatile("v_mfma_f32_16x16x32_bf16 %0, v[172:175], a[180:183], %0" : "+v"(g[0][1][1]) :  : ); SB();
  asm volatile("buffer_load_dwordx4 %0, %1, %2 offen lds" :  : "v"(voS), "s"(rA), "s"(ao[0]) : "memory"); SB();
  asm volatile("v_mfma_f32_16x16x32_bf16 %0, v[172:175], a[184:187], %0" : "+v"(g[0][2][1]) :  : ); SB();
  asm volatile("ds_read_b128 %0, %1 offset:3072" : "=&v"(a1[1][0]) : "v"(er) : "memory"); SB();
  asm volatile("v_mfma_f32_16x16x32_bf16 %0, v[172:175], a[188:191], %0" : "+v"(g[0][3][1]) :  : ); SB();
  asm volatile("ds_read_b128 %0, %1 offset:4096" : "=&v"(a1[1][1]) : "v"(er) : "memory"); SB();
  asm volatile("s_waitcnt vmcnt(23)" :  :  : "memory"); SB();
  asm volatile("v_mfma_f32_16x16x32_bf16 %0, v[176:179], a[192:195], %0" : "+v"(g[0][0][0]) :  : ); SB();
  asm volatile("ds_read_b128 %0, %1 offset:5120" : "=&v"(a1[1][2]) : "v"(er) : "memory"); SB();
  asm volatile("v_mfma_f32_16x16x32_bf16 %0, v[176:179], a[196:199], %0" : "+v"(g[0][1][0]) :  : ); SB();
  asm volatile("buffer_load_dwordx4 %0, %1, %2 offen offset:1024 lds" :  : "v"(voS), "s"(rA), "s"(ao[1]) : "memory"); SB();
  asm volatile("v_mfma_f32_16x16x32_bf16 %0, v[176:179], a[200:203], %0" : "+v"(g[0][2][0]) :  : ); SB();
  ydr = lb + 0 + (w * 2 + (lane >> 5)) * 1024 + (lane & 1) * 512; asm volatile("" : "+v"(ydr)); SB();
  asm volatile("v_mfma_f32_16x16x32_bf16 %0, v[176:179], a[204:207], %0" : "+v"(g[0][3][0]) :  : ); SB();
  ydr = ydr + ((((lane >> 1) & 15) ^ ((lane & 1) << 3)) << 4); asm volatile("" : "+v"(ydr)); SB();
  asm volatile("v_mfma_f32_16x16x32_bf16 %0, v[180:183], a[192:195], %0" : "+v"(g[0][0][1]) :  : ); SB();
  asm volatile("buffer_load_dwordx4 %0, %1, %2 offen offset:2048 lds" :  : "v"(voS), "s"(rA), "s"(ao[2]) : "memory"); SB();
  asm volatile("v_mfma_f32_16x16x32_bf16 %0, v[180:183], a[196:199], %0" : "+v"(g[0][1][1]) :  : ); SB();
  ykr = lb + 59520 + 512 + w * 1024 + l16; asm volatile("" : "+v"(ykr)); SB();
  asm volatile("v_mfma_f32_16x16x32_bf16 %0, v[180:183], a[200:203], %0" : "+v"(g[0][2][1]) :  : ); SB();
  pw2 = er + (122880 - 32768); asm volatile("" : "+v"(pw2)); SB();
  asm volatile("buffer_load_dwordx4 %0, %1, %2 offen offset:3072 lds" :  : "v"(voS), "s"(rA), "s"(ao[3]) : "memory"); SB();
  asm volatile("v_mfma_f32_16x16x32_bf16 %0, v[180:183], a[204:207], %0" : "+v"(g[0][3][1]) :  : ); SB();
  pe[0] = lb + 122880 + (w ^ 1) * 6144 + l16; asm volatile("" : "+v"(pe[0])); SB();
  asm volatile("s_waitcnt vmcnt(24)" :  :  : "memory"); SB();
  asm volatile("v_mfma_f32_16x16x32_bf16 %0, v[184:187], a[208:211], %0" : "+v"(g[0][0][0]) :  : ); SB();
  pe[1] = lb + 122880 + (w ^ 2) * 6144 + l16; asm volatile("" : "+v"(pe[1])); SB();
  asm volatile("v_mfma_f32_16x16x32_bf16 %0, v[184:187], a[212:215], %0" : "+v"(g[0][1][0]) :  : ); SB();
  pe[2] = lb + 122880 + (w ^ 3) * 6144 + l16; asm volatile("" : "+v"(pe[2])); SB();
  asm volatile("v_mfma_f32_16x16x32_bf16 %0, v[184:187], a[216:219], %0" : "+v"(g[0][2][0]) :  : ); SB();
  asm volatile("s_waitcnt lgkmcnt(0)" : "+v"(a1[0][0]), "+v"(a1[0][1]), "+v"(a1[0][2]), "+v"(a1[1][0]), "+v"(a1[1][1]), "+v"(a1[1][2]) :  : "memory"); SB();
  asm volatile("s_add_u32 m0, %0, 102400" :  : "s"(mA) : "memory", "scc"); SB();
  asm volatile("v_mul_f32 %0, %1, %1" : "=v"(gt[0]) : "v"(a1[0][0][0]) : ); SB();
  asm volatile("v_mfma_f32_16x16x32_bf16 %0, v[184:187], a[220:223], %0" : "+v"(g[0][3][0]) :  : ); SB();
  asm volatile("buffer_load_dwordx4 %0, %1, %2 offen lds" :  : "v"(voS), "s"(rA), "s"(ao[4]) : "memory"); SB();
  asm volatile("v_mul_f32 %0, %1, %1" : "=v"(gt[1]) : "v"(a1[0][0][1]) : ); SB();
  asm volatile("v_mfma_f32_16x16x32_bf16 %0, v[188:191], a[208:211], %0" : "+v"(g[0][0][1]) :  : ); SB();
  dl = (int)(wm * 12928 - wn * 256 + w * 128) - 2 * 12928; asm volatile("" : "+s"(dl)); SB();
  asm volatile("v_mfma_f32_16x16x32_bf16 %0, v[188:191], a[212:215], %0" : "+v"(g[0][1][1]) :  : ); SB();
  asm volatile("buffer_load_dwordx4 %0, %1, %2 offen offset:1024 lds" :  : "v"(voS), "s"(rA), "s"(ao[5]) : "memory"); SB();
  asm volatile("v_add_u32 %0, %1, %0" : "+v"(aa[0]) : "s"(dl) : ); SB();
  asm volatile("v_mfma_f32_16x16x32_bf16 %0, v[188:191], a[216:219], %0" : "+v"(g[0][2][1]) :  : ); SB();
  asm volatile("buffer_load_dwordx4 %0, %1, %2 offen offset:2048 lds" :  : "v"(voS), "s"(rA), "s"(ao[6]) : "memory"); SB();
  asm volatile("v_add_u32 %0, %1, %0" : "+v"(aa[1]) : "s"(dl) : ); SB();
  asm volatile("v_mfma_f32_16x16x32_bf16 %0, v[188:191], a[220:223], %0" : "+v"(g[0][3][1]) :  : ); SB();
  asm volatile("v_add_u32 %0, %1, %0" : "+v"(aa[2]) : "s"(dl) : ); SB();
  asm volatile("s_waitcnt vmcnt(25)" :  :  : "memory"); SB();
  asm volatile("v_mfma_f32_16x16x32_bf16 %0, v[192:195], a[224:227], %0" : "+v"(g[0][0][0]) :  : ); SB();
  asm volatile("v_add_u32 %0, %1, %0" : "+v"(aa[3]) : "s"(dl) : ); SB();
  asm volatile("v_mfma_f32_16x16x32_bf16 %0, v[192:195], a[228:231], %0" : "+v"(g[0][1][0]) :  : ); SB();
  asm volatile("buffer_load_dwordx4 %0, %1, %2 offen offset:3072 lds" :  : "v"(voS), "s"(rA), "s"(ao[7]) : "memory"); SB();
  asm volatile("v_add_u32 %0, %1, %0" : "+v"(bb) : "s"(dl) : ); SB();
  asm volatile("v_mfma_f32_16x16x32_bf16 %0, v[192:195], a[232:235], %0" : "+v"(g[0][2][0]) :  : ); SB();
  asm volatile("s_waitcnt vmcnt(12)" :  :  : "memory"); SB();
  asm volatile("ds_read_b128 a[108:111], %0 offset:38912" :  : "v"(aA[0]) : "memory"); SB();
  asm volatile("v_mfma_f32_16x16x32_bf16 %0, v[192:195], a[236:239], %0" : "+v"(g[0][3][0]) :  : ); SB();
  asm volatile("ds_read_b128 a[124:127], %0 offset:38912" :  : "v"(aA[1]) : "memory"); SB();
  asm volatile("v_mul_f32 %0, %1, %1" : "=v"(gt[2]) : "v"(a1[0][0][2]) : ); SB();
  asm volatile("v_mfma_f32_16x16x32_bf16 %0, v[196:199], a[224:227], %0" : "+v"(g[0][0][1]) :  : ); SB();
  asm volatile("v_mul_f32 %0, %1, %1" : "=v"(gt[3]) : "v"(a1[0][0][3]) : ); SB();
  asm volatile("v_mfma_f32_16x16x32_bf16 %0, v[196:199], a[228:231], %0" : "+v"(g[0][1][1]) :  : ); SB();
  asm volatile("buffer_load_dwordx4 v[168:171], %0, %1, %2 offen offset:0 sc1" :  : "v"(vB), "s"(rWS), "s"(65536u) : "memory"); SB();
  asm volatile("v_fma_f32 %0, %0, %1, %2" : "+v"(gt[0]) : "v"(k2[0]), "v"(k1[0]) : ); SB();
  asm volatile("v_mfma_f32_16x16x32_bf16 %0, v[196:199], a[232:235], %0" : "+v"(g[0][2][1]) :  : ); SB();
  asm volatile("v_fma_f32 %0, %0, %1, %2" : "+v"(gt[1]) : "v"(k2[0]), "v"(k1[0]) : ); SB();
  asm volatile("v_mfma_f32_16x16x32_bf16 %0, v[196:199], a[236:239], %0" : "+v"(g[0][3][1]) :  : ); SB();
  asm volatile("v_fma_f32 %0, %0, %1, %2" : "+v"(gt[2]) : "v"(k2[0]), "v"(k1[0]) : ); SB();
  asm volatile("v_mfma_f32_16x16x32_bf16 %0, v[200:203], a[240:243], %0" : "+v"(g[0][0][0]) :  : ); SB();
  asm volatile("buffer_load_dwordx4 v[172:175], %0, %1, %2 offen offset:1024 sc1" :  : "v"(vB), "s"(rWS), "s"(65536u) : "memory"); SB();
  asm volatile("v_fma_f32 %0, %0, %1, %2" : "+v"(gt[3]) : "v"(k2[0]), "v"(k1[0]) : ); SB();
  asm volatile("v_mfma_f32_16x16x32_bf16 %0, v[200:203], a[244:247], %0" : "+v"(g[0][1][0]) :  : ); SB();
  asm volatile("v_xor_b32 %0, 131072, %0" : "+v"(vB) :  : "memory"); SB();
  asm volatile("v_mul_f32 %0, %0, %1" : "+v"(gt[0]) : "v"(a1[0][0][0]) : ); SB();
  asm volatile("v_mfma_f32_16x16x32_bf16 %0, v[200:203], a[248:251], %0" : "+v"(g[0][2][0]) :  : ); SB();
  asm volatile("v_mul_f32 %0, %0, %1" : "+v"(gt[1]) : "v"(a1[0][0][1]) : ); SB();
  asm volatile("v_mfma_f32_16x16x32_bf16 %0, v[200:203], a[252:255], %0" : "+v"(g[0][3][0]) :  : ); SB();
  asm volatile("v_mul_f32 %0, %0, %1" : "+v"(gt[2]) : "v"(a1[0][0][2]) : ); SB();
  asm volatile("v_mfma_f32_16x16x32_bf16 %0, v[204:207], a[240:243], %0" : "+v"(g[0][0][1]) :  : ); SB();
  asm volatile("buffer_load_dwordx4 v[176:179], %0, %1, %2 offen offset:0 sc1" :  : "v"(vB), "s"(rWS), "s"(0u) : "memory"); SB();
  asm volatile("v_mul_f32 %0, %0, %1" : "+v"(gt[3]) : "v"(a1[0][0][3]) : ); SB();
  asm volatile("v_mfma_f32_16x16x32_bf16 %0, v[204:207], a[244:247], %0" : "+v"(g[0][1][1]) :  : ); SB();
  asm volatile("v_exp_f32 %0, %1" : "=v"(ge[0]) : "v"(gt[0]) : ); SB();
  asm volatile("v_mfma_f32_16x16x32_bf16 %0, v[204:207], a[248:251], %0" : "+v"(g[0][2][1]) :  : ); SB();
  asm volatile("v_exp_f32 %0, %1" : "=v"(ge[1]) : "v"(gt[1]) : ); SB();
  asm volatile("v_mfma_f32_16x16x32_bf16 %0, v[204:207], a[252:255], %0" : "+v"(g[0][3][1]) :  : ); SB();
  asm volatile("buffer_load_dwordx4 v[180:183], %0, %1, %2 offen offset:1024 sc1" :  : "v"(vB), "s"(rWS), "s"(0u) : "memory"); SB();
  asm volatile("v_mfma_f32_16x16x32_bf16 %0, v[208:211], a[0:3], %0" : "+v"(g[0][0][0]) :  : ); SB();
  asm volatile("v_exp_f32 %0, %1" : "=v"(ge[2]) : "v"(gt[2]) : ); SB();
  asm volatile("v_mfma_f32_16x16x32_bf16 %0, v[208:211], a[4:7], %0" : "+v"(g[0][1][0]) :  : ); SB();
  asm volatile("v_exp_f32 %0, %1" : "=v"(ge[3]) : "v"(gt[3]) : ); SB();
  asm volatile("v_mfma_f32_16x16x32_bf16 %0, v[208:211], a[8:11], %0" : "+v"(g[0][2][0]) :  : ); SB();
  asm volatile("v_fma_f32 %0, %0, %1, %1" : "+v"(ge[0]) : "v"(xd[0]) : ); SB();
  asm volatile("v_mfma_f32_16x16x32_bf16 %0, v[208:211], a[12:15], %0" : "+v"(g[0][3][0]) :  : ); SB();
  asm volatile("v_fma_f32 %0, %0, %1, %1" : "+v"(ge[1]) : "v"(xd[0]) : ); SB();
  asm volatile("v_mfma_f32_16x16x32_bf16 %0, v[212:215], a[0:3], %0" : "+v"(g[0][0][1]) :  : ); SB();
  asm volatile("v_fma_f32 %0, %0, %1, %1" : "+v"(ge[2]) : "v"(xd[0]) : ); SB();
  asm volatile("v_mfma_f32_16x16x32_bf16 %0, v[212:215], a[4:7], %0" : "+v"(g[0][1][1]) :  : ); SB();
  asm volatile("v_fma_f32 %0, %0, %1, %1" : "+v"(ge[3]) : "v"(xd[0]) : ); SB();
  asm volatile("v_mfma_f32_16x16x32_bf16 %0, v[212:215], a[8:11], %0" : "+v"(g[0][2][1]) :  : ); SB();
  asm volatile("v_rcp_f32 %0, %1" : "=v"(gt[0]) : "v"(ge[0]) : ); SB();
  asm volatile("v_mfma_f32_16x16x32_bf16 %0, v[212:215], a[12:15], %0" : "+v"(g[0][3][1]) :  : ); SB();
  asm volatile("v_rcp_f32 %0, %1" : "=v"(gt[1]) : "v"(ge[1]) : ); SB();
  asm volatile("v_mfma_f32_16x16x32_bf16 %0, v[216:219], a[16:19], %0" : "+v"(g[0][0][0]) :  : ); SB();
  asm volatile("v_rcp_f32 %0, %1" : "=v"(gt[2]) : "v"(ge[2]) : ); SB();
  asm volatile("v_mfma_f32_16x16x32_bf16 %0, v[216:219], a[20:23], %0" : "+v"(g[0][1][0]) :  : ); SB();
  asm volatile("v_rcp_f32 %0, %1" : "=v"(gt[3]) : "v"(ge[3]) : ); SB();
  asm volatile("v_mfma_f32_16x16x32_bf16 %0, v[216:219], a[24:27], %0" : "+v"(g[0][2][0]) :  : ); SB();
  asm volatile("buffer_load_dwordx4 v[184:187], %0, %1, %2 offen offset:0 sc1" :  : "v"(vB), "s"(rWS), "s"(65536u) : "memory"); SB();
  asm volatile("v_mul_f32 %0, %0, %1" : "+v"(a1[0][0][0]) : "v"(gt[0]) : ); SB();
  asm volatile("v_mfma_f32_16x16x32_bf16 %0, v[216:219], a[28:31], %0" : "+v"(g[0][3][0]) :  : ); SB();
  asm volatile("v_mul_f32 %0, %0, %1" : "+v"(a1[0][0][1]) : "v"(gt[1]) : ); SB();
  asm volatile("v_mfma_f32_16x16x32_bf16 %0, v[220:223], a[16:19], %0" : "+v"(g[0][0][1]) :  : ); SB();
  asm volatile("v_mul_f32 %0, %0, %1" : "+v"(a1[0][0][2]) : "v"(gt[2]) : ); SB();
  asm volatile("v_mfma_f32_16x16x32_bf16 %0, v[220:223], a[20:23], %0" : "+v"(g[0][1][1]) :  : ); SB();
  asm volatile("buffer_load_dwordx4 v[188:191], %0, %1, %2 offen offset:1024 sc1" :  : "v"(vB), "s"(rWS), "s"(65536u) : "memory"); SB();
  asm volatile("v_mul_f32 %0, %0, %1" : "+v"(a1[0][0][3]) : "v"(gt[3]) : ); SB();
  asm volatile("v_mfma_f32_16x16x32_bf16 %0, v[220:223], a[24:27], %0" : "+v"(g[0][2][1]) :  : ); SB();
  asm volatile("v_xor_b32 %0, 917504, %0" : "+v"(vB) :  : "memory"); SB();
  asm volatile("v_mul_f32 %0, %1, %1" : "=v"(gt[0]) : "v"(a1[0][1][0]) : ); SB();
  asm volatile("v_mfma_f32_16x16x32_bf16 %0, v[220:223], a[28:31], %0" : "+v"(g[0][3][1]) :  : ); SB();
  asm volatile("v_mul_f32 %0, %1, %1" : "=v"(gt[1]) : "v"(a1[0][1][1]) : ); SB();
  ST(26);
  asm volatile("v_mfma_f32_16x16x32_bf16 %0, v[224:227], a[32:35], %0" : "+v"(g[0][0][0]) :  : ); SB();
  asm volatile("v_mul_f32 %0, %1, %1" : "=v"(gt[2]) : "v"(a1[0][1][2]) : ); SB();
  asm volatile("v_mfma_f32_16x16x32_bf16 %0, v[224:227], a[36:39], %0" : "+v"(g[0][1][0]) :  : ); SB();
  asm volatile("buffer_load_dwordx4 v[192:195], %0, %1, %2 offen offset:0 sc1" :  : "v"(vB), "s"(rWS), "s"(2048u) : "memory"); SB();
  asm volatile("v_mul_f32 %0, %1, %1" : "=v"(gt[3]) : "v"(a1[0][1][3]) : ); SB();
  asm volatile("v_mfma_f32_16x16x32_bf16 %0, v[224:227], a[40:43], %0" : "+v"(g[0][2][0]) :  : ); SB();
  asm volatile("v_fma_f32 %0, %0, %1, %2" : "+v"(gt[0]) : "v"(k2[0]), "v"(k1[0]) : ); SB();
  asm volatile("v_mfma_f32_16x16x32_bf16 %0, v[224:227], a[44:47], %0" : "+v"(g[0][3][0]) :  : ); SB();
  asm volatile("v_fma_f32 %0, %0, %1, %2" : "+v"(gt[1]) : "v"(k2[0]), "v"(k1[0]) : ); SB();
  asm volatile("v_mfma_f32_16x16x32_bf16 %0, v[228:231], a[32:35], %0" : "+v"(g[0][0][1]) :  : ); SB();
  asm volatile("v_fma_f32 %0, %0, %1, %2" : "+v"(gt[2]) : "v"(k2[0]), "v"(k1[0]) : ); SB();
  asm volatile("v_mfma_f32_16x16x32_bf16 %0, v[228:231], a[36:39], %0" : "+v"(g[0][1][1]) :  : ); SB();
  asm volatile("s_waitcnt vmcnt(13)" :  :  : "memory"); SB();
  asm volatile("ds_read_b128 a[128:131], %0 offset:0" :  : "v"(aA[0]) : "memory"); SB();
  asm volatile("v_mfma_f32_16x16x32_bf16 %0, v[228:231], a[40:43], %0" : "+v"(g[0][2][1]) :  : ); SB();
  asm volatile("ds_read_b128 a[144:147], %0 offset:0" :  : "v"(aA[1]) : "memory"); SB();
  asm volatile("v_fma_f32 %0, %0, %1, %2" : "+v"(gt[3]) : "v"(k2[0]), "v"(k1[0]) : ); SB();
  asm volatile("v_mfma_f32_16x16x32_bf16 %0, v[228:231], a[44:47], %0" : "+v"(g[0][3][1]) :  : ); SB();
  asm volatile("buffer_load_dwordx4 v[196:199], %0, %1, %2 offen offset:1024 sc1" :  : "v"(vB), "s"(rWS), "s"(2048u) : "memory"); SB();
  asm volatile("v_mfma_f32_16x16x32_bf16 %0, v[232:235], a[48:51], %0" : "+v"(g[0][0][0]) :  : ); SB();
  asm volatile("buffer_load_dwordx4 v[200:203], %0, %1, %2 offen offset:0 sc1" :  : "v"(vB), "s"(rWS), "s"(67584u) : "memory"); SB();
  asm volatile("v_mul_f32 %0, %0, %1" : "+v"(gt[0]) : "v"(a1[0][1][0]) : ); SB();
  asm volatile("v_mfma_f32_16x16x32_bf16 %0, v[232:235], a[52:55], %0" : "+v"(g[0][1][0]) :  : ); SB();
  asm volatile("v_mul_f32 %0, %0, %1" : "+v"(gt[1]) : "v"(a1[0][1][1]) : ); SB();
  asm volatile("v_mfma_f32_16x16x32_bf16 %0, v[232:235], a[56:59], %0" : "+v"(g[0][2][0]) :  : ); SB();
  asm volatile("v_mul_f32 %0, %0, %1" : "+v"(gt[2]) : "v"(a1[0][1][2]) : ); SB();
  asm volatile("v_mfma_f32_16x16x32_bf16 %0, v[232:235], a[60:63], %0" : "+v"(g[0][3][0]) :  : ); SB();
  asm volatile("s_waitcnt vmcnt(13)" :  :  : "memory"); SB();
  asm volatile("ds_read_b128 a[132:135], %0 offset:2048" :  : "v"(aA[0]) : "memory"); SB();
  asm volatile("v_mfma_f32_16x16x32_bf16 %0, v[236:239], a[48:51], %0" : "+v"(g[0][0][1]) :  : ); SB();
  asm volatile("ds_read_b128 a[148:151], %0 offset:2048" :  : "v"(aA[1]) : "memory"); SB();
  asm volatile("v_mul_f32 %0, %0, %1" : "+v"(gt[3]) : "v"(a1[0][1][3]) : ); SB();
  asm volatile("v_mfma_f32_16x16x32_bf16 %0, v[236:239], a[52:55], %0" : "+v"(g[0][1][1]) :  : ); SB();
  asm volatile("buffer_load_dwordx4 v[204:207], %0, %1, %2 offen offset:1024 sc1" :  : "v"(vB), "s"(rWS), "s"(67584u) : "memory"); SB();
  asm volatile("v_mfma_f32_16x16x32_bf16 %0, v[236:239], a[56:59], %0" : "+v"(g[0][2][1]) :  : ); SB();
  asm volatile("v_xor_b32 %0, 131072, %0" : "+v"(vB) :  : "memory"); SB();
  asm volatile("v_mfma_f32_16x16x32_bf16 %0, v[236:239], a[60:63], %0" : "+v"(g[0][3][1]) :  : ); SB();
  asm volatile("buffer_load_dwordx4 v[208:211], %0, %1, %2 offen offset:0 sc1" :  : "v"(vB), "s"(rWS), "s"(2048u) : "memory"); SB();
  asm volatile("v_mfma_f32_16x16x32_bf16 %0, v[240:243], a[64:67], %0" : "+v"(g[0][0][0]) :  : ); SB();
  asm volatile("v_exp_f32 %0, %1" : "=v"(ge[0]) : "v"(gt[0]) : ); SB();
  asm volatile("v_mfma_f32_16x16x32_bf16 %0, v[240:243], a[68:71], %0" : "+v"(g[0][1][0]) :  : ); SB();
  asm volatile("v_exp_f32 %0, %1" : "=v"(ge[1]) : "v"(gt[1]) : ); SB();
  asm volatile("v_mfma_f32_16x16x32_bf16 %0, v[240:243], a[72:75], %0" : "+v"(g[0][2][0]) :  : ); SB();
  asm volatile("s_waitcnt vmcnt(13)" :  :  : "memory"); SB();
  asm volatile("ds_read_b128 a[136:139], %0 offset:4096" :  : "v"(aA[0]) : "memory"); SB();
  asm volatile("v_mfma_f32_16x16x32_bf16 %0, v[240:243], a[76:79], %0" : "+v"(g[0][3][0]) :  : ); SB();
  asm volatile("ds_read_b128 a[152:155], %0 offset:4096" :  : "v"(aA[1]) : "memory"); SB();
  asm volatile("v_mfma_f32_16x16x32_bf16 %0, v[244:247], a[64:67], %0" : "+v"(g[0][0][1]) :  : ); SB();
  asm volatile("buffer_load_dwordx4 v[212:215], %0, %1, %2 offen offset:1024 sc1" :  : "v"(vB), "s"(rWS), "s"(2048u) : "memory"); SB();
  asm volatile("v_mfma_f32_16x16x32_bf16 %0, v[244:247], a[68:71], %0" : "+v"(g[0][1][1]) :  : ); SB();
  asm volatile("v_exp_f32 %0, %1" : "=v"(ge[2]) : "v"(gt[2]) : ); SB();
  asm volatile("v_mfma_f32_16x16x32_bf16 %0, v[244:247], a[72:75], %0" : "+v"(g[0][2][1]) :  : ); SB();
  asm volatile("buffer_load_dwordx4 v[216:219], %0, %1, %2 offen offset:0 sc1" :  : "v"(vB), "s"(rWS), "s"(67584u) : "memory"); SB();
  asm volatile("v_mfma_f32_16x16x32_bf16 %0, v[244:247], a[76:79], %0" : "+v"(g[0][3][1]) :  : ); SB();
  asm volatile("v_exp_f32 %0, %1" : "=v"(ge[3]) : "v"(gt[3]) : ); SB();
  asm volatile("v_mfma_f32_16x16x32_bf16 %0, v[248:251], a[80:83], %0" : "+v"(g[0][0][0]) :  : ); SB();
  asm volatile("v_fma_f32 %0, %0, %1, %1" : "+v"(ge[0]) : "v"(xd[0]) : ); SB();
  asm volatile("v_mfma_f32_16x16x32_bf16 %0, v[248:251], a[84:87], %0" : "+v"(g[0][1][0]) :  : ); SB();
  asm volatile("v_fma_f32 %0, %0, %1, %1" : "+v"(ge[1]) : "v"(xd[0]) : ); SB();
  asm volatile("v_mfma_f32_16x16x32_bf16 %0, v[248:251], a[88:91], %0" : "+v"(g[0][2][0]) :  : ); SB();
  asm volatile("s_waitcnt vmcnt(13)" :  :  : "memory"); SB();
  asm volatile("ds_read_b128 a[140:143], %0 offset:6144" :  : "v"(aA[0]) : "memory"); SB();
  asm volatile("v_mfma_f32_16x16x32_bf16 %0, v[248:251], a[92:95], %0" : "+v"(g[0][3][0]) :  : ); SB();
  asm volatile("ds_read_b128 a[156:159], %0 offset:6144" :  : "v"(aA[1]) : "memory"); SB();
  asm volatile("v_fma_f32 %0, %0, %1, %1" : "+v"(ge[2]) : "v"(xd[0]) : ); SB();
  asm volatile("v_mfma_f32_16x16x32_bf16 %0, v[252:255], a[80:83], %0" : "+v"(g[0][0][1]) :  : ); SB();
  asm volatile("buffer_load_dwordx4 v[220:223], %0, %1, %2 offen offset:1024 sc1" :  : "v"(vB), "s"(rWS), "s"(67584u) : "memory"); SB();
  asm volatile("v_fma_f32 %0, %0, %1, %1" : "+v"(ge[3]) : "v"(xd[0]) : ); SB();
  asm volatile("v_mfma_f32_16x16x32_bf16 %0, v[252:255], a[84:87], %0" : "+v"(g[0][1][1]) :  : ); SB();
  asm volatile("v_xor_b32 %0, 393216, %0" : "+v"(vB) :  : "memory"); SB();
  asm volatile("v_mfma_f32_16x16x32_bf16 %0, v[252:255], a[88:91], %0" : "+v"(g[0][2][1]) :  : ); SB();
  asm volatile("buffer_load_dwordx4 v[224:227], %0, %1, %2 offen offset:0 sc1" :  : "v"(vB), "s"(rWS), "s"(2048u) : "memory"); SB();
  asm volatile("v_mfma_f32_16x16x32_bf16 %0, v[252:255], a[92:95], %0" : "+v"(g[0][3][1]) :  : ); SB();
  asm volatile("v_rcp_f32 %0, %1" : "=v"(gt[0]) : "v"(ge[0]) : ); SB();
  asm volatile("v_mfma_f32_16x16x32_bf16 %0, v[160:163], a[96:99], %0" : "+v"(g[0][0][0]) :  : ); SB();
  asm volatile("v_rcp_f32 %0, %1" : "=v"(gt[1]) : "v"(ge[1]) : ); SB();
  asm volatile("v_mfma_f32_16x16x32_bf16 %0, v[160:163], a[100:103], %0" : "+v"(g[0][1][0]) :  : ); SB();
  asm volatile("buffer_load_dwordx4 v[228:231], %0, %1, %2 offen offset:1024 sc1" :  : "v"(vB), "s"(rWS), "s"(2048u) : "memory"); SB();
  asm volatile("v_mfma_f32_16x16x32_bf16 %0, v[160:163], a[104:107], %0" : "+v"(g[0][2][0]) :  : ); SB();
  asm volatile("v_rcp_f32 %0, %1" : "=v"(gt[2]) : "v"(ge[2]) : ); SB();
  asm volatile("s_waitcnt lgkmcnt(2)\n\tv_mfma_f32_16x16x32_bf16 %0, v[160:163], a[108:111], %0" : "+v"(g[0][3][0]) :  : ); SB();
  asm volatile("v_rcp_f32 %0, %1" : "=v"(gt[3]) : "v"(ge[3]) : ); SB();
  asm volatile("v_mfma_f32_16x16x32_bf16 %0, v[164:167], a[96:99], %0" : "+v"(g[0][0][1]) :  : ); SB();
  asm volatile("v_mul_f32 %0, %0, %1" : "+v"(a1[0][1][0]) : "v"(gt[0]) : ); SB();
  asm volatile("v_mfma_f32_16x16x32_bf16 %0, v[164:167], a[100:103], %0" : "+v"(g[0][1][1]) :  : ); SB();
  asm volatile("buffer_load_dwordx4 v[232:235], %0, %1, %2 offen offset:0 sc1" :  : "v"(vB), "s"(rWS), "s"(67584u) : "memory"); SB();
  asm volatile("v_mul_f32 %0, %0, %1" : "+v"(a1[0][1][1]) : "v"(gt[1]) : ); SB();
  asm volatile("v_mfma_f32_16x16x32_bf16 %0, v[164:167], a[104:107], %0" : "+v"(g[0][2][1]) :  : ); SB();
  asm volatile("v_mul_f32 %0, %0, %1" : "+v"(a1[0][1][2]) : "v"(gt[2]) : ); SB();
  asm volatile("v_mfma_f32_16x16x32_bf16 %0, v[164:167], a[108:111], %0" : "+v"(g[0][3][1]) :  : ); SB();
  asm volatile("v_mul_f32 %0, %0, %1" : "+v"(a1[0][1][3]) : "v"(gt[3]) : ); SB();
  asm volatile("s_waitcnt vmcnt(15)" :  :  : "memory"); SB();
  asm volatile("v_mfma_f32_16x16x32_bf16 %0, v[168:171], a[112:115], %0" : "+v"(g[0][0][0]) :  : ); SB();
  asm volatile("buffer_load_dwordx4 v[236:239], %0, %1, %2 offen offset:1024 sc1" :  : "v"(vB), "s"(rWS), "s"(67584u) : "memory"); SB();
  asm volatile("v_mul_f32 %0, %1, %1" : "=v"(gt[0]) : "v"(a1[0][2][0]) : ); SB();
  asm volatile("v_mfma_f32_16x16x32_bf16 %0, v[168:171], a[116:119], %0" : "+v"(g[0][1][0]) :  : ); SB();
  asm volatile("v_xor_b32 %0, 131072, %0" : "+v"(vB) :  : "memory"); SB();
  asm volatile("v_mul_f32 %0, %1, %1" : "=v"(gt[1]) : "v"(a1[0][2][1]) : ); SB();
  asm volatile("v_mfma_f32_16x16x32_bf16 %0, v[168:171], a[120:123], %0" : "+v"(g[0][2][0]) :  : ); SB();
  asm volatile("v_mul_f32 %0, %1, %1" : "=v"(gt[2]) : "v"(a1[0][2][2]) : ); SB();
  asm volatile("v_mfma_f32_16x16x32_bf16 %0, v[168:171], a[124:127], %0" : "+v"(g[0][3][0]) :  : ); SB();
  asm volatile("v_mul_f32 %0, %1, %1" : "=v"(gt[3]) : "v"(a1[0][2][3]) : ); SB();
  asm volatile("v_mfma_f32_16x16x32_bf16 %0, v[172:175], a[112:115], %0" : "+v"(g[0][0][1]) :  : ); SB();
  asm volatile("buffer_load_dwordx4 v[240:243], %0, %1, %2 offen offset:0 sc1" :  : "v"(vB), "s"(rWS), "s"(2048u) : "memory"); SB();
  asm volatile("v_fma_f32 %0, %0, %1, %2" : "+v"(gt[0]) : "v"(k2[0]), "v"(k1[0]) : ); SB();
  asm volatile("v_mfma_f32_16x16x32_bf16 %0, v[172:175], a[116:119], %0" : "+v"(g[0][1][1]) :  : ); SB();
  asm volatile("v_fma_f32 %0, %0, %1, %2" : "+v"(gt[1]) : "v"(k2[0]), "v"(k1[0]) : ); SB();
  asm volatile("v_mfma_f32_16x16x32_bf16 %0, v[172:175], a[120:123], %0" : "+v"(g[0][2][1]) :  : ); SB();
  asm volatile("v_fma_f32 %0, %0, %1, %2" : "+v"(gt[2]) : "v"(k2[0]), "v"(k1[0]) : ); SB();
  asm volatile("v_mfma_f32_16x16x32_bf16 %0, v[172:175], a[124:127], %0" : "+v"(g[0][3][1]) :  : ); SB();
  asm volatile("buffer_load_dwordx4 v[244:247], %0, %1, %2 offen offset:1024 sc1" :  : "v"(vB), "s"(rWS), "s"(2048u) : "memory"); SB();
  asm volatile("s_waitcnt vmcnt(16)" :  :  : "memory"); SB();
  asm volatile("v_mfma_f32_16x16x32_bf16 %0, v[176:179], a[128:131], %0" : "+v"(g[0][0][0]) :  : ); SB();
  asm volatile("v_fma_f32 %0, %0, %1, %2" : "+v"(gt[3]) : "v"(k2[0]), "v"(k1[0]) : ); SB();
  asm volatile("v_mfma_f32_16x16x32_bf16 %0, v[176:179], a[132:135], %0" : "+v"(g[0][1][0]) :  : ); SB();
  asm volatile("v_mul_f32 %0, %0, %1" : "+v"(gt[0]) : "v"(a1[0][2][0]) : ); SB();
  asm volatile("v_mfma_f32_16x16x32_bf16 %0, v[176:179], a[136:139], %0" : "+v"(g[0][2][0]) :  : ); SB();
  asm volatile("v_mul_f32 %0, %0, %1" : "+v"(gt[1]) : "v"(a1[0][2][1]) : ); SB();
  asm volatile("s_waitcnt lgkmcnt(0)\n\tv_mfma_f32_16x16x32_bf16 %0, v[176:179], a[140:143], %0" : "+v"(g[0][3][0]) :  : ); SB();
  asm volatile("buffer_load_dwordx4 v[248:251], %0, %1, %2 offen offset:0 sc1" :  : "v"(vB), "s"(rWS), "s"(67584u) : "memory"); SB();
  asm volatile("v_mul_f32 %0, %0, %1" : "+v"(gt[2]) : "v"(a1[0][2][2]) : ); SB();
  asm volatile("v_mfma_f32_16x16x32_bf16 %0, v[180:183], a[128:131], %0" : "+v"(g[0][0][1]) :  : ); SB();
  asm volatile("v_mul_f32 %0, %0, %1" : "+v"(gt[3]) : "v"(a1[0][2][3]) : ); SB();
  asm volatile("v_mfma_f32_16x16x32_bf16 %0, v[180:183], a[132:135], %0" : "+v"(g[0][1][1]) :  : ); SB();
  asm volatile("v_exp_f32 %0, %1" : "=v"(ge[0]) : "v"(gt[0]) : ); SB();
  asm volatile("v_mfma_f32_16x16x32_bf16 %0, v[180:183], a[136:139], %0" : "+v"(g[0][2][1]) :  : ); SB();
  asm volatile("v_exp_f32 %0, %1" : "=v"(ge[1]) : "v"(gt[1]) : ); SB();
  asm volatile("v_mfma_f32_16x16x32_bf16 %0, v[180:183], a[140:143], %0" : "+v"(g[0][3][1]) :  : ); SB();
  asm volatile("v_exp_f32 %0, %1" : "=v"(ge[2]) : "v"(gt[2]) : ); SB();
  asm volatile("s_waitcnt vmcnt(15)" :  :  : "memory"); SB();
  asm volatile("v_mfma_f32_16x16x32_bf16 %0, v[184:187], a[144:147], %0" : "+v"(g[0][0][0]) :  : ); SB();
  asm volatile("v_exp_f32 %0, %1" : "=v"(ge[3]) : "v"(gt[3]) : ); SB();
  asm volatile("v_mfma_f32_16x16x32_bf16 %0, v[184:187], a[148:151], %0" : "+v"(g[0][1][0]) :  : ); SB();
  asm volatile("buffer_load_dwordx4 v[252:255], %0, %1, %2 offen offset:1024 sc1" :  : "v"(vB), "s"(rWS), "s"(67584u) : "memory"); SB();
  asm volatile("v_fma_f32 %0, %0, %1, %1" : "+v"(ge[0]) : "v"(xd[0]) : ); SB();
  asm volatile("v_mfma_f32_16x16x32_bf16 %0, v[184:187], a[152:155], %0" : "+v"(g[0][2][0]) :  : ); SB();
  asm volatile("v_xor_b32 %0, 917504, %0" : "+v"(vB) :  : "memory"); SB();
  asm volatile("v_fma_f32 %0, %0, %1, %1" : "+v"(ge[1]) : "v"(xd[0]) : ); SB();
  asm volatile("v_mfma_f32_16x16x32_bf16 %0, v[184:187], a[156:159], %0" : "+v"(g[0][3][0]) :  : ); SB();
  asm volatile("v_fma_f32 %0, %0, %1, %1" : "+v"(ge[2]) : "v"(xd[0]) : ); SB();
  asm volatile("v_mfma_f32_16x16x32_bf16 %0, v[188:191], a[144:147], %0" : "+v"(g[0][0][1]) :  : ); SB();
  asm volatile("buffer_load_dwordx4 v[160:163], %0, %1, %2 offen offset:0 sc1" :  : "v"(vB), "s"(rWS), "s"(2048u) : "memory"); SB();
  asm volatile("v_fma_f32 %0, %0, %1, %1" : "+v"(ge[3]) : "v"(xd[0]) : ); SB();
  asm volatile("v_mfma_f32_16x16x32_bf16 %0, v[188:191], a[148:151], %0" : "+v"(g[0][1][1]) :  : ); SB();
  asm volatile("v_rcp_f32 %0, %1" : "=v"(gt[0]) : "v"(ge[0]) : ); SB();
  asm volatile("v_mfma_f32_16x16x32_bf16 %0, v[188:191], a[152:155], %0" : "+v"(g[0][2][1]) :  : ); SB();
  asm volatile("v_rcp_f32 %0, %1" : "=v"(gt[1]) : "v"(ge[1]) : ); SB();
  asm volatile("v_mfma_f32_16x16x32_bf16 %0, v[188:191], a[156:159], %0" : "+v"(g[0][3][1]) :  : ); SB();
  asm volatile("v_rcp_f32 %0, %1" : "=v"(gt[2]) : "v"(ge[2]) : ); SB();
  ST(27);
  asm volatile("s_waitcnt vmcnt(15)" :  :  : "memory"); SB();
  asm volatile("v_mfma_f32_16x16x32_bf16 %0, v[192:195], a[160:163], %1" : "=&v"(g[1][0][0]) : "v"(bs4) : ); SB();
  asm volatile("s_barrier" :  :  : "memory"); SB();
  asm volatile("buffer_load_dwordx4 v[164:167], %0, %1, %2 offen offset:1024 sc1" :  : "v"(vB), "s"(rWS), "s"(2048u) : "memory"); SB();
  asm volatile("v_mfma_f32_16x16x32_bf16 %0, v[192:195], a[164:167], 0" : "=&v"(g[1][1][0]) :  : ); SB();
  asm volatile("v_rcp_f32 %0, %1" : "=v"(gt[3]) : "v"(ge[3]) : ); SB();
  asm volatile("v_mfma_f32_16x16x32_bf16 %0, v[192:195], a[168:171], 0" : "=&v"(g[1][2][0]) :  : ); SB();
  asm volatile("ds_write_b128 %0, %1 offset:0" :  : "v"(pw2), "v"(g[0][1][0]) : "memory"); SB();
  asm volatile("v_mfma_f32_16x16x32_bf16 %0, v[192:195], a[172:175], 0" : "=&v"(g[1][3][0]) :  : ); SB();
  asm volatile("buffer_load_dwordx4 v[168:171], %0, %1, %2 offen offset:0 sc1" :  : "v"(vB), "s"(rWS), "s"(67584u) : "memory"); SB();
  asm volatile("v_mul_f32 %0, %0, %1" : "+v"(a1[0][2][0]) : "v"(gt[0]) : ); SB();
  asm volatile("v_mfma_f32_16x16x32_bf16 %0, v[196:199], a[160:163], %1" : "=&v"(g[1][0][1]) : "v"(bs4) : ); SB();
  asm volatile("buffer_load_dwordx4 v[172:175], %0, %1, %2 offen offset:1024 sc1" :  : "v"(vB), "s"(rWS), "s"(67584u) : "memory"); SB();
  asm volatile("v_mul_f32 %0, %0, %1" : "+v"(a1[0][2][1]) : "v"(gt[1]) : ); SB();
  asm volatile("v_mfma_f32_16x16x32_bf16 %0, v[196:199], a[164:167], 0" : "=&v"(g[1][1][1]) :  : ); SB();
  asm volatile("ds_write_b128 %0, %1 offset:1024" :  : "v"(pw2), "v"(g[0][1][1]) : "memory"); SB();
  asm volatile("v_mfma_f32_16x16x32_bf16 %0, v[196:199], a[168:171], 0" : "=&v"(g[1][2][1]) :  : ); SB();
  asm volatile("v_xor_b32 %0, 131072, %0" : "+v"(vB) :  : "memory"); SB();
  asm volatile("v_mul_f32 %0, %0, %1" : "+v"(a1[0][2][2]) : "v"(gt[2]) : ); SB();
  asm volatile("v_mfma_f32_16x16x32_bf16 %0, v[196:199], a[172:175], 0" : "=&v"(g[1][3][1]) :  : ); SB();
  asm volatile("buffer_load_dwordx4 v[176:179], %0, %1, %2 offen offset:0 sc1" :  : "v"(vB), "s"(rWS), "s"(2048u) : "memory"); SB();
  asm volatile("s_waitcnt vmcnt(17)" :  :  : "memory"); SB();
  asm volatile("v_mfma_f32_16x16x32_bf16 %0, v[200:203], a[176:179], %0" : "+v"(g[1][0][0]) :  : ); SB();
  asm volatile("ds_write_b128 %0, %1 offset:2048" :  : "v"(pw2), "v"(g[0][2][0]) : "memory"); SB();
  asm volatile("v_mfma_f32_16x16x32_bf16 %0, v[200:203], a[180:183], %0" : "+v"(g[1][1][0]) :  : ); SB();
  asm volatile("buffer_load_dwordx4 v[180:183], %0, %1, %2 offen offset:1024 sc1" :  : "v"(vB), "s"(rWS), "s"(2048u) : "memory"); SB();
  asm volatile("v_mul_f32 %0, %0, %1" : "+v"(a1[0][2][3]) : "v"(gt[3]) : ); SB();
  asm volatile("v_mfma_f32_16x16x32_bf16 %0, v[200:203], a[184:187], %0" : "+v"(g[1][2][0]) :  : ); SB();
  asm volatile("v_mul_f32 %0, %1, %1" : "=v"(gt[0]) : "v"(a1[1][0][0]) : ); SB();
  asm volatile("v_mfma_f32_16x16x32_bf16 %0, v[200:203], a[188:191], %0" : "+v"(g[1][3][0]) :  : ); SB();
  asm volatile("ds_write_b128 %0, %1 offset:3072" :  : "v"(pw2), "v"(g[0][2][1]) : "memory"); SB();
  asm volatile("v_mfma_f32_16x16x32_bf16 %0, v[204:207], a[176:179], %0" : "+v"(g[1][0][1]) :  : ); SB();
  asm volatile("buffer_load_dwordx4 v[184:187], %0, %1, %2 offen offset:0 sc1" :  : "v"(vB), "s"(rWS), "s"(67584u) : "memory"); SB();
  asm volatile("v_mul_f32 %0, %1, %1" : "=v"(gt[1]) : "v"(a1[1][0][1]) : ); SB();
  asm volatile("v_mfma_f32_16x16x32_bf16 %0, v[204:207], a[180:183], %0" : "+v"(g[1][1][1]) :  : ); SB();
  asm volatile("v_mul_f32 %0, %1, %1" : "=v"(gt[2]) : "v"(a1[1][0][2]) : ); SB();
  asm volatile("v_mfma_f32_16x16x32_bf16 %0, v[204:207], a[184:187], %0" : "+v"(g[1][2][1]) :  : ); SB();
  asm volatile("ds_write_b128 %0, %1 offset:4096" :  : "v"(pw2), "v"(g[0][3][0]) : "memory"); SB();
  asm volatile("v_mfma_f32_16x16x32_bf16 %0, v[204:207], a[188:191], %0" : "+v"(g[1][3][1]) :  : ); SB();
  asm volatile("buffer_load_dwordx4 v[188:191], %0, %1, %2 offen offset:1024 sc1" :  : "v"(vB), "s"(rWS), "s"(67584u) : "memory"); SB();
  asm volatile("s_waitcnt vmcnt(18)" :  :  : "memory"); SB();
  asm volatile("v_mfma_f32_16x16x32_bf16 %0, v[208:211], a[192:195], %0" : "+v"(g[1][0][0]) :  : ); SB();
  asm volatile("v_xor_b32 %0, 393216, %0" : "+v"(vB) :  : "memory"); SB();
  asm volatile("v_mul_f32 %0, %1, %1" : "=v"(gt[3]) : "v"(a1[1][0][3]) : ); SB();
  asm volatile("v_mfma_f32_16x16x32_bf16 %0, v[208:211], a[196:199], %0" : "+v"(g[1][1][0]) :  : ); SB();
  asm volatile("ds_write_b128 %0, %1 offset:5120" :  : "v"(pw2), "v"(g[0][3][1]) : "memory"); SB();
  asm volatile("v_mfma_f32_16x16x32_bf16 %0, v[208:211], a[200:203], %0" : "+v"(g[1][2][0]) :  : ); SB();
  asm volatile("buffer_load_dwordx4 v[192:195], %0, %1, %2 offen offset:0 sc1" :  : "v"(vB), "s"(rWS), "s"(2048u) : "memory"); SB();
  asm volatile("v_fma_f32 %0, %0, %1, %2" : "+v"(gt[0]) : "v"(k2[0]), "v"(k1[0]) : ); SB();
  asm volatile("v_mfma_f32_16x16x32_bf16 %0, v[208:211], a[204:207], %0" : "+v"(g[1][3][0]) :  : ); SB();
  asm volatile("buffer_load_dwordx4 v[196:199], %0, %1, %2 offen offset:1024 sc1" :  : "v"(vB), "s"(rWS), "s"(2048u) : "memory"); SB();
  asm volatile("v_fma_f32 %0, %0, %1, %2" : "+v"(gt[1]) : "v"(k2[0]), "v"(k1[0]) : ); SB();
  asm volatile("v_mfma_f32_16x16x32_bf16 %0, v[212:215], a[192:195], %0" : "+v"(g[1][0][1]) :  : ); SB();
  asm volatile("s_waitcnt lgkmcnt(0)\n\ts_barrier" :  :  : "memory"); SB();
  asm volatile("ds_read_b128 %0, %1 offset:0" : "=&v"(pr[0]) : "v"(pe[0]) : "memory"); SB();
  asm volatile("v_mfma_f32_16x16x32_bf16 %0, v[212:215], a[196:199], %0" : "+v"(g[1][1][1]) :  : ); SB();
  asm volatile("ds_read_b128 %0, %1 offset:1024" : "=&v"(pr[1]) : "v"(pe[0]) : "memory"); SB();
  asm volatile("v_fma_f32 %0, %0, %1, %2" : "+v"(gt[2]) : "v"(k2[0]), "v"(k1[0]) : ); SB();
  asm volatile("v_mfma_f32_16x16x32_bf16 %0, v[212:215], a[200:203], %0" : "+v"(g[1][2][1]) :  : ); SB();
  asm volatile("ds_read_b128 %0, %1 offset:2048" : "=&v"(pr[2]) : "v"(pe[1]) : "memory"); SB();
  asm volatile("v_fma_f32 %0, %0, %1, %2" : "+v"(gt[3]) : "v"(k2[0]), "v"(k1[0]) : ); SB();
  asm volatile("v_mfma_f32_16x16x32_bf16 %0, v[212:215], a[204:207], %0" : "+v"(g[1][3][1]) :  : ); SB();
  asm volatile("ds_read_b128 %0, %1 offset:3072" : "=&v"(pr[3]) : "v"(pe[1]) : "memory"); SB();
  asm volatile("s_waitcnt vmcnt(18)" :  :  : "memory"); SB();
  asm volatile("v_mfma_f32_16x16x32_bf16 %0, v[216:219], a[208:211], %0" : "+v"(g[1][0][0]) :  : ); SB();
  asm volatile("ds_read_b128 %0, %1 offset:4096" : "=&v"(pr[4]) : "v"(pe[2]) : "memory"); SB();
  asm volatile("v_mul_f32 %0, %0, %1" : "+v"(gt[0]) : "v"(a1[1][0][0]) : ); SB();
  asm volatile("v_mfma_f32_16x16x32_bf16 %0, v[216:219], a[212:215], %0" : "+v"(g[1][1][0]) :  : ); SB();
  asm volatile("ds_read_b128 %0, %1 offset:5120" : "=&v"(pr[5]) : "v"(pe[2]) : "memory"); SB();
  asm volatile("v_mul_f32 %0, %0, %1" : "+v"(gt[1]) : "v"(a1[1][0][1]) : ); SB();
  asm volatile("v_mfma_f32_16x16x32_bf16 %0, v[216:219], a[216:219], %0" : "+v"(g[1][2][0]) :  : ); SB();
  asm volatile("buffer_load_dwordx4 v[200:203], %0, %1, %2 offen offset:0 sc1" :  : "v"(vB), "s"(rWS), "s"(67584u) : "memory"); SB();
  asm volatile("v_mul_f32 %0, %0, %1" : "+v"(gt[2]) : "v"(a1[1][0][2]) : ); SB();
  asm volatile("v_mfma_f32_16x16x32_bf16 %0, v[216:219], a[220:223], %0" : "+v"(g[1][3][0]) :  : ); SB();
  asm volatile("buffer_load_dwordx4 v[204:207], %0, %1, %2 offen offset:1024 sc1" :  : "v"(vB), "s"(rWS), "s"(67584u) : "memory"); SB();
  asm volatile("v_mul_f32 %0, %0, %1" : "+v"(gt[3]) : "v"(a1[1][0][3]) : ); SB();
  asm volatile("v_mfma_f32_16x16x32_bf16 %0, v[220:223], a[208:211], %0" : "+v"(g[1][0][1]) :  : ); SB();
  asm volatile("v_xor_b32 %0, 131072, %0" : "+v"(vB) :  : "memory"); SB();
  asm volatile("v_mfma_f32_16x16x32_bf16 %0, v[220:223], a[212:215], %0" : "+v"(g[1][1][1]) :  : ); SB();
  asm volatile("buffer_load_dwordx4 v[208:211], %0, %1, %2 offen offset:0 sc1" :  : "v"(vB), "s"(rWS), "s"(2048u) : "memory"); SB();
  asm volatile("v_mfma_f32_16x16x32_bf16 %0, v[220:223], a[216:219], %0" : "+v"(g[1][2][1]) :  : ); SB();
  asm volatile("buffer_load_dwordx4 v[212:215], %0, %1, %2 offen offset:1024 sc1" :  : "v"(vB), "s"(rWS), "s"(2048u) : "memory"); SB();
  asm volatile("v_mfma_f32_16x16x32_bf16 %0, v[220:223], a[220:223], %0" : "+v"(g[1][3][1]) :  : ); SB();
  asm volatile("s_waitcnt lgkmcnt(0)" : "+v"(pr[0]), "+v"(pr[1]), "+v"(pr[2]), "+v"(pr[3]), "+v"(pr[4]), "+v"(pr[5]) :  : "memory"); SB();
  asm volatile("v_add_f32 %0, %0, %1" : "+v"(pr[0][0]) : "v"(g[0][0][0][0]) : ); SB();
  asm volatile("s_waitcnt vmcnt(20)" :  :  : "memory"); SB();
  asm volatile("v_mfma_f32_16x16x32_bf16 %0, v[224:227], a[224:227], %0" : "+v"(g[1][0][0]) :  : ); SB();
  asm volatile("buffer_load_dwordx4 v[216:219], %0, %1, %2 offen offset:0 sc1" :  : "v"(vB), "s"(rWS), "s"(67584u) : "memory"); SB();
  asm volatile("v_add_f32 %0, %0, %1" : "+v"(pr[0][1]) : "v"(g[0][0][0][1]) : ); SB();
  asm volatile("v_mfma_f32_16x16x32_bf16 %0, v[224:227], a[228:231], %0" : "+v"(g[1][1][0]) :  : ); SB();
  asm volatile("v_add_f32 %0, %0, %1" : "+v"(pr[0][2]) : "v"(g[0][0][0][2]) : ); SB();
  asm volatile("v_mfma_f32_16x16x32_bf16 %0, v[224:227], a[232:235], %0" : "+v"(g[1][2][0]) :  : ); SB();
  asm volatile("v_add_f32 %0, %0, %1" : "+v"(pr[0][3]) : "v"(g[0][0][0][3]) : ); SB();
  asm volatile("v_mfma_f32_16x16x32_bf16 %0, v[224:227], a[236:239], %0" : "+v"(g[1][3][0]) :  : ); SB();
  asm volatile("v_add_f32 %0, %0, %1" : "+v"(pr[1][0]) : "v"(g[0][0][1][0]) : ); SB();
  asm volatile("v_mfma_f32_16x16x32_bf16 %0, v[228:231], a[224:227], %0" : "+v"(g[1][0][1]) :  : ); SB();
  asm volatile("buffer_load_dwordx4 v[220:223], %0, %1, %2 offen offset:1024 sc1" :  : "v"(vB), "s"(rWS), "s"(67584u) : "memory"); SB();
  asm volatile("v_add_f32 %0, %0, %1" : "+v"(pr[1][1]) : "v"(g[0][0][1][1]) : ); SB();
  asm volatile("v_mfma_f32_16x16x32_bf16 %0, v[228:231], a[228:231], %0" : "+v"(g[1][1][1]) :  : ); SB();
  asm volatile("v_xor_b32 %0, 917504, %0" : "+v"(vB) :  : "memory"); SB();
  asm volatile("v_add_f32 %0, %0, %1" : "+v"(pr[1][2]) : "v"(g[0][0][1][2]) : ); SB();
  asm volatile("v_mfma_f32_16x16x32_bf16 %0, v[228:231], a[232:235], %0" : "+v"(g[1][2][1]) :  : ); SB();
  asm volatile("v_add_f32 %0, %0, %1" : "+v"(pr[1][3]) : "v"(g[0][0][1][3]) : ); SB();
  asm volatile("v_mfma_f32_16x16x32_bf16 %0, v[228:231], a[236:239], %0" : "+v"(g[1][3][1]) :  : ); SB();
  asm volatile("v_add_f32 %0, %0, %1" : "+v"(pr[2][0]) : "v"(pr[4][0]) : ); SB();
  asm volatile("s_waitcnt vmcnt(20)" :  :  : "memory"); SB();
  asm volatile("v_mfma_f32_16x16x32_bf16 %0, v[232:235], a[240:243], %0" : "+v"(g[1][0][0]) :  : ); SB();
  asm volatile("buffer_load_dwordx4 v[224:227], %0, %1, %2 offen offset:0 sc1" :  : "v"(vB), "s"(rWS), "s"(4096u) : "memory"); SB();
  asm volatile("v_add_f32 %0, %0, %1" : "+v"(pr[2][1]) : "v"(pr[4][1]) : ); SB();
  asm volatile("v_mfma_f32_16x16x32_bf16 %0, v[232:235], a[244:247], %0" : "+v"(g[1][1][0]) :  : ); SB();
  asm volatile("v_add_f32 %0, %0, %1" : "+v"(pr[2][2]) : "v"(pr[4][2]) : ); SB();
  asm volatile("v_mfma_f32_16x16x32_bf16 %0, v[232:235], a[248:251], %0" : "+v"(g[1][2][0]) :  : ); SB();
  asm volatile("v_add_f32 %0, %0, %1" : "+v"(pr[2][3]) : "v"(pr[4][3]) : ); SB();
  asm volatile("v_mfma_f32_16x16x32_bf16 %0, v[232:235], a[252:255], %0" : "+v"(g[1][3][0]) :  : ); SB();
  asm volatile("buffer_load_dwordx4 v[228:231], %0, %1, %2 offen offset:1024 sc1" :  : "v"(vB), "s"(rWS), "s"(4096u) : "memory"); SB();
  asm volatile("v_add_f32 %0, %0, %1" : "+v"(pr[3][0]) : "v"(pr[5][0]) : ); SB();
  asm volatile("v_mfma_f32_16x16x32_bf16 %0, v[236:239], a[240:243], %0" : "+v"(g[1][0][1]) :  : ); SB();
  asm volatile("v_add_f32 %0, %0, %1" : "+v"(pr[3][1]) : "v"(pr[5][1]) : ); SB();
  asm volatile("v_mfma_f32_16x16x32_bf16 %0, v[236:239], a[244:247], %0" : "+v"(g[1][1][1]) :  : ); SB();
  asm volatile("v_add_f32 %0, %0, %1" : "+v"(pr[3][2]) : "v"(pr[5][2]) : ); SB();
  asm volatile("v_mfma_f32_16x16x32_bf16 %0, v[236:239], a[248:251], %0" : "+v"(g[1][2][1]) :  : ); SB();
  asm volatile("v_add_f32 %0, %0, %1" : "+v"(pr[3][3]) : "v"(pr[5][3]) : ); SB();
  asm volatile("v_mfma_f32_16x16x32_bf16 %0, v[236:239], a[252:255], %0" : "+v"(g[1][3][1]) :  : ); SB();
  asm volatile("v_add_f32 %0, %0, %1" : "+v"(pr[0][0]) : "v"(pr[2][0]) : ); SB();
  asm volatile("s_waitcnt vmcnt(20)" :  :  : "memory"); SB();
  asm volatile("v_mfma_f32_16x16x32_bf16 %0, v[240:243], a[0:3], %0" : "+v"(g[1][0][0]) :  : ); SB();
  asm volatile("buffer_load_dwordx4 v[232:235], %0, %1, %2 offen offset:0 sc1" :  : "v"(vB), "s"(rWS), "s"(69632u) : "memory"); SB();
  asm volatile("v_add_f32 %0, %0, %1" : "+v"(pr[0][1]) : "v"(pr[2][1]) : ); SB();
  asm volatile("v_mfma_f32_16x16x32_bf16 %0, v[240:243], a[4:7], %0" : "+v"(g[1][1][0]) :  : ); SB();
  asm volatile("v_add_f32 %0, %0, %1" : "+v"(pr[0][2]) : "v"(pr[2][2]) : ); SB();
  asm volatile("v_mfma_f32_16x16x32_bf16 %0, v[240:243], a[8:11], %0" : "+v"(g[1][2][0]) :  : ); SB();
  asm volatile("buffer_load_dwordx4 v[236:239], %0, %1, %2 offen offset:1024 sc1" :  : "v"(vB), "s"(rWS), "s"(69632u) : "memory"); SB();
  asm volatile("v_add_f32 %0, %0, %1" : "+v"(pr[0][3]) : "v"(pr[2][3]) : ); SB();
  asm volatile("v_mfma_f32_16x16x32_bf16 %0, v[240:243], a[12:15], %0" : "+v"(g[1][3][0]) :  : ); SB();
  asm volatile("v_xor_b32 %0, 131072, %0" : "+v"(vB) :  : "memory"); SB();
  asm volatile("v_add_f32 %0, %0, %1" : "+v"(pr[1][0]) : "v"(pr[3][0]) : ); SB();
  asm volatile("v_mfma_f32_16x16x32_bf16 %0, v[244:247], a[0:3], %0" : "+v"(g[1][0][1]) :  : ); SB();
  asm volatile("v_add_f32 %0, %0, %1" : "+v"(pr[1][1]) : "v"(pr[3][1]) : ); SB();
  asm volatile("v_mfma_f32_16x16x32_bf16 %0, v[244:247], a[4:7], %0" : "+v"(g[1][1][1]) :  : ); SB();
  asm volatile("v_add_f32 %0, %0, %1" : "+v"(pr[1][2]) : "v"(pr[3][2]) : ); SB();
  asm volatile("v_mfma_f32_16x16x32_bf16 %0, v[244:247], a[8:11], %0" : "+v"(g[1][2][1]) :  : ); SB();
  asm volatile("v_add_f32 %0, %0, %1" : "+v"(pr[1][3]) : "v"(pr[3][3]) : ); SB();
  asm volatile("v_mfma_f32_16x16x32_bf16 %0, v[244:247], a[12:15], %0" : "+v"(g[1][3][1]) :  : ); SB();
  asm volatile("v_mul_f32 %0, %0, %1" : "+v"(pr[0][0]) : "v"(a1[0][0][0]) : ); SB();
  asm volatile("s_waitcnt vmcnt(20)" :  :  : "memory"); SB();
  asm volatile("v_mfma_f32_16x16x32_bf16 %0, v[248:251], a[16:19], %0" : "+v"(g[1][0][0]) :  : ); SB();
  asm volatile("buffer_load_dwordx4 v[240:243], %0, %1, %2 offen offset:0 sc1" :  : "v"(vB), "s"(rWS), "s"(4096u) : "memory"); SB();
  asm volatile("v_mul_f32 %0, %0, %1" : "+v"(pr[0][1]) : "v"(a1[0][0][1]) : ); SB();
  asm volatile("v_mfma_f32_16x16x32_bf16 %0, v[248:251], a[20:23], %0" : "+v"(g[1][1][0]) :  : ); SB();
  asm volatile("buffer_load_dwordx4 v[244:247], %0, %1, %2 offen offset:1024 sc1" :  : "v"(vB), "s"(rWS), "s"(4096u) : "memory"); SB();
  asm volatile("v_mul_f32 %0, %0, %1" : "+v"(pr[0][2]) : "v"(a1[0][0][2]) : ); SB();
  asm volatile("v_mfma_f32_16x16x32_bf16 %0, v[248:251], a[24:27], %0" : "+v"(g[1][2][0]) :  : ); SB();
  asm volatile("v_mul_f32 %0, %0, %1" : "+v"(pr[0][3]) : "v"(a1[0][0][3]) : ); SB();
  asm volatile("v_mfma_f32_16x16x32_bf16 %0, v[248:251], a[28:31], %0" : "+v"(g[1][3][0]) :  : ); SB();
  asm volatile("v_mul_f32 %0, %0, %1" : "+v"(pr[1][0]) : "v"(a1[0][1][0]) : ); SB();
  asm volatile("v_mfma_f32_16x16x32_bf16 %0, v[252:255], a[16:19], %0" : "+v"(g[1][0][1]) :  : ); SB();
  asm volatile("v_mul_f32 %0, %0, %1" : "+v"(pr[1][1]) : "v"(a1[0][1][1]) : ); SB();
  asm volatile("v_mfma_f32_16x16x32_bf16 %0, v[252:255], a[20:23], %0" : "+v"(g[1][1][1]) :  : ); SB();
  asm volatile("v_mul_f32 %0, %0, %1" : "+v"(pr[1][2]) : "v"(a1[0][1][2]) : ); SB();
  asm volatile("v_mfma_f32_16x16x32_bf16 %0, v[252:255], a[24:27], %0" : "+v"(g[1][2][1]) :  : ); SB();
  asm volatile("v_mul_f32 %0, %0, %1" : "+v"(pr[1][3]) : "v"(a1[0][1][3]) : ); SB();
  asm volatile("v_mfma_f32_16x16x32_bf16 %0, v[252:255], a[28:31], %0" : "+v"(g[1][3][1]) :  : ); SB();
  asm volatile("v_cvt_pk_bf16_f32 %0, %1, %2" : "=v"(ho[0][0]) : "v"(pr[0][0]), "v"(pr[0][1]) : ); SB();
  ST(28);
  asm volatile("s_waitcnt vmcnt(20)" :  :  : "memory"); SB();
  asm volatile("v_mfma_f32_16x16x32_bf16 %0, v[160:163], a[32:35], %0" : "+v"(g[1][0][0]) :  : ); SB();
  vH_ = vH; asm volatile("" : "+v"(vH_)); SB();
  asm volatile("v_mfma_f32_16x16x32_bf16 %0, v[160:163], a[36:39], %0" : "+v"(g[1][1][0]) :  : ); SB();
  vO = W_WO + (cb * 120 + w * 30) * 1024 + l16; asm volatile("" : "+v"(vO)); SB();
  asm volatile("v_mfma_f32_16x16x32_bf16 %0, v[160:163], a[40:43], %0" : "+v"(g[1][2][0]) :  : ); SB();
  vFH = W_FH + rb * 256 + w * 64 + (lane & 7) * 8; asm volatile("" : "+v"(vFH)); SB();
  asm volatile("v_mfma_f32_16x16x32_bf16 %0, v[160:163], a[44:47], %0" : "+v"(g[1][3][0]) :  : ); SB();
  vHr = W_HH + (rb * 96 + w * 6) * 1024 + l16; asm volatile("" : "+v"(vHr)); SB();
  asm volatile("v_mfma_f32_16x16x32_bf16 %0, v[164:167], a[32:35], %0" : "+v"(g[1][0][1]) :  : ); SB();
  vX = (rb * 64 + w * 16 + l15) * 1280; asm volatile("" : "+v"(vX)); SB();
  asm volatile("v_mfma_f32_16x16x32_bf16 %0, v[164:167], a[36:39], %0" : "+v"(g[1][1][1]) :  : ); SB();
  vX = vX + cb * 160 + 8 * g4; asm volatile("" : "+v"(vX)); SB();
  asm volatile("v_mfma_f32_16x16x32_bf16 %0, v[164:167], a[40:43], %0" : "+v"(g[1][2][1]) :  : ); SB();
  sFH = W_FH + ((rb * 8 + cb) * 4 + w) * 8; asm volatile("" : "+s"(sFH)); SB();
  asm volatile("v_mfma_f32_16x16x32_bf16 %0, v[164:167], a[44:47], %0" : "+v"(g[1][3][1]) :  : ); SB();
  hso[0] = (w ^ 0) * 24576; asm volatile("" : "+s"(hso[0])); SB();
  asm volatile("s_waitcnt vmcnt(18)" :  :  : "memory"); SB();
  asm volatile("v_mfma_f32_16x16x32_bf16 %0, v[168:171], a[48:51], %0" : "+v"(g[1][0][0]) :  : ); SB();
  hso[1] = hso[0] + 3072; asm volatile("" : "+s"(hso[1])); SB();
  hso[2] = (w ^ 1) * 24576; asm volatile("" : "+s"(hso[2])); SB();
  asm volatile("v_mfma_f32_16x16x32_bf16 %0, v[168:171], a[52:55], %0" : "+v"(g[1][1][0]) :  : ); SB();
  hso[3] = hso[2] + 3072; asm volatile("" : "+s"(hso[3])); SB();
  hso[4] = (w ^ 2) * 24576; asm volatile("" : "+s"(hso[4])); SB();
  asm volatile("v_mfma_f32_16x16x32_bf16 %0, v[168:171], a[56:59], %0" : "+v"(g[1][2][0]) :  : ); SB();
  hso[5] = hso[4] + 3072; asm volatile("" : "+s"(hso[5])); SB();
  hso[6] = (w ^ 3) * 24576; asm volatile("" : "+s"(hso[6])); SB();
  asm volatile("v_mfma_f32_16x16x32_bf16 %0, v[168:171], a[60:63], %0" : "+v"(g[1][3][0]) :  : ); SB();
  hso[7] = hso[6] + 3072; asm volatile("" : "+s"(hso[7])); SB();
  asm volatile("buffer_load_dwordx4 v[248:251], %0, %1, %2 offen offset:0 sc1" :  : "v"(vB), "s"(rWS), "s"(69632u) : "memory"); SB();
  asm volatile("v_mfma_f32_16x16x32_bf16 %0, v[172:175], a[48:51], %0" : "+v"(g[1][0][1]) :  : ); SB();
  asm volatile("buffer_load_dwordx4 v[252:255], %0, %1, %2 offen offset:1024 sc1" :  : "v"(vB), "s"(rWS), "s"(69632u) : "memory"); SB();
  asm volatile("v_cvt_pk_bf16_f32 %0, %1, %2" : "=v"(ho[0][1]) : "v"(pr[0][2]), "v"(pr[0][3]) : ); SB();
  asm volatile("v_mfma_f32_16x16x32_bf16 %0, v[172:175], a[52:55], %0" : "+v"(g[1][1][1]) :  : ); SB();
  asm volatile("v_xor_b32 %0, 393216, %0" : "+v"(vB) :  : "memory"); SB();
  asm volatile("v_cvt_pk_bf16_f32 %0, %1, %2" : "=v"(ho[0][2]) : "v"(pr[1][0]), "v"(pr[1][1]) : ); SB();
  asm volatile("v_mfma_f32_16x16x32_bf16 %0, v[172:175], a[56:59], %0" : "+v"(g[1][2][1]) :  : ); SB();
  asm volatile("buffer_load_dwordx4 v[160:163], %0, %1, %2 offen offset:0 sc1" :  : "v"(vB), "s"(rWS), "s"(4096u) : "memory"); SB();
  asm volatile("v_cvt_pk_bf16_f32 %0, %1, %2" : "=v"(ho[0][3]) : "v"(pr[1][2]), "v"(pr[1][3]) : ); SB();
  asm volatile("v_mfma_f32_16x16x32_bf16 %0, v[172:175], a[60:63], %0" : "+v"(g[1][3][1]) :  : ); SB();
  asm volatile("buffer_load_dwordx4 v[164:167], %0, %1, %2 offen offset:1024 sc1" :  : "v"(vB), "s"(rWS), "s"(4096u) : "memory"); SB();
  asm volatile("s_waitcnt vmcnt(20)" :  :  : "memory"); SB();
  asm volatile("v_mfma_f32_16x16x32_bf16 %0, v[176:179], a[64:67], %0" : "+v"(g[1][0][0]) :  : ); SB();
  asm volatile("v_exp_f32 %0, %1" : "=v"(ge[0]) : "v"(gt[0]) : ); SB();
  asm volatile("v_mfma_f32_16x16x32_bf16 %0, v[176:179], a[68:71], %0" : "+v"(g[1][1][0]) :  : ); SB();
  asm volatile("v_exp_f32 %0, %1" : "=v"(ge[1]) : "v"(gt[1]) : ); SB();
  asm volatile("v_mfma_f32_16x16x32_bf16 %0, v[176:179], a[72:75], %0" : "+v"(g[1][2][0]) :  : ); SB();
  asm volatile("buffer_load_dwordx4 v[168:171], %0, %1, %2 offen offset:0 sc1" :  : "v"(vB), "s"(rWS), "s"(69632u) : "memory"); SB();
  asm volatile("v_mfma_f32_16x16x32_bf16 %0, v[176:179], a[76:79], %0" : "+v"(g[1][3][0]) :  : ); SB();
  asm volatile("v_exp_f32 %0, %1" : "=v"(ge[2]) : "v"(gt[2]) : ); SB();
  asm volatile("v_mfma_f32_16x16x32_bf16 %0, v[180:183], a[64:67], %0" : "+v"(g[1][0][1]) :  : ); SB();
  asm volatile("v_exp_f32 %0, %1" : "=v"(ge[3]) : "v"(gt[3]) : ); SB();
  asm volatile("v_mfma_f32_16x16x32_bf16 %0, v[180:183], a[68:71], %0" : "+v"(g[1][1][1]) :  : ); SB();
  asm volatile("buffer_load_dwordx4 v[172:175], %0, %1, %2 offen offset:1024 sc1" :  : "v"(vB), "s"(rWS), "s"(69632u) : "memory"); SB();
  asm volatile("v_fma_f32 %0, %0, %1, %1" : "+v"(ge[0]) : "v"(xd[0]) : ); SB();
  asm volatile("v_mfma_f32_16x16x32_bf16 %0, v[180:183], a[72:75], %0" : "+v"(g[1][2][1]) :  : ); SB();
  asm volatile("v_xor_b32 %0, 131072, %0" : "+v"(vB) :  : "memory"); SB();
  asm volatile("v_fma_f32 %0, %0, %1, %1" : "+v"(ge[1]) : "v"(xd[0]) : ); SB();
  asm volatile("v_mfma_f32_16x16x32_bf16 %0, v[180:183], a[76:79], %0" : "+v"(g[1][3][1]) :  : ); SB();
  asm volatile("v_fma_f32 %0, %0, %1, %1" : "+v"(ge[2]) : "v"(xd[0]) : ); SB();
  asm volatile("s_waitcnt vmcnt(20)" :  :  : "memory"); SB();
  asm volatile("v_mfma_f32_16x16x32_bf16 %0, v[184:187], a[80:83], %0" : "+v"(g[1][0][0]) :  : ); SB();
  asm volatile("v_fma_f32 %0, %0, %1, %1" : "+v"(ge[3]) : "v"(xd[0]) : ); SB();
  asm volatile("v_mfma_f32_16x16x32_bf16 %0, v[184:187], a[84:87], %0" : "+v"(g[1][1][0]) :  : ); SB();
  asm volatile("buffer_load_dwordx4 v[176:179], %0, %1, %2 offen offset:0 sc1" :  : "v"(vB), "s"(rWS), "s"(4096u) : "memory"); SB();
  asm volatile("v_mfma_f32_16x16x32_bf16 %0, v[184:187], a[88:91], %0" : "+v"(g[1][2][0]) :  : ); SB();
  asm volatile("v_rcp_f32 %0, %1" : "=v"(gt[0]) : "v"(ge[0]) : ); SB();
  asm volatile("v_mfma_f32_16x16x32_bf16 %0, v[184:187], a[92:95], %0" : "+v"(g[1][3][0]) :  : ); SB();
  asm volatile("v_rcp_f32 %0, %1" : "=v"(gt[1]) : "v"(ge[1]) : ); SB();
  asm volatile("v_mfma_f32_16x16x32_bf16 %0, v[188:191], a[80:83], %0" : "+v"(g[1][0][1]) :  : ); SB();
  asm volatile("buffer_load_dwordx4 v[180:183], %0, %1, %2 offen offset:1024 sc1" :  : "v"(vB), "s"(rWS), "s"(4096u) : "memory"); SB();
  asm volatile("v_mfma_f32_16x16x32_bf16 %0, v[188:191], a[84:87], %0" : "+v"(g[1][1][1]) :  : ); SB();
  asm volatile("v_rcp_f32 %0, %1" : "=v"(gt[2]) : "v"(ge[2]) : ); SB();
  asm volatile("v_mfma_f32_16x16x32_bf16 %0, v[188:191], a[88:91], %0" : "+v"(g[1][2][1]) :  : ); SB();
  asm volatile("v_rcp_f32 %0, %1" : "=v"(gt[3]) : "v"(ge[3]) : ); SB();
  asm volatile("v_mfma_f32_16x16x32_bf16 %0, v[188:191], a[92:95], %0" : "+v"(g[1][3][1]) :  : ); SB();
  asm volatile("v_mul_f32 %0, %0, %1" : "+v"(a1[1][0][0]) : "v"(gt[0]) : ); SB();
  asm volatile("s_waitcnt vmcnt(20)" :  :  : "memory"); SB();
  asm volatile("v_mfma_f32_16x16x32_bf16 %0, v[192:195], a[96:99], %0" : "+v"(g[1][0][0]) :  : ); SB();
  asm volatile("buffer_load_dwordx4 v[184:187], %0, %1, %2 offen offset:0 sc1" :  : "v"(vB), "s"(rWS), "s"(69632u) : "memory"); SB();
  asm volatile("v_mul_f32 %0, %0, %1" : "+v"(a1[1][0][1]) : "v"(gt[1]) : ); SB();
  asm volatile("v_mfma_f32_16x16x32_bf16 %0, v[192:195], a[100:103], %0" : "+v"(g[1][1][0]) :  : ); SB();
  asm volatile("v_mul_f32 %0, %0, %1" : "+v"(a1[1][0][2]) : "v"(gt[2]) : ); SB();
  asm volatile("v_mfma_f32_16x16x32_bf16 %0, v[192:195], a[104:107], %0" : "+v"(g[1][2][0]) :  : ); SB();
  asm volatile("v_mul_f32 %0, %0, %1" : "+v"(a1[1][0][3]) : "v"(gt[3]) : ); SB();
  asm volatile("v_mfma_f32_16x16x32_bf16 %0, v[192:195], a[108:111], %0" : "+v"(g[1][3][0]) :  : ); SB();
  asm volatile("v_mul_f32 %0, %1, %1" : "=v"(gt[0]) : "v"(a1[1][1][0]) : ); SB();
  asm volatile("v_mfma_f32_16x16x32_bf16 %0, v[196:199], a[96:99], %0" : "+v"(g[1][0][1]) :  : ); SB();
  asm volatile("buffer_load_dwordx4 v[188:191], %0, %1, %2 offen offset:1024 sc1" :  : "v"(vB), "s"(rWS), "s"(69632u) : "memory"); SB();
  asm volatile("v_mul_f32 %0, %1, %1" : "=v"(gt[1]) : "v"(a1[1][1][1]) : ); SB();
  asm volatile("v_mfma_f32_16x16x32_bf16 %0, v[196:199], a[100:103], %0" : "+v"(g[1][1][1]) :  : ); SB();
  asm volatile("v_xor_b32 %0, 917504, %0" : "+v"(vB) :  : "memory"); SB();
  asm volatile("v_mul_f32 %0, %1, %1" : "=v"(gt[2]) : "v"(a1[1][1][2]) : ); SB();
  asm volatile("v_mfma_f32_16x16x32_bf16 %0, v[196:199], a[104:107], %0" : "+v"(g[1][2][1]) :  : ); SB();
  asm volatile("v_mul_f32 %0, %1, %1" : "=v"(gt[3]) : "v"(a1[1][1][3]) : ); SB();
  asm volatile("v_mfma_f32_16x16x32_bf16 %0, v[196:199], a[108:111], %0" : "+v"(g[1][3][1]) :  : ); SB();
  asm volatile("v_fma_f32 %0, %0, %1, %2" : "+v"(gt[0]) : "v"(k2[0]), "v"(k1[0]) : ); SB();
  asm volatile("s_waitcnt vmcnt(20)" :  :  : "memory"); SB();
  asm volatile("v_mfma_f32_16x16x32_bf16 %0, v[200:203], a[112:115], %0" : "+v"(g[1][0][0]) :  : ); SB();
  asm volatile("buffer_load_dwordx4 v[192:195], %0, %1, %2 offen offset:0 sc1" :  : "v"(vB), "s"(rWS), "s"(4096u) : "memory"); SB();
  asm volatile("v_fma_f32 %0, %0, %1, %2" : "+v"(gt[1]) : "v"(k2[0]), "v"(k1[0]) : ); SB();
  asm volatile("v_mfma_f32_16x16x32_bf16 %0, v[200:203], a[116:119], %0" : "+v"(g[1][1][0]) :  : ); SB();
  asm volatile("v_fma_f32 %0, %0, %1, %2" : "+v"(gt[2]) : "v"(k2[0]), "v"(k1[0]) : ); SB();
  asm volatile("v_mfma_f32_16x16x32_bf16 %0, v[200:203], a[120:123], %0" : "+v"(g[1][2][0]) :  : ); SB();
  asm volatile("buffer_load_dwordx4 v[196:199], %0, %1, %2 offen offset:1024 sc1" :  : "v"(vB), "s"(rWS), "s"(4096u) : "memory"); SB();
  asm volatile("v_fma_f32 %0, %0, %1, %2" : "+v"(gt[3]) : "v"(k2[0]), "v"(k1[0]) : ); SB();
  asm volatile("v_mfma_f32_16x16x32_bf16 %0, v[200:203], a[124:127], %0" : "+v"(g[1][3][0]) :  : ); SB();
  asm volatile("v_mul_f32 %0, %0, %1" : "+v"(gt[0]) : "v"(a1[1][1][0]) : ); SB();
  asm volatile("v_mfma_f32_16x16x32_bf16 %0, v[204:207], a[112:115], %0" : "+v"(g[1][0][1]) :  : ); SB();
  asm volatile("v_mul_f32 %0, %0, %1" : "+v"(gt[1]) : "v"(a1[1][1][1]) : ); SB();
  asm volatile("v_mfma_f32_16x16x32_bf16 %0, v[204:207], a[116:119], %0" : "+v"(g[1][1][1]) :  : ); SB();
  asm volatile("v_mul_f32 %0, %0, %1" : "+v"(gt[2]) : "v"(a1[1][1][2]) : ); SB();
  asm volatile("v_mfma_f32_16x16x32_bf16 %0, v[204:207], a[120:123], %0" : "+v"(g[1][2][1]) :  : ); SB();
  asm volatile("v_mul_f32 %0, %0, %1" : "+v"(gt[3]) : "v"(a1[1][1][3]) : ); SB();
  asm volatile("v_mfma_f32_16x16x32_bf16 %0, v[204:207], a[124:127], %0" : "+v"(g[1][3][1]) :  : ); SB();
  asm volatile("v_exp_f32 %0, %1" : "=v"(ge[0]) : "v"(gt[0]) : ); SB();
  asm volatile("s_waitcnt vmcnt(20)" :  :  : "memory"); SB();
  asm volatile("v_mfma_f32_16x16x32_bf16 %0, v[208:211], a[128:131], %0" : "+v"(g[1][0][0]) :  : ); SB();
  asm volatile("buffer_load_dwordx4 v[200:203], %0, %1, %2 offen offset:0 sc1" :  : "v"(vB), "s"(rWS), "s"(69632u) : "memory"); SB();
  asm volatile("v_mfma_f32_16x16x32_bf16 %0, v[208:211], a[132:135], %0" : "+v"(g[1][1][0]) :  : ); SB();
  asm volatile("buffer_load_dwordx4 v[204:207], %0, %1, %2 offen offset:1024 sc1" :  : "v"(vB), "s"(rWS), "s"(69632u) : "memory"); SB();
  asm volatile("v_mfma_f32_16x16x32_bf16 %0, v[208:211], a[136:139], %0" : "+v"(g[1][2][0]) :  : ); SB();
  asm volatile("v_xor_b32 %0, 131072, %0" : "+v"(vB) :  : "memory"); SB();
  asm volatile("v_mfma_f32_16x16x32_bf16 %0, v[208:211], a[140:143], %0" : "+v"(g[1][3][0]) :  : ); SB();
  asm volatile("v_exp_f32 %0, %1" : "=v"(ge[1]) : "v"(gt[1]) : ); SB();
  asm volatile("v_mfma_f32_16x16x32_bf16 %0, v[212:215], a[128:131], %0" : "+v"(g[1][0][1]) :  : ); SB();
  asm volatile("v_exp_f32 %0, %1" : "=v"(ge[2]) : "v"(gt[2]) : ); SB();
  asm volatile("v_mfma_f32_16x16x32_bf16 %0, v[212:215], a[132:135], %0" : "+v"(g[1][1][1]) :  : ); SB();
  asm volatile("v_exp_f32 %0, %1" : "=v"(ge[3]) : "v"(gt[3]) : ); SB();
  asm volatile("v_mfma_f32_16x16x32_bf16 %0, v[212:215], a[136:139], %0" : "+v"(g[1][2][1]) :  : ); SB();
  asm volatile("v_fma_f32 %0, %0, %1, %1" : "+v"(ge[0]) : "v"(xd[0]) : ); SB();
  asm volatile("v_mfma_f32_16x16x32_bf16 %0, v[212:215], a[140:143], %0" : "+v"(g[1][3][1]) :  : ); SB();
  asm volatile("v_fma_f32 %0, %0, %1, %1" : "+v"(ge[1]) : "v"(xd[0]) : ); SB();
  asm volatile("s_waitcnt vmcnt(20)" :  :  : "memory"); SB();
  asm volatile("v_mfma_f32_16x16x32_bf16 %0, v[216:219], a[144:147], %0" : "+v"(g[1][0][0]) :  : ); SB();
  asm volatile("v_fma_f32 %0, %0, %1, %1" : "+v"(ge[2]) : "v"(xd[0]) : ); SB();
  asm volatile("v_mfma_f32_16x16x32_bf16 %0, v[216:219], a[148:151], %0" : "+v"(g[1][1][0]) :  : ); SB();
  asm volatile("v_fma_f32 %0, %0, %1, %1" : "+v"(ge[3]) : "v"(xd[0]) : ); SB();
  asm volatile("v_mfma_f32_16x16x32_bf16 %0, v[216:219], a[152:155], %0" : "+v"(g[1][2][0]) :  : ); SB();
  asm volatile("v_rcp_f32 %0, %1" : "=v"(gt[0]) : "v"(ge[0]) : ); SB();
  asm volatile("v_mfma_f32_16x16x32_bf16 %0, v[216:219], a[156:159], %0" : "+v"(g[1][3][0]) :  : ); SB();
  asm volatile("v_rcp_f32 %0, %1" : "=v"(gt[1]) : "v"(ge[1]) : ); SB();
  asm volatile("v_mfma_f32_16x16x32_bf16 %0, v[220:223], a[144:147], %0" : "+v"(g[1][0][1]) :  : ); SB();
  asm volatile("buffer_load_dwordx4 v[208:211], %0, %1, %2 offen offset:0 sc1" :  : "v"(vB), "s"(rWS), "s"(4096u) : "memory"); SB();
  asm volatile("v_mfma_f32_16x16x32_bf16 %0, v[220:223], a[148:151], %0" : "+v"(g[1][1][1]) :  : ); SB();
  asm volatile("v_rcp_f32 %0, %1" : "=v"(gt[2]) : "v"(ge[2]) : ); SB();
  asm volatile("v_mfma_f32_16x16x32_bf16 %0, v[220:223], a[152:155], %0" : "+v"(g[1][2][1]) :  : ); SB();
  asm volatile("v_rcp_f32 %0, %1" : "=v"(gt[3]) : "v"(ge[3]) : ); SB();
  asm volatile("v_mfma_f32_16x16x32_bf16 %0, v[220:223], a[156:159], %0" : "+v"(g[1][3][1]) :  : ); SB();
  asm volatile("v_mul_f32 %0, %0, %1" : "+v"(a1[1][1][0]) : "v"(gt[0]) : ); SB();
  ST(29);
  asm volatile("s_waitcnt vmcnt(19)" :  :  : "memory"); SB();
  asm volatile("v_mfma_f32_16x16x32_bf16 %0, v[224:227], a[160:163], %1" : "=&v"(g[0][0][0]) : "v"(bs4) : ); SB();
  asm volatile("s_barrier" :  :  : "memory"); SB();
  asm volatile("buffer_load_dwordx4 v[212:215], %0, %1, %2 offen offset:1024 sc1" :  : "v"(vB), "s"(rWS), "s"(4096u) : "memory"); SB();
  asm volatile("v_mfma_f32_16x16x32_bf16 %0, v[224:227], a[164:167], 0" : "=&v"(g[0][1][0]) :  : ); SB();
  asm volatile("v_mul_f32 %0, %0, %1" : "+v"(a1[1][1][1]) : "v"(gt[1]) : ); SB();
  asm volatile("v_mfma_f32_16x16x32_bf16 %0, v[224:227], a[168:171], 0" : "=&v"(g[0][2][0]) :  : ); SB();
  asm volatile("ds_write_b128 %0, %1 offset:0" :  : "v"(pw2), "v"(g[1][1][0]) : "memory"); SB();
  asm volatile("v_mfma_f32_16x16x32_bf16 %0, v[224:227], a[172:175], 0" : "=&v"(g[0][3][0]) :  : ); SB();
  asm volatile("buffer_load_dwordx4 v[216:219], %0, %1, %2 offen offset:0 sc1" :  : "v"(vB), "s"(rWS), "s"(69632u) : "memory"); SB();
  asm volatile("v_mul_f32 %0, %0, %1" : "+v"(a1[1][1][2]) : "v"(gt[2]) : ); SB();
  asm volatile("v_mfma_f32_16x16x32_bf16 %0, v[228:231], a[160:163], %1" : "=&v"(g[0][0][1]) : "v"(bs4) : ); SB();
  asm volatile("buffer_load_dwordx4 v[220:223], %0, %1, %2 offen offset:1024 sc1" :  : "v"(vB), "s"(rWS), "s"(69632u) : "memory"); SB();
  asm volatile("v_mul_f32 %0, %0, %1" : "+v"(a1[1][1][3]) : "v"(gt[3]) : ); SB();
  asm volatile("v_mfma_f32_16x16x32_bf16 %0, v[228:231], a[164:167], 0" : "=&v"(g[0][1][1]) :  : ); SB();
  asm volatile("ds_write_b128 %0, %1 offset:1024" :  : "v"(pw2), "v"(g[1][1][1]) : "memory"); SB();
  asm volatile("v_mfma_f32_16x16x32_bf16 %0, v[228:231], a[168:171], 0" : "=&v"(g[0][2][1]) :  : ); SB();
  asm volatile("v_xor_b32 %0, 393216, %0" : "+v"(vB) :  : "memory"); SB();
  asm volatile("v_mul_f32 %0, %1, %1" : "=v"(gt[0]) : "v"(a1[1][2][0]) : ); SB();
  asm volatile("v_mfma_f32_16x16x32_bf16 %0, v[228:231], a[172:175], 0" : "=&v"(g[0][3][1]) :  : ); SB();
  asm volatile("v_mul_f32 %0, %1, %1" : "=v"(gt[1]) : "v"(a1[1][2][1]) : ); SB();
  asm volatile("s_waitcnt vmcnt(20)" :  :  : "memory"); SB();
  asm volatile("v_mfma_f32_16x16x32_bf16 %0, v[232:235], a[176:179], %0" : "+v"(g[0][0][0]) :  : ); SB();
  asm volatile("ds_write_b128 %0, %1 offset:2048" :  : "v"(pw2), "v"(g[1][2][0]) : "memory"); SB();
  asm volatile("v_mfma_f32_16x16x32_bf16 %0, v[232:235], a[180:183], %0" : "+v"(g[0][1][0]) :  : ); SB();
  asm volatile("buffer_store_dwordx4 %1, %0, %2, %3 offen offset:0 sc1\n\ts_nop 1" :  : "v"(vH_), "v"(ho[0]), "s"(rWS), "s"(0u) : "memory"); SB();
  asm volatile("v_mul_f32 %0, %1, %1" : "=v"(gt[2]) : "v"(a1[1][2][2]) : ); SB();
  asm volatile("v_mfma_f32_16x16x32_bf16 %0, v[232:235], a[184:187], %0" : "+v"(g[0][2][0]) :  : ); SB();
  asm volatile("buffer_load_dwordx4 v[224:227], %0, %1, %2 offen offset:0 sc1" :  : "v"(vB), "s"(rWS), "s"(4096u) : "memory"); SB();
  asm volatile("v_mul_f32 %0, %1, %1" : "=v"(gt[3]) : "v"(a1[1][2][3]) : ); SB();
  asm volatile("v_mfma_f32_16x16x32_bf16 %0, v[232:235], a[188:191], %0" : "+v"(g[0][3][0]) :  : ); SB();
  asm volatile("ds_write_b128 %0, %1 offset:3072" :  : "v"(pw2), "v"(g[1][2][1]) : "memory"); SB();
  asm volatile("v_mfma_f32_16x16x32_bf16 %0, v[236:239], a[176:179], %0" : "+v"(g[0][0][1]) :  : ); SB();
  asm volatile("buffer_load_dwordx4 v[228:231], %0, %1, %2 offen offset:1024 sc1" :  : "v"(vB), "s"(rWS), "s"(4096u) : "memory"); SB();
  asm volatile("v_fma_f32 %0, %0, %1, %2" : "+v"(gt[0]) : "v"(k2[0]), "v"(k1[0]) : ); SB();
  asm volatile("v_mfma_f32_16x16x32_bf16 %0, v[236:239], a[180:183], %0" : "+v"(g[0][1][1]) :  : ); SB();
  asm volatile("v_fma_f32 %0, %0, %1, %2" : "+v"(gt[1]) : "v"(k2[0]), "v"(k1[0]) : ); SB();
  asm volatile("v_mfma_f32_16x16x32_bf16 %0, v[236:239], a[184:187], %0" : "+v"(g[0][2][1]) :  : ); SB();
  asm volatile("ds_write_b128 %0, %1 offset:4096" :  : "v"(pw2), "v"(g[1][3][0]) : "memory"); SB();
  asm volatile("v_mfma_f32_16x16x32_bf16 %0, v[236:239], a[188:191], %0" : "+v"(g[0][3][1]) :  : ); SB();
  asm volatile("v_fma_f32 %0, %0, %1, %2" : "+v"(gt[2]) : "v"(k2[0]), "v"(k1[0]) : ); SB();
  asm volatile("s_waitcnt vmcnt(21)" :  :  : "memory"); SB();
  asm volatile("v_mfma_f32_16x16x32_bf16 %0, v[240:243], a[192:195], %0" : "+v"(g[0][0][0]) :  : ); SB();
  asm volatile("buffer_load_dwordx4 v[232:235], %0, %1, %2 offen offset:0 sc1" :  : "v"(vB), "s"(rWS), "s"(69632u) : "memory"); SB();
  asm volatile("v_fma_f32 %0, %0, %1, %2" : "+v"(gt[3]) : "v"(k2[0]), "v"(k1[0]) : ); SB();
  asm volatile("v_mfma_f32_16x16x32_bf16 %0, v[240:243], a[196:199], %0" : "+v"(g[0][1][0]) :  : ); SB();
  asm volatile("ds_write_b128 %0, %1 offset:5120" :  : "v"(pw2), "v"(g[1][3][1]) : "memory"); SB();
  asm volatile("v_mfma_f32_16x16x32_bf16 %0, v[240:243], a[200:203], %0" : "+v"(g[0][2][0]) :  : ); SB();
  asm volatile("buffer_load_dwordx4 v[236:239], %0, %1, %2 offen offset:1024 sc1" :  : "v"(vB), "s"(rWS), "s"(69632u) : "memory"); SB();
  asm volatile("v_mul_f32 %0, %0, %1" : "+v"(gt[0]) : "v"(a1[1][2][0]) : ); SB();
  asm volatile("v_mfma_f32_16x16x32_bf16 %0, v[240:243], a[204:207], %0" : "+v"(g[0][3][0]) :  : ); SB();
  asm volatile("v_xor_b32 %0, 131072, %0" : "+v"(vB) :  : "memory"); SB();
  asm volatile("v_mul_f32 %0, %0, %1" : "+v"(gt[1]) : "v"(a1[1][2][1]) : ); SB();
  asm volatile("v_mfma_f32_16x16x32_bf16 %0, v[244:247], a[192:195], %0" : "+v"(g[0][0][1]) :  : ); SB();
  asm volatile("s_waitcnt lgkmcnt(0)\n\ts_barrier" :  :  : "memory"); SB();
  asm volatile("ds_read_b128 %0, %1 offset:0" : "=&v"(pr[0]) : "v"(pe[0]) : "memory"); SB();
  asm volatile("v_mfma_f32_16x16x32_bf16 %0, v[244:247], a[196:199], %0" : "+v"(g[0][1][1]) :  : ); SB();
  asm volatile("ds_read_b128 %0, %1 offset:1024" : "=&v"(pr[1]) : "v"(pe[0]) : "memory"); SB();
  asm volatile("v_mul_f32 %0, %0, %1" : "+v"(gt[2]) : "v"(a1[1][2][2]) : ); SB();
  asm volatile("v_mfma_f32_16x16x32_bf16 %0, v[244:247], a[200:203], %0" : "+v"(g[0][2][1]) :  : ); SB();
  asm volatile("ds_read_b128 %0, %1 offset:2048" : "=&v"(pr[2]) : "v"(pe[1]) : "memory"); SB();
  asm volatile("v_mul_f32 %0, %0, %1" : "+v"(gt[3]) : "v"(a1[1][2][3]) : ); SB();
  asm volatile("v_mfma_f32_16x16x32_bf16 %0, v[244:247], a[204:207], %0" : "+v"(g[0][3][1]) :  : ); SB();
  asm volatile("buffer_load_dwordx4 a[160:163], %0, %1, %2 offen offset:0 sc1" :  : "v"(vO), "s"(rWS), "s"(0u) : "memory"); SB();
  asm volatile("s_waitcnt vmcnt(22)" :  :  : "memory"); SB();
  asm volatile("v_mfma_f32_16x16x32_bf16 %0, v[248:251], a[208:211], %0" : "+v"(g[0][0][0]) :  : ); SB();
  asm volatile("ds_read_b128 %0, %1 offset:3072" : "=&v"(pr[3]) : "v"(pe[1]) : "memory"); SB();
  asm volatile("v_mfma_f32_16x16x32_bf16 %0, v[248:251], a[212:215], %0" : "+v"(g[0][1][0]) :  : ); SB();
  asm volatile("ds_read_b128 %0, %1 offset:4096" : "=&v"(pr[4]) : "v"(pe[2]) : "memory"); SB();
  asm volatile("v_mfma_f32_16x16x32_bf16 %0, v[248:251], a[216:219], %0" : "+v"(g[0][2][0]) :  : ); SB();
  asm volatile("buffer_load_dwordx4 v[240:243], %0, %1, %2 offen offset:0 sc1" :  : "v"(vB), "s"(rWS), "s"(4096u) : "memory"); SB();
  asm volatile("v_mfma_f32_16x16x32_bf16 %0, v[248:251], a[220:223], %0" : "+v"(g[0][3][0]) :  : ); SB();
  asm volatile("ds_read_b128 %0, %1 offset:5120" : "=&v"(pr[5]) : "v"(pe[2]) : "memory"); SB();
  asm volatile("v_mfma_f32_16x16x32_bf16 %0, v[252:255], a[208:211], %0" : "+v"(g[0][0][1]) :  : ); SB();
  asm volatile("buffer_load_dwordx4 v[244:247], %0, %1, %2 offen offset:1024 sc1" :  : "v"(vB), "s"(rWS), "s"(4096u) : "memory"); SB();
  asm volatile("v_mfma_f32_16x16x32_bf16 %0, v[252:255], a[212:215], %0" : "+v"(g[0][1][1]) :  : ); SB();
  asm volatile("buffer_load_dwordx4 a[164:167], %0, %1, %2 offen offset:1024 sc1" :  : "v"(vO), "s"(rWS), "s"(0u) : "memory"); SB();
  asm volatile("v_mfma_f32_16x16x32_bf16 %0, v[252:255], a[216:219], %0" : "+v"(g[0][2][1]) :  : ); SB();
  asm volatile("v_exp_f32 %0, %1" : "=v"(ge[0]) : "v"(gt[0]) : ); SB();
  asm volatile("v_mfma_f32_16x16x32_bf16 %0, v[252:255], a[220:223], %0" : "+v"(g[0][3][1]) :  : ); SB();
  asm volatile("buffer_load_dwordx4 a[168:171], %0, %1, %2 offen offset:2048 sc1" :  : "v"(vO), "s"(rWS), "s"(0u) : "memory"); SB();
  asm volatile("s_waitcnt vmcnt(24)" :  :  : "memory"); SB();
  asm volatile("v_mfma_f32_16x16x32_bf16 %0, v[160:163], a[224:227], %0" : "+v"(g[0][0][0]) :  : ); SB();
  asm volatile("v_exp_f32 %0, %1" : "=v"(ge[1]) : "v"(gt[1]) : ); SB();
  asm volatile("v_mfma_f32_16x16x32_bf16 %0, v[160:163], a[228:231], %0" : "+v"(g[0][1][0]) :  : ); SB();
  asm volatile("s_waitcnt lgkmcnt(0)" : "+v"(pr[0]), "+v"(pr[1]), "+v"(pr[2]), "+v"(pr[3]), "+v"(pr[4]), "+v"(pr[5]) :  : "memory"); SB();
  asm volatile("buffer_load_dwordx4 v[248:251], %0, %1, %2 offen offset:0 sc1" :  : "v"(vB), "s"(rWS), "s"(69632u) : "memory"); SB();
  asm volatile("v_mfma_f32_16x16x32_bf16 %0, v[160:163], a[232:235], %0" : "+v"(g[0][2][0]) :  : ); SB();
  asm volatile("v_add_f32 %0, %0, %1" : "+v"(pr[0][0]) : "v"(g[1][0][0][0]) : ); SB();
  asm volatile("v_mfma_f32_16x16x32_bf16 %0, v[160:163], a[236:239], %0" : "+v"(g[0][3][0]) :  : ); SB();
  asm volatile("v_add_f32 %0, %0, %1" : "+v"(pr[0][1]) : "v"(g[1][0][0][1]) : ); SB();
  asm volatile("v_mfma_f32_16x16x32_bf16 %0, v[164:167], a[224:227], %0" : "+v"(g[0][0][1]) :  : ); SB();
  asm volatile("buffer_load_dwordx4 v[252:255], %0, %1, %2 offen offset:1024 sc1" :  : "v"(vB), "s"(rWS), "s"(69632u) : "memory"); SB();
  asm volatile("v_add_f32 %0, %0, %1" : "+v"(pr[0][2]) : "v"(g[1][0][0][2]) : ); SB();
  asm volatile("v_mfma_f32_16x16x32_bf16 %0, v[164:167], a[228:231], %0" : "+v"(g[0][1][1]) :  : ); SB();
  asm volatile("v_add_f32 %0, %0, %1" : "+v"(pr[0][3]) : "v"(g[1][0][0][3]) : ); SB();
  asm volatile("v_mfma_f32_16x16x32_bf16 %0, v[164:167], a[232:235], %0" : "+v"(g[0][2][1]) :  : ); SB();
  asm volatile("v_add_f32 %0, %0, %1" : "+v"(pr[1][0]) : "v"(g[1][0][1][0]) : ); SB();
  asm volatile("v_mfma_f32_16x16x32_bf16 %0, v[164:167], a[236:239], %0" : "+v"(g[0][3][1]) :  : ); SB();
  asm volatile("v_add_f32 %0, %0, %1" : "+v"(pr[1][1]) : "v"(g[1][0][1][1]) : ); SB();
  asm volatile("s_waitcnt vmcnt(24)" :  :  : "memory"); SB();
  asm volatile("v_mfma_f32_16x16x32_bf16 %0, v[168:171], a[240:243], %0" : "+v"(g[0][0][0]) :  : ); SB();
  asm volatile("buffer_load_dwordx4 a[172:175], %0, %1, %2 offen offset:3072 sc1" :  : "v"(vO), "s"(rWS), "s"(0u) : "memory"); SB();
  asm volatile("v_add_f32 %0, %0, %1" : "+v"(pr[1][2]) : "v"(g[1][0][1][2]) : ); SB();
  asm volatile("v_mfma_f32_16x16x32_bf16 %0, v[168:171], a[244:247], %0" : "+v"(g[0][1][0]) :  : ); SB();
  asm volatile("v_add_f32 %0, %0, %1" : "+v"(pr[1][3]) : "v"(g[1][0][1][3]) : ); SB();
  asm volatile("v_mfma_f32_16x16x32_bf16 %0, v[168:171], a[248:251], %0" : "+v"(g[0][2][0]) :  : ); SB();
  asm volatile("v_add_f32 %0, %0, %1" : "+v"(pr[2][0]) : "v"(pr[4][0]) : ); SB();
  asm volatile("v_mfma_f32_16x16x32_bf16 %0, v[168:171], a[252:255], %0" : "+v"(g[0][3][0]) :  : ); SB();
  asm volatile("buffer_load_dwordx4 a[176:179], %0, %1, %2 offen offset:0 sc1" :  : "v"(vO), "s"(rWS), "s"(4096u) : "memory"); SB();
  asm volatile("v_add_f32 %0, %0, %1" : "+v"(pr[2][1]) : "v"(pr[4][1]) : ); SB();
  asm volatile("v_mfma_f32_16x16x32_bf16 %0, v[172:175], a[240:243], %0" : "+v"(g[0][0][1]) :  : ); SB();
  asm volatile("v_add_f32 %0, %0, %1" : "+v"(pr[2][2]) : "v"(pr[4][2]) : ); SB();
  asm volatile("v_mfma_f32_16x16x32_bf16 %0, v[172:175], a[244:247], %0" : "+v"(g[0][1][1]) :  : ); SB();
  asm volatile("v_add_f32 %0, %0, %1" : "+v"(pr[2][3]) : "v"(pr[4][3]) : ); SB();
  asm volatile("v_mfma_f32_16x16x32_bf16 %0, v[172:175], a[248:251], %0" : "+v"(g[0][2][1]) :  : ); SB();
  asm volatile("v_add_f32 %0, %0, %1" : "+v"(pr[3][0]) : "v"(pr[5][0]) : ); SB();
  asm volatile("v_mfma_f32_16x16x32_bf16 %0, v[172:175], a[252:255], %0" : "+v"(g[0][3][1]) :  : ); SB();
  asm volatile("v_add_f32 %0, %0, %1" : "+v"(pr[3][1]) : "v"(pr[5][1]) : ); SB();
  asm volatile("s_waitcnt vmcnt(24)" :  :  : "memory"); SB();
  asm volatile("v_mfma_f32_16x16x32_bf16 %0, v[176:179], a[0:3], %0" : "+v"(g[0][0][0]) :  : ); SB();
  asm volatile("v_add_f32 %0, %0, %1" : "+v"(pr[3][2]) : "v"(pr[5][2]) : ); SB();
  asm volatile("v_mfma_f32_16x16x32_bf16 %0, v[176:179], a[4:7], %0" : "+v"(g[0][1][0]) :  : ); SB();
  asm volatile("v_add_f32 %0, %0, %1" : "+v"(pr[3][3]) : "v"(pr[5][3]) : ); SB();
  asm volatile("v_mfma_f32_16x16x32_bf16 %0, v[176:179], a[8:11], %0" : "+v"(g[0][2][0]) :  : ); SB();
  asm volatile("v_add_f32 %0, %0, %1" : "+v"(pr[0][0]) : "v"(pr[2][0]) : ); SB();
  asm volatile("v_mfma_f32_16x16x32_bf16 %0, v[176:179], a[12:15], %0" : "+v"(g[0][3][0]) :  : ); SB();
  asm volatile("v_add_f32 %0, %0, %1" : "+v"(pr[0][1]) : "v"(pr[2][1]) : ); SB();
  asm volatile("v_mfma_f32_16x16x32_bf16 %0, v[180:183], a[0:3], %0" : "+v"(g[0][0][1]) :  : ); SB();
  asm volatile("v_add_f32 %0, %0, %1" : "+v"(pr[0][2]) : "v"(pr[2][2]) : ); SB();
  asm volatile("v_mfma_f32_16x16x32_bf16 %0, v[180:183], a[4:7], %0" : "+v"(g[0][1][1]) :  : ); SB();
  asm volatile("v_add_f32 %0, %0, %1" : "+v"(pr[0][3]) : "v"(pr[2][3]) : ); SB();
  asm volatile("v_mfma_f32_16x16x32_bf16 %0, v[180:183], a[8:11], %0" : "+v"(g[0][2][1]) :  : ); SB();
  asm volatile("v_add_f32 %0, %0, %1" : "+v"(pr[1][0]) : "v"(pr[3][0]) : ); SB();
  asm volatile("v_mfma_f32_16x16x32_bf16 %0, v[180:183], a[12:15], %0" : "+v"(g[0][3][1]) :  : ); SB();
  asm volatile("v_add_f32 %0, %0, %1" : "+v"(pr[1][1]) : "v"(pr[3][1]) : ); SB();
  asm volatile("s_waitcnt vmcnt(22)" :  :  : "memory"); SB();
  asm volatile("v_mfma_f32_16x16x32_bf16 %0, v[184:187], a[16:19], %0" : "+v"(g[0][0][0]) :  : ); SB();
  asm volatile("v_add_f32 %0, %0, %1" : "+v"(pr[1][2]) : "v"(pr[3][2]) : ); SB();
  asm volatile("v_mfma_f32_16x16x32_bf16 %0, v[184:187], a[20:23], %0" : "+v"(g[0][1][0]) :  : ); SB();
  asm volatile("v_add_f32 %0, %0, %1" : "+v"(pr[1][3]) : "v"(pr[3][3]) : ); SB();
  asm volatile("v_mfma_f32_16x16x32_bf16 %0, v[184:187], a[24:27], %0" : "+v"(g[0][2][0]) :  : ); SB();
  asm volatile("v_mul_f32 %0, %0, %1" : "+v"(pr[0][0]) : "v"(a1[0][2][0]) : ); SB();
  asm volatile("v_mfma_f32_16x16x32_bf16 %0, v[184:187], a[28:31], %0" : "+v"(g[0][3][0]) :  : ); SB();
  asm volatile("v_mul_f32 %0, %0, %1" : "+v"(pr[0][1]) : "v"(a1[0][2][1]) : ); SB();
  asm volatile("v_mfma_f32_16x16x32_bf16 %0, v[188:191], a[16:19], %0" : "+v"(g[0][0][1]) :  : ); SB();
  asm volatile("v_mul_f32 %0, %0, %1" : "+v"(pr[0][2]) : "v"(a1[0][2][2]) : ); SB();
  asm volatile("v_mfma_f32_16x16x32_bf16 %0, v[188:191], a[20:23], %0" : "+v"(g[0][1][1]) :  : ); SB();
  asm volatile("v_mul_f32 %0, %0, %1" : "+v"(pr[0][3]) : "v"(a1[0][2][3]) : ); SB();
  asm volatile("v_mfma_f32_16x16x32_bf16 %0, v[188:191], a[24:27], %0" : "+v"(g[0][2][1]) :  : ); SB();
  asm volatile("v_mul_f32 %0, %0, %1" : "+v"(pr[1][0]) : "v"(a1[1][0][0]) : ); SB();
  asm volatile("v_mfma_f32_16x16x32_bf16 %0, v[188:191], a[28:31], %0" : "+v"(g[0][3][1]) :  : ); SB();
  asm volatile("buffer_load_dwordx4 a[180:183], %0, %1, %2 offen offset:1024 sc1" :  : "v"(vO), "s"(rWS), "s"(4096u) : "memory"); SB();
  asm volatile("s_waitcnt vmcnt(21)" :  :  : "memory"); SB();
  asm volatile("v_mfma_f32_16x16x32_bf16 %0, v[192:195], a[32:35], %0" : "+v"(g[0][0][0]) :  : ); SB();
  asm volatile("v_mul_f32 %0, %0, %1" : "+v"(pr[1][1]) : "v"(a1[1][0][1]) : ); SB();
  asm volatile("v_mfma_f32_16x16x32_bf16 %0, v[192:195], a[36:39], %0" : "+v"(g[0][1][0]) :  : ); SB();
  asm volatile("v_mul_f32 %0, %0, %1" : "+v"(pr[1][2]) : "v"(a1[1][0][2]) : ); SB();
  asm volatile("v_mfma_f32_16x16x32_bf16 %0, v[192:195], a[40:43], %0" : "+v"(g[0][2][0]) :  : ); SB();
  asm volatile("v_mul_f32 %0, %0, %1" : "+v"(pr[1][3]) : "v"(a1[1][0][3]) : ); SB();
  asm volatile("v_mfma_f32_16x16x32_bf16 %0, v[192:195], a[44:47], %0" : "+v"(g[0][3][0]) :  : ); SB();
  asm volatile("buffer_load_dwordx4 a[184:187], %0, %1, %2 offen offset:2048 sc1" :  : "v"(vO), "s"(rWS), "s"(4096u) : "memory"); SB();
  asm volatile("v_cvt_pk_bf16_f32 %0, %1, %2" : "=v"(ho[1][0]) : "v"(pr[0][0]), "v"(pr[0][1]) : ); SB();
  asm volatile("v_mfma_f32_16x16x32_bf16 %0, v[196:199], a[32:35], %0" : "+v"(g[0][0][1]) :  : ); SB();
  asm volatile("v_cvt_pk_bf16_f32 %0, %1, %2" : "=v"(ho[1][1]) : "v"(pr[0][2]), "v"(pr[0][3]) : ); SB();
  asm volatile("v_mfma_f32_16x16x32_bf16 %0, v[196:199], a[36:39], %0" : "+v"(g[0][1][1]) :  : ); SB();
  asm volatile("v_cvt_pk_bf16_f32 %0, %1, %2" : "=v"(ho[1][2]) : "v"(pr[1][0]), "v"(pr[1][1]) : ); SB();
  asm volatile("v_mfma_f32_16x16x32_bf16 %0, v[196:199], a[40:43], %0" : "+v"(g[0][2][1]) :  : ); SB();
  asm volatile("buffer_load_dwordx4 a[188:191], %0, %1, %2 offen offset:3072 sc1" :  : "v"(vO), "s"(rWS), "s"(4096u) : "memory"); SB();
  asm volatile("v_cvt_pk_bf16_f32 %0, %1, %2" : "=v"(ho[1][3]) : "v"(pr[1][2]), "v"(pr[1][3]) : ); SB();
  asm volatile("v_mfma_f32_16x16x32_bf16 %0, v[196:199], a[44:47], %0" : "+v"(g[0][3][1]) :  : ); SB();
  asm volatile("v_exp_f32 %0, %1" : "=v"(ge[2]) : "v"(gt[2]) : ); SB();
  asm volatile("s_waitcnt vmcnt(21)" :  :  : "memory"); SB();
  asm volatile("v_mfma_f32_16x16x32_bf16 %0, v[200:203], a[48:51], %0" : "+v"(g[0][0][0]) :  : ); SB();
  asm volatile("v_exp_f32 %0, %1" : "=v"(ge[3]) : "v"(gt[3]) : ); SB();
  asm volatile("v_mfma_f32_16x16x32_bf16 %0, v[200:203], a[52:55], %0" : "+v"(g[0][1][0]) :  : ); SB();
  asm volatile("v_fma_f32 %0, %0, %1, %1" : "+v"(ge[0]) : "v"(xd[0]) : ); SB();
  asm volatile("v_mfma_f32_16x16x32_bf16 %0, v[200:203], a[56:59], %0" : "+v"(g[0][2][0]) :  : ); SB();
  asm volatile("buffer_store_dwordx4 %1, %0, %2, %3 offen offset:1024 sc1\n\ts_nop 1" :  : "v"(vH_), "v"(ho[1]), "s"(rWS), "s"(0u) : "memory"); SB();
  asm volatile("v_fma_f32 %0, %0, %1, %1" : "+v"(ge[1]) : "v"(xd[0]) : ); SB();
  asm volatile("v_mfma_f32_16x16x32_bf16 %0, v[200:203], a[60:63], %0" : "+v"(g[0][3][0]) :  : ); SB();
  asm volatile("v_fma_f32 %0, %0, %1, %1" : "+v"(ge[2]) : "v"(xd[0]) : ); SB();
  asm volatile("v_mfma_f32_16x16x32_bf16 %0, v[204:207], a[48:51], %0" : "+v"(g[0][0][1]) :  : ); SB();
  asm volatile("v_fma_f32 %0, %0, %1, %1" : "+v"(ge[3]) : "v"(xd[0]) : ); SB();
  asm volatile("v_mfma_f32_16x16x32_bf16 %0, v[204:207], a[52:55], %0" : "+v"(g[0][1][1]) :  : ); SB();
  asm volatile("buffer_load_dwordx4 a[192:195], %0, %1, %2 offen offset:0 sc1" :  : "v"(vO), "s"(rWS), "s"(8192u) : "memory"); SB();
  asm volatile("v_mfma_f32_16x16x32_bf16 %0, v[204:207], a[56:59], %0" : "+v"(g[0][2][1]) :  : ); SB();
  asm volatile("v_rcp_f32 %0, %1" : "=v"(gt[0]) : "v"(ge[0]) : ); SB();
  asm volatile("v_mfma_f32_16x16x32_bf16 %0, v[204:207], a[60:63], %0" : "+v"(g[0][3][1]) :  : ); SB();
  asm volatile("v_rcp_f32 %0, %1" : "=v"(gt[1]) : "v"(ge[1]) : ); SB();
  asm volatile("s_waitcnt vmcnt(21)" :  :  : "memory"); SB();
  asm volatile("v_mfma_f32_16x16x32_bf16 %0, v[208:211], a[64:67], %0" : "+v"(g[0][0][0]) :  : ); SB();
  asm volatile("ds_read_b128 %0, %1 offset:0" : "=&v"(yq[0]) : "v"(ydr) : "memory"); SB();
  asm volatile("v_mfma_f32_16x16x32_bf16 %0, v[208:211], a[68:71], %0" : "+v"(g[0][1][0]) :  : ); SB();
  asm volatile("ds_read_b128 %0, %1 offset:256" : "=&v"(yq[1]) : "v"(ydr) : "memory"); SB();
  asm volatile("v_mfma_f32_16x16x32_bf16 %0, v[208:211], a[72:75], %0" : "+v"(g[0][2][0]) :  : ); SB();
  asm volatile("ds_read_b128 %0, %1 offset:8192" : "=&v"(yq[2]) : "v"(ydr) : "memory"); SB();
  asm volatile("v_mfma_f32_16x16x32_bf16 %0, v[208:211], a[76:79], %0" : "+v"(g[0][3][0]) :  : ); SB();
  asm volatile("ds_read_b128 %0, %1 offset:8448" : "=&v"(yq[3]) : "v"(ydr) : "memory"); SB();
  asm volatile("v_mfma_f32_16x16x32_bf16 %0, v[212:215], a[64:67], %0" : "+v"(g[0][0][1]) :  : ); SB();
  asm volatile("ds_read_b128 %0, %1 offset:16384" : "=&v"(yq[4]) : "v"(ydr) : "memory"); SB();
  asm volatile("v_mfma_f32_16x16x32_bf16 %0, v[212:215], a[68:71], %0" : "+v"(g[0][1][1]) :  : ); SB();
  asm volatile("ds_read_b128 %0, %1 offset:16640" : "=&v"(yq[5]) : "v"(ydr) : "memory"); SB();
  asm volatile("v_mfma_f32_16x16x32_bf16 %0, v[212:215], a[72:75], %0" : "+v"(g[0][2][1]) :  : ); SB();
  asm volatile("ds_read_b128 %0, %1 offset:24576" : "=&v"(yq[6]) : "v"(ydr) : "memory"); SB();
  asm volatile("v_mfma_f32_16x16x32_bf16 %0, v[212:215], a[76:79], %0" : "+v"(g[0][3][1]) :  : ); SB();
  asm volatile("ds_read_b128 %0, %1 offset:24832" : "=&v"(yq[7]) : "v"(ydr) : "memory"); SB();
  asm volatile("s_waitcnt vmcnt(19)" :  :  : "memory"); SB();
  asm volatile("v_mfma_f32_16x16x32_bf16 %0, v[216:219], a[80:83], %0" : "+v"(g[0][0][0]) :  : ); SB();
  asm volatile("ds_read_b128 %0, %1 offset:25856" : "=&v"(ykv) : "v"(ykr) : "memory"); SB();
  asm volatile("v_mfma_f32_16x16x32_bf16 %0, v[216:219], a[84:87], %0" : "+v"(g[0][1][0]) :  : ); SB();
  asm volatile("buffer_load_dwordx4 a[196:199], %0, %1, %2 offen offset:1024 sc1" :  : "v"(vO), "s"(rWS), "s"(8192u) : "memory"); SB();
  asm volatile("v_mfma_f32_16x16x32_bf16 %0, v[216:219], a[88:91], %0" : "+v"(g[0][2][0]) :  : ); SB();
  asm volatile("buffer_load_dwordx4 a[200:203], %0, %1, %2 offen offset:3072 sc1" :  : "v"(vO), "s"(rWS), "s"(12288u) : "memory"); SB();
  asm volatile("v_mfma_f32_16x16x32_bf16 %0, v[216:219], a[92:95], %0" : "+v"(g[0][3][0]) :  : ); SB();
  asm volatile("buffer_load_dwordx4 a[204:207], %0, %1, %2 offen offset:0 sc1" :  : "v"(vO), "s"(rWS), "s"(16384u) : "memory"); SB();
  asm volatile("v_mfma_f32_16x16x32_bf16 %0, v[220:223], a[80:83], %0" : "+v"(g[0][0][1]) :  : ); SB();
  asm volatile("v_rcp_f32 %0, %1" : "=v"(gt[2]) : "v"(ge[2]) : ); SB();
  asm volatile("v_mfma_f32_16x16x32_bf16 %0, v[220:223], a[84:87], %0" : "+v"(g[0][1][1]) :  : ); SB();
  asm volatile("buffer_load_dwordx4 a[208:211], %0, %1, %2 offen offset:1024 sc1" :  : "v"(vO), "s"(rWS), "s"(16384u) : "memory"); SB();
  asm volatile("v_mfma_f32_16x16x32_bf16 %0, v[220:223], a[88:91], %0" : "+v"(g[0][2][1]) :  : ); SB();
  asm volatile("v_rcp_f32 %0, %1" : "=v"(gt[3]) : "v"(ge[3]) : ); SB();
  asm volatile("v_mfma_f32_16x16x32_bf16 %0, v[220:223], a[92:95], %0" : "+v"(g[0][3][1]) :  : ); SB();
  asm volatile("v_mul_f32 %0, %0, %1" : "+v"(a1[1][2][0]) : "v"(gt[0]) : ); SB();
  asm volatile("s_waitcnt vmcnt(20)" :  :  : "memory"); SB();
  asm volatile("v_mfma_f32_16x16x32_bf16 %0, v[224:227], a[96:99], %0" : "+v"(g[0][0][0]) :  : ); SB();
  asm volatile("ds_read2_b32 a[64:65], %0 offset0:176 offset1:177" :  : "v"(aa[0]) : "memory"); SB();
  asm volatile("v_mul_f32 %0, %0, %1" : "+v"(a1[1][2][1]) : "v"(gt[1]) : ); SB();
  asm volatile("v_mfma_f32_16x16x32_bf16 %0, v[224:227], a[100:103], %0" : "+v"(g[0][1][0]) :  : ); SB();
  asm volatile("ds_read2_b32 a[66:67], %0 offset0:178 offset1:179" :  : "v"(aa[0]) : "memory"); SB();
  asm volatile("v_mul_f32 %0, %0, %1" : "+v"(a1[1][2][2]) : "v"(gt[2]) : ); SB();
  asm volatile("v_mfma_f32_16x16x32_bf16 %0, v[224:227], a[104:107], %0" : "+v"(g[0][2][0]) :  : ); SB();
  asm volatile("ds_read_b128 a[60:63], %0 offset:3776" :  : "v"(bb) : "memory"); SB();
  asm volatile("v_mul_f32 %0, %0, %1" : "+v"(a1[1][2][3]) : "v"(gt[3]) : ); SB();
  asm volatile("v_mfma_f32_16x16x32_bf16 %0, v[224:227], a[108:111], %0" : "+v"(g[0][3][0]) :  : ); SB();
  asm volatile("ds_read_b128 a[56:59], %0 offset:3264" :  : "v"(bb) : "memory"); SB();
  asm volatile("v_mfma_f32_16x16x32_bf16 %0, v[228:231], a[96:99], %0" : "+v"(g[0][0][1]) :  : ); SB();
  asm volatile("ds_read_b128 a[52:55], %0 offset:2752" :  : "v"(bb) : "memory"); SB();
  asm volatile("v_mfma_f32_16x16x32_bf16 %0, v[228:231], a[100:103], %0" : "+v"(g[0][1][1]) :  : ); SB();
  asm volatile("ds_read_b128 a[48:51], %0 offset:2240" :  : "v"(bb) : "memory"); SB();
  asm volatile("v_mfma_f32_16x16x32_bf16 %0, v[228:231], a[104:107], %0" : "+v"(g[0][2][1]) :  : ); SB();
  asm volatile("ds_read2_b32 a[68:69], %0 offset0:48 offset1:49" :  : "v"(aa[0]) : "memory"); SB();
  asm volatile("v_mfma_f32_16x16x32_bf16 %0, v[228:231], a[108:111], %0" : "+v"(g[0][3][1]) :  : ); SB();
  asm volatile("ds_read2_b32 a[70:71], %0 offset0:50 offset1:51" :  : "v"(aa[0]) : "memory"); SB();
  asm volatile("s_waitcnt vmcnt(18)" :  :  : "memory"); SB();
  asm volatile("v_mfma_f32_16x16x32_bf16 %0, v[232:235], a[112:115], %0" : "+v"(g[0][0][0]) :  : ); SB();
  asm volatile("ds_read_b128 a[44:47], %0 offset:1728" :  : "v"(bb) : "memory"); SB();
  asm volatile("v_mfma_f32_16x16x32_bf16 %0, v[232:235], a[116:119], %0" : "+v"(g[0][1][0]) :  : ); SB();
  asm volatile("buffer_load_dwordx4 a[212:215], %0, %1, %2 offen offset:2048 sc1" :  : "v"(vO), "s"(rWS), "s"(16384u) : "memory"); SB();
  asm volatile("v_mfma_f32_16x16x32_bf16 %0, v[232:235], a[120:123], %0" : "+v"(g[0][2][0]) :  : ); SB();
  asm volatile("ds_read2_b32 a[72:73], %0 offset0:176 offset1:177" :  : "v"(aa[1]) : "memory"); SB();
  asm volatile("v_mfma_f32_16x16x32_bf16 %0, v[232:235], a[124:127], %0" : "+v"(g[0][3][0]) :  : ); SB();
  asm volatile("ds_read2_b32 a[74:75], %0 offset0:178 offset1:179" :  : "v"(aa[1]) : "memory"); SB();
  asm volatile("v_mfma_f32_16x16x32_bf16 %0, v[236:239], a[112:115], %0" : "+v"(g[0][0][1]) :  : ); SB();
  asm volatile("ds_read2_b32 a[76:77], %0 offset0:48 offset1:49" :  : "v"(aa[1]) : "memory"); SB();
  asm volatile("v_mfma_f32_16x16x32_bf16 %0, v[236:239], a[116:119], %0" : "+v"(g[0][1][1]) :  : ); SB();
  asm volatile("buffer_load_dwordx4 a[216:219], %0, %1, %2 offen offset:3072 sc1" :  : "v"(vO), "s"(rWS), "s"(16384u) : "memory"); SB();
  asm volatile("v_mfma_f32_16x16x32_bf16 %0, v[236:239], a[120:123], %0" : "+v"(g[0][2][1]) :  : ); SB();
  asm volatile("ds_read2_b32 a[78:79], %0 offset0:50 offset1:51" :  : "v"(aa[1]) : "memory"); SB();
  asm volatile("v_mfma_f32_16x16x32_bf16 %0, v[236:239], a[124:127], %0" : "+v"(g[0][3][1]) :  : ); SB();
  asm volatile("ds_read_b128 a[40:43], %0 offset:1216" :  : "v"(bb) : "memory"); SB();
  asm volatile("s_barrier" :  :  : "memory"); SB();
  asm volatile("s_waitcnt vmcnt(17)" :  :  : "memory"); SB();
  asm volatile("v_mfma_f32_16x16x32_bf16 %0, v[240:243], a[128:131], %0" : "+v"(g[0][0][0]) :  : ); SB();
  asm volatile("buffer_load_dwordx4 a[220:223], %0, %1, %2 offen offset:0 sc1" :  : "v"(vO), "s"(rWS), "s"(20480u) : "memory"); SB();
  asm volatile("v_mfma_f32_16x16x32_bf16 %0, v[240:243], a[132:135], %0" : "+v"(g[0][1][0]) :  : ); SB();
  asm volatile("ds_read2_b32 a[80:81], %0 offset0:176 offset1:177" :  : "v"(aa[2]) : "memory"); SB();
  asm volatile("v_mfma_f32_16x16x32_bf16 %0, v[240:243], a[136:139], %0" : "+v"(g[0][2][0]) :  : ); SB();
  asm volatile("buffer_load_dwordx4 a[224:227], %0, %1, %2 offen offset:1024 sc1" :  : "v"(vO), "s"(rWS), "s"(20480u) : "memory"); SB();
  asm volatile("v_mfma_f32_16x16x32_bf16 %0, v[240:243], a[140:143], %0" : "+v"(g[0][3][0]) :  : ); SB();
  asm volatile("ds_read2_b32 a[82:83], %0 offset0:178 offset1:179" :  : "v"(aa[2]) : "memory"); SB();
  asm volatile("v_mfma_f32_16x16x32_bf16 %0, v[244:247], a[128:131], %0" : "+v"(g[0][0][1]) :  : ); SB();
  asm volatile("ds_read2_b32 a[84:85], %0 offset0:48 offset1:49" :  : "v"(aa[2]) : "memory"); SB();
  asm volatile("v_mfma_f32_16x16x32_bf16 %0, v[244:247], a[132:135], %0" : "+v"(g[0][1][1]) :  : ); SB();
  asm volatile("ds_read2_b32 a[86:87], %0 offset0:50 offset1:51" :  : "v"(aa[2]) : "memory"); SB();
  asm volatile("v_mfma_f32_16x16x32_bf16 %0, v[244:247], a[136:139], %0" : "+v"(g[0][2][1]) :  : ); SB();
  asm volatile("buffer_load_dwordx4 a[228:231], %0, %1, %2 offen offset:2048 sc1" :  : "v"(vO), "s"(rWS), "s"(20480u) : "memory"); SB();
  asm volatile("v_mfma_f32_16x16x32_bf16 %0, v[244:247], a[140:143], %0" : "+v"(g[0][3][1]) :  : ); SB();
  asm volatile("ds_read_b128 a[36:39], %0 offset:704" :  : "v"(bb) : "memory"); SB();
  asm volatile("s_waitcnt vmcnt(16)" :  :  : "memory"); SB();
  asm volatile("v_mfma_f32_16x16x32_bf16 %0, v[248:251], a[148:151], %0" : "+v"(g[0][1][0]) :  : ); SB();
  asm volatile("ds_read2_b32 a[88:89], %0 offset0:176 offset1:177" :  : "v"(aa[3]) : "memory"); SB();
  asm volatile("v_mfma_f32_16x16x32_bf16 %0, v[252:255], a[148:151], %0" : "+v"(g[0][1][1]) :  : ); SB();
  asm volatile("ds_read2_b32 a[90:91], %0 offset0:178 offset1:179" :  : "v"(aa[3]) : "memory"); SB();
  asm volatile("v_mfma_f32_16x16x32_bf16 %0, v[248:251], a[152:155], %0" : "+v"(g[0][2][0]) :  : ); SB();
  asm volatile("ds_read2_b32 a[92:93], %0 offset0:48 offset1:49" :  : "v"(aa[3]) : "memory"); SB();
  asm volatile("v_mfma_f32_16x16x32_bf16 %0, v[252:255], a[152:155], %0" : "+v"(g[0][2][1]) :  : ); SB();
  asm volatile("ds_read2_b32 a[94:95], %0 offset0:50 offset1:51" :  : "v"(aa[3]) : "memory"); SB();
  asm volatile("v_mfma_f32_16x16x32_bf16 %0, v[248:251], a[156:159], %0" : "+v"(g[0][3][0]) :  : ); SB();
  asm volatile("ds_read_b128 a[32:35], %0 offset:192" :  : "v"(bb) : "memory"); SB();
  asm volatile("v_mfma_f32_16x16x32_bf16 %0, v[252:255], a[156:159], %0" : "+v"(g[0][3][1]) :  : ); SB();
  asm volatile("buffer_load_dwordx4 a[232:235], %0, %1, %2 offen offset:3072 sc1" :  : "v"(vO), "s"(rWS), "s"(20480u) : "memory"); SB();
  asm volatile("v_mfma_f32_16x16x32_bf16 %0, v[248:251], a[144:147], %0" : "+v"(g[0][0][0]) :  : ); SB();
  asm volatile("buffer_load_dwordx4 a[236:239], %0, %1, %2 offen offset:0 sc1" :  : "v"(vO), "s"(rWS), "s"(24576u) : "memory"); SB();
  asm volatile("v_mfma_f32_16x16x32_bf16 %0, v[252:255], a[144:147], %0" : "+v"(g[0][0][1]) :  : ); SB();
  asm volatile("buffer_load_dwordx4 a[240:243], %0, %1, %2 offen offset:2048 sc1" :  : "v"(vO), "s"(rWS), "s"(8192u) : "memory"); SB();
  if ((rb | cb | w) == 0) asm volatile("buffer_store_dwordx2 %0, off, %1, 0 sc1" :: "v"(((u32x2){tg0, tg1})), "s"(rWS) : "memory");
  SB();
  ST(17);
  unsigned pw3, p3[3], yw, ys[2], vYr, ya, po;
  asm volatile("s_waitcnt lgkmcnt(5)\n\tv_mfma_f32_16x16x32_bf16 %0, a[64:67], a[60:63], 0" : "=&v"(ac[7]) :  : ); SB();
  asm volatile("ds_read2_b32 a[128:129], %0 offset0:160 offset1:161" :  : "v"(aa[0]) : "memory"); SB();
  asm volatile("" : "+v"(yq[0]), "+v"(yq[1]), "+v"(yq[2]), "+v"(yq[3]), "+v"(yq[4]), "+v"(yq[5]), "+v"(yq[6]), "+v"(yq[7]), "+v"(ykv) :  : "memory"); SB();
  asm volatile("v_mfma_f32_16x16x32_bf16 %0, a[64:67], a[56:59], 0" : "=&v"(ac[6]) :  : ); SB();
  asm volatile("ds_read2_b32 a[130:131], %0 offset0:162 offset1:163" :  : "v"(aa[0]) : "memory"); SB();
  asm volatile("v_add_f32 %0, %0, %1" : "+v"(yq[0][0]) : "v"(yq[2][0]) : ); SB();
  asm volatile("v_mfma_f32_16x16x32_bf16 %0, a[64:67], a[52:55], 0" : "=&v"(ac[5]) :  : ); SB();
  asm volatile("ds_read_b128 a[124:127], %0 offset:3712" :  : "v"(bb) : "memory"); SB();
  asm volatile("v_add_f32 %0, %0, %1" : "+v"(yq[0][1]) : "v"(yq[2][1]) : ); SB();
  asm volatile("v_mfma_f32_16x16x32_bf16 %0, a[64:67], a[48:51], 0" : "=&v"(ac[4]) :  : ); SB();
  asm volatile("ds_write_b128 %0, %1 offset:0" :  : "v"(pw2), "v"(g[0][1][0]) : "memory"); SB();
  asm volatile("v_mfma_f32_16x16x32_bf16 %0, a[68:71], a[56:59], %0" : "+v"(ac[7]) :  : ); SB();
  asm volatile("ds_read_b128 a[120:123], %0 offset:3200" :  : "v"(bb) : "memory"); SB();
  asm volatile("v_add_f32 %0, %0, %1" : "+v"(yq[0][2]) : "v"(yq[2][2]) : ); SB();
  asm volatile("v_mfma_f32_16x16x32_bf16 %0, a[68:71], a[52:55], %0" : "+v"(ac[6]) :  : ); SB();
  asm volatile("ds_read_b128 a[116:119], %0 offset:2688" :  : "v"(bb) : "memory"); SB();
  asm volatile("v_add_f32 %0, %0, %1" : "+v"(yq[0][3]) : "v"(yq[2][3]) : ); SB();
  asm volatile("v_mfma_f32_16x16x32_bf16 %0, a[68:71], a[48:51], %0" : "+v"(ac[5]) :  : ); SB();
  asm volatile("ds_read_b128 a[112:115], %0 offset:2176" :  : "v"(bb) : "memory"); SB();
  asm volatile("v_add_f32 %0, %0, %1" : "+v"(yq[1][0]) : "v"(yq[3][0]) : ); SB();
  asm volatile("v_mfma_f32_16x16x32_bf16 %0, a[68:71], a[44:47], %0" : "+v"(ac[4]) :  : ); SB();
  asm volatile("ds_write_b128 %0, %1 offset:1024" :  : "v"(pw2), "v"(g[0][1][1]) : "memory"); SB();
  asm volatile("v_mfma_f32_16x16x32_bf16 %0, a[72:75], a[52:55], %0" : "+v"(ac[7]) :  : ); SB();
  asm volatile("ds_read2_b32 a[132:133], %0 offset0:32 offset1:33" :  : "v"(aa[0]) : "memory"); SB();
  asm volatile("s_waitcnt vmcnt(13)\n\ts_mov_b64 exec, 1\n\tbuffer_store_dwordx2 %0, off, %1, %2 sc1\n\ts_mov_b64 exec, -1" :  : "v"(((u32x2){tg0, tg1})), "s"(rWS), "s"(sFH) : "memory"); SB();
  asm volatile("v_mfma_f32_16x16x32_bf16 %0, a[72:75], a[48:51], %0" : "+v"(ac[6]) :  : ); SB();
  asm volatile("ds_read2_b32 a[134:135], %0 offset0:34 offset1:35" :  : "v"(aa[0]) : "memory"); SB();
  asm volatile("v_add_f32 %0, %0, %1" : "+v"(yq[1][1]) : "v"(yq[3][1]) : ); SB();
  asm volatile("v_mfma_f32_16x16x32_bf16 %0, a[72:75], a[44:47], %0" : "+v"(ac[5]) :  : ); SB();
  asm volatile("ds_read_b128 a[108:111], %0 offset:1664" :  : "v"(bb) : "memory"); SB();
  asm volatile("v_add_f32 %0, %0, %1" : "+v"(yq[1][2]) : "v"(yq[3][2]) : ); SB();
  asm volatile("v_mfma_f32_16x16x32_bf16 %0, a[64:67], a[44:47], 0" : "=&v"(ac[3]) :  : ); SB();
  asm volatile("ds_write_b128 %0, %1 offset:2048" :  : "v"(pw2), "v"(g[0][2][0]) : "memory"); SB();
  asm volatile("v_mfma_f32_16x16x32_bf16 %0, a[76:79], a[48:51], %0" : "+v"(ac[7]) :  : ); SB();
  asm volatile("ds_read2_b32 a[136:137], %0 offset0:160 offset1:161" :  : "v"(aa[1]) : "memory"); SB();
  asm volatile("v_add_f32 %0, %0, %1" : "+v"(yq[1][3]) : "v"(yq[3][3]) : ); SB();
  asm volatile("v_mfma_f32_16x16x32_bf16 %0, a[76:79], a[44:47], %0" : "+v"(ac[6]) :  : ); SB();
  asm volatile("ds_read2_b32 a[138:139], %0 offset0:162 offset1:163" :  : "v"(aa[1]) : "memory"); SB();
  asm volatile("v_add_f32 %0, %0, %1" : "+v"(yq[4][0]) : "v"(yq[6][0]) : ); SB();
  asm volatile("v_mfma_f32_16x16x32_bf16 %0, a[76:79], a[40:43], %0" : "+v"(ac[5]) :  : ); SB();
  asm volatile("ds_read2_b32 a[140:141], %0 offset0:32 offset1:33" :  : "v"(aa[1]) : "memory"); SB();
  asm volatile("v_add_f32 %0, %0, %1" : "+v"(yq[4][1]) : "v"(yq[6][1]) : ); SB();
  asm volatile("v_mfma_f32_16x16x32_bf16 %0, a[72:75], a[40:43], %0" : "+v"(ac[4]) :  : ); SB();
  asm volatile("ds_write_b128 %0, %1 offset:3072" :  : "v"(pw2), "v"(g[0][2][1]) : "memory"); SB();
  asm volatile("v_mfma_f32_16x16x32_bf16 %0, a[80:83], a[44:47], %0" : "+v"(ac[7]) :  : ); SB();
  asm volatile("ds_read2_b32 a[142:143], %0 offset0:34 offset1:35" :  : "v"(aa[1]) : "memory"); SB();
  asm volatile("v_add_f32 %0, %0, %1" : "+v"(yq[4][2]) : "v"(yq[6][2]) : ); SB();
  asm volatile("v_mfma_f32_16x16x32_bf16 %0, a[80:83], a[40:43], %0" : "+v"(ac[6]) :  : ); SB();
  asm volatile("ds_read_b128 a[104:107], %0 offset:1152" :  : "v"(bb) : "memory"); SB();
  asm volatile("v_add_f32 %0, %0, %1" : "+v"(yq[4][3]) : "v"(yq[6][3]) : ); SB();
  asm volatile("v_mfma_f32_16x16x32_bf16 %0, a[68:71], a[40:43], %0" : "+v"(ac[3]) :  : ); SB();
  asm volatile("ds_read2_b32 a[144:145], %0 offset0:160 offset1:161" :  : "v"(aa[2]) : "memory"); SB();
  asm volatile("v_add_f32 %0, %0, %1" : "+v"(yq[5][0]) : "v"(yq[7][0]) : ); SB();
  asm volatile("v_mfma_f32_16x16x32_bf16 %0, a[64:67], a[40:43], 0" : "=&v"(ac[2]) :  : ); SB();
  asm volatile("ds_write_b128 %0, %1 offset:4096" :  : "v"(pw2), "v"(g[0][3][0]) : "memory"); SB();
  asm volatile("v_mfma_f32_16x16x32_bf16 %0, a[84:87], a[40:43], %0" : "+v"(ac[7]) :  : ); SB();
  asm volatile("ds_read2_b32 a[146:147], %0 offset0:162 offset1:163" :  : "v"(aa[2]) : "memory"); SB();
  asm volatile("buffer_load_dwordx4 a[244:247], %0, %1, %2 offen offset:3072 sc1" :  : "v"(vO), "s"(rWS), "s"(8192u) : "memory"); SB();
  asm volatile("v_mfma_f32_16x16x32_bf16 %0, a[84:87], a[36:39], %0" : "+v"(ac[6]) :  : ); SB();
  asm volatile("ds_read2_b32 a[148:149], %0 offset0:32 offset1:33" :  : "v"(aa[2]) : "memory"); SB();
  asm volatile("v_add_f32 %0, %0, %1" : "+v"(yq[5][1]) : "v"(yq[7][1]) : ); SB();
  asm volatile("v_mfma_f32_16x16x32_bf16 %0, a[80:83], a[36:39], %0" : "+v"(ac[5]) :  : ); SB();
  asm volatile("ds_read2_b32 a[150:151], %0 offset0:34 offset1:35" :  : "v"(aa[2]) : "memory"); SB();
  asm volatile("buffer_load_dwordx4 a[248:251], %0, %1, %2 offen offset:0 sc1" :  : "v"(vO), "s"(rWS), "s"(12288u) : "memory"); SB();
  asm volatile("v_mfma_f32_16x16x32_bf16 %0, a[76:79], a[36:39], %0" : "+v"(ac[4]) :  : ); SB();
  asm volatile("ds_write_b128 %0, %1 offset:5120" :  : "v"(pw2), "v"(g[0][3][1]) : "memory"); SB();
  asm volatile("s_waitcnt lgkmcnt(6)\n\tv_mfma_f32_16x16x32_bf16 %0, a[88:91], a[36:39], %0" : "+v"(ac[7]) :  : ); SB();
  asm volatile("ds_read_b128 a[100:103], %0 offset:640" :  : "v"(bb) : "memory"); SB();
  asm volatile("s_waitcnt lgkmcnt(1)\n\ts_barrier" :  :  : "memory"); SB();
  asm volatile("v_mfma_f32_16x16x32_bf16 %0, a[72:75], a[36:39], %0" : "+v"(ac[3]) :  : ); SB();
  asm volatile("ds_read2_b32 a[152:153], %0 offset0:160 offset1:161" :  : "v"(aa[3]) : "memory"); SB();
  asm volatile("buffer_load_dwordx4 a[252:255], %0, %1, %2 offen offset:1024 sc1" :  : "v"(vO), "s"(rWS), "s"(12288u) : "memory"); SB();
  asm volatile("v_mfma_f32_16x16x32_bf16 %0, a[68:71], a[36:39], %0" : "+v"(ac[2]) :  : ); SB();
  asm volatile("ds_read2_b32 a[154:155], %0 offset0:162 offset1:163" :  : "v"(aa[3]) : "memory"); SB();
  asm volatile("buffer_load_dwordx4 a[0:3], %0, %1, %2 offen offset:2048 sc1" :  : "v"(vO), "s"(rWS), "s"(12288u) : "memory"); SB();
  asm volatile("v_mfma_f32_16x16x32_bf16 %0, a[64:67], a[36:39], 0" : "=&v"(ac[1]) :  : ); SB();
  asm volatile("ds_read_b128 %0, %1 offset:0" : "=&v"(pr[0]) : "v"(pe[0]) : "memory"); SB();
  asm volatile("buffer_load_dwordx4 a[4:7], %0, %1, %2 offen offset:1024 sc1" :  : "v"(vO), "s"(rWS), "s"(24576u) : "memory"); SB();
  asm volatile("v_mfma_f32_16x16x32_bf16 %0, a[92:95], a[32:35], %0" : "+v"(ac[7]) :  : ); SB();
  asm volatile("ds_read2_b32 a[156:157], %0 offset0:32 offset1:33" :  : "v"(aa[3]) : "memory"); SB();
  asm volatile("buffer_load_dwordx4 a[8:11], %0, %1, %2 offen offset:2048 sc1" :  : "v"(vO), "s"(rWS), "s"(24576u) : "memory"); SB();
  asm volatile("v_mfma_f32_16x16x32_bf16 %0, a[88:91], a[32:35], %0" : "+v"(ac[6]) :  : ); SB();
  asm volatile("ds_read2_b32 a[158:159], %0 offset0:34 offset1:35" :  : "v"(aa[3]) : "memory"); SB();
  asm volatile("v_add_f32 %0, %0, %1" : "+v"(yq[5][2]) : "v"(yq[7][2]) : ); SB();
  asm volatile("v_mfma_f32_16x16x32_bf16 %0, a[84:87], a[32:35], %0" : "+v"(ac[5]) :  : ); SB();
  asm volatile("ds_read_b128 a[96:99], %0 offset:128" :  : "v"(bb) : "memory"); SB();
  asm volatile("v_add_f32 %0, %0, %1" : "+v"(yq[5][3]) : "v"(yq[7][3]) : ); SB();
  asm volatile("v_mfma_f32_16x16x32_bf16 %0, a[80:83], a[32:35], %0" : "+v"(ac[4]) :  : ); SB();
  asm volatile("ds_read_b128 %0, %1 offset:1024" : "=&v"(pr[1]) : "v"(pe[0]) : "memory"); SB();
  asm volatile("buffer_load_dwordx4 a[12:15], %0, %1, %2 offen offset:3072 sc1" :  : "v"(vO), "s"(rWS), "s"(24576u) : "memory"); SB();
  asm volatile("v_mfma_f32_16x16x32_bf16 %0, a[76:79], a[32:35], %0" : "+v"(ac[3]) :  : ); SB();
  asm volatile("ds_read_b128 %0, %1 offset:2048" : "=&v"(pr[2]) : "v"(pe[1]) : "memory"); SB();
  asm volatile("v_add_f32 %0, %0, %1" : "+v"(yq[0][0]) : "v"(yq[4][0]) : ); SB();
  asm volatile("v_mfma_f32_16x16x32_bf16 %0, a[72:75], a[32:35], %0" : "+v"(ac[2]) :  : ); SB();
  asm volatile("ds_read_b128 %0, %1 offset:3072" : "=&v"(pr[3]) : "v"(pe[1]) : "memory"); SB();
  asm volatile("v_add_f32 %0, %0, %1" : "+v"(yq[0][1]) : "v"(yq[4][1]) : ); SB();
  asm volatile("v_mfma_f32_16x16x32_bf16 %0, a[68:71], a[32:35], %0" : "+v"(ac[1]) :  : ); SB();
  asm volatile("ds_read_b128 %0, %1 offset:4096" : "=&v"(pr[4]) : "v"(pe[2]) : "memory"); SB();
  asm volatile("buffer_load_dwordx4 a[16:19], %0, %1, %2 offen offset:0 sc1" :  : "v"(vO), "s"(rWS), "s"(28672u) : "memory"); SB();
  asm volatile("v_mfma_f32_16x16x32_bf16 %0, a[64:67], a[32:35], 0" : "=&v"(ac[0]) :  : ); SB();
  asm volatile("ds_read_b128 %0, %1 offset:5120" : "=&v"(pr[5]) : "v"(pe[2]) : "memory"); SB();
  asm volatile("v_add_f32 %0, %0, %1" : "+v"(yq[0][2]) : "v"(yq[4][2]) : ); SB();
  asm volatile("v_mfma_f32_16x16x32_bf16 %0, a[128:131], a[124:127], %0" : "+v"(ac[7]) :  : ); SB();
  asm volatile("ds_read2_b32 a[64:65], %0 offset0:144 offset1:145" :  : "v"(aa[0]) : "memory"); SB();
  asm volatile("v_add_f32 %0, %0, %1" : "+v"(yq[0][3]) : "v"(yq[4][3]) : ); SB();
  asm volatile("v_mfma_f32_16x16x32_bf16 %0, a[128:131], a[120:123], %0" : "+v"(ac[6]) :  : ); SB();
  asm volatile("ds_read2_b32 a[66:67], %0 offset0:146 offset1:147" :  : "v"(aa[0]) : "memory"); SB();
  asm volatile("buffer_load_dwordx4 a[20:23], %0, %1, %2 offen offset:1024 sc1" :  : "v"(vO), "s"(rWS), "s"(28672u) : "memory"); SB();
  asm volatile("v_mfma_f32_16x16x32_bf16 %0, a[128:131], a[116:119], %0" : "+v"(ac[5]) :  : ); SB();
  asm volatile("ds_read_b128 a[60:63], %0 offset:3648" :  : "v"(bb) : "memory"); SB();
  asm volatile("v_add_f32 %0, %0, %1" : "+v"(yq[1][0]) : "v"(yq[5][0]) : ); SB();
  asm volatile("v_mfma_f32_16x16x32_bf16 %0, a[128:131], a[112:115], %0" : "+v"(ac[4]) :  : ); SB();
  pw3 = lb + 98304 + w * 15360 + l16; asm volatile("" : "+v"(pw3)); SB();
  asm volatile("v_mfma_f32_16x16x32_bf16 %0, a[132:135], a[120:123], %0" : "+v"(ac[7]) :  : ); SB();
  asm volatile("ds_read_b128 a[56:59], %0 offset:3136" :  : "v"(bb) : "memory"); SB();
  asm volatile("v_add_f32 %0, %0, %1" : "+v"(yq[1][1]) : "v"(yq[5][1]) : ); SB();
  asm volatile("v_mfma_f32_16x16x32_bf16 %0, a[132:135], a[116:119], %0" : "+v"(ac[6]) :  : ); SB();
  asm volatile("ds_read_b128 a[52:55], %0 offset:2624" :  : "v"(bb) : "memory"); SB();
  asm volatile("s_waitcnt lgkmcnt(5)" : "+v"(pr[0]), "+v"(pr[1]), "+v"(pr[2]), "+v"(pr[3]), "+v"(pr[4]), "+v"(pr[5]) :  : "memory"); SB();
  asm volatile("v_mfma_f32_16x16x32_bf16 %0, a[132:135], a[112:115], %0" : "+v"(ac[5]) :  : ); SB();
  asm volatile("ds_read_b128 a[48:51], %0 offset:2112" :  : "v"(bb) : "memory"); SB();
  asm volatile("v_add_f32 %0, %0, %1" : "+v"(yq[1][2]) : "v"(yq[5][2]) : ); SB();
  asm volatile("v_mfma_f32_16x16x32_bf16 %0, a[132:135], a[108:111], %0" : "+v"(ac[4]) :  : ); SB();
  {
    const f32x4 h0 = ((g[0][0][0] + pr[0]) + (pr[2] + pr[4])) * a1[1][1], h1 = ((g[0][0][1] + pr[1]) + (pr[3] + pr[5])) * a1[1][2];
    ho[2] = (u32x4){pack2(h0[0], h0[1]), pack2(h0[2], h0[3]), pack2(h1[0], h1[1]), pack2(h1[2], h1[3])};
    SB();
  }
  asm volatile("v_mfma_f32_16x16x32_bf16 %0, a[136:139], a[116:119], %0" : "+v"(ac[7]) :  : ); SB();
  asm volatile("ds_read2_b32 a[68:69], %0 offset0:16 offset1:17" :  : "v"(aa[0]) : "memory"); SB();
  asm volatile("v_add_f32 %0, %0, %1" : "+v"(yq[1][3]) : "v"(yq[5][3]) : ); SB();
  asm volatile("v_mfma_f32_16x16x32_bf16 %0, a[136:139], a[112:115], %0" : "+v"(ac[6]) :  : ); SB();
  asm volatile("ds_read2_b32 a[70:71], %0 offset0:18 offset1:19" :  : "v"(aa[0]) : "memory"); SB();
  asm volatile("v_lshlrev_b32 %0, 16, %1" : "=v"(yq[2][0]) : "v"(ykv[0]) : ); SB();
  asm volatile("v_mfma_f32_16x16x32_bf16 %0, a[136:139], a[108:111], %0" : "+v"(ac[5]) :  : ); SB();
  asm volatile("ds_read_b128 a[44:47], %0 offset:1600" :  : "v"(bb) : "memory"); SB();
  asm volatile("v_and_b32 %0, 0xffff0000, %1" : "=v"(yq[2][1]) : "v"(ykv[0]) : ); SB();
  asm volatile("v_mfma_f32_16x16x32_bf16 %0, a[128:131], a[108:111], %0" : "+v"(ac[3]) :  : ); SB();
  asm volatile("buffer_store_dwordx4 %1, %0, %2, %3 offen offset:2048 sc1\n\ts_nop 1" :  : "v"(vH_), "v"(ho[2]), "s"(rWS), "s"(0u) : "memory"); SB();
  asm volatile("v_mfma_f32_16x16x32_bf16 %0, a[140:143], a[112:115], %0" : "+v"(ac[7]) :  : ); SB();
  asm volatile("ds_read2_b32 a[72:73], %0 offset0:144 offset1:145" :  : "v"(aa[1]) : "memory"); SB();
  asm volatile("v_lshlrev_b32 %0, 16, %1" : "=v"(yq[2][2]) : "v"(ykv[1]) : ); SB();
  asm volatile("v_mfma_f32_16x16x32_bf16 %0, a[140:143], a[108:111], %0" : "+v"(ac[6]) :  : ); SB();
  asm volatile("ds_read2_b32 a[74:75], %0 offset0:146 offset1:147" :  : "v"(aa[1]) : "memory"); SB();
  asm volatile("v_and_b32 %0, 0xffff0000, %1" : "=v"(yq[2][3]) : "v"(ykv[1]) : ); SB();
  asm volatile("v_mfma_f32_16x16x32_bf16 %0, a[140:143], a[104:107], %0" : "+v"(ac[5]) :  : ); SB();
  asm volatile("ds_read2_b32 a[76:77], %0 offset0:16 offset1:17" :  : "v"(aa[1]) : "memory"); SB();
  asm volatile("v_lshlrev_b32 %0, 16, %1" : "=v"(yq[3][0]) : "v"(ykv[2]) : ); SB();
  asm volatile("v_mfma_f32_16x16x32_bf16 %0, a[136:139], a[104:107], %0" : "+v"(ac[4]) :  : ); SB();
  p3[0] = lb + 98304 + (w ^ 1) * 15360 + l16; asm volatile("" : "+v"(p3[0])); SB();
  asm volatile("v_mfma_f32_16x16x32_bf16 %0, a[144:147], a[108:111], %0" : "+v"(ac[7]) :  : ); SB();
  asm volatile("ds_read2_b32 a[78:79], %0 offset0:18 offset1:19" :  : "v"(aa[1]) : "memory"); SB();
  asm volatile("v_and_b32 %0, 0xffff0000, %1" : "=v"(yq[3][1]) : "v"(ykv[2]) : ); SB();
  asm volatile("v_mfma_f32_16x16x32_bf16 %0, a[144:147], a[104:107], %0" : "+v"(ac[6]) :  : ); SB();
  asm volatile("ds_read_b128 a[40:43], %0 offset:1088" :  : "v"(bb) : "memory"); SB();
  asm volatile("v_lshlrev_b32 %0, 16, %1" : "=v"(yq[3][2]) : "v"(ykv[3]) : ); SB();
  asm volatile("v_mfma_f32_16x16x32_bf16 %0, a[132:135], a[104:107], %0" : "+v"(ac[3]) :  : ); SB();
  asm volatile("ds_read2_b32 a[80:81], %0 offset0:144 offset1:145" :  : "v"(aa[2]) : "memory"); SB();
  asm volatile("v_and_b32 %0, 0xffff0000, %1" : "=v"(yq[3][3]) : "v"(ykv[3]) : ); SB();
  asm volatile("v_mfma_f32_16x16x32_bf16 %0, a[128:131], a[104:107], %0" : "+v"(ac[2]) :  : ); SB();
  p3[1] = lb + 98304 + (w ^ 2) * 15360 + l16; asm volatile("" : "+v"(p3[1])); SB();
  asm volatile("v_mfma_f32_16x16x32_bf16 %0, a[148:151], a[104:107], %0" : "+v"(ac[7]) :  : ); SB();
  asm volatile("ds_read2_b32 a[82:83], %0 offset0:146 offset1:147" :  : "v"(aa[2]) : "memory"); SB();
  asm volatile("v_fmac_f32 %0, %1, %2" : "+v"(yq[0][0]) : "v"(yq[2][0]), "v"(dD[2]) : ); SB();
  asm volatile("v_mfma_f32_16x16x32_bf16 %0, a[148:151], a[100:103], %0" : "+v"(ac[6]) :  : ); SB();
  asm volatile("ds_read2_b32 a[84:85], %0 offset0:16 offset1:17" :  : "v"(aa[2]) : "memory"); SB();
  asm volatile("v_fmac_f32 %0, %1, %2" : "+v"(yq[0][1]) : "v"(yq[2][1]), "v"(dD[2]) : ); SB();
  asm volatile("v_mfma_f32_16x16x32_bf16 %0, a[144:147], a[100:103], %0" : "+v"(ac[5]) :  : ); SB();
  asm volatile("ds_read2_b32 a[86:87], %0 offset0:18 offset1:19" :  : "v"(aa[2]) : "memory"); SB();
  asm volatile("v_fmac_f32 %0, %1, %2" : "+v"(yq[0][2]) : "v"(yq[2][2]), "v"(dD[2]) : ); SB();
  asm volatile("v_mfma_f32_16x16x32_bf16 %0, a[140:143], a[100:103], %0" : "+v"(ac[4]) :  : ); SB();
  p3[2] = lb + 98304 + (w ^ 3) * 15360 + l16; asm volatile("" : "+v"(p3[2])); SB();
  asm volatile("v_mfma_f32_16x16x32_bf16 %0, a[152:155], a[100:103], %0" : "+v"(ac[7]) :  : ); SB();
  asm volatile("ds_read_b128 a[36:39], %0 offset:576" :  : "v"(bb) : "memory"); SB();
  asm volatile("v_fmac_f32 %0, %1, %2" : "+v"(yq[0][3]) : "v"(yq[2][3]), "v"(dD[2]) : ); SB();
  asm volatile("v_mfma_f32_16x16x32_bf16 %0, a[136:139], a[100:103], %0" : "+v"(ac[3]) :  : ); SB();
  asm volatile("ds_read2_b32 a[88:89], %0 offset0:144 offset1:145" :  : "v"(aa[3]) : "memory"); SB();
  asm volatile("v_fmac_f32 %0, %1, %2" : "+v"(yq[1][0]) : "v"(yq[3][0]), "v"(dD[2]) : ); SB();
  asm volatile("v_mfma_f32_16x16x32_bf16 %0, a[132:135], a[100:103], %0" : "+v"(ac[2]) :  : ); SB();
  asm volatile("ds_read2_b32 a[90:91], %0 offset0:146 offset1:147" :  : "v"(aa[3]) : "memory"); SB();
  asm volatile("v_fmac_f32 %0, %1, %2" : "+v"(yq[1][1]) : "v"(yq[3][1]), "v"(dD[2]) : ); SB();
  asm volatile("v_mfma_f32_16x16x32_bf16 %0, a[128:131], a[100:103], %0" : "+v"(ac[1]) :  : ); SB();
  yw = (lane ^ ((lane >> 3) & 6)) * 16; asm volatile("" : "+v"(yw)); SB();
  asm volatile("v_mfma_f32_16x16x32_bf16 %0, a[156:159], a[96:99], %0" : "+v"(ac[7]) :  : ); SB();
  asm volatile("ds_read2_b32 a[92:93], %0 offset0:16 offset1:17" :  : "v"(aa[3]) : "memory"); SB();
  asm volatile("v_fmac_f32 %0, %1, %2" : "+v"(yq[1][2]) : "v"(yq[3][2]), "v"(dD[2]) : ); SB();
  asm volatile("v_mfma_f32_16x16x32_bf16 %0, a[152:155], a[96:99], %0" : "+v"(ac[6]) :  : ); SB();
  asm volatile("ds_read2_b32 a[94:95], %0 offset0:18 offset1:19" :  : "v"(aa[3]) : "memory"); SB();
  asm volatile("v_fmac_f32 %0, %1, %2" : "+v"(yq[1][3]) : "v"(yq[3][3]), "v"(dD[2]) : ); SB();
  asm volatile("v_mfma_f32_16x16x32_bf16 %0, a[148:151], a[96:99], %0" : "+v"(ac[5]) :  : ); SB();
  asm volatile("ds_read_b128 a[32:35], %0 offset:64" :  : "v"(bb) : "memory"); SB();
  asm volatile("buffer_load_dwordx2 %0, %1, %2, %3 offen sc1" : "=&v"(fh) : "v"(vFH), "s"(rWS), "s"(0u) : "memory"); SB();
  asm volatile("v_mfma_f32_16x16x32_bf16 %0, a[144:147], a[96:99], %0" : "+v"(ac[4]) :  : ); SB();
  yw = yw + lb + 32768 + w * 3072; asm volatile("" : "+v"(yw)); SB();
  asm volatile("v_mfma_f32_16x16x32_bf16 %0, a[140:143], a[96:99], %0" : "+v"(ac[3]) :  : ); SB();
  ya = lb + 32768 + (4 * g4 + q4) * 128 + 8 * p4; asm volatile("" : "+v"(ya)); SB();
  asm volatile("v_mfma_f32_16x16x32_bf16 %0, a[136:139], a[96:99], %0" : "+v"(ac[2]) :  : ); SB();
  po = (2 * g4 + (q4 >> 1)) & 3; asm volatile("" : "+v"(po)); SB();
  asm volatile("v_mfma_f32_16x16x32_bf16 %0, a[132:135], a[96:99], %0" : "+v"(ac[1]) :  : ); SB();
  ya = ya + ((w ^ po) << 5); asm volatile("" : "+v"(ya)); SB();
  asm volatile("v_mfma_f32_16x16x32_bf16 %0, a[128:131], a[96:99], %0" : "+v"(ac[0]) :  : ); SB();
  po = vX + 24 * (g4 & 1); asm volatile("" : "+v"(po)); SB();
  asm volatile("s_waitcnt lgkmcnt(3)\n\tv_mfma_f32_16x16x32_bf16 %0, a[64:67], a[60:63], %0" : "+v"(ac[7]) :  : ); SB();
  asm volatile("ds_read2_b32 a[128:129], %0 offset0:128 offset1:129" :  : "v"(aa[0]) : "memory"); SB();
  asm volatile("global_load_dwordx2 %0, %1, %2 offset:0" : "=&v"(xv[0]) : "v"(vX), "s"(x) : "memory"); SB();
  asm volatile("v_mfma_f32_16x16x32_bf16 %0, a[64:67], a[56:59], %0" : "+v"(ac[6]) :  : ); SB();
  asm volatile("ds_read2_b32 a[130:131], %0 offset0:130 offset1:131" :  : "v"(aa[0]) : "memory"); SB();
  asm volatile("global_load_dwordx2 %0, %1, %2 offset:32" : "=&v"(xv[1]) : "v"(vX), "s"(x) : "memory"); SB();
  asm volatile("v_mfma_f32_16x16x32_bf16 %0, a[64:67], a[52:55], %0" : "+v"(ac[5]) :  : ); SB();
  asm volatile("ds_read_b128 a[124:127], %0 offset:3584" :  : "v"(bb) : "memory"); SB();
  asm volatile("global_load_dwordx2 %0, %1, %2 offset:64" : "=&v"(xv[2]) : "v"(vX), "s"(x) : "memory"); SB();
  asm volatile("v_mfma_f32_16x16x32_bf16 %0, a[64:67], a[48:51], %0" : "+v"(ac[4]) :  : ); SB();
  asm volatile("v_cvt_pk_bf16_f32 %0, %1, %2" : "=v"(y2[0]) : "v"(yq[0][0]), "v"(yq[0][1]) : ); SB();
  asm volatile("v_mfma_f32_16x16x32_bf16 %0, a[68:71], a[56:59], %0" : "+v"(ac[7]) :  : ); SB();
  asm volatile("ds_read_b128 a[120:123], %0 offset:3072" :  : "v"(bb) : "memory"); SB();
  asm volatile("global_load_dwordx2 %0, %1, %2 offset:96" : "=&v"(xv[3]) : "v"(vX), "s"(x) : "memory"); SB();
  asm volatile("v_mfma_f32_16x16x32_bf16 %0, a[68:71], a[52:55], %0" : "+v"(ac[6]) :  : ); SB();
  asm volatile("ds_read_b128 a[96:99], %0 offset:0" :  : "v"(bb) : "memory"); SB();
  asm volatile("v_cvt_pk_bf16_f32 %0, %1, %2" : "=v"(y2[1]) : "v"(yq[0][2]), "v"(yq[0][3]) : ); SB();
  asm volatile("v_mfma_f32_16x16x32_bf16 %0, a[68:71], a[48:51], %0" : "+v"(ac[5]) :  : ); SB();
  asm volatile("ds_read_b128 a[100:103], %0 offset:512" :  : "v"(bb) : "memory"); SB();
  asm volatile("global_load_dwordx2 %0, %1, %2 offset:128" : "=&v"(xv[4]) : "v"(vX), "s"(x) : "memory"); SB();
  asm volatile("v_mfma_f32_16x16x32_bf16 %0, a[68:71], a[44:47], %0" : "+v"(ac[4]) :  : ); SB();
  asm volatile("v_cvt_pk_bf16_f32 %0, %1, %2" : "=v"(y2[2]) : "v"(yq[1][0]), "v"(yq[1][1]) : ); SB();
  asm volatile("v_mfma_f32_16x16x32_bf16 %0, a[72:75], a[52:55], %0" : "+v"(ac[7]) :  : ); SB();
  asm volatile("ds_read2_b32 a[132:133], %0 offset0:0 offset1:1" :  : "v"(aa[0]) : "memory"); SB();
  asm volatile("v_cvt_pk_bf16_f32 %0, %1, %2" : "=v"(y2[3]) : "v"(yq[1][2]), "v"(yq[1][3]) : ); SB();
  asm volatile("v_mfma_f32_16x16x32_bf16 %0, a[72:75], a[48:51], %0" : "+v"(ac[6]) :  : ); SB();
  asm volatile("ds_read2_b32 a[134:135], %0 offset0:2 offset1:3" :  : "v"(aa[0]) : "memory"); SB();
  asm volatile("v_min_u32 %0, -2, %0" : "+v"(y2[3]) :  : ); SB();
  asm volatile("v_mfma_f32_16x16x32_bf16 %0, a[72:75], a[44:47], %0" : "+v"(ac[5]) :  : ); SB();
  asm volatile("ds_read_b128 a[116:119], %0 offset:2560" :  : "v"(bb) : "memory"); SB();
  asm volatile("v_xnor_b32 %0, %0, %1" : "+v"(y2[3]) : "v"(ykv[3]) : ); SB();
  asm volatile("v_mfma_f32_16x16x32_bf16 %0, a[64:67], a[44:47], %0" : "+v"(ac[3]) :  : ); SB();
  asm volatile("v_mfma_f32_16x16x32_bf16 %0, a[76:79], a[48:51], %0" : "+v"(ac[7]) :  : ); SB();
  asm volatile("ds_read_b128 a[104:107], %0 offset:1024" :  : "v"(bb) : "memory"); SB();
  asm volatile("buffer_store_dwordx4 %1, %0, %2, %3 offen offset:0 sc1\n\ts_nop 1" :  : "v"(vF), "v"(y2), "s"(rWS), "s"(uso) : "memory"); SB();
  asm volatile("v_mfma_f32_16x16x32_bf16 %0, a[76:79], a[44:47], %0" : "+v"(ac[6]) :  : ); SB();
  asm volatile("ds_read2_b32 a[136:137], %0 offset0:128 offset1:129" :  : "v"(aa[1]) : "memory"); SB();
  asm volatile("v_mfma_f32_16x16x32_bf16 %0, a[76:79], a[40:43], %0" : "+v"(ac[5]) :  : ); SB();
  asm volatile("ds_read2_b32 a[138:139], %0 offset0:130 offset1:131" :  : "v"(aa[1]) : "memory"); SB();
  asm volatile("v_mfma_f32_16x16x32_bf16 %0, a[72:75], a[40:43], %0" : "+v"(ac[4]) :  : ); SB();
  asm volatile("v_mfma_f32_16x16x32_bf16 %0, a[80:83], a[44:47], %0" : "+v"(ac[7]) :  : ); SB();
  asm volatile("ds_read_b128 a[108:111], %0 offset:1536" :  : "v"(bb) : "memory"); SB();
  asm volatile("v_mfma_f32_16x16x32_bf16 %0, a[80:83], a[40:43], %0" : "+v"(ac[6]) :  : ); SB();
  asm volatile("ds_read_b128 a[112:115], %0 offset:2048" :  : "v"(bb) : "memory"); SB();
  asm volatile("v_mfma_f32_16x16x32_bf16 %0, a[68:71], a[40:43], %0" : "+v"(ac[3]) :  : ); SB();
  asm volatile("ds_read2_b32 a[140:141], %0 offset0:0 offset1:1" :  : "v"(aa[1]) : "memory"); SB();
  asm volatile("v_mfma_f32_16x16x32_bf16 %0, a[64:67], a[40:43], %0" : "+v"(ac[2]) :  : ); SB();
  asm volatile("v_mfma_f32_16x16x32_bf16 %0, a[84:87], a[40:43], %0" : "+v"(ac[7]) :  : ); SB();
  asm volatile("ds_read2_b32 a[142:143], %0 offset0:2 offset1:3" :  : "v"(aa[1]) : "memory"); SB();
  asm volatile("v_mfma_f32_16x16x32_bf16 %0, a[84:87], a[36:39], %0" : "+v"(ac[6]) :  : ); SB();
  asm volatile("ds_read2_b32 a[144:145], %0 offset0:128 offset1:129" :  : "v"(aa[2]) : "memory"); SB();
  asm volatile("v_mfma_f32_16x16x32_bf16 %0, a[80:83], a[36:39], %0" : "+v"(ac[5]) :  : ); SB();
  asm volatile("ds_read2_b32 a[146:147], %0 offset0:130 offset1:131" :  : "v"(aa[2]) : "memory"); SB();
  asm volatile("v_mfma_f32_16x16x32_bf16 %0, a[76:79], a[36:39], %0" : "+v"(ac[4]) :  : ); SB();
  asm volatile("v_mfma_f32_16x16x32_bf16 %0, a[88:91], a[36:39], %0" : "+v"(ac[7]) :  : ); SB();
  asm volatile("ds_read2_b32 a[148:149], %0 offset0:0 offset1:1" :  : "v"(aa[2]) : "memory"); SB();
  asm volatile("v_mfma_f32_16x16x32_bf16 %0, a[72:75], a[36:39], %0" : "+v"(ac[3]) :  : ); SB();
  asm volatile("ds_read2_b32 a[150:151], %0 offset0:2 offset1:3" :  : "v"(aa[2]) : "memory"); SB();
  asm volatile("v_mfma_f32_16x16x32_bf16 %0, a[68:71], a[36:39], %0" : "+v"(ac[2]) :  : ); SB();
  asm volatile("ds_read2_b32 a[152:153], %0 offset0:128 offset1:129" :  : "v"(aa[3]) : "memory"); SB();
  asm volatile("v_mfma_f32_16x16x32_bf16 %0, a[64:67], a[36:39], %0" : "+v"(ac[1]) :  : ); SB();
  asm volatile("s_waitcnt lgkmcnt(7)\n\tv_mfma_f32_16x16x32_bf16 %0, a[92:95], a[32:35], %0" : "+v"(ac[7]) :  : ); SB();
  asm volatile("ds_read2_b32 a[154:155], %0 offset0:130 offset1:131" :  : "v"(aa[3]) : "memory"); SB();
  asm volatile("v_mfma_f32_16x16x32_bf16 %0, a[88:91], a[32:35], %0" : "+v"(ac[6]) :  : ); SB();
  asm volatile("ds_read2_b32 a[156:157], %0 offset0:0 offset1:1" :  : "v"(aa[3]) : "memory"); SB();
  asm volatile("v_mfma_f32_16x16x32_bf16 %0, a[84:87], a[32:35], %0" : "+v"(ac[5]) :  : ); SB();
  asm volatile("ds_read2_b32 a[158:159], %0 offset0:2 offset1:3" :  : "v"(aa[3]) : "memory"); SB();
  asm volatile("v_mfma_f32_16x16x32_bf16 %0, a[80:83], a[32:35], %0" : "+v"(ac[4]) :  : ); SB();
  asm volatile("v_mfma_f32_16x16x32_bf16 %0, a[76:79], a[32:35], %0" : "+v"(ac[3]) :  : ); SB();
  asm volatile("v_mfma_f32_16x16x32_bf16 %0, a[72:75], a[32:35], %0" : "+v"(ac[2]) :  : ); SB();
  asm volatile("v_mfma_f32_16x16x32_bf16 %0, a[68:71], a[32:35], %0" : "+v"(ac[1]) :  : ); SB();
  asm volatile("v_mfma_f32_16x16x32_bf16 %0, a[64:67], a[32:35], %0" : "+v"(ac[0]) :  : ); SB();
  asm volatile("v_mfma_f32_16x16x32_bf16 %0, a[128:131], a[124:127], %0" : "+v"(ac[7]) :  : ); SB();
  asm volatile("v_mfma_f32_16x16x32_bf16 %0, a[128:131], a[120:123], %0" : "+v"(ac[6]) :  : ); SB();
  asm volatile("v_mfma_f32_16x16x32_bf16 %0, a[128:131], a[96:99], %0" : "+v"(ac[0]) :  : ); SB();
  asm volatile("s_waitcnt vmcnt(7)\n\ts_mov_b64 exec, 1\n\tbuffer_store_dwordx2 %0, off, %1, %2 sc1\n\ts_mov_b64 exec, -1" :  : "v"(((u32x2){tg0, tg1})), "s"(rWS), "s"(sFH + 8192) : "memory"); SB();
  asm volatile("v_mfma_f32_16x16x32_bf16 %0, a[128:131], a[100:103], %0" : "+v"(ac[1]) :  : ); SB();
  asm volatile("v_mfma_f32_16x16x32_bf16 %0, a[132:135], a[120:123], %0" : "+v"(ac[7]) :  : ); SB();
  asm volatile("v_mfma_f32_16x16x32_bf16 %0, a[132:135], a[116:119], %0" : "+v"(ac[6]) :  : ); SB();
  asm volatile("v_mfma_f32_16x16x32_bf16 %0, a[128:131], a[104:107], %0" : "+v"(ac[2]) :  : ); SB();
  asm volatile("s_waitcnt vmcnt(7)\n\t.Lc_%=:\n\tv_cmp_ne_u64 vcc, %2, %1\n\ts_cbranch_vccnz .Lr_%=\n\t.Ld_%=:\n\t.subsection 1\n\t.Lr_%=:\n\tv_add_u32 %0, -1, %0\n\ts_nop 1\n\tv_readfirstlane_b32 vcc_hi, %0\n\ts_nop 3\n\ts_cmp_lt_i32 vcc_hi, 1\n\ts_cbranch_scc1 .Ld_%=\n\ts_sleep 2\n\ts_mov_b32 vcc_lo, 0\n\tbuffer_load_dwordx2 %1, %3, %4, vcc_lo offen sc1\n\ts_waitcnt vmcnt(0)\n\ts_branch .Lc_%=\n\t.subsection 0" : "+v"(tmo), "+v"(fh) : "s"(tg), "v"(vFH), "s"(rWS) : "memory", "vcc", "scc"); SB();
  asm volatile("buffer_load_dwordx4 v[164:167], %0, %1, %2 offen offset:0 sc1" :  : "v"(vHr), "s"(rWS), "s"(hso[2]) : "memory"); SB();
  asm volatile("v_mfma_f32_16x16x32_bf16 %0, a[132:135], a[96:99], %0" : "+v"(ac[1]) :  : ); SB();
  asm volatile("buffer_load_dwordx4 v[168:171], %0, %1, %2 offen offset:0 sc1" :  : "v"(vHr), "s"(rWS), "s"(hso[4]) : "memory"); SB();
  asm volatile("v_mfma_f32_16x16x32_bf16 %0, a[136:139], a[116:119], %0" : "+v"(ac[7]) :  : ); SB();
  asm volatile("buffer_load_dwordx4 v[172:175], %0, %1, %2 offen offset:0 sc1" :  : "v"(vHr), "s"(rWS), "s"(hso[6]) : "memory"); SB();
  asm volatile("v_mfma_f32_16x16x32_bf16 %0, a[128:131], a[108:111], %0" : "+v"(ac[3]) :  : ); SB();
  asm volatile("ds_write_b128 %0, %1 offset:0" :  : "v"(dd), "v"(ac[0]) : "memory"); SB();
  asm volatile("v_mfma_f32_16x16x32_bf16 %0, a[132:135], a[100:103], %0" : "+v"(ac[2]) :  : ); SB();
  asm volatile("buffer_load_dwordx4 v[160:163], %0, %1, %2 offen offset:0 sc1" :  : "v"(vHr), "s"(rWS), "s"(hso[0]) : "memory"); SB();
  asm volatile("v_mfma_f32_16x16x32_bf16 %0, a[128:131], a[112:115], %0" : "+v"(ac[4]) :  : ); SB();
  asm volatile("ds_write_b128 %0, %1 offset:1024" :  : "v"(dd), "v"(ac[1]) : "memory"); SB();
  asm volatile("s_waitcnt lgkmcnt(2)\n\tv_mfma_f32_16x16x32_bf16 %0, a[140:143], a[112:115], %0" : "+v"(ac[7]) :  : ); SB();
  asm volatile("buffer_load_dwordx4 v[180:183], %0, %1, %2 offen offset:1024 sc1" :  : "v"(vHr), "s"(rWS), "s"(hso[2]) : "memory"); SB();
  asm volatile("v_mfma_f32_16x16x32_bf16 %0, a[132:135], a[104:107], %0" : "+v"(ac[3]) :  : ); SB();
  asm volatile("v_mfma_f32_16x16x32_bf16 %0, a[136:139], a[96:99], %0" : "+v"(ac[2]) :  : ); SB();
  asm volatile("buffer_load_dwordx4 v[184:187], %0, %1, %2 offen offset:1024 sc1" :  : "v"(vHr), "s"(rWS), "s"(hso[4]) : "memory"); SB();
  asm volatile("v_mfma_f32_16x16x32_bf16 %0, a[132:135], a[108:111], %0" : "+v"(ac[4]) :  : ); SB();
  asm volatile("v_mfma_f32_16x16x32_bf16 %0, a[128:131], a[116:119], %0" : "+v"(ac[5]) :  : ); SB();
  asm volatile("v_mfma_f32_16x16x32_bf16 %0, a[136:139], a[100:103], %0" : "+v"(ac[3]) :  : ); SB();
  asm volatile("v_mfma_f32_16x16x32_bf16 %0, a[136:139], a[112:115], %0" : "+v"(ac[6]) :  : ); SB();
  asm volatile("ds_write_b128 %0, %1 offset:2048" :  : "v"(dd), "v"(ac[2]) : "memory"); SB();
  asm volatile("v_mfma_f32_16x16x32_bf16 %0, a[136:139], a[104:107], %0" : "+v"(ac[4]) :  : ); SB();
  asm volatile("buffer_load_dwordx4 v[188:191], %0, %1, %2 offen offset:1024 sc1" :  : "v"(vHr), "s"(rWS), "s"(hso[6]) : "memory"); SB();
  asm volatile("v_mfma_f32_16x16x32_bf16 %0, a[132:135], a[112:115], %0" : "+v"(ac[5]) :  : ); SB();
  asm volatile("buffer_load_dwordx4 v[176:179], %0, %1, %2 offen offset:1024 sc1" :  : "v"(vHr), "s"(rWS), "s"(hso[0]) : "memory"); SB();
  asm volatile("v_mfma_f32_16x16x32_bf16 %0, a[140:143], a[96:99], %0" : "+v"(ac[3]) :  : ); SB();
  asm volatile("v_mfma_f32_16x16x32_bf16 %0, a[140:143], a[108:111], %0" : "+v"(ac[6]) :  : ); SB();
  asm volatile("v_mfma_f32_16x16x32_bf16 %0, a[140:143], a[100:103], %0" : "+v"(ac[4]) :  : ); SB();
  asm volatile("buffer_load_dwordx4 v[196:199], %0, %1, %2 offen offset:0 sc1" :  : "v"(vHr), "s"(rWS), "s"(hso[3]) : "memory"); SB();
  asm volatile("v_mfma_f32_16x16x32_bf16 %0, a[136:139], a[108:111], %0" : "+v"(ac[5]) :  : ); SB();
  asm volatile("v_mfma_f32_16x16x32_bf16 %0, a[144:147], a[108:111], %0" : "+v"(ac[7]) :  : ); SB();
  asm volatile("ds_write_b128 %0, %1 offset:3072" :  : "v"(dd), "v"(ac[3]) : "memory"); SB();
  asm volatile("v_mfma_f32_16x16x32_bf16 %0, a[144:147], a[104:107], %0" : "+v"(ac[6]) :  : ); SB();
  asm volatile("buffer_load_dwordx4 v[200:203], %0, %1, %2 offen offset:0 sc1" :  : "v"(vHr), "s"(rWS), "s"(hso[5]) : "memory"); SB();
  asm volatile("v_mfma_f32_16x16x32_bf16 %0, a[144:147], a[96:99], %0" : "+v"(ac[4]) :  : ); SB();
  asm volatile("v_mfma_f32_16x16x32_bf16 %0, a[140:143], a[104:107], %0" : "+v"(ac[5]) :  : ); SB();
  asm volatile("v_mfma_f32_16x16x32_bf16 %0, a[148:151], a[104:107], %0" : "+v"(ac[7]) :  : ); SB();
  asm volatile("buffer_load_dwordx4 v[204:207], %0, %1, %2 offen offset:0 sc1" :  : "v"(vHr), "s"(rWS), "s"(hso[7]) : "memory"); SB();
  asm volatile("v_mfma_f32_16x16x32_bf16 %0, a[148:151], a[100:103], %0" : "+v"(ac[6]) :  : ); SB();
  asm volatile("v_mfma_f32_16x16x32_bf16 %0, a[144:147], a[100:103], %0" : "+v"(ac[5]) :  : ); SB();
  asm volatile("ds_write_b128 %0, %1 offset:4096" :  : "v"(dd), "v"(ac[4]) : "memory"); SB();
  asm volatile("v_mfma_f32_16x16x32_bf16 %0, a[152:155], a[100:103], %0" : "+v"(ac[7]) :  : ); SB();
  asm volatile("v_mfma_f32_16x16x32_bf16 %0, a[152:155], a[96:99], %0" : "+v"(ac[6]) :  : ); SB();
  asm volatile("v_mfma_f32_16x16x32_bf16 %0, a[148:151], a[96:99], %0" : "+v"(ac[5]) :  : ); SB();
  asm volatile("v_mfma_f32_16x16x32_bf16 %0, a[156:159], a[96:99], %0" : "+v"(ac[7]) :  : ); SB();
  asm volatile("buffer_load_dwordx4 v[192:195], %0, %1, %2 offen offset:0 sc1" :  : "v"(vHr), "s"(rWS), "s"(hso[1]) : "memory"); SB();
  asm volatile("s_nop 5" : "+v"(ac[5]), "+v"(ac[6]), "+v"(ac[7]) :  : ); SB();
  asm volatile("ds_write_b128 %0, %1 offset:5120" :  : "v"(dd), "v"(ac[5]) : "memory"); SB();
  asm volatile("ds_write_b128 %0, %1 offset:6144" :  : "v"(dd), "v"(ac[6]) : "memory"); SB();
  asm volatile("ds_write_b128 %0, %1 offset:7168" :  : "v"(dd), "v"(ac[7]) : "memory"); SB();
  ST(18);
  f32x4 yp[2][4], o3[4][5];
  u32x4 yk[2], yo[2];
  unsigned vtmp;
  asm volatile("ds_read_b128 %0, %1 offset:0" : "=&v"(yk[0]) : "v"(ykr) : "memory"); SB();
  asm volatile("ds_read_b128 %0, %1 offset:12928" : "=&v"(yk[1]) : "v"(ykr) : "memory"); SB();
  asm volatile("s_waitcnt lgkmcnt(0)\n\ts_barrier" :  :  : "memory"); SB();
  asm volatile("ds_read_b128 %0, %1 offset:0" : "=&v"(yp[0][0]) : "v"(ydr) : "memory"); SB();
  asm volatile("ds_read_b128 %0, %1 offset:256" : "=&v"(yp[0][1]) : "v"(ydr) : "memory"); SB();
  asm volatile("ds_read_b128 %0, %1 offset:8192" : "=&v"(yp[0][2]) : "v"(ydr) : "memory"); SB();
  asm volatile("ds_read_b128 %0, %1 offset:8448" : "=&v"(yp[0][3]) : "v"(ydr) : "memory"); SB();
  asm volatile("ds_read_b128 %0, %1 offset:16384" : "=&v"(yp[1][0]) : "v"(ydr) : "memory"); SB();
  asm volatile("ds_read_b128 %0, %1 offset:16640" : "=&v"(yp[1][1]) : "v"(ydr) : "memory"); SB();
  asm volatile("ds_read_b128 %0, %1 offset:24576" : "=&v"(yp[1][2]) : "v"(ydr) : "memory"); SB();
  asm volatile("ds_read_b128 %0, %1 offset:24832" : "=&v"(yp[1][3]) : "v"(ydr) : "memory"); SB();
  asm volatile("" : "+v"(yk[0]), "+v"(yk[1]) :  : ); SB();
  f32x2 yd[2][4];
#pragma unroll
  for (int n = 0; n < 8; ++n) { const float Dv = dD[n >> 2]; yd[n >> 2][n & 3] = unpk(yk[n >> 2][n & 3]) * (f32x2){Dv, Dv}; asm volatile("" : "+v"(yd[n >> 2][n & 3])); }
  SB();
  asm volatile("s_waitcnt lgkmcnt(3)" : "+v"(yp[0][0]), "+v"(yp[0][1]), "+v"(yp[0][2]), "+v"(yp[0][3]) :  : "memory"); SB();
  {
    const f32x4 s0 = yp[0][0] + yp[0][2], s1 = yp[0][1] + yp[0][3];
    yo[0] = (u32x4){pk2((f32x2){s0[0], s0[1]} + yd[0][0]), pk2((f32x2){s0[2], s0[3]} + yd[0][1]), pk2((f32x2){s1[0], s1[1]} + yd[0][2]), ~(nohole(pk2((f32x2){s1[2], s1[3]} + yd[0][3])) ^ yk[0][3])};
    SB();
  }
  asm volatile("buffer_store_dwordx4 %1, %0, %2, %3 offen offset:0 sc1\n\ts_nop 1" :  : "v"(vF), "v"(yo[0]), "s"(rWS), "s"(W_KV + 0) : "memory"); SB();
  asm volatile("s_waitcnt lgkmcnt(0)" : "+v"(yp[1][0]), "+v"(yp[1][1]), "+v"(yp[1][2]), "+v"(yp[1][3]) :  : "memory"); SB();
  {
    const f32x4 s0 = yp[1][0] + yp[1][2], s1 = yp[1][1] + yp[1][3];
    yo[1] = (u32x4){pk2((f32x2){s0[0], s0[1]} + yd[1][0]), pk2((f32x2){s0[2], s0[3]} + yd[1][1]), pk2((f32x2){s1[0], s1[1]} + yd[1][2]), ~(nohole(pk2((f32x2){s1[2], s1[3]} + yd[1][3])) ^ yk[1][3])};
    SB();
  }
  asm volatile("buffer_store_dwordx4 %1, %0, %2, %3 offen offset:0 sc1\n\ts_nop 1" :  : "v"(vF), "v"(yo[1]), "s"(rWS), "s"(W_KV + 4096) : "memory"); SB();
  ST(19);
  ST(20);
  f32x2 xf[5][2], sv[5][2];
  float rd, mr;
  asm volatile("s_waitcnt vmcnt(10)" :  :  : "memory"); SB();
  asm volatile("v_mfma_f32_16x16x32_bf16 %0, a[160:163], v[164:167], 0" : "=&v"(o3[1][0]) :  : ); SB();
  asm volatile("buffer_load_dwordx2 %0, %1, %2, %3 offen sc1" : "=&v"(fi) : "v"(vFH), "s"(rWS), "s"(8192u) : "memory"); SB();
  asm volatile("v_mfma_f32_16x16x32_bf16 %0, a[164:167], v[164:167], 0" : "=&v"(o3[1][1]) :  : ); SB();
  asm volatile("buffer_load_dwordx4 v[212:215], %0, %1, %2 offen offset:1024 sc1" :  : "v"(vHr), "s"(rWS), "s"(hso[3]) : "memory"); SB();
  asm volatile("v_mfma_f32_16x16x32_bf16 %0, a[168:171], v[164:167], 0" : "=&v"(o3[1][2]) :  : ); SB();
  asm volatile("v_mfma_f32_16x16x32_bf16 %0, a[172:175], v[164:167], 0" : "=&v"(o3[1][3]) :  : ); SB();
  asm volatile("buffer_load_dwordx4 v[216:219], %0, %1, %2 offen offset:1024 sc1" :  : "v"(vHr), "s"(rWS), "s"(hso[5]) : "memory"); SB();
  asm volatile("v_mfma_f32_16x16x32_bf16 %0, a[176:179], v[164:167], 0" : "=&v"(o3[1][4]) :  : ); SB();
  asm volatile("v_mfma_f32_16x16x32_bf16 %0, a[160:163], v[168:171], 0" : "=&v"(o3[2][0]) :  : ); SB();
  asm volatile("v_mfma_f32_16x16x32_bf16 %0, a[164:167], v[168:171], 0" : "=&v"(o3[2][1]) :  : ); SB();
  asm volatile("buffer_load_dwordx4 v[220:223], %0, %1, %2 offen offset:1024 sc1" :  : "v"(vHr), "s"(rWS), "s"(hso[7]) : "memory"); SB();
  asm volatile("v_mfma_f32_16x16x32_bf16 %0, a[168:171], v[168:171], 0" : "=&v"(o3[2][2]) :  : ); SB();
  asm volatile("v_mfma_f32_16x16x32_bf16 %0, a[172:175], v[168:171], 0" : "=&v"(o3[2][3]) :  : ); SB();
  asm volatile("v_mfma_f32_16x16x32_bf16 %0, a[176:179], v[168:171], 0" : "=&v"(o3[2][4]) :  : ); SB();
  asm volatile("v_mfma_f32_16x16x32_bf16 %0, a[160:163], v[172:175], 0" : "=&v"(o3[3][0]) :  : ); SB();
  asm volatile("buffer_load_dwordx4 v[208:211], %0, %1, %2 offen offset:1024 sc1" :  : "v"(vHr), "s"(rWS), "s"(hso[1]) : "memory"); SB();
  asm volatile("v_mfma_f32_16x16x32_bf16 %0, a[164:167], v[172:175], 0" : "=&v"(o3[3][1]) :  : ); SB();
  asm volatile("v_mfma_f32_16x16x32_bf16 %0, a[168:171], v[172:175], 0" : "=&v"(o3[3][2]) :  : ); SB();
  asm volatile("v_mfma_f32_16x16x32_bf16 %0, a[172:175], v[172:175], 0" : "=&v"(o3[3][3]) :  : ); SB();
  asm volatile("v_mfma_f32_16x16x32_bf16 %0, a[176:179], v[172:175], 0" : "=&v"(o3[3][4]) :  : ); SB();
  asm volatile("v_mfma_f32_16x16x32_bf16 %0, a[160:163], v[160:163], 0" : "=&v"(o3[0][0]) :  : ); SB();
  asm volatile("v_mfma_f32_16x16x32_bf16 %0, a[164:167], v[160:163], 0" : "=&v"(o3[0][1]) :  : ); SB();
  asm volatile("v_mfma_f32_16x16x32_bf16 %0, a[168:171], v[160:163], 0" : "=&v"(o3[0][2]) :  : ); SB();
  asm volatile("v_mfma_f32_16x16x32_bf16 %0, a[172:175], v[160:163], 0" : "=&v"(o3[0][3]) :  : ); SB();
  asm volatile("v_mfma_f32_16x16x32_bf16 %0, a[176:179], v[160:163], 0" : "=&v"(o3[0][4]) :  : ); SB();
  asm volatile("s_waitcnt vmcnt(11)" :  :  : "memory"); SB();
  asm volatile("v_mfma_f32_16x16x32_bf16 %0, a[180:183], v[180:183], %0" : "+v"(o3[1][0]) :  : ); SB();
  asm volatile("v_rcp_f32 %0, %1" : "=v"(rd) : "v"(xd[0]) : ); SB();
  asm volatile("v_mfma_f32_16x16x32_bf16 %0, a[184:187], v[180:183], %0" : "+v"(o3[1][1]) :  : ); SB();
  asm volatile("" : "+v"(xv[0]), "+v"(xv[1]), "+v"(xv[2]), "+v"(xv[3]), "+v"(xv[4]) :  : "memory"); SB();
  asm volatile("v_mul_f32 %0, -%1, %2" : "=v"(mr) : "v"(xm[0]), "v"(rd) : ); SB();
  asm volatile("v_mfma_f32_16x16x32_bf16 %0, a[188:191], v[180:183], %0" : "+v"(o3[1][2]) :  : ); SB();
  asm volatile("v_lshlrev_b32 %0, 16, %1" : "=v"(xf[0][0][0]) : "v"(xv[0][0]) : ); SB();
  asm volatile("v_mfma_f32_16x16x32_bf16 %0, a[192:195], v[180:183], %0" : "+v"(o3[1][3]) :  : ); SB();
  asm volatile("v_and_b32 %0, 0xffff0000, %1" : "=v"(xf[0][0][1]) : "v"(xv[0][0]) : ); SB();
  asm volatile("v_mfma_f32_16x16x32_bf16 %0, a[196:199], v[180:183], %0" : "+v"(o3[1][4]) :  : ); SB();
  asm volatile("v_lshlrev_b32 %0, 16, %1" : "=v"(xf[0][1][0]) : "v"(xv[0][1]) : ); SB();
  asm volatile("v_mfma_f32_16x16x32_bf16 %0, a[180:183], v[184:187], %0" : "+v"(o3[2][0]) :  : ); SB();
  asm volatile("v_and_b32 %0, 0xffff0000, %1" : "=v"(xf[0][1][1]) : "v"(xv[0][1]) : ); SB();
  asm volatile("v_mfma_f32_16x16x32_bf16 %0, a[184:187], v[184:187], %0" : "+v"(o3[2][1]) :  : ); SB();
  asm volatile("v_lshlrev_b32 %0, 16, %1" : "=v"(xf[1][0][0]) : "v"(xv[1][0]) : ); SB();
  asm volatile("v_mfma_f32_16x16x32_bf16 %0, a[188:191], v[184:187], %0" : "+v"(o3[2][2]) :  : ); SB();
  asm volatile("v_and_b32 %0, 0xffff0000, %1" : "=v"(xf[1][0][1]) : "v"(xv[1][0]) : ); SB();
  asm volatile("v_mfma_f32_16x16x32_bf16 %0, a[192:195], v[184:187], %0" : "+v"(o3[2][3]) :  : ); SB();
  asm volatile("v_lshlrev_b32 %0, 16, %1" : "=v"(xf[1][1][0]) : "v"(xv[1][1]) : ); SB();
  asm volatile("v_mfma_f32_16x16x32_bf16 %0, a[196:199], v[184:187], %0" : "+v"(o3[2][4]) :  : ); SB();
  asm volatile("v_and_b32 %0, 0xffff0000, %1" : "=v"(xf[1][1][1]) : "v"(xv[1][1]) : ); SB();
  asm volatile("v_mfma_f32_16x16x32_bf16 %0, a[180:183], v[188:191], %0" : "+v"(o3[3][0]) :  : ); SB();
  asm volatile("v_lshlrev_b32 %0, 16, %1" : "=v"(xf[2][0][0]) : "v"(xv[2][0]) : ); SB();
  asm volatile("v_mfma_f32_16x16x32_bf16 %0, a[184:187], v[188:191], %0" : "+v"(o3[3][1]) :  : ); SB();
  asm volatile("v_and_b32 %0, 0xffff0000, %1" : "=v"(xf[2][0][1]) : "v"(xv[2][0]) : ); SB();
  asm volatile("v_mfma_f32_16x16x32_bf16 %0, a[188:191], v[188:191], %0" : "+v"(o3[3][2]) :  : ); SB();
  asm volatile("v_lshlrev_b32 %0, 16, %1" : "=v"(xf[2][1][0]) : "v"(xv[2][1]) : ); SB();
  asm volatile("v_mfma_f32_16x16x32_bf16 %0, a[192:195], v[188:191], %0" : "+v"(o3[3][3]) :  : ); SB();
  asm volatile("v_and_b32 %0, 0xffff0000, %1" : "=v"(xf[2][1][1]) : "v"(xv[2][1]) : ); SB();
  asm volatile("v_mfma_f32_16x16x32_bf16 %0, a[196:199], v[188:191], %0" : "+v"(o3[3][4]) :  : ); SB();
  asm volatile("v_lshlrev_b32 %0, 16, %1" : "=v"(xf[3][0][0]) : "v"(xv[3][0]) : ); SB();
  asm volatile("v_mfma_f32_16x16x32_bf16 %0, a[180:183], v[176:179], %0" : "+v"(o3[0][0]) :  : ); SB();
  asm volatile("v_and_b32 %0, 0xffff0000, %1" : "=v"(xf[3][0][1]) : "v"(xv[3][0]) : ); SB();
  asm volatile("v_mfma_f32_16x16x32_bf16 %0, a[184:187], v[176:179], %0" : "+v"(o3[0][1]) :  : ); SB();
  asm volatile("v_lshlrev_b32 %0, 16, %1" : "=v"(xf[3][1][0]) : "v"(xv[3][1]) : ); SB();
  asm volatile("v_mfma_f32_16x16x32_bf16 %0, a[188:191], v[176:179], %0" : "+v"(o3[0][2]) :  : ); SB();
  asm volatile("v_and_b32 %0, 0xffff0000, %1" : "=v"(xf[3][1][1]) : "v"(xv[3][1]) : ); SB();
  asm volatile("v_mfma_f32_16x16x32_bf16 %0, a[192:195], v[176:179], %0" : "+v"(o3[0][3]) :  : ); SB();
  asm volatile("v_lshlrev_b32 %0, 16, %1" : "=v"(xf[4][0][0]) : "v"(xv[4][0]) : ); SB();
  asm volatile("v_mfma_f32_16x16x32_bf16 %0, a[196:199], v[176:179], %0" : "+v"(o3[0][4]) :  : ); SB();
  asm volatile("v_and_b32 %0, 0xffff0000, %1" : "=v"(xf[4][0][1]) : "v"(xv[4][0]) : ); SB();
  asm volatile("s_waitcnt vmcnt(7)" :  :  : "memory"); SB();
  asm volatile("v_mfma_f32_16x16x32_bf16 %0, a[200:203], v[196:199], %0" : "+v"(o3[1][0]) :  : ); SB();
  asm volatile("v_lshlrev_b32 %0, 16, %1" : "=v"(xf[4][1][0]) : "v"(xv[4][1]) : ); SB();
  asm volatile("v_mfma_f32_16x16x32_bf16 %0, a[204:207], v[196:199], %0" : "+v"(o3[1][1]) :  : ); SB();
  asm volatile("v_and_b32 %0, 0xffff0000, %1" : "=v"(xf[4][1][1]) : "v"(xv[4][1]) : ); SB();
  asm volatile("v_mfma_f32_16x16x32_bf16 %0, a[208:211], v[196:199], %0" : "+v"(o3[1][2]) :  : ); SB();
  asm volatile("v_fma_f32 %0, %1, %2, %3" : "=v"(sv[0][0][0]) : "v"(xf[0][0][0]), "v"(rd), "v"(mr) : ); SB();
  asm volatile("v_mfma_f32_16x16x32_bf16 %0, a[212:215], v[196:199], %0" : "+v"(o3[1][3]) :  : ); SB();
  asm volatile("v_fma_f32 %0, %1, %2, %3" : "=v"(sv[0][0][1]) : "v"(xf[0][0][1]), "v"(rd), "v"(mr) : ); SB();
  asm volatile("v_mfma_f32_16x16x32_bf16 %0, a[216:219], v[196:199], %0" : "+v"(o3[1][4]) :  : ); SB();
  asm volatile("v_fma_f32 %0, %1, %2, %3" : "=v"(sv[0][1][0]) : "v"(xf[0][1][0]), "v"(rd), "v"(mr) : ); SB();
  asm volatile("v_mfma_f32_16x16x32_bf16 %0, a[200:203], v[200:203], %0" : "+v"(o3[2][0]) :  : ); SB();
  asm volatile("v_fma_f32 %0, %1, %2, %3" : "=v"(sv[0][1][1]) : "v"(xf[0][1][1]), "v"(rd), "v"(mr) : ); SB();
  asm volatile("v_mfma_f32_16x16x32_bf16 %0, a[204:207], v[200:203], %0" : "+v"(o3[2][1]) :  : ); SB();
  asm volatile("v_fma_f32 %0, %1, %2, %3" : "=v"(sv[1][0][0]) : "v"(xf[1][0][0]), "v"(rd), "v"(mr) : ); SB();
  asm volatile("v_mfma_f32_16x16x32_bf16 %0, a[208:211], v[200:203], %0" : "+v"(o3[2][2]) :  : ); SB();
  asm volatile("v_fma_f32 %0, %1, %2, %3" : "=v"(sv[1][0][1]) : "v"(xf[1][0][1]), "v"(rd), "v"(mr) : ); SB();
  asm volatile("v_mfma_f32_16x16x32_bf16 %0, a[212:215], v[200:203], %0" : "+v"(o3[2][3]) :  : ); SB();
  asm volatile("v_fma_f32 %0, %1, %2, %3" : "=v"(sv[1][1][0]) : "v"(xf[1][1][0]), "v"(rd), "v"(mr) : ); SB();
  asm volatile("v_mfma_f32_16x16x32_bf16 %0, a[216:219], v[200:203], %0" : "+v"(o3[2][4]) :  : ); SB();
  asm volatile("v_fma_f32 %0, %1, %2, %3" : "=v"(sv[1][1][1]) : "v"(xf[1][1][1]), "v"(rd), "v"(mr) : ); SB();
  asm volatile("v_mfma_f32_16x16x32_bf16 %0, a[200:203], v[204:207], %0" : "+v"(o3[3][0]) :  : ); SB();
  asm volatile("v_fma_f32 %0, %1, %2, %3" : "=v"(sv[2][0][0]) : "v"(xf[2][0][0]), "v"(rd), "v"(mr) : ); SB();
  asm volatile("v_mfma_f32_16x16x32_bf16 %0, a[204:207], v[204:207], %0" : "+v"(o3[3][1]) :  : ); SB();
  asm volatile("v_fma_f32 %0, %1, %2, %3" : "=v"(sv[2][0][1]) : "v"(xf[2][0][1]), "v"(rd), "v"(mr) : ); SB();
  asm volatile("v_mfma_f32_16x16x32_bf16 %0, a[208:211], v[204:207], %0" : "+v"(o3[3][2]) :  : ); SB();
  asm volatile("v_fma_f32 %0, %1, %2, %3" : "=v"(sv[2][1][0]) : "v"(xf[2][1][0]), "v"(rd), "v"(mr) : ); SB();
  asm volatile("v_mfma_f32_16x16x32_bf16 %0, a[212:215], v[204:207], %0" : "+v"(o3[3][3]) :  : ); SB();
  asm volatile("v_fma_f32 %0, %1, %2, %3" : "=v"(sv[2][1][1]) : "v"(xf[2][1][1]), "v"(rd), "v"(mr) : ); SB();
  asm volatile("v_mfma_f32_16x16x32_bf16 %0, a[216:219], v[204:207], %0" : "+v"(o3[3][4]) :  : ); SB();
  asm volatile("v_fma_f32 %0, %1, %2, %3" : "=v"(sv[3][0][0]) : "v"(xf[3][0][0]), "v"(rd), "v"(mr) : ); SB();
  asm volatile("v_mfma_f32_16x16x32_bf16 %0, a[200:203], v[192:195], %0" : "+v"(o3[0][0]) :  : ); SB();
  asm volatile("v_fma_f32 %0, %1, %2, %3" : "=v"(sv[3][0][1]) : "v"(xf[3][0][1]), "v"(rd), "v"(mr) : ); SB();
  asm volatile("v_mfma_f32_16x16x32_bf16 %0, a[204:207], v[192:195], %0" : "+v"(o3[0][1]) :  : ); SB();
  asm volatile("v_fma_f32 %0, %1, %2, %3" : "=v"(sv[3][1][0]) : "v"(xf[3][1][0]), "v"(rd), "v"(mr) : ); SB();
  asm volatile("v_mfma_f32_16x16x32_bf16 %0, a[208:211], v[192:195], %0" : "+v"(o3[0][2]) :  : ); SB();
  asm volatile("v_fma_f32 %0, %1, %2, %3" : "=v"(sv[3][1][1]) : "v"(xf[3][1][1]), "v"(rd), "v"(mr) : ); SB();
  asm volatile("v_mfma_f32_16x16x32_bf16 %0, a[212:215], v[192:195], %0" : "+v"(o3[0][3]) :  : ); SB();
  asm volatile("v_fma_f32 %0, %1, %2, %3" : "=v"(sv[4][0][0]) : "v"(xf[4][0][0]), "v"(rd), "v"(mr) : ); SB();
  asm volatile("v_mfma_f32_16x16x32_bf16 %0, a[216:219], v[192:195], %0" : "+v"(o3[0][4]) :  : ); SB();
  asm volatile("v_fma_f32 %0, %1, %2, %3" : "=v"(sv[4][0][1]) : "v"(xf[4][0][1]), "v"(rd), "v"(mr) : ); SB();
  asm volatile("s_waitcnt vmcnt(4)\n\t.Lc_%=:\n\tv_cmp_ne_u64 vcc, %2, %1\n\ts_cbranch_vccnz .Lr_%=\n\t.Ld_%=:\n\t.subsection 1\n\t.Lr_%=:\n\tv_add_u32 %0, -1, %0\n\ts_nop 1\n\tv_readfirstlane_b32 vcc_hi, %0\n\ts_nop 3\n\ts_cmp_lt_i32 vcc_hi, 1\n\ts_cbranch_scc1 .Ld_%=\n\ts_sleep 2\n\ts_mov_b32 vcc_lo, 8192\n\tbuffer_load_dwordx2 %1, %3, %4, vcc_lo offen sc1\n\ts_waitcnt vmcnt(0)\n\ts_branch .Lc_%=\n\t.subsection 0" : "+v"(tmo), "+v"(fi) : "s"(tg), "v"(vFH), "s"(rWS) : "memory", "vcc", "scc"); SB();
  asm volatile("buffer_load_dwordx4 v[228:231], %0, %1, %2 offen offset:2048 sc1" :  : "v"(vHr), "s"(rWS), "s"(hso[2]) : "memory"); SB();
  asm volatile("buffer_load_dwordx4 v[232:235], %0, %1, %2 offen offset:2048 sc1" :  : "v"(vHr), "s"(rWS), "s"(hso[4]) : "memory"); SB();
  asm volatile("buffer_load_dwordx4 v[236:239], %0, %1, %2 offen offset:2048 sc1" :  : "v"(vHr), "s"(rWS), "s"(hso[6]) : "memory"); SB();
  asm volatile("buffer_load_dwordx4 v[224:227], %0, %1, %2 offen offset:2048 sc1" :  : "v"(vHr), "s"(rWS), "s"(hso[0]) : "memory"); SB();
  asm volatile("buffer_load_dwordx4 v[244:247], %0, %1, %2 offen offset:2048 sc1" :  : "v"(vHr), "s"(rWS), "s"(hso[3]) : "memory"); SB();
  asm volatile("buffer_load_dwordx4 v[248:251], %0, %1, %2 offen offset:2048 sc1" :  : "v"(vHr), "s"(rWS), "s"(hso[5]) : "memory"); SB();
  asm volatile("buffer_load_dwordx4 v[252:255], %0, %1, %2 offen offset:2048 sc1" :  : "v"(vHr), "s"(rWS), "s"(hso[7]) : "memory"); SB();
  asm volatile("s_waitcnt vmcnt(7)" :  :  : "memory"); SB();
  asm volatile("v_mfma_f32_16x16x32_bf16 %0, a[220:223], v[212:215], %0" : "+v"(o3[1][0]) :  : ); SB();
  asm volatile("v_fma_f32 %0, %1, %2, %3" : "=v"(sv[4][1][0]) : "v"(xf[4][1][0]), "v"(rd), "v"(mr) : ); SB();
  asm volatile("v_mfma_f32_16x16x32_bf16 %0, a[224:227], v[212:215], %0" : "+v"(o3[1][1]) :  : ); SB();
  asm volatile("v_fma_f32 %0, %1, %2, %3" : "=v"(sv[4][1][1]) : "v"(xf[4][1][1]), "v"(rd), "v"(mr) : ); SB();
  asm volatile("v_mfma_f32_16x16x32_bf16 %0, a[228:231], v[212:215], %0" : "+v"(o3[1][2]) :  : ); SB();
  asm volatile("v_mfma_f32_16x16x32_bf16 %0, a[232:235], v[212:215], %0" : "+v"(o3[1][3]) :  : ); SB();
  asm volatile("v_mfma_f32_16x16x32_bf16 %0, a[236:239], v[212:215], %0" : "+v"(o3[1][4]) :  : ); SB();
  asm volatile("buffer_load_dwordx4 v[240:243], %0, %1, %2 offen offset:2048 sc1" :  : "v"(vHr), "s"(rWS), "s"(hso[1]) : "memory"); SB();
  asm volatile("v_mfma_f32_16x16x32_bf16 %0, a[220:223], v[216:219], %0" : "+v"(o3[2][0]) :  : ); SB();
  asm volatile("v_mfma_f32_16x16x32_bf16 %0, a[224:227], v[216:219], %0" : "+v"(o3[2][1]) :  : ); SB();
  asm volatile("v_mfma_f32_16x16x32_bf16 %0, a[228:231], v[216:219], %0" : "+v"(o3[2][2]) :  : ); SB();
  asm volatile("v_mfma_f32_16x16x32_bf16 %0, a[232:235], v[216:219], %0" : "+v"(o3[2][3]) :  : ); SB();
  asm volatile("v_mfma_f32_16x16x32_bf16 %0, a[236:239], v[216:219], %0" : "+v"(o3[2][4]) :  : ); SB();
  asm volatile("v_mfma_f32_16x16x32_bf16 %0, a[220:223], v[220:223], %0" : "+v"(o3[3][0]) :  : ); SB();
  asm volatile("v_mfma_f32_16x16x32_bf16 %0, a[224:227], v[220:223], %0" : "+v"(o3[3][1]) :  : ); SB();
  asm volatile("v_mfma_f32_16x16x32_bf16 %0, a[228:231], v[220:223], %0" : "+v"(o3[3][2]) :  : ); SB();
  asm volatile("v_mfma_f32_16x16x32_bf16 %0, a[232:235], v[220:223], %0" : "+v"(o3[3][3]) :  : ); SB();
  asm volatile("v_mfma_f32_16x16x32_bf16 %0, a[236:239], v[220:223], %0" : "+v"(o3[3][4]) :  : ); SB();
  asm volatile("v_mfma_f32_16x16x32_bf16 %0, a[220:223], v[208:211], %0" : "+v"(o3[0][0]) :  : ); SB();
  asm volatile("v_mfma_f32_16x16x32_bf16 %0, a[224:227], v[208:211], %0" : "+v"(o3[0][1]) :  : ); SB();
  asm volatile("v_mfma_f32_16x16x32_bf16 %0, a[228:231], v[208:211], %0" : "+v"(o3[0][2]) :  : ); SB();
  asm volatile("v_mfma_f32_16x16x32_bf16 %0, a[232:235], v[208:211], %0" : "+v"(o3[0][3]) :  : ); SB();
  asm volatile("v_mfma_f32_16x16x32_bf16 %0, a[236:239], v[208:211], %0" : "+v"(o3[0][4]) :  : ); SB();
  ST(30);
  asm volatile("buffer_load_dwordx4 v[160:163], %0, %1, %2 offen offset:0 sc1" :  : "v"(vKV), "s"(rWS), "s"(0u) : "memory"); SB();
  asm volatile("buffer_load_dwordx4 v[164:167], %0, %1, %2 offen offset:0 sc1" :  : "v"(vKV), "s"(rWS), "s"(w3k[0]) : "memory"); SB();
  asm volatile("buffer_load_dwordx4 v[168:171], %0, %1, %2 offen offset:0 sc1" :  : "v"(vKV), "s"(rWS), "s"(w3k[1]) : "memory"); SB();
  asm volatile("s_waitcnt vmcnt(3)" :  :  : "memory"); SB();
  asm volatile("v_mfma_f32_16x16x32_bf16 %0, a[240:243], v[228:231], %0" : "+v"(o3[1][0]) :  : ); SB();
  asm volatile("v_mfma_f32_16x16x32_bf16 %0, a[244:247], v[228:231], %0" : "+v"(o3[1][1]) :  : ); SB();
  asm volatile("v_mfma_f32_16x16x32_bf16 %0, a[248:251], v[228:231], %0" : "+v"(o3[1][2]) :  : ); SB();
  asm volatile("v_mfma_f32_16x16x32_bf16 %0, a[252:255], v[228:231], %0" : "+v"(o3[1][3]) :  : ); SB();
  asm volatile("v_mfma_f32_16x16x32_bf16 %0, a[0:3], v[228:231], %0" : "+v"(o3[1][4]) :  : ); SB();
  asm volatile("v_mfma_f32_16x16x32_bf16 %0, a[4:7], v[244:247], %0" : "+v"(o3[1][0]) :  : ); SB();
  asm volatile("v_mfma_f32_16x16x32_bf16 %0, a[8:11], v[244:247], %0" : "+v"(o3[1][1]) :  : ); SB();
  asm volatile("v_mfma_f32_16x16x32_bf16 %0, a[12:15], v[244:247], %0" : "+v"(o3[1][2]) :  : ); SB();
  asm volatile("v_mfma_f32_16x16x32_bf16 %0, a[16:19], v[244:247], %0" : "+v"(o3[1][3]) :  : ); SB();
  asm volatile("v_mfma_f32_16x16x32_bf16 %0, a[20:23], v[244:247], %0" : "+v"(o3[1][4]) :  : ); SB();
  asm volatile("v_mfma_f32_16x16x32_bf16 %0, a[240:243], v[232:235], %0" : "+v"(o3[2][0]) :  : ); SB();
  asm volatile("ds_write_b128 %0, %1 offset:0" :  : "v"(pw3), "v"(o3[1][0]) : "memory"); SB();
  asm volatile("v_mfma_f32_16x16x32_bf16 %0, a[244:247], v[232:235], %0" : "+v"(o3[2][1]) :  : ); SB();
  asm volatile("v_mfma_f32_16x16x32_bf16 %0, a[248:251], v[232:235], %0" : "+v"(o3[2][2]) :  : ); SB();
  asm volatile("ds_write_b128 %0, %1 offset:1024" :  : "v"(pw3), "v"(o3[1][1]) : "memory"); SB();
  asm volatile("v_mfma_f32_16x16x32_bf16 %0, a[252:255], v[232:235], %0" : "+v"(o3[2][3]) :  : ); SB();
  asm volatile("v_mfma_f32_16x16x32_bf16 %0, a[0:3], v[232:235], %0" : "+v"(o3[2][4]) :  : ); SB();
  asm volatile("ds_write_b128 %0, %1 offset:2048" :  : "v"(pw3), "v"(o3[1][2]) : "memory"); SB();
  asm volatile("v_mfma_f32_16x16x32_bf16 %0, a[4:7], v[248:251], %0" : "+v"(o3[2][0]) :  : ); SB();
  asm volatile("v_mfma_f32_16x16x32_bf16 %0, a[8:11], v[248:251], %0" : "+v"(o3[2][1]) :  : ); SB();
  asm volatile("ds_write_b128 %0, %1 offset:3072" :  : "v"(pw3), "v"(o3[1][3]) : "memory"); SB();
  asm volatile("v_mfma_f32_16x16x32_bf16 %0, a[12:15], v[248:251], %0" : "+v"(o3[2][2]) :  : ); SB();
  asm volatile("v_mfma_f32_16x16x32_bf16 %0, a[16:19], v[248:251], %0" : "+v"(o3[2][3]) :  : ); SB();
  asm volatile("ds_write_b128 %0, %1 offset:4096" :  : "v"(pw3), "v"(o3[1][4]) : "memory"); SB();
  asm volatile("v_mfma_f32_16x16x32_bf16 %0, a[20:23], v[248:251], %0" : "+v"(o3[2][4]) :  : ); SB();
  asm volatile("v_mfma_f32_16x16x32_bf16 %0, a[240:243], v[236:239], %0" : "+v"(o3[3][0]) :  : ); SB();
  asm volatile("ds_write_b128 %0, %1 offset:5120" :  : "v"(pw3), "v"(o3[2][0]) : "memory"); SB();
  asm volatile("v_mfma_f32_16x16x32_bf16 %0, a[244:247], v[236:239], %0" : "+v"(o3[3][1]) :  : ); SB();
  asm volatile("v_mfma_f32_16x16x32_bf16 %0, a[248:251], v[236:239], %0" : "+v"(o3[3][2]) :  : ); SB();
  asm volatile("ds_write_b128 %0, %1 offset:6144" :  : "v"(pw3), "v"(o3[2][1]) : "memory"); SB();
  asm volatile("v_mfma_f32_16x16x32_bf16 %0, a[252:255], v[236:239], %0" : "+v"(o3[3][3]) :  : ); SB();
  asm volatile("v_mfma_f32_16x16x32_bf16 %0, a[0:3], v[236:239], %0" : "+v"(o3[3][4]) :  : ); SB();
  asm volatile("ds_write_b128 %0, %1 offset:7168" :  : "v"(pw3), "v"(o3[2][2]) : "memory"); SB();
  asm volatile("v_mfma_f32_16x16x32_bf16 %0, a[4:7], v[252:255], %0" : "+v"(o3[3][0]) :  : ); SB();
  asm volatile("v_mfma_f32_16x16x32_bf16 %0, a[8:11], v[252:255], %0" : "+v"(o3[3][1]) :  : ); SB();
  asm volatile("ds_write_b128 %0, %1 offset:8192" :  : "v"(pw3), "v"(o3[2][3]) : "memory"); SB();
  asm volatile("v_mfma_f32_16x16x32_bf16 %0, a[12:15], v[252:255], %0" : "+v"(o3[3][2]) :  : ); SB();
  asm volatile("v_mfma_f32_16x16x32_bf16 %0, a[16:19], v[252:255], %0" : "+v"(o3[3][3]) :  : ); SB();
  asm volatile("ds_write_b128 %0, %1 offset:9216" :  : "v"(pw3), "v"(o3[2][4]) : "memory"); SB();
  asm volatile("v_mfma_f32_16x16x32_bf16 %0, a[20:23], v[252:255], %0" : "+v"(o3[3][4]) :  : ); SB();
  asm volatile("v_mfma_f32_16x16x32_bf16 %0, a[240:243], v[224:227], %0" : "+v"(o3[0][0]) :  : ); SB();
  asm volatile("ds_write_b128 %0, %1 offset:10240" :  : "v"(pw3), "v"(o3[3][0]) : "memory"); SB();
  asm volatile("v_mfma_f32_16x16x32_bf16 %0, a[244:247], v[224:227], %0" : "+v"(o3[0][1]) :  : ); SB();
  asm volatile("v_mfma_f32_16x16x32_bf16 %0, a[248:251], v[224:227], %0" : "+v"(o3[0][2]) :  : ); SB();
  asm volatile("ds_write_b128 %0, %1 offset:11264" :  : "v"(pw3), "v"(o3[3][1]) : "memory"); SB();
  asm volatile("v_mfma_f32_16x16x32_bf16 %0, a[252:255], v[224:227], %0" : "+v"(o3[0][3]) :  : ); SB();
  asm volatile("v_mfma_f32_16x16x32_bf16 %0, a[0:3], v[224:227], %0" : "+v"(o3[0][4]) :  : ); SB();
  asm volatile("ds_write_b128 %0, %1 offset:12288" :  : "v"(pw3), "v"(o3[3][2]) : "memory"); SB();
  asm volatile("v_mfma_f32_16x16x32_bf16 %0, a[4:7], v[240:243], %0" : "+v"(o3[0][0]) :  : ); SB();
  asm volatile("v_mfma_f32_16x16x32_bf16 %0, a[8:11], v[240:243], %0" : "+v"(o3[0][1]) :  : ); SB();
  asm volatile("ds_write_b128 %0, %1 offset:13312" :  : "v"(pw3), "v"(o3[3][3]) : "memory"); SB();
  asm volatile("v_mfma_f32_16x16x32_bf16 %0, a[12:15], v[240:243], %0" : "+v"(o3[0][2]) :  : ); SB();
  asm volatile("v_mfma_f32_16x16x32_bf16 %0, a[16:19], v[240:243], %0" : "+v"(o3[0][3]) :  : ); SB();
  asm volatile("ds_write_b128 %0, %1 offset:14336" :  : "v"(pw3), "v"(o3[3][4]) : "memory"); SB();
  asm volatile("v_mfma_f32_16x16x32_bf16 %0, a[20:23], v[240:243], %0" : "+v"(o3[0][4]) :  : ); SB();
  asm volatile("s_waitcnt lgkmcnt(0)\n\ts_barrier" :  :  : "memory"); SB();
  asm volatile("ds_read_b128 %0, %1 offset:0" : "=&v"(o3[1][0]) : "v"(p3[0]) : "memory"); SB();
  asm volatile("ds_read_b128 %0, %1 offset:5120" : "=&v"(o3[2][0]) : "v"(p3[1]) : "memory"); SB();
  asm volatile("ds_read_b128 %0, %1 offset:10240" : "=&v"(o3[3][0]) : "v"(p3[2]) : "memory"); SB();
  asm volatile("ds_read_b128 %0, %1 offset:1024" : "=&v"(o3[1][1]) : "v"(p3[0]) : "memory"); SB();
  asm volatile("ds_read_b128 %0, %1 offset:6144" : "=&v"(o3[2][1]) : "v"(p3[1]) : "memory"); SB();
  asm volatile("ds_read_b128 %0, %1 offset:11264" : "=&v"(o3[3][1]) : "v"(p3[2]) : "memory"); SB();
  asm volatile("ds_read_b128 %0, %1 offset:2048" : "=&v"(o3[1][2]) : "v"(p3[0]) : "memory"); SB();
  asm volatile("ds_read_b128 %0, %1 offset:7168" : "=&v"(o3[2][2]) : "v"(p3[1]) : "memory"); SB();
  asm volatile("ds_read_b128 %0, %1 offset:12288" : "=&v"(o3[3][2]) : "v"(p3[2]) : "memory"); SB();
  asm volatile("ds_read_b128 %0, %1 offset:3072" : "=&v"(o3[1][3]) : "v"(p3[0]) : "memory"); SB();
  asm volatile("ds_read_b128 %0, %1 offset:8192" : "=&v"(o3[2][3]) : "v"(p3[1]) : "memory"); SB();
  asm volatile("ds_read_b128 %0, %1 offset:13312" : "=&v"(o3[3][3]) : "v"(p3[2]) : "memory"); SB();
  asm volatile("ds_read_b128 %0, %1 offset:4096" : "=&v"(o3[1][4]) : "v"(p3[0]) : "memory"); SB();
  asm volatile("ds_read_b128 %0, %1 offset:9216" : "=&v"(o3[2][4]) : "v"(p3[1]) : "memory"); SB();
  asm volatile("ds_read_b128 %0, %1 offset:14336" : "=&v"(o3[3][4]) : "v"(p3[2]) : "memory"); SB();
  ST(21);
  asm volatile("s_waitcnt lgkmcnt(12)" : "+v"(o3[0][0]), "+v"(o3[1][0]), "+v"(o3[2][0]), "+v"(o3[3][0]) :  : "memory"); SB();
  {
    const f32x4 o4 = (o3[0][0] + o3[1][0]) + (o3[2][0] + o3[3][0]);
#pragma unroll
    for (int h = 0; h < 2; ++h) {
      const f32x2 o = {o4[2 * h], o4[2 * h + 1]};
      const f32x2 a = o * (f32x2){-1.4426950f, -1.4426950f};
      const f32x2 e = (f32x2){__builtin_amdgcn_exp2f(a[0]), __builtin_amdgcn_exp2f(a[1])} + (f32x2){1.0f, 1.0f};
      const f32x2 r = {__builtin_amdgcn_rcpf(e[0]), __builtin_amdgcn_rcpf(e[1])};
      sv[0][h] = (sv[0][h] * o) * r;
      asm volatile("" : "+v"(sv[0][h]));
    }
  }
  SB();
  asm volatile("s_waitcnt lgkmcnt(0)" : "+v"(o3[0][1]), "+v"(o3[1][1]), "+v"(o3[2][1]), "+v"(o3[3][1]) :  : "memory"); SB();
  {
    const f32x4 o4 = (o3[0][1] + o3[1][1]) + (o3[2][1] + o3[3][1]);
#pragma unroll
    for (int h = 0; h < 2; ++h) {
      const f32x2 o = {o4[2 * h], o4[2 * h + 1]};
      const f32x2 a = o * (f32x2){-1.4426950f, -1.4426950f};
      const f32x2 e = (f32x2){__builtin_amdgcn_exp2f(a[0]), __builtin_amdgcn_exp2f(a[1])} + (f32x2){1.0f, 1.0f};
      const f32x2 r = {__builtin_amdgcn_rcpf(e[0]), __builtin_amdgcn_rcpf(e[1])};
      sv[1][h] = (sv[1][h] * o) * r;
      asm volatile("" : "+v"(sv[1][h]));
    }
  }
  SB();
  asm volatile("" : "+v"(o3[0][2]), "+v"(o3[1][2]), "+v"(o3[2][2]), "+v"(o3[3][2]) :  : "memory"); SB();
  {
    const f32x4 o4 = (o3[0][2] + o3[1][2]) + (o3[2][2] + o3[3][2]);
#pragma unroll
    for (int h = 0; h < 2; ++h) {
      const f32x2 o = {o4[2 * h], o4[2 * h + 1]};
      const f32x2 a = o * (f32x2){-1.4426950f, -1.4426950f};
      const f32x2 e = (f32x2){__builtin_amdgcn_exp2f(a[0]), __builtin_amdgcn_exp2f(a[1])} + (f32x2){1.0f, 1.0f};
      const f32x2 r = {__builtin_amdgcn_rcpf(e[0]), __builtin_amdgcn_rcpf(e[1])};
      sv[2][h] = (sv[2][h] * o) * r;
      asm volatile("" : "+v"(sv[2][h]));
    }
  }
  SB();
  asm volatile("" : "+v"(o3[0][3]), "+v"(o3[1][3]), "+v"(o3[2][3]), "+v"(o3[3][3]) :  : "memory"); SB();
  {
    const f32x4 o4 = (o3[0][3] + o3[1][3]) + (o3[2][3] + o3[3][3]);
#pragma unroll
    for (int h = 0; h < 2; ++h) {
      const f32x2 o = {o4[2 * h], o4[2 * h + 1]};
      const f32x2 a = o * (f32x2){-1.4426950f, -1.4426950f};
      const f32x2 e = (f32x2){__builtin_amdgcn_exp2f(a[0]), __builtin_amdgcn_exp2f(a[1])} + (f32x2){1.0f, 1.0f};
      const f32x2 r = {__builtin_amdgcn_rcpf(e[0]), __builtin_amdgcn_rcpf(e[1])};
      sv[3][h] = (sv[3][h] * o) * r;
      asm volatile("" : "+v"(sv[3][h]));
    }
  }
  SB();
  asm volatile("" : "+v"(o3[0][4]), "+v"(o3[1][4]), "+v"(o3[2][4]), "+v"(o3[3][4]) :  : "memory"); SB();
  {
    const f32x4 o4 = (o3[0][4] + o3[1][4]) + (o3[2][4] + o3[3][4]);
#pragma unroll
    for (int h = 0; h < 2; ++h) {
      const f32x2 o = {o4[2 * h], o4[2 * h + 1]};
      const f32x2 a = o * (f32x2){-1.4426950f, -1.4426950f};
      const f32x2 e = (f32x2){__builtin_amdgcn_exp2f(a[0]), __builtin_amdgcn_exp2f(a[1])} + (f32x2){1.0f, 1.0f};
      const f32x2 r = {__builtin_amdgcn_rcpf(e[0]), __builtin_amdgcn_rcpf(e[1])};
      sv[4][h] = (sv[4][h] * o) * r;
      asm volatile("" : "+v"(sv[4][h]));
    }
  }
  SB();
  u32x2 yr[5];
  ST(22);
  asm volatile("s_waitcnt vmcnt(0)\n\t.Lc_%=:\n\tv_xnor_b32 v163, v163, %6\n\tv_xnor_b32 v167, v167, %7\n\tv_xnor_b32 v171, v171, %8\n\tv_max3_u32 %1, v163, v167, v171\n\tv_cmp_eq_u32 vcc, -1, %1\n\ts_cbranch_vccnz .Lr_%=\n\t.Ld_%=:\n\t.subsection 1\n\t.Lr_%=:\n\tv_add_u32 %0, -1, %0\n\ts_nop 1\n\tv_readfirstlane_b32 vcc_hi, %0\n\ts_nop 3\n\ts_cmp_lt_i32 vcc_hi, 1\n\ts_cbranch_scc1 .Ld_%=\n\ts_sleep 2\n\tbuffer_load_dwordx4 v[160:163], %2, %3, 0 offen sc1\n\tbuffer_load_dwordx4 v[164:167], %2, %3, %4 offen sc1\n\tbuffer_load_dwordx4 v[168:171], %2, %3, %5 offen sc1\n\ts_waitcnt vmcnt(0)\n\ts_branch .Lc_%=\n\t.subsection 0" : "+v"(tmo), "=&v"(vtmp) : "v"(vKV), "s"(rWS), "s"(w3k[0]), "s"(w3k[1]), "v"(kz[0]), "v"(kz[1]), "v"(kz[2]) : "memory", "vcc", "scc"); SB();
  ST(31);
  asm volatile("ds_write_b128 %0, v[160:163] offset:0" :  : "v"(yw) : "memory"); SB();
  asm volatile("ds_write_b128 %0, v[164:167] offset:1024" :  : "v"(yw) : "memory"); SB();
  asm volatile("ds_write_b128 %0, v[168:171] offset:2048" :  : "v"(yw) : "memory"); SB();
  asm volatile("s_waitcnt lgkmcnt(0)\n\ts_barrier" :  :  : "memory"); SB();
  asm volatile("ds_read_b64_tr_b16 %0, %1 offset:0" : "=&v"(yr[0]) : "v"(ya) : "memory"); SB();
  asm volatile("ds_read_b64_tr_b16 %0, %1 offset:2048" : "=&v"(yr[1]) : "v"(ya) : "memory"); SB();
  asm volatile("ds_read_b64_tr_b16 %0, %1 offset:4096" : "=&v"(yr[2]) : "v"(ya) : "memory"); SB();
  asm volatile("ds_read_b64_tr_b16 %0, %1 offset:6144" : "=&v"(yr[3]) : "v"(ya) : "memory"); SB();
  asm volatile("ds_read_b64_tr_b16 %0, %1 offset:8192" : "=&v"(yr[4]) : "v"(ya) : "memory"); SB();
  asm volatile("s_waitcnt lgkmcnt(0)" : "+v"(yr[0]), "+v"(yr[1]), "+v"(yr[2]), "+v"(yr[3]), "+v"(yr[4]) :  : "memory"); SB();
  {
    unsigned pk[5][2];
#pragma unroll
    for (int n = 0; n < 10; ++n) pk[n >> 1][n & 1] = pk2(__builtin_elementwise_fma(sv[n >> 1][n & 1], unpk(yr[n >> 1][n & 1]), xf[n >> 1][n & 1]));
#pragma unroll
    for (int q = 0; q < 2; ++q) {
      const auto s0 = __builtin_amdgcn_permlane16_swap(pk[2 * q][0], pk[2 * q + 1][0], false, false);
      const auto s1 = __builtin_amdgcn_permlane16_swap(pk[2 * q][1], pk[2 * q + 1][1], false, false);
      asm volatile("s_nop 1\n\tglobal_store_dwordx4 %0, %1, %2 offset:%3 " :: "v"(po), "v"((u32x4){s0[0], s1[0], s0[1], s1[1]}), "s"(out), "i"(64 * q) : "memory");
    }
    asm volatile("global_store_dwordx2 %0, %1, %2 offset:128 " :: "v"(vX), "v"((u32x2){pk[4][0], pk[4][1]}), "s"(out) : "memory");
  }
  ST(23);
  ST_OUT();
  }}}
}

extern "C" __global__ void k_end() { asm volatile(".subsection 2\n.Lcode_end:\n\ts_endpgm\n\t.subsection 0"); }

extern "C" void kernel_launch(void* const* d_in, const int* in_sizes, int n_in,
                              void* d_out, int out_size, void* d_ws, size_t ws_size,
                              hipStream_t stream) {
  (void)hipFuncSetAttribute((const void*)k_all, hipFuncAttributeMaxDynamicSharedMemorySize, 163840);
  k_all<<<256, 256, 163840, stream>>>((const char*)d_in[0], (const char*)d_in[1], (const char*)d_in[2], (const char*)d_in[3], (const char*)d_in[4],
                                      (const char*)d_in[5], (const char*)d_in[7], (const char*)d_in[8], (const char*)d_in[9], (const char*)d_in[10],
                                      (char*)d_out, (char*)d_ws);
}
```

```cpp
#include <hip/hip_runtime.h>
#include <stdint.h>

typedef unsigned short u16;
typedef __attribute__((ext_vector_type(4))) float f32x4;
typedef __attribute__((ext_vector_type(2))) float f32x2;
typedef __attribute__((ext_vector_type(4))) unsigned int u32x4;
typedef __attribute__((ext_vector_type(2))) unsigned int u32x2;
typedef __attribute__((ext_vector_type(2))) __bf16 bf16x2n;
typedef __attribute__((ext_vector_type(8))) unsigned long long u64x8;
typedef __attribute__((ext_vector_type(4))) unsigned long long u64x4;
#define LDS_AS __attribute__((address_space(3)))
#define GAS __attribute__((address_space(1)))
#define SB() __builtin_amdgcn_sched_barrier(0)
#define DI __device__ __forceinline__

#define SGA
#define ST_DECL do {} while (0)
#define ST(i) do {} while (0)
#define ST_OUT() do {} while (0)

constexpr int S_ = 2048, D_ = 640;
constexpr unsigned HOLE = 0xFFFFFFFFu;
constexpr int W_ST = 4096, W_FZ = 768 << 10, W_FK = W_FZ + 8192, W_FH = W_FK + 8192, W_KV = 1 << 20, W_Z2 = 4 << 20, W_HH = 11 << 20, W_WO = 14 << 20;

#define wm (w >> 1)
#define wn (w & 1)
#define g4 (lane >> 4)
#define l15 (lane & 15)
#define q4 ((lane >> 2) & 3)
#define p4 (lane & 3)
#define l16 (lane * 16)
#define nch (3 - (rb >> 4))
#define ch0 (cb * 80 + rb * 2 + min(rb, 16u))
#define c2o ((nch - 2) * 2)
#define c2s (8192u + ((rb >> 4) << 30))
#define w3s ((w + 1) >> 2 << 30)
#define vZ (W_Z2 + ((rb * 2 + wm) * 64 + cb * 6 + wn * 3) * 1024 + l16)
#define vH (W_HH + ((rb * 4 + w) * 24 + cb * 3) * 1024 + l16)

DI unsigned pack2(float lo, float hi) {
  f32x2 v = {lo, hi};
  return __builtin_bit_cast(unsigned, __builtin_convertvector(v, bf16x2n));
}
DI float lo16(unsigned w) { return __uint_as_float(w << 16); }
DI float hi16(unsigned w) { return __uint_as_float(w & 0xFFFF0000u); }
DI float rbf(float f) { return lo16(pack2(f, f)); }
DI f32x2 unpk(unsigned w) { f32x2 r = {lo16(w), hi16(w)}; return r; }
DI unsigned pk2(f32x2 v) { return pack2(v[0], v[1]); }
DI unsigned nohole(unsigned a) { return a < HOLE - 1 ? a : HOLE - 1; }
template <int CTRL> DI float dppf(float v) {
  return __builtin_bit_cast(float, __builtin_amdgcn_update_dpp(0, __builtin_bit_cast(int, v), CTRL, 0xF, 0xF, true));
}
DI float red16(float v) {
  const auto a = __builtin_amdgcn_permlane16_swap(__float_as_uint(v), __float_as_uint(v), false, false);
  return __uint_as_float(a[0]) + __uint_as_float(a[1]);
}
DI float red32(float v) {
  const auto c = __builtin_amdgcn_permlane32_swap(__float_as_uint(v), __float_as_uint(v), false, false);
  return __uint_as_float(c[0]) + __uint_as_float(c[1]);
}
DI float sel32(float a, float b) {
  const auto c = __builtin_amdgcn_permlane32_swap(__float_as_uint(a), __float_as_uint(b), false, false);
  return __uint_as_float(c[0]) + __uint_as_float(c[1]);
}
DI unsigned lds_addr(const void* p) { return (unsigned)(uintptr_t)(LDS_AS const char*)p; }

__global__ __launch_bounds__(256, 1) __attribute__((amdgpu_num_vgpr(160))) SGA void k_all(
    const char*, const char*, const char*, const char*, const char*, const char*, const char*, const char*, const char*, const char*, char*, char*) {
  extern __shared__ __attribute__((aligned(16))) char smem[];
  ST_DECL;
  ST(0);
  u64x8 ka0; u64x4 ka1;
  asm volatile("s_load_dwordx16 %0, %2, 0x0\n\ts_load_dwordx8 %1, %2, 0x40" : "=&s"(ka0), "=&s"(ka1) : "s"(__builtin_amdgcn_kernarg_segment_ptr()) : "memory"); SB();
  const unsigned tid = threadIdx.x, lane0 = tid & 63, w0 = __builtin_amdgcn_readfirstlane(tid >> 6);
  const unsigned b = blockIdx.x, xcd = b & 7, idx = b >> 3;
  const unsigned rb0 = 8 * (xcd >> 1) + (idx >> 2), cb0 = 4 * (xcd & 1) + (idx & 3);
  const unsigned lb = lds_addr(smem);
  const unsigned lane = lane0, w = w0, rb = rb0, cb = cb0;
  {
    unsigned long long pc; unsigned clen;
    asm volatile("s_getpc_b64 %0\n.Lpc_here_%=:\n\ts_mov_b32 %1, .Lcode_end-.Lpc_here_%=" : "=s"(pc), "=s"(clen));
    const unsigned o = min((((lane & 3) * 4 + w) * 32 + idx) * 128, clen - 4u) & ~3u;
    asm volatile("s_mov_b64 exec, 15\n\tglobal_load_dword v254, %0, %1\n\ts_mov_b64 exec, -1" :: "v"(o), "s"(pc) : "memory");
    SB();
  }
  unsigned voA = (rb * 64 + w * 16 + (lane >> 3)) * 1280 + (((lane & 7) ^ (lane >> 3)) << 4), voB[3];
#pragma unroll
  for (int i = 0; i < 3; ++i) { const unsigned o = i * 64 + lane, r = (o * 171) >> 11, c = o - r * 12; voB[i] = (w * 16 + r) * 3072 + cb * 192 + ((c ^ ((r >> 3) << 1)) << 4) - i * 1024; }
  unsigned mX = lb + w * 2048, mW = lb + w * 3072, mA = lb + w * 8192;
  const unsigned rot = ((rb ^ cb) & 3);
  unsigned kx[10], kw_[10], voA2 = voA + 9216;
  auto kofs = [&](int s) {
    asm volatile("s_xor_b32 %0, %2, %3\n\ts_mul_i32 %1, %0, 0x30000\n\ts_lshl_b32 %0, %0, 7" : "=&s"(kx[s]), "=&s"(kw_[s]) : "s"(s < 8 ? rot : rot & 1), "i"(s) : "scc");
  };
  kofs(0); kofs(1); kofs(2);
  asm volatile("" : "+v"(voA), "+v"(voA2), "+v"(voB[0]), "+v"(voB[1]), "+v"(voB[2]));
  asm volatile("" : "+s"(mX), "+s"(mW), "+s"(mA)); SB();
  asm volatile("s_waitcnt lgkmcnt(0)" : "+s"(ka0), "+s"(ka1) :: "memory"); SB();
  const char *x = (const char*)ka0[0], *kin = (const char*)ka0[1], *vin = (const char*)ka0[2], *shift_k = (const char*)ka0[3], *ssm_k = (const char*)ka0[4];
  const char *ssm_D = (const char*)ka0[5], *w_in = (const char*)ka0[6], *w_sp = (const char*)ka0[7], *b_sp = (const char*)ka1[0], *w_out = (const char*)ka1[1];
  char *out = (char*)ka1[2], *ws = (char*)ka1[3];
  const auto rX = __builtin_amdgcn_make_buffer_rsrc((void*)x, 0, S_ * D_ * 2, 0x00020000);
  const auto rW = __builtin_amdgcn_make_buffer_rsrc((void*)(w_in + 1536), 0, 640 * 3072 - 1536, 0x00020000);
  asm volatile("; named registers" :  :  : "v255", "a255"); SB();
  asm volatile("s_add_u32 m0, %0, 81920" :  : "s"(mX) : "memory", "scc"); SB();
  asm volatile("s_nop 0\n\tbuffer_load_dwordx4 %0, %1, %2 offen lds" :  : "v"(voA), "s"(rX), "s"(kx[0]) : "memory"); SB();
  asm volatile("buffer_load_dwordx4 %0, %1, %2 offen offset:1024 lds" :  : "v"(voA2), "s"(rX), "s"(kx[0]) : "memory"); SB();
  asm volatile("s_add_u32 m0, %0, 0" :  : "s"(mW) : "memory", "scc"); SB();
  asm volatile("s_nop 0\n\tbuffer_load_dwordx4 %0, %1, %2 offen lds" :  : "v"(voB[0]), "s"(rW), "s"(kw_[0]) : "memory"); SB();
  asm volatile("buffer_load_dwordx4 %0, %1, %2 offen offset:1024 lds" :  : "v"(voB[1]), "s"(rW), "s"(kw_[0]) : "memory"); SB();
  asm volatile("buffer_load_dwordx4 %0, %1, %2 offen offset:2048 lds" :  : "v"(voB[2]), "s"(rW), "s"(kw_[0]) : "memory"); SB();
  asm volatile("s_add_u32 m0, %0, 90112" :  : "s"(mX) : "memory", "scc"); SB();
  asm volatile("s_nop 0\n\tbuffer_load_dwordx4 %0, %1, %2 offen lds" :  : "v"(voA), "s"(rX), "s"(kx[1]) : "memory"); SB();
  asm volatile("buffer_load_dwordx4 %0, %1, %2 offen offset:1024 lds" :  : "v"(voA2), "s"(rX), "s"(kx[1]) : "memory"); SB();
  kofs(3); kofs(4); kofs(5); kofs(6); kofs(7); kofs(8); kofs(9);
  const auto rWS = __builtin_amdgcn_make_buffer_rsrc((void*)ws, 0, 32 << 20, 0x00020000);
  const auto rK = __builtin_amdgcn_make_buffer_rsrc((void*)kin, 0, S_ * D_ * 2, 0x00020000);
  const auto rV = __builtin_amdgcn_make_buffer_rsrc((void*)vin, 0, S_ * D_ * 2, 0x00020000);
  const auto rS = __builtin_amdgcn_make_buffer_rsrc((void*)(shift_k - 3328), 0, D_ * 8 + 3328, 0x00020000);
  const auto rO = __builtin_amdgcn_make_buffer_rsrc((void*)(w_out - 3072), 0, 768 * 1280 + 3072, 0x00020000);
  const auto rW1 = __builtin_amdgcn_make_buffer_rsrc((void*)w_in, 0, 640 * 3072, 0x00020000);
  { unsigned lane = lane0, w = w0, rb = rb0, cb = cb0;
  asm volatile("" : "+v"(lane)); asm volatile("" : "+s"(w), "+s"(rb), "+s"(cb)); SB();

  const auto rA = __builtin_amdgcn_make_buffer_rsrc((void*)(w_sp - 3072), 0, S_ * S_ * 2 + 3072, 0x00020000);
  unsigned ao[8];
  unsigned voK[4], voV[3], voH = cb * 640 + l16;
#pragma unroll
  for (int n = 0; n < 4; ++n) { const unsigned o = n * 64 + lane, r = (o * 205) >> 11; voK[n] = (rb * 64 + w * 16 - 4) * 1280 + cb * 160 + r * 1120 + lane * 16; }
#pragma unroll
  for (int n = 0; n < 3; ++n) voV[n] = voK[n] + 5120;
  const unsigned wof = (rb >> 3) * 30 + min((rb & 7) * 4 + w, 29u), wok = (wof * 205) >> 10;
  unsigned voO = (lane >> 1) * 1280 + (lane & 1) * 16 + wok * 40960 + (wof - 5 * wok) * 32 + cb * 160;
  unsigned ka = lb + 98304 + w * 8192 + (4 * p4 + q4) * 160 + 8 * g4, oa = lb + 98304 + 7168 + w * 8192 + (4 * g4 + q4) * 32 + 8 * p4;
  unsigned sa = lb + 98304 + 3328 + w * 8192 + (4 * g4 + p4) * 8;
  unsigned kw = lb + 12288 + (4 * g4 + p4) * 144 + w * 32 + q4 * 8, kr = lb + 12288 + (w * 24 + (lane >> 3)) * 144 + (lane & 7) * 16;
  unsigned vKV = W_KV + (cb * 80 + w * 24 + (lane >> 3)) * 4096 + rb * 128 + (lane & 7) * 16, vWO = W_WO + (cb * 120 + wof) * 1024 + l16;
  asm volatile("" : "+v"(ka), "+v"(oa), "+v"(sa), "+v"(kw), "+v"(kr), "+v"(vKV), "+v"(vWO), "+v"(voK[0]), "+v"(voK[1]), "+v"(voK[2]), "+v"(voK[3]),
               "+v"(voV[0]), "+v"(voV[1]), "+v"(voV[2]), "+v"(voH), "+v"(voO));

  asm volatile("s_add_u32 m0, %0, 12288" :  : "s"(mW) : "memory", "scc"); SB();
  asm volatile("s_nop 0\n\tbuffer_load_dwordx4 %0, %1, %2 offen lds" :  : "v"(voB[0]), "s"(rW), "s"(kw_[1]) : "memory"); SB();
  asm volatile("buffer_load_dwordx4 %0, %1, %2 offen offset:1024 lds" :  : "v"(voB[1]), "s"(rW), "s"(kw_[1]) : "memory"); SB();
  asm volatile("buffer_load_dwordx4 %0, %1, %2 offen offset:2048 lds" :  : "v"(voB[2]), "s"(rW), "s"(kw_[1]) : "memory"); SB();
  u32x2 kp[5], kc[5], vv[5], sw[5], wt[2], ko[5];
  u32x4 kq[3];
  f32x2 kt[2], kf[2];
  unsigned ku[5];
  unsigned aX[2][2], aW[2][2][3];
#pragma unroll
  for (int n = 0; n < 4; ++n) aX[n >> 1][n & 1] = lb + 98304 + (wm * 32 + ((n & 1) ^ wn) * 16 + l15) * 128 + (((((n >> 1) ^ wm) * 4 + g4) ^ (l15 & 7)) << 4);
#pragma unroll
  for (int n = 0; n < 6; ++n) {
    aW[0][n / 3][n % 3] = lb + ((n / 3 ^ wm) * 32 + 8 * g4 + q4) * 192 + (((wn * 3 + n % 3) ^ (g4 & 1)) << 5) + 8 * p4;
    aW[1][n / 3][n % 3] = aW[0][n / 3][n % 3] + 49152;
    asm volatile("" : "+v"(aW[0][n / 3][n % 3]), "+v"(aW[1][n / 3][n % 3]));
  }
  unsigned ca = lb + 98304 + 6912 + w * 8192 + g4 * 16, cr = lb + 98304 + 6912 + (w ^ 2) * 8192 + g4 * 16;
  f32x4 co[3];
  asm volatile("" : "+v"(ca), "+v"(cr));

  SB();
  u32x4 ones = {0x3F803F80u, 0x3F803F80u, 0x3F803F80u, 0x3F803F80u};
  f32x4 a2[2][3], a1[2][3], rs, gr, c2[3], c1[3];
  unsigned voS = (rb * 64 + (lane >> 3)) * 4096 + w * 1024 + (cb & 1) * 128 + (((lane & 7) ^ (lane >> 3)) << 4), aA[2];
#pragma unroll
  for (int k = 0; k < 2; ++k) aA[k] = lb + 98304 + w * 8192 + l15 * 128 + (((k * 4 + g4) ^ (l15 & 7)) << 4);
  unsigned c2s_ = c2s, xw = lb + 98304 + 6656 + w * 8192 + l15 * 16, xr_ = lb + 98304 + 6656 + (w ^ 1) * 8192 + l15 * 16;
  u32x2 zt[3][2];
  unsigned zwa[2] = {lb + 98304 + 4096 + w * 8192 + (wn * 16 + l15) * 104 + g4 * 8, lb + 98304 + 4096 + w * 8192 + ((wn ^ 1) * 16 + l15) * 104 + g4 * 8};
  unsigned zra = lb + 98304 + 4096 + w * 8192 + (8 * g4 + q4) * 104 + 8 * p4;
  asm volatile("" : "+v"(zwa[0]), "+v"(zwa[1]), "+v"(zra));
  unsigned vo = W_ST + ((rb * 2 + wm) * 8 + cb) * 1024 + wn * 128 + ((lane >> 5) ^ wn) * 512 + (l15 >> 3) * 256 + (l15 & 7) * 16;
  asm volatile("" : "+v"(ones),
               "+v"(voS), "+v"(aA[0]), "+v"(aA[1]), "+s"(c2s_), "+v"(xw), "+v"(xr_), "+v"(vo));
  asm volatile("" : "+v"(aX[0][0]), "+v"(aX[0][1]), "+v"(aX[1][0]), "+v"(aX[1][1]));

  SB();
  int tmo = (1 << 18);
  u32x2 fk, fz, fh, fi, fy;
  unsigned tg0, tg1;
  float xm[2], xd[2], k1[2], k2[2];
  f32x4 xs[2];
  float xt[4];
  const auto rF = __builtin_amdgcn_make_buffer_rsrc((void*)ssm_k, 0, S_ * D_ * 2, 0x00020000);
  unsigned vF = ch0 * 4096 + w * 1024 + l16;
  asm volatile("" : "+v"(vF));
  unsigned hpo[3], vD = ch0 * 2, vD2 = vD + c2o * 2 - 4, vBs = (rb * 64 + w * 16 + l15) * 2;
  hpo[0] = w ? ch0 * 4096 + w * 1024 - 4 : 0x40000000u; hpo[1] = hpo[0] + 4096; hpo[2] = hpo[0] + c2s;
  asm volatile("" : "+s"(hpo[0]), "+s"(hpo[1]), "+s"(hpo[2]), "+v"(vD), "+v"(vD2), "+v"(vBs));
  unsigned vS = W_ST + (rb * 2 + wm) * 8192 + l16, vFZ = W_FZ + cb * 1024 + w * 256 + (lane & 31) * 8, vZ_ = vZ;
  unsigned vB = W_Z2 + (w * 1024 + (cb & 1) * 128 + cb * 6) * 1024 + l16;
  asm volatile("" : "+v"(vS), "+v"(vFZ), "+v"(vZ_), "+v"(vB));
  SB();
  unsigned ha = lb + 59520 + 4608 + (2040 - 8 * (w * 64 + lane)) * 2;
  unsigned za = lb + 59520 + min(w, 2u) * 12928 + (lane < 32 ? lane * 16 : 4608 + ((lane >> 1) & 1) * 4160 + 4096 + (lane & 1) * 16);
  unsigned ew[2] = {lb + 32768 + w * 6144 + wn * 3072 + l16, lb + 32768 + (w ^ 1) * 6144 + wn * 3072 + l16};
  unsigned aa[4], bb;
  {
    const unsigned c = (l15 + 1) & 1;
    const unsigned o = lb + 59520 + 2 * 12928 - w * 128 - 192;
#pragma unroll
    for (int k = 0; k < 4; ++k) aa[k] = o + 4608 + c * 4160 + (2031 - l15 + 8 * g4 - c) * 2 - 512 - 1024 * k;
    bb = o + 512 + 32 * l15 - 32 + 16 * g4;
  }
  unsigned dd = lb + 0 + w * 8192 + g4 * 256 + ((l15 ^ ((g4 >> 1) << 3)) << 4);
  asm volatile("" : "+v"(aa[0]), "+v"(aa[1]), "+v"(aa[2]), "+v"(aa[3]), "+v"(bb), "+v"(dd), "+v"(vS), "+v"(vFZ), "+v"(vZ_), "+v"(vB), "+v"(ha), "+v"(za), "+v"(ew[0]), "+v"(ew[1]));
  unsigned mU, uso, w3k[2], sFK, sFZ;
  float dD[3], bs;
  u32x4 im[6], zero4;
  ST(1);
  SB();
  asm volatile("s_add_u32 m0, %0, 98304" :  : "s"(mX) : "memory", "scc"); SB();
  asm volatile("s_nop 0\n\tbuffer_load_dwordx4 %0, %1, %2 offen lds" :  : "v"(voA), "s"(rX), "s"(kx[2]) : "memory"); SB();
  asm volatile("buffer_load_dwordx4 %0, %1, %2 offen offset:1024 lds" :  : "v"(voA2), "s"(rX), "s"(kx[2]) : "memory"); SB();
  asm volatile("buffer_load_dwordx2 v[252:253], off, %0, 0 sc1" :  : "s"(rWS) : "memory"); SB();
  asm volatile("s_add_u32 m0, %0, 106496" :  : "s"(mX) : "memory", "scc"); SB();
  asm volatile("s_nop 0\n\tbuffer_load_dwordx4 %0, %1, %2 offen lds" :  : "v"(voA), "s"(rX), "s"(kx[3]) : "memory"); SB();
  asm volatile("buffer_load_dwordx4 %0, %1, %2 offen offset:1024 lds" :  : "v"(voA2), "s"(rX), "s"(kx[3]) : "memory"); SB();
  asm volatile("s_add_u32 m0, %0, 24576" :  : "s"(mW) : "memory", "scc"); SB();
  asm volatile("s_nop 0\n\tbuffer_load_dwordx4 %0, %1, %2 offen lds" :  : "v"(voB[0]), "s"(rW), "s"(kw_[2]) : "memory"); SB();
  asm volatile("s_waitcnt vmcnt(11) lgkmcnt(0)\n\ts_barrier" :  :  : "memory"); SB();
  ST(4);
  asm volatile("ds_read_b128 a[0:3], %0 offset:0" :  : "v"((aX[0][0] - 16384)) : "memory"); SB();
  asm volatile("ds_read_b128 a[4:7], %0 offset:0" :  : "v"((aX[0][1] - 16384)) : "memory"); SB();
  asm volatile("ds_read_b64_tr_b16 v[160:161], %0 offset:0" :  : "v"(aW[0][0][0]) : "memory"); SB();
  asm volatile("ds_read_b64_tr_b16 v[162:163], %0 offset:768" :  : "v"(aW[0][0][0]) : "memory"); SB();
  asm volatile("ds_read_b64_tr_b16 v[164:165], %0 offset:0" :  : "v"(aW[0][0][1]) : "memory"); SB();
  asm volatile("ds_read_b64_tr_b16 v[166:167], %0 offset:768" :  : "v"(aW[0][0][1]) : "memory"); SB();
  asm volatile("ds_read_b64_tr_b16 v[168:169], %0 offset:0" :  : "v"(aW[0][0][2]) : "memory"); SB();
  asm volatile("ds_read_b64_tr_b16 v[170:171], %0 offset:768" :  : "v"(aW[0][0][2]) : "memory"); SB();
  asm volatile("ds_read_b128 a[8:11], %0 offset:0" :  : "v"((aX[1][0] - 16384)) : "memory"); SB();
  asm volatile("ds_read_b128 a[12:15], %0 offset:0" :  : "v"((aX[1][1] - 16384)) : "memory"); SB();
  asm volatile("s_waitcnt lgkmcnt(6)\n\tv_mfma_f32_16x16x32_bf16 %0, v[160:163], a[0:3], 0" : "=&v"(a2[0][0]) :  : ); SB();
  asm volatile("ds_read_b64_tr_b16 v[172:173], %0 offset:0" :  : "v"(aW[0][1][0]) : "memory"); SB();
  asm volatile("buffer_load_dword v242, off, %0, %1" :  : "s"(rF), "s"(hpo[0]) : "memory"); SB();
  asm volatile("v_mfma_f32_16x16x32_bf16 %0, v[160:163], a[4:7], 0" : "=&v"(a2[1][0]) :  : ); SB();
  asm volatile("ds_read_b64_tr_b16 v[174:175], %0 offset:768" :  : "v"(aW[0][1][0]) : "memory"); SB();
  asm volatile("buffer_load_dwordx4 %0, %1, %2 offen offset:1024 lds" :  : "v"(voB[1]), "s"(rW), "s"(kw_[2]) : "memory"); SB();
  asm volatile("v_mfma_f32_16x16x32_bf16 %0, v[160:163], %1, 0" : "=&v"(c2[0]) : "v"(ones) : ); SB();
  asm volatile("ds_read_b64_tr_b16 v[176:177], %0 offset:0" :  : "v"(aW[0][1][1]) : "memory"); SB();
  asm volatile("s_waitcnt lgkmcnt(5)\n\tv_mfma_f32_16x16x32_bf16 %0, v[164:167], a[0:3], 0" : "=&v"(a2[0][1]) :  : ); SB();
  asm volatile("ds_read_b64_tr_b16 v[178:179], %0 offset:768" :  : "v"(aW[0][1][1]) : "memory"); SB();
  asm volatile("v_mfma_f32_16x16x32_bf16 %0, v[164:167], a[4:7], 0" : "=&v"(a2[1][1]) :  : ); SB();
  asm volatile("ds_read_b64_tr_b16 v[180:181], %0 offset:0" :  : "v"(aW[0][1][2]) : "memory"); SB();
  asm volatile("buffer_load_dwordx4 %0, %1, %2 offen offset:2048 lds" :  : "v"(voB[2]), "s"(rW), "s"(kw_[2]) : "memory"); SB();
  asm volatile("v_mfma_f32_16x16x32_bf16 %0, v[164:167], %1, 0" : "=&v"(c2[1]) : "v"(ones) : ); SB();
  asm volatile("ds_read_b64_tr_b16 v[182:183], %0 offset:768" :  : "v"(aW[0][1][2]) : "memory"); SB();
  asm volatile("v_mfma_f32_16x16x32_bf16 %0, v[168:171], a[0:3], 0" : "=&v"(a2[0][2]) :  : ); SB();
  asm volatile("s_waitcnt vmcnt(9)\n\ts_barrier" :  :  : "memory"); SB();
  asm volatile("ds_read_b64_tr_b16 v[184:185], %0 offset:12288" :  : "v"(aW[0][0][0]) : "memory"); SB();
  asm volatile("global_load_ushort v245, %0, %1 offset:0" :  : "v"(vD), "s"(ssm_D) : "memory"); SB();
  asm volatile("v_mfma_f32_16x16x32_bf16 %0, v[168:171], a[4:7], 0" : "=&v"(a2[1][2]) :  : ); SB();
  asm volatile("ds_read_b64_tr_b16 v[186:187], %0 offset:13056" :  : "v"(aW[0][0][0]) : "memory"); SB();
  asm volatile("s_add_u32 m0, %0, 114688" :  : "s"(mX) : "memory", "scc"); SB();
  asm volatile("v_mfma_f32_16x16x32_bf16 %0, v[168:171], %1, 0" : "=&v"(c2[2]) : "v"(ones) : ); SB();
  asm volatile("ds_read_b64_tr_b16 v[188:189], %0 offset:12288" :  : "v"(aW[0][0][1]) : "memory"); SB();
  asm volatile("buffer_load_dwordx4 %0, %1, %2 offen lds" :  : "v"(voA), "s"(rX), "s"(kx[4]) : "memory"); SB();
  asm volatile("v_mfma_f32_16x16x32_bf16 %0, %1, a[0:3], 0" : "=&v"(rs) : "v"(ones) : ); SB();
  asm volatile("ds_read_b128 a[16:19], %0 offset:8192" :  : "v"((aX[0][0] - 16384)) : "memory"); SB();
  asm volatile("ds_read_b128 a[20:23], %0 offset:8192" :  : "v"((aX[0][1] - 16384)) : "memory"); SB();
  asm volatile("ds_read_b64_tr_b16 v[190:191], %0 offset:13056" :  : "v"(aW[0][0][1]) : "memory"); SB();
  asm volatile("buffer_load_dwordx4 %0, %1, %2 offen offset:1024 lds" :  : "v"(voA2), "s"(rX), "s"(kx[4]) : "memory"); SB();
  asm volatile("v_mfma_f32_16x16x32_bf16 %0, a[0:3], a[0:3], 0" : "=&v"(gr) :  : ); SB();
  asm volatile("ds_read_b64_tr_b16 v[192:193], %0 offset:12288" :  : "v"(aW[0][0][2]) : "memory"); SB();
  asm volatile("s_waitcnt lgkmcnt(7)\n\tv_mfma_f32_16x16x32_bf16 %0, v[172:175], a[8:11], %0" : "+v"(a2[0][0]) :  : ); SB();
  asm volatile("ds_read_b64_tr_b16 v[194:195], %0 offset:13056" :  : "v"(aW[0][0][2]) : "memory"); SB();
  asm volatile("v_mfma_f32_16x16x32_bf16 %0, v[172:175], a[12:15], %0" : "+v"(a2[1][0]) :  : ); SB();
  asm volatile("ds_read_b128 a[24:27], %0 offset:8192" :  : "v"((aX[1][0] - 16384)) : "memory"); SB();
  asm volatile("s_add_u32 m0, %0, 122880" :  : "s"(mX) : "memory", "scc"); SB();
  asm volatile("v_mfma_f32_16x16x32_bf16 %0, v[176:179], a[8:11], %0" : "+v"(a2[0][1]) :  : ); SB();
  asm volatile("ds_read_b128 a[28:31], %0 offset:8192" :  : "v"((aX[1][1] - 16384)) : "memory"); SB();
  asm volatile("v_mfma_f32_16x16x32_bf16 %0, v[176:179], a[12:15], %0" : "+v"(a2[1][1]) :  : ); SB();
  asm volatile("buffer_load_dwordx4 %0, %1, %2 offen lds" :  : "v"(voA), "s"(rX), "s"(kx[5]) : "memory"); SB();
  asm volatile("v_mfma_f32_16x16x32_bf16 %0, v[180:183], a[8:11], %0" : "+v"(a2[0][2]) :  : ); SB();
  asm volatile("ds_read_b64_tr_b16 v[160:161], %0 offset:12288" :  : "v"(aW[0][1][0]) : "memory"); SB();
  asm volatile("v_mfma_f32_16x16x32_bf16 %0, v[180:183], a[12:15], %0" : "+v"(a2[1][2]) :  : ); SB();
  asm volatile("ds_read_b64_tr_b16 v[162:163], %0 offset:13056" :  : "v"(aW[0][1][0]) : "memory"); SB();
  asm volatile("v_mfma_f32_16x16x32_bf16 %0, %1, a[8:11], %0" : "+v"(rs) : "v"(ones) : ); SB();
  asm volatile("ds_read_b64_tr_b16 v[164:165], %0 offset:12288" :  : "v"(aW[0][1][1]) : "memory"); SB();
  asm volatile("buffer_load_dword v243, off, %0, %1" :  : "s"(rF), "s"(hpo[1]) : "memory"); SB();
  asm volatile("v_mfma_f32_16x16x32_bf16 %0, a[8:11], a[8:11], %0" : "+v"(gr) :  : ); SB();
  asm volatile("ds_read_b64_tr_b16 v[166:167], %0 offset:13056" :  : "v"(aW[0][1][1]) : "memory"); SB();
  asm volatile("s_waitcnt lgkmcnt(8)\n\tv_mfma_f32_16x16x32_bf16 %0, v[184:187], a[16:19], %0" : "+v"(a2[0][0]) :  : ); SB();
  asm volatile("ds_read_b64_tr_b16 v[168:169], %0 offset:12288" :  : "v"(aW[0][1][2]) : "memory"); SB();
  asm volatile("buffer_load_dwordx4 %0, %1, %2 offen offset:1024 lds" :  : "v"(voA2), "s"(rX), "s"(kx[5]) : "memory"); SB();
  asm volatile("v_mfma_f32_16x16x32_bf16 %0, v[184:187], a[20:23], %0" : "+v"(a2[1][0]) :  : ); SB();
  asm volatile("ds_read_b64_tr_b16 v[170:171], %0 offset:13056" :  : "v"(aW[0][1][2]) : "memory"); SB();
  asm volatile("v_mfma_f32_16x16x32_bf16 %0, v[184:187], %1, %0" : "+v"(c2[0]) : "v"(ones) : ); SB();
  asm volatile("v_mfma_f32_16x16x32_bf16 %0, v[188:191], a[16:19], %0" : "+v"(a2[0][1]) :  : ); SB();
  asm volatile("v_mfma_f32_16x16x32_bf16 %0, v[188:191], a[20:23], %0" : "+v"(a2[1][1]) :  : ); SB();
  asm volatile("s_add_u32 m0, %0, 36864" :  : "s"(mW) : "memory", "scc"); SB();
  asm volatile("s_nop 0\n\tbuffer_load_dwordx4 %0, %1, %2 offen lds" :  : "v"(voB[0]), "s"(rW), "s"(kw_[3]) : "memory"); SB();
  asm volatile("buffer_load_dwordx4 %0, %1, %2 offen offset:1024 lds" :  : "v"(voB[1]), "s"(rW), "s"(kw_[3]) : "memory"); SB();
  asm volatile("buffer_load_dwordx4 %0, %1, %2 offen offset:2048 lds" :  : "v"(voB[2]), "s"(rW), "s"(kw_[3]) : "memory"); SB();
  asm volatile("global_load_ushort v246, %0, %1 offset:2" :  : "v"(vD), "s"(ssm_D) : "memory"); SB();
  asm volatile("buffer_load_dword v244, off, %0, %1" :  : "s"(rF), "s"(hpo[2]) : "memory"); SB();
  asm volatile("s_add_u32 m0, %0, 131072" :  : "s"(mX) : "memory", "scc"); SB();
  asm volatile("s_nop 0\n\tbuffer_load_dwordx4 %0, %1, %2 offen lds" :  : "v"(voA), "s"(rX), "s"(kx[6]) : "memory"); SB();
  asm volatile("buffer_load_dwordx4 %0, %1, %2 offen offset:1024 lds" :  : "v"(voA2), "s"(rX), "s"(kx[6]) : "memory"); SB();
  asm volatile("s_add_u32 m0, %0, 139264" :  : "s"(mX) : "memory", "scc"); SB();
  asm volatile("s_nop 0\n\tbuffer_load_dwordx4 %0, %1, %2 offen lds" :  : "v"(voA), "s"(rX), "s"(kx[7]) : "memory"); SB();
  asm volatile("global_load_ushort v247, %0, %1 offset:4" :  : "v"(vD2), "s"(ssm_D) : "memory"); SB();
  asm volatile("global_load_ushort v248, %0, %1" :  : "v"(vBs), "s"(b_sp) : "memory"); SB();
  asm volatile("s_waitcnt vmcnt(16)\n\ts_barrier" :  :  : "memory"); SB();
  asm volatile("ds_read_b64_tr_b16 v[172:173], %0 offset:24576" :  : "v"(aW[0][0][0]) : "memory"); SB();
  asm volatile("v_mfma_f32_16x16x32_bf16 %0, v[188:191], %1, %0" : "+v"(c2[1]) : "v"(ones) : ); SB();
  asm volatile("ds_read_b64_tr_b16 v[174:175], %0 offset:25344" :  : "v"(aW[0][0][0]) : "memory"); SB();
  asm volatile("s_waitcnt lgkmcnt(2)\n\tv_mfma_f32_16x16x32_bf16 %0, v[192:195], a[16:19], %0" : "+v"(a2[0][2]) :  : ); SB();
  asm volatile("ds_read_b64_tr_b16 v[176:177], %0 offset:24576" :  : "v"(aW[0][0][1]) : "memory"); SB();
  asm volatile("v_mfma_f32_16x16x32_bf16 %0, v[192:195], a[20:23], %0" : "+v"(a2[1][2]) :  : ); SB();
  asm volatile("ds_read_b64_tr_b16 v[178:179], %0 offset:25344" :  : "v"(aW[0][0][1]) : "memory"); SB();
  asm volatile("v_mfma_f32_16x16x32_bf16 %0, v[192:195], %1, %0" : "+v"(c2[2]) : "v"(ones) : ); SB();
  asm volatile("ds_read_b64_tr_b16 v[180:181], %0 offset:24576" :  : "v"(aW[0][0][2]) : "memory"); SB();
  asm volatile("v_mfma_f32_16x16x32_bf16 %0, %1, a[16:19], %0" : "+v"(rs) : "v"(ones) : ); SB();
  asm volatile("ds_read_b128 a[32:35], %0 offset:0" :  : "v"(aX[0][0]) : "memory"); SB();
  asm volatile("ds_read_b128 a[36:39], %0 offset:0" :  : "v"(aX[0][1]) : "memory"); SB();
  asm volatile("ds_read_b64_tr_b16 v[182:183], %0 offset:25344" :  : "v"(aW[0][0][2]) : "memory"); SB();
  asm volatile("v_mfma_f32_16x16x32_bf16 %0, a[16:19], a[16:19], %0" : "+v"(gr) :  : ); SB();
  asm volatile("ds_read_b128 a[40:43], %0 offset:0" :  : "v"(aX[1][0]) : "memory"); SB();
  asm volatile("v_mfma_f32_16x16x32_bf16 %0, v[160:163], a[24:27], %0" : "+v"(a2[0][0]) :  : ); SB();
  asm volatile("ds_read_b128 a[44:47], %0 offset:0" :  : "v"(aX[1][1]) : "memory"); SB();
  asm volatile("v_mfma_f32_16x16x32_bf16 %0, v[160:163], a[28:31], %0" : "+v"(a2[1][0]) :  : ); SB();
  asm volatile("buffer_load_dwordx4 %0, %1, %2 offen offset:1024 lds" :  : "v"(voA2), "s"(rX), "s"(kx[7]) : "memory"); SB();
  asm volatile("s_add_u32 m0, %0, 49152" :  : "s"(mW) : "memory", "scc"); SB();
  asm volatile("v_mfma_f32_16x16x32_bf16 %0, v[164:167], a[24:27], %0" : "+v"(a2[0][1]) :  : ); SB();
  asm volatile("ds_read_b128 a[48:51], %0 offset:8192" :  : "v"(aX[0][0]) : "memory"); SB();
  asm volatile("v_mfma_f32_16x16x32_bf16 %0, v[164:167], a[28:31], %0" : "+v"(a2[1][1]) :  : ); SB();
  asm volatile("ds_read_b128 a[52:55], %0 offset:8192" :  : "v"(aX[0][1]) : "memory"); SB();
  asm volatile("v_mfma_f32_16x16x32_bf16 %0, v[168:171], a[24:27], %0" : "+v"(a2[0][2]) :  : ); SB();
  asm volatile("buffer_load_dwordx4 %0, %1, %2 offen lds" :  : "v"(voB[0]), "s"(rW), "s"(kw_[4]) : "memory"); SB();
  asm volatile("v_mfma_f32_16x16x32_bf16 %0, v[168:171], a[28:31], %0" : "+v"(a2[1][2]) :  : ); SB();
  asm volatile("ds_read_b128 a[56:59], %0 offset:8192" :  : "v"(aX[1][0]) : "memory"); SB();
  asm volatile("v_mfma_f32_16x16x32_bf16 %0, %1, a[24:27], %0" : "+v"(rs) : "v"(ones) : ); SB();
  asm volatile("ds_read_b64_tr_b16 v[184:185], %0 offset:24576" :  : "v"(aW[0][1][0]) : "memory"); SB();
  asm volatile("v_mfma_f32_16x16x32_bf16 %0, a[24:27], a[24:27], %0" : "+v"(gr) :  : ); SB();
  asm volatile("ds_read_b64_tr_b16 v[186:187], %0 offset:25344" :  : "v"(aW[0][1][0]) : "memory"); SB();
  asm volatile("s_waitcnt lgkmcnt(8)\n\tv_mfma_f32_16x16x32_bf16 %0, v[172:175], a[32:35], %0" : "+v"(a2[0][0]) :  : ); SB();
  asm volatile("ds_read_b64_tr_b16 v[188:189], %0 offset:24576" :  : "v"(aW[0][1][1]) : "memory"); SB();
  asm volatile("v_mfma_f32_16x16x32_bf16 %0, v[172:175], a[36:39], %0" : "+v"(a2[1][0]) :  : ); SB();
  asm volatile("ds_read_b64_tr_b16 v[190:191], %0 offset:25344" :  : "v"(aW[0][1][1]) : "memory"); SB();
  asm volatile("buffer_load_dwordx4 %0, %1, %2 offen offset:1024 lds" :  : "v"(voB[1]), "s"(rW), "s"(kw_[4]) : "memory"); SB();
  asm volatile("v_mfma_f32_16x16x32_bf16 %0, v[172:175], %1, %0" : "+v"(c2[0]) : "v"(ones) : ); SB();
  asm volatile("ds_read_b64_tr_b16 v[192:193], %0 offset:24576" :  : "v"(aW[0][1][2]) : "memory"); SB();
  asm volatile("v_mfma_f32_16x16x32_bf16 %0, v[176:179], a[32:35], %0" : "+v"(a2[0][1]) :  : ); SB();
  asm volatile("ds_read_b64_tr_b16 v[194:195], %0 offset:25344" :  : "v"(aW[0][1][2]) : "memory"); SB();
  asm volatile("v_mfma_f32_16x16x32_bf16 %0, v[176:179], a[36:39], %0" : "+v"(a2[1][1]) :  : ); SB();
  asm volatile("ds_read_b128 a[60:63], %0 offset:8192" :  : "v"(aX[1][1]) : "memory"); SB();
  asm volatile("v_mfma_f32_16x16x32_bf16 %0, v[176:179], %1, %0" : "+v"(c2[1]) : "v"(ones) : ); SB();
  asm volatile("buffer_load_dwordx4 %0, %1, %2 offen offset:2048 lds" :  : "v"(voB[2]), "s"(rW), "s"(kw_[4]) : "memory"); SB();
  asm volatile("s_waitcnt lgkmcnt(8)\n\tv_mfma_f32_16x16x32_bf16 %0, v[180:183], a[32:35], %0" : "+v"(a2[0][2]) :  : ); SB();
  asm volatile("s_add_u32 m0, %0, 147456" :  : "s"(mX) : "memory", "scc"); SB();
  asm volatile("v_mfma_f32_16x16x32_bf16 %0, v[180:183], a[36:39], %0" : "+v"(a2[1][2]) :  : ); SB();
  asm volatile("buffer_load_dwordx4 %0, %1, %2 offen lds" :  : "v"(voA), "s"(rX), "s"(kx[8]) : "memory"); SB();
  asm volatile("v_mfma_f32_16x16x32_bf16 %0, v[180:183], %1, %0" : "+v"(c2[2]) : "v"(ones) : ); SB();
  asm volatile("buffer_load_dwordx4 %0, %1, %2 offen offset:1024 lds" :  : "v"(voA2), "s"(rX), "s"(kx[8]) : "memory"); SB();
  asm volatile("s_add_u32 m0, %0, 155648" :  : "s"(mX) : "memory", "scc"); SB();
  asm volatile("s_nop 0\n\tbuffer_load_dwordx4 %0, %1, %2 offen lds" :  : "v"(voA), "s"(rX), "s"(kx[9]) : "memory"); SB();
  asm volatile("s_waitcnt vmcnt(14)\n\ts_barrier" :  :  : "memory"); SB();
  asm volatile("ds_read_b64_tr_b16 v[160:161], %0 offset:36864" :  : "v"(aW[0][0][0]) : "memory"); SB();
  asm volatile("v_mfma_f32_16x16x32_bf16 %0, %1, a[32:35], %0" : "+v"(rs) : "v"(ones) : ); SB();
  asm volatile("ds_read_b64_tr_b16 v[162:163], %0 offset:37632" :  : "v"(aW[0][0][0]) : "memory"); SB();
  asm volatile("v_mfma_f32_16x16x32_bf16 %0, a[32:35], a[32:35], %0" : "+v"(gr) :  : ); SB();
  asm volatile("ds_read_b64_tr_b16 v[164:165], %0 offset:36864" :  : "v"(aW[0][0][1]) : "memory"); SB();
  asm volatile("v_mfma_f32_16x16x32_bf16 %0, %1, a[40:43], %0" : "+v"(rs) : "v"(ones) : ); SB();
  asm volatile("ds_read_b64_tr_b16 v[166:167], %0 offset:37632" :  : "v"(aW[0][0][1]) : "memory"); SB();
  asm volatile("v_mfma_f32_16x16x32_bf16 %0, a[40:43], a[40:43], %0" : "+v"(gr) :  : ); SB();
  asm volatile("ds_read_b64_tr_b16 v[168:169], %0 offset:36864" :  : "v"(aW[0][0][2]) : "memory"); SB();
  asm volatile("s_waitcnt lgkmcnt(5)\n\tv_mfma_f32_16x16x32_bf16 %0, v[184:187], a[40:43], %0" : "+v"(a2[0][0]) :  : ); SB();
  asm volatile("ds_read_b64_tr_b16 v[170:171], %0 offset:37632" :  : "v"(aW[0][0][2]) : "memory"); SB();
  asm volatile("v_mfma_f32_16x16x32_bf16 %0, v[184:187], a[44:47], %0" : "+v"(a2[1][0]) :  : ); SB();
  asm volatile("ds_read_b128 a[64:67], %0 offset:16384" :  : "v"(aX[0][0]) : "memory"); SB();
  asm volatile("v_mfma_f32_16x16x32_bf16 %0, v[188:191], a[40:43], %0" : "+v"(a2[0][1]) :  : ); SB();
  asm volatile("ds_read_b128 a[68:71], %0 offset:16384" :  : "v"(aX[0][1]) : "memory"); SB();
  asm volatile("v_mfma_f32_16x16x32_bf16 %0, v[188:191], a[44:47], %0" : "+v"(a2[1][1]) :  : ); SB();
  asm volatile("ds_read_b128 a[72:75], %0 offset:16384" :  : "v"(aX[1][0]) : "memory"); SB();
  asm volatile("v_mfma_f32_16x16x32_bf16 %0, v[192:195], a[40:43], %0" : "+v"(a2[0][2]) :  : ); SB();
  asm volatile("ds_read_b128 a[76:79], %0 offset:16384" :  : "v"(aX[1][1]) : "memory"); SB();
  asm volatile("v_mfma_f32_16x16x32_bf16 %0, v[192:195], a[44:47], %0" : "+v"(a2[1][2]) :  : ); SB();
  asm volatile("buffer_load_dwordx4 %0, %1, %2 offen offset:1024 lds" :  : "v"(voA2), "s"(rX), "s"(kx[9]) : "memory"); SB();
  asm volatile("s_add_u32 m0, %0, 61440" :  : "s"(mW) : "memory", "scc"); SB();
  asm volatile("v_mfma_f32_16x16x32_bf16 %0, %1, a[48:51], %0" : "+v"(rs) : "v"(ones) : ); SB();
  asm volatile("ds_read_b128 a[80:83], %0 offset:24576" :  : "v"(aX[0][0]) : "memory"); SB();
  asm volatile("v_mfma_f32_16x16x32_bf16 %0, a[48:51], a[48:51], %0" : "+v"(gr) :  : ); SB();
  asm volatile("ds_read_b128 a[84:87], %0 offset:24576" :  : "v"(aX[0][1]) : "memory"); SB();
  asm volatile("v_mfma_f32_16x16x32_bf16 %0, %1, a[56:59], %0" : "+v"(rs) : "v"(ones) : ); SB();
  asm volatile("ds_read_b64_tr_b16 v[172:173], %0 offset:36864" :  : "v"(aW[0][1][0]) : "memory"); SB();
  asm volatile("v_mfma_f32_16x16x32_bf16 %0, a[56:59], a[56:59], %0" : "+v"(gr) :  : ); SB();
  asm volatile("ds_read_b64_tr_b16 v[174:175], %0 offset:37632" :  : "v"(aW[0][1][0]) : "memory"); SB();
  asm volatile("buffer_load_dwordx4 %0, %1, %2 offen lds" :  : "v"(voB[0]), "s"(rW), "s"(kw_[5]) : "memory"); SB();
  asm volatile("s_waitcnt lgkmcnt(8)\n\tv_mfma_f32_16x16x32_bf16 %0, v[160:163], a[48:51], %0" : "+v"(a2[0][0]) :  : ); SB();
  asm volatile("ds_read_b64_tr_b16 v[176:177], %0 offset:36864" :  : "v"(aW[0][1][1]) : "memory"); SB();
  asm volatile("v_mfma_f32_16x16x32_bf16 %0, v[160:163], a[52:55], %0" : "+v"(a2[1][0]) :  : ); SB();
  asm volatile("ds_read_b64_tr_b16 v[178:179], %0 offset:37632" :  : "v"(aW[0][1][1]) : "memory"); SB();
  asm volatile("v_mfma_f32_16x16x32_bf16 %0, v[160:163], %1, %0" : "+v"(c2[0]) : "v"(ones) : ); SB();
  asm volatile("ds_read_b64_tr_b16 v[180:181], %0 offset:36864" :  : "v"(aW[0][1][2]) : "memory"); SB();
  asm volatile("v_mfma_f32_16x16x32_bf16 %0, v[164:167], a[48:51], %0" : "+v"(a2[0][1]) :  : ); SB();
  asm volatile("ds_read_b64_tr_b16 v[182:183], %0 offset:37632" :  : "v"(aW[0][1][2]) : "memory"); SB();
  asm volatile("v_mfma_f32_16x16x32_bf16 %0, v[164:167], a[52:55], %0" : "+v"(a2[1][1]) :  : ); SB();
  asm volatile("ds_read_b128 a[88:91], %0 offset:24576" :  : "v"(aX[1][0]) : "memory"); SB();
  asm volatile("v_mfma_f32_16x16x32_bf16 %0, v[164:167], %1, %0" : "+v"(c2[1]) : "v"(ones) : ); SB();
  asm volatile("ds_read_b128 a[92:95], %0 offset:24576" :  : "v"(aX[1][1]) : "memory"); SB();
  asm volatile("v_mfma_f32_16x16x32_bf16 %0, v[168:171], a[48:51], %0" : "+v"(a2[0][2]) :  : ); SB();
  asm volatile("buffer_load_dwordx4 %0, %1, %2 offen offset:1024 lds" :  : "v"(voB[1]), "s"(rW), "s"(kw_[5]) : "memory"); SB();
  asm volatile("v_mfma_f32_16x16x32_bf16 %0, v[168:171], a[52:55], %0" : "+v"(a2[1][2]) :  : ); SB();
  asm volatile("buffer_load_dwordx4 %0, %1, %2 offen offset:2048 lds" :  : "v"(voB[2]), "s"(rW), "s"(kw_[5]) : "memory"); SB();
  asm volatile("v_mfma_f32_16x16x32_bf16 %0, v[168:171], %1, %0" : "+v"(c2[2]) : "v"(ones) : ); SB();
  asm volatile("s_add_u32 m0, %0, 73728" :  : "s"(mW) : "memory", "scc"); SB();
  asm volatile("s_nop 0\n\tbuffer_load_dwordx4 %0, %1, %2 offen lds" :  : "v"(voB[0]), "s"(rW), "s"(kw_[6]) : "memory"); SB();
  asm volatile("buffer_load_dwordx4 %0, %1, %2 offen offset:1024 lds" :  : "v"(voB[1]), "s"(rW), "s"(kw_[6]) : "memory"); SB();
  asm volatile("buffer_load_dwordx4 %0, %1, %2 offen offset:2048 lds" :  : "v"(voB[2]), "s"(rW), "s"(kw_[6]) : "memory"); SB();
  asm volatile("s_waitcnt vmcnt(10)\n\ts_barrier" :  :  : "memory"); SB();
  asm volatile("ds_read_b64_tr_b16 v[184:185], %0 offset:0" :  : "v"(aW[1][0][0]) : "memory"); SB();
  asm volatile("s_waitcnt lgkmcnt(1)\n\tv_mfma_f32_16x16x32_bf16 %0, %1, a[64:67], %0" : "+v"(rs) : "v"(ones) : ); SB();
  asm volatile("ds_read_b64_tr_b16 v[186:187], %0 offset:768" :  : "v"(aW[1][0][0]) : "memory"); SB();
  asm volatile("s_add_u32 m0, %0, 86016" :  : "s"(mW) : "memory", "scc"); SB();
  asm volatile("v_mfma_f32_16x16x32_bf16 %0, a[64:67], a[64:67], %0" : "+v"(gr) :  : ); SB();
  asm volatile("ds_read_b64_tr_b16 v[188:189], %0 offset:0" :  : "v"(aW[1][0][1]) : "memory"); SB();
  asm volatile("v_mfma_f32_16x16x32_bf16 %0, %1, a[72:75], %0" : "+v"(rs) : "v"(ones) : ); SB();
  asm volatile("ds_read_b64_tr_b16 v[190:191], %0 offset:768" :  : "v"(aW[1][0][1]) : "memory"); SB();
  asm volatile("v_mfma_f32_16x16x32_bf16 %0, a[72:75], a[72:75], %0" : "+v"(gr) :  : ); SB();
  asm volatile("ds_read_b64_tr_b16 v[192:193], %0 offset:0" :  : "v"(aW[1][0][2]) : "memory"); SB();
  asm volatile("v_mfma_f32_16x16x32_bf16 %0, v[172:175], a[56:59], %0" : "+v"(a2[0][0]) :  : ); SB();
  asm volatile("ds_read_b64_tr_b16 v[194:195], %0 offset:768" :  : "v"(aW[1][0][2]) : "memory"); SB();
  asm volatile("buffer_load_dwordx4 %0, %1, %2 offen lds" :  : "v"(voB[0]), "s"(rW), "s"(kw_[7]) : "memory"); SB();
  asm volatile("v_mfma_f32_16x16x32_bf16 %0, v[172:175], a[60:63], %0" : "+v"(a2[1][0]) :  : ); SB();
  asm volatile("ds_read_b128 a[96:99], %0 offset:32768" :  : "v"(aX[0][0]) : "memory"); SB();
  asm volatile("v_mfma_f32_16x16x32_bf16 %0, v[176:179], a[56:59], %0" : "+v"(a2[0][1]) :  : ); SB();
  asm volatile("ds_read_b128 a[100:103], %0 offset:32768" :  : "v"(aX[0][1]) : "memory"); SB();
  asm volatile("v_mfma_f32_16x16x32_bf16 %0, v[176:179], a[60:63], %0" : "+v"(a2[1][1]) :  : ); SB();
  asm volatile("ds_read_b128 a[104:107], %0 offset:32768" :  : "v"(aX[1][0]) : "memory"); SB();
  asm volatile("v_mfma_f32_16x16x32_bf16 %0, v[180:183], a[56:59], %0" : "+v"(a2[0][2]) :  : ); SB();
  asm volatile("buffer_load_dwordx4 %0, %1, %2 offen offset:1024 lds" :  : "v"(voB[1]), "s"(rW), "s"(kw_[7]) : "memory"); SB();
  asm volatile("v_mfma_f32_16x16x32_bf16 %0, v[180:183], a[60:63], %0" : "+v"(a2[1][2]) :  : ); SB();
  asm volatile("ds_read_b128 a[108:111], %0 offset:32768" :  : "v"(aX[1][1]) : "memory"); SB();
  asm volatile("v_mfma_f32_16x16x32_bf16 %0, %1, a[80:83], %0" : "+v"(rs) : "v"(ones) : ); SB();
  asm volatile("ds_read_b128 a[112:115], %0 offset:40960" :  : "v"(aX[0][0]) : "memory"); SB();
  asm volatile("v_mfma_f32_16x16x32_bf16 %0, a[80:83], a[80:83], %0" : "+v"(gr) :  : ); SB();
  asm volatile("ds_read_b128 a[116:119], %0 offset:40960" :  : "v"(aX[0][1]) : "memory"); SB();
  asm volatile("v_mfma_f32_16x16x32_bf16 %0, %1, a[88:91], %0" : "+v"(rs) : "v"(ones) : ); SB();
  asm volatile("ds_read_b64_tr_b16 v[160:161], %0 offset:0" :  : "v"(aW[1][1][0]) : "memory"); SB();
  asm volatile("v_mfma_f32_16x16x32_bf16 %0, a[88:91], a[88:91], %0" : "+v"(gr) :  : ); SB();
  asm volatile("ds_read_b64_tr_b16 v[162:163], %0 offset:768" :  : "v"(aW[1][1][0]) : "memory"); SB();
  asm volatile("s_waitcnt lgkmcnt(8)\n\tv_mfma_f32_16x16x32_bf16 %0, v[184:187], a[64:67], %0" : "+v"(a2[0][0]) :  : ); SB();
  asm volatile("ds_read_b64_tr_b16 v[164:165], %0 offset:0" :  : "v"(aW[1][1][1]) : "memory"); SB();
  asm volatile("v_mfma_f32_16x16x32_bf16 %0, v[184:187], a[68:71], %0" : "+v"(a2[1][0]) :  : ); SB();
  asm volatile("ds_read_b64_tr_b16 v[166:167], %0 offset:768" :  : "v"(aW[1][1][1]) : "memory"); SB();
  asm volatile("v_mfma_f32_16x16x32_bf16 %0, v[184:187], %1, %0" : "+v"(c2[0]) : "v"(ones) : ); SB();
  asm volatile("ds_read_b64_tr_b16 v[168:169], %0 offset:0" :  : "v"(aW[1][1][2]) : "memory"); SB();
  asm volatile("v_mfma_f32_16x16x32_bf16 %0, v[188:191], a[64:67], %0" : "+v"(a2[0][1]) :  : ); SB();
  asm volatile("ds_read_b64_tr_b16 v[170:171], %0 offset:768" :  : "v"(aW[1][1][2]) : "memory"); SB();
  asm volatile("buffer_load_dwordx4 %0, %1, %2 offen offset:2048 lds" :  : "v"(voB[2]), "s"(rW), "s"(kw_[7]) : "memory"); SB();
  asm volatile("v_mfma_f32_16x16x32_bf16 %0, v[188:191], a[68:71], %0" : "+v"(a2[1][1]) :  : ); SB();
  asm volatile("ds_read_b128 a[120:123], %0 offset:40960" :  : "v"(aX[1][0]) : "memory"); SB();
  asm volatile("s_add_u32 m0, %0, 0" :  : "s"(mW) : "memory", "scc"); SB();
  asm volatile("v_mfma_f32_16x16x32_bf16 %0, v[188:191], %1, %0" : "+v"(c2[1]) : "v"(ones) : ); SB();
  asm volatile("ds_read_b128 a[124:127], %0 offset:40960" :  : "v"(aX[1][1]) : "memory"); SB();
  asm volatile("v_mfma_f32_16x16x32_bf16 %0, v[192:195], a[64:67], %0" : "+v"(a2[0][2]) :  : ); SB();
  asm volatile("buffer_load_dwordx4 %0, %1, %2 offen lds" :  : "v"(voB[0]), "s"(rW), "s"(kw_[8]) : "memory"); SB();
  asm volatile("v_mfma_f32_16x16x32_bf16 %0, v[192:195], a[68:71], %0" : "+v"(a2[1][2]) :  : ); SB();
  asm volatile("v_mfma_f32_16x16x32_bf16 %0, v[192:195], %1, %0" : "+v"(c2[2]) : "v"(ones) : ); SB();
  asm volatile("buffer_load_dwordx4 %0, %1, %2 offen offset:1024 lds" :  : "v"(voB[1]), "s"(rW), "s"(kw_[8]) : "memory"); SB();
  asm volatile("buffer_load_dwordx4 %0, %1, %2 offen offset:2048 lds" :  : "v"(voB[2]), "s"(rW), "s"(kw_[8]) : "memory"); SB();
  asm volatile("s_add_u32 m0, %0, 12288" :  : "s"(mW) : "memory", "scc"); SB();
  asm volatile("s_nop 0\n\tbuffer_load_dwordx4 %0, %1, %2 offen lds" :  : "v"(voB[0]), "s"(rW), "s"(kw_[9]) : "memory"); SB();
  asm volatile("s_waitcnt vmcnt(10)\n\ts_barrier" :  :  : "memory"); SB();
  asm volatile("ds_read_b64_tr_b16 v[172:173], %0 offset:12288" :  : "v"(aW[1][0][0]) : "memory"); SB();
  asm volatile("s_waitcnt lgkmcnt(1)\n\tv_mfma_f32_16x16x32_bf16 %0, %1, a[96:99], %0" : "+v"(rs) : "v"(ones) : ); SB();
  asm volatile("ds_read_b64_tr_b16 v[174:175], %0 offset:13056" :  : "v"(aW[1][0][0]) : "memory"); SB();
  asm volatile("v_mfma_f32_16x16x32_bf16 %0, a[96:99], a[96:99], %0" : "+v"(gr) :  : ); SB();
  asm volatile("ds_read_b64_tr_b16 v[176:177], %0 offset:12288" :  : "v"(aW[1][0][1]) : "memory"); SB();
  asm volatile("v_mfma_f32_16x16x32_bf16 %0, %1, a[104:107], %0" : "+v"(rs) : "v"(ones) : ); SB();
  asm volatile("ds_read_b64_tr_b16 v[178:179], %0 offset:13056" :  : "v"(aW[1][0][1]) : "memory"); SB();
  asm volatile("v_mfma_f32_16x16x32_bf16 %0, a[104:107], a[104:107], %0" : "+v"(gr) :  : ); SB();
  asm volatile("ds_read_b64_tr_b16 v[180:181], %0 offset:12288" :  : "v"(aW[1][0][2]) : "memory"); SB();
  asm volatile("v_mfma_f32_16x16x32_bf16 %0, v[160:163], a[72:75], %0" : "+v"(a2[0][0]) :  : ); SB();
  asm volatile("ds_read_b64_tr_b16 v[182:183], %0 offset:13056" :  : "v"(aW[1][0][2]) : "memory"); SB();
  asm volatile("v_mfma_f32_16x16x32_bf16 %0, v[160:163], a[76:79], %0" : "+v"(a2[1][0]) :  : ); SB();
  asm volatile("ds_read_b128 a[128:131], %0 offset:49152" :  : "v"(aX[0][0]) : "memory"); SB();
  asm volatile("v_mfma_f32_16x16x32_bf16 %0, v[164:167], a[72:75], %0" : "+v"(a2[0][1]) :  : ); SB();
  asm volatile("ds_read_b128 a[132:135], %0 offset:49152" :  : "v"(aX[0][1]) : "memory"); SB();
  asm volatile("v_mfma_f32_16x16x32_bf16 %0, v[164:167], a[76:79], %0" : "+v"(a2[1][1]) :  : ); SB();
  asm volatile("ds_read_b128 a[136:139], %0 offset:49152" :  : "v"(aX[1][0]) : "memory"); SB();
  asm volatile("v_mfma_f32_16x16x32_bf16 %0, v[168:171], a[72:75], %0" : "+v"(a2[0][2]) :  : ); SB();
  asm volatile("ds_read_b128 a[140:143], %0 offset:49152" :  : "v"(aX[1][1]) : "memory"); SB();
  asm volatile("v_mfma_f32_16x16x32_bf16 %0, v[168:171], a[76:79], %0" : "+v"(a2[1][2]) :  : ); SB();
  asm volatile("ds_read_b128 a[144:147], %0 offset:57344" :  : "v"(aX[0][0]) : "memory"); SB();
  asm volatile("v_mfma_f32_16x16x32_bf16 %0, %1, a[112:115], %0" : "+v"(rs) : "v"(ones) : ); SB();
  asm volatile("ds_read_b128 a[148:151], %0 offset:57344" :  : "v"(aX[0][1]) : "memory"); SB();
  asm volatile("v_mfma_f32_16x16x32_bf16 %0, a[112:115], a[112:115], %0" : "+v"(gr) :  : ); SB();
  asm volatile("ds_read_b128 a[152:155], %0 offset:57344" :  : "v"(aX[1][0]) : "memory"); SB();
  asm volatile("v_mfma_f32_16x16x32_bf16 %0, %1, a[120:123], %0" : "+v"(rs) : "v"(ones) : ); SB();
  asm volatile("ds_read_b64_tr_b16 v[184:185], %0 offset:12288" :  : "v"(aW[1][1][0]) : "memory"); SB();
  asm volatile("v_mfma_f32_16x16x32_bf16 %0, a[120:123], a[120:123], %0" : "+v"(gr) :  : ); SB();
  asm volatile("ds_read_b64_tr_b16 v[186:187], %0 offset:13056" :  : "v"(aW[1][1][0]) : "memory"); SB();
  asm volatile("s_waitcnt lgkmcnt(9)\n\tv_mfma_f32_16x16x32_bf16 %0, v[172:175], a[80:83], %0" : "+v"(a2[0][0]) :  : ); SB();
  asm volatile("ds_read_b64_tr_b16 v[188:189], %0 offset:12288" :  : "v"(aW[1][1][1]) : "memory"); SB();
  asm volatile("v_mfma_f32_16x16x32_bf16 %0, v[172:175], a[84:87], %0" : "+v"(a2[1][0]) :  : ); SB();
  asm volatile("ds_read_b64_tr_b16 v[190:191], %0 offset:13056" :  : "v"(aW[1][1][1]) : "memory"); SB();
  asm volatile("v_mfma_f32_16x16x32_bf16 %0, v[172:175], %1, %0" : "+v"(c2[0]) : "v"(ones) : ); SB();
  asm volatile("ds_read_b64_tr_b16 v[192:193], %0 offset:12288" :  : "v"(aW[1][1][2]) : "memory"); SB();
  asm volatile("v_mfma_f32_16x16x32_bf16 %0, v[176:179], a[80:83], %0" : "+v"(a2[0][1]) :  : ); SB();
  asm volatile("ds_read_b64_tr_b16 v[194:195], %0 offset:13056" :  : "v"(aW[1][1][2]) : "memory"); SB();
  asm volatile("v_mfma_f32_16x16x32_bf16 %0, v[176:179], a[84:87], %0" : "+v"(a2[1][1]) :  : ); SB();
  asm volatile("ds_read_b128 a[156:159], %0 offset:57344" :  : "v"(aX[1][1]) : "memory"); SB();
  asm volatile("v_mfma_f32_16x16x32_bf16 %0, v[176:179], %1, %0" : "+v"(c2[1]) : "v"(ones) : ); SB();
  ao[0] = (w ^ 0) * 65536 + 3072; asm volatile("" : "+s"(ao[0])); SB();
  ao[1] = ao[0] + 31744; asm volatile("" : "+s"(ao[1])); SB();
  asm volatile("v_mfma_f32_16x16x32_bf16 %0, v[180:183], a[80:83], %0" : "+v"(a2[0][2]) :  : ); SB();
  ao[2] = (w ^ 1) * 65536 + 1024; asm volatile("" : "+s"(ao[2])); SB();
  ao[3] = ao[2] + 31744; asm volatile("" : "+s"(ao[3])); SB();
  asm volatile("v_mfma_f32_16x16x32_bf16 %0, v[180:183], a[84:87], %0" : "+v"(a2[1][2]) :  : ); SB();
  ao[4] = (w ^ 2) * 65536 + 3072; asm volatile("" : "+s"(ao[4])); SB();
  ao[5] = ao[4] + 31744; asm volatile("" : "+s"(ao[5])); SB();
  asm volatile("v_mfma_f32_16x16x32_bf16 %0, v[180:183], %1, %0" : "+v"(c2[2]) : "v"(ones) : ); SB();
  asm volatile("buffer_load_dwordx4 %0, %1, %2 offen offset:1024 lds" :  : "v"(voB[1]), "s"(rW), "s"(kw_[9]) : "memory"); SB();
  asm volatile("buffer_load_dwordx4 %0, %1, %2 offen offset:2048 lds" :  : "v"(voB[2]), "s"(rW), "s"(kw_[9]) : "memory"); SB();
  asm volatile("s_waitcnt vmcnt(9)\n\ts_barrier" :  :  : "memory"); SB();
  asm volatile("ds_read_b64_tr_b16 v[160:161], %0 offset:24576" :  : "v"(aW[1][0][0]) : "memory"); SB();
  asm volatile("s_waitcnt lgkmcnt(1)\n\tv_mfma_f32_16x16x32_bf16 %0, %1, a[128:131], %0" : "+v"(rs) : "v"(ones) : ); SB();
  asm volatile("ds_read_b64_tr_b16 v[162:163], %0 offset:25344" :  : "v"(aW[1][0][0]) : "memory"); SB();
  asm volatile("s_add_u32 m0, %0, 98304" :  : "s"(mA) : "memory", "scc"); SB();
  asm volatile("v_mfma_f32_16x16x32_bf16 %0, a[128:131], a[128:131], %0" : "+v"(gr) :  : ); SB();
  asm volatile("ds_read_b64_tr_b16 v[164:165], %0 offset:24576" :  : "v"(aW[1][0][1]) : "memory"); SB();
  asm volatile("v_mfma_f32_16x16x32_bf16 %0, %1, a[136:139], %0" : "+v"(rs) : "v"(ones) : ); SB();
  asm volatile("ds_read_b64_tr_b16 v[166:167], %0 offset:25344" :  : "v"(aW[1][0][1]) : "memory"); SB();
  asm volatile("v_mfma_f32_16x16x32_bf16 %0, a[136:139], a[136:139], %0" : "+v"(gr) :  : ); SB();
  asm volatile("ds_read_b64_tr_b16 v[168:169], %0 offset:24576" :  : "v"(aW[1][0][2]) : "memory"); SB();
  asm volatile("v_mfma_f32_16x16x32_bf16 %0, v[184:187], a[88:91], %0" : "+v"(a2[0][0]) :  : ); SB();
  asm volatile("ds_read_b64_tr_b16 v[170:171], %0 offset:25344" :  : "v"(aW[1][0][2]) : "memory"); SB();
  asm volatile("v_mfma_f32_16x16x32_bf16 %0, v[184:187], a[92:95], %0" : "+v"(a2[1][0]) :  : ); SB();
  ao[6] = (w ^ 3) * 65536 + 1024; asm volatile("" : "+s"(ao[6])); SB();
  ao[7] = ao[6] + 31744; asm volatile("" : "+s"(ao[7])); SB();
  asm volatile("v_mfma_f32_16x16x32_bf16 %0, v[188:191], a[88:91], %0" : "+v"(a2[0][1]) :  : ); SB();
  asm volatile("buffer_load_dwordx4 %0, %1, %2 offen lds" :  : "v"(voK[0]), "s"(rK), "s"(0u) : "memory"); SB();
  asm volatile("v_mfma_f32_16x16x32_bf16 %0, v[188:191], a[92:95], %0" : "+v"(a2[1][1]) :  : ); SB();
  asm volatile("buffer_load_dwordx4 %0, %1, %2 offen offset:1024 lds" :  : "v"(voK[1]), "s"(rK), "s"(0u) : "memory"); SB();
  asm volatile("v_mfma_f32_16x16x32_bf16 %0, v[192:195], a[88:91], %0" : "+v"(a2[0][2]) :  : ); SB();
  asm volatile("ds_read_b64_tr_b16 v[172:173], %0 offset:24576" :  : "v"(aW[1][1][0]) : "memory"); SB();
  asm volatile("v_mfma_f32_16x16x32_bf16 %0, v[192:195], a[92:95], %0" : "+v"(a2[1][2]) :  : ); SB();
  asm volatile("ds_read_b64_tr_b16 v[174:175], %0 offset:25344" :  : "v"(aW[1][1][0]) : "memory"); SB();
  asm volatile("v_mfma_f32_16x16x32_bf16 %0, %1, a[144:147], %0" : "+v"(rs) : "v"(ones) : ); SB();
  asm volatile("ds_read_b64_tr_b16 v[176:177], %0 offset:24576" :  : "v"(aW[1][1][1]) : "memory"); SB();
  asm volatile("v_mfma_f32_16x16x32_bf16 %0, a[144:147], a[144:147], %0" : "+v"(gr) :  : ); SB();
  asm volatile("ds_read_b64_tr_b16 v[178:179], %0 offset:25344" :  : "v"(aW[1][1][1]) : "memory"); SB();
  asm volatile("v_mfma_f32_16x16x32_bf16 %0, %1, a[152:155], %0" : "+v"(rs) : "v"(ones) : ); SB();
  asm volatile("ds_read_b64_tr_b16 v[180:181], %0 offset:24576" :  : "v"(aW[1][1][2]) : "memory"); SB();
  asm volatile("v_mfma_f32_16x16x32_bf16 %0, a[152:155], a[152:155], %0" : "+v"(gr) :  : ); SB();
  asm volatile("ds_read_b64_tr_b16 v[182:183], %0 offset:25344" :  : "v"(aW[1][1][2]) : "memory"); SB();
  asm volatile("s_waitcnt lgkmcnt(6)\n\tv_mfma_f32_16x16x32_bf16 %0, v[160:163], a[96:99], %0" : "+v"(a2[0][0]) :  : ); SB();
  asm volatile("buffer_load_dwordx4 %0, %1, %2 offen offset:2048 lds" :  : "v"(voK[2]), "s"(rK), "s"(0u) : "memory"); SB();
  asm volatile("s_mov_b32 exec_lo, 0xff\n\ts_mov_b32 exec_hi, 0\n\tbuffer_load_dwordx4 %0, %1, %2 offen offset:3072 lds\n\ts_mov_b64 exec, -1" :  : "v"(voK[3]), "s"(rK), "s"(0u) : "memory"); SB();
  asm volatile("s_mov_b32 exec_lo, -1\n\ts_mov_b32 exec_hi, 0xff\n\tbuffer_load_dwordx4 %0, %1, %2 offen offset:3328 lds\n\ts_mov_b64 exec, -1" :  : "v"(voH), "s"(rS), "s"(0u) : "memory"); SB();
  asm volatile("s_waitcnt vmcnt(11)\n\ts_barrier" :  :  : "memory"); SB();
  asm volatile("ds_read_b64_tr_b16 v[184:185], %0 offset:36864" :  : "v"(aW[1][0][0]) : "memory"); SB();
  asm volatile("s_add_u32 m0, %0, 102400" :  : "s"(mA) : "memory", "scc"); SB();
  asm volatile("v_mfma_f32_16x16x32_bf16 %0, v[160:163], a[100:103], %0" : "+v"(a2[1][0]) :  : ); SB();
  asm volatile("ds_read_b64_tr_b16 v[186:187], %0 offset:37632" :  : "v"(aW[1][0][0]) : "memory"); SB();
  asm volatile("v_mfma_f32_16x16x32_bf16 %0, v[160:163], %1, %0" : "+v"(c2[0]) : "v"(ones) : ); SB();
  asm volatile("ds_read_b64_tr_b16 v[188:189], %0 offset:36864" :  : "v"(aW[1][0][1]) : "memory"); SB();
  asm volatile("v_mfma_f32_16x16x32_bf16 %0, v[164:167], a[96:99], %0" : "+v"(a2[0][1]) :  : ); SB();
  asm volatile("ds_read_b64_tr_b16 v[190:191], %0 offset:37632" :  : "v"(aW[1][0][1]) : "memory"); SB();
  asm volatile("v_mfma_f32_16x16x32_bf16 %0, v[164:167], a[100:103], %0" : "+v"(a2[1][1]) :  : ); SB();
  asm volatile("ds_read_b64_tr_b16 v[192:193], %0 offset:36864" :  : "v"(aW[1][0][2]) : "memory"); SB();
  asm volatile("v_mfma_f32_16x16x32_bf16 %0, v[164:167], %1, %0" : "+v"(c2[1]) : "v"(ones) : ); SB();
  asm volatile("ds_read_b64_tr_b16 v[194:195], %0 offset:37632" :  : "v"(aW[1][0][2]) : "memory"); SB();
  asm volatile("v_mfma_f32_16x16x32_bf16 %0, v[168:171], a[96:99], %0" : "+v"(a2[0][2]) :  : ); SB();
  asm volatile("buffer_load_dwordx4 %0, %1, %2 offen lds" :  : "v"(voV[0]), "s"(rV), "s"(0u) : "memory"); SB();
  asm volatile("v_mfma_f32_16x16x32_bf16 %0, v[168:171], a[100:103], %0" : "+v"(a2[1][2]) :  : ); SB();
  asm volatile("buffer_load_dwordx4 %0, %1, %2 offen offset:1024 lds" :  : "v"(voV[1]), "s"(rV), "s"(0u) : "memory"); SB();
  asm volatile("v_mfma_f32_16x16x32_bf16 %0, v[168:171], %1, %0" : "+v"(c2[2]) : "v"(ones) : ); SB();
  asm volatile("s_waitcnt lgkmcnt(6)\n\tv_mfma_f32_16x16x32_bf16 %0, v[172:175], a[104:107], %0" : "+v"(a2[0][0]) :  : ); SB();
  asm volatile("ds_read_b64_tr_b16 v[160:161], %0 offset:36864" :  : "v"(aW[1][1][0]) : "memory"); SB();
  asm volatile("v_cndmask_b32 %0, %1, %2, %3" : "=v"(xt[0]) : "v"(gr[0]), "v"(gr[1]), "s"(0xAAAAAAAAAAAAAAAAull) : ); SB();
  asm volatile("v_mfma_f32_16x16x32_bf16 %0, v[172:175], a[108:111], %0" : "+v"(a2[1][0]) :  : ); SB();
  asm volatile("s_mov_b32 exec_lo, 0x10001\n\ts_mov_b32 exec_hi, 0x10001\n\tds_write_b128 %0, %1 offset:0\n\ts_mov_b64 exec, -1" :  : "v"(ca), "v"(c2[0]) : "memory"); SB();
  asm volatile("ds_read_b64_tr_b16 v[162:163], %0 offset:37632" :  : "v"(aW[1][1][0]) : "memory"); SB();
  asm volatile("v_cndmask_b32 %0, %1, %2, %3" : "=v"(xt[1]) : "v"(gr[2]), "v"(gr[3]), "s"(0xAAAAAAAAAAAAAAAAull) : ); SB();
  asm volatile("v_mfma_f32_16x16x32_bf16 %0, v[176:179], a[104:107], %0" : "+v"(a2[0][1]) :  : ); SB();
  asm volatile("ds_read_b64_tr_b16 v[164:165], %0 offset:36864" :  : "v"(aW[1][1][1]) : "memory"); SB();
  asm volatile("v_mul_f32 %0, 0x3acccccd, %1" : "=v"(xs[0][0]) : "v"(rs[0]) : ); SB();
  asm volatile("v_mfma_f32_16x16x32_bf16 %0, v[176:179], a[108:111], %0" : "+v"(a2[1][1]) :  : ); SB();
  asm volatile("s_mov_b32 exec_lo, 0x10001\n\ts_mov_b32 exec_hi, 0x10001\n\tds_write_b128 %0, %1 offset:64\n\ts_mov_b64 exec, -1" :  : "v"(ca), "v"(c2[1]) : "memory"); SB();
  asm volatile("ds_read_b64_tr_b16 v[166:167], %0 offset:37632" :  : "v"(aW[1][1][1]) : "memory"); SB();
  asm volatile("v_cndmask_b32 %0, %0, %1, %2" : "+v"(xt[0]) : "v"(xt[1]), "s"(0xCCCCCCCCCCCCCCCCull) : ); SB();
  asm volatile("v_mfma_f32_16x16x32_bf16 %0, v[180:183], a[104:107], %0" : "+v"(a2[0][2]) :  : ); SB();
  asm volatile("ds_read_b64_tr_b16 v[168:169], %0 offset:36864" :  : "v"(aW[1][1][2]) : "memory"); SB();
  asm volatile("v_mul_f32 %0, 0x3a83126f, %1" : "=v"(xt[1]) : "v"(rs[0]) : ); SB();
  asm volatile("v_mfma_f32_16x16x32_bf16 %0, v[180:183], a[108:111], %0" : "+v"(a2[1][2]) :  : ); SB();
  asm volatile("s_mov_b32 exec_lo, 0x10001\n\ts_mov_b32 exec_hi, 0x10001\n\tds_write_b128 %0, %1 offset:128\n\ts_mov_b64 exec, -1" :  : "v"(ca), "v"(c2[2]) : "memory"); SB();
  asm volatile("ds_read_b64_tr_b16 v[170:171], %0 offset:37632" :  : "v"(aW[1][1][2]) : "memory"); SB();
  asm volatile("s_waitcnt lgkmcnt(6)\n\tv_mfma_f32_16x16x32_bf16 %0, v[184:187], a[112:115], %0" : "+v"(a2[0][0]) :  : ); SB();
  asm volatile("s_mov_b32 exec_lo, -1\n\ts_mov_b32 exec_hi, 0\n\tbuffer_load_dwordx4 %0, %1, %2 offen offset:2048 lds\n\ts_mov_b64 exec, -1" :  : "v"(voV[2]), "s"(rV), "s"(0u) : "memory"); SB();
  asm volatile("v_fma_f32 %0, -%1, %2, %3" : "=v"(xs[0][1]) : "v"(rs[0]), "v"(xs[0][0]), "v"(xt[0]) : ); SB();
  asm volatile("v_mfma_f32_16x16x32_bf16 %0, v[184:187], a[116:119], %0" : "+v"(a2[1][0]) :  : ); SB();
  asm volatile("buffer_load_dwordx4 %0, %1, %2 offen offset:3072 lds" :  : "v"(voO), "s"(rO), "s"(0u) : "memory"); SB();
  asm volatile("v_mul_f32 %0, %0, %1" : "+v"(xt[1]) : "v"(xs[0][0]) : ); SB();
  asm volatile("v_mfma_f32_16x16x32_bf16 %0, v[184:187], %1, %0" : "+v"(c2[0]) : "v"(ones) : ); SB();
  if (__builtin_expect((__builtin_amdgcn_ballot_w64(!(xs[0][1] >= xt[1])) & 0xF0000F0000F0000Full) != 0, 0)) {
    const f32x2 mm = {xs[0][0], xs[0][0]};
    f32x2 q2 = {0.f, 0.f};
    const u32x4* xp = (const u32x4*)(x + (size_t)(rb * 64 + w * 16 + l15) * 1280 + g4 * 320);
#pragma unroll 1
    for (int c = 0; c < 20; ++c) {
      u32x4 xq;
      asm volatile("global_load_dwordx4 %0, %1, off\n\ts_waitcnt vmcnt(0)" : "=&v"(xq) : "v"(xp + c) : "memory");
#pragma unroll
      for (int e = 0; e < 4; ++e) { const f32x2 d = unpk(xq[e]) - mm; q2 = __builtin_elementwise_fma(d, d, q2); }
    }
    xs[0][1] = red32(red16(q2[0] + q2[1]));
  }
  SB();
  asm volatile("v_mfma_f32_16x16x32_bf16 %0, v[188:191], a[112:115], %0" : "+v"(a2[0][1]) :  : ); SB();
  asm volatile("s_waitcnt vmcnt(12)\n\ts_barrier" :  :  : "memory"); SB();
  asm volatile("ds_read_b64_tr_b16 v[172:173], %0 offset:0" :  : "v"(aW[0][0][0]) : "memory"); SB();
  asm volatile("v_max_f32 %0, 0, %0" : "+v"(xs[0][1]) :  : ); SB();
  asm volatile("v_mfma_f32_16x16x32_bf16 %0, v[188:191], a[116:119], %0" : "+v"(a2[1][1]) :  : ); SB();
  asm volatile("ds_read_b64_tr_b16 v[174:175], %0 offset:768" :  : "v"(aW[0][0][0]) : "memory"); SB();
  asm volatile("v_mul_f32 %0, 0x3acccccd, %0" : "+v"(xs[0][1]) :  : ); SB();
  asm volatile("v_mfma_f32_16x16x32_bf16 %0, v[188:191], %1, %0" : "+v"(c2[1]) : "v"(ones) : ); SB();
  asm volatile("ds_read_b64_tr_b16 v[176:177], %0 offset:0" :  : "v"(aW[0][0][1]) : "memory"); SB();
  asm volatile("v_cvt_pk_bf16_f32 %0, %0, %0" : "+v"(xs[0][1]) :  : ); SB();
  asm volatile("v_mfma_f32_16x16x32_bf16 %0, v[192:195], a[112:115], %0" : "+v"(a2[0][2]) :  : ); SB();
  asm volatile("ds_read_b64_tr_b16 v[178:179], %0 offset:768" :  : "v"(aW[0][0][1]) : "memory"); SB();
  asm volatile("v_lshlrev_b32 %0, 16, %0" : "+v"(xs[0][1]) :  : ); SB();
  asm volatile("v_mfma_f32_16x16x32_bf16 %0, v[192:195], a[116:119], %0" : "+v"(a2[1][2]) :  : ); SB();
  asm volatile("ds_read_b64_tr_b16 v[180:181], %0 offset:0" :  : "v"(aW[0][0][2]) : "memory"); SB();
  asm volatile("v_add_f32 %0, 0x3727c5ac, %0" : "+v"(xs[0][1]) :  : ); SB();
  asm volatile("v_mfma_f32_16x16x32_bf16 %0, v[192:195], %1, %0" : "+v"(c2[2]) : "v"(ones) : ); SB();
  asm volatile("ds_read_b64_tr_b16 v[182:183], %0 offset:768" :  : "v"(aW[0][0][2]) : "memory"); SB();
  asm volatile("v_cvt_pk_bf16_f32 %0, %0, %0" : "+v"(xs[0][1]) :  : ); SB();
  asm volatile("v_mfma_f32_16x16x32_bf16 %0, v[160:163], a[120:123], %0" : "+v"(a2[0][0]) :  : ); SB();
  asm volatile("buffer_load_dwordx4 v[228:231], %0, %1, %2 offen" :  : "v"(vF), "s"(rF), "s"(0u) : "memory"); SB();
  asm volatile("v_lshlrev_b32 %0, 16, %0" : "+v"(xs[0][1]) :  : ); SB();
  asm volatile("v_mfma_f32_16x16x32_bf16 %0, v[160:163], a[124:127], %0" : "+v"(a2[1][0]) :  : ); SB();
  asm volatile("buffer_load_dwordx4 v[232:235], %0, %1, %2 offen" :  : "v"(vF), "s"(rF), "s"(4096u) : "memory"); SB();
  asm volatile("v_mfma_f32_16x16x32_bf16 %0, v[160:163], %1, %0" : "+v"(c2[0]) : "v"(ones) : ); SB();
  asm volatile("v_sqrt_f32 %0, %0" : "+v"(xs[0][1]) :  : ); SB();
  asm volatile("s_waitcnt lgkmcnt(6)\n\tv_mfma_f32_16x16x32_bf16 %0, v[164:167], a[120:123], %0" : "+v"(a2[0][1]) :  : ); SB();
  asm volatile("ds_read_b64_tr_b16 v[184:185], %0 offset:0" :  : "v"(aW[0][1][0]) : "memory"); SB();
  asm volatile("v_cvt_pk_bf16_f32 %0, %0, %0" : "+v"(xs[0][0]) :  : ); SB();
  asm volatile("v_mfma_f32_16x16x32_bf16 %0, v[164:167], a[124:127], %0" : "+v"(a2[1][1]) :  : ); SB();
  asm volatile("ds_read_b64_tr_b16 v[186:187], %0 offset:768" :  : "v"(aW[0][1][0]) : "memory"); SB();
  asm volatile("v_lshlrev_b32 %0, 16, %0" : "+v"(xs[0][0]) :  : ); SB();
  asm volatile("v_mfma_f32_16x16x32_bf16 %0, v[164:167], %1, %0" : "+v"(c2[1]) : "v"(ones) : ); SB();
  asm volatile("ds_read_b64_tr_b16 v[188:189], %0 offset:0" :  : "v"(aW[0][1][1]) : "memory"); SB();
  asm volatile("v_cvt_pk_bf16_f32 %0, %0, %0" : "+v"(xs[0][1]) :  : ); SB();
  asm volatile("v_mfma_f32_16x16x32_bf16 %0, v[168:171], a[120:123], %0" : "+v"(a2[0][2]) :  : ); SB();
  asm volatile("ds_read_b64_tr_b16 v[190:191], %0 offset:768" :  : "v"(aW[0][1][1]) : "memory"); SB();
  asm volatile("v_lshlrev_b32 %0, 16, %0" : "+v"(xs[0][1]) :  : ); SB();
  asm volatile("v_mfma_f32_16x16x32_bf16 %0, v[168:171], a[124:127], %0" : "+v"(a2[1][2]) :  : ); SB();
  asm volatile("ds_read_b64_tr_b16 v[192:193], %0 offset:0" :  : "v"(aW[0][1][2]) : "memory"); SB();
  asm volatile("v_mfma_f32_16x16x32_bf16 %0, v[168:171], %1, %0" : "+v"(c2[2]) : "v"(ones) : ); SB();
  asm volatile("ds_read_b64_tr_b16 v[194:195], %0 offset:768" :  : "v"(aW[0][1][2]) : "memory"); SB();
  asm volatile("s_waitcnt lgkmcnt(6)\n\tv_mfma_f32_16x16x32_bf16 %0, v[172:175], a[128:131], %0" : "+v"(a2[0][0]) :  : ); SB();
  asm volatile("buffer_load_dwordx4 v[236:239], %0, %1, %2 offen" :  : "v"(vF), "s"(rF), "s"(c2s_) : "memory"); SB();
  asm volatile("v_mfma_f32_16x16x32_bf16 %0, v[172:175], a[132:135], %0" : "+v"(a2[1][0]) :  : ); SB();
  asm volatile("v_rcp_f32 %0, %1" : "=v"(xt[0]) : "v"(xs[0][1]) : ); SB();
  asm volatile("v_mfma_f32_16x16x32_bf16 %0, v[172:175], %1, %0" : "+v"(c2[0]) : "v"(ones) : ); SB();
  asm volatile("s_nop 0\n\tv_mul_f32 %0, 0xc0135761, %1" : "=v"(xs[0][2]) : "v"(xt[0]) : ); SB();
  asm volatile("v_mfma_f32_16x16x32_bf16 %0, v[176:179], a[128:131], %0" : "+v"(a2[0][1]) :  : ); SB();
  asm volatile("v_mul_f32 %0, %1, %1" : "=v"(xt[1]) : "v"(xt[0]) : ); SB();
  asm volatile("v_mul_f32 %0, 0xbdd2d3e8, %0" : "+v"(xt[0]) :  : ); SB();
  asm volatile("v_mul_f32 %0, %1, %2" : "=v"(xs[0][3]) : "v"(xt[0]), "v"(xt[1]) : ); SB();
  asm volatile("s_mov_b32 exec_lo, 0xf0000f\n\ts_mov_b32 exec_hi, 0xf0000f00\n\tds_write_b128 %0, %1\n\ts_mov_b64 exec, -1" :  : "v"(xw), "v"(xs[0]) : "memory"); SB();
  asm volatile("s_waitcnt vmcnt(12) lgkmcnt(0)\n\ts_barrier" :  :  : "memory"); SB();
  asm volatile("ds_read_b64_tr_b16 v[160:161], %0 offset:12288" :  : "v"(aW[0][0][0]) : "memory"); SB();
  asm volatile("s_add_u32 m0, %0, 24576" :  : "s"(mW) : "memory", "scc"); SB();
  asm volatile("v_mfma_f32_16x16x32_bf16 %0, v[176:179], a[132:135], %0" : "+v"(a2[1][1]) :  : ); SB();
  asm volatile("ds_read_b64_tr_b16 v[162:163], %0 offset:13056" :  : "v"(aW[0][0][0]) : "memory"); SB();
  asm volatile("v_mfma_f32_16x16x32_bf16 %0, v[176:179], %1, %0" : "+v"(c2[1]) : "v"(ones) : ); SB();
  asm volatile("ds_read_b64_tr_b16 v[164:165], %0 offset:12288" :  : "v"(aW[0][0][1]) : "memory"); SB();
  asm volatile("v_mfma_f32_16x16x32_bf16 %0, v[180:183], a[128:131], %0" : "+v"(a2[0][2]) :  : ); SB();
  asm volatile("ds_read_b64_tr_b16 v[166:167], %0 offset:13056" :  : "v"(aW[0][0][1]) : "memory"); SB();
  asm volatile("v_mfma_f32_16x16x32_bf16 %0, v[180:183], a[132:135], %0" : "+v"(a2[1][2]) :  : ); SB();
  asm volatile("ds_read_b64_tr_b16 v[168:169], %0 offset:12288" :  : "v"(aW[0][0][2]) : "memory"); SB();
  asm volatile("v_mfma_f32_16x16x32_bf16 %0, v[180:183], %1, %0" : "+v"(c2[2]) : "v"(ones) : ); SB();
  asm volatile("ds_read_b64_tr_b16 v[170:171], %0 offset:13056" :  : "v"(aW[0][0][2]) : "memory"); SB();
  asm volatile("v_mfma_f32_16x16x32_bf16 %0, v[184:187], a[136:139], %0" : "+v"(a2[0][0]) :  : ); SB();
  asm volatile("ds_read_b128 %0, %1 offset:0" : "=&v"(xs[0]) : "v"(xw) : "memory"); SB();
  asm volatile("v_mfma_f32_16x16x32_bf16 %0, v[184:187], a[140:143], %0" : "+v"(a2[1][0]) :  : ); SB();
  asm volatile("ds_read_b128 %0, %1 offset:0" : "=&v"(xs[1]) : "v"(xr_) : "memory"); SB();
  asm volatile("v_mfma_f32_16x16x32_bf16 %0, v[184:187], %1, %0" : "+v"(c2[0]) : "v"(ones) : ); SB();
  asm volatile("ds_read_b128 %0, %1 offset:0" : "=&v"(co[0]) : "v"(cr) : "memory"); SB();
  asm volatile("v_mfma_f32_16x16x32_bf16 %0, v[188:191], a[136:139], %0" : "+v"(a2[0][1]) :  : ); SB();
  asm volatile("ds_read_b64_tr_b16 v[172:173], %0 offset:12288" :  : "v"(aW[0][1][0]) : "memory"); SB();
  asm volatile("v_mfma_f32_16x16x32_bf16 %0, v[188:191], a[140:143], %0" : "+v"(a2[1][1]) :  : ); SB();
  asm volatile("ds_read_b64_tr_b16 v[174:175], %0 offset:13056" :  : "v"(aW[0][1][0]) : "memory"); SB();
  asm volatile("v_mfma_f32_16x16x32_bf16 %0, v[188:191], %1, %0" : "+v"(c2[1]) : "v"(ones) : ); SB();
  asm volatile("ds_read_b64_tr_b16 v[176:177], %0 offset:12288" :  : "v"(aW[0][1][1]) : "memory"); SB();
  asm volatile("v_mfma_f32_16x16x32_bf16 %0, v[192:195], a[136:139], %0" : "+v"(a2[0][2]) :  : ); SB();
  asm volatile("ds_read_b64_tr_b16 v[178:179], %0 offset:13056" :  : "v"(aW[0][1][1]) : "memory"); SB();
  asm volatile("v_mfma_f32_16x16x32_bf16 %0, v[192:195], a[140:143], %0" : "+v"(a2[1][2]) :  : ); SB();
  asm volatile("ds_read_b64_tr_b16 v[180:181], %0 offset:12288" :  : "v"(aW[0][1][2]) : "memory"); SB();
  asm volatile("buffer_load_dwordx4 %0, %1, %2 offen lds" :  : "v"(voB[0]), "s"(rW1), "s"(kw_[0]) : "memory"); SB();
  asm volatile("v_mfma_f32_16x16x32_bf16 %0, v[192:195], %1, %0" : "+v"(c2[2]) : "v"(ones) : ); SB();
  asm volatile("ds_read_b64_tr_b16 v[182:183], %0 offset:13056" :  : "v"(aW[0][1][2]) : "memory"); SB();
  asm volatile("s_waitcnt lgkmcnt(9)\n\tv_mfma_f32_16x16x32_bf16 %0, v[160:163], a[144:147], %0" : "+v"(a2[0][0]) :  : ); SB();
  asm volatile("ds_read_b128 %0, %1 offset:64" : "=&v"(co[1]) : "v"(cr) : "memory"); SB();
  asm volatile("v_mfma_f32_16x16x32_bf16 %0, v[160:163], a[148:151], %0" : "+v"(a2[1][0]) :  : ); SB();
  asm volatile("ds_read_b128 %0, %1 offset:128" : "=&v"(co[2]) : "v"(cr) : "memory"); SB();
  asm volatile("v_mfma_f32_16x16x32_bf16 %0, v[160:163], %1, %0" : "+v"(c2[0]) : "v"(ones) : ); SB();
  asm volatile("v_readfirstlane_b32 %0, v252\n\tv_readfirstlane_b32 %1, v253\n\ts_nop 0\n\ts_add_u32 %0, %0, 1\n\ts_addc_u32 %1, %1, 0" : "=&s"(tg0), "=&s"(tg1) :  : "memory", "scc"); SB();
  asm volatile("v_mfma_f32_16x16x32_bf16 %0, v[164:167], a[144:147], %0" : "+v"(a2[0][1]) :  : ); SB();
  asm volatile("buffer_load_dwordx4 %0, %1, %2 offen offset:1024 lds" :  : "v"(voB[1]), "s"(rW1), "s"(kw_[0]) : "memory"); SB();
  asm volatile("v_mfma_f32_16x16x32_bf16 %0, v[164:167], a[148:151], %0" : "+v"(a2[1][1]) :  : ); SB();
  { unsigned lane = lane0, w = w0, rb = rb0, cb = cb0;
  asm volatile("" : "+v"(lane)); asm volatile("" : "+s"(w), "+s"(rb), "+s"(cb)); SB();

  asm volatile("v_mfma_f32_16x16x32_bf16 %0, v[164:167], %1, %0" : "+v"(c2[1]) : "v"(ones) : ); SB();
  w3k[0] = w3s + 32768; asm volatile("" : "+s"(w3k[0])); SB();
  asm volatile("v_mfma_f32_16x16x32_bf16 %0, v[168:171], a[144:147], %0" : "+v"(a2[0][2]) :  : ); SB();
  w3k[1] = w3s + 65536; asm volatile("" : "+s"(w3k[1])); SB();
  asm volatile("buffer_load_dwordx4 %0, %1, %2 offen offset:2048 lds" :  : "v"(voB[2]), "s"(rW1), "s"(kw_[0]) : "memory"); SB();
  asm volatile("v_mfma_f32_16x16x32_bf16 %0, v[168:171], a[148:151], %0" : "+v"(a2[1][2]) :  : ); SB();
  sFZ = W_FZ + ((cb * 32 + rb) * 4 + w) * 8; asm volatile("" : "+s"(sFZ)); SB();
  asm volatile("v_mfma_f32_16x16x32_bf16 %0, v[168:171], %1, %0" : "+v"(c2[2]) : "v"(ones) : ); SB();
  sFK = sFZ + 8192; asm volatile("" : "+s"(sFK)); SB();
  asm volatile("s_add_u32 m0, %0, 36864" :  : "s"(mW) : "memory", "scc"); SB();
  asm volatile("s_waitcnt lgkmcnt(1)\n\tv_mfma_f32_16x16x32_bf16 %0, v[172:175], a[152:155], %0" : "+v"(a2[0][0]) :  : ); SB();
  mU = lb + w * 1024; asm volatile("" : "+s"(mU)); SB();
  asm volatile("v_mfma_f32_16x16x32_bf16 %0, v[172:175], a[156:159], %0" : "+v"(a2[1][0]) :  : ); SB();
  uso = W_KV + c2s; asm volatile("" : "+s"(uso)); SB();
  asm volatile("v_mfma_f32_16x16x32_bf16 %0, v[172:175], %1, %0" : "+v"(c2[0]) : "v"(ones) : ); SB();
  asm volatile("buffer_load_dwordx4 %0, %1, %2 offen lds" :  : "v"(voB[0]), "s"(rW1), "s"(kw_[1]) : "memory"); SB();
  asm volatile("v_mfma_f32_16x16x32_bf16 %0, v[176:179], a[152:155], %0" : "+v"(a2[0][1]) :  : ); SB();
  asm volatile("buffer_load_dwordx4 %0, %1, %2 offen offset:1024 lds" :  : "v"(voB[1]), "s"(rW1), "s"(kw_[1]) : "memory"); SB();
  asm volatile("v_mfma_f32_16x16x32_bf16 %0, v[176:179], a[156:159], %0" : "+v"(a2[1][1]) :  : ); SB();
  asm volatile("v_mfma_f32_16x16x32_bf16 %0, v[176:179], %1, %0" : "+v"(c2[1]) : "v"(ones) : ); SB();
  asm volatile("v_mfma_f32_16x16x32_bf16 %0, v[180:183], a[152:155], %0" : "+v"(a2[0][2]) :  : ); SB();
  asm volatile("buffer_load_dwordx4 %0, %1, %2 offen offset:2048 lds" :  : "v"(voB[2]), "s"(rW1), "s"(kw_[1]) : "memory"); SB();
  asm volatile("v_mfma_f32_16x16x32_bf16 %0, v[180:183], a[156:159], %0" : "+v"(a2[1][2]) :  : ); SB();
  asm volatile("v_mfma_f32_16x16x32_bf16 %0, v[180:183], %1, %0" : "+v"(c2[2]) : "v"(ones) : ); SB();
  ST(3);
  ST(5);
  const unsigned long long tg = ((unsigned long long)tg1 << 32) | tg0;
  asm volatile("s_waitcnt lgkmcnt(0)" : "+v"(xs[0]), "+v"(xs[1]), "+v"(co[0]), "+v"(co[1]), "+v"(co[2]) :  : "memory"); SB();
  asm volatile("s_nop 3" : "+v"(a2[0][0]), "+v"(a2[0][1]), "+v"(a2[0][2]), "+v"(a2[1][0]), "+v"(a2[1][1]), "+v"(a2[1][2]), "+v"(c2[0]), "+v"(c2[1]), "+v"(c2[2]) :  : ); SB();
  for (int i = 0; i < 2; ++i) { xm[i] = xs[i][0]; xd[i] = xs[i][1]; k1[i] = xs[i][2]; k2[i] = xs[i][3]; }
  c2[0] += co[0]; c2[1] += co[1]; c2[2] += co[2];
  f32x2 ge2[2];
  auto act = [&](f32x2& t, f32x2& e, const f32x4& a, const f32x4& cs, int i, int h) {
    t = __builtin_elementwise_fma((f32x2){-xm[i], -xm[i]}, (f32x2){cs[2 * h], cs[2 * h + 1]}, (f32x2){a[2 * h], a[2 * h + 1]});
    const f32x2 u = t * __builtin_elementwise_fma(t * t, (f32x2){k2[i], k2[i]}, (f32x2){k1[i], k1[i]});
    e = (f32x2){__builtin_amdgcn_exp2f(u[0]), __builtin_amdgcn_exp2f(u[1])};
  };
  auto act2 = [&](f32x2& t, f32x2 e, int i) {
    e = __builtin_elementwise_fma(e, (f32x2){xd[i], xd[i]}, (f32x2){xd[i], xd[i]});
    t *= (f32x2){__builtin_amdgcn_rcpf(e[0]), __builtin_amdgcn_rcpf(e[1])};
  };
  f32x2 gv[2][3][2];
  float sv[2][2], pv[2];
  act(gv[0][0][0], ge2[0], a2[0][0], c2[0], 0, 0); act(gv[0][0][1], ge2[1], a2[0][0], c2[0], 0, 1);
  asm volatile("" : "+v"(gv[0][0][0]), "+v"(gv[0][0][1]), "+v"(ge2[0]), "+v"(ge2[1]));
  SB();
  asm volatile("s_add_u32 m0, %0, 49152" :  : "s"(mW) : "memory", "scc"); SB();
  asm volatile("s_nop 0\n\tbuffer_load_dwordx4 %0, %1, %2 offen lds" :  : "v"(voB[0]), "s"(rW1), "s"(kw_[2]) : "memory"); SB();
  asm volatile("buffer_load_dwordx4 %0, %1, %2 offen offset:1024 lds" :  : "v"(voB[1]), "s"(rW1), "s"(kw_[2]) : "memory"); SB();
  act2(gv[0][0][0], ge2[0], 0); act2(gv[0][0][1], ge2[1], 0);
  asm volatile("" : "+v"(gv[0][0][0]), "+v"(gv[0][0][1]));
  SB();
  asm volatile("buffer_load_dwordx4 %0, %1, %2 offen offset:2048 lds" :  : "v"(voB[2]), "s"(rW1), "s"(kw_[2]) : "memory"); SB();
  act(gv[0][1][0], ge2[0], a2[0][1], c2[1], 0, 0); act(gv[0][1][1], ge2[1], a2[0][1], c2[1], 0, 1);
  asm volatile("" : "+v"(gv[0][1][0]), "+v"(gv[0][1][1]), "+v"(ge2[0]), "+v"(ge2[1]));
  SB();
  asm volatile("s_add_u32 m0, %0, 61440" :  : "s"(mW) : "memory", "scc"); SB();
  asm volatile("s_nop 0\n\tbuffer_load_dwordx4 %0, %1, %2 offen lds" :  : "v"(voB[0]), "s"(rW1), "s"(kw_[3]) : "memory"); SB();
  asm volatile("buffer_load_dwordx4 %0, %1, %2 offen offset:1024 lds" :  : "v"(voB[1]), "s"(rW1), "s"(kw_[3]) : "memory"); SB();
  act2(gv[0][1][0], ge2[0], 0); act2(gv[0][1][1], ge2[1], 0);
  asm volatile("" : "+v"(gv[0][1][0]), "+v"(gv[0][1][1]));
  SB();
  asm volatile("buffer_load_dwordx4 %0, %1, %2 offen offset:2048 lds" :  : "v"(voB[2]), "s"(rW1), "s"(kw_[3]) : "memory"); SB();
  act(gv[0][2][0], ge2[0], a2[0][2], c2[2], 0, 0); act(gv[0][2][1], ge2[1], a2[0][2], c2[2], 0, 1);
  asm volatile("" : "+v"(gv[0][2][0]), "+v"(gv[0][2][1]), "+v"(ge2[0]), "+v"(ge2[1]));
  SB();
  asm volatile("s_add_u32 m0, %0, 73728" :  : "s"(mW) : "memory", "scc"); SB();
  asm volatile("s_nop 0\n\tbuffer_load_dwordx4 %0, %1, %2 offen lds" :  : "v"(voB[0]), "s"(rW1), "s"(kw_[4]) : "memory"); SB();
  asm volatile("buffer_load_dwordx4 %0, %1, %2 offen offset:1024 lds" :  : "v"(voB[1]), "s"(rW1), "s"(kw_[4]) : "memory"); SB();
  act2(gv[0][2][0], ge2[0], 0); act2(gv[0][2][1], ge2[1], 0);
  asm volatile("" : "+v"(gv[0][2][0]), "+v"(gv[0][2][1]));
  SB();
  asm volatile("buffer_load_dwordx4 %0, %1, %2 offen offset:2048 lds" :  : "v"(voB[2]), "s"(rW1), "s"(kw_[4]) : "memory"); SB();
  act(gv[1][0][0], ge2[0], a2[1][0], c2[0], 1, 0); act(gv[1][0][1], ge2[1], a2[1][0], c2[0], 1, 1);
  asm volatile("" : "+v"(gv[1][0][0]), "+v"(gv[1][0][1]), "+v"(ge2[0]), "+v"(ge2[1]));
  SB();
  act2(gv[1][0][0], ge2[0], 1); act2(gv[1][0][1], ge2[1], 1);
  asm volatile("" : "+v"(gv[1][0][0]), "+v"(gv[1][0][1]));
  SB();
  act(gv[1][1][0], ge2[0], a2[1][1], c2[1], 1, 0); act(gv[1][1][1], ge2[1], a2[1][1], c2[1], 1, 1);
  asm volatile("" : "+v"(gv[1][1][0]), "+v"(gv[1][1][1]), "+v"(ge2[0]), "+v"(ge2[1]));
  SB();
  act2(gv[1][1][0], ge2[0], 1); act2(gv[1][1][1], ge2[1], 1);
  asm volatile("" : "+v"(gv[1][1][0]), "+v"(gv[1][1][1]));
  SB();
  act(gv[1][2][0], ge2[0], a2[1][2], c2[2], 1, 0); act(gv[1][2][1], ge2[1], a2[1][2], c2[2], 1, 1);
  asm volatile("" : "+v"(gv[1][2][0]), "+v"(gv[1][2][1]), "+v"(ge2[0]), "+v"(ge2[1]));
  SB();
  asm volatile("s_add_u32 m0, %0, 86016" :  : "s"(mW) : "memory", "scc"); SB();
  asm volatile("s_nop 0\n\tbuffer_load_dwordx4 %0, %1, %2 offen lds" :  : "v"(voB[0]), "s"(rW1), "s"(kw_[5]) : "memory"); SB();
  act2(gv[1][2][0], ge2[0], 1); act2(gv[1][2][1], ge2[1], 1);
  asm volatile("" : "+v"(gv[1][2][0]), "+v"(gv[1][2][1]));
  SB();
  asm volatile("buffer_load_dwordx4 %0, %1, %2 offen offset:1024 lds" :  : "v"(voB[1]), "s"(rW1), "s"(kw_[5]) : "memory"); SB();
#pragma unroll
  for (int i = 0; i < 2; ++i) {
    const unsigned p0 = __float_as_uint(gv[i][0][0][0]);
    const auto pa = __builtin_amdgcn_permlane16_swap(p0, p0, false, false);
    const auto pb = __builtin_amdgcn_permlane32_swap(pa[0], pa[0], false, false);
    pv[i] = __uint_as_float(pb[0]);
  }
  asm volatile("" : "+v"(pv[0]), "+v"(pv[1]));
  f32x2 s1[2], s2[2];
  SB();
  asm volatile("buffer_load_dwordx4 %0, %1, %2 offen offset:2048 lds" :  : "v"(voB[2]), "s"(rW1), "s"(kw_[5]) : "memory"); SB();
#pragma unroll
  for (int m = 0; m < 4; ++m) {
    const int i = m & 1, n = 0 + (m >> 1);
    const f32x2 d = gv[i][n >> 1][n & 1] - (f32x2){pv[i], pv[i]};
    s1[i] = n ? s1[i] + d : d; s2[i] = n ? __builtin_elementwise_fma(d, d, s2[i]) : d * d;
  }
  asm volatile("" : "+v"(s1[0]), "+v"(s1[1]), "+v"(s2[0]), "+v"(s2[1]));
  SB();
  asm volatile("s_add_u32 m0, %0, 0" :  : "s"(mW) : "memory", "scc"); SB();
  asm volatile("s_nop 0\n\tbuffer_load_dwordx4 %0, %1, %2 offen lds" :  : "v"(voB[0]), "s"(rW1), "s"(kw_[6]) : "memory"); SB();
#pragma unroll
  for (int m = 0; m < 4; ++m) {
    const int i = m & 1, n = 2 + (m >> 1);
    const f32x2 d = gv[i][n >> 1][n & 1] - (f32x2){pv[i], pv[i]};
    s1[i] = n ? s1[i] + d : d; s2[i] = n ? __builtin_elementwise_fma(d, d, s2[i]) : d * d;
  }
  asm volatile("" : "+v"(s1[0]), "+v"(s1[1]), "+v"(s2[0]), "+v"(s2[1]));
  SB();
  asm volatile("buffer_load_dwordx4 %0, %1, %2 offen offset:1024 lds" :  : "v"(voB[1]), "s"(rW1), "s"(kw_[6]) : "memory"); SB();
#pragma unroll
  for (int m = 0; m < 4; ++m) {
    const int i = m & 1, n = 4 + (m >> 1);
    const f32x2 d = gv[i][n >> 1][n & 1] - (f32x2){pv[i], pv[i]};
    s1[i] = n ? s1[i] + d : d; s2[i] = n ? __builtin_elementwise_fma(d, d, s2[i]) : d * d;
  }
  asm volatile("" : "+v"(s1[0]), "+v"(s1[1]), "+v"(s2[0]), "+v"(s2[1]));
  SB();
  for (int i = 0; i < 2; ++i) { sv[i][0] = s1[i][0] + s1[i][1]; sv[i][1] = s2[i][0] + s2[i][1]; }
  {
    const float t1 = red16(sel32(sv[0][0], sv[1][0])), t2 = red16(sel32(sv[0][1], sv[1][1]));
    const float dl = t1 * (1.0f / 48.0f);
    const u32x4 so = {__float_as_uint((lane < 32 ? pv[0] : pv[1]) + dl), __float_as_uint(fmaxf(t2 - t1 * dl, 0.f)), tg0, tg1};
    SB();
  asm volatile("s_mov_b32 exec_lo, 0xffff\n\ts_mov_b32 exec_hi, 0xffff\n\tbuffer_store_dwordx4 %1, %0, %2, %3 offen offset:0 sc1\n\ts_mov_b64 exec, -1\n\ts_nop 0" :  : "v"(vo), "v"(so), "s"(rWS), "s"(0u) : "memory"); SB();
  }
  ST(6);
  unsigned zp[2][3][2];
#pragma unroll
  for (int n = 0; n < 12; ++n) { zp[n / 6][(n >> 1) % 3][n & 1] = pk2(gv[n / 6][(n >> 1) % 3][n & 1]); asm volatile("" : "+v"(zp[n / 6][(n >> 1) % 3][n & 1])); }
  SB();
  asm volatile("s_waitcnt vmcnt(24)\n\ts_barrier" :  :  : "memory"); SB();
  ST(2);
  asm volatile("ds_read_b64_tr_b16 %0, %1 offset:0" : "=&v"(wt[0]) : "v"(oa) : "memory"); SB();
  asm volatile("ds_read_b64_tr_b16 %0, %1 offset:512" : "=&v"(wt[1]) : "v"(oa) : "memory"); SB();
  asm volatile("ds_read_b64_tr_b16 %0, %1 offset:0" : "=&v"(kp[0]) : "v"(ka) : "memory"); SB();
  asm volatile("ds_read_b64_tr_b16 %0, %1 offset:640" : "=&v"(kc[0]) : "v"(ka) : "memory"); SB();
  asm volatile("ds_read_b64_tr_b16 %0, %1 offset:4096" : "=&v"(vv[0]) : "v"(ka) : "memory"); SB();
  asm volatile("ds_read_b64 %0, %1 offset:0" : "=&v"(sw[0]) : "v"(sa) : "memory"); SB();
  asm volatile("buffer_load_dwordx4 %0, %1, %2 offen offset:2048 lds" :  : "v"(voB[2]), "s"(rW1), "s"(kw_[6]) : "memory"); SB();
  asm volatile("s_add_u32 m0, %0, 131072" :  : "s"(mA) : "memory", "scc"); SB();
  asm volatile("s_nop 0\n\tbuffer_load_dwordx4 %0, %1, %2 offen lds" :  : "v"(voS), "s"(rA), "s"(ao[0]) : "memory"); SB();
  asm volatile("ds_read_b64_tr_b16 %0, %1 offset:32" : "=&v"(kp[1]) : "v"(ka) : "memory"); SB();
  asm volatile("ds_read_b64_tr_b16 %0, %1 offset:672" : "=&v"(kc[1]) : "v"(ka) : "memory"); SB();
  asm volatile("ds_read_b64_tr_b16 %0, %1 offset:4128" : "=&v"(vv[1]) : "v"(ka) : "memory"); SB();
  asm volatile("ds_read_b64 %0, %1 offset:128" : "=&v"(sw[1]) : "v"(sa) : "memory"); SB();
  asm volatile("buffer_load_dwordx4 %0, %1, %2 offen offset:1024 lds" :  : "v"(voS), "s"(rA), "s"(ao[1]) : "memory"); SB();
  asm volatile("ds_read_b64_tr_b16 %0, %1 offset:64" : "=&v"(kp[2]) : "v"(ka) : "memory"); SB();
  asm volatile("ds_read_b64_tr_b16 %0, %1 offset:704" : "=&v"(kc[2]) : "v"(ka) : "memory"); SB();
  asm volatile("ds_read_b64_tr_b16 %0, %1 offset:4160" : "=&v"(vv[2]) : "v"(ka) : "memory"); SB();
  asm volatile("ds_read_b64 %0, %1 offset:256" : "=&v"(sw[2]) : "v"(sa) : "memory"); SB();
  asm volatile("buffer_load_dwordx4 %0, %1, %2 offen offset:2048 lds" :  : "v"(voS), "s"(rA), "s"(ao[2]) : "memory"); SB();
  asm volatile("ds_read_b64_tr_b16 %0, %1 offset:96" : "=&v"(kp[3]) : "v"(ka) : "memory"); SB();
  asm volatile("ds_read_b64_tr_b16 %0, %1 offset:736" : "=&v"(kc[3]) : "v"(ka) : "memory"); SB();
  asm volatile("ds_read_b64_tr_b16 %0, %1 offset:4192" : "=&v"(vv[3]) : "v"(ka) : "memory"); SB();
  asm volatile("ds_read_b64 %0, %1 offset:384" : "=&v"(sw[3]) : "v"(sa) : "memory"); SB();
  asm volatile("ds_read_b64_tr_b16 %0, %1 offset:128" : "=&v"(kp[4]) : "v"(ka) : "memory"); SB();
  asm volatile("ds_read_b64_tr_b16 %0, %1 offset:768" : "=&v"(kc[4]) : "v"(ka) : "memory"); SB();
  asm volatile("ds_read_b64_tr_b16 %0, %1 offset:4224" : "=&v"(vv[4]) : "v"(ka) : "memory"); SB();
  asm volatile("ds_read_b64 %0, %1 offset:512" : "=&v"(sw[4]) : "v"(sa) : "memory"); SB();
  asm volatile("buffer_load_dwordx4 %0, %1, %2 offen offset:3072 lds" :  : "v"(voS), "s"(rA), "s"(ao[3]) : "memory"); SB();
  asm volatile("s_waitcnt lgkmcnt(8)" : "+v"(kp[0]), "+v"(kc[0]), "+v"(vv[0]), "+v"(sw[0]), "+v"(wt[0]), "+v"(wt[1]) :  : "memory"); SB();
  asm volatile("v_alignbit_b32 %4, %14, %14, 16\n\tv_alignbit_b32 %5, %10, %9, 16\n\tv_alignbit_b32 %6, %11, %10, 16\n\tv_alignbit_b32 %7, %13, %13, 16\n\tv_alignbit_b32 %8, %12, %11, 16\n\tv_dot2_f32_bf16 %0, %5, %4, 0\n\tv_dot2_f32_bf16 %1, %10, %4, 0\n\tv_dot2_f32_bf16 %2, %6, %4, 0\n\tv_dot2_f32_bf16 %3, %11, %4, 0\n\tv_dot2_f32_bf16 %0, %6, %7, %0\n\tv_dot2_f32_bf16 %1, %11, %7, %1\n\tv_dot2_f32_bf16 %2, %8, %7, %2\n\tv_dot2_f32_bf16 %3, %12, %7, %3" : "=&v"(kt[0][0]), "=&v"(kt[0][1]), "=&v"(kt[1][0]), "=&v"(kt[1][1]), "=&v"(ku[0]), "=&v"(ku[1]), "=&v"(ku[2]), "=&v"(ku[3]), "=&v"(ku[4]) : "v"(kp[0][0]), "v"(kp[0][1]), "v"(kc[0][0]), "v"(kc[0][1]), "v"(sw[0][0]), "v"(sw[0][1]) : ); SB();
  asm volatile("s_add_u32 m0, %0, 135168" :  : "s"(mA) : "memory", "scc"); SB();
  asm volatile("s_nop 0\n\tbuffer_load_dwordx4 %0, %1, %2 offen lds" :  : "v"(voS), "s"(rA), "s"(ao[4]) : "memory"); SB();
  asm volatile("v_lshlrev_b32 %0, 16, %4\n\tv_and_b32 %1, 0xffff0000, %4\n\tv_lshlrev_b32 %2, 16, %5\n\tv_and_b32 %3, 0xffff0000, %5" : "=&v"(kf[0][0]), "=&v"(kf[0][1]), "=&v"(kf[1][0]), "=&v"(kf[1][1]) : "v"(vv[0][0]), "v"(vv[0][1]) : ); SB();
  kt[0] *= kf[0]; kt[1] *= kf[1]; SB();
  ko[0] = (u32x2){pk2(kt[0]), pk2(kt[1])};
  asm volatile("" : "+v"(ko[0]));
  SB();
  asm volatile("buffer_load_dwordx4 %0, %1, %2 offen offset:1024 lds" :  : "v"(voS), "s"(rA), "s"(ao[5]) : "memory"); SB();
  asm volatile("ds_write_b64 %0, %1 offset:0" :  : "v"(kw), "v"(ko[0]) : "memory"); SB();
  asm volatile("" : "+v"(kp[1]), "+v"(kc[1]), "+v"(vv[1]), "+v"(sw[1]) :  : "memory"); SB();
  asm volatile("v_alignbit_b32 %4, %14, %14, 16\n\tv_alignbit_b32 %5, %10, %9, 16\n\tv_alignbit_b32 %6, %11, %10, 16\n\tv_alignbit_b32 %7, %13, %13, 16\n\tv_alignbit_b32 %8, %12, %11, 16\n\tv_dot2_f32_bf16 %0, %5, %4, 0\n\tv_dot2_f32_bf16 %1, %10, %4, 0\n\tv_dot2_f32_bf16 %2, %6, %4, 0\n\tv_dot2_f32_bf16 %3, %11, %4, 0\n\tv_dot2_f32_bf16 %0, %6, %7, %0\n\tv_dot2_f32_bf16 %1, %11, %7, %1\n\tv_dot2_f32_bf16 %2, %8, %7, %2\n\tv_dot2_f32_bf16 %3, %12, %7, %3" : "=&v"(kt[0][0]), "=&v"(kt[0][1]), "=&v"(kt[1][0]), "=&v"(kt[1][1]), "=&v"(ku[0]), "=&v"(ku[1]), "=&v"(ku[2]), "=&v"(ku[3]), "=&v"(ku[4]) : "v"(kp[1][0]), "v"(kp[1][1]), "v"(kc[1][0]), "v"(kc[1][1]), "v"(sw[1][0]), "v"(sw[1][1]) : ); SB();
  asm volatile("buffer_load_dwordx4 %0, %1, %2 offen offset:2048 lds" :  : "v"(voS), "s"(rA), "s"(ao[6]) : "memory"); SB();
  asm volatile("v_lshlrev_b32 %0, 16, %4\n\tv_and_b32 %1, 0xffff0000, %4\n\tv_lshlrev_b32 %2, 16, %5\n\tv_and_b32 %3, 0xffff0000, %5" : "=&v"(kf[0][0]), "=&v"(kf[0][1]), "=&v"(kf[1][0]), "=&v"(kf[1][1]) : "v"(vv[1][0]), "v"(vv[1][1]) : ); SB();
  kt[0] *= kf[0]; kt[1] *= kf[1]; SB();
  ko[1] = (u32x2){pk2(kt[0]), pk2(kt[1])};
  asm volatile("" : "+v"(ko[1]));
  SB();
  asm volatile("ds_write_b64 %0, %1 offset:2304" :  : "v"(kw), "v"(ko[1]) : "memory"); SB();
  asm volatile("" : "+v"(kp[2]), "+v"(kc[2]), "+v"(vv[2]), "+v"(sw[2]) :  : "memory"); SB();
  asm volatile("v_alignbit_b32 %4, %14, %14, 16\n\tv_alignbit_b32 %5, %10, %9, 16\n\tv_alignbit_b32 %6, %11, %10, 16\n\tv_alignbit_b32 %7, %13, %13, 16\n\tv_alignbit_b32 %8, %12, %11, 16\n\tv_dot2_f32_bf16 %0, %5, %4, 0\n\tv_dot2_f32_bf16 %1, %10, %4, 0\n\tv_dot2_f32_bf16 %2, %6, %4, 0\n\tv_dot2_f32_bf16 %3, %11, %4, 0\n\tv_dot2_f32_bf16 %0, %6, %7, %0\n\tv_dot2_f32_bf16 %1, %11, %7, %1\n\tv_dot2_f32_bf16 %2, %8, %7, %2\n\tv_dot2_f32_bf16 %3, %12, %7, %3" : "=&v"(kt[0][0]), "=&v"(kt[0][1]), "=&v"(kt[1][0]), "=&v"(kt[1][1]), "=&v"(ku[0]), "=&v"(ku[1]), "=&v"(ku[2]), "=&v"(ku[3]), "=&v"(ku[4]) : "v"(kp[2][0]), "v"(kp[2][1]), "v"(kc[2][0]), "v"(kc[2][1]), "v"(sw[2][0]), "v"(sw[2][1]) : ); SB();
  asm volatile("buffer_load_dwordx4 %0, %1, %2 offen offset:3072 lds" :  : "v"(voS), "s"(rA), "s"(ao[7]) : "memory"); SB();
  asm volatile("v_xor_b32 %0, 128, %0" : "+v"(voS) :  : "memory"); SB();
  asm volatile("v_lshlrev_b32 %0, 16, %4\n\tv_and_b32 %1, 0xffff0000, %4\n\tv_lshlrev_b32 %2, 16, %5\n\tv_and_b32 %3, 0xffff0000, %5" : "=&v"(kf[0][0]), "=&v"(kf[0][1]), "=&v"(kf[1][0]), "=&v"(kf[1][1]) : "v"(vv[2][0]), "v"(vv[2][1]) : ); SB();
  kt[0] *= kf[0]; kt[1] *= kf[1]; SB();
  ko[2] = (u32x2){pk2(kt[0]), pk2(kt[1])};
  asm volatile("" : "+v"(ko[2]));
  SB();
  asm volatile("ds_write_b64 %0, %1 offset:4608" :  : "v"(kw), "v"(ko[2]) : "memory"); SB();
  asm volatile("s_waitcnt lgkmcnt(2)" : "+v"(kp[3]), "+v"(kc[3]), "+v"(vv[3]), "+v"(sw[3]) :  : "memory"); SB();
  asm volatile("v_alignbit_b32 %4, %14, %14, 16\n\tv_alignbit_b32 %5, %10, %9, 16\n\tv_alignbit_b32 %6, %11, %10, 16\n\tv_alignbit_b32 %7, %13, %13, 16\n\tv_alignbit_b32 %8, %12, %11, 16\n\tv_dot2_f32_bf16 %0, %5, %4, 0\n\tv_dot2_f32_bf16 %1, %10, %4, 0\n\tv_dot2_f32_bf16 %2, %6, %4, 0\n\tv_dot2_f32_bf16 %3, %11, %4, 0\n\tv_dot2_f32_bf16 %0, %6, %7, %0\n\tv_dot2_f32_bf16 %1, %11, %7, %1\n\tv_dot2_f32_bf16 %2, %8, %7, %2\n\tv_dot2_f32_bf16 %3, %12, %7, %3" : "=&v"(kt[0][0]), "=&v"(kt[0][1]), "=&v"(kt[1][0]), "=&v"(kt[1][1]), "=&v"(ku[0]), "=&v"(ku[1]), "=&v"(ku[2]), "=&v"(ku[3]), "=&v"(ku[4]) : "v"(kp[3][0]), "v"(kp[3][1]), "v"(kc[3][0]), "v"(kc[3][1]), "v"(sw[3][0]), "v"(sw[3][1]) : ); SB();
  asm volatile("s_add_u32 m0, %0, 98304" :  : "s"(mA) : "memory", "scc"); SB();
  asm volatile("s_nop 0\n\tbuffer_load_dwordx4 %0, %1, %2 offen lds" :  : "v"(voS), "s"(rA), "s"(ao[0]) : "memory"); SB();
  asm volatile("buffer_load_dwordx4 %0, %1, %2 offen offset:1024 lds" :  : "v"(voS), "s"(rA), "s"(ao[1]) : "memory"); SB();
  asm volatile("v_lshlrev_b32 %0, 16, %4\n\tv_and_b32 %1, 0xffff0000, %4\n\tv_lshlrev_b32 %2, 16, %5\n\tv_and_b32 %3, 0xffff0000, %5" : "=&v"(kf[0][0]), "=&v"(kf[0][1]), "=&v"(kf[1][0]), "=&v"(kf[1][1]) : "v"(vv[3][0]), "v"(vv[3][1]) : ); SB();
  kt[0] *= kf[0]; kt[1] *= kf[1]; SB();
  ko[3] = (u32x2){pk2(kt[0]), pk2(kt[1])};
  asm volatile("" : "+v"(ko[3]));
  SB();
  asm volatile("buffer_load_dwordx4 %0, %1, %2 offen offset:2048 lds" :  : "v"(voS), "s"(rA), "s"(ao[2]) : "memory"); SB();
  asm volatile("ds_write_b64 %0, %1 offset:6912" :  : "v"(kw), "v"(ko[3]) : "memory"); SB();
  asm volatile("" : "+v"(kp[4]), "+v"(kc[4]), "+v"(vv[4]), "+v"(sw[4]) :  : "memory"); SB();
  asm volatile("v_alignbit_b32 %4, %14, %14, 16\n\tv_alignbit_b32 %5, %10, %9, 16\n\tv_alignbit_b32 %6, %11, %10, 16\n\tv_alignbit_b32 %7, %13, %13, 16\n\tv_alignbit_b32 %8, %12, %11, 16\n\tv_dot2_f32_bf16 %0, %5, %4, 0\n\tv_dot2_f32_bf16 %1, %10, %4, 0\n\tv_dot2_f32_bf16 %2, %6, %4, 0\n\tv_dot2_f32_bf16 %3, %11, %4, 0\n\tv_dot2_f32_bf16 %0, %6, %7, %0\n\tv_dot2_f32_bf16 %1, %11, %7, %1\n\tv_dot2_f32_bf16 %2, %8, %7, %2\n\tv_dot2_f32_bf16 %3, %12, %7, %3" : "=&v"(kt[0][0]), "=&v"(kt[0][1]), "=&v"(kt[1][0]), "=&v"(kt[1][1]), "=&v"(ku[0]), "=&v"(ku[1]), "=&v"(ku[2]), "=&v"(ku[3]), "=&v"(ku[4]) : "v"(kp[4][0]), "v"(kp[4][1]), "v"(kc[4][0]), "v"(kc[4][1]), "v"(sw[4][0]), "v"(sw[4][1]) : ); SB();
  asm volatile("v_lshlrev_b32 %0, 16, %4\n\tv_and_b32 %1, 0xffff0000, %4\n\tv_lshlrev_b32 %2, 16, %5\n\tv_and_b32 %3, 0xffff0000, %5" : "=&v"(kf[0][0]), "=&v"(kf[0][1]), "=&v"(kf[1][0]), "=&v"(kf[1][1]) : "v"(vv[4][0]), "v"(vv[4][1]) : ); SB();
  kt[0] *= kf[0]; kt[1] *= kf[1]; SB();
  ko[4] = (u32x2){pk2(kt[0]), pk2(kt[1])};
  asm volatile("" : "+v"(ko[4]));
  SB();
  asm volatile("ds_write_b64 %0, %1 offset:9216" :  : "v"(kw), "v"(ko[4]) : "memory"); SB();
  ST(16);
  asm volatile("s_waitcnt vmcnt(27) lgkmcnt(0)\n\ts_barrier" :  :  : "memory"); SB();
  asm volatile("ds_read_b128 %0, %1 offset:0" : "=&v"(kq[0]) : "v"(kr) : "memory"); SB();
  asm volatile("ds_read_b128 %0, %1 offset:1152" : "=&v"(kq[1]) : "v"(kr) : "memory"); SB();
  asm volatile("ds_read_b128 %0, %1 offset:2304" : "=&v"(kq[2]) : "v"(kr) : "memory"); SB();
  asm volatile("ds_read_b64_tr_b16 v[184:185], %0 offset:24576" :  : "v"(aW[0][0][0]) : "memory"); SB();
  asm volatile("ds_read_b64_tr_b16 v[186:187], %0 offset:25344" :  : "v"(aW[0][0][0]) : "memory"); SB();
  asm volatile("ds_read_b64_tr_b16 v[188:189], %0 offset:24576" :  : "v"(aW[0][0][1]) : "memory"); SB();
  asm volatile("ds_read_b64_tr_b16 v[190:191], %0 offset:25344" :  : "v"(aW[0][0][1]) : "memory"); SB();
  asm volatile("ds_read_b64_tr_b16 v[192:193], %0 offset:24576" :  : "v"(aW[0][0][2]) : "memory"); SB();
  asm volatile("ds_read_b64_tr_b16 v[194:195], %0 offset:25344" :  : "v"(aW[0][0][2]) : "memory"); SB();
  ST(7);
  asm volatile("s_waitcnt lgkmcnt(6)" : "+v"(kq[0]), "+v"(kq[1]), "+v"(kq[2]) :  : "memory"); SB();
  unsigned kz[3] = {kq[0][3], kq[1][3] | (w + 1) >> 2, kq[2][3] | (w + 1) >> 2};
  asm volatile("" : "+v"(kz[0]), "+v"(kz[1]), "+v"(kz[2])); SB();
  asm volatile("buffer_load_dwordx4 v[196:199], %0, %1, %2 offen offset:0 sc1" :  : "v"(vS), "s"(rWS), "s"(0u) : "memory"); SB();
  asm volatile("buffer_load_dwordx4 v[200:203], %0, %1, %2 offen offset:1024 sc1" :  : "v"(vS), "s"(rWS), "s"(0u) : "memory"); SB();
  asm volatile("s_waitcnt vmcnt(11)" :  :  : "memory"); SB();
  asm volatile("ds_read_b128 a[160:163], %0 offset:32768" :  : "v"(aA[0]) : "memory"); SB();
  asm volatile("s_waitcnt lgkmcnt(2)\n\tv_mfma_f32_16x16x32_bf16 %0, v[184:187], a[0:3], 0" : "=&v"(a1[0][0]) :  : ); SB();
  asm volatile("ds_read_b64_tr_b16 v[160:161], %0 offset:24576" :  : "v"(aW[0][1][0]) : "memory"); SB();
  asm volatile("v_mfma_f32_16x16x32_bf16 %0, v[184:187], a[4:7], 0" : "=&v"(a1[1][0]) :  : ); SB();
  asm volatile("ds_read_b64_tr_b16 v[162:163], %0 offset:25344" :  : "v"(aW[0][1][0]) : "memory"); SB();
  asm volatile("v_mfma_f32_16x16x32_bf16 %0, v[184:187], %1, 0" : "=&v"(c1[0]) : "v"(ones) : ); SB();
  asm volatile("ds_read_b64_tr_b16 v[164:165], %0 offset:24576" :  : "v"(aW[0][1][1]) : "memory"); SB();
  asm volatile("v_mfma_f32_16x16x32_bf16 %0, v[188:191], a[0:3], 0" : "=&v"(a1[0][1]) :  : ); SB();
  asm volatile("ds_read_b64_tr_b16 v[166:167], %0 offset:25344" :  : "v"(aW[0][1][1]) : "memory"); SB();
  asm volatile("v_mfma_f32_16x16x32_bf16 %0, v[188:191], a[4:7], 0" : "=&v"(a1[1][1]) :  : ); SB();
  asm volatile("ds_read_b64_tr_b16 v[168:169], %0 offset:24576" :  : "v"(aW[0][1][2]) : "memory"); SB();
  asm volatile("v_mfma_f32_16x16x32_bf16 %0, v[188:191], %1, 0" : "=&v"(c1[1]) : "v"(ones) : ); SB();
  asm volatile("ds_read_b64_tr_b16 v[170:171], %0 offset:25344" :  : "v"(aW[0][1][2]) : "memory"); SB();
  asm volatile("s_waitcnt lgkmcnt(7)\n\tv_mfma_f32_16x16x32_bf16 %0, v[192:195], a[0:3], 0" : "=&v"(a1[0][2]) :  : ); SB();
  asm volatile("ds_read_b64_tr_b16 v[172:173], %0 offset:36864" :  : "v"(aW[0][0][0]) : "memory"); SB();
  asm volatile("buffer_load_dwordx4 v[204:207], %0, %1, %2 offen offset:2048 sc1" :  : "v"(vS), "s"(rWS), "s"(0u) : "memory"); SB();
  asm volatile("v_mfma_f32_16x16x32_bf16 %0, v[192:195], a[4:7], 0" : "=&v"(a1[1][2]) :  : ); SB();
  asm volatile("ds_read_b64_tr_b16 v[174:175], %0 offset:37632" :  : "v"(aW[0][0][0]) : "memory"); SB();
  asm volatile("v_mfma_f32_16x16x32_bf16 %0, v[192:195], %1, 0" : "=&v"(c1[2]) : "v"(ones) : ); SB();
  asm volatile("ds_read_b64_tr_b16 v[176:177], %0 offset:36864" :  : "v"(aW[0][0][1]) : "memory"); SB();
  asm volatile("s_waitcnt lgkmcnt(7)\n\tv_mfma_f32_16x16x32_bf16 %0, v[160:163], a[8:11], %0" : "+v"(a1[0][0]) :  : ); SB();
  asm volatile("ds_read_b64_tr_b16 v[178:179], %0 offset:37632" :  : "v"(aW[0][0][1]) : "memory"); SB();
  asm volatile("buffer_load_dwordx4 v[208:211], %0, %1, %2 offen offset:3072 sc1" :  : "v"(vS), "s"(rWS), "s"(0u) : "memory"); SB();
  asm volatile("v_mfma_f32_16x16x32_bf16 %0, v[160:163], a[12:15], %0" : "+v"(a1[1][0]) :  : ); SB();
  asm volatile("ds_read_b64_tr_b16 v[180:181], %0 offset:36864" :  : "v"(aW[0][0][2]) : "memory"); SB();
  asm volatile("s_waitcnt lgkmcnt(7)\n\tv_mfma_f32_16x16x32_bf16 %0, v[164:167], a[8:11], %0" : "+v"(a1[0][1]) :  : ); SB();
  asm volatile("ds_read_b64_tr_b16 v[182:183], %0 offset:37632" :  : "v"(aW[0][0][2]) : "memory"); SB();
  asm volatile("v_mfma_f32_16x16x32_bf16 %0, v[164:167], a[12:15], %0" : "+v"(a1[1][1]) :  : ); SB();
  asm volatile("ds_read_b64_tr_b16 v[184:185], %0 offset:36864" :  : "v"(aW[0][1][0]) : "memory"); SB();
  asm volatile("s_waitcnt lgkmcnt(7)\n\tv_mfma_f32_16x16x32_bf16 %0, v[168:171], a[8:11], %0" : "+v"(a1[0][2]) :  : ); SB();
  asm volatile("ds_read_b64_tr_b16 v[186:187], %0 offset:37632" :  : "v"(aW[0][1][0]) : "memory"); SB();
  asm volatile("v_mfma_f32_16x16x32_bf16 %0, v[168:171], a[12:15], %0" : "+v"(a1[1][2]) :  : ); SB();
  asm volatile("ds_read_b64_tr_b16 v[188:189], %0 offset:36864" :  : "v"(aW[0][1][1]) : "memory"); SB();
  asm volatile("buffer_load_dwordx4 v[212:215], %0, %1, %2 offen offset:0 sc1" :  : "v"(vS), "s"(rWS), "s"(4096u) : "memory"); SB();
  asm volatile("s_waitcnt lgkmcnt(7)\n\tv_mfma_f32_16x16x32_bf16 %0, v[172:175], a[16:19], %0" : "+v"(a1[0][0]) :  : ); SB();
  asm volatile("ds_read_b64_tr_b16 v[190:191], %0 offset:37632" :  : "v"(aW[0][1][1]) : "memory"); SB();
  asm volatile("v_mfma_f32_16x16x32_bf16 %0, v[172:175], a[20:23], %0" : "+v"(a1[1][0]) :  : ); SB();
  asm volatile("ds_read_b64_tr_b16 v[192:193], %0 offset:36864" :  : "v"(aW[0][1][2]) : "memory"); SB();
  asm volatile("v_mfma_f32_16x16x32_bf16 %0, v[172:175], %1, %0" : "+v"(c1[0]) : "v"(ones) : ); SB();
  asm volatile("ds_read_b64_tr_b16 v[194:195], %0 offset:37632" :  : "v"(aW[0][1][2]) : "memory"); SB();
  asm volatile("s_waitcnt lgkmcnt(8)\n\tv_mfma_f32_16x16x32_bf16 %0, v[176:179], a[16:19], %0" : "+v"(a1[0][1]) :  : ); SB();
  asm volatile("s_barrier" :  :  : "memory"); SB();
  asm volatile("ds_read_b64_tr_b16 v[160:161], %0 offset:0" :  : "v"(aW[1][0][0]) : "memory"); SB();
  asm volatile("v_mfma_f32_16x16x32_bf16 %0, v[176:179], a[20:23], %0" : "+v"(a1[1][1]) :  : ); SB();
  asm volatile("ds_read_b64_tr_b16 v[162:163], %0 offset:768" :  : "v"(aW[1][0][0]) : "memory"); SB();
  asm volatile("buffer_load_dwordx4 v[216:219], %0, %1, %2 offen offset:1024 sc1" :  : "v"(vS), "s"(rWS), "s"(4096u) : "memory"); SB();
  asm volatile("v_mfma_f32_16x16x32_bf16 %0, v[176:179], %1, %0" : "+v"(c1[1]) : "v"(ones) : ); SB();
  asm volatile("ds_read_b64_tr_b16 v[164:165], %0 offset:0" :  : "v"(aW[1][0][1]) : "memory"); SB();
  asm volatile("s_waitcnt lgkmcnt(7)\n\tv_mfma_f32_16x16x32_bf16 %0, v[180:183], a[16:19], %0" : "+v"(a1[0][2]) :  : ); SB();
  asm volatile("ds_read_b64_tr_b16 v[166:167], %0 offset:768" :  : "v"(aW[1][0][1]) : "memory"); SB();
  asm volatile("v_mfma_f32_16x16x32_bf16 %0, v[180:183], a[20:23], %0" : "+v"(a1[1][2]) :  : ); SB();
  asm volatile("ds_read_b64_tr_b16 v[168:169], %0 offset:0" :  : "v"(aW[1][0][2]) : "memory"); SB();
  asm volatile("v_mfma_f32_16x16x32_bf16 %0, v[180:183], %1, %0" : "+v"(c1[2]) : "v"(ones) : ); SB();
  asm volatile("ds_read_b64_tr_b16 v[170:171], %0 offset:768" :  : "v"(aW[1][0][2]) : "memory"); SB();
  asm volatile("buffer_load_dwordx4 v[220:223], %0, %1, %2 offen offset:2048 sc1" :  : "v"(vS), "s"(rWS), "s"(4096u) : "memory"); SB();
  asm volatile("v_mfma_f32_16x16x32_bf16 %0, v[184:187], a[24:27], %0" : "+v"(a1[0][0]) :  : ); SB();
  asm volatile("ds_read_b64_tr_b16 v[172:173], %0 offset:0" :  : "v"(aW[1][1][0]) : "memory"); SB();
  asm volatile("v_mfma_f32_16x16x32_bf16 %0, v[184:187], a[28:31], %0" : "+v"(a1[1][0]) :  : ); SB();
  asm volatile("ds_read_b64_tr_b16 v[174:175], %0 offset:768" :  : "v"(aW[1][1][0]) : "memory"); SB();
  asm volatile("s_waitcnt lgkmcnt(8)\n\tv_mfma_f32_16x16x32_bf16 %0, v[188:191], a[24:27], %0" : "+v"(a1[0][1]) :  : ); SB();
  asm volatile("ds_read_b64_tr_b16 v[176:177], %0 offset:0" :  : "v"(aW[1][1][1]) : "memory"); SB();
  asm volatile("buffer_load_dwordx4 v[224:227], %0, %1, %2 offen offset:3072 sc1" :  : "v"(vS), "s"(rWS), "s"(4096u) : "memory"); SB();
  asm volatile("v_mfma_f32_16x16x32_bf16 %0, v[188:191], a[28:31], %0" : "+v"(a1[1][1]) :  : ); SB();
  asm volatile("ds_read_b64_tr_b16 v[178:179], %0 offset:768" :  : "v"(aW[1][1][1]) : "memory"); SB();
  asm volatile("v_mfma_f32_16x16x32_bf16 %0, v[192:195], a[24:27], %0" : "+v"(a1[0][2]) :  : ); SB();
  asm volatile("ds_read_b64_tr_b16 v[180:181], %0 offset:0" :  : "v"(aW[1][1][2]) : "memory"); SB();
  asm volatile("v_mfma_f32_16x16x32_bf16 %0, v[192:195], a[28:31], %0" : "+v"(a1[1][2]) :  : ); SB();
  asm volatile("ds_read_b64_tr_b16 v[182:183], %0 offset:768" :  : "v"(aW[1][1][2]) : "memory"); SB();
  asm volatile("buffer_store_dwordx4 %1, %0, %2, %3 offen offset:0 sc1\n\ts_nop 1" :  : "v"(vKV), "v"(kq[0]), "s"(rWS), "s"(0u) : "memory"); SB();
  ST(8);
  asm volatile("ds_write_b64 %0, %1 offset:0" :  : "v"(zwa[0]), "v"(((u32x2){zp[0][0][0], zp[0][0][1]})) : "memory"); SB();
  asm volatile("ds_write_b64 %0, %1 offset:32" :  : "v"(zwa[0]), "v"(((u32x2){zp[0][1][0], zp[0][1][1]})) : "memory"); SB();
  asm volatile("ds_write_b64 %0, %1 offset:64" :  : "v"(zwa[0]), "v"(((u32x2){zp[0][2][0], zp[0][2][1]})) : "memory"); SB();
  asm volatile("buffer_store_dwordx4 %1, %0, %2, %3 offen offset:0 sc1\n\ts_nop 1" :  : "v"(vKV), "v"(kq[1]), "s"(rWS), "s"(w3k[0]) : "memory"); SB();
  asm volatile("buffer_store_dwordx4 %1, %0, %2, %3 offen offset:0 sc1\n\ts_nop 1" :  : "v"(vKV), "v"(kq[2]), "s"(rWS), "s"(w3k[1]) : "memory"); SB();
  asm volatile("ds_read_b128 a[176:179], %0 offset:32768" :  : "v"(aA[1]) : "memory"); SB();
  asm volatile("s_waitcnt vmcnt(18)" :  :  : "memory"); SB();
  asm volatile("ds_read_b128 a[164:167], %0 offset:34816" :  : "v"(aA[0]) : "memory"); SB();
  asm volatile("ds_read_b128 a[180:183], %0 offset:34816" :  : "v"(aA[1]) : "memory"); SB();
  asm volatile("ds_write_b64 %0, %1 offset:0" :  : "v"(zwa[1]), "v"(((u32x2){zp[1][0][0], zp[1][0][1]})) : "memory"); SB();
  asm volatile("ds_write_b64 %0, %1 offset:32" :  : "v"(zwa[1]), "v"(((u32x2){zp[1][1][0], zp[1][1][1]})) : "memory"); SB();
  asm volatile("ds_write_b64 %0, %1 offset:64" :  : "v"(zwa[1]), "v"(((u32x2){zp[1][2][0], zp[1][2][1]})) : "memory"); SB();
  asm volatile("buffer_store_dwordx4 %1, %0, %2, %3 offen offset:0 sc1\n\ts_nop 1" :  : "v"(vWO), "v"(((u32x4){wt[0][0], wt[0][1], wt[1][0], wt[1][1]})), "s"(rWS), "s"(0u) : "memory"); SB();
  asm volatile("s_waitcnt vmcnt(17)" :  :  : "memory"); SB();
  asm volatile("ds_read_b128 a[168:171], %0 offset:36864" :  : "v"(aA[0]) : "memory"); SB();
  asm volatile("ds_read_b128 a[184:187], %0 offset:36864" :  : "v"(aA[1]) : "memory"); SB();
  asm volatile("s_waitcnt vmcnt(15)" :  :  : "memory"); SB();
  asm volatile("ds_read_b128 a[172:175], %0 offset:38912" :  : "v"(aA[0]) : "memory"); SB();
  asm volatile("ds_read_b64_tr_b16 %0, %1 offset:0" : "=&v"(zt[0][0]) : "v"(zra) : "memory"); SB();
  asm volatile("ds_read_b64_tr_b16 %0, %1 offset:416" : "=&v"(zt[0][1]) : "v"(zra) : "memory"); SB();
  asm volatile("ds_read_b64_tr_b16 %0, %1 offset:32" : "=&v"(zt[1][0]) : "v"(zra) : "memory"); SB();
  asm volatile("ds_read_b64_tr_b16 %0, %1 offset:448" : "=&v"(zt[1][1]) : "v"(zra) : "memory"); SB();
  asm volatile("ds_read_b64_tr_b16 %0, %1 offset:64" : "=&v"(zt[2][0]) : "v"(zra) : "memory"); SB();
  asm volatile("ds_read_b64_tr_b16 %0, %1 offset:480" : "=&v"(zt[2][1]) : "v"(zra) : "memory"); SB();
  asm volatile("s_waitcnt lgkmcnt(0)" : "+v"(zt[0][0]), "+v"(zt[0][1]), "+v"(zt[1][0]), "+v"(zt[1][1]), "+v"(zt[2][0]), "+v"(zt[2][1]) :  : "memory"); SB();
  asm volatile("ds_read_b128 a[188:191], %0 offset:38912" :  : "v"(aA[1]) : "memory"); SB();
  asm volatile("s_waitcnt vmcnt(13)" :  :  : "memory"); SB();
  asm volatile("ds_read_b128 a[192:195], %0 offset:0" :  : "v"(aA[0]) : "memory"); SB();
  asm volatile("ds_read_b128 a[208:211], %0 offset:0" :  : "v"(aA[1]) : "memory"); SB();
  asm volatile("s_add_u32 m0, %0, 12288" :  : "s"(mW) : "memory", "scc"); SB();
  asm volatile("s_nop 0\n\tbuffer_load_dwordx4 %0, %1, %2 offen lds" :  : "v"(voB[0]), "s"(rW1), "s"(kw_[7]) : "memory"); SB();
  asm volatile("buffer_load_dwordx4 %0, %1, %2 offen offset:1024 lds" :  : "v"(voB[1]), "s"(rW1), "s"(kw_[7]) : "memory"); SB();
  asm volatile("buffer_load_dwordx4 %0, %1, %2 offen offset:2048 lds" :  : "v"(voB[2]), "s"(rW1), "s"(kw_[7]) : "memory"); SB();
  asm volatile("s_add_u32 m0, %0, 24576" :  : "s"(mW) : "memory", "scc"); SB();
  asm volatile("s_nop 0\n\tbuffer_load_dwordx4 %0, %1, %2 offen lds" :  : "v"(voB[0]), "s"(rW1), "s"(kw_[8]) : "memory"); SB();
  asm volatile("buffer_load_dwordx4 %0, %1, %2 offen offset:1024 lds" :  : "v"(voB[1]), "s"(rW1), "s"(kw_[8]) : "memory"); SB();
  asm volatile("buffer_load_dwordx4 %0, %1, %2 offen offset:2048 lds" :  : "v"(voB[2]), "s"(rW1), "s"(kw_[8]) : "memory"); SB();
  asm volatile("s_waitcnt vmcnt(10)\n\t.Lc_%=:\n\tv_cmpx_eq_u64 vcc, %1, v[198:199]\n\tv_cmpx_eq_u64 vcc, %1, v[202:203]\n\tv_cmpx_eq_u64 vcc, %1, v[206:207]\n\tv_cmpx_eq_u64 vcc, %1, v[210:211]\n\tv_cmpx_eq_u64 vcc, %1, v[214:215]\n\tv_cmpx_eq_u64 vcc, %1, v[218:219]\n\tv_cmpx_eq_u64 vcc, %1, v[222:223]\n\tv_cmpx_eq_u64 vcc, %1, v[226:227]\n\ts_cmp_eq_u64 vcc, -1\n\ts_mov_b64 exec, -1\n\ts_cbranch_scc0 .Lr_%=\n\t.Ld_%=:\n\t.subsection 1\n\t.Lr_%=:\n\tv_add_u32 %0, -1, %0\n\ts_nop 1\n\tv_readfirstlane_b32 vcc_hi, %0\n\ts_nop 3\n\ts_cmp_lt_i32 vcc_hi, 1\n\ts_cbranch_scc1 .Ld_%=\n\ts_sleep 2\n\ts_movk_i32 vcc_lo, 0x1000\n\tbuffer_load_dwordx4 v[196:199], %2, %3, 0 offen offset:0 sc1\n\tbuffer_load_dwordx4 v[200:203], %2, %3, 0 offen offset:1024 sc1\n\tbuffer_load_dwordx4 v[204:207], %2, %3, 0 offen offset:2048 sc1\n\tbuffer_load_dwordx4 v[208:211], %2, %3, 0 offen offset:3072 sc1\n\tbuffer_load_dwordx4 v[212:215], %2, %3, vcc_lo offen offset:0 sc1\n\tbuffer_load_dwordx4 v[216:219], %2, %3, vcc_lo offen offset:1024 sc1\n\tbuffer_load_dwordx4 v[220:223], %2, %3, vcc_lo offen offset:2048 sc1\n\tbuffer_load_dwordx4 v[224:227], %2, %3, vcc_lo offen offset:3072 sc1\n\ts_waitcnt vmcnt(0)\n\ts_branch .Lc_%=\n\t.subsection 0" : "+v"(tmo) : "s"(tg), "v"(vS), "s"(rWS) : "memory", "vcc", "scc"); SB();
  ST(9);
  {
    f32x2 s4[4], dm[4];
  asm volatile("v_pk_add_f32 %0, v[196:197], v[200:201]\n\tv_pk_add_f32 %1, v[204:205], v[208:209]\n\tv_pk_add_f32 %2, v[212:213], v[216:217]\n\tv_pk_add_f32 %3, v[220:221], v[224:225]" : "=&v"(s4[0]), "=&v"(s4[1]), "=&v"(s4[2]), "=&v"(s4[3]) :  : ); SB();
    const f32x2 sm = (s4[0] + s4[1]) + (s4[2] + s4[3]);
    const float mean = (sm[0] + dppf<0x128>(sm[0])) * (1.0f / 16.0f);
  asm volatile("v_sub_f32 %0, v196, %8\n\tv_sub_f32 %1, v200, %8\n\tv_sub_f32 %2, v204, %8\n\tv_sub_f32 %3, v208, %8\n\tv_sub_f32 %4, v212, %8\n\tv_sub_f32 %5, v216, %8\n\tv_sub_f32 %6, v220, %8\n\tv_sub_f32 %7, v224, %8" : "=&v"(dm[0][0]), "=&v"(dm[0][1]), "=&v"(dm[1][0]), "=&v"(dm[1][1]), "=&v"(dm[2][0]), "=&v"(dm[2][1]), "=&v"(dm[3][0]), "=&v"(dm[3][1]) : "v"(mean) : ); SB();
    const f32x2 dq = __builtin_elementwise_fma(dm[0], dm[0], dm[1] * dm[1]) + __builtin_elementwise_fma(dm[2], dm[2], dm[3] * dm[3]);
    float qs = __builtin_fmaf(dq[0] + dq[1], 48.0f, sm[1]);
    qs += dppf<0x128>(qs);
    float r = __builtin_amdgcn_rcpf(rbf(__builtin_amdgcn_sqrtf(rbf(rbf(qs * (1.0f / 768.0f)) + 1e-5f))));
    float mr = -rbf(mean) * r;
    asm volatile("" : "+v"(r), "+v"(mr)); SB();
  asm volatile("s_add_u32 m0, %0, 98304" :  : "s"(mA) : "memory", "scc"); SB();
  asm volatile("s_nop 0\n\tbuffer_load_dwordx4 %0, %1, %2 offen offset:3072 lds" :  : "v"(voS), "s"(rA), "s"(ao[3]) : "memory"); SB();
  asm volatile("s_add_u32 m0, %0, 102400" :  : "s"(mA) : "memory", "scc"); SB();
  asm volatile("s_nop 0\n\tbuffer_load_dwordx4 %0, %1, %2 offen lds" :  : "v"(voS), "s"(rA), "s"(ao[4]) : "memory"); SB();
    const f32x2 re[4] = {{dppf<0x150>(r), dppf<0x151>(r)}, {dppf<0x152>(r), dppf<0x153>(r)}, {dppf<0x154>(r), dppf<0x155>(r)}, {dppf<0x156>(r), dppf<0x157>(r)}};
    const f32x2 me[4] = {{dppf<0x150>(mr), dppf<0x151>(mr)}, {dppf<0x152>(mr), dppf<0x153>(mr)}, {dppf<0x154>(mr), dppf<0x155>(mr)}, {dppf<0x156>(mr), dppf<0x157>(mr)}};
    u32x4 zo[3];
#pragma unroll
    for (int e = 0; e < 4; ++e) zo[0][e] = pk2(__builtin_elementwise_fma(unpk(zt[0][e >> 1][e & 1]), re[e], me[e]));
    SB();
  asm volatile("buffer_store_dwordx4 %1, %0, %2, %3 offen offset:0 sc1\n\ts_nop 1" :  : "v"(vZ_), "v"(zo[0]), "s"(rWS), "s"(0u) : "memory"); SB();
#pragma unroll
    for (int e = 0; e < 4; ++e) zo[1][e] = pk2(__builtin_elementwise_fma(unpk(zt[1][e >> 1][e & 1]), re[e], me[e]));
    SB();
  asm volatile("buffer_store_dwordx4 %1, %0, %2, %3 offen offset:1024 sc1\n\ts_nop 1" :  : "v"(vZ_), "v"(zo[1]), "s"(rWS), "s"(0u) : "memory"); SB();
#pragma unroll
    for (int e = 0; e < 4; ++e) zo[2][e] = pk2(__builtin_elementwise_fma(unpk(zt[2][e >> 1][e & 1]), re[e], me[e]));
    SB();
  asm volatile("buffer_store_dwordx4 %1, %0, %2, %3 offen offset:2048 sc1\n\ts_nop 1" :  : "v"(vZ_), "v"(zo[2]), "s"(rWS), "s"(0u) : "memory"); SB();
  }
  ST(10);
  asm volatile("v_mfma_f32_16x16x32_bf16 %0, v[160:163], a[32:35], %0" : "+v"(a1[0][0]) :  : ); SB();
  asm volatile("ds_read_b64_tr_b16 v[184:185], %0 offset:12288" :  : "v"(aW[1][0][0]) : "memory"); SB();
  asm volatile("v_mfma_f32_16x16x32_bf16 %0, v[160:163], a[36:39], %0" : "+v"(a1[1][0]) :  : ); SB();
  asm volatile("ds_read_b64_tr_b16 v[186:187], %0 offset:13056" :  : "v"(aW[1][0][0]) : "memory"); SB();
  asm volatile("v_mfma_f32_16x16x32_bf16 %0, v[160:163], %1, %0" : "+v"(c1[0]) : "v"(ones) : ); SB();
  asm volatile("ds_read_b64_tr_b16 v[188:189], %0 offset:12288" :  : "v"(aW[1][0][1]) : "memory"); SB();
  asm volatile("v_mfma_f32_16x16x32_bf16 %0, v[164:167], a[32:35], %0" : "+v"(a1[0][1]) :  : ); SB();
  asm volatile("ds_read_b64_tr_b16 v[190:191], %0 offset:13056" :  : "v"(aW[1][0][1]) : "memory"); SB();
  asm volatile("v_mfma_f32_16x16x32_bf16 %0, v[164:167], a[36:39], %0" : "+v"(a1[1][1]) :  : ); SB();
  asm volatile("ds_read_b64_tr_b16 v[192:193], %0 offset:12288" :  : "v"(aW[1][0][2]) : "memory"); SB();
  asm volatile("v_mfma_f32_16x16x32_bf16 %0, v[164:167], %1, %0" : "+v"(c1[1]) : "v"(ones) : ); SB();
  asm volatile("ds_read_b64_tr_b16 v[194:195], %0 offset:13056" :  : "v"(aW[1][0][2]) : "memory"); SB();
  asm volatile("v_mfma_f32_16x16x32_bf16 %0, v[168:171], a[32:35], %0" : "+v"(a1[0][2]) :  : ); SB();
  ca = lb + 57344 + w * 192 + g4 * 16; asm volatile("" : "+v"(ca)); SB();
  asm volatile("v_mfma_f32_16x16x32_bf16 %0, v[168:171], a[36:39], %0" : "+v"(a1[1][2]) :  : ); SB();
  cr = lb + 57344 + (w ^ 2) * 192 + g4 * 16; asm volatile("" : "+v"(cr)); SB();
  asm volatile("v_mfma_f32_16x16x32_bf16 %0, v[168:171], %1, %0" : "+v"(c1[2]) : "v"(ones) : ); SB();
  asm volatile("buffer_load_dwordx4 %0, %1, %2 offen offset:1024 lds" :  : "v"(voS), "s"(rA), "s"(ao[5]) : "memory"); SB();
  asm volatile("v_mfma_f32_16x16x32_bf16 %0, v[172:175], a[40:43], %0" : "+v"(a1[0][0]) :  : ); SB();
  asm volatile("ds_read_b64_tr_b16 v[160:161], %0 offset:12288" :  : "v"(aW[1][1][0]) : "memory"); SB();
  asm volatile("s_waitcnt vmcnt(12)\n\ts_mov_b64 exec, 1\n\tbuffer_store_dwordx2 %0, off, %1, %2 sc1\n\ts_mov_b64 exec, -1" :  : "v"(((u32x2){tg0, tg1})), "s"(rWS), "s"(sFK) : "memory"); SB();
  asm volatile("v_mfma_f32_16x16x32_bf16 %0, v[172:175], a[44:47], %0" : "+v"(a1[1][0]) :  : ); SB();
  asm volatile("ds_read_b64_tr_b16 v[162:163], %0 offset:13056" :  : "v"(aW[1][1][0]) : "memory"); SB();
  asm volatile("v_lshlrev_b32 %0, 16, v245" : "=v"(dD[0]) :  : ); SB();
  asm volatile("v_mfma_f32_16x16x32_bf16 %0, v[176:179], a[40:43], %0" : "+v"(a1[0][1]) :  : ); SB();
  asm volatile("ds_read_b64_tr_b16 v[164:165], %0 offset:12288" :  : "v"(aW[1][1][1]) : "memory"); SB();
  asm volatile("v_lshlrev_b32 %0, 16, v246" : "=v"(dD[1]) :  : ); SB();
  asm volatile("v_mfma_f32_16x16x32_bf16 %0, v[176:179], a[44:47], %0" : "+v"(a1[1][1]) :  : ); SB();
  asm volatile("ds_read_b64_tr_b16 v[166:167], %0 offset:13056" :  : "v"(aW[1][1][1]) : "memory"); SB();
  asm volatile("v_lshlrev_b32 %0, 16, v247" : "=v"(dD[2]) :  : ); SB();
  asm volatile("v_mfma_f32_16x16x32_bf16 %0, v[180:183], a[40:43], %0" : "+v"(a1[0][2]) :  : ); SB();
  asm volatile("ds_read_b64_tr_b16 v[168:169], %0 offset:12288" :  : "v"(aW[1][1][2]) : "memory"); SB();
  asm volatile("v_lshlrev_b32 %0, 16, v248" : "=v"(bs) :  : ); SB();
  asm volatile("v_mfma_f32_16x16x32_bf16 %0, v[180:183], a[44:47], %0" : "+v"(a1[1][2]) :  : ); SB();
  asm volatile("ds_read_b64_tr_b16 v[170:171], %0 offset:13056" :  : "v"(aW[1][1][2]) : "memory"); SB();
  asm volatile("s_waitcnt lgkmcnt(6)\n\tv_mfma_f32_16x16x32_bf16 %0, v[184:187], a[48:51], %0" : "+v"(a1[0][0]) :  : ); SB();
  asm volatile("s_barrier" :  :  : "memory"); SB();
  asm volatile("ds_read_b64_tr_b16 v[172:173], %0 offset:24576" :  : "v"(aW[1][0][0]) : "memory"); SB();
  asm volatile("s_add_u32 m0, %0, 36864" :  : "s"(mW) : "memory", "scc"); SB();
  asm volatile("v_mfma_f32_16x16x32_bf16 %0, v[184:187], a[52:55], %0" : "+v"(a1[1][0]) :  : ); SB();
  asm volatile("ds_read_b64_tr_b16 v[174:175], %0 offset:25344" :  : "v"(aW[1][0][0]) : "memory"); SB();
  asm volatile("buffer_load_dwordx4 %0, %1, %2 offen lds" :  : "v"(voB[0]), "s"(rW1), "s"(kw_[9]) : "memory"); SB();
  asm volatile("v_mfma_f32_16x16x32_bf16 %0, v[184:187], %1, %0" : "+v"(c1[0]) : "v"(ones) : ); SB();
  asm volatile("ds_read_b64_tr_b16 v[176:177], %0 offset:24576" :  : "v"(aW[1][0][1]) : "memory"); SB();
  asm volatile("v_mov_b32_dpp v242, v231 wave_shr:1 row_mask:0xf bank_mask:0xf" :  :  : ); SB();
  asm volatile("v_mfma_f32_16x16x32_bf16 %0, v[188:191], a[48:51], %0" : "+v"(a1[0][1]) :  : ); SB();
  asm volatile("ds_read_b64_tr_b16 v[178:179], %0 offset:25344" :  : "v"(aW[1][0][1]) : "memory"); SB();
  asm volatile("v_alignbit_b32 %0, v231, v231, 16" : "=v"(im[0][0]) :  : ); SB();
  asm volatile("v_mfma_f32_16x16x32_bf16 %0, v[188:191], a[52:55], %0" : "+v"(a1[1][1]) :  : ); SB();
  asm volatile("ds_read_b64_tr_b16 v[180:181], %0 offset:24576" :  : "v"(aW[1][0][2]) : "memory"); SB();
  asm volatile("buffer_load_dwordx4 %0, %1, %2 offen offset:1024 lds" :  : "v"(voB[1]), "s"(rW1), "s"(kw_[9]) : "memory"); SB();
  asm volatile("v_mfma_f32_16x16x32_bf16 %0, v[188:191], %1, %0" : "+v"(c1[1]) : "v"(ones) : ); SB();
  asm volatile("ds_read_b64_tr_b16 v[182:183], %0 offset:25344" :  : "v"(aW[1][0][2]) : "memory"); SB();
  asm volatile("v_alignbit_b32 %0, v230, v230, 16" : "=v"(im[0][1]) :  : ); SB();
  asm volatile("v_mfma_f32_16x16x32_bf16 %0, v[192:195], a[48:51], %0" : "+v"(a1[0][2]) :  : ); SB();
  asm volatile("buffer_load_dwordx4 %0, %1, %2 offen offset:2048 lds" :  : "v"(voB[2]), "s"(rW1), "s"(kw_[9]) : "memory"); SB();
  asm volatile("v_alignbit_b32 %0, v229, v229, 16" : "=v"(im[0][2]) :  : ); SB();
  asm volatile("v_mfma_f32_16x16x32_bf16 %0, v[192:195], a[52:55], %0" : "+v"(a1[1][2]) :  : ); SB();
  asm volatile("v_alignbit_b32 %0, v228, v228, 16" : "=v"(im[0][3]) :  : ); SB();
  asm volatile("v_mfma_f32_16x16x32_bf16 %0, v[192:195], %1, %0" : "+v"(c1[2]) : "v"(ones) : ); SB();
  asm volatile("v_bfi_b32 %0, %1, v231, v230" : "=v"(im[1][0]) : "s"(0xFFFFu) : ); SB();
  asm volatile("s_waitcnt lgkmcnt(6)\n\tv_mfma_f32_16x16x32_bf16 %0, v[160:163], a[56:59], %0" : "+v"(a1[0][0]) :  : ); SB();
  asm volatile("ds_read_b64_tr_b16 v[184:185], %0 offset:24576" :  : "v"(aW[1][1][0]) : "memory"); SB();
  asm volatile("s_add_u32 m0, %0, 102400" :  : "s"(mA) : "memory", "scc"); SB();
  asm volatile("v_mfma_f32_16x16x32_bf16 %0, v[160:163], a[60:63], %0" : "+v"(a1[1][0]) :  : ); SB();
  asm volatile("ds_read_b64_tr_b16 v[186:187], %0 offset:25344" :  : "v"(aW[1][1][0]) : "memory"); SB();
  asm volatile("v_bfi_b32 %0, %1, v230, v229" : "=v"(im[1][1]) : "s"(0xFFFFu) : ); SB();
  asm volatile("v_mfma_f32_16x16x32_bf16 %0, v[164:167], a[56:59], %0" : "+v"(a1[0][1]) :  : ); SB();
  asm volatile("ds_read_b64_tr_b16 v[188:189], %0 offset:24576" :  : "v"(aW[1][1][1]) : "memory"); SB();
  asm volatile("v_bfi_b32 %0, %1, v229, v228" : "=v"(im[1][2]) : "s"(0xFFFFu) : ); SB();
  asm volatile("v_mfma_f32_16x16x32_bf16 %0, v[164:167], a[60:63], %0" : "+v"(a1[1][1]) :  : ); SB();
  asm volatile("ds_read_b64_tr_b16 v[190:191], %0 offset:25344" :  : "v"(aW[1][1][1]) : "memory"); SB();
  asm volatile("v_bfi_b32 %0, %1, v228, v242" : "=v"(im[1][3]) : "s"(0xFFFFu) : ); SB();
  asm volatile("v_mfma_f32_16x16x32_bf16 %0, v[168:171], a[56:59], %0" : "+v"(a1[0][2]) :  : ); SB();
  asm volatile("ds_read_b64_tr_b16 v[192:193], %0 offset:24576" :  : "v"(aW[1][1][2]) : "memory"); SB();
  asm volatile("buffer_load_dwordx4 %0, %1, %2 offen offset:2048 lds" :  : "v"(voS), "s"(rA), "s"(ao[6]) : "memory"); SB();
  asm volatile("v_mfma_f32_16x16x32_bf16 %0, v[168:171], a[60:63], %0" : "+v"(a1[1][2]) :  : ); SB();
  asm volatile("ds_read_b64_tr_b16 v[194:195], %0 offset:25344" :  : "v"(aW[1][1][2]) : "memory"); SB();
  asm volatile("s_waitcnt lgkmcnt(6)\n\tv_mfma_f32_16x16x32_bf16 %0, v[172:175], a[64:67], %0" : "+v"(a1[0][0]) :  : ); SB();
  asm volatile("ds_read_b64_tr_b16 v[160:161], %0 offset:36864" :  : "v"(aW[1][0][0]) : "memory"); SB();
  asm volatile("v_mov_b32_dpp v243, v235 wave_shr:1 row_mask:0xf bank_mask:0xf" :  :  : ); SB();
  asm volatile("v_mfma_f32_16x16x32_bf16 %0, v[172:175], a[68:71], %0" : "+v"(a1[1][0]) :  : ); SB();
  asm volatile("ds_read_b64_tr_b16 v[162:163], %0 offset:37632" :  : "v"(aW[1][0][0]) : "memory"); SB();
  asm volatile("v_alignbit_b32 %0, v235, v235, 16" : "=v"(im[2][0]) :  : ); SB();
  asm volatile("v_mfma_f32_16x16x32_bf16 %0, v[172:175], %1, %0" : "+v"(c1[0]) : "v"(ones) : ); SB();
  asm volatile("ds_read_b64_tr_b16 v[164:165], %0 offset:36864" :  : "v"(aW[1][0][1]) : "memory"); SB();
  asm volatile("v_alignbit_b32 %0, v234, v234, 16" : "=v"(im[2][1]) :  : ); SB();
  asm volatile("v_mfma_f32_16x16x32_bf16 %0, v[176:179], a[64:67], %0" : "+v"(a1[0][1]) :  : ); SB();
  asm volatile("ds_read_b64_tr_b16 v[166:167], %0 offset:37632" :  : "v"(aW[1][0][1]) : "memory"); SB();
  asm volatile("buffer_load_dwordx4 %0, %1, %2 offen offset:3072 lds" :  : "v"(voS), "s"(rA), "s"(ao[7]) : "memory"); SB();
  asm volatile("v_mfma_f32_16x16x32_bf16 %0, v[176:179], a[68:71], %0" : "+v"(a1[1][1]) :  : ); SB();
  asm volatile("ds_read_b64_tr_b16 v[168:169], %0 offset:36864" :  : "v"(aW[1][0][2]) : "memory"); SB();
  asm volatile("v_alignbit_b32 %0, v233, v233, 16" : "=v"(im[2][2]) :  : ); SB();
  asm volatile("v_mfma_f32_16x16x32_bf16 %0, v[176:179], %1, %0" : "+v"(c1[1]) : "v"(ones) : ); SB();
  asm volatile("ds_read_b64_tr_b16 v[170:171], %0 offset:37632" :  : "v"(aW[1][0][2]) : "memory"); SB();
  asm volatile("v_alignbit_b32 %0, v232, v232, 16" : "=v"(im[2][3]) :  : ); SB();
  asm volatile("v_mfma_f32_16x16x32_bf16 %0, v[180:183], a[64:67], %0" : "+v"(a1[0][2]) :  : ); SB();
  asm volatile("s_waitcnt vmcnt(11)" :  :  : "memory"); SB();
  asm volatile("ds_read_b128 a[196:199], %0 offset:2048" :  : "v"(aA[0]) : "memory"); SB();
  asm volatile("v_mfma_f32_16x16x32_bf16 %0, v[180:183], a[68:71], %0" : "+v"(a1[1][2]) :  : ); SB();
  asm volatile("ds_read_b128 a[212:215], %0 offset:2048" :  : "v"(aA[1]) : "memory"); SB();
  asm volatile("v_bfi_b32 %0, %1, v235, v234" : "=v"(im[3][0]) : "s"(0xFFFFu) : ); SB();
  asm volatile("v_mfma_f32_16x16x32_bf16 %0, v[180:183], %1, %0" : "+v"(c1[2]) : "v"(ones) : ); SB();
  asm volatile("v_xor_b32 %0, 384, %0" : "+v"(voS) :  : "memory"); SB();
  asm volatile("s_waitcnt lgkmcnt(8)\n\tv_mfma_f32_16x16x32_bf16 %0, v[184:187], a[72:75], %0" : "+v"(a1[0][0]) :  : ); SB();
  asm volatile("ds_read_b64_tr_b16 v[172:173], %0 offset:36864" :  : "v"(aW[1][1][0]) : "memory"); SB();
  asm volatile("s_add_u32 m0, %0, 131072" :  : "s"(mA) : "memory", "scc"); SB();
  asm volatile("v_mfma_f32_16x16x32_bf16 %0, v[184:187], a[76:79], %0" : "+v"(a1[1][0]) :  : ); SB();
  asm volatile("ds_read_b64_tr_b16 v[174:175], %0 offset:37632" :  : "v"(aW[1][1][0]) : "memory"); SB();
  asm volatile("buffer_load_dwordx4 %0, %1, %2 offen lds" :  : "v"(voS), "s"(rA), "s"(ao[0]) : "memory"); SB();
  asm volatile("v_mfma_f32_16x16x32_bf16 %0, v[188:191], a[72:75], %0" : "+v"(a1[0][1]) :  : ); SB();
  asm volatile("ds_read_b64_tr_b16 v[176:177], %0 offset:36864" :  : "v"(aW[1][1][1]) : "memory"); SB();
  asm volatile("buffer_load_dwordx4 %0, %1, %2 offen offset:1024 lds" :  : "v"(voS), "s"(rA), "s"(ao[1]) : "memory"); SB();
  asm volatile("v_mfma_f32_16x16x32_bf16 %0, v[188:191], a[76:79], %0" : "+v"(a1[1][1]) :  : ); SB();
  asm volatile("ds_read_b64_tr_b16 v[178:179], %0 offset:37632" :  : "v"(aW[1][1][1]) : "memory"); SB();
  asm volatile("v_bfi_b32 %0, %1, v234, v233" : "=v"(im[3][1]) : "s"(0xFFFFu) : ); SB();
  asm volatile("v_mfma_f32_16x16x32_bf16 %0, v[192:195], a[72:75], %0" : "+v"(a1[0][2]) :  : ); SB();
  asm volatile("ds_read_b64_tr_b16 v[180:181], %0 offset:36864" :  : "v"(aW[1][1][2]) : "memory"); SB();
  asm volatile("v_bfi_b32 %0, %1, v233, v232" : "=v"(im[3][2]) : "s"(0xFFFFu) : ); SB();
  asm volatile("v_mfma_f32_16x16x32_bf16 %0, v[192:195], a[76:79], %0" : "+v"(a1[1][2]) :  : ); SB();
  asm volatile("ds_read_b64_tr_b16 v[182:183], %0 offset:37632" :  : "v"(aW[1][1][2]) : "memory"); SB();
  asm volatile("s_waitcnt lgkmcnt(8)\n\tv_mfma_f32_16x16x32_bf16 %0, v[160:163], a[80:83], %0" : "+v"(a1[0][0]) :  : ); SB();
  asm volatile("s_barrier" :  :  : "memory"); SB();
  asm volatile("ds_read_b64_tr_b16 v[184:185], %0 offset:0" :  : "v"(aW[0][0][0]) : "memory"); SB();
  asm volatile("v_bfi_b32 %0, %1, v232, v243" : "=v"(im[3][3]) : "s"(0xFFFFu) : ); SB();
  asm volatile("v_mfma_f32_16x16x32_bf16 %0, v[160:163], a[84:87], %0" : "+v"(a1[1][0]) :  : ); SB();
  asm volatile("ds_read_b64_tr_b16 v[186:187], %0 offset:768" :  : "v"(aW[0][0][0]) : "memory"); SB();
  asm volatile("v_mov_b32_dpp v244, v239 wave_shr:1 row_mask:0xf bank_mask:0xf" :  :  : ); SB();
  asm volatile("v_mfma_f32_16x16x32_bf16 %0, v[160:163], %1, %0" : "+v"(c1[0]) : "v"(ones) : ); SB();
  asm volatile("ds_read_b64_tr_b16 v[188:189], %0 offset:0" :  : "v"(aW[0][0][1]) : "memory"); SB();
  asm volatile("v_alignbit_b32 %0, v239, v239, 16" : "=v"(im[4][0]) :  : ); SB();
  asm volatile("v_mfma_f32_16x16x32_bf16 %0, v[164:167], a[80:83], %0" : "+v"(a1[0][1]) :  : ); SB();
  asm volatile("ds_read_b64_tr_b16 v[190:191], %0 offset:768" :  : "v"(aW[0][0][1]) : "memory"); SB();
  asm volatile("v_alignbit_b32 %0, v238, v238, 16" : "=v"(im[4][1]) :  : ); SB();
  asm volatile("v_mfma_f32_16x16x32_bf16 %0, v[164:167], a[84:87], %0" : "+v"(a1[1][1]) :  : ); SB();
  asm volatile("ds_read_b64_tr_b16 v[192:193], %0 offset:0" :  : "v"(aW[0][0][2]) : "memory"); SB();
  asm volatile("v_alignbit_b32 %0, v237, v237, 16" : "=v"(im[4][2]) :  : ); SB();
  asm volatile("v_mfma_f32_16x16x32_bf16 %0, v[164:167], %1, %0" : "+v"(c1[1]) : "v"(ones) : ); SB();
  asm volatile("ds_read_b64_tr_b16 v[194:195], %0 offset:768" :  : "v"(aW[0][0][2]) : "memory"); SB();
  asm volatile("buffer_load_dwordx4 %0, %1, %2 offen offset:2048 lds" :  : "v"(voS), "s"(rA), "s"(ao[2]) : "memory"); SB();
  asm volatile("v_mfma_f32_16x16x32_bf16 %0, v[168:171], a[80:83], %0" : "+v"(a1[0][2]) :  : ); SB();
  asm volatile("buffer_load_dwordx4 %0, %1, %2 offen offset:3072 lds" :  : "v"(voS), "s"(rA), "s"(ao[3]) : "memory"); SB();
  asm volatile("v_alignbit_b32 %0, v236, v236, 16" : "=v"(im[4][3]) :  : ); SB();
  asm volatile("v_mfma_f32_16x16x32_bf16 %0, v[168:171], a[84:87], %0" : "+v"(a1[1][2]) :  : ); SB();
  asm volatile("v_bfi_b32 %0, %1, v239, v238" : "=v"(im[5][0]) : "s"(0xFFFFu) : ); SB();
  asm volatile("v_mfma_f32_16x16x32_bf16 %0, v[168:171], %1, %0" : "+v"(c1[2]) : "v"(ones) : ); SB();
  asm volatile("v_bfi_b32 %0, %1, v238, v237" : "=v"(im[5][1]) : "s"(0xFFFFu) : ); SB();
  asm volatile("s_waitcnt lgkmcnt(5)\n\tv_mfma_f32_16x16x32_bf16 %0, v[172:175], a[88:91], %0" : "+v"(a1[0][0]) :  : ); SB();
  asm volatile("ds_read_b64_tr_b16 v[160:161], %0 offset:0" :  : "v"(aW[0][1][0]) : "memory"); SB();
  asm volatile("s_add_u32 m0, %0, 135168" :  : "s"(mA) : "memory", "scc"); SB();
  asm volatile("v_mfma_f32_16x16x32_bf16 %0, v[172:175], a[92:95], %0" : "+v"(a1[1][0]) :  : ); SB();
  asm volatile("ds_read_b64_tr_b16 v[162:163], %0 offset:768" :  : "v"(aW[0][1][0]) : "memory"); SB();
  asm volatile("v_bfi_b32 %0, %1, v237, v236" : "=v"(im[5][2]) : "s"(0xFFFFu) : ); SB();
  asm volatile("v_mfma_f32_16x16x32_bf16 %0, v[176:179], a[88:91], %0" : "+v"(a1[0][1]) :  : ); SB();
  asm volatile("ds_read_b64_tr_b16 v[164:165], %0 offset:0" :  : "v"(aW[0][1][1]) : "memory"); SB();
  asm volatile("v_bfi_b32 %0, %1, v236, v244" : "=v"(im[5][3]) : "s"(0xFFFFu) : ); SB();
  asm volatile("v_mfma_f32_16x16x32_bf16 %0, v[176:179], a[92:95], %0" : "+v"(a1[1][1]) :  : ); SB();
  asm volatile("ds_read_b64_tr_b16 v[166:167], %0 offset:768" :  : "v"(aW[0][1][1]) : "memory"); SB();
  asm volatile("v_mov_b32 %0, 0" : "=v"(zero4[0]) :  : ); SB();
  asm volatile("v_mfma_f32_16x16x32_bf16 %0, v[180:183], a[88:91], %0" : "+v"(a1[0][2]) :  : ); SB();
  asm volatile("ds_read_b64_tr_b16 v[168:169], %0 offset:0" :  : "v"(aW[0][1][2]) : "memory"); SB();
  asm volatile("v_mov_b32 %0, 0" : "=v"(zero4[1]) :  : ); SB();
  asm volatile("v_mfma_f32_16x16x32_bf16 %0, v[180:183], a[92:95], %0" : "+v"(a1[1][2]) :  : ); SB();
  asm volatile("ds_read_b64_tr_b16 v[170:171], %0 offset:768" :  : "v"(aW[0][1][2]) : "memory"); SB();
  asm volatile("s_waitcnt lgkmcnt(6)\n\tv_mfma_f32_16x16x32_bf16 %0, v[184:187], a[96:99], %0" : "+v"(a1[0][0]) :  : ); SB();
  asm volatile("ds_read_b64_tr_b16 v[172:173], %0 offset:12288" :  : "v"(aW[0][0][0]) : "memory"); SB();
  asm volatile("v_mov_b32 %0, 0" : "=v"(zero4[2]) :  : ); SB();
  asm volatile("v_mfma_f32_16x16x32_bf16 %0, v[184:187], a[100:103], %0" : "+v"(a1[1][0]) :  : ); SB();
  asm volatile("ds_read_b64_tr_b16 v[174:175], %0 offset:13056" :  : "v"(aW[0][0][0]) : "memory"); SB();
  asm volatile("v_mov_b32 %0, 0" : "=v"(zero4[3]) :  : ); SB();
  asm volatile("v_mfma_f32_16x16x32_bf16 %0, v[184:187], %1, %0" : "+v"(c1[0]) : "v"(ones) : ); SB();
  asm volatile("ds_read_b64_tr_b16 v[176:177], %0 offset:12288" :  : "v"(aW[0][0][1]) : "memory"); SB();
  asm volatile("v_mfma_f32_16x16x32_bf16 %0, v[188:191], a[96:99], %0" : "+v"(a1[0][1]) :  : ); SB();
  asm volatile("ds_read_b64_tr_b16 v[178:179], %0 offset:13056" :  : "v"(aW[0][0][1]) : "memory"); SB();
  asm volatile("v_mfma_f32_16x16x32_bf16 %0, v[188:191], a[100:103], %0" : "+v"(a1[1][1]) :  : ); SB();
  asm volatile("ds_read_b64_tr_b16 v[180:181], %0 offset:12288" :  : "v"(aW[0][0][2]) : "memory"); SB();
  asm volatile("v_mfma_f32_16x16x32_bf16 %0, v[188:191], %1, %0" : "+v"(c1[1]) : "v"(ones) : ); SB();
  asm volatile("ds_read_b64_tr_b16 v[182:183], %0 offset:13056" :  : "v"(aW[0][0][2]) : "memory"); SB();
  asm volatile("v_mfma_f32_16x16x32_bf16 %0, v[192:195], a[96:99], %0" : "+v"(a1[0][2]) :  : ); SB();
  asm volatile("buffer_load_dwordx4 %0, %1, %2 offen lds" :  : "v"(voS), "s"(rA), "s"(ao[4]) : "memory"); SB();
  asm volatile("v_mfma_f32_16x16x32_bf16 %0, v[192:195], a[100:103], %0" : "+v"(a1[1][2]) :  : ); SB();
  asm volatile("buffer_load_dwordx4 %0, %1, %2 offen offset:1024 lds" :  : "v"(voS), "s"(rA), "s"(ao[5]) : "memory"); SB();
  asm volatile("v_mfma_f32_16x16x32_bf16 %0, v[192:195], %1, %0" : "+v"(c1[2]) : "v"(ones) : ); SB();
  asm volatile("s_waitcnt lgkmcnt(6)\n\tv_mfma_f32_16x16x32_bf16 %0, v[160:163], a[104:107], %0" : "+v"(a1[0][0]) :  : ); SB();
  asm volatile("ds_read_b64_tr_b16 v[184:185], %0 offset:12288" :  : "v"(aW[0][1][0]) : "memory"); SB();
  asm volatile("v_mfma_f32_16x16x32_bf16 %0, v[160:163], a[108:111], %0" : "+v"(a1[1][0]) :  : ); SB();
  asm volatile("s_mov_b32 exec_lo, 0x10001\n\ts_mov_b32 exec_hi, 0x10001\n\tds_write_b128 %0, %1 offset:0\n\ts_mov_b64 exec, -1" :  : "v"(ca), "v"(c1[0]) : "memory"); SB();
  asm volatile("ds_read_b64_tr_b16 v[186:187], %0 offset:13056" :  : "v"(aW[0][1][0]) : "memory"); SB();
  asm volatile("v_mfma_f32_16x16x32_bf16 %0, v[164:167], a[104:107], %0" : "+v"(a1[0][1]) :  : ); SB();
  asm volatile("ds_read_b64_tr_b16 v[188:189], %0 offset:12288" :  : "v"(aW[0][1][1]) : "memory"); SB();
  asm volatile("v_mfma_f32_16x16x32_bf16 %0, v[164:167], a[108:111], %0" : "+v"(a1[1][1]) :  : ); SB();
  asm volatile("s_mov_b32 exec_lo, 0x10001\n\ts_mov_b32 exec_hi, 0x10001\n\tds_write_b128 %0, %1 offset:64\n\ts_mov_b64 exec, -1" :  : "v"(ca), "v"(c1[1]) : "memory"); SB();
  asm volatile("ds_read_b64_tr_b16 v[190:191], %0 offset:13056" :  : "v"(aW[0][1][1]) : "memory"); SB();
  asm volatile("s_waitcnt vmcnt(13)\n\ts_mov_b64 exec, 1\n\tbuffer_store_dwordx2 %0, off, %1, %2 sc1\n\ts_mov_b64 exec, -1" :  : "v"(((u32x2){tg0, tg1})), "s"(rWS), "s"(sFZ) : "memory"); SB();
  asm volatile("v_mfma_f32_16x16x32_bf16 %0, v[168:171], a[104:107], %0" : "+v"(a1[0][2]) :  : ); SB();
  asm volatile("ds_read_b64_tr_b16 v[192:193], %0 offset:12288" :  : "v"(aW[0][1][2]) : "memory"); SB();
  asm volatile("s_waitcnt vmcnt(13)" :  :  : "memory"); SB();
  asm volatile("v_mfma_f32_16x16x32_bf16 %0, v[168:171], a[108:111], %0" : "+v"(a1[1][2]) :  : ); SB();
  asm volatile("s_mov_b32 exec_lo, 0x10001\n\ts_mov_b32 exec_hi, 0x10001\n\tds_write_b128 %0, %1 offset:128\n\ts_mov_b64 exec, -1" :  : "v"(ca), "v"(c1[2]) : "memory"); SB();
  asm volatile("ds_read_b64_tr_b16 v[194:195], %0 offset:13056" :  : "v"(aW[0][1][2]) : "memory"); SB();
  asm volatile("s_waitcnt lgkmcnt(5)\n\tv_mfma_f32_16x16x32_bf16 %0, v[172:175], a[112:115], %0" : "+v"(a1[0][0]) :  : ); SB();
  asm volatile("ds_read_b128 a[200:203], %0 offset:4096" :  : "v"(aA[0]) : "memory"); SB();
  asm volatile("v_mfma_f32_16x16x32_bf16 %0, v[172:175], a[116:119], %0" : "+v"(a1[1][0]) :  : ); SB();
  asm volatile("ds_read_b128 a[216:219], %0 offset:4096" :  : "v"(aA[1]) : "memory"); SB();
  asm volatile("v_mfma_f32_16x16x32_bf16 %0, v[172:175], %1, %0" : "+v"(c1[0]) : "v"(ones) : ); SB();
  asm volatile("buffer_load_dwordx4 %0, %1, %2 offen offset:2048 lds" :  : "v"(voS), "s"(rA), "s"(ao[6]) : "memory"); SB();
  asm volatile("v_mfma_f32_16x16x32_bf16 %0, v[176:179], a[112:115], %0" : "+v"(a1[0][1]) :  : ); SB();
  asm volatile("buffer_load_dwordx4 %0, %1, %2 offen offset:3072 lds" :  : "v"(voS), "s"(rA), "s"(ao[7]) : "memory"); SB();
  asm volatile("v_xor_b32 %0, 128, %0" : "+v"(voS) :  : "memory"); SB();
  asm volatile("s_add_u32 m0, %0, 98304" :  : "s"(mA) : "memory", "scc"); SB();
  asm volatile("s_nop 0\n\tbuffer_load_dwordx4 %0, %1, %2 offen lds" :  : "v"(voS), "s"(rA), "s"(ao[0]) : "memory"); SB();
  asm volatile("buffer_load_dwordx4 %0, %1, %2 offen offset:1024 lds" :  : "v"(voS), "s"(rA), "s"(ao[1]) : "memory"); SB();
  asm volatile("s_waitcnt vmcnt(13) lgkmcnt(0)\n\ts_barrier" :  :  : "memory"); SB();
  asm volatile("ds_read_b64_tr_b16 v[160:161], %0 offset:24576" :  : "v"(aW[0][0][0]) : "memory"); SB();
  asm volatile("v_mfma_f32_16x16x32_bf16 %0, v[176:179], a[116:119], %0" : "+v"(a1[1][1]) :  : ); SB();
  asm volatile("ds_read_b64_tr_b16 v[162:163], %0 offset:25344" :  : "v"(aW[0][0][0]) : "memory"); SB();
  asm volatile("v_mfma_f32_16x16x32_bf16 %0, v[176:179], %1, %0" : "+v"(c1[1]) : "v"(ones) : ); SB();
  asm volatile("ds_read_b64_tr_b16 v[164:165], %0 offset:24576" :  : "v"(aW[0][0][1]) : "memory"); SB();
  asm volatile("v_mfma_f32_16x16x32_bf16 %0, v[180:183], a[112:115], %0" : "+v"(a1[0][2]) :  : ); SB();
  asm volatile("ds_read_b64_tr_b16 v[166:167], %0 offset:25344" :  : "v"(aW[0][0][1]) : "memory"); SB();
  asm volatile("v_mfma_f32_16x16x32_bf16 %0, v[180:183], a[116:119], %0" : "+v"(a1[1][2]) :  : ); SB();
  asm volatile("ds_read_b64_tr_b16 v[168:169], %0 offset:24576" :  : "v"(aW[0][0][2]) : "memory"); SB();
  asm volatile("v_mfma_f32_16x16x32_bf16 %0, v[180:183], %1, %0" : "+v"(c1[2]) : "v"(ones) : ); SB();
  asm volatile("ds_read_b64_tr_b16 v[170:171], %0 offset:25344" :  : "v"(aW[0][0][2]) : "memory"); SB();
  asm volatile("s_waitcnt vmcnt(11)" :  :  : "memory"); SB();
  asm volatile("v_mfma_f32_16x16x32_bf16 %0, v[184:187], a[120:123], %0" : "+v"(a1[0][0]) :  : ); SB();
  asm volatile("ds_read_b128 a[204:207], %0 offset:6144" :  : "v"(aA[0]) : "memory"); SB();
  asm volatile("v_mfma_f32_16x16x32_bf16 %0, v[184:187], a[124:127], %0" : "+v"(a1[1][0]) :  : ); SB();
  asm volatile("ds_read_b128 a[220:223], %0 offset:6144" :  : "v"(aA[1]) : "memory"); SB();
  asm volatile("v_mfma_f32_16x16x32_bf16 %0, v[184:187], %1, %0" : "+v"(c1[0]) : "v"(ones) : ); SB();
  asm volatile("buffer_load_dwordx4 %0, %1, %2 offen offset:2048 lds" :  : "v"(voS), "s"(rA), "s"(ao[2]) : "memory"); SB();
  asm volatile("v_mfma_f32_16x16x32_bf16 %0, v[188:191], a[120:123], %0" : "+v"(a1[0][1]) :  : ); SB();
  asm volatile("ds_read_b64_tr_b16 v[172:173], %0 offset:24576" :  : "v"(aW[0][1][0]) : "memory"); SB();
  asm volatile("buffer_load_dwordx4 %0, %1, %2 offen offset:3072 lds" :  : "v"(voS), "s"(rA), "s"(ao[3]) : "memory"); SB();
  asm volatile("v_mfma_f32_16x16x32_bf16 %0, v[188:191], a[124:127], %0" : "+v"(a1[1][1]) :  : ); SB();
  asm volatile("ds_read_b64_tr_b16 v[174:175], %0 offset:25344" :  : "v"(aW[0][1][0]) : "memory"); SB();
  asm volatile("v_mfma_f32_16x16x32_bf16 %0, v[188:191], %1, %0" : "+v"(c1[1]) : "v"(ones) : ); SB();
  asm volatile("ds_read_b64_tr_b16 v[176:177], %0 offset:24576" :  : "v"(aW[0][1][1]) : "memory"); SB();
  asm volatile("v_mfma_f32_16x16x32_bf16 %0, v[192:195], a[120:123], %0" : "+v"(a1[0][2]) :  : ); SB();
  asm volatile("ds_read_b64_tr_b16 v[178:179], %0 offset:25344" :  : "v"(aW[0][1][1]) : "memory"); SB();
  asm volatile("s_waitcnt vmcnt(11)" :  :  : "memory"); SB();
  asm volatile("v_mfma_f32_16x16x32_bf16 %0, v[192:195], a[124:127], %0" : "+v"(a1[1][2]) :  : ); SB();
  asm volatile("ds_read_b64_tr_b16 v[180:181], %0 offset:24576" :  : "v"(aW[0][1][2]) : "memory"); SB();
  asm volatile("ds_read_b128 a[224:227], %0 offset:32768" :  : "v"(aA[0]) : "memory"); SB();
  asm volatile("v_mfma_f32_16x16x32_bf16 %0, v[192:195], %1, %0" : "+v"(c1[2]) : "v"(ones) : ); SB();
  asm volatile("ds_read_b64_tr_b16 v[182:183], %0 offset:25344" :  : "v"(aW[0][1][2]) : "memory"); SB();
  asm volatile("ds_read_b128 a[240:243], %0 offset:32768" :  : "v"(aA[1]) : "memory"); SB();
  asm volatile("s_waitcnt lgkmcnt(9)\n\tv_mfma_f32_16x16x32_bf16 %0, v[160:163], a[128:131], %0" : "+v"(a1[0][0]) :  : ); SB();
  asm volatile("s_add_u32 m0, %0, 102400" :  : "s"(mA) : "memory", "scc"); SB();
  asm volatile("ds_read_b128 %0, %1 offset:0" : "=&v"(co[0]) : "v"(cr) : "memory"); SB();
  asm volatile("v_mfma_f32_16x16x32_bf16 %0, v[160:163], a[132:135], %0" : "+v"(a1[1][0]) :  : ); SB();
  asm volatile("buffer_load_dwordx4 %0, %1, %2 offen lds" :  : "v"(voS), "s"(rA), "s"(ao[4]) : "memory"); SB();
  asm volatile("v_mfma_f32_16x16x32_bf16 %0, v[160:163], %1, %0" : "+v"(c1[0]) : "v"(ones) : ); SB();
  asm volatile("buffer_load_dwordx4 %0, %1, %2 offen offset:1024 lds" :  : "v"(voS), "s"(rA), "s"(ao[5]) : "memory"); SB();
  asm volatile("s_waitcnt lgkmcnt(8)" :  :  : "memory"); SB();
  asm volatile("v_mfma_f32_16x16x32_bf16 %0, v[164:167], a[128:131], %0" : "+v"(a1[0][1]) :  : ); SB();
  asm volatile("ds_read_b64_tr_b16 v[184:185], %0 offset:36864" :  : "v"(aW[0][0][0]) : "memory"); SB();
  asm volatile("s_waitcnt vmcnt(11)" :  :  : "memory"); SB();
  asm volatile("v_mfma_f32_16x16x32_bf16 %0, v[164:167], a[132:135], %0" : "+v"(a1[1][1]) :  : ); SB();
  asm volatile("ds_read_b64_tr_b16 v[186:187], %0 offset:37632" :  : "v"(aW[0][0][0]) : "memory"); SB();
  asm volatile("ds_read_b128 a[228:231], %0 offset:34816" :  : "v"(aA[0]) : "memory"); SB();
  asm volatile("v_mfma_f32_16x16x32_bf16 %0, v[164:167], %1, %0" : "+v"(c1[1]) : "v"(ones) : ); SB();
  asm volatile("ds_read_b64_tr_b16 v[188:189], %0 offset:36864" :  : "v"(aW[0][0][1]) : "memory"); SB();
  asm volatile("ds_read_b128 a[244:247], %0 offset:34816" :  : "v"(aA[1]) : "memory"); SB();
  asm volatile("v_mfma_f32_16x16x32_bf16 %0, v[168:171], a[128:131], %0" : "+v"(a1[0][2]) :  : ); SB();
  asm volatile("ds_read_b64_tr_b16 v[190:191], %0 offset:37632" :  : "v"(aW[0][0][1]) : "memory"); SB();
  asm volatile("buffer_load_dwordx4 %0, %1, %2 offen offset:2048 lds" :  : "v"(voS), "s"(rA), "s"(ao[6]) : "memory"); SB();
  asm volatile("v_mfma_f32_16x16x32_bf16 %0, v[168:171], a[132:135], %0" : "+v"(a1[1][2]) :  : ); SB();
  asm volatile("ds_read_b64_tr_b16 v[192:193], %0 offset:36864" :  : "v"(aW[0][0][2]) : "memory"); SB();
  asm volatile("buffer_load_dwordx4 %0, %1, %2 offen offset:3072 lds" :  : "v"(voS), "s"(rA), "s"(ao[7]) : "memory"); SB();
  asm volatile("v_mfma_f32_16x16x32_bf16 %0, v[168:171], %1, %0" : "+v"(c1[2]) : "v"(ones) : ); SB();
  asm volatile("ds_read_b64_tr_b16 v[194:195], %0 offset:37632" :  : "v"(aW[0][0][2]) : "memory"); SB();
  asm volatile("s_waitcnt lgkmcnt(8)\n\tv_mfma_f32_16x16x32_bf16 %0, v[172:175], a[136:139], %0" : "+v"(a1[0][0]) :  : ); SB();
  asm volatile("ds_read_b128 %0, %1 offset:64" : "=&v"(co[1]) : "v"(cr) : "memory"); SB();
  asm volatile("s_waitcnt vmcnt(11)" :  :  : "memory"); SB();
  asm volatile("v_mfma_f32_16x16x32_bf16 %0, v[172:175], a[140:143], %0" : "+v"(a1[1][0]) :  : ); SB();
  asm volatile("ds_read_b128 a[232:235], %0 offset:36864" :  : "v"(aA[0]) : "memory"); SB();
  asm volatile("v_mfma_f32_16x16x32_bf16 %0, v[172:175], %1, %0" : "+v"(c1[0]) : "v"(ones) : ); SB();
  asm volatile("ds_read_b128 a[248:251], %0 offset:36864" :  : "v"(aA[1]) : "memory"); SB();
  asm volatile("v_mfma_f32_16x16x32_bf16 %0, v[176:179], a[136:139], %0" : "+v"(a1[0][1]) :  : ); SB();
  asm volatile("ds_read_b64_tr_b16 v[160:161], %0 offset:36864" :  : "v"(aW[0][1][0]) : "memory"); SB();
  asm volatile("v_xor_b32 %0, 896, %0" : "+v"(voS) :  : "memory"); SB();
  asm volatile("v_mfma_f32_16x16x32_bf16 %0, v[176:179], a[140:143], %0" : "+v"(a1[1][1]) :  : ); SB();
  asm volatile("ds_read_b64_tr_b16 v[162:163], %0 offset:37632" :  : "v"(aW[0][1][0]) : "memory"); SB();
  asm volatile("buffer_load_dwordx2 %0, %1, %2, %3 offen sc1" : "=&v"(fk) : "v"(vFZ), "s"(rWS), "s"(8192u) : "memory"); SB();
  asm volatile("v_mfma_f32_16x16x32_bf16 %0, v[176:179], %1, %0" : "+v"(c1[1]) : "v"(ones) : ); SB();
  asm volatile("ds_read_b64_tr_b16 v[164:165], %0 offset:36864" :  : "v"(aW[0][1][1]) : "memory"); SB();
  asm volatile("s_add_u32 m0, %0, 131072" :  : "s"(mA) : "memory", "scc"); SB();
  asm volatile("v_mfma_f32_16x16x32_bf16 %0, v[180:183], a[136:139], %0" : "+v"(a1[0][2]) :  : ); SB();
  asm volatile("ds_read_b64_tr_b16 v[166:167], %0 offset:37632" :  : "v"(aW[0][1][1]) : "memory"); SB();
  asm volatile("buffer_load_dwordx4 %0, %1, %2 offen lds" :  : "v"(voS), "s"(rA), "s"(ao[0]) : "memory"); SB();
  asm volatile("v_mfma_f32_16x16x32_bf16 %0, v[180:183], a[140:143], %0" : "+v"(a1[1][2]) :  : ); SB();
  asm volatile("ds_read_b64_tr_b16 v[168:169], %0 offset:36864" :  : "v"(aW[0][1][2]) : "memory"); SB();
  asm volatile("v_mfma_f32_16x16x32_bf16 %0, v[180:183], %1, %0" : "+v"(c1[2]) : "v"(ones) : ); SB();
  asm volatile("ds_read_b64_tr_b16 v[170:171], %0 offset:37632" :  : "v"(aW[0][1][2]) : "memory"); SB();
  asm volatile("s_waitcnt lgkmcnt(9)\n\tv_mfma_f32_16x16x32_bf16 %0, v[184:187], a[144:147], %0" : "+v"(a1[0][0]) :  : ); SB();
  asm volatile("ds_read_b128 %0, %1 offset:128" : "=&v"(co[2]) : "v"(cr) : "memory"); SB();
  asm volatile("v_mfma_f32_16x16x32_bf16 %0, v[184:187], a[148:151], %0" : "+v"(a1[1][0]) :  : ); SB();
  asm volatile("ds_write_b128 %0, %1 offset:0" :  : "v"(ha), "v"(im[0]) : "memory"); SB();
  asm volatile("ds_write_b128 %0, %1 offset:4160" :  : "v"(ha), "v"(im[1]) : "memory"); SB();
  asm volatile("buffer_load_dwordx4 %0, %1, %2 offen offset:1024 lds" :  : "v"(voS), "s"(rA), "s"(ao[1]) : "memory"); SB();
  asm volatile("v_mfma_f32_16x16x32_bf16 %0, v[184:187], %1, %0" : "+v"(c1[0]) : "v"(ones) : ); SB();
  asm volatile("ds_write_b128 %0, %1 offset:12928" :  : "v"(ha), "v"(im[2]) : "memory"); SB();
  asm volatile("ds_write_b128 %0, %1 offset:17088" :  : "v"(ha), "v"(im[3]) : "memory"); SB();
  asm volatile("s_waitcnt vmcnt(11)" :  :  : "memory"); SB();
  asm volatile("ds_read_b128 a[236:239], %0 offset:38912" :  : "v"(aA[0]) : "memory"); SB();
  asm volatile("v_mfma_f32_16x16x32_bf16 %0, v[188:191], a[144:147], %0" : "+v"(a1[0][1]) :  : ); SB();
  asm volatile("ds_write_b128 %0, %1 offset:25856" :  : "v"(ha), "v"(im[4]) : "memory"); SB();
  asm volatile("ds_write_b128 %0, %1 offset:30016" :  : "v"(ha), "v"(im[5]) : "memory"); SB();
  asm volatile("ds_read_b128 a[252:255], %0 offset:38912" :  : "v"(aA[1]) : "memory"); SB();
  asm volatile("s_waitcnt vmcnt(9)" :  :  : "memory"); SB();
  asm volatile("v_mfma_f32_16x16x32_bf16 %0, v[188:191], a[148:151], %0" : "+v"(a1[1][1]) :  : ); SB();
  asm volatile("ds_read_b128 a[0:3], %0 offset:0" :  : "v"(aA[0]) : "memory"); SB();
  asm volatile("v_mfma_f32_16x16x32_bf16 %0, v[188:191], %1, %0" : "+v"(c1[1]) : "v"(ones) : ); SB();
  asm volatile("ds_read_b128 a[16:19], %0 offset:0" :  : "v"(aA[1]) : "memory"); SB();
  asm volatile("v_mfma_f32_16x16x32_bf16 %0, v[192:195], a[144:147], %0" : "+v"(a1[0][2]) :  : ); SB();
  asm volatile("ds_write_b128 %0, %1 offset:0" :  : "v"(za), "v"(zero4) : "memory"); SB();
  asm volatile("buffer_load_dwordx4 %0, %1, %2 offen offset:2048 lds" :  : "v"(voS), "s"(rA), "s"(ao[2]) : "memory"); SB();
  asm volatile("v_mfma_f32_16x16x32_bf16 %0, v[192:195], a[148:151], %0" : "+v"(a1[1][2]) :  : ); SB();
  asm volatile("buffer_load_dwordx4 %0, %1, %2 offen offset:3072 lds" :  : "v"(voS), "s"(rA), "s"(ao[3]) : "memory"); SB();
  asm volatile("s_waitcnt lgkmcnt(5)" :  :  : "memory"); SB();
  asm volatile("v_mfma_f32_16x16x32_bf16 %0, v[192:195], %1, %0" : "+v"(c1[2]) : "v"(ones) : ); SB();
  asm volatile("v_mfma_f32_16x16x32_bf16 %0, v[160:163], a[152:155], %0" : "+v"(a1[0][0]) :  : ); SB();
  asm volatile("v_mfma_f32_16x16x32_bf16 %0, v[160:163], a[156:159], %0" : "+v"(a1[1][0]) :  : ); SB();
  asm volatile("s_add_u32 m0, %0, 135168" :  : "s"(mA) : "memory", "scc"); SB();
  asm volatile("v_mfma_f32_16x16x32_bf16 %0, v[160:163], %1, %0" : "+v"(c1[0]) : "v"(ones) : ); SB();
  asm volatile("buffer_load_dwordx4 %0, %1, %2 offen lds" :  : "v"(voS), "s"(rA), "s"(ao[4]) : "memory"); SB();
  asm volatile("v_mfma_f32_16x16x32_bf16 %0, v[164:167], a[152:155], %0" : "+v"(a1[0][1]) :  : ); SB();
  asm volatile("v_mfma_f32_16x16x32_bf16 %0, v[164:167], a[156:159], %0" : "+v"(a1[1][1]) :  : ); SB();
  asm volatile("v_mfma_f32_16x16x32_bf16 %0, v[164:167], %1, %0" : "+v"(c1[1]) : "v"(ones) : ); SB();
  asm volatile("buffer_load_dwordx4 %0, %1, %2 offen offset:1024 lds" :  : "v"(voS), "s"(rA), "s"(ao[5]) : "memory"); SB();
  asm volatile("s_waitcnt lgkmcnt(0)" :  :  : "memory"); SB();
  asm volatile("v_mfma_f32_16x16x32_bf16 %0, v[168:171], a[152:155], %0" : "+v"(a1[0][2]) :  : ); SB();
  asm volatile("v_mfma_f32_16x16x32_bf16 %0, v[168:171], a[156:159], %0" : "+v"(a1[1][2]) :  : ); SB();
  asm volatile("v_mfma_f32_16x16x32_bf16 %0, v[168:171], %1, %0" : "+v"(c1[2]) : "v"(ones) : ); SB();
  asm volatile("buffer_load_dwordx4 %0, %1, %2 offen offset:2048 lds" :  : "v"(voS), "s"(rA), "s"(ao[6]) : "memory"); SB();
  asm volatile("s_waitcnt vmcnt(12)" :  :  : "memory"); SB();
  ST(11);
  f32x4 ac[8];
  u32x2 cA[2][8][2];
  asm volatile("ds_read_b128 a[4:7], %0 offset:2048" :  : "v"(aA[0]) : "memory"); SB();
  asm volatile("ds_read_b128 a[20:23], %0 offset:2048" :  : "v"(aA[1]) : "memory"); SB();
  asm volatile("buffer_load_dwordx4 %0, %1, %2 offen offset:3072 lds" :  : "v"(voS), "s"(rA), "s"(ao[7]) : "memory"); SB();
  asm volatile("v_xor_b32 %0, 128, %0" : "+v"(voS) :  : "memory"); SB();
  asm volatile("s_add_u32 m0, %0, 98304" :  : "s"(mA) : "memory", "scc"); SB();
  asm volatile("s_nop 0\n\tbuffer_load_dwordx4 %0, %1, %2 offen lds" :  : "v"(voS), "s"(rA), "s"(ao[0]) : "memory"); SB();
  asm volatile("buffer_load_dwordx4 %0, %1, %2 offen offset:1024 lds" :  : "v"(voS), "s"(rA), "s"(ao[1]) : "memory"); SB();
  asm volatile("buffer_load_dwordx2 %0, %1, %2, %3 offen sc1" : "=&v"(fz) : "v"(vFZ), "s"(rWS), "s"(0u) : "memory"); SB();
  asm volatile("s_waitcnt vmcnt(14)" :  :  : "memory"); SB();
  asm volatile("ds_read_b128 a[8:11], %0 offset:4096" :  : "v"(aA[0]) : "memory"); SB();
  asm volatile("ds_read_b128 a[24:27], %0 offset:4096" :  : "v"(aA[1]) : "memory"); SB();
  asm volatile("s_waitcnt lgkmcnt(2)" :  :  : "memory"); SB();
  asm volatile("buffer_load_dwordx4 %0, %1, %2 offen offset:2048 lds" :  : "v"(voS), "s"(rA), "s"(ao[2]) : "memory"); SB();
  asm volatile("buffer_load_dwordx4 %0, %1, %2 offen offset:3072 lds" :  : "v"(voS), "s"(rA), "s"(ao[3]) : "memory"); SB();
  asm volatile("s_waitcnt vmcnt(14)" :  :  : "memory"); SB();
  asm volatile("ds_read_b128 a[12:15], %0 offset:6144" :  : "v"(aA[0]) : "memory"); SB();
  asm volatile("ds_read_b128 a[28:31], %0 offset:6144" :  : "v"(aA[1]) : "memory"); SB();
  asm volatile("s_waitcnt lgkmcnt(2)" :  :  : "memory"); SB();
  asm volatile("s_add_u32 m0, %0, 102400" :  : "s"(mA) : "memory", "scc"); SB();
  asm volatile("s_nop 0\n\tbuffer_load_dwordx4 %0, %1, %2 offen lds" :  : "v"(voS), "s"(rA), "s"(ao[4]) : "memory"); SB();
  asm volatile("s_waitcnt vmcnt(14)\n\t.Lc_%=:\n\tv_cmp_ne_u64 vcc, %2, %1\n\ts_cbranch_vccnz .Lr_%=\n\t.Ld_%=:\n\t.subsection 1\n\t.Lr_%=:\n\tv_add_u32 %0, -1, %0\n\ts_nop 1\n\tv_readfirstlane_b32 vcc_hi, %0\n\ts_nop 3\n\ts_cmp_lt_i32 vcc_hi, 1\n\ts_cbranch_scc1 .Ld_%=\n\ts_sleep 2\n\ts_mov_b32 vcc_lo, 8192\n\tbuffer_load_dwordx2 %1, %3, %4, vcc_lo offen sc1\n\ts_waitcnt vmcnt(0)\n\ts_branch .Lc_%=\n\t.subsection 0" : "+v"(tmo), "+v"(fk) : "s"(tg), "v"(vFZ), "s"(rWS) : "memory", "vcc", "scc"); SB();
  asm volatile("s_add_u32 m0, %0, 85888" :  : "s"(mU) : "memory", "scc"); SB();
  asm volatile("s_nop 0\n\tbuffer_load_dwordx4 %0, %1, %2 offen sc1 lds" :  : "v"(vF), "s"(rWS), "s"(uso) : "memory"); SB();
  asm volatile("s_add_u32 m0, %0, 102400" :  : "s"(mA) : "memory", "scc"); SB();
  asm volatile("s_nop 0\n\tbuffer_load_dwordx4 %0, %1, %2 offen offset:1024 lds" :  : "v"(voS), "s"(rA), "s"(ao[5]) : "memory"); SB();
  asm volatile("s_waitcnt lgkmcnt(0)" :  :  : "memory"); SB();
  asm volatile("s_waitcnt vmcnt(14)" :  :  : "memory"); SB();
  asm volatile("ds_read_b128 a[32:35], %0 offset:32768" :  : "v"(aA[0]) : "memory"); SB();
  asm volatile("ds_read_b128 a[48:51], %0 offset:32768" :  : "v"(aA[1]) : "memory"); SB();
  asm volatile("buffer_load_dwordx4 %0, %1, %2 offen offset:2048 lds" :  : "v"(voS), "s"(rA), "s"(ao[6]) : "memory"); SB();
  asm volatile("buffer_load_dwordx4 %0, %1, %2 offen offset:3072 lds" :  : "v"(voS), "s"(rA), "s"(ao[7]) : "memory"); SB();
  asm volatile("s_waitcnt lgkmcnt(0)" :  :  : "memory"); SB();
  asm volatile("v_xor_b32 %0, 384, %0" : "+v"(voS) :  : "memory"); SB();
  asm volatile("s_waitcnt vmcnt(14)" :  :  : "memory"); SB();
  asm volatile("ds_read_b128 a[36:39], %0 offset:34816" :  : "v"(aA[0]) : "memory"); SB();
  asm volatile("ds_read_b128 a[52:55], %0 offset:34816" :  : "v"(aA[1]) : "memory"); SB();
  asm volatile("s_add_u32 m0, %0, 131072" :  : "s"(mA) : "memory", "scc"); SB();
  asm volatile("s_nop 0\n\tbuffer_load_dwordx4 %0, %1, %2 offen lds" :  : "v"(voS), "s"(rA), "s"(ao[0]) : "memory"); SB();
  asm volatile("buffer_load_dwordx4 %0, %1, %2 offen offset:1024 lds" :  : "v"(voS), "s"(rA), "s"(ao[1]) : "memory"); SB();
  asm volatile("s_waitcnt vmcnt(14)" :  :  : "memory"); SB();
  asm volatile("ds_read_b128 a[40:43], %0 offset:36864" :  : "v"(aA[0]) : "memory"); SB();
  asm volatile("ds_read_b128 a[56:59], %0 offset:36864" :  : "v"(aA[1]) : "memory"); SB();
  asm volatile("s_waitcnt lgkmcnt(2)" :  :  : "memory"); SB();
  asm volatile("buffer_load_dwordx4 %0, %1, %2 offen offset:2048 lds" :  : "v"(voS), "s"(rA), "s"(ao[2]) : "memory"); SB();
  asm volatile("buffer_load_dwordx4 %0, %1, %2 offen offset:3072 lds" :  : "v"(voS), "s"(rA), "s"(ao[3]) : "memory"); SB();
  asm volatile("s_waitcnt vmcnt(14)" :  :  : "memory"); SB();
  asm volatile("ds_read_b128 a[44:47], %0 offset:38912" :  : "v"(aA[0]) : "memory"); SB();
  asm volatile("ds_read_b128 a[60:63], %0 offset:38912" :  : "v"(aA[1]) : "memory"); SB();
  asm volatile("s_waitcnt lgkmcnt(2)" :  :  : "memory"); SB();
  asm volatile("s_add_u32 m0, %0, 135168" :  : "s"(mA) : "memory", "scc"); SB();
  asm volatile("s_nop 0\n\tbuffer_load_dwordx4 %0, %1, %2 offen lds" :  : "v"(voS), "s"(rA), "s"(ao[4]) : "memory"); SB();
  asm volatile("buffer_load_dwordx4 %0, %1, %2 offen offset:1024 lds" :  : "v"(voS), "s"(rA), "s"(ao[5]) : "memory"); SB();
  asm volatile("s_waitcnt vmcnt(14)" :  :  : "memory"); SB();
  asm volatile("ds_read_b128 a[64:67], %0 offset:0" :  : "v"(aA[0]) : "memory"); SB();
  asm volatile("ds_read_b128 a[80:83], %0 offset:0" :  : "v"(aA[1]) : "memory"); SB();
  asm volatile("s_waitcnt vmcnt(13)\n\t.Lc_%=:\n\tv_cmp_ne_u64 vcc, %2, %1\n\ts_cbranch_vccnz .Lr_%=\n\t.Ld_%=:\n\t.subsection 1\n\t.Lr_%=:\n\tv_add_u32 %0, -1, %0\n\ts_nop 1\n\tv_readfirstlane_b32 vcc_hi, %0\n\ts_nop 3\n\ts_cmp_lt_i32 vcc_hi, 1\n\ts_cbranch_scc1 .Ld_%=\n\ts_sleep 2\n\ts_mov_b32 vcc_lo, 0\n\tbuffer_load_dwordx2 %1, %3, %4, vcc_lo offen sc1\n\ts_waitcnt vmcnt(0)\n\ts_branch .Lc_%=\n\t.subsection 0" : "+v"(tmo), "+v"(fz) : "s"(tg), "v"(vFZ), "s"(rWS) : "memory", "vcc", "scc"); SB();
  asm volatile("buffer_load_dwordx4 v[160:163], %0, %1, %2 offen offset:0 sc1" :  : "v"(vB), "s"(rWS), "s"(0u) : "memory"); SB();
  asm volatile("s_waitcnt lgkmcnt(2)" :  :  : "memory"); SB();
  asm volatile("buffer_load_dwordx4 v[164:167], %0, %1, %2 offen offset:1024 sc1" :  : "v"(vB), "s"(rWS), "s"(0u) : "memory"); SB();
  asm volatile("s_waitcnt vmcnt(13)" :  :  : "memory"); SB();
  asm volatile("ds_read_b128 a[68:71], %0 offset:2048" :  : "v"(aA[0]) : "memory"); SB();
  asm volatile("ds_read_b128 a[84:87], %0 offset:2048" :  : "v"(aA[1]) : "memory"); SB();
  asm volatile("buffer_load_dwordx4 v[168:171], %0, %1, %2 offen offset:0 sc1" :  : "v"(vB), "s"(rWS), "s"(65536u) : "memory"); SB();
  asm volatile("s_waitcnt vmcnt(12) lgkmcnt(0)\n\ts_barrier" :  :  : "memory"); SB();
  ST(14);
  asm volatile("ds_read2_b32 %0, %1 offset0:176 offset1:177" : "=&v"(cA[0][0][0]) : "v"(aa[0]) : "memory"); SB();
  asm volatile("ds_read2_b32 %0, %1 offset0:178 offset1:179" : "=&v"(cA[0][0][1]) : "v"(aa[0]) : "memory"); SB();
  asm volatile("ds_read_b128 a[124:127], %0 offset:3776" :  : "v"(bb) : "memory"); SB();
  asm volatile("ds_read_b128 a[120:123], %0 offset:3264" :  : "v"(bb) : "memory"); SB();
  asm volatile("ds_read_b128 a[116:119], %0 offset:2752" :  : "v"(bb) : "memory"); SB();
  asm volatile("ds_read_b128 a[112:115], %0 offset:2240" :  : "v"(bb) : "memory"); SB();
  asm volatile("ds_read2_b32 %0, %1 offset0:48 offset1:49" : "=&v"(cA[0][1][0]) : "v"(aa[0]) : "memory"); SB();
  asm volatile("ds_read2_b32 %0, %1 offset0:50 offset1:51" : "=&v"(cA[0][1][1]) : "v"(aa[0]) : "memory"); SB();
  asm volatile("ds_read_b128 a[108:111], %0 offset:1728" :  : "v"(bb) : "memory"); SB();
  asm volatile("ds_read2_b32 %0, %1 offset0:176 offset1:177" : "=&v"(cA[0][2][0]) : "v"(aa[1]) : "memory"); SB();
  asm volatile("" : "+v"(a1[0][0]), "+v"(a1[0][1]), "+v"(a1[0][2]), "+v"(a1[1][0]), "+v"(a1[1][1]), "+v"(a1[1][2]), "+v"(c1[0]), "+v"(c1[1]), "+v"(c1[2]), "+v"(co[0]), "+v"(co[1]), "+v"(co[2]) :  : ); SB();
  c1[0] += co[0]; c1[1] += co[1]; c1[2] += co[2];
#pragma unroll
  for (int n = 0; n < 6; ++n) { a1[n / 3][n % 3] = __builtin_elementwise_fma((f32x4){-xm[n / 3], -xm[n / 3], -xm[n / 3], -xm[n / 3]}, c1[n % 3], a1[n / 3][n % 3]); asm volatile("" : "+v"(a1[n / 3][n % 3])); }
  SB();
  asm volatile("s_waitcnt vmcnt(11)" :  :  : "memory"); SB();
  asm volatile("ds_read_b128 a[72:75], %0 offset:4096" :  : "v"(aA[0]) : "memory"); SB();
  asm volatile("ds_read_b128 a[88:91], %0 offset:4096" :  : "v"(aA[1]) : "memory"); SB();
  asm volatile("buffer_load_dwordx4 v[172:175], %0, %1, %2 offen offset:1024 sc1" :  : "v"(vB), "s"(rWS), "s"(65536u) : "memory"); SB();
  asm volatile("v_xor_b32 %0, 131072, %0" : "+v"(vB) :  : "memory"); SB();
  asm volatile("buffer_load_dwordx4 v[176:179], %0, %1, %2 offen offset:0 sc1" :  : "v"(vB), "s"(rWS), "s"(0u) : "memory"); SB();
  asm volatile("s_waitcnt vmcnt(11)" :  :  : "memory"); SB();
  asm volatile("ds_read_b128 a[76:79], %0 offset:6144" :  : "v"(aA[0]) : "memory"); SB();
  asm volatile("ds_read_b128 a[92:95], %0 offset:6144" :  : "v"(aA[1]) : "memory"); SB();
  if (nch == 3) {
  asm volatile("s_waitcnt lgkmcnt(5)\n\tv_mfma_f32_16x16x32_bf16 %0, %1, a[124:127], 0" : "=&v"(ac[7]) : "v"(((u32x4){cA[0][0][0][0], cA[0][0][0][1], cA[0][0][1][0], cA[0][0][1][1]})) : ); SB();
  asm volatile("ds_read2_b32 %0, %1 offset0:178 offset1:179" : "=&v"(cA[0][2][1]) : "v"(aa[1]) : "memory"); SB();
  asm volatile("v_mfma_f32_16x16x32_bf16 %0, %1, a[120:123], 0" : "=&v"(ac[6]) : "v"(((u32x4){cA[0][0][0][0], cA[0][0][0][1], cA[0][0][1][0], cA[0][0][1][1]})) : ); SB();
  asm volatile("ds_read2_b32 %0, %1 offset0:48 offset1:49" : "=&v"(cA[0][3][0]) : "v"(aa[1]) : "memory"); SB();
  asm volatile("v_mfma_f32_16x16x32_bf16 %0, %1, a[116:119], 0" : "=&v"(ac[5]) : "v"(((u32x4){cA[0][0][0][0], cA[0][0][0][1], cA[0][0][1][0], cA[0][0][1][1]})) : ); SB();
  asm volatile("ds_read2_b32 %0, %1 offset0:50 offset1:51" : "=&v"(cA[0][3][1]) : "v"(aa[1]) : "memory"); SB();
  asm volatile("v_mfma_f32_16x16x32_bf16 %0, %1, a[112:115], 0" : "=&v"(ac[4]) : "v"(((u32x4){cA[0][0][0][0], cA[0][0][0][1], cA[0][0][1][0], cA[0][0][1][1]})) : ); SB();
  asm volatile("ds_read_b128 a[104:107], %0 offset:1216" :  : "v"(bb) : "memory"); SB();
  asm volatile("buffer_load_dwordx4 v[180:183], %0, %1, %2 offen offset:1024 sc1" :  : "v"(vB), "s"(rWS), "s"(0u) : "memory"); SB();
  asm volatile("v_mfma_f32_16x16x32_bf16 %0, %1, a[120:123], %0" : "+v"(ac[7]) : "v"(((u32x4){cA[0][1][0][0], cA[0][1][0][1], cA[0][1][1][0], cA[0][1][1][1]})) : ); SB();
  asm volatile("ds_read2_b32 %0, %1 offset0:176 offset1:177" : "=&v"(cA[0][4][0]) : "v"(aa[2]) : "memory"); SB();
  asm volatile("v_mfma_f32_16x16x32_bf16 %0, %1, a[116:119], %0" : "+v"(ac[6]) : "v"(((u32x4){cA[0][1][0][0], cA[0][1][0][1], cA[0][1][1][0], cA[0][1][1][1]})) : ); SB();
  asm volatile("ds_read2_b32 %0, %1 offset0:178 offset1:179" : "=&v"(cA[0][4][1]) : "v"(aa[2]) : "memory"); SB();
  asm volatile("v_mfma_f32_16x16x32_bf16 %0, %1, a[112:115], %0" : "+v"(ac[5]) : "v"(((u32x4){cA[0][1][0][0], cA[0][1][0][1], cA[0][1][1][0], cA[0][1][1][1]})) : ); SB();
  asm volatile("ds_read2_b32 %0, %1 offset0:48 offset1:49" : "=&v"(cA[0][5][0]) : "v"(aa[2]) : "memory"); SB();
  asm volatile("buffer_load_dwordx4 v[184:187], %0, %1, %2 offen offset:0 sc1" :  : "v"(vB), "s"(rWS), "s"(65536u) : "memory"); SB();
  asm volatile("v_mfma_f32_16x16x32_bf16 %0, %1, a[108:111], %0" : "+v"(ac[4]) : "v"(((u32x4){cA[0][1][0][0], cA[0][1][0][1], cA[0][1][1][0], cA[0][1][1][1]})) : ); SB();
  asm volatile("ds_read2_b32 %0, %1 offset0:50 offset1:51" : "=&v"(cA[0][5][1]) : "v"(aa[2]) : "memory"); SB();
  asm volatile("s_waitcnt lgkmcnt(7)\n\tv_mfma_f32_16x16x32_bf16 %0, %1, a[116:119], %0" : "+v"(ac[7]) : "v"(((u32x4){cA[0][2][0][0], cA[0][2][0][1], cA[0][2][1][0], cA[0][2][1][1]})) : ); SB();
  asm volatile("ds_read_b128 a[100:103], %0 offset:704" :  : "v"(bb) : "memory"); SB();
  asm volatile("v_mfma_f32_16x16x32_bf16 %0, %1, a[112:115], %0" : "+v"(ac[6]) : "v"(((u32x4){cA[0][2][0][0], cA[0][2][0][1], cA[0][2][1][0], cA[0][2][1][1]})) : ); SB();
  asm volatile("ds_read2_b32 %0, %1 offset0:176 offset1:177" : "=&v"(cA[0][6][0]) : "v"(aa[3]) : "memory"); SB();
  asm volatile("v_mfma_f32_16x16x32_bf16 %0, %1, a[108:111], %0" : "+v"(ac[5]) : "v"(((u32x4){cA[0][2][0][0], cA[0][2][0][1], cA[0][2][1][0], cA[0][2][1][1]})) : ); SB();
  asm volatile("ds_read2_b32 %0, %1 offset0:178 offset1:179" : "=&v"(cA[0][6][1]) : "v"(aa[3]) : "memory"); SB();
  asm volatile("buffer_load_dwordx4 v[188:191], %0, %1, %2 offen offset:1024 sc1" :  : "v"(vB), "s"(rWS), "s"(65536u) : "memory"); SB();
  asm volatile("v_mfma_f32_16x16x32_bf16 %0, %1, a[108:111], 0" : "=&v"(ac[3]) : "v"(((u32x4){cA[0][0][0][0], cA[0][0][0][1], cA[0][0][1][0], cA[0][0][1][1]})) : ); SB();
  asm volatile("ds_read2_b32 %0, %1 offset0:48 offset1:49" : "=&v"(cA[0][7][0]) : "v"(aa[3]) : "memory"); SB();
  asm volatile("s_waitcnt lgkmcnt(9)\n\tv_mfma_f32_16x16x32_bf16 %0, %1, a[112:115], %0" : "+v"(ac[7]) : "v"(((u32x4){cA[0][3][0][0], cA[0][3][0][1], cA[0][3][1][0], cA[0][3][1][1]})) : ); SB();
  asm volatile("ds_read2_b32 %0, %1 offset0:50 offset1:51" : "=&v"(cA[0][7][1]) : "v"(aa[3]) : "memory"); SB();
  asm volatile("v_mfma_f32_16x16x32_bf16 %0, %1, a[108:111], %0" : "+v"(ac[6]) : "v"(((u32x4){cA[0][3][0][0], cA[0][3][0][1], cA[0][3][1][0], cA[0][3][1][1]})) : ); SB();
  asm volatile("ds_read_b128 a[96:99], %0 offset:192" :  : "v"(bb) : "memory"); SB();
  asm volatile("v_xor_b32 %0, 393216, %0" : "+v"(vB) :  : "memory"); SB();
  asm volatile("s_waitcnt lgkmcnt(8)\n\tv_mfma_f32_16x16x32_bf16 %0, %1, a[104:107], %0" : "+v"(ac[5]) : "v"(((u32x4){cA[0][3][0][0], cA[0][3][0][1], cA[0][3][1][0], cA[0][3][1][1]})) : ); SB();
  asm volatile("ds_read2_b32 %0, %1 offset0:160 offset1:161" : "=&v"(cA[1][0][0]) : "v"(aa[0]) : "memory"); SB();
  asm volatile("buffer_load_dwordx4 v[192:195], %0, %1, %2 offen offset:0 sc1" :  : "v"(vB), "s"(rWS), "s"(0u) : "memory"); SB();
  asm volatile("v_mfma_f32_16x16x32_bf16 %0, %1, a[104:107], %0" : "+v"(ac[4]) : "v"(((u32x4){cA[0][2][0][0], cA[0][2][0][1], cA[0][2][1][0], cA[0][2][1][1]})) : ); SB();
  asm volatile("buffer_load_dwordx4 v[196:199], %0, %1, %2 offen offset:1024 sc1" :  : "v"(vB), "s"(rWS), "s"(0u) : "memory"); SB();
  asm volatile("v_mfma_f32_16x16x32_bf16 %0, %1, a[108:111], %0" : "+v"(ac[7]) : "v"(((u32x4){cA[0][4][0][0], cA[0][4][0][1], cA[0][4][1][0], cA[0][4][1][1]})) : ); SB();
  asm volatile("ds_read2_b32 %0, %1 offset0:162 offset1:163" : "=&v"(cA[1][0][1]) : "v"(aa[0]) : "memory"); SB();
  asm volatile("v_mfma_f32_16x16x32_bf16 %0, %1, a[104:107], %0" : "+v"(ac[6]) : "v"(((u32x4){cA[0][4][0][0], cA[0][4][0][1], cA[0][4][1][0], cA[0][4][1][1]})) : ); SB();
  asm volatile("ds_read_b128 a[156:159], %0 offset:3712" :  : "v"(bb) : "memory"); SB();
  asm volatile("v_mfma_f32_16x16x32_bf16 %0, %1, a[104:107], %0" : "+v"(ac[3]) : "v"(((u32x4){cA[0][1][0][0], cA[0][1][0][1], cA[0][1][1][0], cA[0][1][1][1]})) : ); SB();
  asm volatile("ds_read_b128 a[152:155], %0 offset:3200" :  : "v"(bb) : "memory"); SB();
  asm volatile("v_mfma_f32_16x16x32_bf16 %0, %1, a[104:107], 0" : "=&v"(ac[2]) : "v"(((u32x4){cA[0][0][0][0], cA[0][0][0][1], cA[0][0][1][0], cA[0][0][1][1]})) : ); SB();
  asm volatile("buffer_load_dwordx4 v[200:203], %0, %1, %2 offen offset:0 sc1" :  : "v"(vB), "s"(rWS), "s"(65536u) : "memory"); SB();
  asm volatile("s_waitcnt lgkmcnt(6)\n\tv_mfma_f32_16x16x32_bf16 %0, %1, a[104:107], %0" : "+v"(ac[7]) : "v"(((u32x4){cA[0][5][0][0], cA[0][5][0][1], cA[0][5][1][0], cA[0][5][1][1]})) : ); SB();
  asm volatile("ds_read_b128 a[128:131], %0 offset:128" :  : "v"(bb) : "memory"); SB();
  asm volatile("v_mfma_f32_16x16x32_bf16 %0, %1, a[100:103], %0" : "+v"(ac[6]) : "v"(((u32x4){cA[0][5][0][0], cA[0][5][0][1], cA[0][5][1][0], cA[0][5][1][1]})) : ); SB();
  asm volatile("ds_read_b128 a[132:135], %0 offset:640" :  : "v"(bb) : "memory"); SB();
  asm volatile("v_mfma_f32_16x16x32_bf16 %0, %1, a[100:103], %0" : "+v"(ac[5]) : "v"(((u32x4){cA[0][4][0][0], cA[0][4][0][1], cA[0][4][1][0], cA[0][4][1][1]})) : ); SB();
  asm volatile("ds_read2_b32 %0, %1 offset0:32 offset1:33" : "=&v"(cA[1][1][0]) : "v"(aa[0]) : "memory"); SB();
  asm volatile("v_mfma_f32_16x16x32_bf16 %0, %1, a[100:103], %0" : "+v"(ac[4]) : "v"(((u32x4){cA[0][3][0][0], cA[0][3][0][1], cA[0][3][1][0], cA[0][3][1][1]})) : ); SB();
  asm volatile("buffer_load_dwordx4 v[204:207], %0, %1, %2 offen offset:1024 sc1" :  : "v"(vB), "s"(rWS), "s"(65536u) : "memory"); SB();
  asm volatile("v_mfma_f32_16x16x32_bf16 %0, %1, a[100:103], %0" : "+v"(ac[7]) : "v"(((u32x4){cA[0][6][0][0], cA[0][6][0][1], cA[0][6][1][0], cA[0][6][1][1]})) : ); SB();
  asm volatile("ds_read2_b32 %0, %1 offset0:34 offset1:35" : "=&v"(cA[1][1][1]) : "v"(aa[0]) : "memory"); SB();
  asm volatile("v_mfma_f32_16x16x32_bf16 %0, %1, a[100:103], %0" : "+v"(ac[3]) : "v"(((u32x4){cA[0][2][0][0], cA[0][2][0][1], cA[0][2][1][0], cA[0][2][1][1]})) : ); SB();
  asm volatile("ds_read_b128 a[148:151], %0 offset:2688" :  : "v"(bb) : "memory"); SB();
  asm volatile("v_mfma_f32_16x16x32_bf16 %0, %1, a[100:103], %0" : "+v"(ac[2]) : "v"(((u32x4){cA[0][1][0][0], cA[0][1][0][1], cA[0][1][1][0], cA[0][1][1][1]})) : ); SB();
  asm volatile("ds_read_b128 a[136:139], %0 offset:1152" :  : "v"(bb) : "memory"); SB();
  asm volatile("v_mfma_f32_16x16x32_bf16 %0, %1, a[100:103], 0" : "=&v"(ac[1]) : "v"(((u32x4){cA[0][0][0][0], cA[0][0][0][1], cA[0][0][1][0], cA[0][0][1][1]})) : ); SB();
  asm volatile("v_xor_b32 %0, 262144, %0" : "+v"(vB) :  : "memory"); SB();
  asm volatile("s_waitcnt lgkmcnt(7)\n\tv_mfma_f32_16x16x32_bf16 %0, %1, a[96:99], %0" : "+v"(ac[7]) : "v"(((u32x4){cA[0][7][0][0], cA[0][7][0][1], cA[0][7][1][0], cA[0][7][1][1]})) : ); SB();
  asm volatile("ds_read2_b32 %0, %1 offset0:160 offset1:161" : "=&v"(cA[1][2][0]) : "v"(aa[1]) : "memory"); SB();
  asm volatile("buffer_load_dwordx4 v[208:211], %0, %1, %2 offen offset:0 sc1" :  : "v"(vB), "s"(rWS), "s"(2048u) : "memory"); SB();
  asm volatile("v_mfma_f32_16x16x32_bf16 %0, %1, a[96:99], %0" : "+v"(ac[6]) : "v"(((u32x4){cA[0][6][0][0], cA[0][6][0][1], cA[0][6][1][0], cA[0][6][1][1]})) : ); SB();
  asm volatile("ds_read2_b32 %0, %1 offset0:162 offset1:163" : "=&v"(cA[1][2][1]) : "v"(aa[1]) : "memory"); SB();
  asm volatile("v_mfma_f32_16x16x32_bf16 %0, %1, a[96:99], %0" : "+v"(ac[5]) : "v"(((u32x4){cA[0][5][0][0], cA[0][5][0][1], cA[0][5][1][0], cA[0][5][1][1]})) : ); SB();
  asm volatile("ds_read_b128 a[140:143], %0 offset:1664" :  : "v"(bb) : "memory"); SB();
  asm volatile("buffer_load_dwordx4 v[212:215], %0, %1, %2 offen offset:1024 sc1" :  : "v"(vB), "s"(rWS), "s"(2048u) : "memory"); SB();
  asm volatile("v_mfma_f32_16x16x32_bf16 %0, %1, a[96:99], %0" : "+v"(ac[4]) : "v"(((u32x4){cA[0][4][0][0], cA[0][4][0][1], cA[0][4][1][0], cA[0][4][1][1]})) : ); SB();
  asm volatile("ds_write_b128 %0, %1 offset:0" :  : "v"(ew[0]), "v"(a1[0][0]) : "memory"); SB();
  asm volatile("v_mfma_f32_16x16x32_bf16 %0, %1, a[96:99], %0" : "+v"(ac[3]) : "v"(((u32x4){cA[0][3][0][0], cA[0][3][0][1], cA[0][3][1][0], cA[0][3][1][1]})) : ); SB();
  asm volatile("ds_read_b128 a[144:147], %0 offset:2176" :  : "v"(bb) : "memory"); SB();
  asm volatile("v_mfma_f32_16x16x32_bf16 %0, %1, a[96:99], %0" : "+v"(ac[2]) : "v"(((u32x4){cA[0][2][0][0], cA[0][2][0][1], cA[0][2][1][0], cA[0][2][1][1]})) : ); SB();
  asm volatile("ds_read2_b32 %0, %1 offset0:32 offset1:33" : "=&v"(cA[1][3][0]) : "v"(aa[1]) : "memory"); SB();
  asm volatile("v_mfma_f32_16x16x32_bf16 %0, %1, a[96:99], %0" : "+v"(ac[1]) : "v"(((u32x4){cA[0][1][0][0], cA[0][1][0][1], cA[0][1][1][0], cA[0][1][1][1]})) : ); SB();
  asm volatile("ds_read2_b32 %0, %1 offset0:34 offset1:35" : "=&v"(cA[1][3][1]) : "v"(aa[1]) : "memory"); SB();
  asm volatile("v_mfma_f32_16x16x32_bf16 %0, %1, a[96:99], 0" : "=&v"(ac[0]) : "v"(((u32x4){cA[0][0][0][0], cA[0][0][0][1], cA[0][0][1][0], cA[0][0][1][1]})) : ); SB();
  asm volatile("buffer_load_dwordx4 v[216:219], %0, %1, %2 offen offset:0 sc1" :  : "v"(vB), "s"(rWS), "s"(67584u) : "memory"); SB();
  asm volatile("v_mfma_f32_16x16x32_bf16 %0, %1, a[156:159], %0" : "+v"(ac[7]) : "v"(((u32x4){cA[1][0][0][0], cA[1][0][0][1], cA[1][0][1][0], cA[1][0][1][1]})) : ); SB();
  asm volatile("ds_read2_b32 %0, %1 offset0:160 offset1:161" : "=&v"(cA[1][4][0]) : "v"(aa[2]) : "memory"); SB();
  asm volatile("s_waitcnt lgkmcnt(7)\n\tv_mfma_f32_16x16x32_bf16 %0, %1, a[152:155], %0" : "+v"(ac[6]) : "v"(((u32x4){cA[1][0][0][0], cA[1][0][0][1], cA[1][0][1][0], cA[1][0][1][1]})) : ); SB();
  asm volatile("ds_read2_b32 %0, %1 offset0:162 offset1:163" : "=&v"(cA[1][4][1]) : "v"(aa[2]) : "memory"); SB();
  asm volatile("v_mfma_f32_16x16x32_bf16 %0, %1, a[128:131], %0" : "+v"(ac[0]) : "v"(((u32x4){cA[1][0][0][0], cA[1][0][0][1], cA[1][0][1][0], cA[1][0][1][1]})) : ); SB();
  asm volatile("ds_read2_b32 %0, %1 offset0:32 offset1:33" : "=&v"(cA[1][5][0]) : "v"(aa[2]) : "memory"); SB();
  asm volatile("v_mfma_f32_16x16x32_bf16 %0, %1, a[132:135], %0" : "+v"(ac[1]) : "v"(((u32x4){cA[1][0][0][0], cA[1][0][0][1], cA[1][0][1][0], cA[1][0][1][1]})) : ); SB();
  asm volatile("buffer_load_dwordx4 v[220:223], %0, %1, %2 offen offset:1024 sc1" :  : "v"(vB), "s"(rWS), "s"(67584u) : "memory"); SB();
  asm volatile("v_mfma_f32_16x16x32_bf16 %0, %1, a[152:155], %0" : "+v"(ac[7]) : "v"(((u32x4){cA[1][1][0][0], cA[1][1][0][1], cA[1][1][1][0], cA[1][1][1][1]})) : ); SB();
  asm volatile("ds_read2_b32 %0, %1 offset0:34 offset1:35" : "=&v"(cA[1][5][1]) : "v"(aa[2]) : "memory"); SB();
  asm volatile("v_mfma_f32_16x16x32_bf16 %0, %1, a[148:151], %0" : "+v"(ac[6]) : "v"(((u32x4){cA[1][1][0][0], cA[1][1][0][1], cA[1][1][1][0], cA[1][1][1][1]})) : ); SB();
  asm volatile("ds_read2_b32 %0, %1 offset0:160 offset1:161" : "=&v"(cA[1][6][0]) : "v"(aa[3]) : "memory"); SB();
  asm volatile("v_mfma_f32_16x16x32_bf16 %0, %1, a[136:139], %0" : "+v"(ac[2]) : "v"(((u32x4){cA[1][0][0][0], cA[1][0][0][1], cA[1][0][1][0], cA[1][0][1][1]})) : ); SB();
  asm volatile("ds_read2_b32 %0, %1 offset0:162 offset1:163" : "=&v"(cA[1][6][1]) : "v"(aa[3]) : "memory"); SB();
  asm volatile("v_mfma_f32_16x16x32_bf16 %0, %1, a[128:131], %0" : "+v"(ac[1]) : "v"(((u32x4){cA[1][1][0][0], cA[1][1][0][1], cA[1][1][1][0], cA[1][1][1][1]})) : ); SB();
  asm volatile("ds_write_b128 %0, %1 offset:0" :  : "v"(dd), "v"(ac[0]) : "memory"); SB();
  asm volatile("s_waitcnt lgkmcnt(7)\n\tv_mfma_f32_16x16x32_bf16 %0, %1, a[148:151], %0" : "+v"(ac[7]) : "v"(((u32x4){cA[1][2][0][0], cA[1][2][0][1], cA[1][2][1][0], cA[1][2][1][1]})) : ); SB();
  asm volatile("ds_read2_b32 %0, %1 offset0:32 offset1:33" : "=&v"(cA[1][7][0]) : "v"(aa[3]) : "memory"); SB();
  asm volatile("v_mfma_f32_16x16x32_bf16 %0, %1, a[140:143], %0" : "+v"(ac[3]) : "v"(((u32x4){cA[1][0][0][0], cA[1][0][0][1], cA[1][0][1][0], cA[1][0][1][1]})) : ); SB();
  asm volatile("ds_read2_b32 %0, %1 offset0:34 offset1:35" : "=&v"(cA[1][7][1]) : "v"(aa[3]) : "memory"); SB();
  asm volatile("v_mfma_f32_16x16x32_bf16 %0, %1, a[132:135], %0" : "+v"(ac[2]) : "v"(((u32x4){cA[1][1][0][0], cA[1][1][0][1], cA[1][1][1][0], cA[1][1][1][1]})) : ); SB();
  asm volatile("v_xor_b32 %0, 131072, %0" : "+v"(vB) :  : "memory"); SB();
  asm volatile("v_mfma_f32_16x16x32_bf16 %0, %1, a[144:147], %0" : "+v"(ac[4]) : "v"(((u32x4){cA[1][0][0][0], cA[1][0][0][1], cA[1][0][1][0], cA[1][0][1][1]})) : ); SB();
  asm volatile("ds_write_b128 %0, %1 offset:1024" :  : "v"(dd), "v"(ac[1]) : "memory"); SB();
  asm volatile("v_mfma_f32_16x16x32_bf16 %0, %1, a[144:147], %0" : "+v"(ac[7]) : "v"(((u32x4){cA[1][3][0][0], cA[1][3][0][1], cA[1][3][1][0], cA[1][3][1][1]})) : ); SB();
  asm volatile("buffer_load_dwordx4 v[224:227], %0, %1, %2 offen offset:0 sc1" :  : "v"(vB), "s"(rWS), "s"(2048u) : "memory"); SB();
  asm volatile("v_mfma_f32_16x16x32_bf16 %0, %1, a[136:139], %0" : "+v"(ac[3]) : "v"(((u32x4){cA[1][1][0][0], cA[1][1][0][1], cA[1][1][1][0], cA[1][1][1][1]})) : ); SB();
  asm volatile("buffer_load_dwordx4 v[228:231], %0, %1, %2 offen offset:1024 sc1" :  : "v"(vB), "s"(rWS), "s"(2048u) : "memory"); SB();
  asm volatile("v_mfma_f32_16x16x32_bf16 %0, %1, a[128:131], %0" : "+v"(ac[2]) : "v"(((u32x4){cA[1][2][0][0], cA[1][2][0][1], cA[1][2][1][0], cA[1][2][1][1]})) : ); SB();
  asm volatile("ds_write_b128 %0, %1 offset:1024" :  : "v"(ew[0]), "v"(a1[0][1]) : "memory"); SB();
  asm volatile("v_mfma_f32_16x16x32_bf16 %0, %1, a[140:143], %0" : "+v"(ac[4]) : "v"(((u32x4){cA[1][1][0][0], cA[1][1][0][1], cA[1][1][1][0], cA[1][1][1][1]})) : ); SB();
  asm volatile("buffer_load_dwordx4 v[232:235], %0, %1, %2 offen offset:0 sc1" :  : "v"(vB), "s"(rWS), "s"(67584u) : "memory"); SB();
  asm volatile("v_mfma_f32_16x16x32_bf16 %0, %1, a[148:151], %0" : "+v"(ac[5]) : "v"(((u32x4){cA[1][0][0][0], cA[1][0][0][1], cA[1][0][1][0], cA[1][0][1][1]})) : ); SB();
  asm volatile("ds_write_b128 %0, %1 offset:2048" :  : "v"(ew[0]), "v"(a1[0][2]) : "memory"); SB();
  asm volatile("v_mfma_f32_16x16x32_bf16 %0, %1, a[132:135], %0" : "+v"(ac[3]) : "v"(((u32x4){cA[1][2][0][0], cA[1][2][0][1], cA[1][2][1][0], cA[1][2][1][1]})) : ); SB();
  asm volatile("buffer_load_dwordx4 v[236:239], %0, %1, %2 offen offset:1024 sc1" :  : "v"(vB), "s"(rWS), "s"(67584u) : "memory"); SB();
  asm volatile("v_mfma_f32_16x16x32_bf16 %0, %1, a[144:147], %0" : "+v"(ac[6]) : "v"(((u32x4){cA[1][2][0][0], cA[1][2][0][1], cA[1][2][1][0], cA[1][2][1][1]})) : ); SB();
  asm volatile("ds_write_b128 %0, %1 offset:2048" :  : "v"(dd), "v"(ac[2]) : "memory"); SB();
  asm volatile("v_mfma_f32_16x16x32_bf16 %0, %1, a[136:139], %0" : "+v"(ac[4]) : "v"(((u32x4){cA[1][2][0][0], cA[1][2][0][1], cA[1][2][1][0], cA[1][2][1][1]})) : ); SB();
  asm volatile("v_xor_b32 %0, 393216, %0" : "+v"(vB) :  : "memory"); SB();
  asm volatile("v_mfma_f32_16x16x32_bf16 %0, %1, a[144:147], %0" : "+v"(ac[5]) : "v"(((u32x4){cA[1][1][0][0], cA[1][1][0][1], cA[1][1][1][0], cA[1][1][1][1]})) : ); SB();
  asm volatile("buffer_load_dwordx4 v[240:243], %0, %1, %2 offen offset:0 sc1" :  : "v"(vB), "s"(rWS), "s"(2048u) : "memory"); SB();
  asm volatile("v_mfma_f32_16x16x32_bf16 %0, %1, a[128:131], %0" : "+v"(ac[3]) : "v"(((u32x4){cA[1][3][0][0], cA[1][3][0][1], cA[1][3][1][0], cA[1][3][1][1]})) : ); SB();
  asm volatile("ds_write_b128 %0, %1 offset:0" :  : "v"(ew[1]), "v"(a1[1][0]) : "memory"); SB();
  asm volatile("v_mfma_f32_16x16x32_bf16 %0, %1, a[140:143], %0" : "+v"(ac[6]) : "v"(((u32x4){cA[1][3][0][0], cA[1][3][0][1], cA[1][3][1][0], cA[1][3][1][1]})) : ); SB();
  asm volatile("buffer_load_dwordx4 v[244:247], %0, %1, %2 offen offset:1024 sc1" :  : "v"(vB), "s"(rWS), "s"(2048u) : "memory"); SB();
  asm volatile("v_mfma_f32_16x16x32_bf16 %0, %1, a[132:135], %0" : "+v"(ac[4]) : "v"(((u32x4){cA[1][3][0][0], cA[1][3][0][1], cA[1][3][1][0], cA[1][3][1][1]})) : ); SB();
  asm volatile("ds_write_b128 %0, %1 offset:1024" :  : "v"(ew[1]), "v"(a1[1][1]) : "memory"); SB();
  asm volatile("v_mfma_f32_16x16x32_bf16 %0, %1, a[140:143], %0" : "+v"(ac[5]) : "v"(((u32x4){cA[1][2][0][0], cA[1][2][0][1], cA[1][2][1][0], cA[1][2][1][1]})) : ); SB();
  asm volatile("s_waitcnt lgkmcnt(3)\n\tv_mfma_f32_16x16x32_bf16 %0, %1, a[140:143], %0" : "+v"(ac[7]) : "v"(((u32x4){cA[1][4][0][0], cA[1][4][0][1], cA[1][4][1][0], cA[1][4][1][1]})) : ); SB();
  asm volatile("ds_write_b128 %0, %1 offset:3072" :  : "v"(dd), "v"(ac[3]) : "memory"); SB();
  asm volatile("v_mfma_f32_16x16x32_bf16 %0, %1, a[136:139], %0" : "+v"(ac[6]) : "v"(((u32x4){cA[1][4][0][0], cA[1][4][0][1], cA[1][4][1][0], cA[1][4][1][1]})) : ); SB();
  asm volatile("buffer_load_dwordx4 v[248:251], %0, %1, %2 offen offset:0 sc1" :  : "v"(vB), "s"(rWS), "s"(67584u) : "memory"); SB();
  asm volatile("v_mfma_f32_16x16x32_bf16 %0, %1, a[128:131], %0" : "+v"(ac[4]) : "v"(((u32x4){cA[1][4][0][0], cA[1][4][0][1], cA[1][4][1][0], cA[1][4][1][1]})) : ); SB();
  asm volatile("buffer_load_dwordx4 v[252:255], %0, %1, %2 offen offset:1024 sc1" :  : "v"(vB), "s"(rWS), "s"(67584u) : "memory"); SB();
  asm volatile("v_mfma_f32_16x16x32_bf16 %0, %1, a[136:139], %0" : "+v"(ac[5]) : "v"(((u32x4){cA[1][3][0][0], cA[1][3][0][1], cA[1][3][1][0], cA[1][3][1][1]})) : ); SB();
  asm volatile("v_xor_b32 %0, 131072, %0" : "+v"(vB) :  : "memory"); SB();
  asm volatile("v_mfma_f32_16x16x32_bf16 %0, %1, a[136:139], %0" : "+v"(ac[7]) : "v"(((u32x4){cA[1][5][0][0], cA[1][5][0][1], cA[1][5][1][0], cA[1][5][1][1]})) : ); SB();
  asm volatile("ds_write_b128 %0, %1 offset:2048" :  : "v"(ew[1]), "v"(a1[1][2]) : "memory"); SB();
  asm volatile("v_mfma_f32_16x16x32_bf16 %0, %1, a[132:135], %0" : "+v"(ac[6]) : "v"(((u32x4){cA[1][5][0][0], cA[1][5][0][1], cA[1][5][1][0], cA[1][5][1][1]})) : ); SB();
  asm volatile("buffer_load_dwordx4 %0, %1, %2 offen offset:2048 lds" :  : "v"(voS), "s"(rA), "s"(ao[6]) : "memory"); SB();
  asm volatile("v_mfma_f32_16x16x32_bf16 %0, %1, a[132:135], %0" : "+v"(ac[5]) : "v"(((u32x4){cA[1][4][0][0], cA[1][4][0][1], cA[1][4][1][0], cA[1][4][1][1]})) : ); SB();
  asm volatile("ds_write_b128 %0, %1 offset:4096" :  : "v"(dd), "v"(ac[4]) : "memory"); SB();
  asm volatile("v_mfma_f32_16x16x32_bf16 %0, %1, a[132:135], %0" : "+v"(ac[7]) : "v"(((u32x4){cA[1][6][0][0], cA[1][6][0][1], cA[1][6][1][0], cA[1][6][1][1]})) : ); SB();
  asm volatile("buffer_load_dwordx4 %0, %1, %2 offen offset:3072 lds" :  : "v"(voS), "s"(rA), "s"(ao[7]) : "memory"); SB();
  asm volatile("v_mfma_f32_16x16x32_bf16 %0, %1, a[128:131], %0" : "+v"(ac[6]) : "v"(((u32x4){cA[1][6][0][0], cA[1][6][0][1], cA[1][6][1][0], cA[1][6][1][1]})) : ); SB();
  asm volatile("v_xor_b32 %0, 128, %0" : "+v"(voS) :  : "memory"); SB();
  asm volatile("v_mfma_f32_16x16x32_bf16 %0, %1, a[128:131], %0" : "+v"(ac[5]) : "v"(((u32x4){cA[1][5][0][0], cA[1][5][0][1], cA[1][5][1][0], cA[1][5][1][1]})) : ); SB();
  asm volatile("v_mfma_f32_16x16x32_bf16 %0, %1, a[128:131], %0" : "+v"(ac[7]) : "v"(((u32x4){cA[1][7][0][0], cA[1][7][0][1], cA[1][7][1][0], cA[1][7][1][1]})) : ); SB();
  asm volatile("s_add_u32 m0, %0, 98304" :  : "s"(mA) : "memory", "scc"); SB();
  asm volatile("s_nop 0\n\tbuffer_load_dwordx4 %0, %1, %2 offen lds" :  : "v"(voS), "s"(rA), "s"(ao[0]) : "memory"); SB();
  asm volatile("s_nop 5" : "+v"(ac[5]), "+v"(ac[6]), "+v"(ac[7]) :  : ); SB();
  asm volatile("ds_write_b128 %0, %1 offset:6144" :  : "v"(dd), "v"(ac[6]) : "memory"); SB();
  asm volatile("ds_write_b128 %0, %1 offset:5120" :  : "v"(dd), "v"(ac[5]) : "memory"); SB();
  asm volatile("ds_write_b128 %0, %1 offset:7168" :  : "v"(dd), "v"(ac[7]) : "memory"); SB();
  } else {
  asm volatile("; ELSE_BEGIN"); SB();
  asm volatile("s_waitcnt lgkmcnt(0)" ::: "memory"); SB();
  asm volatile("buffer_load_dwordx4 v[180:183], %0, %1, %2 offen offset:1024 sc1" :  : "v"(vB), "s"(rWS), "s"(0u) : "memory"); SB();
  asm volatile("s_waitcnt lgkmcnt(0)" ::: "memory"); SB();
  asm volatile("buffer_load_dwordx4 v[184:187], %0, %1, %2 offen offset:0 sc1" :  : "v"(vB), "s"(rWS), "s"(65536u) : "memory"); SB();
  asm volatile("s_waitcnt lgkmcnt(0)" ::: "memory"); SB();
  asm volatile("buffer_load_dwordx4 v[188:191], %0, %1, %2 offen offset:1024 sc1" :  : "v"(vB), "s"(rWS), "s"(65536u) : "memory"); SB();
  asm volatile("s_waitcnt lgkmcnt(0)" ::: "memory"); SB();
  asm volatile("v_xor_b32 %0, 393216, %0" : "+v"(vB) :  : "memory"); SB();
  asm volatile("s_waitcnt lgkmcnt(0)" ::: "memory"); SB();
  asm volatile("buffer_load_dwordx4 v[192:195], %0, %1, %2 offen offset:0 sc1" :  : "v"(vB), "s"(rWS), "s"(0u) : "memory"); SB();
  asm volatile("s_waitcnt lgkmcnt(0)" ::: "memory"); SB();
  asm volatile("buffer_load_dwordx4 v[196:199], %0, %1, %2 offen offset:1024 sc1" :  : "v"(vB), "s"(rWS), "s"(0u) : "memory"); SB();
  asm volatile("s_waitcnt lgkmcnt(0)" ::: "memory"); SB();
  asm volatile("buffer_load_dwordx4 v[200:203], %0, %1, %2 offen offset:0 sc1" :  : "v"(vB), "s"(rWS), "s"(65536u) : "memory"); SB();
  asm volatile("s_waitcnt lgkmcnt(0)" ::: "memory"); SB();
  asm volatile("buffer_load_dwordx4 v[204:207], %0, %1, %2 offen offset:1024 sc1" :  : "v"(vB), "s"(rWS), "s"(65536u) : "memory"); SB();
  asm volatile("s_waitcnt lgkmcnt(0)" ::: "memory"); SB();
  asm volatile("v_xor_b32 %0, 262144, %0" : "+v"(vB) :  : "memory"); SB();
  asm volatile("s_waitcnt lgkmcnt(0)" ::: "memory"); SB();
  asm volatile("buffer_load_dwordx4 v[208:211], %0, %1, %2 offen offset:0 sc1" :  : "v"(vB), "s"(rWS), "s"(2048u) : "memory"); SB();
  asm volatile("s_waitcnt lgkmcnt(0)" ::: "memory"); SB();
  asm volatile("buffer_load_dwordx4 v[212:215], %0, %1, %2 offen offset:1024 sc1" :  : "v"(vB), "s"(rWS), "s"(2048u) : "memory"); SB();
  asm volatile("s_waitcnt lgkmcnt(0)" ::: "memory"); SB();
  asm volatile("ds_write_b128 %0, %1 offset:0" :  : "v"(ew[0]), "v"(a1[0][0]) : "memory"); SB();
  asm volatile("s_waitcnt lgkmcnt(0)" ::: "memory"); SB();
  asm volatile("buffer_load_dwordx4 v[216:219], %0, %1, %2 offen offset:0 sc1" :  : "v"(vB), "s"(rWS), "s"(67584u) : "memory"); SB();
  asm volatile("s_waitcnt lgkmcnt(0)" ::: "memory"); SB();
  asm volatile("buffer_load_dwordx4 v[220:223], %0, %1, %2 offen offset:1024 sc1" :  : "v"(vB), "s"(rWS), "s"(67584u) : "memory"); SB();
  asm volatile("s_waitcnt lgkmcnt(0)" ::: "memory"); SB();
  asm volatile("v_xor_b32 %0, 131072, %0" : "+v"(vB) :  : "memory"); SB();
  asm volatile("s_waitcnt lgkmcnt(0)" ::: "memory"); SB();
  asm volatile("buffer_load_dwordx4 v[224:227], %0, %1, %2 offen offset:0 sc1" :  : "v"(vB), "s"(rWS), "s"(2048u) : "memory"); SB();
  asm volatile("s_waitcnt lgkmcnt(0)" ::: "memory"); SB();
  asm volatile("buffer_load_dwordx4 v[228:231], %0, %1, %2 offen offset:1024 sc1" :  : "v"(vB), "s"(rWS), "s"(2048u) : "memory"); SB();
  asm volatile("s_waitcnt lgkmcnt(0)" ::: "memory"); SB();
  asm volatile("ds_write_b128 %0, %1 offset:1024" :  : "v"(ew[0]), "v"(a1[0][1]) : "memory"); SB();
  asm volatile("s_waitcnt lgkmcnt(0)" ::: "memory"); SB();
  asm volatile("buffer_load_dwordx4 v[232:235], %0, %1, %2 offen offset:0 sc1" :  : "v"(vB), "s"(rWS), "s"(67584u) : "memory"); SB();
  asm volatile("s_waitcnt lgkmcnt(0)" ::: "memory"); SB();
  asm volatile("ds_write_b128 %0, %1 offset:2048" :  : "v"(ew[0]), "v"(a1[0][2]) : "memory"); SB();
  asm volatile("s_waitcnt lgkmcnt(0)" ::: "memory"); SB();
  asm volatile("buffer_load_dwordx4 v[236:239], %0, %1, %2 offen offset:1024 sc1" :  : "v"(vB), "s"(rWS), "s"(67584u) : "memory"); SB();
  asm volatile("s_waitcnt lgkmcnt(0)" ::: "memory"); SB();
  asm volatile("v_xor_b32 %0, 393216, %0" : "+v"(vB) :  : "memory"); SB();
  asm volatile("s_waitcnt lgkmcnt(0)" ::: "memory"); SB();
  asm volatile("buffer_load_dwordx4 v[240:243], %0, %1, %2 offen offset:0 sc1" :  : "v"(vB), "s"(rWS), "s"(2048u) : "memory"); SB();
  asm volatile("s_waitcnt lgkmcnt(0)" ::: "memory"); SB();
  asm volatile("ds_write_b128 %0, %1 offset:0" :  : "v"(ew[1]), "v"(a1[1][0]) : "memory"); SB();
  asm volatile("s_waitcnt lgkmcnt(0)" ::: "memory"); SB();
  asm volatile("buffer_load_dwordx4 v[244:247], %0, %1, %2 offen offset:1024 sc1" :  : "v"(vB), "s"(rWS), "s"(2048u) : "memory"); SB();
  asm volatile("s_waitcnt lgkmcnt(0)" ::: "memory"); SB();
  asm volatile("ds_write_b128 %0, %1 offset:1024" :  : "v"(ew[1]), "v"(a1[1][1]) : "memory"); SB();
  asm volatile("s_waitcnt lgkmcnt(0)" ::: "memory"); SB();
  asm volatile("buffer_load_dwordx4 v[248:251], %0, %1, %2 offen offset:0 sc1" :  : "v"(vB), "s"(rWS), "s"(67584u) : "memory"); SB();
  asm volatile("s_waitcnt lgkmcnt(0)" ::: "memory"); SB();
  asm volatile("buffer_load_dwordx4 v[252:255], %0, %1, %2 offen offset:1024 sc1" :  : "v"(vB), "s"(rWS), "s"(67584u) : "memory"); SB();
  asm volatile("s_waitcnt lgkmcnt(0)" ::: "memory"); SB();
  asm volatile("v_xor_b32 %0, 131072, %0" : "+v"(vB) :  : "memory"); SB();
  asm volatile("s_waitcnt lgkmcnt(0)" ::: "memory"); SB();
  asm volatile("ds_write_b128 %0, %1 offset:2048" :  : "v"(ew[1]), "v"(a1[1][2]) : "memory"); SB();
  asm volatile("s_waitcnt lgkmcnt(0)" ::: "memory"); SB();
  asm volatile("s_waitcnt lgkmcnt(0)" ::: "memory"); SB();
  asm volatile("buffer_load_dwordx4 %0, %1, %2 offen offset:2048 lds" :  : "v"(voS), "s"(rA), "s"(ao[6]) : "memory"); SB();
  asm volatile("s_waitcnt lgkmcnt(0)" ::: "memory"); SB();
  asm volatile("s_waitcnt lgkmcnt(0)" ::: "memory"); SB();
  asm volatile("buffer_load_dwordx4 %0, %1, %2 offen offset:3072 lds" :  : "v"(voS), "s"(rA), "s"(ao[7]) : "memory"); SB();
  asm volatile("s_waitcnt lgkmcnt(0)" ::: "memory"); SB();
  asm volatile("s_waitcnt lgkmcnt(0)" ::: "memory"); SB();
  asm volatile("v_xor_b32 %0, 128, %0" : "+v"(voS) :  : "memory"); SB();
  asm volatile("s_waitcnt lgkmcnt(0)" ::: "memory"); SB();
  asm volatile("s_add_u32 m0, %0, 98304" :  : "s"(mA) : "memory", "scc"); SB();
  asm volatile("s_waitcnt lgkmcnt(0)" ::: "memory"); SB();
  asm volatile("s_nop 0\n\tbuffer_load_dwordx4 %0, %1, %2 offen lds" :  : "v"(voS), "s"(rA), "s"(ao[0]) : "memory"); SB();
  asm volatile("s_waitcnt lgkmcnt(0) ; ELSE_END" ::: "memory"); SB();
  }
  ST(15);
  f32x4 g[2][4][2], pr[6], yq[8], bs4 = {bs, bs, bs, bs};
  u32x4 ho[3], ykv, y2;
  u32x2 xv[5];
  float gt[4], ge[4];
  unsigned vH_, pw2, pe[3], vX, vO, vFH, vFY, vHr, hso[8], ys2, ydr, ykr, er, sFH, sFY;
  int dl;
  asm volatile("" : "+v"(bs4)); SB();
  asm volatile("s_waitcnt vmcnt(25)" :  :  : "memory"); SB();
  asm volatile("v_mfma_f32_16x16x32_bf16 %0, v[160:163], a[160:163], %1" : "=&v"(g[0][0][0]) : "v"(bs4) : ); SB();
  { unsigned lane = lane0, w = w0, rb = rb0, cb = cb0;
  asm volatile("" : "+v"(lane)); asm volatile("" : "+s"(w), "+s"(rb), "+s"(cb)); SB();

  asm volatile("v_mfma_f32_16x16x32_bf16 %0, v[160:163], a[164:167], 0" : "=&v"(g[0][1][0]) :  : ); SB();
  er = lb + 32768 + w * 6144 + l16; asm volatile("" : "+v"(er)); SB();
  asm volatile("v_mfma_f32_16x16x32_bf16 %0, v[160:163], a[168:171], 0" : "=&v"(g[0][2][0]) :  : ); SB();
  asm volatile("s_waitcnt lgkmcnt(0)\n\ts_barrier" :  :  : "memory"); SB();
  asm volatile("ds_read_b128 %0, %1 offset:0" : "=&v"(a1[0][0]) : "v"(er) : "memory"); SB();
  asm volatile("v_mfma_f32_16x16x32_bf16 %0, v[160:163], a[172:175], 0" : "=&v"(g[0][3][0]) :  : ); SB();
  asm volatile("ds_read_b128 %0, %1 offset:1024" : "=&v"(a1[0][1]) : "v"(er) : "memory"); SB();
  asm volatile("v_mfma_f32_16x16x32_bf16 %0, v[164:167], a[160:163], %1" : "=&v"(g[0][0][1]) : "v"(bs4) : ); SB();
  asm volatile("ds_read_b128 %0, %1 offset:2048" : "=&v"(a1[0][2]) : "v"(er) : "memory"); SB();
  asm volatile("v_mfma_f32_16x16x32_bf16 %0, v[164:167], a[164:167], 0" : "=&v"(g[0][1][1]) :  : ); SB();
  asm volatile("ds_read_b128 %0, %1 offset:3072" : "=&v"(a1[1][0]) : "v"(er) : "memory"); SB();
  asm volatile("v_mfma_f32_16x16x32_bf16 %0, v[164:167], a[168:171], 0" : "=&v"(g[0][2][1]) :  : ); SB();
  asm volatile("ds_read_b128 %0, %1 offset:4096" : "=&v"(a1[1][1]) : "v"(er) : "memory"); SB();
  asm volatile("v_mfma_f32_16x16x32_bf16 %0, v[164:167], a[172:175], 0" : "=&v"(g[0][3][1]) :  : ); SB();
  asm volatile("ds_read_b128 %0, %1 offset:5120" : "=&v"(a1[1][2]) : "v"(er) : "memory"); SB();
  ST(25);
  asm volatile("s_waitcnt vmcnt(23)" :  :  : "memory"); SB();
  asm volatile("v_mfma_f32_16x16x32_bf16 %0, v[168:171], a[176:179], %0" : "+v"(g[0][0][0]) :  : ); SB();
  ydr = lb + 0 + (w * 2 + (lane >> 5)) * 1024 + (lane & 1) * 512; asm volatile("" : "+v"(ydr)); SB();
  asm volatile("v_mfma_f32_16x16x32_bf16 %0, v[168:171], a[180:183], %0" : "+v"(g[0][1][0]) :  : ); SB();
  asm volatile("ds_read_b128 a[96:99], %0 offset:32768" :  : "v"(aA[0]) : "memory"); SB();
  asm volatile("v_mfma_f32_16x16x32_bf16 %0, v[168:171], a[184:187], %0" : "+v"(g[0][2][0]) :  : ); SB();
  asm volatile("ds_read_b128 a[112:115], %0 offset:32768" :  : "v"(aA[1]) : "memory"); SB();
  asm volatile("v_mfma_f32_16x16x32_bf16 %0, v[168:171], a[188:191], %0" : "+v"(g[0][3][0]) :  : ); SB();
  asm volatile("ds_read_b128 a[100:103], %0 offset:34816" :  : "v"(aA[0]) : "memory"); SB();
  asm volatile("v_mfma_f32_16x16x32_bf16 %0, v[172:175], a[176:179], %0" : "+v"(g[0][0][1]) :  : ); SB();
  asm volatile("ds_read_b128 a[116:119], %0 offset:34816" :  : "v"(aA[1]) : "memory"); SB();
  asm volatile("v_mfma_f32_16x16x32_bf16 %0, v[172:175], a[180:183], %0" : "+v"(g[0][1][1]) :  : ); SB();
  asm volatile("ds_read_b128 a[104:107], %0 offset:36864" :  : "v"(aA[0]) : "memory"); SB();
  asm volatile("v_mfma_f32_16x16x32_bf16 %0, v[172:175], a[184:187], %0" : "+v"(g[0][2][1]) :  : ); SB();
  asm volatile("ds_read_b128 a[120:123], %0 offset:36864" :  : "v"(aA[1]) : "memory"); SB();
  asm volatile("v_mfma_f32_16x16x32_bf16 %0, v[172:175], a[188:191], %0" : "+v"(g[0][3][1]) :  : ); SB();
  asm volatile("buffer_load_dwordx4 v[160:163], %0, %1, %2 offen offset:0 sc1" :  : "v"(vB), "s"(rWS), "s"(0u) : "memory"); SB();
  asm volatile("s_waitcnt vmcnt(22)" :  :  : "memory"); SB();
  asm volatile("v_mfma_f32_16x16x32_bf16 %0, v[176:179], a[192:195], %0" : "+v"(g[0][0][0]) :  : ); SB();
  asm volatile("buffer_load_dwordx4 v[164:167], %0, %1, %2 offen offset:1024 sc1" :  : "v"(vB), "s"(rWS), "s"(0u) : "memory"); SB();
  asm volatile("v_mfma_f32_16x16x32_bf16 %0, v[176:179], a[196:199], %0" : "+v"(g[0][1][0]) :  : ); SB();
  ydr = ydr + ((((lane >> 1) & 15) ^ ((lane & 1) << 3)) << 4); asm volatile("" : "+v"(ydr)); SB();
  asm volatile("v_mfma_f32_16x16x32_bf16 %0, v[176:179], a[200:203], %0" : "+v"(g[0][2][0]) :  : ); SB();
  ykr = lb + 59520 + 512 + w * 1024 + l16; asm volatile("" : "+v"(ykr)); SB();
  asm volatile("v_mfma_f32_16x16x32_bf16 %0, v[176:179], a[204:207], %0" : "+v"(g[0][3][0]) :  : ); SB();
  asm volatile("buffer_load_dwordx4 v[168:171], %0, %1, %2 offen offset:0 sc1" :  : "v"(vB), "s"(rWS), "s"(65536u) : "memory"); SB();
  pw2 = er + (122880 - 32768); asm volatile("" : "+v"(pw2)); SB();
  asm volatile("v_mfma_f32_16x16x32_bf16 %0, v[180:183], a[192:195], %0" : "+v"(g[0][0][1]) :  : ); SB();
  pe[0] = lb + 122880 + (w ^ 1) * 6144 + l16; asm volatile("" : "+v"(pe[0])); SB();
  asm volatile("v_mfma_f32_16x16x32_bf16 %0, v[180:183], a[196:199], %0" : "+v"(g[0][1][1]) :  : ); SB();
  pe[1] = lb + 122880 + (w ^ 2) * 6144 + l16; asm volatile("" : "+v"(pe[1])); SB();
  asm volatile("v_mfma_f32_16x16x32_bf16 %0, v[180:183], a[200:203], %0" : "+v"(g[0][2][1]) :  : ); SB();
  pe[2] = lb + 122880 + (w ^ 3) * 6144 + l16; asm volatile("" : "+v"(pe[2])); SB();
  asm volatile("v_mfma_f32_16x16x32_bf16 %0, v[180:183], a[204:207], %0" : "+v"(g[0][3][1]) :  : ); SB();
  asm volatile("buffer_load_dwordx4 v[172:175], %0, %1, %2 offen offset:1024 sc1" :  : "v"(vB), "s"(rWS), "s"(65536u) : "memory"); SB();
  asm volatile("s_waitcnt vmcnt(23)" :  :  : "memory"); SB();
  asm volatile("v_mfma_f32_16x16x32_bf16 %0, v[184:187], a[208:211], %0" : "+v"(g[0][0][0]) :  : ); SB();
  asm volatile("v_xor_b32 %0, 917504, %0" : "+v"(vB) :  : "memory"); SB();
  asm volatile("s_waitcnt lgkmcnt(0)" : "+v"(a1[0][0]), "+v"(a1[0][1]), "+v"(a1[0][2]), "+v"(a1[1][0]), "+v"(a1[1][1]), "+v"(a1[1][2]) :  : "memory"); SB();
  asm volatile("v_mfma_f32_16x16x32_bf16 %0, v[184:187], a[212:215], %0" : "+v"(g[0][1][0]) :  : ); SB();
  dl = (int)(wm * 12928 - wn * 256 + w * 128) - 2 * 12928; asm volatile("" : "+s"(dl)); SB();
  asm volatile("v_mfma_f32_16x16x32_bf16 %0, v[184:187], a[216:219], %0" : "+v"(g[0][2][0]) :  : ); SB();
  asm volatile("buffer_load_dwordx4 v[176:179], %0, %1, %2 offen offset:0 sc1" :  : "v"(vB), "s"(rWS), "s"(0u) : "memory"); SB();
  asm volatile("v_add_u32 %0, %1, %0" : "+v"(aa[0]) : "s"(dl) : ); SB();
  asm volatile("v_mfma_f32_16x16x32_bf16 %0, v[184:187], a[220:223], %0" : "+v"(g[0][3][0]) :  : ); SB();
  asm volatile("v_add_u32 %0, %1, %0" : "+v"(aa[1]) : "s"(dl) : ); SB();
  asm volatile("v_mfma_f32_16x16x32_bf16 %0, v[188:191], a[208:211], %0" : "+v"(g[0][0][1]) :  : ); SB();
  asm volatile("v_add_u32 %0, %1, %0" : "+v"(aa[2]) : "s"(dl) : ); SB();
  asm volatile("v_mfma_f32_16x16x32_bf16 %0, v[188:191], a[212:215], %0" : "+v"(g[0][1][1]) :  : ); SB();
  asm volatile("v_add_u32 %0, %1, %0" : "+v"(aa[3]) : "s"(dl) : ); SB();
  asm volatile("v_mfma_f32_16x16x32_bf16 %0, v[188:191], a[216:219], %0" : "+v"(g[0][2][1]) :  : ); SB();
  asm volatile("buffer_load_dwordx4 v[180:183], %0, %1, %2 offen offset:1024 sc1" :  : "v"(vB), "s"(rWS), "s"(0u) : "memory"); SB();
  asm volatile("v_add_u32 %0, %1, %0" : "+v"(bb) : "s"(dl) : ); SB();
  asm volatile("v_mfma_f32_16x16x32_bf16 %0, v[188:191], a[220:223], %0" : "+v"(g[0][3][1]) :  : ); SB();
  asm volatile("v_mul_f32 %0, %1, %1" : "=v"(gt[0]) : "v"(a1[0][0][0]) : ); SB();
  asm volatile("s_waitcnt vmcnt(23)" :  :  : "memory"); SB();
  asm volatile("v_mfma_f32_16x16x32_bf16 %0, v[192:195], a[224:227], %0" : "+v"(g[0][0][0]) :  : ); SB();
  asm volatile("v_mul_f32 %0, %1, %1" : "=v"(gt[1]) : "v"(a1[0][0][1]) : ); SB();
  asm volatile("v_mfma_f32_16x16x32_bf16 %0, v[192:195], a[228:231], %0" : "+v"(g[0][1][0]) :  : ); SB();
  asm volatile("buffer_load_dwordx4 v[184:187], %0, %1, %2 offen offset:0 sc1" :  : "v"(vB), "s"(rWS), "s"(65536u) : "memory"); SB();
  asm volatile("v_mul_f32 %0, %1, %1" : "=v"(gt[2]) : "v"(a1[0][0][2]) : ); SB();
  asm volatile("v_mfma_f32_16x16x32_bf16 %0, v[192:195], a[232:235], %0" : "+v"(g[0][2][0]) :  : ); SB();
  asm volatile("v_mul_f32 %0, %1, %1" : "=v"(gt[3]) : "v"(a1[0][0][3]) : ); SB();
  asm volatile("v_mfma_f32_16x16x32_bf16 %0, v[192:195], a[236:239], %0" : "+v"(g[0][3][0]) :  : ); SB();
  asm volatile("v_fma_f32 %0, %0, %1, %2" : "+v"(gt[0]) : "v"(k2[0]), "v"(k1[0]) : ); SB();
  asm volatile("v_mfma_f32_16x16x32_bf16 %0, v[196:199], a[224:227], %0" : "+v"(g[0][0][1]) :  : ); SB();
  asm volatile("v_fma_f32 %0, %0, %1, %2" : "+v"(gt[1]) : "v"(k2[0]), "v"(k1[0]) : ); SB();
  asm volatile("v_mfma_f32_16x16x32_bf16 %0, v[196:199], a[228:231], %0" : "+v"(g[0][1][1]) :  : ); SB();
  asm volatile("buffer_load_dwordx4 v[188:191], %0, %1, %2 offen offset:1024 sc1" :  : "v"(vB), "s"(rWS), "s"(65536u) : "memory"); SB();
  asm volatile("v_fma_f32 %0, %0, %1, %2" : "+v"(gt[2]) : "v"(k2[0]), "v"(k1[0]) : ); SB();
  asm volatile("v_mfma_f32_16x16x32_bf16 %0, v[196:199], a[232:235], %0" : "+v"(g[0][2][1]) :  : ); SB();
  asm volatile("v_xor_b32 %0, 131072, %0" : "+v"(vB) :  : "memory"); SB();
  asm volatile("v_fma_f32 %0, %0, %1, %2" : "+v"(gt[3]) : "v"(k2[0]), "v"(k1[0]) : ); SB();
  asm volatile("v_mfma_f32_16x16x32_bf16 %0, v[196:199], a[236:239], %0" : "+v"(g[0][3][1]) :  : ); SB();
  asm volatile("v_mul_f32 %0, %0, %1" : "+v"(gt[0]) : "v"(a1[0][0][0]) : ); SB();
  asm volatile("s_waitcnt vmcnt(23)" :  :  : "memory"); SB();
  asm volatile("v_mfma_f32_16x16x32_bf16 %0, v[200:203], a[240:243], %0" : "+v"(g[0][0][0]) :  : ); SB();
  asm volatile("buffer_load_dwordx4 v[192:195], %0, %1, %2 offen offset:0 sc1" :  : "v"(vB), "s"(rWS), "s"(0u) : "memory"); SB();
  asm volatile("v_mul_f32 %0, %0, %1" : "+v"(gt[1]) : "v"(a1[0][0][1]) : ); SB();
  asm volatile("v_mfma_f32_16x16x32_bf16 %0, v[200:203], a[244:247], %0" : "+v"(g[0][1][0]) :  : ); SB();
  asm volatile("s_waitcnt vmcnt(10)" :  :  : "memory"); SB();
  asm volatile("ds_read_b128 a[108:111], %0 offset:38912" :  : "v"(aA[0]) : "memory"); SB();
  asm volatile("v_mfma_f32_16x16x32_bf16 %0, v[200:203], a[248:251], %0" : "+v"(g[0][2][0]) :  : ); SB();
  asm volatile("ds_read_b128 a[124:127], %0 offset:38912" :  : "v"(aA[1]) : "memory"); SB();
  asm volatile("v_mul_f32 %0, %0, %1" : "+v"(gt[2]) : "v"(a1[0][0][2]) : ); SB();
  asm volatile("v_mfma_f32_16x16x32_bf16 %0, v[200:203], a[252:255], %0" : "+v"(g[0][3][0]) :  : ); SB();
  asm volatile("v_mul_f32 %0, %0, %1" : "+v"(gt[3]) : "v"(a1[0][0][3]) : ); SB();
  asm volatile("v_mfma_f32_16x16x32_bf16 %0, v[204:207], a[240:243], %0" : "+v"(g[0][0][1]) :  : ); SB();
  asm volatile("buffer_load_dwordx4 v[196:199], %0, %1, %2 offen offset:1024 sc1" :  : "v"(vB), "s"(rWS), "s"(0u) : "memory"); SB();
  asm volatile("v_exp_f32 %0, %1" : "=v"(ge[0]) : "v"(gt[0]) : ); SB();
  asm volatile("v_mfma_f32_16x16x32_bf16 %0, v[204:207], a[244:247], %0" : "+v"(g[0][1][1]) :  : ); SB();
  asm volatile("v_exp_f32 %0, %1" : "=v"(ge[1]) : "v"(gt[1]) : ); SB();
  asm volatile("v_mfma_f32_16x16x32_bf16 %0, v[204:207], a[248:251], %0" : "+v"(g[0][2][1]) :  : ); SB();
  asm volatile("v_exp_f32 %0, %1" : "=v"(ge[2]) : "v"(gt[2]) : ); SB();
  asm volatile("v_mfma_f32_16x16x32_bf16 %0, v[204:207], a[252:255], %0" : "+v"(g[0][3][1]) :  : ); SB();
  asm volatile("buffer_load_dwordx4 %0, %1, %2 offen offset:1024 lds" :  : "v"(voS), "s"(rA), "s"(ao[1]) : "memory"); SB();
  asm volatile("v_mfma_f32_16x16x32_bf16 %0, v[208:211], a[160:163], %1" : "=&v"(g[1][0][0]) : "v"(bs4) : ); SB();
  asm volatile("v_exp_f32 %0, %1" : "=v"(ge[3]) : "v"(gt[3]) : ); SB();
  asm volatile("v_mfma_f32_16x16x32_bf16 %0, v[208:211], a[164:167], 0" : "=&v"(g[1][1][0]) :  : ); SB();
  asm volatile("v_fma_f32 %0, %0, %1, %1" : "+v"(ge[0]) : "v"(xd[0]) : ); SB();
  asm volatile("v_mfma_f32_16x16x32_bf16 %0, v[208:211], a[168:171], 0" : "=&v"(g[1][2][0]) :  : ); SB();
  asm volatile("buffer_load_dwordx4 v[200:203], %0, %1, %2 offen offset:0 sc1" :  : "v"(vB), "s"(rWS), "s"(65536u) : "memory"); SB();
  asm volatile("v_fma_f32 %0, %0, %1, %1" : "+v"(ge[1]) : "v"(xd[0]) : ); SB();
  asm volatile("v_mfma_f32_16x16x32_bf16 %0, v[208:211], a[172:175], 0" : "=&v"(g[1][3][0]) :  : ); SB();
  asm volatile("v_fma_f32 %0, %0, %1, %1" : "+v"(ge[2]) : "v"(xd[0]) : ); SB();
  asm volatile("v_mfma_f32_16x16x32_bf16 %0, v[212:215], a[160:163], %1" : "=&v"(g[1][0][1]) : "v"(bs4) : ); SB();
  asm volatile("v_fma_f32 %0, %0, %1, %1" : "+v"(ge[3]) : "v"(xd[0]) : ); SB();
  asm volatile("v_mfma_f32_16x16x32_bf16 %0, v[212:215], a[164:167], 0" : "=&v"(g[1][1][1]) :  : ); SB();
  asm volatile("v_rcp_f32 %0, %1" : "=v"(gt[0]) : "v"(ge[0]) : ); SB();
  asm volatile("v_mfma_f32_16x16x32_bf16 %0, v[212:215], a[168:171], 0" : "=&v"(g[1][2][1]) :  : ); SB();
  asm volatile("buffer_load_dwordx4 v[204:207], %0, %1, %2 offen offset:1024 sc1" :  : "v"(vB), "s"(rWS), "s"(65536u) : "memory"); SB();
  asm volatile("v_rcp_f32 %0, %1" : "=v"(gt[1]) : "v"(ge[1]) : ); SB();
  asm volatile("v_mfma_f32_16x16x32_bf16 %0, v[212:215], a[172:175], 0" : "=&v"(g[1][3][1]) :  : ); SB();
  asm volatile("v_xor_b32 %0, 393216, %0" : "+v"(vB) :  : "memory"); SB();
  asm volatile("v_mfma_f32_16x16x32_bf16 %0, v[216:219], a[176:179], %0" : "+v"(g[1][0][0]) :  : ); SB();
  asm volatile("v_rcp_f32 %0, %1" : "=v"(gt[2]) : "v"(ge[2]) : ); SB();
  asm volatile("v_mfma_f32_16x16x32_bf16 %0, v[216:219], a[180:183], %0" : "+v"(g[1][1][0]) :  : ); SB();
  asm volatile("buffer_load_dwordx4 v[208:211], %0, %1, %2 offen offset:0 sc1" :  : "v"(vB), "s"(rWS), "s"(0u) : "memory"); SB();
  asm volatile("v_rcp_f32 %0, %1" : "=v"(gt[3]) : "v"(ge[3]) : ); SB();
  asm volatile("v_mfma_f32_16x16x32_bf16 %0, v[216:219], a[184:187], %0" : "+v"(g[1][2][0]) :  : ); SB();
  asm volatile("v_mul_f32 %0, %0, %1" : "+v"(a1[0][0][0]) : "v"(gt[0]) : ); SB();
  asm volatile("v_mfma_f32_16x16x32_bf16 %0, v[216:219], a[188:191], %0" : "+v"(g[1][3][0]) :  : ); SB();
  asm volatile("v_mul_f32 %0, %0, %1" : "+v"(a1[0][0][1]) : "v"(gt[1]) : ); SB();
  asm volatile("v_mfma_f32_16x16x32_bf16 %0, v[220:223], a[176:179], %0" : "+v"(g[1][0][1]) :  : ); SB();
  asm volatile("v_mul_f32 %0, %0, %1" : "+v"(a1[0][0][2]) : "v"(gt[2]) : ); SB();
  asm volatile("v_mfma_f32_16x16x32_bf16 %0, v[220:223], a[180:183], %0" : "+v"(g[1][1][1]) :  : ); SB();
  asm volatile("v_mul_f32 %0, %0, %1" : "+v"(a1[0][0][3]) : "v"(gt[3]) : ); SB();
  asm volatile("v_mfma_f32_16x16x32_bf16 %0, v[220:223], a[184:187], %0" : "+v"(g[1][2][1]) :  : ); SB();
  asm volatile("v_mul_f32 %0, %1, %1" : "=v"(gt[0]) : "v"(a1[0][1][0]) : ); SB();
  asm volatile("v_mfma_f32_16x16x32_bf16 %0, v[220:223], a[188:191], %0" : "+v"(g[1][3][1]) :  : ); SB();
  asm volatile("v_mul_f32 %0, %1, %1" : "=v"(gt[1]) : "v"(a1[0][1][1]) : ); SB();
  ST(26);
  asm volatile("v_mfma_f32_16x16x32_bf16 %0, v[224:227], a[192:195], %0" : "+v"(g[1][0][0]) :  : ); SB();
  vH_ = vH; asm volatile("" : "+v"(vH_)); SB();
  asm volatile("v_mfma_f32_16x16x32_bf16 %0, v[224:227], a[196:199], %0" : "+v"(g[1][1][0]) :  : ); SB();
  vO = W_WO + (cb * 120 + w * 30) * 1024 + l16; asm volatile("" : "+v"(vO)); SB();
  asm volatile("v_mfma_f32_16x16x32_bf16 %0, v[224:227], a[200:203], %0" : "+v"(g[1][2][0]) :  : ); SB();
  vFH = W_FH + rb * 256 + w * 64 + (lane & 7) * 8; asm volatile("" : "+v"(vFH)); SB();
  asm volatile("v_mfma_f32_16x16x32_bf16 %0, v[224:227], a[204:207], %0" : "+v"(g[1][3][0]) :  : ); SB();
  vHr = W_HH + (rb * 96 + w * 6) * 1024 + l16; asm volatile("" : "+v"(vHr)); SB();
  asm volatile("v_mfma_f32_16x16x32_bf16 %0, v[228:231], a[192:195], %0" : "+v"(g[1][0][1]) :  : ); SB();
  vX = (rb * 64 + w * 16 + l15) * 1280; asm volatile("" : "+v"(vX)); SB();
  asm volatile("v_mfma_f32_16x16x32_bf16 %0, v[228:231], a[196:199], %0" : "+v"(g[1][1][1]) :  : ); SB();
  vX = vX + cb * 160 + 8 * g4; asm volatile("" : "+v"(vX)); SB();
  asm volatile("v_mfma_f32_16x16x32_bf16 %0, v[228:231], a[200:203], %0" : "+v"(g[1][2][1]) :  : ); SB();
  sFH = W_FH + ((rb * 8 + cb) * 4 + w) * 8; asm volatile("" : "+s"(sFH)); SB();
  asm volatile("v_mfma_f32_16x16x32_bf16 %0, v[228:231], a[204:207], %0" : "+v"(g[1][3][1]) :  : ); SB();
  hso[0] = (w ^ 0) * 24576; asm volatile("" : "+s"(hso[0])); SB();
  asm volatile("v_mfma_f32_16x16x32_bf16 %0, v[232:235], a[208:211], %0" : "+v"(g[1][0][0]) :  : ); SB();
  hso[1] = hso[0] + 3072; asm volatile("" : "+s"(hso[1])); SB();
  hso[2] = (w ^ 1) * 24576; asm volatile("" : "+s"(hso[2])); SB();
  asm volatile("v_mfma_f32_16x16x32_bf16 %0, v[232:235], a[212:215], %0" : "+v"(g[1][1][0]) :  : ); SB();
  hso[3] = hso[2] + 3072; asm volatile("" : "+s"(hso[3])); SB();
  hso[4] = (w ^ 2) * 24576; asm volatile("" : "+s"(hso[4])); SB();
  asm volatile("v_mfma_f32_16x16x32_bf16 %0, v[232:235], a[216:219], %0" : "+v"(g[1][2][0]) :  : ); SB();
  hso[5] = hso[4] + 3072; asm volatile("" : "+s"(hso[5])); SB();
  hso[6] = (w ^ 3) * 24576; asm volatile("" : "+s"(hso[6])); SB();
  asm volatile("v_mfma_f32_16x16x32_bf16 %0, v[232:235], a[220:223], %0" : "+v"(g[1][3][0]) :  : ); SB();
  asm volatile("buffer_load_dwordx4 v[212:215], %0, %1, %2 offen offset:1024 sc1" :  : "v"(vB), "s"(rWS), "s"(0u) : "memory"); SB();
  hso[7] = hso[6] + 3072; asm volatile("" : "+s"(hso[7])); SB();
  asm volatile("v_mfma_f32_16x16x32_bf16 %0, v[236:239], a[208:211], %0" : "+v"(g[1][0][1]) :  : ); SB();
  asm volatile("buffer_load_dwordx4 %0, %1, %2 offen offset:2048 lds" :  : "v"(voS), "s"(rA), "s"(ao[2]) : "memory"); SB();
  asm volatile("v_mul_f32 %0, %1, %1" : "=v"(gt[2]) : "v"(a1[0][1][2]) : ); SB();
  asm volatile("v_mfma_f32_16x16x32_bf16 %0, v[236:239], a[212:215], %0" : "+v"(g[1][1][1]) :  : ); SB();
  asm volatile("v_mul_f32 %0, %1, %1" : "=v"(gt[3]) : "v"(a1[0][1][3]) : ); SB();
  asm volatile("v_mfma_f32_16x16x32_bf16 %0, v[236:239], a[216:219], %0" : "+v"(g[1][2][1]) :  : ); SB();
  asm volatile("v_fma_f32 %0, %0, %1, %2" : "+v"(gt[0]) : "v"(k2[0]), "v"(k1[0]) : ); SB();
  asm volatile("v_mfma_f32_16x16x32_bf16 %0, v[236:239], a[220:223], %0" : "+v"(g[1][3][1]) :  : ); SB();
  asm volatile("buffer_load_dwordx4 %0, %1, %2 offen offset:3072 lds" :  : "v"(voS), "s"(rA), "s"(ao[3]) : "memory"); SB();
  asm volatile("v_mfma_f32_16x16x32_bf16 %0, v[240:243], a[224:227], %0" : "+v"(g[1][0][0]) :  : ); SB();
  asm volatile("v_fma_f32 %0, %0, %1, %2" : "+v"(gt[1]) : "v"(k2[0]), "v"(k1[0]) : ); SB();
  asm volatile("v_mfma_f32_16x16x32_bf16 %0, v[240:243], a[228:231], %0" : "+v"(g[1][1][0]) :  : ); SB();
  asm volatile("v_fma_f32 %0, %0, %1, %2" : "+v"(gt[2]) : "v"(k2[0]), "v"(k1[0]) : ); SB();
  asm volatile("v_mfma_f32_16x16x32_bf16 %0, v[240:243], a[232:235], %0" : "+v"(g[1][2][0]) :  : ); SB();
  asm volatile("s_add_u32 m0, %0, 102400" :  : "s"(mA) : "memory", "scc"); SB();
  asm volatile("v_fma_f32 %0, %0, %1, %2" : "+v"(gt[3]) : "v"(k2[0]), "v"(k1[0]) : ); SB();
  asm volatile("v_mfma_f32_16x16x32_bf16 %0, v[240:243], a[236:239], %0" : "+v"(g[1][3][0]) :  : ); SB();
  asm volatile("buffer_load_dwordx4 %0, %1, %2 offen lds" :  : "v"(voS), "s"(rA), "s"(ao[4]) : "memory"); SB();
  asm volatile("v_mul_f32 %0, %0, %1" : "+v"(gt[0]) : "v"(a1[0][1][0]) : ); SB();
  asm volatile("v_mfma_f32_16x16x32_bf16 %0, v[244:247], a[224:227], %0" : "+v"(g[1][0][1]) :  : ); SB();
  asm volatile("v_mul_f32 %0, %0, %1" : "+v"(gt[1]) : "v"(a1[0][1][1]) : ); SB();
  asm volatile("v_mfma_f32_16x16x32_bf16 %0, v[244:247], a[228:231], %0" : "+v"(g[1][1][1]) :  : ); SB();
  asm volatile("v_mul_f32 %0, %0, %1" : "+v"(gt[2]) : "v"(a1[0][1][2]) : ); SB();
  asm volatile("v_mfma_f32_16x16x32_bf16 %0, v[244:247], a[232:235], %0" : "+v"(g[1][2][1]) :  : ); SB();
  asm volatile("buffer_load_dwordx4 %0, %1, %2 offen offset:1024 lds" :  : "v"(voS), "s"(rA), "s"(ao[5]) : "memory"); SB();
  asm volatile("v_mul_f32 %0, %0, %1" : "+v"(gt[3]) : "v"(a1[0][1][3]) : ); SB();
  asm volatile("v_mfma_f32_16x16x32_bf16 %0, v[244:247], a[236:239], %0" : "+v"(g[1][3][1]) :  : ); SB();
  asm volatile("v_exp_f32 %0, %1" : "=v"(ge[0]) : "v"(gt[0]) : ); SB();
  asm volatile("v_mfma_f32_16x16x32_bf16 %0, v[248:251], a[240:243], %0" : "+v"(g[1][0][0]) :  : ); SB();
  asm volatile("v_exp_f32 %0, %1" : "=v"(ge[1]) : "v"(gt[1]) : ); SB();
  asm volatile("v_mfma_f32_16x16x32_bf16 %0, v[248:251], a[244:247], %0" : "+v"(g[1][1][0]) :  : ); SB();
  asm volatile("buffer_load_dwordx4 %0, %1, %2 offen offset:2048 lds" :  : "v"(voS), "s"(rA), "s"(ao[6]) : "memory"); SB();
  asm volatile("v_exp_f32 %0, %1" : "=v"(ge[2]) : "v"(gt[2]) : ); SB();
  asm volatile("v_mfma_f32_16x16x32_bf16 %0, v[248:251], a[248:251], %0" : "+v"(g[1][2][0]) :  : ); SB();
  asm volatile("v_exp_f32 %0, %1" : "=v"(ge[3]) : "v"(gt[3]) : ); SB();
  asm volatile("v_mfma_f32_16x16x32_bf16 %0, v[248:251], a[252:255], %0" : "+v"(g[1][3][0]) :  : ); SB();
  asm volatile("v_fma_f32 %0, %0, %1, %1" : "+v"(ge[0]) : "v"(xd[0]) : ); SB();
  asm volatile("v_mfma_f32_16x16x32_bf16 %0, v[252:255], a[240:243], %0" : "+v"(g[1][0][1]) :  : ); SB();
  asm volatile("v_fma_f32 %0, %0, %1, %1" : "+v"(ge[1]) : "v"(xd[0]) : ); SB();
  asm volatile("v_mfma_f32_16x16x32_bf16 %0, v[252:255], a[244:247], %0" : "+v"(g[1][1][1]) :  : ); SB();
  asm volatile("buffer_load_dwordx4 %0, %1, %2 offen offset:3072 lds" :  : "v"(voS), "s"(rA), "s"(ao[7]) : "memory"); SB();
  asm volatile("v_fma_f32 %0, %0, %1, %1" : "+v"(ge[2]) : "v"(xd[0]) : ); SB();
  asm volatile("v_mfma_f32_16x16x32_bf16 %0, v[252:255], a[248:251], %0" : "+v"(g[1][2][1]) :  : ); SB();
  asm volatile("v_fma_f32 %0, %0, %1, %1" : "+v"(ge[3]) : "v"(xd[0]) : ); SB();
  asm volatile("v_mfma_f32_16x16x32_bf16 %0, v[252:255], a[252:255], %0" : "+v"(g[1][3][1]) :  : ); SB();
  asm volatile("v_rcp_f32 %0, %1" : "=v"(gt[0]) : "v"(ge[0]) : ); SB();
  asm volatile("s_waitcnt vmcnt(19)" :  :  : "memory"); SB();
  asm volatile("v_mfma_f32_16x16x32_bf16 %0, v[160:163], a[0:3], %0" : "+v"(g[0][0][0]) :  : ); SB();
  asm volatile("buffer_load_dwordx4 v[216:219], %0, %1, %2 offen offset:0 sc1" :  : "v"(vB), "s"(rWS), "s"(65536u) : "memory"); SB();
  asm volatile("v_rcp_f32 %0, %1" : "=v"(gt[1]) : "v"(ge[1]) : ); SB();
  asm volatile("v_mfma_f32_16x16x32_bf16 %0, v[160:163], a[4:7], %0" : "+v"(g[0][1][0]) :  : ); SB();
  asm volatile("v_rcp_f32 %0, %1" : "=v"(gt[2]) : "v"(ge[2]) : ); SB();
  asm volatile("v_mfma_f32_16x16x32_bf16 %0, v[160:163], a[8:11], %0" : "+v"(g[0][2][0]) :  : ); SB();
  asm volatile("v_rcp_f32 %0, %1" : "=v"(gt[3]) : "v"(ge[3]) : ); SB();
  asm volatile("v_mfma_f32_16x16x32_bf16 %0, v[160:163], a[12:15], %0" : "+v"(g[0][3][0]) :  : ); SB();
  asm volatile("s_waitcnt vmcnt(11)" :  :  : "memory"); SB();
  asm volatile("ds_read_b128 a[128:131], %0 offset:0" :  : "v"(aA[0]) : "memory"); SB();
  asm volatile("v_mfma_f32_16x16x32_bf16 %0, v[164:167], a[0:3], %0" : "+v"(g[0][0][1]) :  : ); SB();
  asm volatile("ds_read_b128 a[144:147], %0 offset:0" :  : "v"(aA[1]) : "memory"); SB();
  asm volatile("v_mul_f32 %0, %0, %1" : "+v"(a1[0][1][0]) : "v"(gt[0]) : ); SB();
  asm volatile("v_mfma_f32_16x16x32_bf16 %0, v[164:167], a[4:7], %0" : "+v"(g[0][1][1]) :  : ); SB();
  asm volatile("buffer_load_dwordx4 v[220:223], %0, %1, %2 offen offset:1024 sc1" :  : "v"(vB), "s"(rWS), "s"(65536u) : "memory"); SB();
  asm volatile("v_mul_f32 %0, %0, %1" : "+v"(a1[0][1][1]) : "v"(gt[1]) : ); SB();
  asm volatile("v_mfma_f32_16x16x32_bf16 %0, v[164:167], a[8:11], %0" : "+v"(g[0][2][1]) :  : ); SB();
  asm volatile("v_xor_b32 %0, 131072, %0" : "+v"(vB) :  : "memory"); SB();
  asm volatile("v_mul_f32 %0, %0, %1" : "+v"(a1[0][1][2]) : "v"(gt[2]) : ); SB();
  asm volatile("v_mfma_f32_16x16x32_bf16 %0, v[164:167], a[12:15], %0" : "+v"(g[0][3][1]) :  : ); SB();
  asm volatile("buffer_load_dwordx4 v[224:227], %0, %1, %2 offen offset:0 sc1" :  : "v"(vB), "s"(rWS), "s"(0u) : "memory"); SB();
  asm volatile("v_mfma_f32_16x16x32_bf16 %0, v[168:171], a[16:19], %0" : "+v"(g[0][0][0]) :  : ); SB();
  asm volatile("v_mul_f32 %0, %0, %1" : "+v"(a1[0][1][3]) : "v"(gt[3]) : ); SB();
  asm volatile("v_mfma_f32_16x16x32_bf16 %0, v[168:171], a[20:23], %0" : "+v"(g[0][1][0]) :  : ); SB();
  asm volatile("v_mul_f32 %0, %1, %1" : "=v"(gt[0]) : "v"(a1[0][2][0]) : ); SB();
  asm volatile("v_mfma_f32_16x16x32_bf16 %0, v[168:171], a[24:27], %0" : "+v"(g[0][2][0]) :  : ); SB();
  asm volatile("buffer_load_dwordx4 v[228:231], %0, %1, %2 offen offset:1024 sc1" :  : "v"(vB), "s"(rWS), "s"(0u) : "memory"); SB();
  asm volatile("v_mul_f32 %0, %1, %1" : "=v"(gt[1]) : "v"(a1[0][2][1]) : ); SB();
  asm volatile("v_mfma_f32_16x16x32_bf16 %0, v[168:171], a[28:31], %0" : "+v"(g[0][3][0]) :  : ); SB();
  asm volatile("v_mul_f32 %0, %1, %1" : "=v"(gt[2]) : "v"(a1[0][2][2]) : ); SB();
  asm volatile("v_mfma_f32_16x16x32_bf16 %0, v[172:175], a[16:19], %0" : "+v"(g[0][0][1]) :  : ); SB();
  asm volatile("v_mul_f32 %0, %1, %1" : "=v"(gt[3]) : "v"(a1[0][2][3]) : ); SB();
  asm volatile("v_mfma_f32_16x16x32_bf16 %0, v[172:175], a[20:23], %0" : "+v"(g[0][1][1]) :  : ); SB();
  asm volatile("v_fma_f32 %0, %0, %1, %2" : "+v"(gt[0]) : "v"(k2[0]), "v"(k1[0]) : ); SB();
  asm volatile("v_mfma_f32_16x16x32_bf16 %0, v[172:175], a[24:27], %0" : "+v"(g[0][2][1]) :  : ); SB();
  asm volatile("buffer_load_dwordx4 v[232:235], %0, %1, %2 offen offset:0 sc1" :  : "v"(vB), "s"(rWS), "s"(65536u) : "memory"); SB();
  asm volatile("v_fma_f32 %0, %0, %1, %2" : "+v"(gt[1]) : "v"(k2[0]), "v"(k1[0]) : ); SB();
  asm volatile("v_mfma_f32_16x16x32_bf16 %0, v[172:175], a[28:31], %0" : "+v"(g[0][3][1]) :  : ); SB();
  asm volatile("v_fma_f32 %0, %0, %1, %2" : "+v"(gt[2]) : "v"(k2[0]), "v"(k1[0]) : ); SB();
  asm volatile("v_mfma_f32_16x16x32_bf16 %0, v[176:179], a[32:35], %0" : "+v"(g[0][0][0]) :  : ); SB();
  asm volatile("v_fma_f32 %0, %0, %1, %2" : "+v"(gt[3]) : "v"(k2[0]), "v"(k1[0]) : ); SB();
  asm volatile("v_mfma_f32_16x16x32_bf16 %0, v[176:179], a[36:39], %0" : "+v"(g[0][1][0]) :  : ); SB();
  asm volatile("buffer_load_dwordx4 v[236:239], %0, %1, %2 offen offset:1024 sc1" :  : "v"(vB), "s"(rWS), "s"(65536u) : "memory"); SB();
  asm volatile("v_mul_f32 %0, %0, %1" : "+v"(gt[0]) : "v"(a1[0][2][0]) : ); SB();
  asm volatile("v_mfma_f32_16x16x32_bf16 %0, v[176:179], a[40:43], %0" : "+v"(g[0][2][0]) :  : ); SB();
  asm volatile("v_xor_b32 %0, 524288, %0" : "+v"(vB) :  : "memory"); SB();
  asm volatile("v_mul_f32 %0, %0, %1" : "+v"(gt[1]) : "v"(a1[0][2][1]) : ); SB();
  asm volatile("v_mfma_f32_16x16x32_bf16 %0, v[176:179], a[44:47], %0" : "+v"(g[0][3][0]) :  : ); SB();
  asm volatile("v_mul_f32 %0, %0, %1" : "+v"(gt[2]) : "v"(a1[0][2][2]) : ); SB();
  asm volatile("v_mfma_f32_16x16x32_bf16 %0, v[180:183], a[32:35], %0" : "+v"(g[0][0][1]) :  : ); SB();
  asm volatile("v_mul_f32 %0, %0, %1" : "+v"(gt[3]) : "v"(a1[0][2][3]) : ); SB();
  asm volatile("v_mfma_f32_16x16x32_bf16 %0, v[180:183], a[36:39], %0" : "+v"(g[0][1][1]) :  : ); SB();
  asm volatile("buffer_load_dwordx4 v[240:243], %0, %1, %2 offen offset:0 sc1" :  : "v"(vB), "s"(rWS), "s"(2048u) : "memory"); SB();
  asm volatile("v_exp_f32 %0, %1" : "=v"(ge[0]) : "v"(gt[0]) : ); SB();
  asm volatile("v_mfma_f32_16x16x32_bf16 %0, v[180:183], a[40:43], %0" : "+v"(g[0][2][1]) :  : ); SB();
  asm volatile("v_exp_f32 %0, %1" : "=v"(ge[1]) : "v"(gt[1]) : ); SB();
  asm volatile("v_mfma_f32_16x16x32_bf16 %0, v[180:183], a[44:47], %0" : "+v"(g[0][3][1]) :  : ); SB();
  asm volatile("v_exp_f32 %0, %1" : "=v"(ge[2]) : "v"(gt[2]) : ); SB();
  asm volatile("v_mfma_f32_16x16x32_bf16 %0, v[184:187], a[48:51], %0" : "+v"(g[0][0][0]) :  : ); SB();
  asm volatile("v_exp_f32 %0, %1" : "=v"(ge[3]) : "v"(gt[3]) : ); SB();
  asm volatile("v_mfma_f32_16x16x32_bf16 %0, v[184:187], a[52:55], %0" : "+v"(g[0][1][0]) :  : ); SB();
  asm volatile("v_fma_f32 %0, %0, %1, %1" : "+v"(ge[0]) : "v"(xd[0]) : ); SB();
  asm volatile("v_mfma_f32_16x16x32_bf16 %0, v[184:187], a[56:59], %0" : "+v"(g[0][2][0]) :  : ); SB();
  asm volatile("v_fma_f32 %0, %0, %1, %1" : "+v"(ge[1]) : "v"(xd[0]) : ); SB();
  asm volatile("v_mfma_f32_16x16x32_bf16 %0, v[184:187], a[60:63], %0" : "+v"(g[0][3][0]) :  : ); SB();
  asm volatile("v_fma_f32 %0, %0, %1, %1" : "+v"(ge[2]) : "v"(xd[0]) : ); SB();
  asm volatile("v_mfma_f32_16x16x32_bf16 %0, v[188:191], a[48:51], %0" : "+v"(g[0][0][1]) :  : ); SB();
  asm volatile("buffer_load_dwordx4 v[244:247], %0, %1, %2 offen offset:1024 sc1" :  : "v"(vB), "s"(rWS), "s"(2048u) : "memory"); SB();
  asm volatile("v_fma_f32 %0, %0, %1, %1" : "+v"(ge[3]) : "v"(xd[0]) : ); SB();
  asm volatile("v_mfma_f32_16x16x32_bf16 %0, v[188:191], a[52:55], %0" : "+v"(g[0][1][1]) :  : ); SB();
  asm volatile("v_rcp_f32 %0, %1" : "=v"(gt[0]) : "v"(ge[0]) : ); SB();
  asm volatile("v_mfma_f32_16x16x32_bf16 %0, v[188:191], a[56:59], %0" : "+v"(g[0][2][1]) :  : ); SB();
  asm volatile("v_rcp_f32 %0, %1" : "=v"(gt[1]) : "v"(ge[1]) : ); SB();
  asm volatile("v_mfma_f32_16x16x32_bf16 %0, v[188:191], a[60:63], %0" : "+v"(g[0][3][1]) :  : ); SB();
  asm volatile("buffer_load_dwordx4 v[248:251], %0, %1, %2 offen offset:0 sc1" :  : "v"(vB), "s"(rWS), "s"(67584u) : "memory"); SB();
  ST(27);
  asm volatile("v_mfma_f32_16x16x32_bf16 %0, v[192:195], a[64:67], %0" : "+v"(g[0][0][0]) :  : ); SB();
  asm volatile("v_rcp_f32 %0, %1" : "=v"(gt[2]) : "v"(ge[2]) : ); SB();
  asm volatile("v_mfma_f32_16x16x32_bf16 %0, v[192:195], a[68:71], %0" : "+v"(g[0][1][0]) :  : ); SB();
  asm volatile("v_rcp_f32 %0, %1" : "=v"(gt[3]) : "v"(ge[3]) : ); SB();
  asm volatile("v_mfma_f32_16x16x32_bf16 %0, v[192:195], a[72:75], %0" : "+v"(g[0][2][0]) :  : ); SB();
  asm volatile("v_mul_f32 %0, %0, %1" : "+v"(a1[0][2][0]) : "v"(gt[0]) : ); SB();
  asm volatile("v_mfma_f32_16x16x32_bf16 %0, v[192:195], a[76:79], %0" : "+v"(g[0][3][0]) :  : ); SB();
  asm volatile("s_waitcnt vmcnt(13)" :  :  : "memory"); SB();
  asm volatile("ds_read_b128 a[132:135], %0 offset:2048" :  : "v"(aA[0]) : "memory"); SB();
  asm volatile("v_mfma_f32_16x16x32_bf16 %0, v[196:199], a[64:67], %0" : "+v"(g[0][0][1]) :  : ); SB();
  asm volatile("ds_read_b128 a[148:151], %0 offset:2048" :  : "v"(aA[1]) : "memory"); SB();
  asm volatile("v_mul_f32 %0, %0, %1" : "+v"(a1[0][2][1]) : "v"(gt[1]) : ); SB();
  asm volatile("v_mfma_f32_16x16x32_bf16 %0, v[196:199], a[68:71], %0" : "+v"(g[0][1][1]) :  : ); SB();
  asm volatile("buffer_load_dwordx4 v[252:255], %0, %1, %2 offen offset:1024 sc1" :  : "v"(vB), "s"(rWS), "s"(67584u) : "memory"); SB();
  asm volatile("v_mul_f32 %0, %0, %1" : "+v"(a1[0][2][2]) : "v"(gt[2]) : ); SB();
  asm volatile("v_mfma_f32_16x16x32_bf16 %0, v[196:199], a[72:75], %0" : "+v"(g[0][2][1]) :  : ); SB();
  asm volatile("v_xor_b32 %0, 917504, %0" : "+v"(vB) :  : "memory"); SB();
  asm volatile("v_mul_f32 %0, %0, %1" : "+v"(a1[0][2][3]) : "v"(gt[3]) : ); SB();
  asm volatile("v_mfma_f32_16x16x32_bf16 %0, v[196:199], a[76:79], %0" : "+v"(g[0][3][1]) :  : ); SB();
  asm volatile("buffer_load_dwordx4 v[160:163], %0, %1, %2 offen offset:0 sc1" :  : "v"(vB), "s"(rWS), "s"(2048u) : "memory"); SB();
  asm volatile("v_mfma_f32_16x16x32_bf16 %0, v[200:203], a[80:83], %0" : "+v"(g[0][0][0]) :  : ); SB();
  asm volatile("v_mul_f32 %0, %1, %1" : "=v"(gt[0]) : "v"(a1[1][0][0]) : ); SB();
  asm volatile("v_mfma_f32_16x16x32_bf16 %0, v[200:203], a[84:87], %0" : "+v"(g[0][1][0]) :  : ); SB();
  asm volatile("v_mul_f32 %0, %1, %1" : "=v"(gt[1]) : "v"(a1[1][0][1]) : ); SB();
  asm volatile("v_mfma_f32_16x16x32_bf16 %0, v[200:203], a[88:91], %0" : "+v"(g[0][2][0]) :  : ); SB();
  asm volatile("s_waitcnt vmcnt(13)" :  :  : "memory"); SB();
  asm volatile("ds_read_b128 a[136:139], %0 offset:4096" :  : "v"(aA[0]) : "memory"); SB();
  asm volatile("v_mfma_f32_16x16x32_bf16 %0, v[200:203], a[92:95], %0" : "+v"(g[0][3][0]) :  : ); SB();
  asm volatile("ds_read_b128 a[152:155], %0 offset:4096" :  : "v"(aA[1]) : "memory"); SB();
  asm volatile("v_mul_f32 %0, %1, %1" : "=v"(gt[2]) : "v"(a1[1][0][2]) : ); SB();
  asm volatile("v_mfma_f32_16x16x32_bf16 %0, v[204:207], a[80:83], %0" : "+v"(g[0][0][1]) :  : ); SB();
  asm volatile("buffer_load_dwordx4 v[164:167], %0, %1, %2 offen offset:1024 sc1" :  : "v"(vB), "s"(rWS), "s"(2048u) : "memory"); SB();
  asm volatile("v_mul_f32 %0, %1, %1" : "=v"(gt[3]) : "v"(a1[1][0][3]) : ); SB();
  asm volatile("v_mfma_f32_16x16x32_bf16 %0, v[204:207], a[84:87], %0" : "+v"(g[0][1][1]) :  : ); SB();
  asm volatile("v_fma_f32 %0, %0, %1, %2" : "+v"(gt[0]) : "v"(k2[0]), "v"(k1[0]) : ); SB();
  asm volatile("v_mfma_f32_16x16x32_bf16 %0, v[204:207], a[88:91], %0" : "+v"(g[0][2][1]) :  : ); SB();
  asm volatile("buffer_load_dwordx4 v[168:171], %0, %1, %2 offen offset:0 sc1" :  : "v"(vB), "s"(rWS), "s"(67584u) : "memory"); SB();
  asm volatile("v_fma_f32 %0, %0, %1, %2" : "+v"(gt[1]) : "v"(k2[0]), "v"(k1[0]) : ); SB();
  asm volatile("v_mfma_f32_16x16x32_bf16 %0, v[204:207], a[92:95], %0" : "+v"(g[0][3][1]) :  : ); SB();
  asm volatile("v_fma_f32 %0, %0, %1, %2" : "+v"(gt[2]) : "v"(k2[0]), "v"(k1[0]) : ); SB();
  asm volatile("v_mfma_f32_16x16x32_bf16 %0, v[208:211], a[96:99], %0" : "+v"(g[0][0][0]) :  : ); SB();
  asm volatile("v_fma_f32 %0, %0, %1, %2" : "+v"(gt[3]) : "v"(k2[0]), "v"(k1[0]) : ); SB();
  asm volatile("v_mfma_f32_16x16x32_bf16 %0, v[208:211], a[100:103], %0" : "+v"(g[0][1][0]) :  : ); SB();
  asm volatile("s_waitcnt vmcnt(13)" :  :  : "memory"); SB();
  asm volatile("ds_read_b128 a[140:143], %0 offset:6144" :  : "v"(aA[0]) : "memory"); SB();
  asm volatile("v_mfma_f32_16x16x32_bf16 %0, v[208:211], a[104:107], %0" : "+v"(g[0][2][0]) :  : ); SB();
  asm volatile("ds_read_b128 a[156:159], %0 offset:6144" :  : "v"(aA[1]) : "memory"); SB();
  asm volatile("s_waitcnt lgkmcnt(4)\n\tv_mfma_f32_16x16x32_bf16 %0, v[208:211], a[108:111], %0" : "+v"(g[0][3][0]) :  : ); SB();
  asm volatile("buffer_load_dwordx4 v[172:175], %0, %1, %2 offen offset:1024 sc1" :  : "v"(vB), "s"(rWS), "s"(67584u) : "memory"); SB();
  asm volatile("v_mul_f32 %0, %0, %1" : "+v"(gt[0]) : "v"(a1[1][0][0]) : ); SB();
  asm volatile("v_mfma_f32_16x16x32_bf16 %0, v[212:215], a[96:99], %0" : "+v"(g[0][0][1]) :  : ); SB();
  asm volatile("v_xor_b32 %0, 131072, %0" : "+v"(vB) :  : "memory"); SB();
  asm volatile("v_mul_f32 %0, %0, %1" : "+v"(gt[1]) : "v"(a1[1][0][1]) : ); SB();
  asm volatile("v_mfma_f32_16x16x32_bf16 %0, v[212:215], a[100:103], %0" : "+v"(g[0][1][1]) :  : ); SB();
  asm volatile("buffer_load_dwordx4 v[176:179], %0, %1, %2 offen offset:0 sc1" :  : "v"(vB), "s"(rWS), "s"(2048u) : "memory"); SB();
  asm volatile("v_mul_f32 %0, %0, %1" : "+v"(gt[2]) : "v"(a1[1][0][2]) : ); SB();
  asm volatile("v_mfma_f32_16x16x32_bf16 %0, v[212:215], a[104:107], %0" : "+v"(g[0][2][1]) :  : ); SB();
  asm volatile("v_mul_f32 %0, %0, %1" : "+v"(gt[3]) : "v"(a1[1][0][3]) : ); SB();
  asm volatile("v_mfma_f32_16x16x32_bf16 %0, v[212:215], a[108:111], %0" : "+v"(g[0][3][1]) :  : ); SB();
  asm volatile("v_exp_f32 %0, %1" : "=v"(ge[0]) : "v"(gt[0]) : ); SB();
  asm volatile("s_waitcnt vmcnt(13)" :  :  : "memory"); SB();
  asm volatile("v_mfma_f32_16x16x32_bf16 %0, v[216:219], a[112:115], %0" : "+v"(g[0][0][0]) :  : ); SB();
  asm volatile("buffer_load_dwordx4 v[180:183], %0, %1, %2 offen offset:1024 sc1" :  : "v"(vB), "s"(rWS), "s"(2048u) : "memory"); SB();
  asm volatile("v_exp_f32 %0, %1" : "=v"(ge[1]) : "v"(gt[1]) : ); SB();
  asm volatile("v_mfma_f32_16x16x32_bf16 %0, v[216:219], a[116:119], %0" : "+v"(g[0][1][0]) :  : ); SB();
  asm volatile("v_exp_f32 %0, %1" : "=v"(ge[2]) : "v"(gt[2]) : ); SB();
  asm volatile("v_mfma_f32_16x16x32_bf16 %0, v[216:219], a[120:123], %0" : "+v"(g[0][2][0]) :  : ); SB();
  asm volatile("v_exp_f32 %0, %1" : "=v"(ge[3]) : "v"(gt[3]) : ); SB();
  asm volatile("v_mfma_f32_16x16x32_bf16 %0, v[216:219], a[124:127], %0" : "+v"(g[0][3][0]) :  : ); SB();
  asm volatile("buffer_load_dwordx4 v[184:187], %0, %1, %2 offen offset:0 sc1" :  : "v"(vB), "s"(rWS), "s"(67584u) : "memory"); SB();
  asm volatile("v_fma_f32 %0, %0, %1, %1" : "+v"(ge[0]) : "v"(xd[0]) : ); SB();
  asm volatile("v_mfma_f32_16x16x32_bf16 %0, v[220:223], a[112:115], %0" : "+v"(g[0][0][1]) :  : ); SB();
  asm volatile("v_fma_f32 %0, %0, %1, %1" : "+v"(ge[1]) : "v"(xd[0]) : ); SB();
  asm volatile("v_mfma_f32_16x16x32_bf16 %0, v[220:223], a[116:119], %0" : "+v"(g[0][1][1]) :  : ); SB();
  asm volatile("v_fma_f32 %0, %0, %1, %1" : "+v"(ge[2]) : "v"(xd[0]) : ); SB();
  asm volatile("v_mfma_f32_16x16x32_bf16 %0, v[220:223], a[120:123], %0" : "+v"(g[0][2][1]) :  : ); SB();
  asm volatile("v_fma_f32 %0, %0, %1, %1" : "+v"(ge[3]) : "v"(xd[0]) : ); SB();
  asm volatile("v_mfma_f32_16x16x32_bf16 %0, v[220:223], a[124:127], %0" : "+v"(g[0][3][1]) :  : ); SB();
  asm volatile("buffer_load_dwordx4 v[188:191], %0, %1, %2 offen offset:1024 sc1" :  : "v"(vB), "s"(rWS), "s"(67584u) : "memory"); SB();
  asm volatile("s_waitcnt vmcnt(14)" :  :  : "memory"); SB();
  asm volatile("v_mfma_f32_16x16x32_bf16 %0, v[224:227], a[128:131], %0" : "+v"(g[0][0][0]) :  : ); SB();
  asm volatile("v_xor_b32 %0, 393216, %0" : "+v"(vB) :  : "memory"); SB();
  asm volatile("v_rcp_f32 %0, %1" : "=v"(gt[0]) : "v"(ge[0]) : ); SB();
  asm volatile("v_mfma_f32_16x16x32_bf16 %0, v[224:227], a[132:135], %0" : "+v"(g[0][1][0]) :  : ); SB();
  asm volatile("v_rcp_f32 %0, %1" : "=v"(gt[1]) : "v"(ge[1]) : ); SB();
  asm volatile("s_waitcnt lgkmcnt(0)\n\tv_mfma_f32_16x16x32_bf16 %0, v[224:227], a[136:139], %0" : "+v"(g[0][2][0]) :  : ); SB();
  asm volatile("buffer_load_dwordx4 v[192:195], %0, %1, %2 offen offset:0 sc1" :  : "v"(vB), "s"(rWS), "s"(2048u) : "memory"); SB();
  asm volatile("v_rcp_f32 %0, %1" : "=v"(gt[2]) : "v"(ge[2]) : ); SB();
  asm volatile("v_mfma_f32_16x16x32_bf16 %0, v[224:227], a[140:143], %0" : "+v"(g[0][3][0]) :  : ); SB();
  asm volatile("v_rcp_f32 %0, %1" : "=v"(gt[3]) : "v"(ge[3]) : ); SB();
  asm volatile("v_mfma_f32_16x16x32_bf16 %0, v[228:231], a[128:131], %0" : "+v"(g[0][0][1]) :  : ); SB();
  asm volatile("v_mul_f32 %0, %0, %1" : "+v"(a1[1][0][0]) : "v"(gt[0]) : ); SB();
  asm volatile("v_mfma_f32_16x16x32_bf16 %0, v[228:231], a[132:135], %0" : "+v"(g[0][1][1]) :  : ); SB();
  asm volatile("v_mul_f32 %0, %0, %1" : "+v"(a1[1][0][1]) : "v"(gt[1]) : ); SB();
  asm volatile("v_mfma_f32_16x16x32_bf16 %0, v[228:231], a[136:139], %0" : "+v"(g[0][2][1]) :  : ); SB();
  asm volatile("buffer_load_dwordx4 v[196:199], %0, %1, %2 offen offset:1024 sc1" :  : "v"(vB), "s"(rWS), "s"(2048u) : "memory"); SB();
  asm volatile("v_mul_f32 %0, %0, %1" : "+v"(a1[1][0][2]) : "v"(gt[2]) : ); SB();
  asm volatile("v_mfma_f32_16x16x32_bf16 %0, v[228:231], a[140:143], %0" : "+v"(g[0][3][1]) :  : ); SB();
  asm volatile("v_mul_f32 %0, %0, %1" : "+v"(a1[1][0][3]) : "v"(gt[3]) : ); SB();
  asm volatile("s_waitcnt vmcnt(14)" :  :  : "memory"); SB();
  asm volatile("v_mfma_f32_16x16x32_bf16 %0, v[232:235], a[144:147], %0" : "+v"(g[0][0][0]) :  : ); SB();
  asm volatile("v_mul_f32 %0, %1, %1" : "=v"(gt[0]) : "v"(a1[1][1][0]) : ); SB();
  asm volatile("v_mfma_f32_16x16x32_bf16 %0, v[232:235], a[148:151], %0" : "+v"(g[0][1][0]) :  : ); SB();
  asm volatile("buffer_load_dwordx4 v[200:203], %0, %1, %2 offen offset:0 sc1" :  : "v"(vB), "s"(rWS), "s"(67584u) : "memory"); SB();
  asm volatile("v_mul_f32 %0, %1, %1" : "=v"(gt[1]) : "v"(a1[1][1][1]) : ); SB();
  asm volatile("v_mfma_f32_16x16x32_bf16 %0, v[232:235], a[152:155], %0" : "+v"(g[0][2][0]) :  : ); SB();
  asm volatile("v_mul_f32 %0, %1, %1" : "=v"(gt[2]) : "v"(a1[1][1][2]) : ); SB();
  asm volatile("v_mfma_f32_16x16x32_bf16 %0, v[232:235], a[156:159], %0" : "+v"(g[0][3][0]) :  : ); SB();
  asm volatile("v_mul_f32 %0, %1, %1" : "=v"(gt[3]) : "v"(a1[1][1][3]) : ); SB();
  asm volatile("v_mfma_f32_16x16x32_bf16 %0, v[236:239], a[144:147], %0" : "+v"(g[0][0][1]) :  : ); SB();
  asm volatile("v_fma_f32 %0, %0, %1, %2" : "+v"(gt[0]) : "v"(k2[0]), "v"(k1[0]) : ); SB();
  asm volatile("v_mfma_f32_16x16x32_bf16 %0, v[236:239], a[148:151], %0" : "+v"(g[0][1][1]) :  : ); SB();
  asm volatile("buffer_load_dwordx4 v[204:207], %0, %1, %2 offen offset:1024 sc1" :  : "v"(vB), "s"(rWS), "s"(67584u) : "memory"); SB();
  asm volatile("v_fma_f32 %0, %0, %1, %2" : "+v"(gt[1]) : "v"(k2[0]), "v"(k1[0]) : ); SB();
  asm volatile("v_mfma_f32_16x16x32_bf16 %0, v[236:239], a[152:155], %0" : "+v"(g[0][2][1]) :  : ); SB();
  asm volatile("v_xor_b32 %0, 131072, %0" : "+v"(vB) :  : "memory"); SB();
  asm volatile("v_fma_f32 %0, %0, %1, %2" : "+v"(gt[2]) : "v"(k2[0]), "v"(k1[0]) : ); SB();
  asm volatile("v_mfma_f32_16x16x32_bf16 %0, v[236:239], a[156:159], %0" : "+v"(g[0][3][1]) :  : ); SB();
  asm volatile("v_fma_f32 %0, %0, %1, %2" : "+v"(gt[3]) : "v"(k2[0]), "v"(k1[0]) : ); SB();
  asm volatile("s_waitcnt vmcnt(14)" :  :  : "memory"); SB();
  asm volatile("v_mfma_f32_16x16x32_bf16 %0, v[240:243], a[0:3], %0" : "+v"(g[1][0][0]) :  : ); SB();
  asm volatile("s_barrier" :  :  : "memory"); SB();
  asm volatile("buffer_load_dwordx4 v[208:211], %0, %1, %2 offen offset:0 sc1" :  : "v"(vB), "s"(rWS), "s"(2048u) : "memory"); SB();
  asm volatile("v_mfma_f32_16x16x32_bf16 %0, v[240:243], a[4:7], %0" : "+v"(g[1][1][0]) :  : ); SB();
  asm volatile("v_mul_f32 %0, %0, %1" : "+v"(gt[0]) : "v"(a1[1][1][0]) : ); SB();
  asm volatile("v_mfma_f32_16x16x32_bf16 %0, v[240:243], a[8:11], %0" : "+v"(g[1][2][0]) :  : ); SB();
  asm volatile("ds_write_b128 %0, %1 offset:0" :  : "v"(pw2), "v"(g[0][1][0]) : "memory"); SB();
  asm volatile("v_mfma_f32_16x16x32_bf16 %0, v[240:243], a[12:15], %0" : "+v"(g[1][3][0]) :  : ); SB();
  asm volatile("buffer_load_dwordx4 v[212:215], %0, %1, %2 offen offset:1024 sc1" :  : "v"(vB), "s"(rWS), "s"(2048u) : "memory"); SB();
  asm volatile("v_mul_f32 %0, %0, %1" : "+v"(gt[1]) : "v"(a1[1][1][1]) : ); SB();
  asm volatile("v_mfma_f32_16x16x32_bf16 %0, v[244:247], a[0:3], %0" : "+v"(g[1][0][1]) :  : ); SB();
  asm volatile("v_mul_f32 %0, %0, %1" : "+v"(gt[2]) : "v"(a1[1][1][2]) : ); SB();
  asm volatile("v_mfma_f32_16x16x32_bf16 %0, v[244:247], a[4:7], %0" : "+v"(g[1][1][1]) :  : ); SB();
  asm volatile("ds_write_b128 %0, %1 offset:1024" :  : "v"(pw2), "v"(g[0][1][1]) : "memory"); SB();
  asm volatile("v_mfma_f32_16x16x32_bf16 %0, v[244:247], a[8:11], %0" : "+v"(g[1][2][1]) :  : ); SB();
  asm volatile("buffer_load_dwordx4 v[216:219], %0, %1, %2 offen offset:0 sc1" :  : "v"(vB), "s"(rWS), "s"(67584u) : "memory"); SB();
  asm volatile("v_mul_f32 %0, %0, %1" : "+v"(gt[3]) : "v"(a1[1][1][3]) : ); SB();
  asm volatile("v_mfma_f32_16x16x32_bf16 %0, v[244:247], a[12:15], %0" : "+v"(g[1][3][1]) :  : ); SB();
  asm volatile("v_exp_f32 %0, %1" : "=v"(ge[0]) : "v"(gt[0]) : ); SB();
  asm volatile("s_waitcnt vmcnt(15)" :  :  : "memory"); SB();
  asm volatile("v_mfma_f32_16x16x32_bf16 %0, v[248:251], a[16:19], %0" : "+v"(g[1][0][0]) :  : ); SB();
  asm volatile("ds_write_b128 %0, %1 offset:2048" :  : "v"(pw2), "v"(g[0][2][0]) : "memory"); SB();
  asm volatile("v_mfma_f32_16x16x32_bf16 %0, v[248:251], a[20:23], %0" : "+v"(g[1][1][0]) :  : ); SB();
  asm volatile("buffer_load_dwordx4 v[220:223], %0, %1, %2 offen offset:1024 sc1" :  : "v"(vB), "s"(rWS), "s"(67584u) : "memory"); SB();
  asm volatile("v_exp_f32 %0, %1" : "=v"(ge[1]) : "v"(gt[1]) : ); SB();
  asm volatile("v_mfma_f32_16x16x32_bf16 %0, v[248:251], a[24:27], %0" : "+v"(g[1][2][0]) :  : ); SB();
  asm volatile("v_xor_b32 %0, 917504, %0" : "+v"(vB) :  : "memory"); SB();
  asm volatile("v_exp_f32 %0, %1" : "=v"(ge[2]) : "v"(gt[2]) : ); SB();
  asm volatile("v_mfma_f32_16x16x32_bf16 %0, v[248:251], a[28:31], %0" : "+v"(g[1][3][0]) :  : ); SB();
  asm volatile("ds_write_b128 %0, %1 offset:3072" :  : "v"(pw2), "v"(g[0][2][1]) : "memory"); SB();
  asm volatile("v_mfma_f32_16x16x32_bf16 %0, v[252:255], a[16:19], %0" : "+v"(g[1][0][1]) :  : ); SB();
  asm volatile("buffer_load_dwordx4 v[224:227], %0, %1, %2 offen offset:0 sc1" :  : "v"(vB), "s"(rWS), "s"(4096u) : "memory"); SB();
  asm volatile("v_exp_f32 %0, %1" : "=v"(ge[3]) : "v"(gt[3]) : ); SB();
  asm volatile("v_mfma_f32_16x16x32_bf16 %0, v[252:255], a[20:23], %0" : "+v"(g[1][1][1]) :  : ); SB();
  asm volatile("buffer_load_dwordx4 v[228:231], %0, %1, %2 offen offset:1024 sc1" :  : "v"(vB), "s"(rWS), "s"(4096u) : "memory"); SB();
  asm volatile("v_fma_f32 %0, %0, %1, %1" : "+v"(ge[0]) : "v"(xd[0]) : ); SB();
  asm volatile("v_mfma_f32_16x16x32_bf16 %0, v[252:255], a[24:27], %0" : "+v"(g[1][2][1]) :  : ); SB();
  asm volatile("ds_write_b128 %0, %1 offset:4096" :  : "v"(pw2), "v"(g[0][3][0]) : "memory"); SB();
  asm volatile("v_mfma_f32_16x16x32_bf16 %0, v[252:255], a[28:31], %0" : "+v"(g[1][3][1]) :  : ); SB();
  asm volatile("v_fma_f32 %0, %0, %1, %1" : "+v"(ge[1]) : "v"(xd[0]) : ); SB();
  ST(28);
  asm volatile("s_waitcnt vmcnt(16)" :  :  : "memory"); SB();
  asm volatile("v_mfma_f32_16x16x32_bf16 %0, v[160:163], a[32:35], %0" : "+v"(g[1][0][0]) :  : ); SB();
  asm volatile("buffer_load_dwordx4 v[232:235], %0, %1, %2 offen offset:0 sc1" :  : "v"(vB), "s"(rWS), "s"(69632u) : "memory"); SB();
  asm volatile("v_fma_f32 %0, %0, %1, %1" : "+v"(ge[2]) : "v"(xd[0]) : ); SB();
  asm volatile("v_mfma_f32_16x16x32_bf16 %0, v[160:163], a[36:39], %0" : "+v"(g[1][1][0]) :  : ); SB();
  asm volatile("ds_write_b128 %0, %1 offset:5120" :  : "v"(pw2), "v"(g[0][3][1]) : "memory"); SB();
  asm volatile("v_mfma_f32_16x16x32_bf16 %0, v[160:163], a[40:43], %0" : "+v"(g[1][2][0]) :  : ); SB();
  asm volatile("buffer_load_dwordx4 v[236:239], %0, %1, %2 offen offset:1024 sc1" :  : "v"(vB), "s"(rWS), "s"(69632u) : "memory"); SB();
  asm volatile("v_fma_f32 %0, %0, %1, %1" : "+v"(ge[3]) : "v"(xd[0]) : ); SB();
  asm volatile("v_mfma_f32_16x16x32_bf16 %0, v[160:163], a[44:47], %0" : "+v"(g[1][3][0]) :  : ); SB();
  asm volatile("v_xor_b32 %0, 131072, %0" : "+v"(vB) :  : "memory"); SB();
  asm volatile("v_rcp_f32 %0, %1" : "=v"(gt[0]) : "v"(ge[0]) : ); SB();
  asm volatile("v_mfma_f32_16x16x32_bf16 %0, v[164:167], a[32:35], %0" : "+v"(g[1][0][1]) :  : ); SB();
  asm volatile("s_waitcnt lgkmcnt(0)\n\ts_barrier" :  :  : "memory"); SB();
  asm volatile("ds_read_b128 %0, %1 offset:0" : "=&v"(pr[0]) : "v"(pe[0]) : "memory"); SB();
  asm volatile("v_mfma_f32_16x16x32_bf16 %0, v[164:167], a[36:39], %0" : "+v"(g[1][1][1]) :  : ); SB();
  asm volatile("ds_read_b128 %0, %1 offset:1024" : "=&v"(pr[1]) : "v"(pe[0]) : "memory"); SB();
  asm volatile("v_rcp_f32 %0, %1" : "=v"(gt[1]) : "v"(ge[1]) : ); SB();
  asm volatile("v_mfma_f32_16x16x32_bf16 %0, v[164:167], a[40:43], %0" : "+v"(g[1][2][1]) :  : ); SB();
  asm volatile("ds_read_b128 %0, %1 offset:2048" : "=&v"(pr[2]) : "v"(pe[1]) : "memory"); SB();
  asm volatile("v_rcp_f32 %0, %1" : "=v"(gt[2]) : "v"(ge[2]) : ); SB();
  asm volatile("v_mfma_f32_16x16x32_bf16 %0, v[164:167], a[44:47], %0" : "+v"(g[1][3][1]) :  : ); SB();
  asm volatile("ds_read_b128 %0, %1 offset:3072" : "=&v"(pr[3]) : "v"(pe[1]) : "memory"); SB();
  asm volatile("s_waitcnt vmcnt(16)" :  :  : "memory"); SB();
  asm volatile("v_mfma_f32_16x16x32_bf16 %0, v[168:171], a[48:51], %0" : "+v"(g[1][0][0]) :  : ); SB();
  asm volatile("ds_read_b128 %0, %1 offset:4096" : "=&v"(pr[4]) : "v"(pe[2]) : "memory"); SB();
  asm volatile("v_rcp_f32 %0, %1" : "=v"(gt[3]) : "v"(ge[3]) : ); SB();
  asm volatile("v_mfma_f32_16x16x32_bf16 %0, v[168:171], a[52:55], %0" : "+v"(g[1][1][0]) :  : ); SB();
  asm volatile("buffer_load_dwordx4 v[240:243], %0, %1, %2 offen offset:0 sc1" :  : "v"(vB), "s"(rWS), "s"(4096u) : "memory"); SB();
  asm volatile("v_mul_f32 %0, %0, %1" : "+v"(a1[1][1][0]) : "v"(gt[0]) : ); SB();
  asm volatile("v_mfma_f32_16x16x32_bf16 %0, v[168:171], a[56:59], %0" : "+v"(g[1][2][0]) :  : ); SB();
  asm volatile("ds_read_b128 %0, %1 offset:5120" : "=&v"(pr[5]) : "v"(pe[2]) : "memory"); SB();
  asm volatile("v_mul_f32 %0, %0, %1" : "+v"(a1[1][1][1]) : "v"(gt[1]) : ); SB();
  asm volatile("v_mfma_f32_16x16x32_bf16 %0, v[168:171], a[60:63], %0" : "+v"(g[1][3][0]) :  : ); SB();
  asm volatile("buffer_load_dwordx4 v[244:247], %0, %1, %2 offen offset:1024 sc1" :  : "v"(vB), "s"(rWS), "s"(4096u) : "memory"); SB();
  asm volatile("v_mul_f32 %0, %0, %1" : "+v"(a1[1][1][2]) : "v"(gt[2]) : ); SB();
  asm volatile("v_mfma_f32_16x16x32_bf16 %0, v[172:175], a[48:51], %0" : "+v"(g[1][0][1]) :  : ); SB();
  asm volatile("v_mul_f32 %0, %0, %1" : "+v"(a1[1][1][3]) : "v"(gt[3]) : ); SB();
  asm volatile("v_mfma_f32_16x16x32_bf16 %0, v[172:175], a[52:55], %0" : "+v"(g[1][1][1]) :  : ); SB();
  asm volatile("buffer_load_dwordx4 v[248:251], %0, %1, %2 offen offset:0 sc1" :  : "v"(vB), "s"(rWS), "s"(69632u) : "memory"); SB();
  asm volatile("v_mul_f32 %0, %1, %1" : "=v"(gt[0]) : "v"(a1[1][2][0]) : ); SB();
  asm volatile("v_mfma_f32_16x16x32_bf16 %0, v[172:175], a[56:59], %0" : "+v"(g[1][2][1]) :  : ); SB();
  asm volatile("v_mul_f32 %0, %1, %1" : "=v"(gt[1]) : "v"(a1[1][2][1]) : ); SB();
  asm volatile("v_mfma_f32_16x16x32_bf16 %0, v[172:175], a[60:63], %0" : "+v"(g[1][3][1]) :  : ); SB();
  asm volatile("v_mul_f32 %0, %1, %1" : "=v"(gt[2]) : "v"(a1[1][2][2]) : ); SB();
  asm volatile("s_waitcnt vmcnt(17)" :  :  : "memory"); SB();
  asm volatile("v_mfma_f32_16x16x32_bf16 %0, v[176:179], a[64:67], %0" : "+v"(g[1][0][0]) :  : ); SB();
  asm volatile("s_waitcnt lgkmcnt(0)" : "+v"(pr[0]), "+v"(pr[1]), "+v"(pr[2]), "+v"(pr[3]), "+v"(pr[4]), "+v"(pr[5]) :  : "memory"); SB();
  asm volatile("buffer_load_dwordx4 v[252:255], %0, %1, %2 offen offset:1024 sc1" :  : "v"(vB), "s"(rWS), "s"(69632u) : "memory"); SB();
  asm volatile("v_mfma_f32_16x16x32_bf16 %0, v[176:179], a[68:71], %0" : "+v"(g[1][1][0]) :  : ); SB();
  asm volatile("v_xor_b32 %0, 393216, %0" : "+v"(vB) :  : "memory"); SB();
  asm volatile("v_add_f32 %0, %0, %1" : "+v"(pr[0][0]) : "v"(g[0][0][0][0]) : ); SB();
  asm volatile("v_mfma_f32_16x16x32_bf16 %0, v[176:179], a[72:75], %0" : "+v"(g[1][2][0]) :  : ); SB();
  asm volatile("v_add_f32 %0, %0, %1" : "+v"(pr[0][1]) : "v"(g[0][0][0][1]) : ); SB();
  asm volatile("v_mfma_f32_16x16x32_bf16 %0, v[176:179], a[76:79], %0" : "+v"(g[1][3][0]) :  : ); SB();
  asm volatile("buffer_load_dwordx4 v[160:163], %0, %1, %2 offen offset:0 sc1" :  : "v"(vB), "s"(rWS), "s"(4096u) : "memory"); SB();
  asm volatile("v_add_f32 %0, %0, %1" : "+v"(pr[0][2]) : "v"(g[0][0][0][2]) : ); SB();
  asm volatile("v_mfma_f32_16x16x32_bf16 %0, v[180:183], a[64:67], %0" : "+v"(g[1][0][1]) :  : ); SB();
  asm volatile("v_add_f32 %0, %0, %1" : "+v"(pr[0][3]) : "v"(g[0][0][0][3]) : ); SB();
  asm volatile("v_mfma_f32_16x16x32_bf16 %0, v[180:183], a[68:71], %0" : "+v"(g[1][1][1]) :  : ); SB();
  asm volatile("v_add_f32 %0, %0, %1" : "+v"(pr[1][0]) : "v"(g[0][0][1][0]) : ); SB();
  asm volatile("v_mfma_f32_16x16x32_bf16 %0, v[180:183], a[72:75], %0" : "+v"(g[1][2][1]) :  : ); SB();
  asm volatile("buffer_load_dwordx4 v[164:167], %0, %1, %2 offen offset:1024 sc1" :  : "v"(vB), "s"(rWS), "s"(4096u) : "memory"); SB();
  asm volatile("v_add_f32 %0, %0, %1" : "+v"(pr[1][1]) : "v"(g[0][0][1][1]) : ); SB();
  asm volatile("v_mfma_f32_16x16x32_bf16 %0, v[180:183], a[76:79], %0" : "+v"(g[1][3][1]) :  : ); SB();
  asm volatile("v_add_f32 %0, %0, %1" : "+v"(pr[1][2]) : "v"(g[0][0][1][2]) : ); SB();
  asm volatile("s_waitcnt vmcnt(18)" :  :  : "memory"); SB();
  asm volatile("v_mfma_f32_16x16x32_bf16 %0, v[184:187], a[80:83], %0" : "+v"(g[1][0][0]) :  : ); SB();
  asm volatile("v_add_f32 %0, %0, %1" : "+v"(pr[1][3]) : "v"(g[0][0][1][3]) : ); SB();
  asm volatile("v_mfma_f32_16x16x32_bf16 %0, v[184:187], a[84:87], %0" : "+v"(g[1][1][0]) :  : ); SB();
  asm volatile("v_add_f32 %0, %0, %1" : "+v"(pr[2][0]) : "v"(pr[4][0]) : ); SB();
  asm volatile("v_mfma_f32_16x16x32_bf16 %0, v[184:187], a[88:91], %0" : "+v"(g[1][2][0]) :  : ); SB();
  asm volatile("buffer_load_dwordx4 v[168:171], %0, %1, %2 offen offset:0 sc1" :  : "v"(vB), "s"(rWS), "s"(69632u) : "memory"); SB();
  asm volatile("v_add_f32 %0, %0, %1" : "+v"(pr[2][1]) : "v"(pr[4][1]) : ); SB();
  asm volatile("v_mfma_f32_16x16x32_bf16 %0, v[184:187], a[92:95], %0" : "+v"(g[1][3][0]) :  : ); SB();
  asm volatile("v_add_f32 %0, %0, %1" : "+v"(pr[2][2]) : "v"(pr[4][2]) : ); SB();
  asm volatile("v_mfma_f32_16x16x32_bf16 %0, v[188:191], a[80:83], %0" : "+v"(g[1][0][1]) :  : ); SB();
  asm volatile("v_add_f32 %0, %0, %1" : "+v"(pr[2][3]) : "v"(pr[4][3]) : ); SB();
  asm volatile("v_mfma_f32_16x16x32_bf16 %0, v[188:191], a[84:87], %0" : "+v"(g[1][1][1]) :  : ); SB();
  asm volatile("buffer_load_dwordx4 v[172:175], %0, %1, %2 offen offset:1024 sc1" :  : "v"(vB), "s"(rWS), "s"(69632u) : "memory"); SB();
  asm volatile("v_add_f32 %0, %0, %1" : "+v"(pr[3][0]) : "v"(pr[5][0]) : ); SB();
  asm volatile("v_mfma_f32_16x16x32_bf16 %0, v[188:191], a[88:91], %0" : "+v"(g[1][2][1]) :  : ); SB();
  asm volatile("v_xor_b32 %0, 131072, %0" : "+v"(vB) :  : "memory"); SB();
  asm volatile("v_add_f32 %0, %0, %1" : "+v"(pr[3][1]) : "v"(pr[5][1]) : ); SB();
  asm volatile("v_mfma_f32_16x16x32_bf16 %0, v[188:191], a[92:95], %0" : "+v"(g[1][3][1]) :  : ); SB();
  asm volatile("v_add_f32 %0, %0, %1" : "+v"(pr[3][2]) : "v"(pr[5][2]) : ); SB();
  asm volatile("s_waitcnt vmcnt(18)" :  :  : "memory"); SB();
  asm volatile("v_mfma_f32_16x16x32_bf16 %0, v[192:195], a[96:99], %0" : "+v"(g[1][0][0]) :  : ); SB();
  asm volatile("v_add_f32 %0, %0, %1" : "+v"(pr[3][3]) : "v"(pr[5][3]) : ); SB();
  asm volatile("v_mfma_f32_16x16x32_bf16 %0, v[192:195], a[100:103], %0" : "+v"(g[1][1][0]) :  : ); SB();
  asm volatile("buffer_load_dwordx4 v[176:179], %0, %1, %2 offen offset:0 sc1" :  : "v"(vB), "s"(rWS), "s"(4096u) : "memory"); SB();
  asm volatile("v_add_f32 %0, %0, %1" : "+v"(pr[0][0]) : "v"(pr[2][0]) : ); SB();
  asm volatile("v_mfma_f32_16x16x32_bf16 %0, v[192:195], a[104:107], %0" : "+v"(g[1][2][0]) :  : ); SB();
  asm volatile("v_add_f32 %0, %0, %1" : "+v"(pr[0][1]) : "v"(pr[2][1]) : ); SB();
  asm volatile("v_mfma_f32_16x16x32_bf16 %0, v[192:195], a[108:111], %0" : "+v"(g[1][3][0]) :  : ); SB();
  asm volatile("v_add_f32 %0, %0, %1" : "+v"(pr[0][2]) : "v"(pr[2][2]) : ); SB();
  asm volatile("v_mfma_f32_16x16x32_bf16 %0, v[196:199], a[96:99], %0" : "+v"(g[1][0][1]) :  : ); SB();
  asm volatile("buffer_load_dwordx4 v[180:183], %0, %1, %2 offen offset:1024 sc1" :  : "v"(vB), "s"(rWS), "s"(4096u) : "memory"); SB();
  asm volatile("v_add_f32 %0, %0, %1" : "+v"(pr[0][3]) : "v"(pr[2][3]) : ); SB();
  asm volatile("v_mfma_f32_16x16x32_bf16 %0, v[196:199], a[100:103], %0" : "+v"(g[1][1][1]) :  : ); SB();
  asm volatile("v_add_f32 %0, %0, %1" : "+v"(pr[1][0]) : "v"(pr[3][0]) : ); SB();
  asm volatile("v_mfma_f32_16x16x32_bf16 %0, v[196:199], a[104:107], %0" : "+v"(g[1][2][1]) :  : ); SB();
  asm volatile("v_add_f32 %0, %0, %1" : "+v"(pr[1][1]) : "v"(pr[3][1]) : ); SB();
  asm volatile("v_mfma_f32_16x16x32_bf16 %0, v[196:199], a[108:111], %0" : "+v"(g[1][3][1]) :  : ); SB();
  asm volatile("v_add_f32 %0, %0, %1" : "+v"(pr[1][2]) : "v"(pr[3][2]) : ); SB();
  asm volatile("s_waitcnt vmcnt(18)" :  :  : "memory"); SB();
  asm volatile("v_mfma_f32_16x16x32_bf16 %0, v[200:203], a[112:115], %0" : "+v"(g[1][0][0]) :  : ); SB();
  asm volatile("buffer_load_dwordx4 v[184:187], %0, %1, %2 offen offset:0 sc1" :  : "v"(vB), "s"(rWS), "s"(69632u) : "memory"); SB();
  asm volatile("v_add_f32 %0, %0, %1" : "+v"(pr[1][3]) : "v"(pr[3][3]) : ); SB();
  asm volatile("v_mfma_f32_16x16x32_bf16 %0, v[200:203], a[116:119], %0" : "+v"(g[1][1][0]) :  : ); SB();
  asm volatile("v_mul_f32 %0, %0, %1" : "+v"(pr[0][0]) : "v"(a1[0][0][0]) : ); SB();
  asm volatile("v_mfma_f32_16x16x32_bf16 %0, v[200:203], a[120:123], %0" : "+v"(g[1][2][0]) :  : ); SB();
  asm volatile("v_mul_f32 %0, %0, %1" : "+v"(pr[0][1]) : "v"(a1[0][0][1]) : ); SB();
  asm volatile("v_mfma_f32_16x16x32_bf16 %0, v[200:203], a[124:127], %0" : "+v"(g[1][3][0]) :  : ); SB();
  asm volatile("buffer_load_dwordx4 v[188:191], %0, %1, %2 offen offset:1024 sc1" :  : "v"(vB), "s"(rWS), "s"(69632u) : "memory"); SB();
  asm volatile("v_mul_f32 %0, %0, %1" : "+v"(pr[0][2]) : "v"(a1[0][0][2]) : ); SB();
  asm volatile("v_mfma_f32_16x16x32_bf16 %0, v[204:207], a[112:115], %0" : "+v"(g[1][0][1]) :  : ); SB();
  asm volatile("v_xor_b32 %0, 917504, %0" : "+v"(vB) :  : "memory"); SB();
  asm volatile("v_mul_f32 %0, %0, %1" : "+v"(pr[0][3]) : "v"(a1[0][0][3]) : ); SB();
  asm volatile("v_mfma_f32_16x16x32_bf16 %0, v[204:207], a[116:119], %0" : "+v"(g[1][1][1]) :  : ); SB();
  asm volatile("v_mul_f32 %0, %0, %1" : "+v"(pr[1][0]) : "v"(a1[0][1][0]) : ); SB();
  asm volatile("v_mfma_f32_16x16x32_bf16 %0, v[204:207], a[120:123], %0" : "+v"(g[1][2][1]) :  : ); SB();
  asm volatile("v_mul_f32 %0, %0, %1" : "+v"(pr[1][1]) : "v"(a1[0][1][1]) : ); SB();
  asm volatile("v_mfma_f32_16x16x32_bf16 %0, v[204:207], a[124:127], %0" : "+v"(g[1][3][1]) :  : ); SB();
  asm volatile("buffer_load_dwordx4 v[192:195], %0, %1, %2 offen offset:0 sc1" :  : "v"(vB), "s"(rWS), "s"(4096u) : "memory"); SB();
  asm volatile("s_waitcnt vmcnt(19)" :  :  : "memory"); SB();
  asm volatile("v_mfma_f32_16x16x32_bf16 %0, v[208:211], a[128:131], %0" : "+v"(g[1][0][0]) :  : ); SB();
  asm volatile("v_mul_f32 %0, %0, %1" : "+v"(pr[1][2]) : "v"(a1[0][1][2]) : ); SB();
  asm volatile("v_mfma_f32_16x16x32_bf16 %0, v[208:211], a[132:135], %0" : "+v"(g[1][1][0]) :  : ); SB();
  asm volatile("v_mul_f32 %0, %0, %1" : "+v"(pr[1][3]) : "v"(a1[0][1][3]) : ); SB();
  asm volatile("v_mfma_f32_16x16x32_bf16 %0, v[208:211], a[136:139], %0" : "+v"(g[1][2][0]) :  : ); SB();
  asm volatile("buffer_load_dwordx4 v[196:199], %0, %1, %2 offen offset:1024 sc1" :  : "v"(vB), "s"(rWS), "s"(4096u) : "memory"); SB();
  asm volatile("v_cvt_pk_bf16_f32 %0, %1, %2" : "=v"(ho[0][0]) : "v"(pr[0][0]), "v"(pr[0][1]) : ); SB();
  asm volatile("v_mfma_f32_16x16x32_bf16 %0, v[208:211], a[140:143], %0" : "+v"(g[1][3][0]) :  : ); SB();
  asm volatile("v_cvt_pk_bf16_f32 %0, %1, %2" : "=v"(ho[0][1]) : "v"(pr[0][2]), "v"(pr[0][3]) : ); SB();
  asm volatile("v_mfma_f32_16x16x32_bf16 %0, v[212:215], a[128:131], %0" : "+v"(g[1][0][1]) :  : ); SB();
  asm volatile("v_cvt_pk_bf16_f32 %0, %1, %2" : "=v"(ho[0][2]) : "v"(pr[1][0]), "v"(pr[1][1]) : ); SB();
  asm volatile("v_mfma_f32_16x16x32_bf16 %0, v[212:215], a[132:135], %0" : "+v"(g[1][1][1]) :  : ); SB();
  asm volatile("v_cvt_pk_bf16_f32 %0, %1, %2" : "=v"(ho[0][3]) : "v"(pr[1][2]), "v"(pr[1][3]) : ); SB();
  asm volatile("v_mfma_f32_16x16x32_bf16 %0, v[212:215], a[136:139], %0" : "+v"(g[1][2][1]) :  : ); SB();
  asm volatile("v_mul_f32 %0, %1, %1" : "=v"(gt[3]) : "v"(a1[1][2][3]) : ); SB();
  asm volatile("v_mfma_f32_16x16x32_bf16 %0, v[212:215], a[140:143], %0" : "+v"(g[1][3][1]) :  : ); SB();
  asm volatile("v_fma_f32 %0, %0, %1, %2" : "+v"(gt[0]) : "v"(k2[0]), "v"(k1[0]) : ); SB();
  asm volatile("s_waitcnt vmcnt(18)" :  :  : "memory"); SB();
  asm volatile("v_mfma_f32_16x16x32_bf16 %0, v[216:219], a[144:147], %0" : "+v"(g[1][0][0]) :  : ); SB();
  asm volatile("v_fma_f32 %0, %0, %1, %2" : "+v"(gt[1]) : "v"(k2[0]), "v"(k1[0]) : ); SB();
  asm volatile("v_mfma_f32_16x16x32_bf16 %0, v[216:219], a[148:151], %0" : "+v"(g[1][1][0]) :  : ); SB();
  asm volatile("buffer_store_dwordx4 %1, %0, %2, %3 offen offset:0 sc1\n\ts_nop 1" :  : "v"(vH_), "v"(ho[0]), "s"(rWS), "s"(0u) : "memory"); SB();
  asm volatile("v_fma_f32 %0, %0, %1, %2" : "+v"(gt[2]) : "v"(k2[0]), "v"(k1[0]) : ); SB();
  asm volatile("v_mfma_f32_16x16x32_bf16 %0, v[216:219], a[152:155], %0" : "+v"(g[1][2][0]) :  : ); SB();
  asm volatile("v_fma_f32 %0, %0, %1, %2" : "+v"(gt[3]) : "v"(k2[0]), "v"(k1[0]) : ); SB();
  asm volatile("v_mfma_f32_16x16x32_bf16 %0, v[216:219], a[156:159], %0" : "+v"(g[1][3][0]) :  : ); SB();
  asm volatile("v_mul_f32 %0, %0, %1" : "+v"(gt[0]) : "v"(a1[1][2][0]) : ); SB();
  asm volatile("v_mfma_f32_16x16x32_bf16 %0, v[220:223], a[144:147], %0" : "+v"(g[1][0][1]) :  : ); SB();
  asm volatile("v_mul_f32 %0, %0, %1" : "+v"(gt[1]) : "v"(a1[1][2][1]) : ); SB();
  asm volatile("v_mfma_f32_16x16x32_bf16 %0, v[220:223], a[148:151], %0" : "+v"(g[1][1][1]) :  : ); SB();
  asm volatile("buffer_load_dwordx4 v[200:203], %0, %1, %2 offen offset:0 sc1" :  : "v"(vB), "s"(rWS), "s"(69632u) : "memory"); SB();
  asm volatile("v_mul_f32 %0, %0, %1" : "+v"(gt[2]) : "v"(a1[1][2][2]) : ); SB();
  asm volatile("v_mfma_f32_16x16x32_bf16 %0, v[220:223], a[152:155], %0" : "+v"(g[1][2][1]) :  : ); SB();
  asm volatile("v_mul_f32 %0, %0, %1" : "+v"(gt[3]) : "v"(a1[1][2][3]) : ); SB();
  asm volatile("v_mfma_f32_16x16x32_bf16 %0, v[220:223], a[156:159], %0" : "+v"(g[1][3][1]) :  : ); SB();
  asm volatile("v_exp_f32 %0, %1" : "=v"(ge[0]) : "v"(gt[0]) : ); SB();
  ST(29);
  asm volatile("s_waitcnt vmcnt(18)" :  :  : "memory"); SB();
  asm volatile("v_mfma_f32_16x16x32_bf16 %0, v[224:227], a[160:163], %1" : "=&v"(g[0][0][0]) : "v"(bs4) : ); SB();
  asm volatile("s_barrier" :  :  : "memory"); SB();
  asm volatile("buffer_load_dwordx4 v[204:207], %0, %1, %2 offen offset:1024 sc1" :  : "v"(vB), "s"(rWS), "s"(69632u) : "memory"); SB();
  asm volatile("v_mfma_f32_16x16x32_bf16 %0, v[224:227], a[164:167], 0" : "=&v"(g[0][1][0]) :  : ); SB();
  asm volatile("v_xor_b32 %0, 131072, %0" : "+v"(vB) :  : "memory"); SB();
  asm volatile("v_exp_f32 %0, %1" : "=v"(ge[1]) : "v"(gt[1]) : ); SB();
  asm volatile("v_mfma_f32_16x16x32_bf16 %0, v[224:227], a[168:171], 0" : "=&v"(g[0][2][0]) :  : ); SB();
  asm volatile("ds_write_b128 %0, %1 offset:0" :  : "v"(pw2), "v"(g[1][1][0]) : "memory"); SB();
  asm volatile("v_mfma_f32_16x16x32_bf16 %0, v[224:227], a[172:175], 0" : "=&v"(g[0][3][0]) :  : ); SB();
  asm volatile("buffer_load_dwordx4 v[208:211], %0, %1, %2 offen offset:0 sc1" :  : "v"(vB), "s"(rWS), "s"(4096u) : "memory"); SB();
  asm volatile("v_exp_f32 %0, %1" : "=v"(ge[2]) : "v"(gt[2]) : ); SB();
  asm volatile("v_mfma_f32_16x16x32_bf16 %0, v[228:231], a[160:163], %1" : "=&v"(g[0][0][1]) : "v"(bs4) : ); SB();
  asm volatile("buffer_load_dwordx4 v[212:215], %0, %1, %2 offen offset:1024 sc1" :  : "v"(vB), "s"(rWS), "s"(4096u) : "memory"); SB();
  asm volatile("v_exp_f32 %0, %1" : "=v"(ge[3]) : "v"(gt[3]) : ); SB();
  asm volatile("v_mfma_f32_16x16x32_bf16 %0, v[228:231], a[164:167], 0" : "=&v"(g[0][1][1]) :  : ); SB();
  asm volatile("ds_write_b128 %0, %1 offset:1024" :  : "v"(pw2), "v"(g[1][1][1]) : "memory"); SB();
  asm volatile("v_mfma_f32_16x16x32_bf16 %0, v[228:231], a[168:171], 0" : "=&v"(g[0][2][1]) :  : ); SB();
  asm volatile("buffer_load_dwordx4 v[216:219], %0, %1, %2 offen offset:0 sc1" :  : "v"(vB), "s"(rWS), "s"(69632u) : "memory"); SB();
  asm volatile("v_fma_f32 %0, %0, %1, %1" : "+v"(ge[0]) : "v"(xd[0]) : ); SB();
  asm volatile("v_mfma_f32_16x16x32_bf16 %0, v[228:231], a[172:175], 0" : "=&v"(g[0][3][1]) :  : ); SB();
  asm volatile("v_fma_f32 %0, %0, %1, %1" : "+v"(ge[1]) : "v"(xd[0]) : ); SB();
  asm volatile("s_waitcnt vmcnt(20)" :  :  : "memory"); SB();
  asm volatile("v_mfma_f32_16x16x32_bf16 %0, v[232:235], a[176:179], %0" : "+v"(g[0][0][0]) :  : ); SB();
  asm volatile("ds_write_b128 %0, %1 offset:2048" :  : "v"(pw2), "v"(g[1][2][0]) : "memory"); SB();
  asm volatile("v_mfma_f32_16x16x32_bf16 %0, v[232:235], a[180:183], %0" : "+v"(g[0][1][0]) :  : ); SB();
  asm volatile("buffer_load_dwordx4 v[220:223], %0, %1, %2 offen offset:1024 sc1" :  : "v"(vB), "s"(rWS), "s"(69632u) : "memory"); SB();
  asm volatile("v_fma_f32 %0, %0, %1, %1" : "+v"(ge[2]) : "v"(xd[0]) : ); SB();
  asm volatile("v_mfma_f32_16x16x32_bf16 %0, v[232:235], a[184:187], %0" : "+v"(g[0][2][0]) :  : ); SB();
  asm volatile("v_xor_b32 %0, 393216, %0" : "+v"(vB) :  : "memory"); SB();
  asm volatile("v_fma_f32 %0, %0, %1, %1" : "+v"(ge[3]) : "v"(xd[0]) : ); SB();
  asm volatile("v_mfma_f32_16x16x32_bf16 %0, v[232:235], a[188:191], %0" : "+v"(g[0][3][0]) :  : ); SB();
  asm volatile("ds_write_b128 %0, %1 offset:3072" :  : "v"(pw2), "v"(g[1][2][1]) : "memory"); SB();
  asm volatile("v_mfma_f32_16x16x32_bf16 %0, v[236:239], a[176:179], %0" : "+v"(g[0][0][1]) :  : ); SB();
  asm volatile("buffer_load_dwordx4 v[224:227], %0, %1, %2 offen offset:0 sc1" :  : "v"(vB), "s"(rWS), "s"(4096u) : "memory"); SB();
  asm volatile("v_rcp_f32 %0, %1" : "=v"(gt[0]) : "v"(ge[0]) : ); SB();
  asm volatile("v_mfma_f32_16x16x32_bf16 %0, v[236:239], a[180:183], %0" : "+v"(g[0][1][1]) :  : ); SB();
  asm volatile("v_rcp_f32 %0, %1" : "=v"(gt[1]) : "v"(ge[1]) : ); SB();
  asm volatile("v_mfma_f32_16x16x32_bf16 %0, v[236:239], a[184:187], %0" : "+v"(g[0][2][1]) :  : ); SB();
  asm volatile("ds_write_b128 %0, %1 offset:4096" :  : "v"(pw2), "v"(g[1][3][0]) : "memory"); SB();
  asm volatile("v_mfma_f32_16x16x32_bf16 %0, v[236:239], a[188:191], %0" : "+v"(g[0][3][1]) :  : ); SB();
  asm volatile("buffer_load_dwordx4 v[228:231], %0, %1, %2 offen offset:1024 sc1" :  : "v"(vB), "s"(rWS), "s"(4096u) : "memory"); SB();
  asm volatile("s_waitcnt vmcnt(21)" :  :  : "memory"); SB();
  asm volatile("v_mfma_f32_16x16x32_bf16 %0, v[240:243], a[192:195], %0" : "+v"(g[0][0][0]) :  : ); SB();
  asm volatile("v_rcp_f32 %0, %1" : "=v"(gt[2]) : "v"(ge[2]) : ); SB();
  asm volatile("v_mfma_f32_16x16x32_bf16 %0, v[240:243], a[196:199], %0" : "+v"(g[0][1][0]) :  : ); SB();
  asm volatile("ds_write_b128 %0, %1 offset:5120" :  : "v"(pw2), "v"(g[1][3][1]) : "memory"); SB();
  asm volatile("v_mfma_f32_16x16x32_bf16 %0, v[240:243], a[200:203], %0" : "+v"(g[0][2][0]) :  : ); SB();
  asm volatile("buffer_load_dwordx4 v[232:235], %0, %1, %2 offen offset:0 sc1" :  : "v"(vB), "s"(rWS), "s"(69632u) : "memory"); SB();
  asm volatile("v_rcp_f32 %0, %1" : "=v"(gt[3]) : "v"(ge[3]) : ); SB();
  asm volatile("v_mfma_f32_16x16x32_bf16 %0, v[240:243], a[204:207], %0" : "+v"(g[0][3][0]) :  : ); SB();
  asm volatile("buffer_load_dwordx4 v[236:239], %0, %1, %2 offen offset:1024 sc1" :  : "v"(vB), "s"(rWS), "s"(69632u) : "memory"); SB();
  asm volatile("v_mul_f32 %0, %0, %1" : "+v"(a1[1][2][0]) : "v"(gt[0]) : ); SB();
  asm volatile("v_mfma_f32_16x16x32_bf16 %0, v[244:247], a[192:195], %0" : "+v"(g[0][0][1]) :  : ); SB();
  asm volatile("s_waitcnt lgkmcnt(0)\n\ts_barrier" :  :  : "memory"); SB();
  asm volatile("ds_read_b128 %0, %1 offset:0" : "=&v"(pr[0]) : "v"(pe[0]) : "memory"); SB();
  asm volatile("v_mfma_f32_16x16x32_bf16 %0, v[244:247], a[196:199], %0" : "+v"(g[0][1][1]) :  : ); SB();
  asm volatile("ds_read_b128 %0, %1 offset:1024" : "=&v"(pr[1]) : "v"(pe[0]) : "memory"); SB();
  asm volatile("v_mul_f32 %0, %0, %1" : "+v"(a1[1][2][1]) : "v"(gt[1]) : ); SB();
  asm volatile("v_mfma_f32_16x16x32_bf16 %0, v[244:247], a[200:203], %0" : "+v"(g[0][2][1]) :  : ); SB();
  asm volatile("ds_read_b128 %0, %1 offset:2048" : "=&v"(pr[2]) : "v"(pe[1]) : "memory"); SB();
  asm volatile("v_mul_f32 %0, %0, %1" : "+v"(a1[1][2][2]) : "v"(gt[2]) : ); SB();
  asm volatile("v_mfma_f32_16x16x32_bf16 %0, v[244:247], a[204:207], %0" : "+v"(g[0][3][1]) :  : ); SB();
  asm volatile("ds_read_b128 %0, %1 offset:3072" : "=&v"(pr[3]) : "v"(pe[1]) : "memory"); SB();
  asm volatile("s_waitcnt vmcnt(21)" :  :  : "memory"); SB();
  asm volatile("v_mfma_f32_16x16x32_bf16 %0, v[248:251], a[208:211], %0" : "+v"(g[0][0][0]) :  : ); SB();
  asm volatile("ds_read_b128 %0, %1 offset:4096" : "=&v"(pr[4]) : "v"(pe[2]) : "memory"); SB();
  asm volatile("v_mul_f32 %0, %0, %1" : "+v"(a1[1][2][3]) : "v"(gt[3]) : ); SB();
  asm volatile("v_mfma_f32_16x16x32_bf16 %0, v[248:251], a[212:215], %0" : "+v"(g[0][1][0]) :  : ); SB();
  asm volatile("ds_read_b128 %0, %1 offset:5120" : "=&v"(pr[5]) : "v"(pe[2]) : "memory"); SB();
  asm volatile("v_mfma_f32_16x16x32_bf16 %0, v[248:251], a[216:219], %0" : "+v"(g[0][2][0]) :  : ); SB();
  asm volatile("v_xor_b32 %0, 131072, %0" : "+v"(vB) :  : "memory"); SB();
  asm volatile("v_mfma_f32_16x16x32_bf16 %0, v[248:251], a[220:223], %0" : "+v"(g[0][3][0]) :  : ); SB();
  asm volatile("buffer_load_dwordx4 v[240:243], %0, %1, %2 offen offset:0 sc1" :  : "v"(vB), "s"(rWS), "s"(4096u) : "memory"); SB();
  asm volatile("v_mfma_f32_16x16x32_bf16 %0, v[252:255], a[208:211], %0" : "+v"(g[0][0][1]) :  : ); SB();
  asm volatile("buffer_load_dwordx4 v[244:247], %0, %1, %2 offen offset:1024 sc1" :  : "v"(vB), "s"(rWS), "s"(4096u) : "memory"); SB();
  asm volatile("v_mfma_f32_16x16x32_bf16 %0, v[252:255], a[212:215], %0" : "+v"(g[0][1][1]) :  : ); SB();
  asm volatile("v_mfma_f32_16x16x32_bf16 %0, v[252:255], a[216:219], %0" : "+v"(g[0][2][1]) :  : ); SB();
  asm volatile("v_mfma_f32_16x16x32_bf16 %0, v[252:255], a[220:223], %0" : "+v"(g[0][3][1]) :  : ); SB();
  asm volatile("s_waitcnt lgkmcnt(0)" : "+v"(pr[0]), "+v"(pr[1]), "+v"(pr[2]), "+v"(pr[3]), "+v"(pr[4]), "+v"(pr[5]) :  : "memory"); SB();
  asm volatile("s_add_u32 m0, %0, 60032" :  : "s"(mU) : "memory", "scc"); SB();
  asm volatile("s_waitcnt vmcnt(21)" :  :  : "memory"); SB();
  asm volatile("v_mfma_f32_16x16x32_bf16 %0, v[160:163], a[224:227], %0" : "+v"(g[0][0][0]) :  : ); SB();
  asm volatile("buffer_load_dwordx4 %0, %1, %2 offen sc1 lds" :  : "v"(vF), "s"(rWS), "s"(W_KV) : "memory"); SB();
  asm volatile("v_add_f32 %0, %0, %1" : "+v"(pr[0][0]) : "v"(g[1][0][0][0]) : ); SB();
  asm volatile("v_mfma_f32_16x16x32_bf16 %0, v[160:163], a[228:231], %0" : "+v"(g[0][1][0]) :  : ); SB();
  asm volatile("v_add_f32 %0, %0, %1" : "+v"(pr[0][1]) : "v"(g[1][0][0][1]) : ); SB();
  asm volatile("v_mfma_f32_16x16x32_bf16 %0, v[160:163], a[232:235], %0" : "+v"(g[0][2][0]) :  : ); SB();
  asm volatile("v_add_f32 %0, %0, %1" : "+v"(pr[0][2]) : "v"(g[1][0][0][2]) : ); SB();
  asm volatile("v_mfma_f32_16x16x32_bf16 %0, v[160:163], a[236:239], %0" : "+v"(g[0][3][0]) :  : ); SB();
  asm volatile("v_add_f32 %0, %0, %1" : "+v"(pr[0][3]) : "v"(g[1][0][0][3]) : ); SB();
  asm volatile("v_mfma_f32_16x16x32_bf16 %0, v[164:167], a[224:227], %0" : "+v"(g[0][0][1]) :  : ); SB();
  asm volatile("v_add_f32 %0, %0, %1" : "+v"(pr[1][0]) : "v"(g[1][0][1][0]) : ); SB();
  asm volatile("v_mfma_f32_16x16x32_bf16 %0, v[164:167], a[228:231], %0" : "+v"(g[0][1][1]) :  : ); SB();
  asm volatile("v_add_f32 %0, %0, %1" : "+v"(pr[1][1]) : "v"(g[1][0][1][1]) : ); SB();
  asm volatile("v_mfma_f32_16x16x32_bf16 %0, v[164:167], a[232:235], %0" : "+v"(g[0][2][1]) :  : ); SB();
  asm volatile("v_add_f32 %0, %0, %1" : "+v"(pr[1][2]) : "v"(g[1][0][1][2]) : ); SB();
  asm volatile("v_mfma_f32_16x16x32_bf16 %0, v[164:167], a[236:239], %0" : "+v"(g[0][3][1]) :  : ); SB();
  asm volatile("v_add_f32 %0, %0, %1" : "+v"(pr[1][3]) : "v"(g[1][0][1][3]) : ); SB();
  asm volatile("s_waitcnt vmcnt(20)" :  :  : "memory"); SB();
  asm volatile("v_mfma_f32_16x16x32_bf16 %0, v[168:171], a[240:243], %0" : "+v"(g[0][0][0]) :  : ); SB();
  asm volatile("v_add_f32 %0, %0, %1" : "+v"(pr[2][0]) : "v"(pr[4][0]) : ); SB();
  asm volatile("v_mfma_f32_16x16x32_bf16 %0, v[168:171], a[244:247], %0" : "+v"(g[0][1][0]) :  : ); SB();
  asm volatile("s_add_u32 m0, %0, 72960" :  : "s"(mU) : "memory", "scc"); SB();
  asm volatile("v_add_f32 %0, %0, %1" : "+v"(pr[2][1]) : "v"(pr[4][1]) : ); SB();
  asm volatile("v_mfma_f32_16x16x32_bf16 %0, v[168:171], a[248:251], %0" : "+v"(g[0][2][0]) :  : ); SB();
  asm volatile("buffer_load_dwordx4 %0, %1, %2 offen sc1 lds" :  : "v"(vF), "s"(rWS), "s"(W_KV + 4096) : "memory"); SB();
  asm volatile("v_add_f32 %0, %0, %1" : "+v"(pr[2][2]) : "v"(pr[4][2]) : ); SB();
  asm volatile("v_mfma_f32_16x16x32_bf16 %0, v[168:171], a[252:255], %0" : "+v"(g[0][3][0]) :  : ); SB();
  asm volatile("v_add_f32 %0, %0, %1" : "+v"(pr[2][3]) : "v"(pr[4][3]) : ); SB();
  asm volatile("v_mfma_f32_16x16x32_bf16 %0, v[172:175], a[240:243], %0" : "+v"(g[0][0][1]) :  : ); SB();
  asm volatile("v_add_f32 %0, %0, %1" : "+v"(pr[3][0]) : "v"(pr[5][0]) : ); SB();
  asm volatile("v_mfma_f32_16x16x32_bf16 %0, v[172:175], a[244:247], %0" : "+v"(g[0][1][1]) :  : ); SB();
  asm volatile("buffer_load_dwordx4 v[248:251], %0, %1, %2 offen offset:0 sc1" :  : "v"(vB), "s"(rWS), "s"(69632u) : "memory"); SB();
  asm volatile("v_add_f32 %0, %0, %1" : "+v"(pr[3][1]) : "v"(pr[5][1]) : ); SB();
  asm volatile("v_mfma_f32_16x16x32_bf16 %0, v[172:175], a[248:251], %0" : "+v"(g[0][2][1]) :  : ); SB();
  asm volatile("v_add_f32 %0, %0, %1" : "+v"(pr[3][2]) : "v"(pr[5][2]) : ); SB();
  asm volatile("v_mfma_f32_16x16x32_bf16 %0, v[172:175], a[252:255], %0" : "+v"(g[0][3][1]) :  : ); SB();
  asm volatile("v_add_f32 %0, %0, %1" : "+v"(pr[3][3]) : "v"(pr[5][3]) : ); SB();
  asm volatile("s_waitcnt vmcnt(20)" :  :  : "memory"); SB();
  asm volatile("v_mfma_f32_16x16x32_bf16 %0, v[176:179], a[0:3], %0" : "+v"(g[0][0][0]) :  : ); SB();
  asm volatile("v_add_f32 %0, %0, %1" : "+v"(pr[0][0]) : "v"(pr[2][0]) : ); SB();
  asm volatile("v_mfma_f32_16x16x32_bf16 %0, v[176:179], a[4:7], %0" : "+v"(g[0][1][0]) :  : ); SB();
  asm volatile("buffer_load_dwordx4 v[252:255], %0, %1, %2 offen offset:1024 sc1" :  : "v"(vB), "s"(rWS), "s"(69632u) : "memory"); SB();
  asm volatile("v_add_f32 %0, %0, %1" : "+v"(pr[0][1]) : "v"(pr[2][1]) : ); SB();
  asm volatile("v_mfma_f32_16x16x32_bf16 %0, v[176:179], a[8:11], %0" : "+v"(g[0][2][0]) :  : ); SB();
  asm volatile("v_add_f32 %0, %0, %1" : "+v"(pr[0][2]) : "v"(pr[2][2]) : ); SB();
  asm volatile("v_mfma_f32_16x16x32_bf16 %0, v[176:179], a[12:15], %0" : "+v"(g[0][3][0]) :  : ); SB();
  asm volatile("v_add_f32 %0, %0, %1" : "+v"(pr[0][3]) : "v"(pr[2][3]) : ); SB();
  asm volatile("v_mfma_f32_16x16x32_bf16 %0, v[180:183], a[0:3], %0" : "+v"(g[0][0][1]) :  : ); SB();
  asm volatile("buffer_load_dwordx4 a[160:163], %0, %1, %2 offen offset:0 sc1" :  : "v"(vO), "s"(rWS), "s"(0u) : "memory"); SB();
  asm volatile("v_add_f32 %0, %0, %1" : "+v"(pr[1][0]) : "v"(pr[3][0]) : ); SB();
  asm volatile("v_mfma_f32_16x16x32_bf16 %0, v[180:183], a[4:7], %0" : "+v"(g[0][1][1]) :  : ); SB();
  asm volatile("v_add_f32 %0, %0, %1" : "+v"(pr[1][1]) : "v"(pr[3][1]) : ); SB();
  asm volatile("v_mfma_f32_16x16x32_bf16 %0, v[180:183], a[8:11], %0" : "+v"(g[0][2][1]) :  : ); SB();
  asm volatile("v_add_f32 %0, %0, %1" : "+v"(pr[1][2]) : "v"(pr[3][2]) : ); SB();
  asm volatile("v_mfma_f32_16x16x32_bf16 %0, v[180:183], a[12:15], %0" : "+v"(g[0][3][1]) :  : ); SB();
  asm volatile("v_add_f32 %0, %0, %1" : "+v"(pr[1][3]) : "v"(pr[3][3]) : ); SB();
  asm volatile("s_waitcnt vmcnt(20)" :  :  : "memory"); SB();
  asm volatile("v_mfma_f32_16x16x32_bf16 %0, v[184:187], a[16:19], %0" : "+v"(g[0][0][0]) :  : ); SB();
  asm volatile("buffer_load_dwordx4 a[164:167], %0, %1, %2 offen offset:1024 sc1" :  : "v"(vO), "s"(rWS), "s"(0u) : "memory"); SB();
  asm volatile("v_mul_f32 %0, %0, %1" : "+v"(pr[0][0]) : "v"(a1[0][2][0]) : ); SB();
  asm volatile("v_mfma_f32_16x16x32_bf16 %0, v[184:187], a[20:23], %0" : "+v"(g[0][1][0]) :  : ); SB();
  asm volatile("v_mul_f32 %0, %0, %1" : "+v"(pr[0][1]) : "v"(a1[0][2][1]) : ); SB();
  asm volatile("v_mfma_f32_16x16x32_bf16 %0, v[184:187], a[24:27], %0" : "+v"(g[0][2][0]) :  : ); SB();
  asm volatile("v_mul_f32 %0, %0, %1" : "+v"(pr[0][2]) : "v"(a1[0][2][2]) : ); SB();
  asm volatile("v_mfma_f32_16x16x32_bf16 %0, v[184:187], a[28:31], %0" : "+v"(g[0][3][0]) :  : ); SB();
  asm volatile("buffer_load_dwordx4 a[168:171], %0, %1, %2 offen offset:2048 sc1" :  : "v"(vO), "s"(rWS), "s"(0u) : "memory"); SB();
  asm volatile("v_mul_f32 %0, %0, %1" : "+v"(pr[0][3]) : "v"(a1[0][2][3]) : ); SB();
  asm volatile("v_mfma_f32_16x16x32_bf16 %0, v[188:191], a[16:19], %0" : "+v"(g[0][0][1]) :  : ); SB();
  asm volatile("v_mul_f32 %0, %0, %1" : "+v"(pr[1][0]) : "v"(a1[1][0][0]) : ); SB();
  asm volatile("v_mfma_f32_16x16x32_bf16 %0, v[188:191], a[20:23], %0" : "+v"(g[0][1][1]) :  : ); SB();
  asm volatile("v_mul_f32 %0, %0, %1" : "+v"(pr[1][1]) : "v"(a1[1][0][1]) : ); SB();
  asm volatile("v_mfma_f32_16x16x32_bf16 %0, v[188:191], a[24:27], %0" : "+v"(g[0][2][1]) :  : ); SB();
  asm volatile("v_mul_f32 %0, %0, %1" : "+v"(pr[1][2]) : "v"(a1[1][0][2]) : ); SB();
  asm volatile("v_mfma_f32_16x16x32_bf16 %0, v[188:191], a[28:31], %0" : "+v"(g[0][3][1]) :  : ); SB();
  asm volatile("buffer_load_dwordx4 a[172:175], %0, %1, %2 offen offset:3072 sc1" :  : "v"(vO), "s"(rWS), "s"(0u) : "memory"); SB();
  asm volatile("s_waitcnt vmcnt(21)" :  :  : "memory"); SB();
  asm volatile("v_mfma_f32_16x16x32_bf16 %0, v[192:195], a[32:35], %0" : "+v"(g[0][0][0]) :  : ); SB();
  asm volatile("v_mul_f32 %0, %0, %1" : "+v"(pr[1][3]) : "v"(a1[1][0][3]) : ); SB();
  asm volatile("v_mfma_f32_16x16x32_bf16 %0, v[192:195], a[36:39], %0" : "+v"(g[0][1][0]) :  : ); SB();
  asm volatile("v_cvt_pk_bf16_f32 %0, %1, %2" : "=v"(ho[1][0]) : "v"(pr[0][0]), "v"(pr[0][1]) : ); SB();
  asm volatile("v_mfma_f32_16x16x32_bf16 %0, v[192:195], a[40:43], %0" : "+v"(g[0][2][0]) :  : ); SB();
  asm volatile("buffer_load_dwordx4 a[176:179], %0, %1, %2 offen offset:0 sc1" :  : "v"(vO), "s"(rWS), "s"(4096u) : "memory"); SB();
  asm volatile("v_cvt_pk_bf16_f32 %0, %1, %2" : "=v"(ho[1][1]) : "v"(pr[0][2]), "v"(pr[0][3]) : ); SB();
  asm volatile("v_mfma_f32_16x16x32_bf16 %0, v[192:195], a[44:47], %0" : "+v"(g[0][3][0]) :  : ); SB();
  asm volatile("v_cvt_pk_bf16_f32 %0, %1, %2" : "=v"(ho[1][2]) : "v"(pr[1][0]), "v"(pr[1][1]) : ); SB();
  asm volatile("v_mfma_f32_16x16x32_bf16 %0, v[196:199], a[32:35], %0" : "+v"(g[0][0][1]) :  : ); SB();
  asm volatile("v_cvt_pk_bf16_f32 %0, %1, %2" : "=v"(ho[1][3]) : "v"(pr[1][2]), "v"(pr[1][3]) : ); SB();
  asm volatile("v_mfma_f32_16x16x32_bf16 %0, v[196:199], a[36:39], %0" : "+v"(g[0][1][1]) :  : ); SB();
  asm volatile("v_mfma_f32_16x16x32_bf16 %0, v[196:199], a[40:43], %0" : "+v"(g[0][2][1]) :  : ); SB();
  asm volatile("buffer_store_dwordx4 %1, %0, %2, %3 offen offset:1024 sc1\n\ts_nop 1" :  : "v"(vH_), "v"(ho[1]), "s"(rWS), "s"(0u) : "memory"); SB();
  asm volatile("v_mfma_f32_16x16x32_bf16 %0, v[196:199], a[44:47], %0" : "+v"(g[0][3][1]) :  : ); SB();
  asm volatile("s_waitcnt vmcnt(20)" :  :  : "memory"); SB();
  asm volatile("v_mfma_f32_16x16x32_bf16 %0, v[200:203], a[48:51], %0" : "+v"(g[0][0][0]) :  : ); SB();
  asm volatile("v_mfma_f32_16x16x32_bf16 %0, v[200:203], a[52:55], %0" : "+v"(g[0][1][0]) :  : ); SB();
  asm volatile("v_mfma_f32_16x16x32_bf16 %0, v[200:203], a[56:59], %0" : "+v"(g[0][2][0]) :  : ); SB();
  asm volatile("buffer_load_dwordx4 a[180:183], %0, %1, %2 offen offset:1024 sc1" :  : "v"(vO), "s"(rWS), "s"(4096u) : "memory"); SB();
  asm volatile("v_mfma_f32_16x16x32_bf16 %0, v[200:203], a[60:63], %0" : "+v"(g[0][3][0]) :  : ); SB();
  asm volatile("v_mfma_f32_16x16x32_bf16 %0, v[204:207], a[48:51], %0" : "+v"(g[0][0][1]) :  : ); SB();
  asm volatile("v_mfma_f32_16x16x32_bf16 %0, v[204:207], a[52:55], %0" : "+v"(g[0][1][1]) :  : ); SB();
  asm volatile("buffer_load_dwordx4 a[184:187], %0, %1, %2 offen offset:2048 sc1" :  : "v"(vO), "s"(rWS), "s"(4096u) : "memory"); SB();
  asm volatile("v_mfma_f32_16x16x32_bf16 %0, v[204:207], a[56:59], %0" : "+v"(g[0][2][1]) :  : ); SB();
  asm volatile("v_mfma_f32_16x16x32_bf16 %0, v[204:207], a[60:63], %0" : "+v"(g[0][3][1]) :  : ); SB();
  asm volatile("s_waitcnt vmcnt(20)" :  :  : "memory"); SB();
  asm volatile("v_mfma_f32_16x16x32_bf16 %0, v[208:211], a[64:67], %0" : "+v"(g[0][0][0]) :  : ); SB();
  asm volatile("ds_read_b128 %0, %1 offset:0" : "=&v"(yq[0]) : "v"(ydr) : "memory"); SB();
  asm volatile("v_mfma_f32_16x16x32_bf16 %0, v[208:211], a[68:71], %0" : "+v"(g[0][1][0]) :  : ); SB();
  asm volatile("ds_read_b128 %0, %1 offset:256" : "=&v"(yq[1]) : "v"(ydr) : "memory"); SB();
  asm volatile("v_mfma_f32_16x16x32_bf16 %0, v[208:211], a[72:75], %0" : "+v"(g[0][2][0]) :  : ); SB();
  asm volatile("ds_read_b128 %0, %1 offset:8192" : "=&v"(yq[2]) : "v"(ydr) : "memory"); SB();
  asm volatile("v_mfma_f32_16x16x32_bf16 %0, v[208:211], a[76:79], %0" : "+v"(g[0][3][0]) :  : ); SB();
  asm volatile("ds_read_b128 %0, %1 offset:8448" : "=&v"(yq[3]) : "v"(ydr) : "memory"); SB();
  asm volatile("v_mfma_f32_16x16x32_bf16 %0, v[212:215], a[64:67], %0" : "+v"(g[0][0][1]) :  : ); SB();
  asm volatile("ds_read_b128 %0, %1 offset:16384" : "=&v"(yq[4]) : "v"(ydr) : "memory"); SB();
  asm volatile("v_mfma_f32_16x16x32_bf16 %0, v[212:215], a[68:71], %0" : "+v"(g[0][1][1]) :  : ); SB();
  asm volatile("ds_read_b128 %0, %1 offset:16640" : "=&v"(yq[5]) : "v"(ydr) : "memory"); SB();
  asm volatile("v_mfma_f32_16x16x32_bf16 %0, v[212:215], a[72:75], %0" : "+v"(g[0][2][1]) :  : ); SB();
  asm volatile("ds_read_b128 %0, %1 offset:24576" : "=&v"(yq[6]) : "v"(ydr) : "memory"); SB();
  asm volatile("v_mfma_f32_16x16x32_bf16 %0, v[212:215], a[76:79], %0" : "+v"(g[0][3][1]) :  : ); SB();
  asm volatile("ds_read_b128 %0, %1 offset:24832" : "=&v"(yq[7]) : "v"(ydr) : "memory"); SB();
  asm volatile("s_waitcnt vmcnt(18)" :  :  : "memory"); SB();
  asm volatile("v_mfma_f32_16x16x32_bf16 %0, v[216:219], a[80:83], %0" : "+v"(g[0][0][0]) :  : ); SB();
  asm volatile("ds_read_b128 %0, %1 offset:25856" : "=&v"(ykv) : "v"(ykr) : "memory"); SB();
  asm volatile("v_mfma_f32_16x16x32_bf16 %0, v[216:219], a[84:87], %0" : "+v"(g[0][1][0]) :  : ); SB();
  asm volatile("buffer_load_dwordx4 a[188:191], %0, %1, %2 offen offset:3072 sc1" :  : "v"(vO), "s"(rWS), "s"(4096u) : "memory"); SB();
  asm volatile("v_mfma_f32_16x16x32_bf16 %0, v[216:219], a[88:91], %0" : "+v"(g[0][2][0]) :  : ); SB();
  asm volatile("buffer_load_dwordx4 a[192:195], %0, %1, %2 offen offset:0 sc1" :  : "v"(vO), "s"(rWS), "s"(8192u) : "memory"); SB();
  asm volatile("v_mfma_f32_16x16x32_bf16 %0, v[216:219], a[92:95], %0" : "+v"(g[0][3][0]) :  : ); SB();
  asm volatile("v_mfma_f32_16x16x32_bf16 %0, v[220:223], a[80:83], %0" : "+v"(g[0][0][1]) :  : ); SB();
  asm volatile("v_mfma_f32_16x16x32_bf16 %0, v[220:223], a[84:87], %0" : "+v"(g[0][1][1]) :  : ); SB();
  asm volatile("buffer_load_dwordx4 a[196:199], %0, %1, %2 offen offset:1024 sc1" :  : "v"(vO), "s"(rWS), "s"(8192u) : "memory"); SB();
  asm volatile("v_mfma_f32_16x16x32_bf16 %0, v[220:223], a[88:91], %0" : "+v"(g[0][2][1]) :  : ); SB();
  asm volatile("v_mfma_f32_16x16x32_bf16 %0, v[220:223], a[92:95], %0" : "+v"(g[0][3][1]) :  : ); SB();
  asm volatile("s_waitcnt vmcnt(13)\n\ts_barrier" :  :  : "memory"); SB();
  asm volatile("s_waitcnt vmcnt(11)" :  :  : "memory"); SB();
  asm volatile("v_mfma_f32_16x16x32_bf16 %0, v[224:227], a[100:103], %0" : "+v"(g[0][1][0]) :  : ); SB();
  asm volatile("ds_read2_b32 a[64:65], %0 offset0:176 offset1:177" :  : "v"(aa[0]) : "memory"); SB();
  asm volatile("v_mfma_f32_16x16x32_bf16 %0, v[228:231], a[100:103], %0" : "+v"(g[0][1][1]) :  : ); SB();
  asm volatile("ds_read2_b32 a[66:67], %0 offset0:178 offset1:179" :  : "v"(aa[0]) : "memory"); SB();
  asm volatile("v_mfma_f32_16x16x32_bf16 %0, v[232:235], a[116:119], %0" : "+v"(g[0][1][0]) :  : ); SB();
  asm volatile("ds_read_b128 a[60:63], %0 offset:3776" :  : "v"(bb) : "memory"); SB();
  asm volatile("v_mfma_f32_16x16x32_bf16 %0, v[236:239], a[116:119], %0" : "+v"(g[0][1][1]) :  : ); SB();
  asm volatile("ds_read_b128 a[56:59], %0 offset:3264" :  : "v"(bb) : "memory"); SB();
  asm volatile("v_mfma_f32_16x16x32_bf16 %0, v[240:243], a[132:135], %0" : "+v"(g[0][1][0]) :  : ); SB();
  asm volatile("buffer_load_dwordx4 a[200:203], %0, %1, %2 offen offset:3072 sc1" :  : "v"(vO), "s"(rWS), "s"(12288u) : "memory"); SB();
  asm volatile("v_mfma_f32_16x16x32_bf16 %0, v[244:247], a[132:135], %0" : "+v"(g[0][1][1]) :  : ); SB();
  asm volatile("ds_read_b128 a[52:55], %0 offset:2752" :  : "v"(bb) : "memory"); SB();
  asm volatile("v_mfma_f32_16x16x32_bf16 %0, v[248:251], a[148:151], %0" : "+v"(g[0][1][0]) :  : ); SB();
  asm volatile("ds_read_b128 a[48:51], %0 offset:2240" :  : "v"(bb) : "memory"); SB();
  asm volatile("v_mfma_f32_16x16x32_bf16 %0, v[252:255], a[148:151], %0" : "+v"(g[0][1][1]) :  : ); SB();
  asm volatile("buffer_load_dwordx4 a[204:207], %0, %1, %2 offen offset:0 sc1" :  : "v"(vO), "s"(rWS), "s"(16384u) : "memory"); SB();
  asm volatile("v_mfma_f32_16x16x32_bf16 %0, v[224:227], a[104:107], %0" : "+v"(g[0][2][0]) :  : ); SB();
  asm volatile("ds_read2_b32 a[68:69], %0 offset0:48 offset1:49" :  : "v"(aa[0]) : "memory"); SB();
  asm volatile("v_mfma_f32_16x16x32_bf16 %0, v[228:231], a[104:107], %0" : "+v"(g[0][2][1]) :  : ); SB();
  asm volatile("ds_read2_b32 a[70:71], %0 offset0:50 offset1:51" :  : "v"(aa[0]) : "memory"); SB();
  asm volatile("v_mfma_f32_16x16x32_bf16 %0, v[232:235], a[120:123], %0" : "+v"(g[0][2][0]) :  : ); SB();
  asm volatile("ds_write_b128 %0, %1 offset:0" :  : "v"(pw2), "v"(g[0][1][0]) : "memory"); SB();
  asm volatile("v_mfma_f32_16x16x32_bf16 %0, v[236:239], a[120:123], %0" : "+v"(g[0][2][1]) :  : ); SB();
  asm volatile("buffer_load_dwordx4 a[208:211], %0, %1, %2 offen offset:1024 sc1" :  : "v"(vO), "s"(rWS), "s"(16384u) : "memory"); SB();
  asm volatile("v_mfma_f32_16x16x32_bf16 %0, v[240:243], a[136:139], %0" : "+v"(g[0][2][0]) :  : ); SB();
  asm volatile("ds_read_b128 a[44:47], %0 offset:1728" :  : "v"(bb) : "memory"); SB();
  asm volatile("v_mfma_f32_16x16x32_bf16 %0, v[244:247], a[136:139], %0" : "+v"(g[0][2][1]) :  : ); SB();
  asm volatile("buffer_load_dwordx4 a[212:215], %0, %1, %2 offen offset:2048 sc1" :  : "v"(vO), "s"(rWS), "s"(16384u) : "memory"); SB();
  asm volatile("v_mfma_f32_16x16x32_bf16 %0, v[248:251], a[152:155], %0" : "+v"(g[0][2][0]) :  : ); SB();
  asm volatile("ds_write_b128 %0, %1 offset:1024" :  : "v"(pw2), "v"(g[0][1][1]) : "memory"); SB();
  asm volatile("v_mfma_f32_16x16x32_bf16 %0, v[252:255], a[152:155], %0" : "+v"(g[0][2][1]) :  : ); SB();
  asm volatile("ds_read2_b32 a[72:73], %0 offset0:176 offset1:177" :  : "v"(aa[1]) : "memory"); SB();
  asm volatile("v_mfma_f32_16x16x32_bf16 %0, v[224:227], a[108:111], %0" : "+v"(g[0][3][0]) :  : ); SB();
  asm volatile("buffer_load_dwordx4 a[216:219], %0, %1, %2 offen offset:3072 sc1" :  : "v"(vO), "s"(rWS), "s"(16384u) : "memory"); SB();
  asm volatile("v_mfma_f32_16x16x32_bf16 %0, v[228:231], a[108:111], %0" : "+v"(g[0][3][1]) :  : ); SB();
  asm volatile("ds_read2_b32 a[74:75], %0 offset0:178 offset1:179" :  : "v"(aa[1]) : "memory"); SB();
  asm volatile("v_mfma_f32_16x16x32_bf16 %0, v[232:235], a[124:127], %0" : "+v"(g[0][3][0]) :  : ); SB();
  asm volatile("ds_write_b128 %0, %1 offset:2048" :  : "v"(pw2), "v"(g[0][2][0]) : "memory"); SB();
  asm volatile("v_mfma_f32_16x16x32_bf16 %0, v[236:239], a[124:127], %0" : "+v"(g[0][3][1]) :  : ); SB();
  asm volatile("ds_read2_b32 a[76:77], %0 offset0:48 offset1:49" :  : "v"(aa[1]) : "memory"); SB();
  asm volatile("v_mfma_f32_16x16x32_bf16 %0, v[240:243], a[140:143], %0" : "+v"(g[0][3][0]) :  : ); SB();
  asm volatile("ds_read2_b32 a[78:79], %0 offset0:50 offset1:51" :  : "v"(aa[1]) : "memory"); SB();
  asm volatile("v_mfma_f32_16x16x32_bf16 %0, v[244:247], a[140:143], %0" : "+v"(g[0][3][1]) :  : ); SB();
  asm volatile("ds_write_b128 %0, %1 offset:3072" :  : "v"(pw2), "v"(g[0][2][1]) : "memory"); SB();
  asm volatile("v_mfma_f32_16x16x32_bf16 %0, v[248:251], a[156:159], %0" : "+v"(g[0][3][0]) :  : ); SB();
  asm volatile("ds_read_b128 a[40:43], %0 offset:1216" :  : "v"(bb) : "memory"); SB();
  asm volatile("v_mfma_f32_16x16x32_bf16 %0, v[252:255], a[156:159], %0" : "+v"(g[0][3][1]) :  : ); SB();
  asm volatile("ds_read2_b32 a[80:81], %0 offset0:176 offset1:177" :  : "v"(aa[2]) : "memory"); SB();
  asm volatile("v_mfma_f32_16x16x32_bf16 %0, v[224:227], a[96:99], %0" : "+v"(g[0][0][0]) :  : ); SB();
  asm volatile("ds_read2_b32 a[82:83], %0 offset0:178 offset1:179" :  : "v"(aa[2]) : "memory"); SB();
  asm volatile("v_mfma_f32_16x16x32_bf16 %0, v[228:231], a[96:99], %0" : "+v"(g[0][0][1]) :  : ); SB();
  asm volatile("ds_read2_b32 a[84:85], %0 offset0:48 offset1:49" :  : "v"(aa[2]) : "memory"); SB();
  asm volatile("v_mfma_f32_16x16x32_bf16 %0, v[232:235], a[112:115], %0" : "+v"(g[0][0][0]) :  : ); SB();
  asm volatile("ds_write_b128 %0, %1 offset:4096" :  : "v"(pw2), "v"(g[0][3][0]) : "memory"); SB();
  asm volatile("v_mfma_f32_16x16x32_bf16 %0, v[236:239], a[112:115], %0" : "+v"(g[0][0][1]) :  : ); SB();
  asm volatile("ds_read2_b32 a[86:87], %0 offset0:50 offset1:51" :  : "v"(aa[2]) : "memory"); SB();
  asm volatile("v_mfma_f32_16x16x32_bf16 %0, v[240:243], a[128:131], %0" : "+v"(g[0][0][0]) :  : ); SB();
  asm volatile("ds_read_b128 a[36:39], %0 offset:704" :  : "v"(bb) : "memory"); SB();
  asm volatile("v_mfma_f32_16x16x32_bf16 %0, v[244:247], a[128:131], %0" : "+v"(g[0][0][1]) :  : ); SB();
  asm volatile("ds_write_b128 %0, %1 offset:5120" :  : "v"(pw2), "v"(g[0][3][1]) : "memory"); SB();
  asm volatile("v_mfma_f32_16x16x32_bf16 %0, v[248:251], a[144:147], %0" : "+v"(g[0][0][0]) :  : ); SB();
  asm volatile("ds_read2_b32 a[88:89], %0 offset0:176 offset1:177" :  : "v"(aa[3]) : "memory"); SB();
  asm volatile("v_mfma_f32_16x16x32_bf16 %0, v[252:255], a[144:147], %0" : "+v"(g[0][0][1]) :  : ); SB();
  asm volatile("s_waitcnt vmcnt(10)\n\ts_mov_b64 exec, 1\n\tbuffer_store_dwordx2 %0, off, %1, %2 sc1\n\ts_mov_b64 exec, -1" :  : "v"(((u32x2){tg0, tg1})), "s"(rWS), "s"(sFH) : "memory"); SB();
  asm volatile("buffer_load_dwordx4 a[220:223], %0, %1, %2 offen offset:0 sc1" :  : "v"(vO), "s"(rWS), "s"(20480u) : "memory"); SB();
  if ((rb | cb | w) == 0) asm volatile("buffer_store_dwordx2 %0, off, %1, 0 sc1" :: "v"(((u32x2){tg0, tg1})), "s"(rWS) : "memory");
  SB();
  ST(17);
  unsigned pw3, p3[3], yw, ys[2], vYr, ya, po;
  asm volatile("s_waitcnt lgkmcnt(5)\n\tv_mfma_f32_16x16x32_bf16 %0, a[64:67], a[60:63], 0" : "=&v"(ac[7]) :  : ); SB();
  asm volatile("ds_read2_b32 a[90:91], %0 offset0:178 offset1:179" :  : "v"(aa[3]) : "memory"); SB();
  asm volatile("" : "+v"(yq[0]), "+v"(yq[1]), "+v"(yq[2]), "+v"(yq[3]), "+v"(yq[4]), "+v"(yq[5]), "+v"(yq[6]), "+v"(yq[7]), "+v"(ykv) :  : "memory"); SB();
  asm volatile("v_mfma_f32_16x16x32_bf16 %0, a[64:67], a[56:59], 0" : "=&v"(ac[6]) :  : ); SB();
  asm volatile("ds_read2_b32 a[92:93], %0 offset0:48 offset1:49" :  : "v"(aa[3]) : "memory"); SB();
  asm volatile("v_add_f32 %0, %0, %1" : "+v"(yq[0][0]) : "v"(yq[2][0]) : ); SB();
  asm volatile("v_mfma_f32_16x16x32_bf16 %0, a[64:67], a[52:55], 0" : "=&v"(ac[5]) :  : ); SB();
  asm volatile("ds_read2_b32 a[94:95], %0 offset0:50 offset1:51" :  : "v"(aa[3]) : "memory"); SB();
  asm volatile("v_add_f32 %0, %0, %1" : "+v"(yq[0][1]) : "v"(yq[2][1]) : ); SB();
  asm volatile("v_mfma_f32_16x16x32_bf16 %0, a[64:67], a[48:51], 0" : "=&v"(ac[4]) :  : ); SB();
  asm volatile("ds_read_b128 a[32:35], %0 offset:192" :  : "v"(bb) : "memory"); SB();
  asm volatile("v_add_f32 %0, %0, %1" : "+v"(yq[0][2]) : "v"(yq[2][2]) : ); SB();
  asm volatile("v_mfma_f32_16x16x32_bf16 %0, a[68:71], a[56:59], %0" : "+v"(ac[7]) :  : ); SB();
  asm volatile("ds_read2_b32 a[128:129], %0 offset0:160 offset1:161" :  : "v"(aa[0]) : "memory"); SB();
  asm volatile("s_waitcnt lgkmcnt(6)\n\ts_barrier" :  :  : "memory"); SB();
  asm volatile("v_mfma_f32_16x16x32_bf16 %0, a[68:71], a[52:55], %0" : "+v"(ac[6]) :  : ); SB();
  asm volatile("ds_read2_b32 a[130:131], %0 offset0:162 offset1:163" :  : "v"(aa[0]) : "memory"); SB();
  asm volatile("buffer_load_dwordx4 a[224:227], %0, %1, %2 offen offset:1024 sc1" :  : "v"(vO), "s"(rWS), "s"(20480u) : "memory"); SB();
  asm volatile("v_mfma_f32_16x16x32_bf16 %0, a[68:71], a[48:51], %0" : "+v"(ac[5]) :  : ); SB();
  asm volatile("ds_read_b128 a[124:127], %0 offset:3712" :  : "v"(bb) : "memory"); SB();
  asm volatile("v_add_f32 %0, %0, %1" : "+v"(yq[0][3]) : "v"(yq[2][3]) : ); SB();
  asm volatile("v_mfma_f32_16x16x32_bf16 %0, a[68:71], a[44:47], %0" : "+v"(ac[4]) :  : ); SB();
  asm volatile("ds_read_b128 %0, %1 offset:0" : "=&v"(pr[0]) : "v"(pe[0]) : "memory"); SB();
  asm volatile("v_add_f32 %0, %0, %1" : "+v"(yq[1][0]) : "v"(yq[3][0]) : ); SB();
  asm volatile("v_mfma_f32_16x16x32_bf16 %0, a[72:75], a[52:55], %0" : "+v"(ac[7]) :  : ); SB();
  asm volatile("ds_read_b128 a[120:123], %0 offset:3200" :  : "v"(bb) : "memory"); SB();
  asm volatile("buffer_load_dwordx4 a[228:231], %0, %1, %2 offen offset:2048 sc1" :  : "v"(vO), "s"(rWS), "s"(20480u) : "memory"); SB();
  asm volatile("v_mfma_f32_16x16x32_bf16 %0, a[72:75], a[48:51], %0" : "+v"(ac[6]) :  : ); SB();
  asm volatile("ds_read_b128 a[116:119], %0 offset:2688" :  : "v"(bb) : "memory"); SB();
  asm volatile("v_add_f32 %0, %0, %1" : "+v"(yq[1][1]) : "v"(yq[3][1]) : ); SB();
  asm volatile("v_mfma_f32_16x16x32_bf16 %0, a[72:75], a[44:47], %0" : "+v"(ac[5]) :  : ); SB();
  asm volatile("ds_read_b128 a[112:115], %0 offset:2176" :  : "v"(bb) : "memory"); SB();
  asm volatile("v_add_f32 %0, %0, %1" : "+v"(yq[1][2]) : "v"(yq[3][2]) : ); SB();
  asm volatile("v_mfma_f32_16x16x32_bf16 %0, a[64:67], a[44:47], 0" : "=&v"(ac[3]) :  : ); SB();
  asm volatile("ds_read_b128 %0, %1 offset:1024" : "=&v"(pr[1]) : "v"(pe[0]) : "memory"); SB();
  asm volatile("v_add_f32 %0, %0, %1" : "+v"(yq[1][3]) : "v"(yq[3][3]) : ); SB();
  asm volatile("v_mfma_f32_16x16x32_bf16 %0, a[76:79], a[48:51], %0" : "+v"(ac[7]) :  : ); SB();
  asm volatile("ds_read2_b32 a[132:133], %0 offset0:32 offset1:33" :  : "v"(aa[0]) : "memory"); SB();
  asm volatile("buffer_load_dwordx4 a[232:235], %0, %1, %2 offen offset:3072 sc1" :  : "v"(vO), "s"(rWS), "s"(20480u) : "memory"); SB();
  asm volatile("v_mfma_f32_16x16x32_bf16 %0, a[76:79], a[44:47], %0" : "+v"(ac[6]) :  : ); SB();
  asm volatile("ds_read2_b32 a[134:135], %0 offset0:34 offset1:35" :  : "v"(aa[0]) : "memory"); SB();
  asm volatile("v_add_f32 %0, %0, %1" : "+v"(yq[4][0]) : "v"(yq[6][0]) : ); SB();
  asm volatile("v_mfma_f32_16x16x32_bf16 %0, a[76:79], a[40:43], %0" : "+v"(ac[5]) :  : ); SB();
  asm volatile("ds_read_b128 a[108:111], %0 offset:1664" :  : "v"(bb) : "memory"); SB();
  asm volatile("v_add_f32 %0, %0, %1" : "+v"(yq[4][1]) : "v"(yq[6][1]) : ); SB();
  asm volatile("v_mfma_f32_16x16x32_bf16 %0, a[72:75], a[40:43], %0" : "+v"(ac[4]) :  : ); SB();
  asm volatile("ds_read_b128 %0, %1 offset:2048" : "=&v"(pr[2]) : "v"(pe[1]) : "memory"); SB();
  asm volatile("buffer_load_dwordx4 a[236:239], %0, %1, %2 offen offset:0 sc1" :  : "v"(vO), "s"(rWS), "s"(24576u) : "memory"); SB();
  asm volatile("v_mfma_f32_16x16x32_bf16 %0, a[80:83], a[44:47], %0" : "+v"(ac[7]) :  : ); SB();
  asm volatile("ds_read2_b32 a[136:137], %0 offset0:160 offset1:161" :  : "v"(aa[1]) : "memory"); SB();
  asm volatile("v_add_f32 %0, %0, %1" : "+v"(yq[4][2]) : "v"(yq[6][2]) : ); SB();
  asm volatile("v_mfma_f32_16x16x32_bf16 %0, a[80:83], a[40:43], %0" : "+v"(ac[6]) :  : ); SB();
  asm volatile("ds_read2_b32 a[138:139], %0 offset0:162 offset1:163" :  : "v"(aa[1]) : "memory"); SB();
  asm volatile("v_add_f32 %0, %0, %1" : "+v"(yq[4][3]) : "v"(yq[6][3]) : ); SB();
  asm volatile("v_mfma_f32_16x16x32_bf16 %0, a[68:71], a[40:43], %0" : "+v"(ac[3]) :  : ); SB();
  asm volatile("ds_read2_b32 a[140:141], %0 offset0:32 offset1:33" :  : "v"(aa[1]) : "memory"); SB();
  asm volatile("buffer_load_dwordx4 a[240:243], %0, %1, %2 offen offset:2048 sc1" :  : "v"(vO), "s"(rWS), "s"(8192u) : "memory"); SB();
  asm volatile("v_mfma_f32_16x16x32_bf16 %0, a[64:67], a[40:43], 0" : "=&v"(ac[2]) :  : ); SB();
  asm volatile("ds_read_b128 %0, %1 offset:3072" : "=&v"(pr[3]) : "v"(pe[1]) : "memory"); SB();
  asm volatile("v_add_f32 %0, %0, %1" : "+v"(yq[5][0]) : "v"(yq[7][0]) : ); SB();
  asm volatile("v_mfma_f32_16x16x32_bf16 %0, a[84:87], a[40:43], %0" : "+v"(ac[7]) :  : ); SB();
  asm volatile("ds_read2_b32 a[142:143], %0 offset0:34 offset1:35" :  : "v"(aa[1]) : "memory"); SB();
  asm volatile("v_add_f32 %0, %0, %1" : "+v"(yq[5][1]) : "v"(yq[7][1]) : ); SB();
  asm volatile("v_mfma_f32_16x16x32_bf16 %0, a[84:87], a[36:39], %0" : "+v"(ac[6]) :  : ); SB();
  asm volatile("ds_read_b128 a[104:107], %0 offset:1152" :  : "v"(bb) : "memory"); SB();
  asm volatile("buffer_load_dwordx4 a[244:247], %0, %1, %2 offen offset:3072 sc1" :  : "v"(vO), "s"(rWS), "s"(8192u) : "memory"); SB();
  asm volatile("v_mfma_f32_16x16x32_bf16 %0, a[80:83], a[36:39], %0" : "+v"(ac[5]) :  : ); SB();
  asm volatile("ds_read2_b32 a[144:145], %0 offset0:160 offset1:161" :  : "v"(aa[2]) : "memory"); SB();
  asm volatile("v_add_f32 %0, %0, %1" : "+v"(yq[5][2]) : "v"(yq[7][2]) : ); SB();
  asm volatile("v_mfma_f32_16x16x32_bf16 %0, a[76:79], a[36:39], %0" : "+v"(ac[4]) :  : ); SB();
  asm volatile("ds_read_b128 %0, %1 offset:4096" : "=&v"(pr[4]) : "v"(pe[2]) : "memory"); SB();
  asm volatile("s_waitcnt lgkmcnt(8)\n\tv_mfma_f32_16x16x32_bf16 %0, a[88:91], a[36:39], %0" : "+v"(ac[7]) :  : ); SB();
  asm volatile("ds_read2_b32 a[146:147], %0 offset0:162 offset1:163" :  : "v"(aa[2]) : "memory"); SB();
  asm volatile("buffer_load_dwordx4 a[248:251], %0, %1, %2 offen offset:0 sc1" :  : "v"(vO), "s"(rWS), "s"(12288u) : "memory"); SB();
  asm volatile("v_mfma_f32_16x16x32_bf16 %0, a[72:75], a[36:39], %0" : "+v"(ac[3]) :  : ); SB();
  asm volatile("ds_read2_b32 a[148:149], %0 offset0:32 offset1:33" :  : "v"(aa[2]) : "memory"); SB();
  asm volatile("v_add_f32 %0, %0, %1" : "+v"(yq[5][3]) : "v"(yq[7][3]) : ); SB();
  asm volatile("v_mfma_f32_16x16x32_bf16 %0, a[68:71], a[36:39], %0" : "+v"(ac[2]) :  : ); SB();
  asm volatile("ds_read2_b32 a[150:151], %0 offset0:34 offset1:35" :  : "v"(aa[2]) : "memory"); SB();
  asm volatile("v_add_f32 %0, %0, %1" : "+v"(yq[0][0]) : "v"(yq[4][0]) : ); SB();
  asm volatile("v_mfma_f32_16x16x32_bf16 %0, a[64:67], a[36:39], 0" : "=&v"(ac[1]) :  : ); SB();
  asm volatile("ds_read_b128 %0, %1 offset:5120" : "=&v"(pr[5]) : "v"(pe[2]) : "memory"); SB();
  asm volatile("v_add_f32 %0, %0, %1" : "+v"(yq[0][1]) : "v"(yq[4][1]) : ); SB();
  asm volatile("v_mfma_f32_16x16x32_bf16 %0, a[92:95], a[32:35], %0" : "+v"(ac[7]) :  : ); SB();
  asm volatile("ds_read_b128 a[100:103], %0 offset:640" :  : "v"(bb) : "memory"); SB();
  asm volatile("buffer_load_dwordx4 a[252:255], %0, %1, %2 offen offset:1024 sc1" :  : "v"(vO), "s"(rWS), "s"(12288u) : "memory"); SB();
  asm volatile("v_mfma_f32_16x16x32_bf16 %0, a[88:91], a[32:35], %0" : "+v"(ac[6]) :  : ); SB();
  asm volatile("ds_read2_b32 a[152:153], %0 offset0:160 offset1:161" :  : "v"(aa[3]) : "memory"); SB();
  asm volatile("v_add_f32 %0, %0, %1" : "+v"(yq[0][2]) : "v"(yq[4][2]) : ); SB();
  asm volatile("v_mfma_f32_16x16x32_bf16 %0, a[84:87], a[32:35], %0" : "+v"(ac[5]) :  : ); SB();
  asm volatile("ds_read2_b32 a[154:155], %0 offset0:162 offset1:163" :  : "v"(aa[3]) : "memory"); SB();
  asm volatile("v_add_f32 %0, %0, %1" : "+v"(yq[0][3]) : "v"(yq[4][3]) : ); SB();
  asm volatile("v_mfma_f32_16x16x32_bf16 %0, a[80:83], a[32:35], %0" : "+v"(ac[4]) :  : ); SB();
  pw3 = lb + 98304 + w * 15360 + l16; asm volatile("" : "+v"(pw3)); SB();
  asm volatile("v_mfma_f32_16x16x32_bf16 %0, a[76:79], a[32:35], %0" : "+v"(ac[3]) :  : ); SB();
  asm volatile("ds_read2_b32 a[156:157], %0 offset0:32 offset1:33" :  : "v"(aa[3]) : "memory"); SB();
  asm volatile("v_add_f32 %0, %0, %1" : "+v"(yq[1][0]) : "v"(yq[5][0]) : ); SB();
  asm volatile("v_mfma_f32_16x16x32_bf16 %0, a[72:75], a[32:35], %0" : "+v"(ac[2]) :  : ); SB();
  asm volatile("ds_read2_b32 a[158:159], %0 offset0:34 offset1:35" :  : "v"(aa[3]) : "memory"); SB();
  asm volatile("s_waitcnt lgkmcnt(5)" : "+v"(pr[0]), "+v"(pr[1]), "+v"(pr[2]), "+v"(pr[3]), "+v"(pr[4]), "+v"(pr[5]) :  : "memory"); SB();
  asm volatile("v_mfma_f32_16x16x32_bf16 %0, a[68:71], a[32:35], %0" : "+v"(ac[1]) :  : ); SB();
  asm volatile("ds_read_b128 a[96:99], %0 offset:128" :  : "v"(bb) : "memory"); SB();
  asm volatile("v_add_f32 %0, %0, %1" : "+v"(yq[1][1]) : "v"(yq[5][1]) : ); SB();
  asm volatile("v_mfma_f32_16x16x32_bf16 %0, a[64:67], a[32:35], 0" : "=&v"(ac[0]) :  : ); SB();
  {
    const f32x4 h0 = ((g[0][0][0] + pr[0]) + (pr[2] + pr[4])) * a1[1][1], h1 = ((g[0][0][1] + pr[1]) + (pr[3] + pr[5])) * a1[1][2];
    ho[2] = (u32x4){pack2(h0[0], h0[1]), pack2(h0[2], h0[3]), pack2(h1[0], h1[1]), pack2(h1[2], h1[3])};
    SB();
  }
  asm volatile("v_mfma_f32_16x16x32_bf16 %0, a[128:131], a[124:127], %0" : "+v"(ac[7]) :  : ); SB();
  asm volatile("ds_read2_b32 a[64:65], %0 offset0:144 offset1:145" :  : "v"(aa[0]) : "memory"); SB();
  asm volatile("v_add_f32 %0, %0, %1" : "+v"(yq[1][2]) : "v"(yq[5][2]) : ); SB();
  asm volatile("v_mfma_f32_16x16x32_bf16 %0, a[128:131], a[120:123], %0" : "+v"(ac[6]) :  : ); SB();
  asm volatile("ds_read2_b32 a[66:67], %0 offset0:146 offset1:147" :  : "v"(aa[0]) : "memory"); SB();
  asm volatile("v_add_f32 %0, %0, %1" : "+v"(yq[1][3]) : "v"(yq[5][3]) : ); SB();
  asm volatile("v_mfma_f32_16x16x32_bf16 %0, a[128:131], a[116:119], %0" : "+v"(ac[5]) :  : ); SB();
  asm volatile("ds_read_b128 a[60:63], %0 offset:3648" :  : "v"(bb) : "memory"); SB();
  asm volatile("buffer_load_dwordx4 a[0:3], %0, %1, %2 offen offset:2048 sc1" :  : "v"(vO), "s"(rWS), "s"(12288u) : "memory"); SB();
  asm volatile("v_mfma_f32_16x16x32_bf16 %0, a[128:131], a[112:115], %0" : "+v"(ac[4]) :  : ); SB();
  asm volatile("buffer_store_dwordx4 %1, %0, %2, %3 offen offset:2048 sc1\n\ts_nop 1" :  : "v"(vH_), "v"(ho[2]), "s"(rWS), "s"(0u) : "memory"); SB();
  asm volatile("v_mfma_f32_16x16x32_bf16 %0, a[132:135], a[120:123], %0" : "+v"(ac[7]) :  : ); SB();
  asm volatile("ds_read_b128 a[56:59], %0 offset:3136" :  : "v"(bb) : "memory"); SB();
  asm volatile("v_lshlrev_b32 %0, 16, %1" : "=v"(yq[2][0]) : "v"(ykv[0]) : ); SB();
  asm volatile("v_mfma_f32_16x16x32_bf16 %0, a[132:135], a[116:119], %0" : "+v"(ac[6]) :  : ); SB();
  asm volatile("ds_read_b128 a[52:55], %0 offset:2624" :  : "v"(bb) : "memory"); SB();
  asm volatile("v_and_b32 %0, 0xffff0000, %1" : "=v"(yq[2][1]) : "v"(ykv[0]) : ); SB();
  asm volatile("v_mfma_f32_16x16x32_bf16 %0, a[132:135], a[112:115], %0" : "+v"(ac[5]) :  : ); SB();
  asm volatile("ds_read_b128 a[48:51], %0 offset:2112" :  : "v"(bb) : "memory"); SB();
  asm volatile("v_lshlrev_b32 %0, 16, %1" : "=v"(yq[2][2]) : "v"(ykv[1]) : ); SB();
  asm volatile("v_mfma_f32_16x16x32_bf16 %0, a[132:135], a[108:111], %0" : "+v"(ac[4]) :  : ); SB();
  p3[0] = lb + 98304 + (w ^ 1) * 15360 + l16; asm volatile("" : "+v"(p3[0])); SB();
  asm volatile("v_mfma_f32_16x16x32_bf16 %0, a[136:139], a[116:119], %0" : "+v"(ac[7]) :  : ); SB();
  asm volatile("ds_read2_b32 a[68:69], %0 offset0:16 offset1:17" :  : "v"(aa[0]) : "memory"); SB();
  asm volatile("v_and_b32 %0, 0xffff0000, %1" : "=v"(yq[2][3]) : "v"(ykv[1]) : ); SB();
  asm volatile("v_mfma_f32_16x16x32_bf16 %0, a[136:139], a[112:115], %0" : "+v"(ac[6]) :  : ); SB();
  asm volatile("ds_read2_b32 a[70:71], %0 offset0:18 offset1:19" :  : "v"(aa[0]) : "memory"); SB();
  asm volatile("v_lshlrev_b32 %0, 16, %1" : "=v"(yq[3][0]) : "v"(ykv[2]) : ); SB();
  asm volatile("v_mfma_f32_16x16x32_bf16 %0, a[136:139], a[108:111], %0" : "+v"(ac[5]) :  : ); SB();
  asm volatile("ds_read_b128 a[44:47], %0 offset:1600" :  : "v"(bb) : "memory"); SB();
  asm volatile("v_and_b32 %0, 0xffff0000, %1" : "=v"(yq[3][1]) : "v"(ykv[2]) : ); SB();
  asm volatile("v_mfma_f32_16x16x32_bf16 %0, a[128:131], a[108:111], %0" : "+v"(ac[3]) :  : ); SB();
  p3[1] = lb + 98304 + (w ^ 2) * 15360 + l16; asm volatile("" : "+v"(p3[1])); SB();
  asm volatile("v_mfma_f32_16x16x32_bf16 %0, a[140:143], a[112:115], %0" : "+v"(ac[7]) :  : ); SB();
  asm volatile("ds_read2_b32 a[72:73], %0 offset0:144 offset1:145" :  : "v"(aa[1]) : "memory"); SB();
  asm volatile("buffer_load_dwordx4 a[4:7], %0, %1, %2 offen offset:1024 sc1" :  : "v"(vO), "s"(rWS), "s"(24576u) : "memory"); SB();
  asm volatile("v_mfma_f32_16x16x32_bf16 %0, a[140:143], a[108:111], %0" : "+v"(ac[6]) :  : ); SB();
  asm volatile("ds_read2_b32 a[74:75], %0 offset0:146 offset1:147" :  : "v"(aa[1]) : "memory"); SB();
  asm volatile("v_lshlrev_b32 %0, 16, %1" : "=v"(yq[3][2]) : "v"(ykv[3]) : ); SB();
  asm volatile("v_mfma_f32_16x16x32_bf16 %0, a[140:143], a[104:107], %0" : "+v"(ac[5]) :  : ); SB();
  asm volatile("ds_read2_b32 a[76:77], %0 offset0:16 offset1:17" :  : "v"(aa[1]) : "memory"); SB();
  asm volatile("v_and_b32 %0, 0xffff0000, %1" : "=v"(yq[3][3]) : "v"(ykv[3]) : ); SB();
  asm volatile("v_mfma_f32_16x16x32_bf16 %0, a[136:139], a[104:107], %0" : "+v"(ac[4]) :  : ); SB();
  p3[2] = lb + 98304 + (w ^ 3) * 15360 + l16; asm volatile("" : "+v"(p3[2])); SB();
  asm volatile("v_mfma_f32_16x16x32_bf16 %0, a[144:147], a[108:111], %0" : "+v"(ac[7]) :  : ); SB();
  asm volatile("ds_read2_b32 a[78:79], %0 offset0:18 offset1:19" :  : "v"(aa[1]) : "memory"); SB();
  asm volatile("buffer_load_dwordx4 a[8:11], %0, %1, %2 offen offset:2048 sc1" :  : "v"(vO), "s"(rWS), "s"(24576u) : "memory"); SB();
  asm volatile("v_mfma_f32_16x16x32_bf16 %0, a[144:147], a[104:107], %0" : "+v"(ac[6]) :  : ); SB();
  asm volatile("ds_read_b128 a[40:43], %0 offset:1088" :  : "v"(bb) : "memory"); SB();
  asm volatile("v_fmac_f32 %0, %1, %2" : "+v"(yq[0][0]) : "v"(yq[2][0]), "v"(dD[2]) : ); SB();
  asm volatile("v_mfma_f32_16x16x32_bf16 %0, a[132:135], a[104:107], %0" : "+v"(ac[3]) :  : ); SB();
  asm volatile("ds_read2_b32 a[80:81], %0 offset0:144 offset1:145" :  : "v"(aa[2]) : "memory"); SB();
  asm volatile("v_fmac_f32 %0, %1, %2" : "+v"(yq[0][1]) : "v"(yq[2][1]), "v"(dD[2]) : ); SB();
  asm volatile("v_mfma_f32_16x16x32_bf16 %0, a[128:131], a[104:107], %0" : "+v"(ac[2]) :  : ); SB();
  yw = (lane ^ ((lane >> 3) & 6)) * 16; asm volatile("" : "+v"(yw)); SB();
  asm volatile("v_mfma_f32_16x16x32_bf16 %0, a[148:151], a[104:107], %0" : "+v"(ac[7]) :  : ); SB();
  asm volatile("ds_read2_b32 a[82:83], %0 offset0:146 offset1:147" :  : "v"(aa[2]) : "memory"); SB();
  asm volatile("buffer_load_dwordx4 a[12:15], %0, %1, %2 offen offset:3072 sc1" :  : "v"(vO), "s"(rWS), "s"(24576u) : "memory"); SB();
  asm volatile("s_waitcnt lgkmcnt(6)\n\tv_mfma_f32_16x16x32_bf16 %0, a[148:151], a[100:103], %0" : "+v"(ac[6]) :  : ); SB();
  asm volatile("ds_read2_b32 a[84:85], %0 offset0:16 offset1:17" :  : "v"(aa[2]) : "memory"); SB();
  asm volatile("v_fmac_f32 %0, %1, %2" : "+v"(yq[0][2]) : "v"(yq[2][2]), "v"(dD[2]) : ); SB();
  asm volatile("v_mfma_f32_16x16x32_bf16 %0, a[144:147], a[100:103], %0" : "+v"(ac[5]) :  : ); SB();
  asm volatile("ds_read2_b32 a[86:87], %0 offset0:18 offset1:19" :  : "v"(aa[2]) : "memory"); SB();
  asm volatile("buffer_load_dwordx4 a[16:19], %0, %1, %2 offen offset:0 sc1" :  : "v"(vO), "s"(rWS), "s"(28672u) : "memory"); SB();
  asm volatile("v_mfma_f32_16x16x32_bf16 %0, a[140:143], a[100:103], %0" : "+v"(ac[4]) :  : ); SB();
  yw = yw + lb + 32768 + w * 3072; asm volatile("" : "+v"(yw)); SB();
  asm volatile("v_mfma_f32_16x16x32_bf16 %0, a[152:155], a[100:103], %0" : "+v"(ac[7]) :  : ); SB();
  asm volatile("ds_read_b128 a[36:39], %0 offset:576" :  : "v"(bb) : "memory"); SB();
  asm volatile("v_fmac_f32 %0, %1, %2" : "+v"(yq[0][3]) : "v"(yq[2][3]), "v"(dD[2]) : ); SB();
  asm volatile("v_mfma_f32_16x16x32_bf16 %0, a[136:139], a[100:103], %0" : "+v"(ac[3]) :  : ); SB();
  asm volatile("ds_read2_b32 a[88:89], %0 offset0:144 offset1:145" :  : "v"(aa[3]) : "memory"); SB();
  asm volatile("buffer_load_dwordx4 a[20:23], %0, %1, %2 offen offset:1024 sc1" :  : "v"(vO), "s"(rWS), "s"(28672u) : "memory"); SB();
  asm volatile("v_mfma_f32_16x16x32_bf16 %0, a[132:135], a[100:103], %0" : "+v"(ac[2]) :  : ); SB();
  asm volatile("ds_read2_b32 a[90:91], %0 offset0:146 offset1:147" :  : "v"(aa[3]) : "memory"); SB();
  asm volatile("v_fmac_f32 %0, %1, %2" : "+v"(yq[1][0]) : "v"(yq[3][0]), "v"(dD[2]) : ); SB();
  asm volatile("v_mfma_f32_16x16x32_bf16 %0, a[128:131], a[100:103], %0" : "+v"(ac[1]) :  : ); SB();
  ya = lb + 32768 + (4 * g4 + q4) * 128 + 8 * p4; asm volatile("" : "+v"(ya)); SB();
  asm volatile("v_mfma_f32_16x16x32_bf16 %0, a[156:159], a[96:99], %0" : "+v"(ac[7]) :  : ); SB();
  asm volatile("ds_read2_b32 a[92:93], %0 offset0:16 offset1:17" :  : "v"(aa[3]) : "memory"); SB();
  asm volatile("v_fmac_f32 %0, %1, %2" : "+v"(yq[1][1]) : "v"(yq[3][1]), "v"(dD[2]) : ); SB();
  asm volatile("v_mfma_f32_16x16x32_bf16 %0, a[152:155], a[96:99], %0" : "+v"(ac[6]) :  : ); SB();
  asm volatile("ds_read2_b32 a[94:95], %0 offset0:18 offset1:19" :  : "v"(aa[3]) : "memory"); SB();
  asm volatile("buffer_load_dwordx2 %0, %1, %2, %3 offen sc1" : "=&v"(fh) : "v"(vFH), "s"(rWS), "s"(0u) : "memory"); SB();
  asm volatile("v_mfma_f32_16x16x32_bf16 %0, a[148:151], a[96:99], %0" : "+v"(ac[5]) :  : ); SB();
  asm volatile("ds_read_b128 a[32:35], %0 offset:64" :  : "v"(bb) : "memory"); SB();
  asm volatile("v_fmac_f32 %0, %1, %2" : "+v"(yq[1][2]) : "v"(yq[3][2]), "v"(dD[2]) : ); SB();
  asm volatile("v_mfma_f32_16x16x32_bf16 %0, a[144:147], a[96:99], %0" : "+v"(ac[4]) :  : ); SB();
  po = (2 * g4 + (q4 >> 1)) & 3; asm volatile("" : "+v"(po)); SB();
  asm volatile("v_mfma_f32_16x16x32_bf16 %0, a[140:143], a[96:99], %0" : "+v"(ac[3]) :  : ); SB();
  ya = ya + ((w ^ po) << 5); asm volatile("" : "+v"(ya)); SB();
  asm volatile("v_mfma_f32_16x16x32_bf16 %0, a[136:139], a[96:99], %0" : "+v"(ac[2]) :  : ); SB();
  po = vX + 24 * (g4 & 1); asm volatile("" : "+v"(po)); SB();
  asm volatile("v_mfma_f32_16x16x32_bf16 %0, a[132:135], a[96:99], %0" : "+v"(ac[1]) :  : ); SB();
  asm volatile("v_fmac_f32 %0, %1, %2" : "+v"(yq[1][3]) : "v"(yq[3][3]), "v"(dD[2]) : ); SB();
  asm volatile("v_mfma_f32_16x16x32_bf16 %0, a[128:131], a[96:99], %0" : "+v"(ac[0]) :  : ); SB();
  asm volatile("v_cvt_pk_bf16_f32 %0, %1, %2" : "=v"(y2[0]) : "v"(yq[0][0]), "v"(yq[0][1]) : ); SB();
  asm volatile("v_mfma_f32_16x16x32_bf16 %0, a[64:67], a[60:63], %0" : "+v"(ac[7]) :  : ); SB();
  asm volatile("ds_read2_b32 a[128:129], %0 offset0:128 offset1:129" :  : "v"(aa[0]) : "memory"); SB();
  asm volatile("global_load_dwordx2 %0, %1, %2 offset:0" : "=&v"(xv[0]) : "v"(vX), "s"(x) : "memory"); SB();
  asm volatile("v_mfma_f32_16x16x32_bf16 %0, a[64:67], a[56:59], %0" : "+v"(ac[6]) :  : ); SB();
  asm volatile("ds_read2_b32 a[130:131], %0 offset0:130 offset1:131" :  : "v"(aa[0]) : "memory"); SB();
  asm volatile("global_load_dwordx2 %0, %1, %2 offset:32" : "=&v"(xv[1]) : "v"(vX), "s"(x) : "memory"); SB();
  asm volatile("v_mfma_f32_16x16x32_bf16 %0, a[64:67], a[52:55], %0" : "+v"(ac[5]) :  : ); SB();
  asm volatile("ds_read_b128 a[124:127], %0 offset:3584" :  : "v"(bb) : "memory"); SB();
  asm volatile("v_cvt_pk_bf16_f32 %0, %1, %2" : "=v"(y2[1]) : "v"(yq[0][2]), "v"(yq[0][3]) : ); SB();
  asm volatile("v_mfma_f32_16x16x32_bf16 %0, a[64:67], a[48:51], %0" : "+v"(ac[4]) :  : ); SB();
  asm volatile("v_cvt_pk_bf16_f32 %0, %1, %2" : "=v"(y2[2]) : "v"(yq[1][0]), "v"(yq[1][1]) : ); SB();
  asm volatile("v_mfma_f32_16x16x32_bf16 %0, a[68:71], a[56:59], %0" : "+v"(ac[7]) :  : ); SB();
  asm volatile("ds_read_b128 a[120:123], %0 offset:3072" :  : "v"(bb) : "memory"); SB();
  asm volatile("v_cvt_pk_bf16_f32 %0, %1, %2" : "=v"(y2[3]) : "v"(yq[1][2]), "v"(yq[1][3]) : ); SB();
  asm volatile("v_mfma_f32_16x16x32_bf16 %0, a[68:71], a[52:55], %0" : "+v"(ac[6]) :  : ); SB();
  asm volatile("ds_read_b128 a[96:99], %0 offset:0" :  : "v"(bb) : "memory"); SB();
  asm volatile("v_min_u32 %0, -2, %0" : "+v"(y2[3]) :  : ); SB();
  asm volatile("v_mfma_f32_16x16x32_bf16 %0, a[68:71], a[48:51], %0" : "+v"(ac[5]) :  : ); SB();
  asm volatile("ds_read_b128 a[100:103], %0 offset:512" :  : "v"(bb) : "memory"); SB();
  asm volatile("v_xnor_b32 %0, %0, %1" : "+v"(y2[3]) : "v"(ykv[3]) : ); SB();
  asm volatile("v_mfma_f32_16x16x32_bf16 %0, a[68:71], a[44:47], %0" : "+v"(ac[4]) :  : ); SB();
  asm volatile("buffer_store_dwordx4 %1, %0, %2, %3 offen offset:0 sc1\n\ts_nop 1" :  : "v"(vF), "v"(y2), "s"(rWS), "s"(uso) : "memory"); SB();
  asm volatile("s_waitcnt lgkmcnt(6)\n\tv_mfma_f32_16x16x32_bf16 %0, a[72:75], a[52:55], %0" : "+v"(ac[7]) :  : ); SB();
  asm volatile("ds_read2_b32 a[132:133], %0 offset0:0 offset1:1" :  : "v"(aa[0]) : "memory"); SB();
  asm volatile("v_mfma_f32_16x16x32_bf16 %0, a[72:75], a[48:51], %0" : "+v"(ac[6]) :  : ); SB();
  asm volatile("ds_read2_b32 a[134:135], %0 offset0:2 offset1:3" :  : "v"(aa[0]) : "memory"); SB();
  asm volatile("v_mfma_f32_16x16x32_bf16 %0, a[72:75], a[44:47], %0" : "+v"(ac[5]) :  : ); SB();
  asm volatile("ds_read_b128 a[116:119], %0 offset:2560" :  : "v"(bb) : "memory"); SB();
  asm volatile("v_mfma_f32_16x16x32_bf16 %0, a[64:67], a[44:47], %0" : "+v"(ac[3]) :  : ); SB();
  asm volatile("global_load_dwordx2 %0, %1, %2 offset:64" : "=&v"(xv[2]) : "v"(vX), "s"(x) : "memory"); SB();
  asm volatile("v_mfma_f32_16x16x32_bf16 %0, a[76:79], a[48:51], %0" : "+v"(ac[7]) :  : ); SB();
  asm volatile("ds_read_b128 a[104:107], %0 offset:1024" :  : "v"(bb) : "memory"); SB();
  asm volatile("v_mfma_f32_16x16x32_bf16 %0, a[76:79], a[44:47], %0" : "+v"(ac[6]) :  : ); SB();
  asm volatile("ds_read2_b32 a[136:137], %0 offset0:128 offset1:129" :  : "v"(aa[1]) : "memory"); SB();
  asm volatile("v_mfma_f32_16x16x32_bf16 %0, a[76:79], a[40:43], %0" : "+v"(ac[5]) :  : ); SB();
  asm volatile("ds_read2_b32 a[138:139], %0 offset0:130 offset1:131" :  : "v"(aa[1]) : "memory"); SB();
  asm volatile("v_mfma_f32_16x16x32_bf16 %0, a[72:75], a[40:43], %0" : "+v"(ac[4]) :  : ); SB();
  asm volatile("global_load_dwordx2 %0, %1, %2 offset:96" : "=&v"(xv[3]) : "v"(vX), "s"(x) : "memory"); SB();
  asm volatile("v_mfma_f32_16x16x32_bf16 %0, a[80:83], a[44:47], %0" : "+v"(ac[7]) :  : ); SB();
  asm volatile("ds_read_b128 a[108:111], %0 offset:1536" :  : "v"(bb) : "memory"); SB();
  asm volatile("v_mfma_f32_16x16x32_bf16 %0, a[80:83], a[40:43], %0" : "+v"(ac[6]) :  : ); SB();
  asm volatile("ds_read_b128 a[112:115], %0 offset:2048" :  : "v"(bb) : "memory"); SB();
  asm volatile("v_mfma_f32_16x16x32_bf16 %0, a[68:71], a[40:43], %0" : "+v"(ac[3]) :  : ); SB();
  asm volatile("ds_read2_b32 a[140:141], %0 offset0:0 offset1:1" :  : "v"(aa[1]) : "memory"); SB();
  asm volatile("v_mfma_f32_16x16x32_bf16 %0, a[64:67], a[40:43], %0" : "+v"(ac[2]) :  : ); SB();
  asm volatile("global_load_dwordx2 %0, %1, %2 offset:128" : "=&v"(xv[4]) : "v"(vX), "s"(x) : "memory"); SB();
  asm volatile("v_mfma_f32_16x16x32_bf16 %0, a[84:87], a[40:43], %0" : "+v"(ac[7]) :  : ); SB();
  asm volatile("ds_read2_b32 a[142:143], %0 offset0:2 offset1:3" :  : "v"(aa[1]) : "memory"); SB();
  asm volatile("v_mfma_f32_16x16x32_bf16 %0, a[84:87], a[36:39], %0" : "+v"(ac[6]) :  : ); SB();
  asm volatile("ds_read2_b32 a[144:145], %0 offset0:128 offset1:129" :  : "v"(aa[2]) : "memory"); SB();
  asm volatile("v_mfma_f32_16x16x32_bf16 %0, a[80:83], a[36:39], %0" : "+v"(ac[5]) :  : ); SB();
  asm volatile("ds_read2_b32 a[146:147], %0 offset0:130 offset1:131" :  : "v"(aa[2]) : "memory"); SB();
  asm volatile("v_mfma_f32_16x16x32_bf16 %0, a[76:79], a[36:39], %0" : "+v"(ac[4]) :  : ); SB();
  asm volatile("v_mfma_f32_16x16x32_bf16 %0, a[88:91], a[36:39], %0" : "+v"(ac[7]) :  : ); SB();
  asm volatile("ds_read2_b32 a[148:149], %0 offset0:0 offset1:1" :  : "v"(aa[2]) : "memory"); SB();
  asm volatile("v_mfma_f32_16x16x32_bf16 %0, a[72:75], a[36:39], %0" : "+v"(ac[3]) :  : ); SB();
  asm volatile("ds_read2_b32 a[150:151], %0 offset0:2 offset1:3" :  : "v"(aa[2]) : "memory"); SB();
  asm volatile("v_mfma_f32_16x16x32_bf16 %0, a[68:71], a[36:39], %0" : "+v"(ac[2]) :  : ); SB();
  asm volatile("ds_read2_b32 a[152:153], %0 offset0:128 offset1:129" :  : "v"(aa[3]) : "memory"); SB();
  asm volatile("v_mfma_f32_16x16x32_bf16 %0, a[64:67], a[36:39], %0" : "+v"(ac[1]) :  : ); SB();
  asm volatile("v_mfma_f32_16x16x32_bf16 %0, a[92:95], a[32:35], %0" : "+v"(ac[7]) :  : ); SB();
  asm volatile("ds_read2_b32 a[154:155], %0 offset0:130 offset1:131" :  : "v"(aa[3]) : "memory"); SB();
  asm volatile("v_mfma_f32_16x16x32_bf16 %0, a[88:91], a[32:35], %0" : "+v"(ac[6]) :  : ); SB();
  asm volatile("ds_read2_b32 a[156:157], %0 offset0:0 offset1:1" :  : "v"(aa[3]) : "memory"); SB();
  asm volatile("v_mfma_f32_16x16x32_bf16 %0, a[84:87], a[32:35], %0" : "+v"(ac[5]) :  : ); SB();
  asm volatile("ds_read2_b32 a[158:159], %0 offset0:2 offset1:3" :  : "v"(aa[3]) : "memory"); SB();
  asm volatile("s_waitcnt vmcnt(12)\n\ts_mov_b64 exec, 1\n\tbuffer_store_dwordx2 %0, off, %1, %2 sc1\n\ts_mov_b64 exec, -1" :  : "v"(((u32x2){tg0, tg1})), "s"(rWS), "s"(sFH + 8192) : "memory"); SB();
  asm volatile("v_mfma_f32_16x16x32_bf16 %0, a[80:83], a[32:35], %0" : "+v"(ac[4]) :  : ); SB();
  asm volatile("v_mfma_f32_16x16x32_bf16 %0, a[76:79], a[32:35], %0" : "+v"(ac[3]) :  : ); SB();
  asm volatile("v_mfma_f32_16x16x32_bf16 %0, a[72:75], a[32:35], %0" : "+v"(ac[2]) :  : ); SB();
  asm volatile("v_mfma_f32_16x16x32_bf16 %0, a[68:71], a[32:35], %0" : "+v"(ac[1]) :  : ); SB();
  asm volatile("v_mfma_f32_16x16x32_bf16 %0, a[64:67], a[32:35], %0" : "+v"(ac[0]) :  : ); SB();
  asm volatile("s_waitcnt lgkmcnt(3)\n\tv_mfma_f32_16x16x32_bf16 %0, a[128:131], a[124:127], %0" : "+v"(ac[7]) :  : ); SB();
  asm volatile("v_mfma_f32_16x16x32_bf16 %0, a[128:131], a[120:123], %0" : "+v"(ac[6]) :  : ); SB();
  asm volatile("v_mfma_f32_16x16x32_bf16 %0, a[128:131], a[96:99], %0" : "+v"(ac[0]) :  : ); SB();
  asm volatile("v_mfma_f32_16x16x32_bf16 %0, a[128:131], a[100:103], %0" : "+v"(ac[1]) :  : ); SB();
  asm volatile("v_mfma_f32_16x16x32_bf16 %0, a[132:135], a[120:123], %0" : "+v"(ac[7]) :  : ); SB();
  asm volatile("v_mfma_f32_16x16x32_bf16 %0, a[132:135], a[116:119], %0" : "+v"(ac[6]) :  : ); SB();
  asm volatile("v_mfma_f32_16x16x32_bf16 %0, a[128:131], a[104:107], %0" : "+v"(ac[2]) :  : ); SB();
  asm volatile("ds_write_b128 %0, %1 offset:0" :  : "v"(dd), "v"(ac[0]) : "memory"); SB();
  asm volatile("v_mfma_f32_16x16x32_bf16 %0, a[132:135], a[96:99], %0" : "+v"(ac[1]) :  : ); SB();
  asm volatile("v_mfma_f32_16x16x32_bf16 %0, a[136:139], a[116:119], %0" : "+v"(ac[7]) :  : ); SB();
  asm volatile("v_mfma_f32_16x16x32_bf16 %0, a[128:131], a[108:111], %0" : "+v"(ac[3]) :  : ); SB();
  asm volatile("v_mfma_f32_16x16x32_bf16 %0, a[132:135], a[100:103], %0" : "+v"(ac[2]) :  : ); SB();
  asm volatile("v_mfma_f32_16x16x32_bf16 %0, a[128:131], a[112:115], %0" : "+v"(ac[4]) :  : ); SB();
  asm volatile("s_waitcnt vmcnt(7)\n\t.Lc_%=:\n\tv_cmp_ne_u64 vcc, %2, %1\n\ts_cbranch_vccnz .Lr_%=\n\t.Ld_%=:\n\t.subsection 1\n\t.Lr_%=:\n\tv_add_u32 %0, -1, %0\n\ts_nop 1\n\tv_readfirstlane_b32 vcc_hi, %0\n\ts_nop 3\n\ts_cmp_lt_i32 vcc_hi, 1\n\ts_cbranch_scc1 .Ld_%=\n\ts_sleep 2\n\ts_mov_b32 vcc_lo, 0\n\tbuffer_load_dwordx2 %1, %3, %4, vcc_lo offen sc1\n\ts_waitcnt vmcnt(0)\n\ts_branch .Lc_%=\n\t.subsection 0" : "+v"(tmo), "+v"(fh) : "s"(tg), "v"(vFH), "s"(rWS) : "memory", "vcc", "scc"); SB();
  asm volatile("buffer_load_dwordx4 v[164:167], %0, %1, %2 offen offset:0 sc1" :  : "v"(vHr), "s"(rWS), "s"(hso[2]) : "memory"); SB();
  asm volatile("v_mfma_f32_16x16x32_bf16 %0, a[140:143], a[112:115], %0" : "+v"(ac[7]) :  : ); SB();
  asm volatile("buffer_load_dwordx4 v[168:171], %0, %1, %2 offen offset:0 sc1" :  : "v"(vHr), "s"(rWS), "s"(hso[4]) : "memory"); SB();
  asm volatile("v_mfma_f32_16x16x32_bf16 %0, a[132:135], a[104:107], %0" : "+v"(ac[3]) :  : ); SB();
  asm volatile("ds_write_b128 %0, %1 offset:1024" :  : "v"(dd), "v"(ac[1]) : "memory"); SB();
  asm volatile("v_mfma_f32_16x16x32_bf16 %0, a[136:139], a[96:99], %0" : "+v"(ac[2]) :  : ); SB();
  asm volatile("buffer_load_dwordx4 v[172:175], %0, %1, %2 offen offset:0 sc1" :  : "v"(vHr), "s"(rWS), "s"(hso[6]) : "memory"); SB();
  asm volatile("v_mfma_f32_16x16x32_bf16 %0, a[132:135], a[108:111], %0" : "+v"(ac[4]) :  : ); SB();
  asm volatile("v_mfma_f32_16x16x32_bf16 %0, a[128:131], a[116:119], %0" : "+v"(ac[5]) :  : ); SB();
  asm volatile("v_mfma_f32_16x16x32_bf16 %0, a[136:139], a[100:103], %0" : "+v"(ac[3]) :  : ); SB();
  asm volatile("v_mfma_f32_16x16x32_bf16 %0, a[136:139], a[112:115], %0" : "+v"(ac[6]) :  : ); SB();
  asm volatile("buffer_load_dwordx4 v[160:163], %0, %1, %2 offen offset:0 sc1" :  : "v"(vHr), "s"(rWS), "s"(hso[0]) : "memory"); SB();
  asm volatile("v_mfma_f32_16x16x32_bf16 %0, a[136:139], a[104:107], %0" : "+v"(ac[4]) :  : ); SB();
  asm volatile("ds_write_b128 %0, %1 offset:2048" :  : "v"(dd), "v"(ac[2]) : "memory"); SB();
  asm volatile("v_mfma_f32_16x16x32_bf16 %0, a[132:135], a[112:115], %0" : "+v"(ac[5]) :  : ); SB();
  asm volatile("buffer_load_dwordx4 v[180:183], %0, %1, %2 offen offset:1024 sc1" :  : "v"(vHr), "s"(rWS), "s"(hso[2]) : "memory"); SB();
  asm volatile("v_mfma_f32_16x16x32_bf16 %0, a[140:143], a[96:99], %0" : "+v"(ac[3]) :  : ); SB();
  asm volatile("v_mfma_f32_16x16x32_bf16 %0, a[140:143], a[108:111], %0" : "+v"(ac[6]) :  : ); SB();
  asm volatile("v_mfma_f32_16x16x32_bf16 %0, a[140:143], a[100:103], %0" : "+v"(ac[4]) :  : ); SB();
  asm volatile("v_mfma_f32_16x16x32_bf16 %0, a[136:139], a[108:111], %0" : "+v"(ac[5]) :  : ); SB();
  asm volatile("buffer_load_dwordx4 v[184:187], %0, %1, %2 offen offset:1024 sc1" :  : "v"(vHr), "s"(rWS), "s"(hso[4]) : "memory"); SB();
  asm volatile("v_mfma_f32_16x16x32_bf16 %0, a[144:147], a[108:111], %0" : "+v"(ac[7]) :  : ); SB();
  asm volatile("ds_write_b128 %0, %1 offset:3072" :  : "v"(dd), "v"(ac[3]) : "memory"); SB();
  asm volatile("v_mfma_f32_16x16x32_bf16 %0, a[144:147], a[104:107], %0" : "+v"(ac[6]) :  : ); SB();
  asm volatile("buffer_load_dwordx4 v[188:191], %0, %1, %2 offen offset:1024 sc1" :  : "v"(vHr), "s"(rWS), "s"(hso[6]) : "memory"); SB();
  asm volatile("v_mfma_f32_16x16x32_bf16 %0, a[144:147], a[96:99], %0" : "+v"(ac[4]) :  : ); SB();
  asm volatile("v_mfma_f32_16x16x32_bf16 %0, a[140:143], a[104:107], %0" : "+v"(ac[5]) :  : ); SB();
  asm volatile("v_mfma_f32_16x16x32_bf16 %0, a[148:151], a[104:107], %0" : "+v"(ac[7]) :  : ); SB();
  asm volatile("v_mfma_f32_16x16x32_bf16 %0, a[148:151], a[100:103], %0" : "+v"(ac[6]) :  : ); SB();
  asm volatile("buffer_load_dwordx4 v[176:179], %0, %1, %2 offen offset:1024 sc1" :  : "v"(vHr), "s"(rWS), "s"(hso[0]) : "memory"); SB();
  asm volatile("v_mfma_f32_16x16x32_bf16 %0, a[144:147], a[100:103], %0" : "+v"(ac[5]) :  : ); SB();
  asm volatile("ds_write_b128 %0, %1 offset:4096" :  : "v"(dd), "v"(ac[4]) : "memory"); SB();
  asm volatile("s_waitcnt lgkmcnt(2)\n\tv_mfma_f32_16x16x32_bf16 %0, a[152:155], a[100:103], %0" : "+v"(ac[7]) :  : ); SB();
  asm volatile("buffer_load_dwordx4 v[196:199], %0, %1, %2 offen offset:0 sc1" :  : "v"(vHr), "s"(rWS), "s"(hso[3]) : "memory"); SB();
  asm volatile("v_mfma_f32_16x16x32_bf16 %0, a[152:155], a[96:99], %0" : "+v"(ac[6]) :  : ); SB();
  asm volatile("v_mfma_f32_16x16x32_bf16 %0, a[148:151], a[96:99], %0" : "+v"(ac[5]) :  : ); SB();
  asm volatile("v_mfma_f32_16x16x32_bf16 %0, a[156:159], a[96:99], %0" : "+v"(ac[7]) :  : ); SB();
  asm volatile("buffer_load_dwordx4 v[200:203], %0, %1, %2 offen offset:0 sc1" :  : "v"(vHr), "s"(rWS), "s"(hso[5]) : "memory"); SB();
  asm volatile("s_nop 5" : "+v"(ac[5]), "+v"(ac[6]), "+v"(ac[7]) :  : ); SB();
  asm volatile("ds_write_b128 %0, %1 offset:6144" :  : "v"(dd), "v"(ac[6]) : "memory"); SB();
  asm volatile("ds_write_b128 %0, %1 offset:5120" :  : "v"(dd), "v"(ac[5]) : "memory"); SB();
  asm volatile("ds_write_b128 %0, %1 offset:7168" :  : "v"(dd), "v"(ac[7]) : "memory"); SB();
  ST(18);
  f32x4 yp[2][4], o3[4][5];
  u32x4 yk[2], yo[2];
  unsigned vtmp;
  asm volatile("ds_read_b128 %0, %1 offset:0" : "=&v"(yk[0]) : "v"(ykr) : "memory"); SB();
  asm volatile("ds_read_b128 %0, %1 offset:12928" : "=&v"(yk[1]) : "v"(ykr) : "memory"); SB();
  asm volatile("s_waitcnt lgkmcnt(0)\n\ts_barrier" :  :  : "memory"); SB();
  asm volatile("ds_read_b128 %0, %1 offset:0" : "=&v"(yp[0][0]) : "v"(ydr) : "memory"); SB();
  asm volatile("ds_read_b128 %0, %1 offset:256" : "=&v"(yp[0][1]) : "v"(ydr) : "memory"); SB();
  asm volatile("ds_read_b128 %0, %1 offset:8192" : "=&v"(yp[0][2]) : "v"(ydr) : "memory"); SB();
  asm volatile("ds_read_b128 %0, %1 offset:8448" : "=&v"(yp[0][3]) : "v"(ydr) : "memory"); SB();
  asm volatile("ds_read_b128 %0, %1 offset:16384" : "=&v"(yp[1][0]) : "v"(ydr) : "memory"); SB();
  asm volatile("ds_read_b128 %0, %1 offset:16640" : "=&v"(yp[1][1]) : "v"(ydr) : "memory"); SB();
  asm volatile("ds_read_b128 %0, %1 offset:24576" : "=&v"(yp[1][2]) : "v"(ydr) : "memory"); SB();
  asm volatile("ds_read_b128 %0, %1 offset:24832" : "=&v"(yp[1][3]) : "v"(ydr) : "memory"); SB();
  asm volatile("" : "+v"(yk[0]), "+v"(yk[1]) :  : ); SB();
  f32x2 yd[2][4];
#pragma unroll
  for (int n = 0; n < 8; ++n) { const float Dv = dD[n >> 2]; yd[n >> 2][n & 3] = unpk(yk[n >> 2][n & 3]) * (f32x2){Dv, Dv}; asm volatile("" : "+v"(yd[n >> 2][n & 3])); }
  SB();
  asm volatile("s_waitcnt lgkmcnt(3)" : "+v"(yp[0][0]), "+v"(yp[0][1]), "+v"(yp[0][2]), "+v"(yp[0][3]) :  : "memory"); SB();
  {
    const f32x4 s0 = yp[0][0] + yp[0][2], s1 = yp[0][1] + yp[0][3];
    yo[0] = (u32x4){pk2((f32x2){s0[0], s0[1]} + yd[0][0]), pk2((f32x2){s0[2], s0[3]} + yd[0][1]), pk2((f32x2){s1[0], s1[1]} + yd[0][2]), ~(nohole(pk2((f32x2){s1[2], s1[3]} + yd[0][3])) ^ yk[0][3])};
    SB();
  }
  asm volatile("buffer_store_dwordx4 %1, %0, %2, %3 offen offset:0 sc1\n\ts_nop 1" :  : "v"(vF), "v"(yo[0]), "s"(rWS), "s"(W_KV + 0) : "memory"); SB();
  asm volatile("s_waitcnt lgkmcnt(0)" : "+v"(yp[1][0]), "+v"(yp[1][1]), "+v"(yp[1][2]), "+v"(yp[1][3]) :  : "memory"); SB();
  {
    const f32x4 s0 = yp[1][0] + yp[1][2], s1 = yp[1][1] + yp[1][3];
    yo[1] = (u32x4){pk2((f32x2){s0[0], s0[1]} + yd[1][0]), pk2((f32x2){s0[2], s0[3]} + yd[1][1]), pk2((f32x2){s1[0], s1[1]} + yd[1][2]), ~(nohole(pk2((f32x2){s1[2], s1[3]} + yd[1][3])) ^ yk[1][3])};
    SB();
  }
  asm volatile("buffer_store_dwordx4 %1, %0, %2, %3 offen offset:0 sc1\n\ts_nop 1" :  : "v"(vF), "v"(yo[1]), "s"(rWS), "s"(W_KV + 4096) : "memory"); SB();
  ST(19);
  ST(20);
  f32x2 xf[5][2], sv[5][2];
  float rd, mr;
  asm volatile("s_waitcnt vmcnt(8)" :  :  : "memory"); SB();
  asm volatile("v_mfma_f32_16x16x32_bf16 %0, a[160:163], v[164:167], 0" : "=&v"(o3[1][0]) :  : ); SB();
  asm volatile("buffer_load_dwordx2 %0, %1, %2, %3 offen sc1" : "=&v"(fi) : "v"(vFH), "s"(rWS), "s"(8192u) : "memory"); SB();
  asm volatile("v_mfma_f32_16x16x32_bf16 %0, a[164:167], v[164:167], 0" : "=&v"(o3[1][1]) :  : ); SB();
  asm volatile("v_mfma_f32_16x16x32_bf16 %0, a[168:171], v[164:167], 0" : "=&v"(o3[1][2]) :  : ); SB();
  asm volatile("v_mfma_f32_16x16x32_bf16 %0, a[172:175], v[164:167], 0" : "=&v"(o3[1][3]) :  : ); SB();
  asm volatile("buffer_load_dwordx4 v[204:207], %0, %1, %2 offen offset:0 sc1" :  : "v"(vHr), "s"(rWS), "s"(hso[7]) : "memory"); SB();
  asm volatile("v_mfma_f32_16x16x32_bf16 %0, a[176:179], v[164:167], 0" : "=&v"(o3[1][4]) :  : ); SB();
  asm volatile("v_mfma_f32_16x16x32_bf16 %0, a[160:163], v[168:171], 0" : "=&v"(o3[2][0]) :  : ); SB();
  asm volatile("v_mfma_f32_16x16x32_bf16 %0, a[164:167], v[168:171], 0" : "=&v"(o3[2][1]) :  : ); SB();
  asm volatile("v_mfma_f32_16x16x32_bf16 %0, a[168:171], v[168:171], 0" : "=&v"(o3[2][2]) :  : ); SB();
  asm volatile("buffer_load_dwordx4 v[192:195], %0, %1, %2 offen offset:0 sc1" :  : "v"(vHr), "s"(rWS), "s"(hso[1]) : "memory"); SB();
  asm volatile("v_mfma_f32_16x16x32_bf16 %0, a[172:175], v[168:171], 0" : "=&v"(o3[2][3]) :  : ); SB();
  asm volatile("v_mfma_f32_16x16x32_bf16 %0, a[176:179], v[168:171], 0" : "=&v"(o3[2][4]) :  : ); SB();
  asm volatile("v_mfma_f32_16x16x32_bf16 %0, a[160:163], v[172:175], 0" : "=&v"(o3[3][0]) :  : ); SB();
  asm volatile("buffer_load_dwordx4 v[212:215], %0, %1, %2 offen offset:1024 sc1" :  : "v"(vHr), "s"(rWS), "s"(hso[3]) : "memory"); SB();
  asm volatile("v_mfma_f32_16x16x32_bf16 %0, a[164:167], v[172:175], 0" : "=&v"(o3[3][1]) :  : ); SB();
  asm volatile("v_mfma_f32_16x16x32_bf16 %0, a[168:171], v[172:175], 0" : "=&v"(o3[3][2]) :  : ); SB();
  asm volatile("v_mfma_f32_16x16x32_bf16 %0, a[172:175], v[172:175], 0" : "=&v"(o3[3][3]) :  : ); SB();
  asm volatile("v_mfma_f32_16x16x32_bf16 %0, a[176:179], v[172:175], 0" : "=&v"(o3[3][4]) :  : ); SB();
  asm volatile("buffer_load_dwordx4 v[216:219], %0, %1, %2 offen offset:1024 sc1" :  : "v"(vHr), "s"(rWS), "s"(hso[5]) : "memory"); SB();
  asm volatile("s_waitcnt vmcnt(9)" :  :  : "memory"); SB();
  asm volatile("v_mfma_f32_16x16x32_bf16 %0, a[180:183], v[180:183], %0" : "+v"(o3[1][0]) :  : ); SB();
  asm volatile("v_rcp_f32 %0, %1" : "=v"(rd) : "v"(xd[0]) : ); SB();
  asm volatile("v_mfma_f32_16x16x32_bf16 %0, a[184:187], v[180:183], %0" : "+v"(o3[1][1]) :  : ); SB();
  asm volatile("" : "+v"(xv[0]), "+v"(xv[1]), "+v"(xv[2]), "+v"(xv[3]), "+v"(xv[4]) :  : "memory"); SB();
  asm volatile("v_mul_f32 %0, -%1, %2" : "=v"(mr) : "v"(xm[0]), "v"(rd) : ); SB();
  asm volatile("v_mfma_f32_16x16x32_bf16 %0, a[188:191], v[180:183], %0" : "+v"(o3[1][2]) :  : ); SB();
  asm volatile("buffer_load_dwordx4 v[220:223], %0, %1, %2 offen offset:1024 sc1" :  : "v"(vHr), "s"(rWS), "s"(hso[7]) : "memory"); SB();
  asm volatile("v_lshlrev_b32 %0, 16, %1" : "=v"(xf[0][0][0]) : "v"(xv[0][0]) : ); SB();
  asm volatile("v_mfma_f32_16x16x32_bf16 %0, a[192:195], v[180:183], %0" : "+v"(o3[1][3]) :  : ); SB();
  asm volatile("v_and_b32 %0, 0xffff0000, %1" : "=v"(xf[0][0][1]) : "v"(xv[0][0]) : ); SB();
  asm volatile("v_mfma_f32_16x16x32_bf16 %0, a[196:199], v[180:183], %0" : "+v"(o3[1][4]) :  : ); SB();
  asm volatile("v_lshlrev_b32 %0, 16, %1" : "=v"(xf[0][1][0]) : "v"(xv[0][1]) : ); SB();
  asm volatile("v_mfma_f32_16x16x32_bf16 %0, a[180:183], v[184:187], %0" : "+v"(o3[2][0]) :  : ); SB();
  asm volatile("v_and_b32 %0, 0xffff0000, %1" : "=v"(xf[0][1][1]) : "v"(xv[0][1]) : ); SB();
  asm volatile("v_mfma_f32_16x16x32_bf16 %0, a[184:187], v[184:187], %0" : "+v"(o3[2][1]) :  : ); SB();
  asm volatile("buffer_load_dwordx4 v[208:211], %0, %1, %2 offen offset:1024 sc1" :  : "v"(vHr), "s"(rWS), "s"(hso[1]) : "memory"); SB();
  asm volatile("v_lshlrev_b32 %0, 16, %1" : "=v"(xf[1][0][0]) : "v"(xv[1][0]) : ); SB();
  asm volatile("v_mfma_f32_16x16x32_bf16 %0, a[188:191], v[184:187], %0" : "+v"(o3[2][2]) :  : ); SB();
  asm volatile("v_and_b32 %0, 0xffff0000, %1" : "=v"(xf[1][0][1]) : "v"(xv[1][0]) : ); SB();
  asm volatile("v_mfma_f32_16x16x32_bf16 %0, a[192:195], v[184:187], %0" : "+v"(o3[2][3]) :  : ); SB();
  asm volatile("v_lshlrev_b32 %0, 16, %1" : "=v"(xf[1][1][0]) : "v"(xv[1][1]) : ); SB();
  asm volatile("v_mfma_f32_16x16x32_bf16 %0, a[196:199], v[184:187], %0" : "+v"(o3[2][4]) :  : ); SB();
  asm volatile("v_and_b32 %0, 0xffff0000, %1" : "=v"(xf[1][1][1]) : "v"(xv[1][1]) : ); SB();
  asm volatile("v_mfma_f32_16x16x32_bf16 %0, a[180:183], v[188:191], %0" : "+v"(o3[3][0]) :  : ); SB();
  asm volatile("v_lshlrev_b32 %0, 16, %1" : "=v"(xf[2][0][0]) : "v"(xv[2][0]) : ); SB();
  asm volatile("v_mfma_f32_16x16x32_bf16 %0, a[184:187], v[188:191], %0" : "+v"(o3[3][1]) :  : ); SB();
  asm volatile("v_and_b32 %0, 0xffff0000, %1" : "=v"(xf[2][0][1]) : "v"(xv[2][0]) : ); SB();
  asm volatile("v_mfma_f32_16x16x32_bf16 %0, a[188:191], v[188:191], %0" : "+v"(o3[3][2]) :  : ); SB();
  asm volatile("v_lshlrev_b32 %0, 16, %1" : "=v"(xf[2][1][0]) : "v"(xv[2][1]) : ); SB();
  asm volatile("v_mfma_f32_16x16x32_bf16 %0, a[192:195], v[188:191], %0" : "+v"(o3[3][3]) :  : ); SB();
  asm volatile("v_and_b32 %0, 0xffff0000, %1" : "=v"(xf[2][1][1]) : "v"(xv[2][1]) : ); SB();
  asm volatile("v_mfma_f32_16x16x32_bf16 %0, a[196:199], v[188:191], %0" : "+v"(o3[3][4]) :  : ); SB();
  asm volatile("v_lshlrev_b32 %0, 16, %1" : "=v"(xf[3][0][0]) : "v"(xv[3][0]) : ); SB();
  asm volatile("s_waitcnt vmcnt(6)\n\t.Lc_%=:\n\tv_cmp_ne_u64 vcc, %2, %1\n\ts_cbranch_vccnz .Lr_%=\n\t.Ld_%=:\n\t.subsection 1\n\t.Lr_%=:\n\tv_add_u32 %0, -1, %0\n\ts_nop 1\n\tv_readfirstlane_b32 vcc_hi, %0\n\ts_nop 3\n\ts_cmp_lt_i32 vcc_hi, 1\n\ts_cbranch_scc1 .Ld_%=\n\ts_sleep 2\n\ts_mov_b32 vcc_lo, 8192\n\tbuffer_load_dwordx2 %1, %3, %4, vcc_lo offen sc1\n\ts_waitcnt vmcnt(0)\n\ts_branch .Lc_%=\n\t.subsection 0" : "+v"(tmo), "+v"(fi) : "s"(tg), "v"(vFH), "s"(rWS) : "memory", "vcc", "scc"); SB();
  asm volatile("buffer_load_dwordx4 v[228:231], %0, %1, %2 offen offset:2048 sc1" :  : "v"(vHr), "s"(rWS), "s"(hso[2]) : "memory"); SB();
  asm volatile("buffer_load_dwordx4 v[232:235], %0, %1, %2 offen offset:2048 sc1" :  : "v"(vHr), "s"(rWS), "s"(hso[4]) : "memory"); SB();
  asm volatile("buffer_load_dwordx4 v[236:239], %0, %1, %2 offen offset:2048 sc1" :  : "v"(vHr), "s"(rWS), "s"(hso[6]) : "memory"); SB();
  asm volatile("s_waitcnt vmcnt(7)" :  :  : "memory"); SB();
  asm volatile("v_mfma_f32_16x16x32_bf16 %0, a[200:203], v[196:199], %0" : "+v"(o3[1][0]) :  : ); SB();
  asm volatile("v_and_b32 %0, 0xffff0000, %1" : "=v"(xf[3][0][1]) : "v"(xv[3][0]) : ); SB();
  asm volatile("v_mfma_f32_16x16x32_bf16 %0, a[204:207], v[196:199], %0" : "+v"(o3[1][1]) :  : ); SB();
  asm volatile("buffer_load_dwordx4 v[224:227], %0, %1, %2 offen offset:2048 sc1" :  : "v"(vHr), "s"(rWS), "s"(hso[0]) : "memory"); SB();
  asm volatile("v_lshlrev_b32 %0, 16, %1" : "=v"(xf[3][1][0]) : "v"(xv[3][1]) : ); SB();
  asm volatile("v_mfma_f32_16x16x32_bf16 %0, a[208:211], v[196:199], %0" : "+v"(o3[1][2]) :  : ); SB();
  asm volatile("v_and_b32 %0, 0xffff0000, %1" : "=v"(xf[3][1][1]) : "v"(xv[3][1]) : ); SB();
  asm volatile("v_mfma_f32_16x16x32_bf16 %0, a[212:215], v[196:199], %0" : "+v"(o3[1][3]) :  : ); SB();
  asm volatile("v_lshlrev_b32 %0, 16, %1" : "=v"(xf[4][0][0]) : "v"(xv[4][0]) : ); SB();
  asm volatile("v_mfma_f32_16x16x32_bf16 %0, a[216:219], v[196:199], %0" : "+v"(o3[1][4]) :  : ); SB();
  asm volatile("v_and_b32 %0, 0xffff0000, %1" : "=v"(xf[4][0][1]) : "v"(xv[4][0]) : ); SB();
  asm volatile("v_mfma_f32_16x16x32_bf16 %0, a[200:203], v[200:203], %0" : "+v"(o3[2][0]) :  : ); SB();
  asm volatile("buffer_load_dwordx4 v[244:247], %0, %1, %2 offen offset:2048 sc1" :  : "v"(vHr), "s"(rWS), "s"(hso[3]) : "memory"); SB();
  asm volatile("v_lshlrev_b32 %0, 16, %1" : "=v"(xf[4][1][0]) : "v"(xv[4][1]) : ); SB();
  asm volatile("v_mfma_f32_16x16x32_bf16 %0, a[204:207], v[200:203], %0" : "+v"(o3[2][1]) :  : ); SB();
  asm volatile("v_and_b32 %0, 0xffff0000, %1" : "=v"(xf[4][1][1]) : "v"(xv[4][1]) : ); SB();
  asm volatile("v_mfma_f32_16x16x32_bf16 %0, a[208:211], v[200:203], %0" : "+v"(o3[2][2]) :  : ); SB();
  asm volatile("v_fma_f32 %0, %1, %2, %3" : "=v"(sv[0][0][0]) : "v"(xf[0][0][0]), "v"(rd), "v"(mr) : ); SB();
  asm volatile("v_mfma_f32_16x16x32_bf16 %0, a[212:215], v[200:203], %0" : "+v"(o3[2][3]) :  : ); SB();
  asm volatile("buffer_load_dwordx4 v[248:251], %0, %1, %2 offen offset:2048 sc1" :  : "v"(vHr), "s"(rWS), "s"(hso[5]) : "memory"); SB();
  asm volatile("v_fma_f32 %0, %1, %2, %3" : "=v"(sv[0][0][1]) : "v"(xf[0][0][1]), "v"(rd), "v"(mr) : ); SB();
  asm volatile("v_mfma_f32_16x16x32_bf16 %0, a[216:219], v[200:203], %0" : "+v"(o3[2][4]) :  : ); SB();
  asm volatile("v_fma_f32 %0, %1, %2, %3" : "=v"(sv[0][1][0]) : "v"(xf[0][1][0]), "v"(rd), "v"(mr) : ); SB();
  asm volatile("v_mfma_f32_16x16x32_bf16 %0, a[200:203], v[204:207], %0" : "+v"(o3[3][0]) :  : ); SB();
  asm volatile("v_fma_f32 %0, %1, %2, %3" : "=v"(sv[0][1][1]) : "v"(xf[0][1][1]), "v"(rd), "v"(mr) : ); SB();
  asm volatile("v_mfma_f32_16x16x32_bf16 %0, a[204:207], v[204:207], %0" : "+v"(o3[3][1]) :  : ); SB();
  asm volatile("v_fma_f32 %0, %1, %2, %3" : "=v"(sv[1][0][0]) : "v"(xf[1][0][0]), "v"(rd), "v"(mr) : ); SB();
  asm volatile("v_mfma_f32_16x16x32_bf16 %0, a[208:211], v[204:207], %0" : "+v"(o3[3][2]) :  : ); SB();
  asm volatile("buffer_load_dwordx4 v[252:255], %0, %1, %2 offen offset:2048 sc1" :  : "v"(vHr), "s"(rWS), "s"(hso[7]) : "memory"); SB();
  asm volatile("v_fma_f32 %0, %1, %2, %3" : "=v"(sv[1][0][1]) : "v"(xf[1][0][1]), "v"(rd), "v"(mr) : ); SB();
  asm volatile("v_mfma_f32_16x16x32_bf16 %0, a[212:215], v[204:207], %0" : "+v"(o3[3][3]) :  : ); SB();
  asm volatile("v_fma_f32 %0, %1, %2, %3" : "=v"(sv[1][1][0]) : "v"(xf[1][1][0]), "v"(rd), "v"(mr) : ); SB();
  asm volatile("v_mfma_f32_16x16x32_bf16 %0, a[216:219], v[204:207], %0" : "+v"(o3[3][4]) :  : ); SB();
  asm volatile("v_fma_f32 %0, %1, %2, %3" : "=v"(sv[1][1][1]) : "v"(xf[1][1][1]), "v"(rd), "v"(mr) : ); SB();
  asm volatile("s_waitcnt vmcnt(7)" :  :  : "memory"); SB();
  asm volatile("v_mfma_f32_16x16x32_bf16 %0, a[220:223], v[212:215], %0" : "+v"(o3[1][0]) :  : ); SB();
  asm volatile("buffer_load_dwordx4 v[240:243], %0, %1, %2 offen offset:2048 sc1" :  : "v"(vHr), "s"(rWS), "s"(hso[1]) : "memory"); SB();
  asm volatile("v_fma_f32 %0, %1, %2, %3" : "=v"(sv[2][0][0]) : "v"(xf[2][0][0]), "v"(rd), "v"(mr) : ); SB();
  asm volatile("v_mfma_f32_16x16x32_bf16 %0, a[224:227], v[212:215], %0" : "+v"(o3[1][1]) :  : ); SB();
  asm volatile("v_fma_f32 %0, %1, %2, %3" : "=v"(sv[2][0][1]) : "v"(xf[2][0][1]), "v"(rd), "v"(mr) : ); SB();
  asm volatile("v_mfma_f32_16x16x32_bf16 %0, a[228:231], v[212:215], %0" : "+v"(o3[1][2]) :  : ); SB();
  asm volatile("v_fma_f32 %0, %1, %2, %3" : "=v"(sv[2][1][0]) : "v"(xf[2][1][0]), "v"(rd), "v"(mr) : ); SB();
  asm volatile("v_mfma_f32_16x16x32_bf16 %0, a[232:235], v[212:215], %0" : "+v"(o3[1][3]) :  : ); SB();
  asm volatile("v_fma_f32 %0, %1, %2, %3" : "=v"(sv[2][1][1]) : "v"(xf[2][1][1]), "v"(rd), "v"(mr) : ); SB();
  asm volatile("v_mfma_f32_16x16x32_bf16 %0, a[236:239], v[212:215], %0" : "+v"(o3[1][4]) :  : ); SB();
  asm volatile("v_fma_f32 %0, %1, %2, %3" : "=v"(sv[3][0][0]) : "v"(xf[3][0][0]), "v"(rd), "v"(mr) : ); SB();
  asm volatile("v_mfma_f32_16x16x32_bf16 %0, a[220:223], v[216:219], %0" : "+v"(o3[2][0]) :  : ); SB();
  asm volatile("v_fma_f32 %0, %1, %2, %3" : "=v"(sv[3][0][1]) : "v"(xf[3][0][1]), "v"(rd), "v"(mr) : ); SB();
  asm volatile("v_mfma_f32_16x16x32_bf16 %0, a[224:227], v[216:219], %0" : "+v"(o3[2][1]) :  : ); SB();
  asm volatile("v_fma_f32 %0, %1, %2, %3" : "=v"(sv[3][1][0]) : "v"(xf[3][1][0]), "v"(rd), "v"(mr) : ); SB();
  asm volatile("v_mfma_f32_16x16x32_bf16 %0, a[228:231], v[216:219], %0" : "+v"(o3[2][2]) :  : ); SB();
  asm volatile("v_fma_f32 %0, %1, %2, %3" : "=v"(sv[3][1][1]) : "v"(xf[3][1][1]), "v"(rd), "v"(mr) : ); SB();
  asm volatile("v_mfma_f32_16x16x32_bf16 %0, a[232:235], v[216:219], %0" : "+v"(o3[2][3]) :  : ); SB();
  asm volatile("v_fma_f32 %0, %1, %2, %3" : "=v"(sv[4][0][0]) : "v"(xf[4][0][0]), "v"(rd), "v"(mr) : ); SB();
  asm volatile("v_mfma_f32_16x16x32_bf16 %0, a[236:239], v[216:219], %0" : "+v"(o3[2][4]) :  : ); SB();
  asm volatile("v_fma_f32 %0, %1, %2, %3" : "=v"(sv[4][0][1]) : "v"(xf[4][0][1]), "v"(rd), "v"(mr) : ); SB();
  asm volatile("v_mfma_f32_16x16x32_bf16 %0, a[220:223], v[220:223], %0" : "+v"(o3[3][0]) :  : ); SB();
  asm volatile("v_fma_f32 %0, %1, %2, %3" : "=v"(sv[4][1][0]) : "v"(xf[4][1][0]), "v"(rd), "v"(mr) : ); SB();
  asm volatile("v_mfma_f32_16x16x32_bf16 %0, a[224:227], v[220:223], %0" : "+v"(o3[3][1]) :  : ); SB();
  asm volatile("v_fma_f32 %0, %1, %2, %3" : "=v"(sv[4][1][1]) : "v"(xf[4][1][1]), "v"(rd), "v"(mr) : ); SB();
  asm volatile("v_mfma_f32_16x16x32_bf16 %0, a[228:231], v[220:223], %0" : "+v"(o3[3][2]) :  : ); SB();
  asm volatile("v_mfma_f32_16x16x32_bf16 %0, a[232:235], v[220:223], %0" : "+v"(o3[3][3]) :  : ); SB();
  asm volatile("v_mfma_f32_16x16x32_bf16 %0, a[236:239], v[220:223], %0" : "+v"(o3[3][4]) :  : ); SB();
  ST(30);
  asm volatile("buffer_load_dwordx4 v[164:167], %0, %1, %2 offen offset:0 sc1" :  : "v"(vKV), "s"(rWS), "s"(0u) : "memory"); SB();
  asm volatile("buffer_load_dwordx4 v[168:171], %0, %1, %2 offen offset:0 sc1" :  : "v"(vKV), "s"(rWS), "s"(w3k[0]) : "memory"); SB();
  asm volatile("buffer_load_dwordx4 v[172:175], %0, %1, %2 offen offset:0 sc1" :  : "v"(vKV), "s"(rWS), "s"(w3k[1]) : "memory"); SB();
  asm volatile("s_waitcnt vmcnt(3)" :  :  : "memory"); SB();
  asm volatile("v_mfma_f32_16x16x32_bf16 %0, a[240:243], v[228:231], %0" : "+v"(o3[1][0]) :  : ); SB();
  asm volatile("v_mfma_f32_16x16x32_bf16 %0, a[244:247], v[228:231], %0" : "+v"(o3[1][1]) :  : ); SB();
  asm volatile("v_mfma_f32_16x16x32_bf16 %0, a[248:251], v[228:231], %0" : "+v"(o3[1][2]) :  : ); SB();
  asm volatile("v_mfma_f32_16x16x32_bf16 %0, a[252:255], v[228:231], %0" : "+v"(o3[1][3]) :  : ); SB();
  asm volatile("v_mfma_f32_16x16x32_bf16 %0, a[0:3], v[228:231], %0" : "+v"(o3[1][4]) :  : ); SB();
  asm volatile("v_mfma_f32_16x16x32_bf16 %0, a[4:7], v[244:247], %0" : "+v"(o3[1][0]) :  : ); SB();
  asm volatile("v_mfma_f32_16x16x32_bf16 %0, a[8:11], v[244:247], %0" : "+v"(o3[1][1]) :  : ); SB();
  asm volatile("v_mfma_f32_16x16x32_bf16 %0, a[12:15], v[244:247], %0" : "+v"(o3[1][2]) :  : ); SB();
  asm volatile("v_mfma_f32_16x16x32_bf16 %0, a[16:19], v[244:247], %0" : "+v"(o3[1][3]) :  : ); SB();
  asm volatile("v_mfma_f32_16x16x32_bf16 %0, a[20:23], v[244:247], %0" : "+v"(o3[1][4]) :  : ); SB();
  asm volatile("v_mfma_f32_16x16x32_bf16 %0, a[240:243], v[232:235], %0" : "+v"(o3[2][0]) :  : ); SB();
  asm volatile("ds_write_b128 %0, %1 offset:0" :  : "v"(pw3), "v"(o3[1][0]) : "memory"); SB();
  asm volatile("v_mfma_f32_16x16x32_bf16 %0, a[244:247], v[232:235], %0" : "+v"(o3[2][1]) :  : ); SB();
  asm volatile("v_mfma_f32_16x16x32_bf16 %0, a[248:251], v[232:235], %0" : "+v"(o3[2][2]) :  : ); SB();
  asm volatile("v_mfma_f32_16x16x32_bf16 %0, a[252:255], v[232:235], %0" : "+v"(o3[2][3]) :  : ); SB();
  asm volatile("ds_write_b128 %0, %1 offset:1024" :  : "v"(pw3), "v"(o3[1][1]) : "memory"); SB();
  asm volatile("v_mfma_f32_16x16x32_bf16 %0, a[0:3], v[232:235], %0" : "+v"(o3[2][4]) :  : ); SB();
  asm volatile("v_mfma_f32_16x16x32_bf16 %0, a[4:7], v[248:251], %0" : "+v"(o3[2][0]) :  : ); SB();
  asm volatile("v_mfma_f32_16x16x32_bf16 %0, a[8:11], v[248:251], %0" : "+v"(o3[2][1]) :  : ); SB();
  asm volatile("ds_write_b128 %0, %1 offset:2048" :  : "v"(pw3), "v"(o3[1][2]) : "memory"); SB();
  asm volatile("v_mfma_f32_16x16x32_bf16 %0, a[12:15], v[248:251], %0" : "+v"(o3[2][2]) :  : ); SB();
  asm volatile("v_mfma_f32_16x16x32_bf16 %0, a[16:19], v[248:251], %0" : "+v"(o3[2][3]) :  : ); SB();
  asm volatile("v_mfma_f32_16x16x32_bf16 %0, a[20:23], v[248:251], %0" : "+v"(o3[2][4]) :  : ); SB();
  asm volatile("ds_write_b128 %0, %1 offset:3072" :  : "v"(pw3), "v"(o3[1][3]) : "memory"); SB();
  asm volatile("v_mfma_f32_16x16x32_bf16 %0, a[240:243], v[236:239], %0" : "+v"(o3[3][0]) :  : ); SB();
  asm volatile("v_mfma_f32_16x16x32_bf16 %0, a[244:247], v[236:239], %0" : "+v"(o3[3][1]) :  : ); SB();
  asm volatile("v_mfma_f32_16x16x32_bf16 %0, a[248:251], v[236:239], %0" : "+v"(o3[3][2]) :  : ); SB();
  asm volatile("ds_write_b128 %0, %1 offset:4096" :  : "v"(pw3), "v"(o3[1][4]) : "memory"); SB();
  asm volatile("v_mfma_f32_16x16x32_bf16 %0, a[252:255], v[236:239], %0" : "+v"(o3[3][3]) :  : ); SB();
  asm volatile("v_mfma_f32_16x16x32_bf16 %0, a[0:3], v[236:239], %0" : "+v"(o3[3][4]) :  : ); SB();
  asm volatile("v_mfma_f32_16x16x32_bf16 %0, a[4:7], v[252:255], %0" : "+v"(o3[3][0]) :  : ); SB();
  asm volatile("ds_write_b128 %0, %1 offset:5120" :  : "v"(pw3), "v"(o3[2][0]) : "memory"); SB();
  asm volatile("v_mfma_f32_16x16x32_bf16 %0, a[8:11], v[252:255], %0" : "+v"(o3[3][1]) :  : ); SB();
  asm volatile("v_mfma_f32_16x16x32_bf16 %0, a[12:15], v[252:255], %0" : "+v"(o3[3][2]) :  : ); SB();
  asm volatile("v_mfma_f32_16x16x32_bf16 %0, a[16:19], v[252:255], %0" : "+v"(o3[3][3]) :  : ); SB();
  asm volatile("ds_write_b128 %0, %1 offset:6144" :  : "v"(pw3), "v"(o3[2][1]) : "memory"); SB();
  asm volatile("v_mfma_f32_16x16x32_bf16 %0, a[20:23], v[252:255], %0" : "+v"(o3[3][4]) :  : ); SB();
  asm volatile("v_mfma_f32_16x16x32_bf16 %0, a[160:163], v[160:163], 0" : "=&v"(o3[0][0]) :  : ); SB();
  asm volatile("v_mfma_f32_16x16x32_bf16 %0, a[164:167], v[160:163], 0" : "=&v"(o3[0][1]) :  : ); SB();
  asm volatile("ds_write_b128 %0, %1 offset:7168" :  : "v"(pw3), "v"(o3[2][2]) : "memory"); SB();
  asm volatile("v_mfma_f32_16x16x32_bf16 %0, a[168:171], v[160:163], 0" : "=&v"(o3[0][2]) :  : ); SB();
  asm volatile("v_mfma_f32_16x16x32_bf16 %0, a[172:175], v[160:163], 0" : "=&v"(o3[0][3]) :  : ); SB();
  asm volatile("v_mfma_f32_16x16x32_bf16 %0, a[176:179], v[160:163], 0" : "=&v"(o3[0][4]) :  : ); SB();
  asm volatile("ds_write_b128 %0, %1 offset:8192" :  : "v"(pw3), "v"(o3[2][3]) : "memory"); SB();
  asm volatile("v_mfma_f32_16x16x32_bf16 %0, a[180:183], v[176:179], %0" : "+v"(o3[0][0]) :  : ); SB();
  asm volatile("v_mfma_f32_16x16x32_bf16 %0, a[184:187], v[176:179], %0" : "+v"(o3[0][1]) :  : ); SB();
  asm volatile("v_mfma_f32_16x16x32_bf16 %0, a[188:191], v[176:179], %0" : "+v"(o3[0][2]) :  : ); SB();
  asm volatile("ds_write_b128 %0, %1 offset:9216" :  : "v"(pw3), "v"(o3[2][4]) : "memory"); SB();
  asm volatile("v_mfma_f32_16x16x32_bf16 %0, a[192:195], v[176:179], %0" : "+v"(o3[0][3]) :  : ); SB();
  asm volatile("v_mfma_f32_16x16x32_bf16 %0, a[196:199], v[176:179], %0" : "+v"(o3[0][4]) :  : ); SB();
  asm volatile("v_mfma_f32_16x16x32_bf16 %0, a[200:203], v[192:195], %0" : "+v"(o3[0][0]) :  : ); SB();
  asm volatile("ds_write_b128 %0, %1 offset:10240" :  : "v"(pw3), "v"(o3[3][0]) : "memory"); SB();
  asm volatile("v_mfma_f32_16x16x32_bf16 %0, a[204:207], v[192:195], %0" : "+v"(o3[0][1]) :  : ); SB();
  asm volatile("v_mfma_f32_16x16x32_bf16 %0, a[208:211], v[192:195], %0" : "+v"(o3[0][2]) :  : ); SB();
  asm volatile("v_mfma_f32_16x16x32_bf16 %0, a[212:215], v[192:195], %0" : "+v"(o3[0][3]) :  : ); SB();
  asm volatile("ds_write_b128 %0, %1 offset:11264" :  : "v"(pw3), "v"(o3[3][1]) : "memory"); SB();
  asm volatile("v_mfma_f32_16x16x32_bf16 %0, a[216:219], v[192:195], %0" : "+v"(o3[0][4]) :  : ); SB();
  asm volatile("v_mfma_f32_16x16x32_bf16 %0, a[220:223], v[208:211], %0" : "+v"(o3[0][0]) :  : ); SB();
  asm volatile("v_mfma_f32_16x16x32_bf16 %0, a[224:227], v[208:211], %0" : "+v"(o3[0][1]) :  : ); SB();
  asm volatile("ds_write_b128 %0, %1 offset:12288" :  : "v"(pw3), "v"(o3[3][2]) : "memory"); SB();
  asm volatile("v_mfma_f32_16x16x32_bf16 %0, a[228:231], v[208:211], %0" : "+v"(o3[0][2]) :  : ); SB();
  asm volatile("v_mfma_f32_16x16x32_bf16 %0, a[232:235], v[208:211], %0" : "+v"(o3[0][3]) :  : ); SB();
  asm volatile("v_mfma_f32_16x16x32_bf16 %0, a[236:239], v[208:211], %0" : "+v"(o3[0][4]) :  : ); SB();
  asm volatile("ds_write_b128 %0, %1 offset:13312" :  : "v"(pw3), "v"(o3[3][3]) : "memory"); SB();
  asm volatile("v_mfma_f32_16x16x32_bf16 %0, a[240:243], v[224:227], %0" : "+v"(o3[0][0]) :  : ); SB();
  asm volatile("v_mfma_f32_16x16x32_bf16 %0, a[244:247], v[224:227], %0" : "+v"(o3[0][1]) :  : ); SB();
  asm volatile("v_mfma_f32_16x16x32_bf16 %0, a[248:251], v[224:227], %0" : "+v"(o3[0][2]) :  : ); SB();
  asm volatile("ds_write_b128 %0, %1 offset:14336" :  : "v"(pw3), "v"(o3[3][4]) : "memory"); SB();
  asm volatile("v_mfma_f32_16x16x32_bf16 %0, a[252:255], v[224:227], %0" : "+v"(o3[0][3]) :  : ); SB();
  asm volatile("v_mfma_f32_16x16x32_bf16 %0, a[0:3], v[224:227], %0" : "+v"(o3[0][4]) :  : ); SB();
  asm volatile("v_mfma_f32_16x16x32_bf16 %0, a[4:7], v[240:243], %0" : "+v"(o3[0][0]) :  : ); SB();
  asm volatile("v_mfma_f32_16x16x32_bf16 %0, a[8:11], v[240:243], %0" : "+v"(o3[0][1]) :  : ); SB();
  asm volatile("v_mfma_f32_16x16x32_bf16 %0, a[12:15], v[240:243], %0" : "+v"(o3[0][2]) :  : ); SB();
  asm volatile("v_mfma_f32_16x16x32_bf16 %0, a[16:19], v[240:243], %0" : "+v"(o3[0][3]) :  : ); SB();
  asm volatile("v_mfma_f32_16x16x32_bf16 %0, a[20:23], v[240:243], %0" : "+v"(o3[0][4]) :  : ); SB();
  asm volatile("s_waitcnt lgkmcnt(0)\n\ts_barrier" :  :  : "memory"); SB();
  asm volatile("ds_read_b128 %0, %1 offset:0" : "=&v"(o3[1][0]) : "v"(p3[0]) : "memory"); SB();
  asm volatile("ds_read_b128 %0, %1 offset:5120" : "=&v"(o3[2][0]) : "v"(p3[1]) : "memory"); SB();
  asm volatile("ds_read_b128 %0, %1 offset:10240" : "=&v"(o3[3][0]) : "v"(p3[2]) : "memory"); SB();
  asm volatile("ds_read_b128 %0, %1 offset:1024" : "=&v"(o3[1][1]) : "v"(p3[0]) : "memory"); SB();
  asm volatile("ds_read_b128 %0, %1 offset:6144" : "=&v"(o3[2][1]) : "v"(p3[1]) : "memory"); SB();
  asm volatile("ds_read_b128 %0, %1 offset:11264" : "=&v"(o3[3][1]) : "v"(p3[2]) : "memory"); SB();
  asm volatile("ds_read_b128 %0, %1 offset:2048" : "=&v"(o3[1][2]) : "v"(p3[0]) : "memory"); SB();
  asm volatile("ds_read_b128 %0, %1 offset:7168" : "=&v"(o3[2][2]) : "v"(p3[1]) : "memory"); SB();
  asm volatile("ds_read_b128 %0, %1 offset:12288" : "=&v"(o3[3][2]) : "v"(p3[2]) : "memory"); SB();
  asm volatile("ds_read_b128 %0, %1 offset:3072" : "=&v"(o3[1][3]) : "v"(p3[0]) : "memory"); SB();
  asm volatile("ds_read_b128 %0, %1 offset:8192" : "=&v"(o3[2][3]) : "v"(p3[1]) : "memory"); SB();
  asm volatile("ds_read_b128 %0, %1 offset:13312" : "=&v"(o3[3][3]) : "v"(p3[2]) : "memory"); SB();
  asm volatile("ds_read_b128 %0, %1 offset:4096" : "=&v"(o3[1][4]) : "v"(p3[0]) : "memory"); SB();
  asm volatile("ds_read_b128 %0, %1 offset:9216" : "=&v"(o3[2][4]) : "v"(p3[1]) : "memory"); SB();
  asm volatile("ds_read_b128 %0, %1 offset:14336" : "=&v"(o3[3][4]) : "v"(p3[2]) : "memory"); SB();
  ST(21);
  u32x2 yr[5];
  asm volatile("s_waitcnt lgkmcnt(12)" : "+v"(o3[0][0]), "+v"(o3[1][0]), "+v"(o3[2][0]), "+v"(o3[3][0]) :  : "memory"); SB();
#pragma unroll
  for (int j = 0; j <= 0; ++j) {
    const f32x4 o4 = (o3[0][j] + o3[1][j]) + (o3[2][j] + o3[3][j]);
#pragma unroll
    for (int h = 0; h < 2; ++h) {
      const f32x2 o = {o4[2 * h], o4[2 * h + 1]};
      const f32x2 a = o * (f32x2){-1.4426950f, -1.4426950f};
      const f32x2 e = (f32x2){__builtin_amdgcn_exp2f(a[0]), __builtin_amdgcn_exp2f(a[1])} + (f32x2){1.0f, 1.0f};
      const f32x2 r = {__builtin_amdgcn_rcpf(e[0]), __builtin_amdgcn_rcpf(e[1])};
      sv[j][h] = (sv[j][h] * o) * r;
    }
  }
  asm volatile("" : "+v"(sv[0][0]), "+v"(sv[0][1]));
  SB();
  asm volatile("s_waitcnt lgkmcnt(0)" : "+v"(o3[0][1]), "+v"(o3[1][1]), "+v"(o3[2][1]), "+v"(o3[3][1]) :  : "memory"); SB();
#pragma unroll
  for (int j = 1; j <= 1; ++j) {
    const f32x4 o4 = (o3[0][j] + o3[1][j]) + (o3[2][j] + o3[3][j]);
#pragma unroll
    for (int h = 0; h < 2; ++h) {
      const f32x2 o = {o4[2 * h], o4[2 * h + 1]};
      const f32x2 a = o * (f32x2){-1.4426950f, -1.4426950f};
      const f32x2 e = (f32x2){__builtin_amdgcn_exp2f(a[0]), __builtin_amdgcn_exp2f(a[1])} + (f32x2){1.0f, 1.0f};
      const f32x2 r = {__builtin_amdgcn_rcpf(e[0]), __builtin_amdgcn_rcpf(e[1])};
      sv[j][h] = (sv[j][h] * o) * r;
    }
  }
  asm volatile("" : "+v"(sv[1][0]), "+v"(sv[1][1]));
  SB();
  asm volatile("" : "+v"(o3[0][2]), "+v"(o3[1][2]), "+v"(o3[2][2]), "+v"(o3[3][2]) :  : "memory"); SB();
#pragma unroll
  for (int j = 2; j <= 2; ++j) {
    const f32x4 o4 = (o3[0][j] + o3[1][j]) + (o3[2][j] + o3[3][j]);
#pragma unroll
    for (int h = 0; h < 2; ++h) {
      const f32x2 o = {o4[2 * h], o4[2 * h + 1]};
      const f32x2 a = o * (f32x2){-1.4426950f, -1.4426950f};
      const f32x2 e = (f32x2){__builtin_amdgcn_exp2f(a[0]), __builtin_amdgcn_exp2f(a[1])} + (f32x2){1.0f, 1.0f};
      const f32x2 r = {__builtin_amdgcn_rcpf(e[0]), __builtin_amdgcn_rcpf(e[1])};
      sv[j][h] = (sv[j][h] * o) * r;
    }
  }
  asm volatile("" : "+v"(sv[2][0]), "+v"(sv[2][1]));
  SB();
  asm volatile("" : "+v"(o3[0][3]), "+v"(o3[1][3]), "+v"(o3[2][3]), "+v"(o3[3][3]) :  : "memory"); SB();
#pragma unroll
  for (int j = 3; j <= 3; ++j) {
    const f32x4 o4 = (o3[0][j] + o3[1][j]) + (o3[2][j] + o3[3][j]);
#pragma unroll
    for (int h = 0; h < 2; ++h) {
      const f32x2 o = {o4[2 * h], o4[2 * h + 1]};
      const f32x2 a = o * (f32x2){-1.4426950f, -1.4426950f};
      const f32x2 e = (f32x2){__builtin_amdgcn_exp2f(a[0]), __builtin_amdgcn_exp2f(a[1])} + (f32x2){1.0f, 1.0f};
      const f32x2 r = {__builtin_amdgcn_rcpf(e[0]), __builtin_amdgcn_rcpf(e[1])};
      sv[j][h] = (sv[j][h] * o) * r;
    }
  }
  asm volatile("" : "+v"(sv[3][0]), "+v"(sv[3][1]));
  SB();
  asm volatile("" : "+v"(o3[0][4]), "+v"(o3[1][4]), "+v"(o3[2][4]), "+v"(o3[3][4]) :  : "memory"); SB();
#pragma unroll
  for (int j = 4; j <= 4; ++j) {
    const f32x4 o4 = (o3[0][j] + o3[1][j]) + (o3[2][j] + o3[3][j]);
#pragma unroll
    for (int h = 0; h < 2; ++h) {
      const f32x2 o = {o4[2 * h], o4[2 * h + 1]};
      const f32x2 a = o * (f32x2){-1.4426950f, -1.4426950f};
      const f32x2 e = (f32x2){__builtin_amdgcn_exp2f(a[0]), __builtin_amdgcn_exp2f(a[1])} + (f32x2){1.0f, 1.0f};
      const f32x2 r = {__builtin_amdgcn_rcpf(e[0]), __builtin_amdgcn_rcpf(e[1])};
      sv[j][h] = (sv[j][h] * o) * r;
    }
  }
  asm volatile("" : "+v"(sv[4][0]), "+v"(sv[4][1]));
  SB();
  ST(22);
  asm volatile("s_waitcnt vmcnt(0)\n\t.Lc_%=:\n\tv_xnor_b32 v167, v167, %6\n\tv_xnor_b32 v171, v171, %7\n\tv_xnor_b32 v175, v175, %8\n\tv_max3_u32 %1, v167, v171, v175\n\tv_cmp_eq_u32 vcc, -1, %1\n\ts_cbranch_vccnz .Lr_%=\n\t.Ld_%=:\n\t.subsection 1\n\t.Lr_%=:\n\tv_add_u32 %0, -1, %0\n\ts_nop 1\n\tv_readfirstlane_b32 vcc_hi, %0\n\ts_nop 3\n\ts_cmp_lt_i32 vcc_hi, 1\n\ts_cbranch_scc1 .Ld_%=\n\ts_sleep 2\n\tbuffer_load_dwordx4 v[164:167], %2, %3, 0 offen sc1\n\tbuffer_load_dwordx4 v[168:171], %2, %3, %4 offen sc1\n\tbuffer_load_dwordx4 v[172:175], %2, %3, %5 offen sc1\n\ts_waitcnt vmcnt(0)\n\ts_branch .Lc_%=\n\t.subsection 0" : "+v"(tmo), "=&v"(vtmp) : "v"(vKV), "s"(rWS), "s"(w3k[0]), "s"(w3k[1]), "v"(kz[0]), "v"(kz[1]), "v"(kz[2]) : "memory", "vcc", "scc"); SB();
  ST(31);
  asm volatile("ds_write_b128 %0, v[164:167] offset:0" :  : "v"(yw) : "memory"); SB();
  asm volatile("ds_write_b128 %0, v[168:171] offset:1024" :  : "v"(yw) : "memory"); SB();
  asm volatile("ds_write_b128 %0, v[172:175] offset:2048" :  : "v"(yw) : "memory"); SB();
  asm volatile("s_waitcnt lgkmcnt(0)\n\ts_barrier" :  :  : "memory"); SB();
  asm volatile("ds_read_b64_tr_b16 %0, %1 offset:0" : "=&v"(yr[0]) : "v"(ya) : "memory"); SB();
  asm volatile("ds_read_b64_tr_b16 %0, %1 offset:2048" : "=&v"(yr[1]) : "v"(ya) : "memory"); SB();
  asm volatile("ds_read_b64_tr_b16 %0, %1 offset:4096" : "=&v"(yr[2]) : "v"(ya) : "memory"); SB();
  asm volatile("ds_read_b64_tr_b16 %0, %1 offset:6144" : "=&v"(yr[3]) : "v"(ya) : "memory"); SB();
  asm volatile("ds_read_b64_tr_b16 %0, %1 offset:8192" : "=&v"(yr[4]) : "v"(ya) : "memory"); SB();
  asm volatile("s_waitcnt lgkmcnt(0)" : "+v"(yr[0]), "+v"(yr[1]), "+v"(yr[2]), "+v"(yr[3]), "+v"(yr[4]) :  : "memory"); SB();
  {
    unsigned pk[5][2];
#pragma unroll
    for (int n = 0; n < 10; ++n) pk[n >> 1][n & 1] = pk2(__builtin_elementwise_fma(sv[n >> 1][n & 1], unpk(yr[n >> 1][n & 1]), xf[n >> 1][n & 1]));
#pragma unroll
    for (int q = 0; q < 2; ++q) {
      const auto s0 = __builtin_amdgcn_permlane16_swap(pk[2 * q][0], pk[2 * q + 1][0], false, false);
      const auto s1 = __builtin_amdgcn_permlane16_swap(pk[2 * q][1], pk[2 * q + 1][1], false, false);
      asm volatile("s_nop 1\n\tglobal_store_dwordx4 %0, %1, %2 offset:%3 " :: "v"(po), "v"((u32x4){s0[0], s1[0], s0[1], s1[1]}), "s"(out), "i"(64 * q) : "memory");
    }
    asm volatile("global_store_dwordx2 %0, %1, %2 offset:128 " :: "v"(vX), "v"((u32x2){pk[4][0], pk[4][1]}), "s"(out) : "memory");
  }
  ST(23);
  ST_OUT();
  }}}
}

extern "C" __global__ void k_end() { asm volatile(".subsection 2\n.Lcode_end:\n\ts_endpgm\n\t.subsection 0"); }

extern "C" void kernel_launch(void* const* d_in, const int* in_sizes, int n_in,
                              void* d_out, int out_size, void* d_ws, size_t ws_size,
                              hipStream_t stream) {
  (void)hipFuncSetAttribute((const void*)k_all, hipFuncAttributeMaxDynamicSharedMemorySize, 163840);
  k_all<<<256, 256, 163840, stream>>>((const char*)d_in[0], (const char*)d_in[1], (const char*)d_in[2], (const char*)d_in[3], (const char*)d_in[4],
                                      (const char*)d_in[5], (const char*)d_in[7], (const char*)d_in[8], (const char*)d_in[9], (const char*)d_in[10],
                                      (char*)d_out, (char*)d_ws);
}
```

```cpp
#include <hip/hip_runtime.h>
#include <stdint.h>

typedef unsigned short u16;
typedef __attribute__((ext_vector_type(4))) float f32x4;
typedef __attribute__((ext_vector_type(2))) float f32x2;
typedef __attribute__((ext_vector_type(4))) unsigned int u32x4;
typedef __attribute__((ext_vector_type(2))) unsigned int u32x2;
typedef __attribute__((ext_vector_type(2))) __bf16 bf16x2n;
typedef __attribute__((ext_vector_type(8))) unsigned long long u64x8;
typedef __attribute__((ext_vector_type(4))) unsigned long long u64x4;
#define LDS_AS __attribute__((address_space(3)))
#define GAS __attribute__((address_space(1)))
#define SB() __builtin_amdgcn_sched_barrier(0)
#define DI __device__ __forceinline__

#define SGA
#define ST_DECL do {} while (0)
#define ST(i) do {} while (0)
#define ST_OUT() do {} while (0)

constexpr int S_ = 2048, D_ = 640;
constexpr unsigned HOLE = 0xFFFFFFFFu;
constexpr int W_ST = 4096, W_FZ = 768 << 10, W_FK = W_FZ + 8192, W_FH = W_FK + 8192, W_KV = 1 << 20, W_Z2 = 4 << 20, W_HH = 11 << 20, W_WO = 14 << 20;

#define wm (w >> 1)
#define wn (w & 1)
#define g4 (lane >> 4)
#define l15 (lane & 15)
#define q4 ((lane >> 2) & 3)
#define p4 (lane & 3)
#define l16 (lane * 16)
#define nch (3 - (rb >> 4))
#define ch0 (cb * 80 + rb * 2 + min(rb, 16u))
#define c2o ((nch - 2) * 2)
#define c2s (8192u + ((rb >> 4) << 30))
#define w3s ((w + 1) >> 2 << 30)
#define vZ (W_Z2 + ((rb * 2 + wm) * 64 + cb * 6 + wn * 3) * 1024 + l16)
#define vH (W_HH + ((rb * 4 + w) * 24 + cb * 3) * 1024 + l16)

DI unsigned pack2(float lo, float hi) {
  f32x2 v = {lo, hi};
  return __builtin_bit_cast(unsigned, __builtin_convertvector(v, bf16x2n));
}
DI float lo16(unsigned w) { return __uint_as_float(w << 16); }
DI float hi16(unsigned w) { return __uint_as_float(w & 0xFFFF0000u); }
DI float rbf(float f) { return lo16(pack2(f, f)); }
DI f32x2 unpk(unsigned w) { f32x2 r = {lo16(w), hi16(w)}; return r; }
DI unsigned pk2(f32x2 v) { return pack2(v[0], v[1]); }
DI unsigned nohole(unsigned a) { return a < HOLE - 1 ? a : HOLE - 1; }
template <int CTRL> DI float dppf(float v) {
  return __builtin_bit_cast(float, __builtin_amdgcn_update_dpp(0, __builtin_bit_cast(int, v), CTRL, 0xF, 0xF, true));
}
DI float red16(float v) {
  const auto a = __builtin_amdgcn_permlane16_swap(__float_as_uint(v), __float_as_uint(v), false, false);
  return __uint_as_float(a[0]) + __uint_as_float(a[1]);
}
DI float red32(float v) {
  const auto c = __builtin_amdgcn_permlane32_swap(__float_as_uint(v), __float_as_uint(v), false, false);
  return __uint_as_float(c[0]) + __uint_as_float(c[1]);
}
DI float sel32(float a, float b) {
  const auto c = __builtin_amdgcn_permlane32_swap(__float_as_uint(a), __float_as_uint(b), false, false);
  return __uint_as_float(c[0]) + __uint_as_float(c[1]);
}
DI unsigned lds_addr(const void* p) { return (unsigned)(uintptr_t)(LDS_AS const char*)p; }

__global__ __launch_bounds__(256, 1) __attribute__((amdgpu_num_vgpr(160))) SGA void k_all(
    const char*, const char*, const char*, const char*, const char*, const char*, const char*, const char*, const char*, const char*, char*, char*) {
  extern __shared__ __attribute__((aligned(16))) char smem[];
  ST_DECL;
  ST(0);
  u64x8 ka0; u64x4 ka1;
  asm volatile("s_load_dwordx16 %0, %2, 0x0\n\ts_load_dwordx8 %1, %2, 0x40" : "=&s"(ka0), "=&s"(ka1) : "s"(__builtin_amdgcn_kernarg_segment_ptr()) : "memory"); SB();
  const unsigned tid = threadIdx.x, lane0 = tid & 63, w0 = __builtin_amdgcn_readfirstlane(tid >> 6);
  const unsigned b = blockIdx.x, xcd = b & 7, idx = b >> 3;
  const unsigned rb0 = 8 * (xcd >> 1) + (idx >> 2), cb0 = 4 * (xcd & 1) + (idx & 3);
  const unsigned lb = lds_addr(smem);
  const unsigned lane = lane0, w = w0, rb = rb0, cb = cb0;
  {
    unsigned long long pc; unsigned clen;
    asm volatile("s_getpc_b64 %0\n.Lpc_here_%=:\n\ts_mov_b32 %1, .Lcode_end-.Lpc_here_%=" : "=s"(pc), "=s"(clen));
    const unsigned o = min((((lane & 3) * 4 + w) * 32 + idx) * 128, clen - 4u) & ~3u;
    asm volatile("s_mov_b64 exec, 15\n\tglobal_load_dword v254, %0, %1\n\ts_mov_b64 exec, -1" :: "v"(o), "s"(pc) : "memory");
    SB();
  }
  unsigned voA = (rb * 64 + w * 16 + (lane >> 3)) * 1280 + (((lane & 7) ^ (lane >> 3)) << 4), voB[3];
#pragma unroll
  for (int i = 0; i < 3; ++i) { const unsigned o = i * 64 + lane, r = (o * 171) >> 11, c = o - r * 12; voB[i] = (w * 16 + r) * 3072 + cb * 192 + ((c ^ ((r >> 3) << 1)) << 4) - i * 1024; }
  unsigned mX = lb + w * 2048, mW = lb + w * 3072, mA = lb + w * 8192;
  const unsigned rot = ((rb ^ cb) & 3);
  unsigned kx[10], kw_[10], voA2 = voA + 9216;
  auto kofs = [&](int s) {
    asm volatile("s_xor_b32 %0, %2, %3\n\ts_mul_i32 %1, %0, 0x30000\n\ts_lshl_b32 %0, %0, 7" : "=&s"(kx[s]), "=&s"(kw_[s]) : "s"(s < 8 ? rot : rot & 1), "i"(s) : "scc");
  };
  kofs(0); kofs(1); kofs(2);
  asm volatile("" : "+v"(voA), "+v"(voA2), "+v"(voB[0]), "+v"(voB[1]), "+v"(voB[2]));
  asm volatile("" : "+s"(mX), "+s"(mW), "+s"(mA)); SB();
  asm volatile("s_waitcnt lgkmcnt(0)" : "+s"(ka0), "+s"(ka1) :: "memory"); SB();
  const char *x = (const char*)ka0[0], *kin = (const char*)ka0[1], *vin = (const char*)ka0[2], *shift_k = (const char*)ka0[3], *ssm_k = (const char*)ka0[4];
  const char *ssm_D = (const char*)ka0[5], *w_in = (const char*)ka0[6], *w_sp = (const char*)ka0[7], *b_sp = (const char*)ka1[0], *w_out = (const char*)ka1[1];
  char *out = (char*)ka1[2], *ws = (char*)ka1[3];
  const auto rX = __builtin_amdgcn_make_buffer_rsrc((void*)x, 0, S_ * D_ * 2, 0x00020000);
  const auto rW = __builtin_amdgcn_make_buffer_rsrc((void*)(w_in + 1536), 0, 640 * 3072 - 1536, 0x00020000);
  asm volatile("; named registers" :  :  : "v255", "a255"); SB();
  asm volatile("s_add_u32 m0, %0, 81920" :  : "s"(mX) : "memory", "scc"); SB();
  asm volatile("s_nop 0\n\tbuffer_load_dwordx4 %0, %1, %2 offen lds" :  : "v"(voA), "s"(rX), "s"(kx[0]) : "memory"); SB();
  asm volatile("buffer_load_dwordx4 %0, %1, %2 offen offset:1024 lds" :  : "v"(voA2), "s"(rX), "s"(kx[0]) : "memory"); SB();
  asm volatile("s_add_u32 m0, %0, 0" :  : "s"(mW) : "memory", "scc"); SB();
  asm volatile("s_nop 0\n\tbuffer_load_dwordx4 %0, %1, %2 offen lds" :  : "v"(voB[0]), "s"(rW), "s"(kw_[0]) : "memory"); SB();
  asm volatile("buffer_load_dwordx4 %0, %1, %2 offen offset:1024 lds" :  : "v"(voB[1]), "s"(rW), "s"(kw_[0]) : "memory"); SB();
  asm volatile("buffer_load_dwordx4 %0, %1, %2 offen offset:2048 lds" :  : "v"(voB[2]), "s"(rW), "s"(kw_[0]) : "memory"); SB();
  asm volatile("s_add_u32 m0, %0, 90112" :  : "s"(mX) : "memory", "scc"); SB();
  asm volatile("s_nop 0\n\tbuffer_load_dwordx4 %0, %1, %2 offen lds" :  : "v"(voA), "s"(rX), "s"(kx[1]) : "memory"); SB();
  asm volatile("buffer_load_dwordx4 %0, %1, %2 offen offset:1024 lds" :  : "v"(voA2), "s"(rX), "s"(kx[1]) : "memory"); SB();
  kofs(3); kofs(4); kofs(5); kofs(6); kofs(7); kofs(8); kofs(9);
  const auto rWS = __builtin_amdgcn_make_buffer_rsrc((void*)ws, 0, 32 << 20, 0x00020000);
  const auto rK = __builtin_amdgcn_make_buffer_rsrc((void*)kin, 0, S_ * D_ * 2, 0x00020000);
  const auto rV = __builtin_amdgcn_make_buffer_rsrc((void*)vin, 0, S_ * D_ * 2, 0x00020000);
  const auto rS = __builtin_amdgcn_make_buffer_rsrc((void*)(shift_k - 3328), 0, D_ * 8 + 3328, 0x00020000);
  const auto rO = __builtin_amdgcn_make_buffer_rsrc((void*)(w_out - 3072), 0, 768 * 1280 + 3072, 0x00020000);
  const auto rW1 = __builtin_amdgcn_make_buffer_rsrc((void*)w_in, 0, 640 * 3072, 0x00020000);
  { unsigned lane = lane0, w = w0, rb = rb0, cb = cb0;
  asm volatile("" : "+v"(lane)); asm volatile("" : "+s"(w), "+s"(rb), "+s"(cb)); SB();

  const auto rA = __builtin_amdgcn_make_buffer_rsrc((void*)(w_sp - 3072), 0, S_ * S_ * 2 + 3072, 0x00020000);
  unsigned ao[8];
  unsigned voK[4], voV[3], voH = cb * 640 + l16;
#pragma unroll
  for (int n = 0; n < 4; ++n) { const unsigned o = n * 64 + lane, r = (o * 205) >> 11; voK[n] = (rb * 64 + w * 16 - 4) * 1280 + cb * 160 + r * 1120 + lane * 16; }
#pragma unroll
  for (int n = 0; n < 3; ++n) voV[n] = voK[n] + 5120;
  const unsigned wof = (rb >> 3) * 30 + min((rb & 7) * 4 + w, 29u), wok = (wof * 205) >> 10;
  unsigned voO = (lane >> 1) * 1280 + (lane & 1) * 16 + wok * 40960 + (wof - 5 * wok) * 32 + cb * 160;
  unsigned ka = lb + 98304 + w * 8192 + (4 * p4 + q4) * 160 + 8 * g4, oa = lb + 98304 + 7168 + w * 8192 + (4 * g4 + q4) * 32 + 8 * p4;
  unsigned sa = lb + 98304 + 3328 + w * 8192 + (4 * g4 + p4) * 8;
  unsigned kw = lb + 12288 + (4 * g4 + p4) * 144 + w * 32 + q4 * 8, kr = lb + 12288 + (w * 24 + (lane >> 3)) * 144 + (lane & 7) * 16;
  unsigned vKV = W_KV + (cb * 80 + w * 24 + (lane >> 3)) * 4096 + rb * 128 + (lane & 7) * 16, vWO = W_WO + (cb * 120 + wof) * 1024 + l16;
  asm volatile("" : "+v"(ka), "+v"(oa), "+v"(sa), "+v"(kw), "+v"(kr), "+v"(vKV), "+v"(vWO), "+v"(voK[0]), "+v"(voK[1]), "+v"(voK[2]), "+v"(voK[3]),
               "+v"(voV[0]), "+v"(voV[1]), "+v"(voV[2]), "+v"(voH), "+v"(voO));

  asm volatile("s_add_u32 m0, %0, 12288" :  : "s"(mW) : "memory", "scc"); SB();
  asm volatile("s_nop 0\n\tbuffer_load_dwordx4 %0, %1, %2 offen lds" :  : "v"(voB[0]), "s"(rW), "s"(kw_[1]) : "memory"); SB();
  asm volatile("buffer_load_dwordx4 %0, %1, %2 offen offset:1024 lds" :  : "v"(voB[1]), "s"(rW), "s"(kw_[1]) : "memory"); SB();
  asm volatile("buffer_load_dwordx4 %0, %1, %2 offen offset:2048 lds" :  : "v"(voB[2]), "s"(rW), "s"(kw_[1]) : "memory"); SB();
  u32x2 kp[5], kc[5], vv[5], sw[5], wt[2], ko[5];
  u32x4 kq[3];
  f32x2 kt[2], kf[2];
  unsigned ku[5];
  unsigned aX[2][2], aW[2][2][3];
#pragma unroll
  for (int n = 0; n < 4; ++n) aX[n >> 1][n & 1] = lb + 98304 + (wm * 32 + ((n & 1) ^ wn) * 16 + l15) * 128 + (((((n >> 1) ^ wm) * 4 + g4) ^ (l15 & 7)) << 4);
#pragma unroll
  for (int n = 0; n < 6; ++n) {
    aW[0][n / 3][n % 3] = lb + ((n / 3 ^ wm) * 32 + 8 * g4 + q4) * 192 + (((wn * 3 + n % 3) ^ (g4 & 1)) << 5) + 8 * p4;
    aW[1][n / 3][n % 3] = aW[0][n / 3][n % 3] + 49152;
    asm volatile("" : "+v"(aW[0][n / 3][n % 3]), "+v"(aW[1][n / 3][n % 3]));
  }
  unsigned ca = lb + 98304 + 6912 + w * 8192 + g4 * 16, cr = lb + 98304 + 6912 + (w ^ 2) * 8192 + g4 * 16;
  f32x4 co[3];
  asm volatile("" : "+v"(ca), "+v"(cr));

  SB();
  u32x4 ones = {0x3F803F80u, 0x3F803F80u, 0x3F803F80u, 0x3F803F80u};
  f32x4 a2[2][3], a1[2][3], rs, gr, c2[3], c1[3];
  unsigned voS = (rb * 64 + (lane >> 3)) * 4096 + w * 1024 + (cb & 1) * 128 + (((lane & 7) ^ (lane >> 3)) << 4), aA[2];
#pragma unroll
  for (int k = 0; k < 2; ++k) aA[k] = lb + 98304 + w * 8192 + l15 * 128 + (((k * 4 + g4) ^ (l15 & 7)) << 4);
  unsigned c2s_ = c2s, xw = lb + 98304 + 6656 + w * 8192 + l15 * 16, xr_ = lb + 98304 + 6656 + (w ^ 1) * 8192 + l15 * 16;
  u32x2 zt[3][2];
  unsigned zwa[2] = {lb + 98304 + 4096 + w * 8192 + (wn * 16 + l15) * 104 + g4 * 8, lb + 98304 + 4096 + w * 8192 + ((wn ^ 1) * 16 + l15) * 104 + g4 * 8};
  unsigned zra = lb + 98304 + 4096 + w * 8192 + (8 * g4 + q4) * 104 + 8 * p4;
  asm volatile("" : "+v"(zwa[0]), "+v"(zwa[1]), "+v"(zra));
  unsigned vo = W_ST + ((rb * 2 + wm) * 8 + cb) * 1024 + wn * 128 + ((lane >> 5) ^ wn) * 512 + (l15 >> 3) * 256 + (l15 & 7) * 16;
  asm volatile("" : "+v"(ones),
               "+v"(voS), "+v"(aA[0]), "+v"(aA[1]), "+s"(c2s_), "+v"(xw), "+v"(xr_), "+v"(vo));
  asm volatile("" : "+v"(aX[0][0]), "+v"(aX[0][1]), "+v"(aX[1][0]), "+v"(aX[1][1]));

  SB();
  int tmo = (1 << 18);
  u32x2 fk, fz, fh, fi, fy;
  unsigned tg0, tg1;
  float xm[2], xd[2], k1[2], k2[2];
  f32x4 xs[2];
  float xt[4];
  const auto rF = __builtin_amdgcn_make_buffer_rsrc((void*)ssm_k, 0, S_ * D_ * 2, 0x00020000);
  unsigned vF = ch0 * 4096 + w * 1024 + l16;
  asm volatile("" : "+v"(vF));
  unsigned hpo[3], vD = ch0 * 2, vD2 = vD + c2o * 2 - 4, vBs = (rb * 64 + w * 16 + l15) * 2;
  hpo[0] = w ? ch0 * 4096 + w * 1024 - 4 : 0x40000000u; hpo[1] = hpo[0] + 4096; hpo[2] = hpo[0] + c2s;
  asm volatile("" : "+s"(hpo[0]), "+s"(hpo[1]), "+s"(hpo[2]), "+v"(vD), "+v"(vD2), "+v"(vBs));
  unsigned vS = W_ST + (rb * 2 + wm) * 8192 + l16, vFZ = W_FZ + cb * 1024 + w * 256 + (lane & 31) * 8, vZ_ = vZ;
  unsigned vB = W_Z2 + (w * 1024 + (cb & 1) * 128 + cb * 6) * 1024 + l16;
  asm volatile("" : "+v"(vS), "+v"(vFZ), "+v"(vZ_), "+v"(vB));
  SB();
  unsigned ha = lb + 59520 + 4608 + (2040 - 8 * (w * 64 + lane)) * 2;
  unsigned za = lb + 59520 + min(w, 2u) * 12928 + (lane < 32 ? lane * 16 : 4608 + ((lane >> 1) & 1) * 4160 + 4096 + (lane & 1) * 16);
  unsigned ew[2] = {lb + 32768 + w * 6144 + wn * 3072 + l16, lb + 32768 + (w ^ 1) * 6144 + wn * 3072 + l16};
  unsigned aa[4], bb;
  {
    const unsigned c = (l15 + 1) & 1;
    const unsigned o = lb + 59520 + 2 * 12928 - w * 128 - 192;
#pragma unroll
    for (int k = 0; k < 4; ++k) aa[k] = o + 4608 + c * 4160 + (2031 - l15 + 8 * g4 - c) * 2 - 512 - 1024 * k;
    bb = o + 512 + 32 * l15 - 32 + 16 * g4;
  }
  unsigned dd = lb + 0 + w * 8192 + g4 * 256 + ((l15 ^ ((g4 >> 1) << 3)) << 4);
  asm volatile("" : "+v"(aa[0]), "+v"(aa[1]), "+v"(aa[2]), "+v"(aa[3]), "+v"(bb), "+v"(dd), "+v"(vS), "+v"(vFZ), "+v"(vZ_), "+v"(vB), "+v"(ha), "+v"(za), "+v"(ew[0]), "+v"(ew[1]));
  unsigned mU, uso, w3k[2], sFK, sFZ;
  float dD[3], bs;
  u32x4 im[6], zero4;
  ST(1);
  SB();
  asm volatile("s_add_u32 m0, %0, 98304" :  : "s"(mX) : "memory", "scc"); SB();
  asm volatile("s_nop 0\n\tbuffer_load_dwordx4 %0, %1, %2 offen lds" :  : "v"(voA), "s"(rX), "s"(kx[2]) : "memory"); SB();
  asm volatile("buffer_load_dwordx4 %0, %1, %2 offen offset:1024 lds" :  : "v"(voA2), "s"(rX), "s"(kx[2]) : "memory"); SB();
  asm volatile("buffer_load_dwordx2 v[252:253], off, %0, 0 sc1" :  : "s"(rWS) : "memory"); SB();
  asm volatile("s_add_u32 m0, %0, 106496" :  : "s"(mX) : "memory", "scc"); SB();
  asm volatile("s_nop 0\n\tbuffer_load_dwordx4 %0, %1, %2 offen lds" :  : "v"(voA), "s"(rX), "s"(kx[3]) : "memory"); SB();
  asm volatile("buffer_load_dwordx4 %0, %1, %2 offen offset:1024 lds" :  : "v"(voA2), "s"(rX), "s"(kx[3]) : "memory"); SB();
  asm volatile("s_add_u32 m0, %0, 24576" :  : "s"(mW) : "memory", "scc"); SB();
  asm volatile("s_nop 0\n\tbuffer_load_dwordx4 %0, %1, %2 offen lds" :  : "v"(voB[0]), "s"(rW), "s"(kw_[2]) : "memory"); SB();
  asm volatile("s_waitcnt vmcnt(11) lgkmcnt(0)\n\ts_barrier" :  :  : "memory"); SB();
  ST(4);
  asm volatile("ds_read_b128 a[0:3], %0 offset:0" :  : "v"((aX[0][0] - 16384)) : "memory"); SB();
  asm volatile("ds_read_b128 a[4:7], %0 offset:0" :  : "v"((aX[0][1] - 16384)) : "memory"); SB();
  asm volatile("ds_read_b64_tr_b16 v[160:161], %0 offset:0" :  : "v"(aW[0][0][0]) : "memory"); SB();
  asm volatile("ds_read_b64_tr_b16 v[162:163], %0 offset:768" :  : "v"(aW[0][0][0]) : "memory"); SB();
  asm volatile("ds_read_b64_tr_b16 v[164:165], %0 offset:0" :  : "v"(aW[0][0][1]) : "memory"); SB();
  asm volatile("ds_read_b64_tr_b16 v[166:167], %0 offset:768" :  : "v"(aW[0][0][1]) : "memory"); SB();
  asm volatile("ds_read_b64_tr_b16 v[168:169], %0 offset:0" :  : "v"(aW[0][0][2]) : "memory"); SB();
  asm volatile("ds_read_b64_tr_b16 v[170:171], %0 offset:768" :  : "v"(aW[0][0][2]) : "memory"); SB();
  asm volatile("ds_read_b128 a[8:11], %0 offset:0" :  : "v"((aX[1][0] - 16384)) : "memory"); SB();
  asm volatile("ds_read_b128 a[12:15], %0 offset:0" :  : "v"((aX[1][1] - 16384)) : "memory"); SB();
  asm volatile("s_waitcnt lgkmcnt(6)\n\tv_mfma_f32_16x16x32_bf16 %0, v[160:163], a[0:3], 0" : "=&v"(a2[0][0]) :  : ); SB();
  asm volatile("ds_read_b64_tr_b16 v[172:173], %0 offset:0" :  : "v"(aW[0][1][0]) : "memory"); SB();
  asm volatile("buffer_load_dword v242, off, %0, %1" :  : "s"(rF), "s"(hpo[0]) : "memory"); SB();
  asm volatile("v_mfma_f32_16x16x32_bf16 %0, v[160:163], a[4:7], 0" : "=&v"(a2[1][0]) :  : ); SB();
  asm volatile("ds_read_b64_tr_b16 v[174:175], %0 offset:768" :  : "v"(aW[0][1][0]) : "memory"); SB();
  asm volatile("buffer_load_dwordx4 %0, %1, %2 offen offset:1024 lds" :  : "v"(voB[1]), "s"(rW), "s"(kw_[2]) : "memory"); SB();
  asm volatile("v_mfma_f32_16x16x32_bf16 %0, v[160:163], %1, 0" : "=&v"(c2[0]) : "v"(ones) : ); SB();
  asm volatile("ds_read_b64_tr_b16 v[176:177], %0 offset:0" :  : "v"(aW[0][1][1]) : "memory"); SB();
  asm volatile("s_waitcnt lgkmcnt(5)\n\tv_mfma_f32_16x16x32_bf16 %0, v[164:167], a[0:3], 0" : "=&v"(a2[0][1]) :  : ); SB();
  asm volatile("ds_read_b64_tr_b16 v[178:179], %0 offset:768" :  : "v"(aW[0][1][1]) : "memory"); SB();
  asm volatile("v_mfma_f32_16x16x32_bf16 %0, v[164:167], a[4:7], 0" : "=&v"(a2[1][1]) :  : ); SB();
  asm volatile("ds_read_b64_tr_b16 v[180:181], %0 offset:0" :  : "v"(aW[0][1][2]) : "memory"); SB();
  asm volatile("buffer_load_dwordx4 %0, %1, %2 offen offset:2048 lds" :  : "v"(voB[2]), "s"(rW), "s"(kw_[2]) : "memory"); SB();
  asm volatile("v_mfma_f32_16x16x32_bf16 %0, v[164:167], %1, 0" : "=&v"(c2[1]) : "v"(ones) : ); SB();
  asm volatile("ds_read_b64_tr_b16 v[182:183], %0 offset:768" :  : "v"(aW[0][1][2]) : "memory"); SB();
  asm volatile("v_mfma_f32_16x16x32_bf16 %0, v[168:171], a[0:3], 0" : "=&v"(a2[0][2]) :  : ); SB();
  asm volatile("s_waitcnt vmcnt(9)\n\ts_barrier" :  :  : "memory"); SB();
  asm volatile("ds_read_b64_tr_b16 v[184:185], %0 offset:12288" :  : "v"(aW[0][0][0]) : "memory"); SB();
  asm volatile("global_load_ushort v245, %0, %1 offset:0" :  : "v"(vD), "s"(ssm_D) : "memory"); SB();
  asm volatile("v_mfma_f32_16x16x32_bf16 %0, v[168:171], a[4:7], 0" : "=&v"(a2[1][2]) :  : ); SB();
  asm volatile("ds_read_b64_tr_b16 v[186:187], %0 offset:13056" :  : "v"(aW[0][0][0]) : "memory"); SB();
  asm volatile("s_add_u32 m0, %0, 114688" :  : "s"(mX) : "memory", "scc"); SB();
  asm volatile("v_mfma_f32_16x16x32_bf16 %0, v[168:171], %1, 0" : "=&v"(c2[2]) : "v"(ones) : ); SB();
  asm volatile("ds_read_b64_tr_b16 v[188:189], %0 offset:12288" :  : "v"(aW[0][0][1]) : "memory"); SB();
  asm volatile("buffer_load_dwordx4 %0, %1, %2 offen lds" :  : "v"(voA), "s"(rX), "s"(kx[4]) : "memory"); SB();
  asm volatile("v_mfma_f32_16x16x32_bf16 %0, %1, a[0:3], 0" : "=&v"(rs) : "v"(ones) : ); SB();
  asm volatile("ds_read_b128 a[16:19], %0 offset:8192" :  : "v"((aX[0][0] - 16384)) : "memory"); SB();
  asm volatile("ds_read_b128 a[20:23], %0 offset:8192" :  : "v"((aX[0][1] - 16384)) : "memory"); SB();
  asm volatile("ds_read_b64_tr_b16 v[190:191], %0 offset:13056" :  : "v"(aW[0][0][1]) : "memory"); SB();
  asm volatile("buffer_load_dwordx4 %0, %1, %2 offen offset:1024 lds" :  : "v"(voA2), "s"(rX), "s"(kx[4]) : "memory"); SB();
  asm volatile("v_mfma_f32_16x16x32_bf16 %0, a[0:3], a[0:3], 0" : "=&v"(gr) :  : ); SB();
  asm volatile("ds_read_b64_tr_b16 v[192:193], %0 offset:12288" :  : "v"(aW[0][0][2]) : "memory"); SB();
  asm volatile("s_waitcnt lgkmcnt(7)\n\tv_mfma_f32_16x16x32_bf16 %0, v[172:175], a[8:11], %0" : "+v"(a2[0][0]) :  : ); SB();
  asm volatile("ds_read_b64_tr_b16 v[194:195], %0 offset:13056" :  : "v"(aW[0][0][2]) : "memory"); SB();
  asm volatile("v_mfma_f32_16x16x32_bf16 %0, v[172:175], a[12:15], %0" : "+v"(a2[1][0]) :  : ); SB();
  asm volatile("ds_read_b128 a[24:27], %0 offset:8192" :  : "v"((aX[1][0] - 16384)) : "memory"); SB();
  asm volatile("s_add_u32 m0, %0, 122880" :  : "s"(mX) : "memory", "scc"); SB();
  asm volatile("v_mfma_f32_16x16x32_bf16 %0, v[176:179], a[8:11], %0" : "+v"(a2[0][1]) :  : ); SB();
  asm volatile("ds_read_b128 a[28:31], %0 offset:8192" :  : "v"((aX[1][1] - 16384)) : "memory"); SB();
  asm volatile("v_mfma_f32_16x16x32_bf16 %0, v[176:179], a[12:15], %0" : "+v"(a2[1][1]) :  : ); SB();
  asm volatile("buffer_load_dwordx4 %0, %1, %2 offen lds" :  : "v"(voA), "s"(rX), "s"(kx[5]) : "memory"); SB();
  asm volatile("v_mfma_f32_16x16x32_bf16 %0, v[180:183], a[8:11], %0" : "+v"(a2[0][2]) :  : ); SB();
  asm volatile("ds_read_b64_tr_b16 v[160:161], %0 offset:12288" :  : "v"(aW[0][1][0]) : "memory"); SB();
  asm volatile("v_mfma_f32_16x16x32_bf16 %0, v[180:183], a[12:15], %0" : "+v"(a2[1][2]) :  : ); SB();
  asm volatile("ds_read_b64_tr_b16 v[162:163], %0 offset:13056" :  : "v"(aW[0][1][0]) : "memory"); SB();
  asm volatile("v_mfma_f32_16x16x32_bf16 %0, %1, a[8:11], %0" : "+v"(rs) : "v"(ones) : ); SB();
  asm volatile("ds_read_b64_tr_b16 v[164:165], %0 offset:12288" :  : "v"(aW[0][1][1]) : "memory"); SB();
  asm volatile("buffer_load_dword v243, off, %0, %1" :  : "s"(rF), "s"(hpo[1]) : "memory"); SB();
  asm volatile("v_mfma_f32_16x16x32_bf16 %0, a[8:11], a[8:11], %0" : "+v"(gr) :  : ); SB();
  asm volatile("ds_read_b64_tr_b16 v[166:167], %0 offset:13056" :  : "v"(aW[0][1][1]) : "memory"); SB();
  asm volatile("s_waitcnt lgkmcnt(8)\n\tv_mfma_f32_16x16x32_bf16 %0, v[184:187], a[16:19], %0" : "+v"(a2[0][0]) :  : ); SB();
  asm volatile("ds_read_b64_tr_b16 v[168:169], %0 offset:12288" :  : "v"(aW[0][1][2]) : "memory"); SB();
  asm volatile("buffer_load_dwordx4 %0, %1, %2 offen offset:1024 lds" :  : "v"(voA2), "s"(rX), "s"(kx[5]) : "memory"); SB();
  asm volatile("v_mfma_f32_16x16x32_bf16 %0, v[184:187], a[20:23], %0" : "+v"(a2[1][0]) :  : ); SB();
  asm volatile("ds_read_b64_tr_b16 v[170:171], %0 offset:13056" :  : "v"(aW[0][1][2]) : "memory"); SB();
  asm volatile("v_mfma_f32_16x16x32_bf16 %0, v[184:187], %1, %0" : "+v"(c2[0]) : "v"(ones) : ); SB();
  asm volatile("v_mfma_f32_16x16x32_bf16 %0, v[188:191], a[16:19], %0" : "+v"(a2[0][1]) :  : ); SB();
  asm volatile("v_mfma_f32_16x16x32_bf16 %0, v[188:191], a[20:23], %0" : "+v"(a2[1][1]) :  : ); SB();
  asm volatile("s_add_u32 m0, %0, 36864" :  : "s"(mW) : "memory", "scc"); SB();
  asm volatile("s_nop 0\n\tbuffer_load_dwordx4 %0, %1, %2 offen lds" :  : "v"(voB[0]), "s"(rW), "s"(kw_[3]) : "memory"); SB();
  asm volatile("buffer_load_dwordx4 %0, %1, %2 offen offset:1024 lds" :  : "v"(voB[1]), "s"(rW), "s"(kw_[3]) : "memory"); SB();
  asm volatile("buffer_load_dwordx4 %0, %1, %2 offen offset:2048 lds" :  : "v"(voB[2]), "s"(rW), "s"(kw_[3]) : "memory"); SB();
  asm volatile("global_load_ushort v246, %0, %1 offset:2" :  : "v"(vD), "s"(ssm_D) : "memory"); SB();
  asm volatile("buffer_load_dword v244, off, %0, %1" :  : "s"(rF), "s"(hpo[2]) : "memory"); SB();
  asm volatile("s_add_u32 m0, %0, 131072" :  : "s"(mX) : "memory", "scc"); SB();
  asm volatile("s_nop 0\n\tbuffer_load_dwordx4 %0, %1, %2 offen lds" :  : "v"(voA), "s"(rX), "s"(kx[6]) : "memory"); SB();
  asm volatile("buffer_load_dwordx4 %0, %1, %2 offen offset:1024 lds" :  : "v"(voA2), "s"(rX), "s"(kx[6]) : "memory"); SB();
  asm volatile("s_add_u32 m0, %0, 139264" :  : "s"(mX) : "memory", "scc"); SB();
  asm volatile("s_nop 0\n\tbuffer_load_dwordx4 %0, %1, %2 offen lds" :  : "v"(voA), "s"(rX), "s"(kx[7]) : "memory"); SB();
  asm volatile("global_load_ushort v247, %0, %1 offset:4" :  : "v"(vD2), "s"(ssm_D) : "memory"); SB();
  asm volatile("global_load_ushort v248, %0, %1" :  : "v"(vBs), "s"(b_sp) : "memory"); SB();
  asm volatile("s_waitcnt vmcnt(16)\n\ts_barrier" :  :  : "memory"); SB();
  asm volatile("ds_read_b64_tr_b16 v[172:173], %0 offset:24576" :  : "v"(aW[0][0][0]) : "memory"); SB();
  asm volatile("v_mfma_f32_16x16x32_bf16 %0, v[188:191], %1, %0" : "+v"(c2[1]) : "v"(ones) : ); SB();
  asm volatile("ds_read_b64_tr_b16 v[174:175], %0 offset:25344" :  : "v"(aW[0][0][0]) : "memory"); SB();
  asm volatile("s_waitcnt lgkmcnt(2)\n\tv_mfma_f32_16x16x32_bf16 %0, v[192:195], a[16:19], %0" : "+v"(a2[0][2]) :  : ); SB();
  asm volatile("ds_read_b64_tr_b16 v[176:177], %0 offset:24576" :  : "v"(aW[0][0][1]) : "memory"); SB();
  asm volatile("v_mfma_f32_16x16x32_bf16 %0, v[192:195], a[20:23], %0" : "+v"(a2[1][2]) :  : ); SB();
  asm volatile("ds_read_b64_tr_b16 v[178:179], %0 offset:25344" :  : "v"(aW[0][0][1]) : "memory"); SB();
  asm volatile("v_mfma_f32_16x16x32_bf16 %0, v[192:195], %1, %0" : "+v"(c2[2]) : "v"(ones) : ); SB();
  asm volatile("ds_read_b64_tr_b16 v[180:181], %0 offset:24576" :  : "v"(aW[0][0][2]) : "memory"); SB();
  asm volatile("v_mfma_f32_16x16x32_bf16 %0, %1, a[16:19], %0" : "+v"(rs) : "v"(ones) : ); SB();
  asm volatile("ds_read_b128 a[32:35], %0 offset:0" :  : "v"(aX[0][0]) : "memory"); SB();
  asm volatile("ds_read_b128 a[36:39], %0 offset:0" :  : "v"(aX[0][1]) : "memory"); SB();
  asm volatile("ds_read_b64_tr_b16 v[182:183], %0 offset:25344" :  : "v"(aW[0][0][2]) : "memory"); SB();
  asm volatile("v_mfma_f32_16x16x32_bf16 %0, a[16:19], a[16:19], %0" : "+v"(gr) :  : ); SB();
  asm volatile("ds_read_b128 a[40:43], %0 offset:0" :  : "v"(aX[1][0]) : "memory"); SB();
  asm volatile("v_mfma_f32_16x16x32_bf16 %0, v[160:163], a[24:27], %0" : "+v"(a2[0][0]) :  : ); SB();
  asm volatile("ds_read_b128 a[44:47], %0 offset:0" :  : "v"(aX[1][1]) : "memory"); SB();
  asm volatile("v_mfma_f32_16x16x32_bf16 %0, v[160:163], a[28:31], %0" : "+v"(a2[1][0]) :  : ); SB();
  asm volatile("buffer_load_dwordx4 %0, %1, %2 offen offset:1024 lds" :  : "v"(voA2), "s"(rX), "s"(kx[7]) : "memory"); SB();
  asm volatile("s_add_u32 m0, %0, 49152" :  : "s"(mW) : "memory", "scc"); SB();
  asm volatile("v_mfma_f32_16x16x32_bf16 %0, v[164:167], a[24:27], %0" : "+v"(a2[0][1]) :  : ); SB();
  asm volatile("ds_read_b128 a[48:51], %0 offset:8192" :  : "v"(aX[0][0]) : "memory"); SB();
  asm volatile("v_mfma_f32_16x16x32_bf16 %0, v[164:167], a[28:31], %0" : "+v"(a2[1][1]) :  : ); SB();
  asm volatile("ds_read_b128 a[52:55], %0 offset:8192" :  : "v"(aX[0][1]) : "memory"); SB();
  asm volatile("v_mfma_f32_16x16x32_bf16 %0, v[168:171], a[24:27], %0" : "+v"(a2[0][2]) :  : ); SB();
  asm volatile("buffer_load_dwordx4 %0, %1, %2 offen lds" :  : "v"(voB[0]), "s"(rW), "s"(kw_[4]) : "memory"); SB();
  asm volatile("v_mfma_f32_16x16x32_bf16 %0, v[168:171], a[28:31], %0" : "+v"(a2[1][2]) :  : ); SB();
  asm volatile("ds_read_b128 a[56:59], %0 offset:8192" :  : "v"(aX[1][0]) : "memory"); SB();
  asm volatile("v_mfma_f32_16x16x32_bf16 %0, %1, a[24:27], %0" : "+v"(rs) : "v"(ones) : ); SB();
  asm volatile("ds_read_b64_tr_b16 v[184:185], %0 offset:24576" :  : "v"(aW[0][1][0]) : "memory"); SB();
  asm volatile("v_mfma_f32_16x16x32_bf16 %0, a[24:27], a[24:27], %0" : "+v"(gr) :  : ); SB();
  asm volatile("ds_read_b64_tr_b16 v[186:187], %0 offset:25344" :  : "v"(aW[0][1][0]) : "memory"); SB();
  asm volatile("s_waitcnt lgkmcnt(8)\n\tv_mfma_f32_16x16x32_bf16 %0, v[172:175], a[32:35], %0" : "+v"(a2[0][0]) :  : ); SB();
  asm volatile("ds_read_b64_tr_b16 v[188:189], %0 offset:24576" :  : "v"(aW[0][1][1]) : "memory"); SB();
  asm volatile("v_mfma_f32_16x16x32_bf16 %0, v[172:175], a[36:39], %0" : "+v"(a2[1][0]) :  : ); SB();
  asm volatile("ds_read_b64_tr_b16 v[190:191], %0 offset:25344" :  : "v"(aW[0][1][1]) : "memory"); SB();
  asm volatile("buffer_load_dwordx4 %0, %1, %2 offen offset:1024 lds" :  : "v"(voB[1]), "s"(rW), "s"(kw_[4]) : "memory"); SB();
  asm volatile("v_mfma_f32_16x16x32_bf16 %0, v[172:175], %1, %0" : "+v"(c2[0]) : "v"(ones) : ); SB();
  asm volatile("ds_read_b64_tr_b16 v[192:193], %0 offset:24576" :  : "v"(aW[0][1][2]) : "memory"); SB();
  asm volatile("v_mfma_f32_16x16x32_bf16 %0, v[176:179], a[32:35], %0" : "+v"(a2[0][1]) :  : ); SB();
  asm volatile("ds_read_b64_tr_b16 v[194:195], %0 offset:25344" :  : "v"(aW[0][1][2]) : "memory"); SB();
  asm volatile("v_mfma_f32_16x16x32_bf16 %0, v[176:179], a[36:39], %0" : "+v"(a2[1][1]) :  : ); SB();
  asm volatile("ds_read_b128 a[60:63], %0 offset:8192" :  : "v"(aX[1][1]) : "memory"); SB();
  asm volatile("v_mfma_f32_16x16x32_bf16 %0, v[176:179], %1, %0" : "+v"(c2[1]) : "v"(ones) : ); SB();
  asm volatile("buffer_load_dwordx4 %0, %1, %2 offen offset:2048 lds" :  : "v"(voB[2]), "s"(rW), "s"(kw_[4]) : "memory"); SB();
  asm volatile("s_waitcnt lgkmcnt(8)\n\tv_mfma_f32_16x16x32_bf16 %0, v[180:183], a[32:35], %0" : "+v"(a2[0][2]) :  : ); SB();
  asm volatile("s_add_u32 m0, %0, 147456" :  : "s"(mX) : "memory", "scc"); SB();
  asm volatile("v_mfma_f32_16x16x32_bf16 %0, v[180:183], a[36:39], %0" : "+v"(a2[1][2]) :  : ); SB();
  asm volatile("buffer_load_dwordx4 %0, %1, %2 offen lds" :  : "v"(voA), "s"(rX), "s"(kx[8]) : "memory"); SB();
  asm volatile("v_mfma_f32_16x16x32_bf16 %0, v[180:183], %1, %0" : "+v"(c2[2]) : "v"(ones) : ); SB();
  asm volatile("buffer_load_dwordx4 %0, %1, %2 offen offset:1024 lds" :  : "v"(voA2), "s"(rX), "s"(kx[8]) : "memory"); SB();
  asm volatile("s_add_u32 m0, %0, 155648" :  : "s"(mX) : "memory", "scc"); SB();
  asm volatile("s_nop 0\n\tbuffer_load_dwordx4 %0, %1, %2 offen lds" :  : "v"(voA), "s"(rX), "s"(kx[9]) : "memory"); SB();
  asm volatile("s_waitcnt vmcnt(14)\n\ts_barrier" :  :  : "memory"); SB();
  asm volatile("ds_read_b64_tr_b16 v[160:161], %0 offset:36864" :  : "v"(aW[0][0][0]) : "memory"); SB();
  asm volatile("v_mfma_f32_16x16x32_bf16 %0, %1, a[32:35], %0" : "+v"(rs) : "v"(ones) : ); SB();
  asm volatile("ds_read_b64_tr_b16 v[162:163], %0 offset:37632" :  : "v"(aW[0][0][0]) : "memory"); SB();
  asm volatile("v_mfma_f32_16x16x32_bf16 %0, a[32:35], a[32:35], %0" : "+v"(gr) :  : ); SB();
  asm volatile("ds_read_b64_tr_b16 v[164:165], %0 offset:36864" :  : "v"(aW[0][0][1]) : "memory"); SB();
  asm volatile("v_mfma_f32_16x16x32_bf16 %0, %1, a[40:43], %0" : "+v"(rs) : "v"(ones) : ); SB();
  asm volatile("ds_read_b64_tr_b16 v[166:167], %0 offset:37632" :  : "v"(aW[0][0][1]) : "memory"); SB();
  asm volatile("v_mfma_f32_16x16x32_bf16 %0, a[40:43], a[40:43], %0" : "+v"(gr) :  : ); SB();
  asm volatile("ds_read_b64_tr_b16 v[168:169], %0 offset:36864" :  : "v"(aW[0][0][2]) : "memory"); SB();
  asm volatile("s_waitcnt lgkmcnt(5)\n\tv_mfma_f32_16x16x32_bf16 %0, v[184:187], a[40:43], %0" : "+v"(a2[0][0]) :  : ); SB();
  asm volatile("ds_read_b64_tr_b16 v[170:171], %0 offset:37632" :  : "v"(aW[0][0][2]) : "memory"); SB();
  asm volatile("v_mfma_f32_16x16x32_bf16 %0, v[184:187], a[44:47], %0" : "+v"(a2[1][0]) :  : ); SB();
  asm volatile("ds_read_b128 a[64:67], %0 offset:16384" :  : "v"(aX[0][0]) : "memory"); SB();
  asm volatile("v_mfma_f32_16x16x32_bf16 %0, v[188:191], a[40:43], %0" : "+v"(a2[0][1]) :  : ); SB();
  asm volatile("ds_read_b128 a[68:71], %0 offset:16384" :  : "v"(aX[0][1]) : "memory"); SB();
  asm volatile("v_mfma_f32_16x16x32_bf16 %0, v[188:191], a[44:47], %0" : "+v"(a2[1][1]) :  : ); SB();
  asm volatile("ds_read_b128 a[72:75], %0 offset:16384" :  : "v"(aX[1][0]) : "memory"); SB();
  asm volatile("v_mfma_f32_16x16x32_bf16 %0, v[192:195], a[40:43], %0" : "+v"(a2[0][2]) :  : ); SB();
  asm volatile("ds_read_b128 a[76:79], %0 offset:16384" :  : "v"(aX[1][1]) : "memory"); SB();
  asm volatile("v_mfma_f32_16x16x32_bf16 %0, v[192:195], a[44:47], %0" : "+v"(a2[1][2]) :  : ); SB();
  asm volatile("buffer_load_dwordx4 %0, %1, %2 offen offset:1024 lds" :  : "v"(voA2), "s"(rX), "s"(kx[9]) : "memory"); SB();
  asm volatile("s_add_u32 m0, %0, 61440" :  : "s"(mW) : "memory", "scc"); SB();
  asm volatile("v_mfma_f32_16x16x32_bf16 %0, %1, a[48:51], %0" : "+v"(rs) : "v"(ones) : ); SB();
  asm volatile("ds_read_b128 a[80:83], %0 offset:24576" :  : "v"(aX[0][0]) : "memory"); SB();
  asm volatile("v_mfma_f32_16x16x32_bf16 %0, a[48:51], a[48:51], %0" : "+v"(gr) :  : ); SB();
  asm volatile("ds_read_b128 a[84:87], %0 offset:24576" :  : "v"(aX[0][1]) : "memory"); SB();
  asm volatile("v_mfma_f32_16x16x32_bf16 %0, %1, a[56:59], %0" : "+v"(rs) : "v"(ones) : ); SB();
  asm volatile("ds_read_b64_tr_b16 v[172:173], %0 offset:36864" :  : "v"(aW[0][1][0]) : "memory"); SB();
  asm volatile("v_mfma_f32_16x16x32_bf16 %0, a[56:59], a[56:59], %0" : "+v"(gr) :  : ); SB();
  asm volatile("ds_read_b64_tr_b16 v[174:175], %0 offset:37632" :  : "v"(aW[0][1][0]) : "memory"); SB();
  asm volatile("buffer_load_dwordx4 %0, %1, %2 offen lds" :  : "v"(voB[0]), "s"(rW), "s"(kw_[5]) : "memory"); SB();
  asm volatile("s_waitcnt lgkmcnt(8)\n\tv_mfma_f32_16x16x32_bf16 %0, v[160:163], a[48:51], %0" : "+v"(a2[0][0]) :  : ); SB();
  asm volatile("ds_read_b64_tr_b16 v[176:177], %0 offset:36864" :  : "v"(aW[0][1][1]) : "memory"); SB();
  asm volatile("v_mfma_f32_16x16x32_bf16 %0, v[160:163], a[52:55], %0" : "+v"(a2[1][0]) :  : ); SB();
  asm volatile("ds_read_b64_tr_b16 v[178:179], %0 offset:37632" :  : "v"(aW[0][1][1]) : "memory"); SB();
  asm volatile("v_mfma_f32_16x16x32_bf16 %0, v[160:163], %1, %0" : "+v"(c2[0]) : "v"(ones) : ); SB();
  asm volatile("ds_read_b64_tr_b16 v[180:181], %0 offset:36864" :  : "v"(aW[0][1][2]) : "memory"); SB();
  asm volatile("v_mfma_f32_16x16x32_bf16 %0, v[164:167], a[48:51], %0" : "+v"(a2[0][1]) :  : ); SB();
  asm volatile("ds_read_b64_tr_b16 v[182:183], %0 offset:37632" :  : "v"(aW[0][1][2]) : "memory"); SB();
  asm volatile("v_mfma_f32_16x16x32_bf16 %0, v[164:167], a[52:55], %0" : "+v"(a2[1][1]) :  : ); SB();
  asm volatile("ds_read_b128 a[88:91], %0 offset:24576" :  : "v"(aX[1][0]) : "memory"); SB();
  asm volatile("v_mfma_f32_16x16x32_bf16 %0, v[164:167], %1, %0" : "+v"(c2[1]) : "v"(ones) : ); SB();
  asm volatile("ds_read_b128 a[92:95], %0 offset:24576" :  : "v"(aX[1][1]) : "memory"); SB();
  asm volatile("v_mfma_f32_16x16x32_bf16 %0, v[168:171], a[48:51], %0" : "+v"(a2[0][2]) :  : ); SB();
  asm volatile("buffer_load_dwordx4 %0, %1, %2 offen offset:1024 lds" :  : "v"(voB[1]), "s"(rW), "s"(kw_[5]) : "memory"); SB();
  asm volatile("v_mfma_f32_16x16x32_bf16 %0, v[168:171], a[52:55], %0" : "+v"(a2[1][2]) :  : ); SB();
  asm volatile("buffer_load_dwordx4 %0, %1, %2 offen offset:2048 lds" :  : "v"(voB[2]), "s"(rW), "s"(kw_[5]) : "memory"); SB();
  asm volatile("v_mfma_f32_16x16x32_bf16 %0, v[168:171], %1, %0" : "+v"(c2[2]) : "v"(ones) : ); SB();
  asm volatile("s_add_u32 m0, %0, 73728" :  : "s"(mW) : "memory", "scc"); SB();
  asm volatile("s_nop 0\n\tbuffer_load_dwordx4 %0, %1, %2 offen lds" :  : "v"(voB[0]), "s"(rW), "s"(kw_[6]) : "memory"); SB();
  asm volatile("buffer_load_dwordx4 %0, %1, %2 offen offset:1024 lds" :  : "v"(voB[1]), "s"(rW), "s"(kw_[6]) : "memory"); SB();
  asm volatile("buffer_load_dwordx4 %0, %1, %2 offen offset:2048 lds" :  : "v"(voB[2]), "s"(rW), "s"(kw_[6]) : "memory"); SB();
  asm volatile("s_waitcnt vmcnt(10)\n\ts_barrier" :  :  : "memory"); SB();
  asm volatile("ds_read_b64_tr_b16 v[184:185], %0 offset:0" :  : "v"(aW[1][0][0]) : "memory"); SB();
  asm volatile("s_waitcnt lgkmcnt(1)\n\tv_mfma_f32_16x16x32_bf16 %0, %1, a[64:67], %0" : "+v"(rs) : "v"(ones) : ); SB();
  asm volatile("ds_read_b64_tr_b16 v[186:187], %0 offset:768" :  : "v"(aW[1][0][0]) : "memory"); SB();
  asm volatile("s_add_u32 m0, %0, 86016" :  : "s"(mW) : "memory", "scc"); SB();
  asm volatile("v_mfma_f32_16x16x32_bf16 %0, a[64:67], a[64:67], %0" : "+v"(gr) :  : ); SB();
  asm volatile("ds_read_b64_tr_b16 v[188:189], %0 offset:0" :  : "v"(aW[1][0][1]) : "memory"); SB();
  asm volatile("v_mfma_f32_16x16x32_bf16 %0, %1, a[72:75], %0" : "+v"(rs) : "v"(ones) : ); SB();
  asm volatile("ds_read_b64_tr_b16 v[190:191], %0 offset:768" :  : "v"(aW[1][0][1]) : "memory"); SB();
  asm volatile("v_mfma_f32_16x16x32_bf16 %0, a[72:75], a[72:75], %0" : "+v"(gr) :  : ); SB();
  asm volatile("ds_read_b64_tr_b16 v[192:193], %0 offset:0" :  : "v"(aW[1][0][2]) : "memory"); SB();
  asm volatile("v_mfma_f32_16x16x32_bf16 %0, v[172:175], a[56:59], %0" : "+v"(a2[0][0]) :  : ); SB();
  asm volatile("ds_read_b64_tr_b16 v[194:195], %0 offset:768" :  : "v"(aW[1][0][2]) : "memory"); SB();
  asm volatile("buffer_load_dwordx4 %0, %1, %2 offen lds" :  : "v"(voB[0]), "s"(rW), "s"(kw_[7]) : "memory"); SB();
  asm volatile("v_mfma_f32_16x16x32_bf16 %0, v[172:175], a[60:63], %0" : "+v"(a2[1][0]) :  : ); SB();
  asm volatile("ds_read_b128 a[96:99], %0 offset:32768" :  : "v"(aX[0][0]) : "memory"); SB();
  asm volatile("v_mfma_f32_16x16x32_bf16 %0, v[176:179], a[56:59], %0" : "+v"(a2[0][1]) :  : ); SB();
  asm volatile("ds_read_b128 a[100:103], %0 offset:32768" :  : "v"(aX[0][1]) : "memory"); SB();
  asm volatile("v_mfma_f32_16x16x32_bf16 %0, v[176:179], a[60:63], %0" : "+v"(a2[1][1]) :  : ); SB();
  asm volatile("ds_read_b128 a[104:107], %0 offset:32768" :  : "v"(aX[1][0]) : "memory"); SB();
  asm volatile("v_mfma_f32_16x16x32_bf16 %0, v[180:183], a[56:59], %0" : "+v"(a2[0][2]) :  : ); SB();
  asm volatile("buffer_load_dwordx4 %0, %1, %2 offen offset:1024 lds" :  : "v"(voB[1]), "s"(rW), "s"(kw_[7]) : "memory"); SB();
  asm volatile("v_mfma_f32_16x16x32_bf16 %0, v[180:183], a[60:63], %0" : "+v"(a2[1][2]) :  : ); SB();
  asm volatile("ds_read_b128 a[108:111], %0 offset:32768" :  : "v"(aX[1][1]) : "memory"); SB();
  asm volatile("v_mfma_f32_16x16x32_bf16 %0, %1, a[80:83], %0" : "+v"(rs) : "v"(ones) : ); SB();
  asm volatile("ds_read_b128 a[112:115], %0 offset:40960" :  : "v"(aX[0][0]) : "memory"); SB();
  asm volatile("v_mfma_f32_16x16x32_bf16 %0, a[80:83], a[80:83], %0" : "+v"(gr) :  : ); SB();
  asm volatile("ds_read_b128 a[116:119], %0 offset:40960" :  : "v"(aX[0][1]) : "memory"); SB();
  asm volatile("v_mfma_f32_16x16x32_bf16 %0, %1, a[88:91], %0" : "+v"(rs) : "v"(ones) : ); SB();
  asm volatile("ds_read_b64_tr_b16 v[160:161], %0 offset:0" :  : "v"(aW[1][1][0]) : "memory"); SB();
  asm volatile("v_mfma_f32_16x16x32_bf16 %0, a[88:91], a[88:91], %0" : "+v"(gr) :  : ); SB();
  asm volatile("ds_read_b64_tr_b16 v[162:163], %0 offset:768" :  : "v"(aW[1][1][0]) : "memory"); SB();
  asm volatile("s_waitcnt lgkmcnt(8)\n\tv_mfma_f32_16x16x32_bf16 %0, v[184:187], a[64:67], %0" : "+v"(a2[0][0]) :  : ); SB();
  asm volatile("ds_read_b64_tr_b16 v[164:165], %0 offset:0" :  : "v"(aW[1][1][1]) : "memory"); SB();
  asm volatile("v_mfma_f32_16x16x32_bf16 %0, v[184:187], a[68:71], %0" : "+v"(a2[1][0]) :  : ); SB();
  asm volatile("ds_read_b64_tr_b16 v[166:167], %0 offset:768" :  : "v"(aW[1][1][1]) : "memory"); SB();
  asm volatile("v_mfma_f32_16x16x32_bf16 %0, v[184:187], %1, %0" : "+v"(c2[0]) : "v"(ones) : ); SB();
  asm volatile("ds_read_b64_tr_b16 v[168:169], %0 offset:0" :  : "v"(aW[1][1][2]) : "memory"); SB();
  asm volatile("v_mfma_f32_16x16x32_bf16 %0, v[188:191], a[64:67], %0" : "+v"(a2[0][1]) :  : ); SB();
  asm volatile("ds_read_b64_tr_b16 v[170:171], %0 offset:768" :  : "v"(aW[1][1][2]) : "memory"); SB();
  asm volatile("buffer_load_dwordx4 %0, %1, %2 offen offset:2048 lds" :  : "v"(voB[2]), "s"(rW), "s"(kw_[7]) : "memory"); SB();
  asm volatile("v_mfma_f32_16x16x32_bf16 %0, v[188:191], a[68:71], %0" : "+v"(a2[1][1]) :  : ); SB();
  asm volatile("ds_read_b128 a[120:123], %0 offset:40960" :  : "v"(aX[1][0]) : "memory"); SB();
  asm volatile("s_add_u32 m0, %0, 0" :  : "s"(mW) : "memory", "scc"); SB();
  asm volatile("v_mfma_f32_16x16x32_bf16 %0, v[188:191], %1, %0" : "+v"(c2[1]) : "v"(ones) : ); SB();
  asm volatile("ds_read_b128 a[124:127], %0 offset:40960" :  : "v"(aX[1][1]) : "memory"); SB();
  asm volatile("v_mfma_f32_16x16x32_bf16 %0, v[192:195], a[64:67], %0" : "+v"(a2[0][2]) :  : ); SB();
  asm volatile("buffer_load_dwordx4 %0, %1, %2 offen lds" :  : "v"(voB[0]), "s"(rW), "s"(kw_[8]) : "memory"); SB();
  asm volatile("v_mfma_f32_16x16x32_bf16 %0, v[192:195], a[68:71], %0" : "+v"(a2[1][2]) :  : ); SB();
  asm volatile("v_mfma_f32_16x16x32_bf16 %0, v[192:195], %1, %0" : "+v"(c2[2]) : "v"(ones) : ); SB();
  asm volatile("buffer_load_dwordx4 %0, %1, %2 offen offset:1024 lds" :  : "v"(voB[1]), "s"(rW), "s"(kw_[8]) : "memory"); SB();
  asm volatile("buffer_load_dwordx4 %0, %1, %2 offen offset:2048 lds" :  : "v"(voB[2]), "s"(rW), "s"(kw_[8]) : "memory"); SB();
  asm volatile("s_add_u32 m0, %0, 12288" :  : "s"(mW) : "memory", "scc"); SB();
  asm volatile("s_nop 0\n\tbuffer_load_dwordx4 %0, %1, %2 offen lds" :  : "v"(voB[0]), "s"(rW), "s"(kw_[9]) : "memory"); SB();
  asm volatile("s_waitcnt vmcnt(10)\n\ts_barrier" :  :  : "memory"); SB();
  asm volatile("ds_read_b64_tr_b16 v[172:173], %0 offset:12288" :  : "v"(aW[1][0][0]) : "memory"); SB();
  asm volatile("s_waitcnt lgkmcnt(1)\n\tv_mfma_f32_16x16x32_bf16 %0, %1, a[96:99], %0" : "+v"(rs) : "v"(ones) : ); SB();
  asm volatile("ds_read_b64_tr_b16 v[174:175], %0 offset:13056" :  : "v"(aW[1][0][0]) : "memory"); SB();
  asm volatile("v_mfma_f32_16x16x32_bf16 %0, a[96:99], a[96:99], %0" : "+v"(gr) :  : ); SB();
  asm volatile("ds_read_b64_tr_b16 v[176:177], %0 offset:12288" :  : "v"(aW[1][0][1]) : "memory"); SB();
  asm volatile("v_mfma_f32_16x16x32_bf16 %0, %1, a[104:107], %0" : "+v"(rs) : "v"(ones) : ); SB();
  asm volatile("ds_read_b64_tr_b16 v[178:179], %0 offset:13056" :  : "v"(aW[1][0][1]) : "memory"); SB();
  asm volatile("v_mfma_f32_16x16x32_bf16 %0, a[104:107], a[104:107], %0" : "+v"(gr) :  : ); SB();
  asm volatile("ds_read_b64_tr_b16 v[180:181], %0 offset:12288" :  : "v"(aW[1][0][2]) : "memory"); SB();
  asm volatile("v_mfma_f32_16x16x32_bf16 %0, v[160:163], a[72:75], %0" : "+v"(a2[0][0]) :  : ); SB();
  asm volatile("ds_read_b64_tr_b16 v[182:183], %0 offset:13056" :  : "v"(aW[1][0][2]) : "memory"); SB();
  asm volatile("v_mfma_f32_16x16x32_bf16 %0, v[160:163], a[76:79], %0" : "+v"(a2[1][0]) :  : ); SB();
  asm volatile("ds_read_b128 a[128:131], %0 offset:49152" :  : "v"(aX[0][0]) : "memory"); SB();
  asm volatile("v_mfma_f32_16x16x32_bf16 %0, v[164:167], a[72:75], %0" : "+v"(a2[0][1]) :  : ); SB();
  asm volatile("ds_read_b128 a[132:135], %0 offset:49152" :  : "v"(aX[0][1]) : "memory"); SB();
  asm volatile("v_mfma_f32_16x16x32_bf16 %0, v[164:167], a[76:79], %0" : "+v"(a2[1][1]) :  : ); SB();
  asm volatile("ds_read_b128 a[136:139], %0 offset:49152" :  : "v"(aX[1][0]) : "memory"); SB();
  asm volatile("v_mfma_f32_16x16x32_bf16 %0, v[168:171], a[72:75], %0" : "+v"(a2[0][2]) :  : ); SB();
  asm volatile("ds_read_b128 a[140:143], %0 offset:49152" :  : "v"(aX[1][1]) : "memory"); SB();
  asm volatile("v_mfma_f32_16x16x32_bf16 %0, v[168:171], a[76:79], %0" : "+v"(a2[1][2]) :  : ); SB();
  asm volatile("ds_read_b128 a[144:147], %0 offset:57344" :  : "v"(aX[0][0]) : "memory"); SB();
  asm volatile("v_mfma_f32_16x16x32_bf16 %0, %1, a[112:115], %0" : "+v"(rs) : "v"(ones) : ); SB();
  asm volatile("ds_read_b128 a[148:151], %0 offset:57344" :  : "v"(aX[0][1]) : "memory"); SB();
  asm volatile("v_mfma_f32_16x16x32_bf16 %0, a[112:115], a[112:115], %0" : "+v"(gr) :  : ); SB();
  asm volatile("ds_read_b128 a[152:155], %0 offset:57344" :  : "v"(aX[1][0]) : "memory"); SB();
  asm volatile("v_mfma_f32_16x16x32_bf16 %0, %1, a[120:123], %0" : "+v"(rs) : "v"(ones) : ); SB();
  asm volatile("ds_read_b64_tr_b16 v[184:185], %0 offset:12288" :  : "v"(aW[1][1][0]) : "memory"); SB();
  asm volatile("v_mfma_f32_16x16x32_bf16 %0, a[120:123], a[120:123], %0" : "+v"(gr) :  : ); SB();
  asm volatile("ds_read_b64_tr_b16 v[186:187], %0 offset:13056" :  : "v"(aW[1][1][0]) : "memory"); SB();
  asm volatile("s_waitcnt lgkmcnt(9)\n\tv_mfma_f32_16x16x32_bf16 %0, v[172:175], a[80:83], %0" : "+v"(a2[0][0]) :  : ); SB();
  asm volatile("ds_read_b64_tr_b16 v[188:189], %0 offset:12288" :  : "v"(aW[1][1][1]) : "memory"); SB();
  asm volatile("v_mfma_f32_16x16x32_bf16 %0, v[172:175], a[84:87], %0" : "+v"(a2[1][0]) :  : ); SB();
  asm volatile("ds_read_b64_tr_b16 v[190:191], %0 offset:13056" :  : "v"(aW[1][1][1]) : "memory"); SB();
  asm volatile("v_mfma_f32_16x16x32_bf16 %0, v[172:175], %1, %0" : "+v"(c2[0]) : "v"(ones) : ); SB();
  asm volatile("ds_read_b64_tr_b16 v[192:193], %0 offset:12288" :  : "v"(aW[1][1][2]) : "memory"); SB();
  asm volatile("v_mfma_f32_16x16x32_bf16 %0, v[176:179], a[80:83], %0" : "+v"(a2[0][1]) :  : ); SB();
  asm volatile("ds_read_b64_tr_b16 v[194:195], %0 offset:13056" :  : "v"(aW[1][1][2]) : "memory"); SB();
  asm volatile("v_mfma_f32_16x16x32_bf16 %0, v[176:179], a[84:87], %0" : "+v"(a2[1][1]) :  : ); SB();
  asm volatile("ds_read_b128 a[156:159], %0 offset:57344" :  : "v"(aX[1][1]) : "memory"); SB();
  asm volatile("v_mfma_f32_16x16x32_bf16 %0, v[176:179], %1, %0" : "+v"(c2[1]) : "v"(ones) : ); SB();
  ao[0] = (w ^ 0) * 65536 + 3072; asm volatile("" : "+s"(ao[0])); SB();
  ao[1] = ao[0] + 31744; asm volatile("" : "+s"(ao[1])); SB();
  asm volatile("v_mfma_f32_16x16x32_bf16 %0, v[180:183], a[80:83], %0" : "+v"(a2[0][2]) :  : ); SB();
  ao[2] = (w ^ 1) * 65536 + 1024; asm volatile("" : "+s"(ao[2])); SB();
  ao[3] = ao[2] + 31744; asm volatile("" : "+s"(ao[3])); SB();
  asm volatile("v_mfma_f32_16x16x32_bf16 %0, v[180:183], a[84:87], %0" : "+v"(a2[1][2]) :  : ); SB();
  ao[4] = (w ^ 2) * 65536 + 3072; asm volatile("" : "+s"(ao[4])); SB();
  ao[5] = ao[4] + 31744; asm volatile("" : "+s"(ao[5])); SB();
  asm volatile("v_mfma_f32_16x16x32_bf16 %0, v[180:183], %1, %0" : "+v"(c2[2]) : "v"(ones) : ); SB();
  asm volatile("buffer_load_dwordx4 %0, %1, %2 offen offset:1024 lds" :  : "v"(voB[1]), "s"(rW), "s"(kw_[9]) : "memory"); SB();
  asm volatile("buffer_load_dwordx4 %0, %1, %2 offen offset:2048 lds" :  : "v"(voB[2]), "s"(rW), "s"(kw_[9]) : "memory"); SB();
  asm volatile("s_waitcnt vmcnt(9)\n\ts_barrier" :  :  : "memory"); SB();
  asm volatile("ds_read_b64_tr_b16 v[160:161], %0 offset:24576" :  : "v"(aW[1][0][0]) : "memory"); SB();
  asm volatile("s_waitcnt lgkmcnt(1)\n\tv_mfma_f32_16x16x32_bf16 %0, %1, a[128:131], %0" : "+v"(rs) : "v"(ones) : ); SB();
  asm volatile("ds_read_b64_tr_b16 v[162:163], %0 offset:25344" :  : "v"(aW[1][0][0]) : "memory"); SB();
  asm volatile("s_add_u32 m0, %0, 98304" :  : "s"(mA) : "memory", "scc"); SB();
  asm volatile("v_mfma_f32_16x16x32_bf16 %0, a[128:131], a[128:131], %0" : "+v"(gr) :  : ); SB();
  asm volatile("ds_read_b64_tr_b16 v[164:165], %0 offset:24576" :  : "v"(aW[1][0][1]) : "memory"); SB();
  asm volatile("v_mfma_f32_16x16x32_bf16 %0, %1, a[136:139], %0" : "+v"(rs) : "v"(ones) : ); SB();
  asm volatile("ds_read_b64_tr_b16 v[166:167], %0 offset:25344" :  : "v"(aW[1][0][1]) : "memory"); SB();
  asm volatile("v_mfma_f32_16x16x32_bf16 %0, a[136:139], a[136:139], %0" : "+v"(gr) :  : ); SB();
  asm volatile("ds_read_b64_tr_b16 v[168:169], %0 offset:24576" :  : "v"(aW[1][0][2]) : "memory"); SB();
  asm volatile("v_mfma_f32_16x16x32_bf16 %0, v[184:187], a[88:91], %0" : "+v"(a2[0][0]) :  : ); SB();
  asm volatile("ds_read_b64_tr_b16 v[170:171], %0 offset:25344" :  : "v"(aW[1][0][2]) : "memory"); SB();
  asm volatile("v_mfma_f32_16x16x32_bf16 %0, v[184:187], a[92:95], %0" : "+v"(a2[1][0]) :  : ); SB();
  ao[6] = (w ^ 3) * 65536 + 1024; asm volatile("" : "+s"(ao[6])); SB();
  ao[7] = ao[6] + 31744; asm volatile("" : "+s"(ao[7])); SB();
  asm volatile("v_mfma_f32_16x16x32_bf16 %0, v[188:191], a[88:91], %0" : "+v"(a2[0][1]) :  : ); SB();
  asm volatile("buffer_load_dwordx4 %0, %1, %2 offen lds" :  : "v"(voK[0]), "s"(rK), "s"(0u) : "memory"); SB();
  asm volatile("v_mfma_f32_16x16x32_bf16 %0, v[188:191], a[92:95], %0" : "+v"(a2[1][1]) :  : ); SB();
  asm volatile("buffer_load_dwordx4 %0, %1, %2 offen offset:1024 lds" :  : "v"(voK[1]), "s"(rK), "s"(0u) : "memory"); SB();
  asm volatile("v_mfma_f32_16x16x32_bf16 %0, v[192:195], a[88:91], %0" : "+v"(a2[0][2]) :  : ); SB();
  asm volatile("ds_read_b64_tr_b16 v[172:173], %0 offset:24576" :  : "v"(aW[1][1][0]) : "memory"); SB();
  asm volatile("v_mfma_f32_16x16x32_bf16 %0, v[192:195], a[92:95], %0" : "+v"(a2[1][2]) :  : ); SB();
  asm volatile("ds_read_b64_tr_b16 v[174:175], %0 offset:25344" :  : "v"(aW[1][1][0]) : "memory"); SB();
  asm volatile("v_mfma_f32_16x16x32_bf16 %0, %1, a[144:147], %0" : "+v"(rs) : "v"(ones) : ); SB();
  asm volatile("ds_read_b64_tr_b16 v[176:177], %0 offset:24576" :  : "v"(aW[1][1][1]) : "memory"); SB();
  asm volatile("v_mfma_f32_16x16x32_bf16 %0, a[144:147], a[144:147], %0" : "+v"(gr) :  : ); SB();
  asm volatile("ds_read_b64_tr_b16 v[178:179], %0 offset:25344" :  : "v"(aW[1][1][1]) : "memory"); SB();
  asm volatile("v_mfma_f32_16x16x32_bf16 %0, %1, a[152:155], %0" : "+v"(rs) : "v"(ones) : ); SB();
  asm volatile("ds_read_b64_tr_b16 v[180:181], %0 offset:24576" :  : "v"(aW[1][1][2]) : "memory"); SB();
  asm volatile("v_mfma_f32_16x16x32_bf16 %0, a[152:155], a[152:155], %0" : "+v"(gr) :  : ); SB();
  asm volatile("ds_read_b64_tr_b16 v[182:183], %0 offset:25344" :  : "v"(aW[1][1][2]) : "memory"); SB();
  asm volatile("s_waitcnt lgkmcnt(6)\n\tv_mfma_f32_16x16x32_bf16 %0, v[160:163], a[96:99], %0" : "+v"(a2[0][0]) :  : ); SB();
  asm volatile("buffer_load_dwordx4 %0, %1, %2 offen offset:2048 lds" :  : "v"(voK[2]), "s"(rK), "s"(0u) : "memory"); SB();
  asm volatile("s_mov_b32 exec_lo, 0xff\n\ts_mov_b32 exec_hi, 0\n\tbuffer_load_dwordx4 %0, %1, %2 offen offset:3072 lds\n\ts_mov_b64 exec, -1" :  : "v"(voK[3]), "s"(rK), "s"(0u) : "memory"); SB();
  asm volatile("s_mov_b32 exec_lo, -1\n\ts_mov_b32 exec_hi, 0xff\n\tbuffer_load_dwordx4 %0, %1, %2 offen offset:3328 lds\n\ts_mov_b64 exec, -1" :  : "v"(voH), "s"(rS), "s"(0u) : "memory"); SB();
  asm volatile("s_waitcnt vmcnt(11)\n\ts_barrier" :  :  : "memory"); SB();
  asm volatile("ds_read_b64_tr_b16 v[184:185], %0 offset:36864" :  : "v"(aW[1][0][0]) : "memory"); SB();
  asm volatile("s_add_u32 m0, %0, 102400" :  : "s"(mA) : "memory", "scc"); SB();
  asm volatile("v_mfma_f32_16x16x32_bf16 %0, v[160:163], a[100:103], %0" : "+v"(a2[1][0]) :  : ); SB();
  asm volatile("ds_read_b64_tr_b16 v[186:187], %0 offset:37632" :  : "v"(aW[1][0][0]) : "memory"); SB();
  asm volatile("v_mfma_f32_16x16x32_bf16 %0, v[160:163], %1, %0" : "+v"(c2[0]) : "v"(ones) : ); SB();
  asm volatile("ds_read_b64_tr_b16 v[188:189], %0 offset:36864" :  : "v"(aW[1][0][1]) : "memory"); SB();
  asm volatile("v_mfma_f32_16x16x32_bf16 %0, v[164:167], a[96:99], %0" : "+v"(a2[0][1]) :  : ); SB();
  asm volatile("ds_read_b64_tr_b16 v[190:191], %0 offset:37632" :  : "v"(aW[1][0][1]) : "memory"); SB();
  asm volatile("v_mfma_f32_16x16x32_bf16 %0, v[164:167], a[100:103], %0" : "+v"(a2[1][1]) :  : ); SB();
  asm volatile("ds_read_b64_tr_b16 v[192:193], %0 offset:36864" :  : "v"(aW[1][0][2]) : "memory"); SB();
  asm volatile("v_mfma_f32_16x16x32_bf16 %0, v[164:167], %1, %0" : "+v"(c2[1]) : "v"(ones) : ); SB();
  asm volatile("ds_read_b64_tr_b16 v[194:195], %0 offset:37632" :  : "v"(aW[1][0][2]) : "memory"); SB();
  asm volatile("v_mfma_f32_16x16x32_bf16 %0, v[168:171], a[96:99], %0" : "+v"(a2[0][2]) :  : ); SB();
  asm volatile("buffer_load_dwordx4 %0, %1, %2 offen lds" :  : "v"(voV[0]), "s"(rV), "s"(0u) : "memory"); SB();
  asm volatile("v_mfma_f32_16x16x32_bf16 %0, v[168:171], a[100:103], %0" : "+v"(a2[1][2]) :  : ); SB();
  asm volatile("buffer_load_dwordx4 %0, %1, %2 offen offset:1024 lds" :  : "v"(voV[1]), "s"(rV), "s"(0u) : "memory"); SB();
  asm volatile("v_mfma_f32_16x16x32_bf16 %0, v[168:171], %1, %0" : "+v"(c2[2]) : "v"(ones) : ); SB();
  asm volatile("s_waitcnt lgkmcnt(6)\n\tv_mfma_f32_16x16x32_bf16 %0, v[172:175], a[104:107], %0" : "+v"(a2[0][0]) :  : ); SB();
  asm volatile("ds_read_b64_tr_b16 v[160:161], %0 offset:36864" :  : "v"(aW[1][1][0]) : "memory"); SB();
  asm volatile("v_cndmask_b32 %0, %1, %2, %3" : "=v"(xt[0]) : "v"(gr[0]), "v"(gr[1]), "s"(0xAAAAAAAAAAAAAAAAull) : ); SB();
  asm volatile("v_mfma_f32_16x16x32_bf16 %0, v[172:175], a[108:111], %0" : "+v"(a2[1][0]) :  : ); SB();
  asm volatile("s_mov_b32 exec_lo, 0x10001\n\ts_mov_b32 exec_hi, 0x10001\n\tds_write_b128 %0, %1 offset:0\n\ts_mov_b64 exec, -1" :  : "v"(ca), "v"(c2[0]) : "memory"); SB();
  asm volatile("ds_read_b64_tr_b16 v[162:163], %0 offset:37632" :  : "v"(aW[1][1][0]) : "memory"); SB();
  asm volatile("v_cndmask_b32 %0, %1, %2, %3" : "=v"(xt[1]) : "v"(gr[2]), "v"(gr[3]), "s"(0xAAAAAAAAAAAAAAAAull) : ); SB();
  asm volatile("v_mfma_f32_16x16x32_bf16 %0, v[176:179], a[104:107], %0" : "+v"(a2[0][1]) :  : ); SB();
  asm volatile("ds_read_b64_tr_b16 v[164:165], %0 offset:36864" :  : "v"(aW[1][1][1]) : "memory"); SB();
  asm volatile("v_mul_f32 %0, 0x3acccccd, %1" : "=v"(xs[0][0]) : "v"(rs[0]) : ); SB();
  asm volatile("v_mfma_f32_16x16x32_bf16 %0, v[176:179], a[108:111], %0" : "+v"(a2[1][1]) :  : ); SB();
  asm volatile("s_mov_b32 exec_lo, 0x10001\n\ts_mov_b32 exec_hi, 0x10001\n\tds_write_b128 %0, %1 offset:64\n\ts_mov_b64 exec, -1" :  : "v"(ca), "v"(c2[1]) : "memory"); SB();
  asm volatile("ds_read_b64_tr_b16 v[166:167], %0 offset:37632" :  : "v"(aW[1][1][1]) : "memory"); SB();
  asm volatile("v_cndmask_b32 %0, %0, %1, %2" : "+v"(xt[0]) : "v"(xt[1]), "s"(0xCCCCCCCCCCCCCCCCull) : ); SB();
  asm volatile("v_mfma_f32_16x16x32_bf16 %0, v[180:183], a[104:107], %0" : "+v"(a2[0][2]) :  : ); SB();
  asm volatile("ds_read_b64_tr_b16 v[168:169], %0 offset:36864" :  : "v"(aW[1][1][2]) : "memory"); SB();
  asm volatile("v_mul_f32 %0, 0x3a83126f, %1" : "=v"(xt[1]) : "v"(rs[0]) : ); SB();
  asm volatile("v_mfma_f32_16x16x32_bf16 %0, v[180:183], a[108:111], %0" : "+v"(a2[1][2]) :  : ); SB();
  asm volatile("s_mov_b32 exec_lo, 0x10001\n\ts_mov_b32 exec_hi, 0x10001\n\tds_write_b128 %0, %1 offset:128\n\ts_mov_b64 exec, -1" :  : "v"(ca), "v"(c2[2]) : "memory"); SB();
  asm volatile("ds_read_b64_tr_b16 v[170:171], %0 offset:37632" :  : "v"(aW[1][1][2]) : "memory"); SB();
  asm volatile("s_waitcnt lgkmcnt(6)\n\tv_mfma_f32_16x16x32_bf16 %0, v[184:187], a[112:115], %0" : "+v"(a2[0][0]) :  : ); SB();
  asm volatile("s_mov_b32 exec_lo, -1\n\ts_mov_b32 exec_hi, 0\n\tbuffer_load_dwordx4 %0, %1, %2 offen offset:2048 lds\n\ts_mov_b64 exec, -1" :  : "v"(voV[2]), "s"(rV), "s"(0u) : "memory"); SB();
  asm volatile("v_fma_f32 %0, -%1, %2, %3" : "=v"(xs[0][1]) : "v"(rs[0]), "v"(xs[0][0]), "v"(xt[0]) : ); SB();
  asm volatile("v_mfma_f32_16x16x32_bf16 %0, v[184:187], a[116:119], %0" : "+v"(a2[1][0]) :  : ); SB();
  asm volatile("buffer_load_dwordx4 %0, %1, %2 offen offset:3072 lds" :  : "v"(voO), "s"(rO), "s"(0u) : "memory"); SB();
  asm volatile("v_mul_f32 %0, %0, %1" : "+v"(xt[1]) : "v"(xs[0][0]) : ); SB();
  asm volatile("v_mfma_f32_16x16x32_bf16 %0, v[184:187], %1, %0" : "+v"(c2[0]) : "v"(ones) : ); SB();
  if (__builtin_expect((__builtin_amdgcn_ballot_w64(!(xs[0][1] >= xt[1])) & 0xF0000F0000F0000Full) != 0, 0)) {
    const f32x2 mm = {xs[0][0], xs[0][0]};
    f32x2 q2 = {0.f, 0.f};
    const u32x4* xp = (const u32x4*)(x + (size_t)(rb * 64 + w * 16 + l15) * 1280 + g4 * 320);
#pragma unroll 1
    for (int c = 0; c < 20; ++c) {
      u32x4 xq;
      asm volatile("global_load_dwordx4 %0, %1, off\n\ts_waitcnt vmcnt(0)" : "=&v"(xq) : "v"(xp + c) : "memory");
#pragma unroll
      for (int e = 0; e < 4; ++e) { const f32x2 d = unpk(xq[e]) - mm; q2 = __builtin_elementwise_fma(d, d, q2); }
    }
    xs[0][1] = red32(red16(q2[0] + q2[1]));
  }
  SB();
  asm volatile("v_mfma_f32_16x16x32_bf16 %0, v[188:191], a[112:115], %0" : "+v"(a2[0][1]) :  : ); SB();
  asm volatile("s_waitcnt vmcnt(12)\n\ts_barrier" :  :  : "memory"); SB();
  asm volatile("ds_read_b64_tr_b16 v[172:173], %0 offset:0" :  : "v"(aW[0][0][0]) : "memory"); SB();
  asm volatile("v_max_f32 %0, 0, %0" : "+v"(xs[0][1]) :  : ); SB();
  asm volatile("v_mfma_f32_16x16x32_bf16 %0, v[188:191], a[116:119], %0" : "+v"(a2[1][1]) :  : ); SB();
  asm volatile("ds_read_b64_tr_b16 v[174:175], %0 offset:768" :  : "v"(aW[0][0][0]) : "memory"); SB();
  asm volatile("v_mul_f32 %0, 0x3acccccd, %0" : "+v"(xs[0][1]) :  : ); SB();
  asm volatile("v_mfma_f32_16x16x32_bf16 %0, v[188:191], %1, %0" : "+v"(c2[1]) : "v"(ones) : ); SB();
  asm volatile("ds_read_b64_tr_b16 v[176:177], %0 offset:0" :  : "v"(aW[0][0][1]) : "memory"); SB();
  asm volatile("v_cvt_pk_bf16_f32 %0, %0, %0" : "+v"(xs[0][1]) :  : ); SB();
  asm volatile("v_mfma_f32_16x16x32_bf16 %0, v[192:195], a[112:115], %0" : "+v"(a2[0][2]) :  : ); SB();
  asm volatile("ds_read_b64_tr_b16 v[178:179], %0 offset:768" :  : "v"(aW[0][0][1]) : "memory"); SB();
  asm volatile("v_lshlrev_b32 %0, 16, %0" : "+v"(xs[0][1]) :  : ); SB();
  asm volatile("v_mfma_f32_16x16x32_bf16 %0, v[192:195], a[116:119], %0" : "+v"(a2[1][2]) :  : ); SB();
  asm volatile("ds_read_b64_tr_b16 v[180:181], %0 offset:0" :  : "v"(aW[0][0][2]) : "memory"); SB();
  asm volatile("v_add_f32 %0, 0x3727c5ac, %0" : "+v"(xs[0][1]) :  : ); SB();
  asm volatile("v_mfma_f32_16x16x32_bf16 %0, v[192:195], %1, %0" : "+v"(c2[2]) : "v"(ones) : ); SB();
  asm volatile("ds_read_b64_tr_b16 v[182:183], %0 offset:768" :  : "v"(aW[0][0][2]) : "memory"); SB();
  asm volatile("v_cvt_pk_bf16_f32 %0, %0, %0" : "+v"(xs[0][1]) :  : ); SB();
  asm volatile("v_mfma_f32_16x16x32_bf16 %0, v[160:163], a[120:123], %0" : "+v"(a2[0][0]) :  : ); SB();
  asm volatile("buffer_load_dwordx4 v[228:231], %0, %1, %2 offen" :  : "v"(vF), "s"(rF), "s"(0u) : "memory"); SB();
  asm volatile("v_lshlrev_b32 %0, 16, %0" : "+v"(xs[0][1]) :  : ); SB();
  asm volatile("v_mfma_f32_16x16x32_bf16 %0, v[160:163], a[124:127], %0" : "+v"(a2[1][0]) :  : ); SB();
  asm volatile("buffer_load_dwordx4 v[232:235], %0, %1, %2 offen" :  : "v"(vF), "s"(rF), "s"(4096u) : "memory"); SB();
  asm volatile("v_mfma_f32_16x16x32_bf16 %0, v[160:163], %1, %0" : "+v"(c2[0]) : "v"(ones) : ); SB();
  asm volatile("v_sqrt_f32 %0, %0" : "+v"(xs[0][1]) :  : ); SB();
  asm volatile("s_waitcnt lgkmcnt(6)\n\tv_mfma_f32_16x16x32_bf16 %0, v[164:167], a[120:123], %0" : "+v"(a2[0][1]) :  : ); SB();
  asm volatile("ds_read_b64_tr_b16 v[184:185], %0 offset:0" :  : "v"(aW[0][1][0]) : "memory"); SB();
  asm volatile("v_cvt_pk_bf16_f32 %0, %0, %0" : "+v"(xs[0][0]) :  : ); SB();
  asm volatile("v_mfma_f32_16x16x32_bf16 %0, v[164:167], a[124:127], %0" : "+v"(a2[1][1]) :  : ); SB();
  asm volatile("ds_read_b64_tr_b16 v[186:187], %0 offset:768" :  : "v"(aW[0][1][0]) : "memory"); SB();
  asm volatile("v_lshlrev_b32 %0, 16, %0" : "+v"(xs[0][0]) :  : ); SB();
  asm volatile("v_mfma_f32_16x16x32_bf16 %0, v[164:167], %1, %0" : "+v"(c2[1]) : "v"(ones) : ); SB();
  asm volatile("ds_read_b64_tr_b16 v[188:189], %0 offset:0" :  : "v"(aW[0][1][1]) : "memory"); SB();
  asm volatile("v_cvt_pk_bf16_f32 %0, %0, %0" : "+v"(xs[0][1]) :  : ); SB();
  asm volatile("v_mfma_f32_16x16x32_bf16 %0, v[168:171], a[120:123], %0" : "+v"(a2[0][2]) :  : ); SB();
  asm volatile("ds_read_b64_tr_b16 v[190:191], %0 offset:768" :  : "v"(aW[0][1][1]) : "memory"); SB();
  asm volatile("v_lshlrev_b32 %0, 16, %0" : "+v"(xs[0][1]) :  : ); SB();
  asm volatile("v_mfma_f32_16x16x32_bf16 %0, v[168:171], a[124:127], %0" : "+v"(a2[1][2]) :  : ); SB();
  asm volatile("ds_read_b64_tr_b16 v[192:193], %0 offset:0" :  : "v"(aW[0][1][2]) : "memory"); SB();
  asm volatile("v_mfma_f32_16x16x32_bf16 %0, v[168:171], %1, %0" : "+v"(c2[2]) : "v"(ones) : ); SB();
  asm volatile("ds_read_b64_tr_b16 v[194:195], %0 offset:768" :  : "v"(aW[0][1][2]) : "memory"); SB();
  asm volatile("s_waitcnt lgkmcnt(6)\n\tv_mfma_f32_16x16x32_bf16 %0, v[172:175], a[128:131], %0" : "+v"(a2[0][0]) :  : ); SB();
  asm volatile("buffer_load_dwordx4 v[236:239], %0, %1, %2 offen" :  : "v"(vF), "s"(rF), "s"(c2s_) : "memory"); SB();
  asm volatile("v_mfma_f32_16x16x32_bf16 %0, v[172:175], a[132:135], %0" : "+v"(a2[1][0]) :  : ); SB();
  asm volatile("v_rcp_f32 %0, %1" : "=v"(xt[0]) : "v"(xs[0][1]) : ); SB();
  asm volatile("v_mfma_f32_16x16x32_bf16 %0, v[172:175], %1, %0" : "+v"(c2[0]) : "v"(ones) : ); SB();
  asm volatile("s_nop 0\n\tv_mul_f32 %0, 0xc0135761, %1" : "=v"(xs[0][2]) : "v"(xt[0]) : ); SB();
  asm volatile("v_mfma_f32_16x16x32_bf16 %0, v[176:179], a[128:131], %0" : "+v"(a2[0][1]) :  : ); SB();
  asm volatile("v_mul_f32 %0, %1, %1" : "=v"(xt[1]) : "v"(xt[0]) : ); SB();
  asm volatile("v_mul_f32 %0, 0xbdd2d3e8, %0" : "+v"(xt[0]) :  : ); SB();
  asm volatile("v_mul_f32 %0, %1, %2" : "=v"(xs[0][3]) : "v"(xt[0]), "v"(xt[1]) : ); SB();
  asm volatile("s_mov_b32 exec_lo, 0xf0000f\n\ts_mov_b32 exec_hi, 0xf0000f00\n\tds_write_b128 %0, %1\n\ts_mov_b64 exec, -1" :  : "v"(xw), "v"(xs[0]) : "memory"); SB();
  asm volatile("s_waitcnt vmcnt(12) lgkmcnt(0)\n\ts_barrier" :  :  : "memory"); SB();
  asm volatile("ds_read_b64_tr_b16 v[160:161], %0 offset:12288" :  : "v"(aW[0][0][0]) : "memory"); SB();
  asm volatile("s_add_u32 m0, %0, 24576" :  : "s"(mW) : "memory", "scc"); SB();
  asm volatile("v_mfma_f32_16x16x32_bf16 %0, v[176:179], a[132:135], %0" : "+v"(a2[1][1]) :  : ); SB();
  asm volatile("ds_read_b64_tr_b16 v[162:163], %0 offset:13056" :  : "v"(aW[0][0][0]) : "memory"); SB();
  asm volatile("v_mfma_f32_16x16x32_bf16 %0, v[176:179], %1, %0" : "+v"(c2[1]) : "v"(ones) : ); SB();
  asm volatile("ds_read_b64_tr_b16 v[164:165], %0 offset:12288" :  : "v"(aW[0][0][1]) : "memory"); SB();
  asm volatile("v_mfma_f32_16x16x32_bf16 %0, v[180:183], a[128:131], %0" : "+v"(a2[0][2]) :  : ); SB();
  asm volatile("ds_read_b64_tr_b16 v[166:167], %0 offset:13056" :  : "v"(aW[0][0][1]) : "memory"); SB();
  asm volatile("v_mfma_f32_16x16x32_bf16 %0, v[180:183], a[132:135], %0" : "+v"(a2[1][2]) :  : ); SB();
  asm volatile("ds_read_b64_tr_b16 v[168:169], %0 offset:12288" :  : "v"(aW[0][0][2]) : "memory"); SB();
  asm volatile("v_mfma_f32_16x16x32_bf16 %0, v[180:183], %1, %0" : "+v"(c2[2]) : "v"(ones) : ); SB();
  asm volatile("ds_read_b64_tr_b16 v[170:171], %0 offset:13056" :  : "v"(aW[0][0][2]) : "memory"); SB();
  asm volatile("v_mfma_f32_16x16x32_bf16 %0, v[184:187], a[136:139], %0" : "+v"(a2[0][0]) :  : ); SB();
  asm volatile("ds_read_b128 %0, %1 offset:0" : "=&v"(xs[0]) : "v"(xw) : "memory"); SB();
  asm volatile("v_mfma_f32_16x16x32_bf16 %0, v[184:187], a[140:143], %0" : "+v"(a2[1][0]) :  : ); SB();
  asm volatile("ds_read_b128 %0, %1 offset:0" : "=&v"(xs[1]) : "v"(xr_) : "memory"); SB();
  asm volatile("v_mfma_f32_16x16x32_bf16 %0, v[184:187], %1, %0" : "+v"(c2[0]) : "v"(ones) : ); SB();
  asm volatile("ds_read_b128 %0, %1 offset:0" : "=&v"(co[0]) : "v"(cr) : "memory"); SB();
  asm volatile("v_mfma_f32_16x16x32_bf16 %0, v[188:191], a[136:139], %0" : "+v"(a2[0][1]) :  : ); SB();
  asm volatile("ds_read_b64_tr_b16 v[172:173], %0 offset:12288" :  : "v"(aW[0][1][0]) : "memory"); SB();
  asm volatile("v_mfma_f32_16x16x32_bf16 %0, v[188:191], a[140:143], %0" : "+v"(a2[1][1]) :  : ); SB();
  asm volatile("ds_read_b64_tr_b16 v[174:175], %0 offset:13056" :  : "v"(aW[0][1][0]) : "memory"); SB();
  asm volatile("v_mfma_f32_16x16x32_bf16 %0, v[188:191], %1, %0" : "+v"(c2[1]) : "v"(ones) : ); SB();
  asm volatile("ds_read_b64_tr_b16 v[176:177], %0 offset:12288" :  : "v"(aW[0][1][1]) : "memory"); SB();
  asm volatile("v_mfma_f32_16x16x32_bf16 %0, v[192:195], a[136:139], %0" : "+v"(a2[0][2]) :  : ); SB();
  asm volatile("ds_read_b64_tr_b16 v[178:179], %0 offset:13056" :  : "v"(aW[0][1][1]) : "memory"); SB();
  asm volatile("v_mfma_f32_16x16x32_bf16 %0, v[192:195], a[140:143], %0" : "+v"(a2[1][2]) :  : ); SB();
  asm volatile("ds_read_b64_tr_b16 v[180:181], %0 offset:12288" :  : "v"(aW[0][1][2]) : "memory"); SB();
  asm volatile("buffer_load_dwordx4 %0, %1, %2 offen lds" :  : "v"(voB[0]), "s"(rW1), "s"(kw_[0]) : "memory"); SB();
  asm volatile("v_mfma_f32_16x16x32_bf16 %0, v[192:195], %1, %0" : "+v"(c2[2]) : "v"(ones) : ); SB();
  asm volatile("ds_read_b64_tr_b16 v[182:183], %0 offset:13056" :  : "v"(aW[0][1][2]) : "memory"); SB();
  asm volatile("s_waitcnt lgkmcnt(9)\n\tv_mfma_f32_16x16x32_bf16 %0, v[160:163], a[144:147], %0" : "+v"(a2[0][0]) :  : ); SB();
  asm volatile("ds_read_b128 %0, %1 offset:64" : "=&v"(co[1]) : "v"(cr) : "memory"); SB();
  asm volatile("v_mfma_f32_16x16x32_bf16 %0, v[160:163], a[148:151], %0" : "+v"(a2[1][0]) :  : ); SB();
  asm volatile("ds_read_b128 %0, %1 offset:128" : "=&v"(co[2]) : "v"(cr) : "memory"); SB();
  asm volatile("v_mfma_f32_16x16x32_bf16 %0, v[160:163], %1, %0" : "+v"(c2[0]) : "v"(ones) : ); SB();
  asm volatile("v_readfirstlane_b32 %0, v252\n\tv_readfirstlane_b32 %1, v253\n\ts_nop 0\n\ts_add_u32 %0, %0, 1\n\ts_addc_u32 %1, %1, 0" : "=&s"(tg0), "=&s"(tg1) :  : "memory", "scc"); SB();
  asm volatile("v_mfma_f32_16x16x32_bf16 %0, v[164:167], a[144:147], %0" : "+v"(a2[0][1]) :  : ); SB();
  asm volatile("buffer_load_dwordx4 %0, %1, %2 offen offset:1024 lds" :  : "v"(voB[1]), "s"(rW1), "s"(kw_[0]) : "memory"); SB();
  asm volatile("v_mfma_f32_16x16x32_bf16 %0, v[164:167], a[148:151], %0" : "+v"(a2[1][1]) :  : ); SB();
  { unsigned lane = lane0, w = w0, rb = rb0, cb = cb0;
  asm volatile("" : "+v"(lane)); asm volatile("" : "+s"(w), "+s"(rb), "+s"(cb)); SB();

  asm volatile("v_mfma_f32_16x16x32_bf16 %0, v[164:167], %1, %0" : "+v"(c2[1]) : "v"(ones) : ); SB();
  w3k[0] = w3s + 32768; asm volatile("" : "+s"(w3k[0])); SB();
  asm volatile("v_mfma_f32_16x16x32_bf16 %0, v[168:171], a[144:147], %0" : "+v"(a2[0][2]) :  : ); SB();
  w3k[1] = w3s + 65536; asm volatile("" : "+s"(w3k[1])); SB();
  asm volatile("buffer_load_dwordx4 %0, %1, %2 offen offset:2048 lds" :  : "v"(voB[2]), "s"(rW1), "s"(kw_[0]) : "memory"); SB();
  asm volatile("v_mfma_f32_16x16x32_bf16 %0, v[168:171], a[148:151], %0" : "+v"(a2[1][2]) :  : ); SB();
  sFZ = W_FZ + ((cb * 32 + rb) * 4 + w) * 8; asm volatile("" : "+s"(sFZ)); SB();
  asm volatile("v_mfma_f32_16x16x32_bf16 %0, v[168:171], %1, %0" : "+v"(c2[2]) : "v"(ones) : ); SB();
  sFK = sFZ + 8192; asm volatile("" : "+s"(sFK)); SB();
  asm volatile("s_add_u32 m0, %0, 36864" :  : "s"(mW) : "memory", "scc"); SB();
  asm volatile("s_waitcnt lgkmcnt(1)\n\tv_mfma_f32_16x16x32_bf16 %0, v[172:175], a[152:155], %0" : "+v"(a2[0][0]) :  : ); SB();
  mU = lb + w * 1024; asm volatile("" : "+s"(mU)); SB();
  asm volatile("v_mfma_f32_16x16x32_bf16 %0, v[172:175], a[156:159], %0" : "+v"(a2[1][0]) :  : ); SB();
  uso = W_KV + c2s; asm volatile("" : "+s"(uso)); SB();
  asm volatile("v_mfma_f32_16x16x32_bf16 %0, v[172:175], %1, %0" : "+v"(c2[0]) : "v"(ones) : ); SB();
  asm volatile("buffer_load_dwordx4 %0, %1, %2 offen lds" :  : "v"(voB[0]), "s"(rW1), "s"(kw_[1]) : "memory"); SB();
  asm volatile("v_mfma_f32_16x16x32_bf16 %0, v[176:179], a[152:155], %0" : "+v"(a2[0][1]) :  : ); SB();
  asm volatile("buffer_load_dwordx4 %0, %1, %2 offen offset:1024 lds" :  : "v"(voB[1]), "s"(rW1), "s"(kw_[1]) : "memory"); SB();
  asm volatile("v_mfma_f32_16x16x32_bf16 %0, v[176:179], a[156:159], %0" : "+v"(a2[1][1]) :  : ); SB();
  asm volatile("v_mfma_f32_16x16x32_bf16 %0, v[176:179], %1, %0" : "+v"(c2[1]) : "v"(ones) : ); SB();
  asm volatile("v_mfma_f32_16x16x32_bf16 %0, v[180:183], a[152:155], %0" : "+v"(a2[0][2]) :  : ); SB();
  asm volatile("buffer_load_dwordx4 %0, %1, %2 offen offset:2048 lds" :  : "v"(voB[2]), "s"(rW1), "s"(kw_[1]) : "memory"); SB();
  asm volatile("v_mfma_f32_16x16x32_bf16 %0, v[180:183], a[156:159], %0" : "+v"(a2[1][2]) :  : ); SB();
  asm volatile("v_mfma_f32_16x16x32_bf16 %0, v[180:183], %1, %0" : "+v"(c2[2]) : "v"(ones) : ); SB();
  ST(3);
  ST(5);
  const unsigned long long tg = ((unsigned long long)tg1 << 32) | tg0;
  asm volatile("s_waitcnt lgkmcnt(0)" : "+v"(xs[0]), "+v"(xs[1]), "+v"(co[0]), "+v"(co[1]), "+v"(co[2]) :  : "memory"); SB();
  asm volatile("s_nop 3" : "+v"(a2[0][0]), "+v"(a2[0][1]), "+v"(a2[0][2]), "+v"(a2[1][0]), "+v"(a2[1][1]), "+v"(a2[1][2]), "+v"(c2[0]), "+v"(c2[1]), "+v"(c2[2]) :  : ); SB();
  for (int i = 0; i < 2; ++i) { xm[i] = xs[i][0]; xd[i] = xs[i][1]; k1[i] = xs[i][2]; k2[i] = xs[i][3]; }
  c2[0] += co[0]; c2[1] += co[1]; c2[2] += co[2];
  f32x2 ge2[2];
  auto act = [&](f32x2& t, f32x2& e, const f32x4& a, const f32x4& cs, int i, int h) {
    t = __builtin_elementwise_fma((f32x2){-xm[i], -xm[i]}, (f32x2){cs[2 * h], cs[2 * h + 1]}, (f32x2){a[2 * h], a[2 * h + 1]});
    const f32x2 u = t * __builtin_elementwise_fma(t * t, (f32x2){k2[i], k2[i]}, (f32x2){k1[i], k1[i]});
    e = (f32x2){__builtin_amdgcn_exp2f(u[0]), __builtin_amdgcn_exp2f(u[1])};
  };
  auto act2 = [&](f32x2& t, f32x2 e, int i) {
    e = __builtin_elementwise_fma(e, (f32x2){xd[i], xd[i]}, (f32x2){xd[i], xd[i]});
    t *= (f32x2){__builtin_amdgcn_rcpf(e[0]), __builtin_amdgcn_rcpf(e[1])};
  };
  f32x2 gv[2][3][2];
  float sv[2][2], pv[2];
  act(gv[0][0][0], ge2[0], a2[0][0], c2[0], 0, 0); act(gv[0][0][1], ge2[1], a2[0][0], c2[0], 0, 1);
  asm volatile("" : "+v"(gv[0][0][0]), "+v"(gv[0][0][1]), "+v"(ge2[0]), "+v"(ge2[1]));
  SB();
  asm volatile("s_add_u32 m0, %0, 49152" :  : "s"(mW) : "memory", "scc"); SB();
  asm volatile("s_nop 0\n\tbuffer_load_dwordx4 %0, %1, %2 offen lds" :  : "v"(voB[0]), "s"(rW1), "s"(kw_[2]) : "memory"); SB();
  asm volatile("buffer_load_dwordx4 %0, %1, %2 offen offset:1024 lds" :  : "v"(voB[1]), "s"(rW1), "s"(kw_[2]) : "memory"); SB();
  act2(gv[0][0][0], ge2[0], 0); act2(gv[0][0][1], ge2[1], 0);
  asm volatile("" : "+v"(gv[0][0][0]), "+v"(gv[0][0][1]));
  SB();
  asm volatile("buffer_load_dwordx4 %0, %1, %2 offen offset:2048 lds" :  : "v"(voB[2]), "s"(rW1), "s"(kw_[2]) : "memory"); SB();
  act(gv[0][1][0], ge2[0], a2[0][1], c2[1], 0, 0); act(gv[0][1][1], ge2[1], a2[0][1], c2[1], 0, 1);
  asm volatile("" : "+v"(gv[0][1][0]), "+v"(gv[0][1][1]), "+v"(ge2[0]), "+v"(ge2[1]));
  SB();
  asm volatile("s_add_u32 m0, %0, 61440" :  : "s"(mW) : "memory", "scc"); SB();
  asm volatile("s_nop 0\n\tbuffer_load_dwordx4 %0, %1, %2 offen lds" :  : "v"(voB[0]), "s"(rW1), "s"(kw_[3]) : "memory"); SB();
  asm volatile("buffer_load_dwordx4 %0, %1, %2 offen offset:1024 lds" :  : "v"(voB[1]), "s"(rW1), "s"(kw_[3]) : "memory"); SB();
  act2(gv[0][1][0], ge2[0], 0); act2(gv[0][1][1], ge2[1], 0);
  asm volatile("" : "+v"(gv[0][1][0]), "+v"(gv[0][1][1]));
  SB();
  asm volatile("buffer_load_dwordx4 %0, %1, %2 offen offset:2048 lds" :  : "v"(voB[2]), "s"(rW1), "s"(kw_[3]) : "memory"); SB();
  act(gv[0][2][0], ge2[0], a2[0][2], c2[2], 0, 0); act(gv[0][2][1], ge2[1], a2[0][2], c2[2], 0, 1);
  asm volatile("" : "+v"(gv[0][2][0]), "+v"(gv[0][2][1]), "+v"(ge2[0]), "+v"(ge2[1]));
  SB();
  asm volatile("s_add_u32 m0, %0, 73728" :  : "s"(mW) : "memory", "scc"); SB();
  asm volatile("s_nop 0\n\tbuffer_load_dwordx4 %0, %1, %2 offen lds" :  : "v"(voB[0]), "s"(rW1), "s"(kw_[4]) : "memory"); SB();
  asm volatile("buffer_load_dwordx4 %0, %1, %2 offen offset:1024 lds" :  : "v"(voB[1]), "s"(rW1), "s"(kw_[4]) : "memory"); SB();
  act2(gv[0][2][0], ge2[0], 0); act2(gv[0][2][1], ge2[1], 0);
  asm volatile("" : "+v"(gv[0][2][0]), "+v"(gv[0][2][1]));
  SB();
  asm volatile("buffer_load_dwordx4 %0, %1, %2 offen offset:2048 lds" :  : "v"(voB[2]), "s"(rW1), "s"(kw_[4]) : "memory"); SB();
  act(gv[1][0][0], ge2[0], a2[1][0], c2[0], 1, 0); act(gv[1][0][1], ge2[1], a2[1][0], c2[0], 1, 1);
  asm volatile("" : "+v"(gv[1][0][0]), "+v"(gv[1][0][1]), "+v"(ge2[0]), "+v"(ge2[1]));
  SB();
  act2(gv[1][0][0], ge2[0], 1); act2(gv[1][0][1], ge2[1], 1);
  asm volatile("" : "+v"(gv[1][0][0]), "+v"(gv[1][0][1]));
  SB();
  act(gv[1][1][0], ge2[0], a2[1][1], c2[1], 1, 0); act(gv[1][1][1], ge2[1], a2[1][1], c2[1], 1, 1);
  asm volatile("" : "+v"(gv[1][1][0]), "+v"(gv[1][1][1]), "+v"(ge2[0]), "+v"(ge2[1]));
  SB();
  act2(gv[1][1][0], ge2[0], 1); act2(gv[1][1][1], ge2[1], 1);
  asm volatile("" : "+v"(gv[1][1][0]), "+v"(gv[1][1][1]));
  SB();
  act(gv[1][2][0], ge2[0], a2[1][2], c2[2], 1, 0); act(gv[1][2][1], ge2[1], a2[1][2], c2[2], 1, 1);
  asm volatile("" : "+v"(gv[1][2][0]), "+v"(gv[1][2][1]), "+v"(ge2[0]), "+v"(ge2[1]));
  SB();
  asm volatile("s_add_u32 m0, %0, 86016" :  : "s"(mW) : "memory", "scc"); SB();
  asm volatile("s_nop 0\n\tbuffer_load_dwordx4 %0, %1, %2 offen lds" :  : "v"(voB[0]), "s"(rW1), "s"(kw_[5]) : "memory"); SB();
  act2(gv[1][2][0], ge2[0], 1); act2(gv[1][2][1], ge2[1], 1);
  asm volatile("" : "+v"(gv[1][2][0]), "+v"(gv[1][2][1]));
  SB();
  asm volatile("buffer_load_dwordx4 %0, %1, %2 offen offset:1024 lds" :  : "v"(voB[1]), "s"(rW1), "s"(kw_[5]) : "memory"); SB();
#pragma unroll
  for (int i = 0; i < 2; ++i) {
    const unsigned p0 = __float_as_uint(gv[i][0][0][0]);
    const auto pa = __builtin_amdgcn_permlane16_swap(p0, p0, false, false);
    const auto pb = __builtin_amdgcn_permlane32_swap(pa[0], pa[0], false, false);
    pv[i] = __uint_as_float(pb[0]);
  }
  asm volatile("" : "+v"(pv[0]), "+v"(pv[1]));
  f32x2 s1[2], s2[2];
  SB();
  asm volatile("buffer_load_dwordx4 %0, %1, %2 offen offset:2048 lds" :  : "v"(voB[2]), "s"(rW1), "s"(kw_[5]) : "memory"); SB();
#pragma unroll
  for (int m = 0; m < 4; ++m) {
    const int i = m & 1, n = 0 + (m >> 1);
    const f32x2 d = gv[i][n >> 1][n & 1] - (f32x2){pv[i], pv[i]};
    s1[i] = n ? s1[i] + d : d; s2[i] = n ? __builtin_elementwise_fma(d, d, s2[i]) : d * d;
  }
  asm volatile("" : "+v"(s1[0]), "+v"(s1[1]), "+v"(s2[0]), "+v"(s2[1]));
  SB();
  asm volatile("s_add_u32 m0, %0, 0" :  : "s"(mW) : "memory", "scc"); SB();
  asm volatile("s_nop 0\n\tbuffer_load_dwordx4 %0, %1, %2 offen lds" :  : "v"(voB[0]), "s"(rW1), "s"(kw_[6]) : "memory"); SB();
#pragma unroll
  for (int m = 0; m < 4; ++m) {
    const int i = m & 1, n = 2 + (m >> 1);
    const f32x2 d = gv[i][n >> 1][n & 1] - (f32x2){pv[i], pv[i]};
    s1[i] = n ? s1[i] + d : d; s2[i] = n ? __builtin_elementwise_fma(d, d, s2[i]) : d * d;
  }
  asm volatile("" : "+v"(s1[0]), "+v"(s1[1]), "+v"(s2[0]), "+v"(s2[1]));
  SB();
  asm volatile("buffer_load_dwordx4 %0, %1, %2 offen offset:1024 lds" :  : "v"(voB[1]), "s"(rW1), "s"(kw_[6]) : "memory"); SB();
#pragma unroll
  for (int m = 0; m < 4; ++m) {
    const int i = m & 1, n = 4 + (m >> 1);
    const f32x2 d = gv[i][n >> 1][n & 1] - (f32x2){pv[i], pv[i]};
    s1[i] = n ? s1[i] + d : d; s2[i] = n ? __builtin_elementwise_fma(d, d, s2[i]) : d * d;
  }
  asm volatile("" : "+v"(s1[0]), "+v"(s1[1]), "+v"(s2[0]), "+v"(s2[1]));
  SB();
  for (int i = 0; i < 2; ++i) { sv[i][0] = s1[i][0] + s1[i][1]; sv[i][1] = s2[i][0] + s2[i][1]; }
  {
    const float t1 = red16(sel32(sv[0][0], sv[1][0])), t2 = red16(sel32(sv[0][1], sv[1][1]));
    const float dl = t1 * (1.0f / 48.0f);
    const u32x4 so = {__float_as_uint((lane < 32 ? pv[0] : pv[1]) + dl), __float_as_uint(fmaxf(t2 - t1 * dl, 0.f)), tg0, tg1};
    SB();
  asm volatile("s_mov_b32 exec_lo, 0xffff\n\ts_mov_b32 exec_hi, 0xffff\n\tbuffer_store_dwordx4 %1, %0, %2, %3 offen offset:0 sc1\n\ts_mov_b64 exec, -1\n\ts_nop 0" :  : "v"(vo), "v"(so), "s"(rWS), "s"(0u) : "memory"); SB();
  }
  ST(6);
  unsigned zp[2][3][2];
#pragma unroll
  for (int n = 0; n < 12; ++n) { zp[n / 6][(n >> 1) % 3][n & 1] = pk2(gv[n / 6][(n >> 1) % 3][n & 1]); asm volatile("" : "+v"(zp[n / 6][(n >> 1) % 3][n & 1])); }
  SB();
  asm volatile("s_waitcnt vmcnt(24)\n\ts_barrier" :  :  : "memory"); SB();
  ST(2);
  asm volatile("ds_read_b64_tr_b16 %0, %1 offset:0" : "=&v"(wt[0]) : "v"(oa) : "memory"); SB();
  asm volatile("ds_read_b64_tr_b16 %0, %1 offset:512" : "=&v"(wt[1]) : "v"(oa) : "memory"); SB();
  asm volatile("ds_read_b64_tr_b16 %0, %1 offset:0" : "=&v"(kp[0]) : "v"(ka) : "memory"); SB();
  asm volatile("ds_read_b64_tr_b16 %0, %1 offset:640" : "=&v"(kc[0]) : "v"(ka) : "memory"); SB();
  asm volatile("ds_read_b64_tr_b16 %0, %1 offset:4096" : "=&v"(vv[0]) : "v"(ka) : "memory"); SB();
  asm volatile("ds_read_b64 %0, %1 offset:0" : "=&v"(sw[0]) : "v"(sa) : "memory"); SB();
  asm volatile("buffer_load_dwordx4 %0, %1, %2 offen offset:2048 lds" :  : "v"(voB[2]), "s"(rW1), "s"(kw_[6]) : "memory"); SB();
  asm volatile("s_add_u32 m0, %0, 131072" :  : "s"(mA) : "memory", "scc"); SB();
  asm volatile("s_nop 0\n\tbuffer_load_dwordx4 %0, %1, %2 offen lds" :  : "v"(voS), "s"(rA), "s"(ao[0]) : "memory"); SB();
  asm volatile("ds_read_b64_tr_b16 %0, %1 offset:32" : "=&v"(kp[1]) : "v"(ka) : "memory"); SB();
  asm volatile("ds_read_b64_tr_b16 %0, %1 offset:672" : "=&v"(kc[1]) : "v"(ka) : "memory"); SB();
  asm volatile("ds_read_b64_tr_b16 %0, %1 offset:4128" : "=&v"(vv[1]) : "v"(ka) : "memory"); SB();
  asm volatile("ds_read_b64 %0, %1 offset:128" : "=&v"(sw[1]) : "v"(sa) : "memory"); SB();
  asm volatile("buffer_load_dwordx4 %0, %1, %2 offen offset:1024 lds" :  : "v"(voS), "s"(rA), "s"(ao[1]) : "memory"); SB();
  asm volatile("ds_read_b64_tr_b16 %0, %1 offset:64" : "=&v"(kp[2]) : "v"(ka) : "memory"); SB();
  asm volatile("ds_read_b64_tr_b16 %0, %1 offset:704" : "=&v"(kc[2]) : "v"(ka) : "memory"); SB();
  asm volatile("ds_read_b64_tr_b16 %0, %1 offset:4160" : "=&v"(vv[2]) : "v"(ka) : "memory"); SB();
  asm volatile("ds_read_b64 %0, %1 offset:256" : "=&v"(sw[2]) : "v"(sa) : "memory"); SB();
  asm volatile("buffer_load_dwordx4 %0, %1, %2 offen offset:2048 lds" :  : "v"(voS), "s"(rA), "s"(ao[2]) : "memory"); SB();
  asm volatile("ds_read_b64_tr_b16 %0, %1 offset:96" : "=&v"(kp[3]) : "v"(ka) : "memory"); SB();
  asm volatile("ds_read_b64_tr_b16 %0, %1 offset:736" : "=&v"(kc[3]) : "v"(ka) : "memory"); SB();
  asm volatile("ds_read_b64_tr_b16 %0, %1 offset:4192" : "=&v"(vv[3]) : "v"(ka) : "memory"); SB();
  asm volatile("ds_read_b64 %0, %1 offset:384" : "=&v"(sw[3]) : "v"(sa) : "memory"); SB();
  asm volatile("ds_read_b64_tr_b16 %0, %1 offset:128" : "=&v"(kp[4]) : "v"(ka) : "memory"); SB();
  asm volatile("ds_read_b64_tr_b16 %0, %1 offset:768" : "=&v"(kc[4]) : "v"(ka) : "memory"); SB();
  asm volatile("ds_read_b64_tr_b16 %0, %1 offset:4224" : "=&v"(vv[4]) : "v"(ka) : "memory"); SB();
  asm volatile("ds_read_b64 %0, %1 offset:512" : "=&v"(sw[4]) : "v"(sa) : "memory"); SB();
  asm volatile("buffer_load_dwordx4 %0, %1, %2 offen offset:3072 lds" :  : "v"(voS), "s"(rA), "s"(ao[3]) : "memory"); SB();
  asm volatile("s_waitcnt lgkmcnt(8)" : "+v"(kp[0]), "+v"(kc[0]), "+v"(vv[0]), "+v"(sw[0]), "+v"(wt[0]), "+v"(wt[1]) :  : "memory"); SB();
  asm volatile("v_alignbit_b32 %4, %14, %14, 16\n\tv_alignbit_b32 %5, %10, %9, 16\n\tv_alignbit_b32 %6, %11, %10, 16\n\tv_alignbit_b32 %7, %13, %13, 16\n\tv_alignbit_b32 %8, %12, %11, 16\n\tv_dot2_f32_bf16 %0, %5, %4, 0\n\tv_dot2_f32_bf16 %1, %10, %4, 0\n\tv_dot2_f32_bf16 %2, %6, %4, 0\n\tv_dot2_f32_bf16 %3, %11, %4, 0\n\tv_dot2_f32_bf16 %0, %6, %7, %0\n\tv_dot2_f32_bf16 %1, %11, %7, %1\n\tv_dot2_f32_bf16 %2, %8, %7, %2\n\tv_dot2_f32_bf16 %3, %12, %7, %3" : "=&v"(kt[0][0]), "=&v"(kt[0][1]), "=&v"(kt[1][0]), "=&v"(kt[1][1]), "=&v"(ku[0]), "=&v"(ku[1]), "=&v"(ku[2]), "=&v"(ku[3]), "=&v"(ku[4]) : "v"(kp[0][0]), "v"(kp[0][1]), "v"(kc[0][0]), "v"(kc[0][1]), "v"(sw[0][0]), "v"(sw[0][1]) : ); SB();
  asm volatile("s_add_u32 m0, %0, 135168" :  : "s"(mA) : "memory", "scc"); SB();
  asm volatile("s_nop 0\n\tbuffer_load_dwordx4 %0, %1, %2 offen lds" :  : "v"(voS), "s"(rA), "s"(ao[4]) : "memory"); SB();
  asm volatile("v_lshlrev_b32 %0, 16, %4\n\tv_and_b32 %1, 0xffff0000, %4\n\tv_lshlrev_b32 %2, 16, %5\n\tv_and_b32 %3, 0xffff0000, %5" : "=&v"(kf[0][0]), "=&v"(kf[0][1]), "=&v"(kf[1][0]), "=&v"(kf[1][1]) : "v"(vv[0][0]), "v"(vv[0][1]) : ); SB();
  kt[0] *= kf[0]; kt[1] *= kf[1]; SB();
  ko[0] = (u32x2){pk2(kt[0]), pk2(kt[1])};
  asm volatile("" : "+v"(ko[0]));
  SB();
  asm volatile("buffer_load_dwordx4 %0, %1, %2 offen offset:1024 lds" :  : "v"(voS), "s"(rA), "s"(ao[5]) : "memory"); SB();
  asm volatile("ds_write_b64 %0, %1 offset:0" :  : "v"(kw), "v"(ko[0]) : "memory"); SB();
  asm volatile("" : "+v"(kp[1]), "+v"(kc[1]), "+v"(vv[1]), "+v"(sw[1]) :  : "memory"); SB();
  asm volatile("v_alignbit_b32 %4, %14, %14, 16\n\tv_alignbit_b32 %5, %10, %9, 16\n\tv_alignbit_b32 %6, %11, %10, 16\n\tv_alignbit_b32 %7, %13, %13, 16\n\tv_alignbit_b32 %8, %12, %11, 16\n\tv_dot2_f32_bf16 %0, %5, %4, 0\n\tv_dot2_f32_bf16 %1, %10, %4, 0\n\tv_dot2_f32_bf16 %2, %6, %4, 0\n\tv_dot2_f32_bf16 %3, %11, %4, 0\n\tv_dot2_f32_bf16 %0, %6, %7, %0\n\tv_dot2_f32_bf16 %1, %11, %7, %1\n\tv_dot2_f32_bf16 %2, %8, %7, %2\n\tv_dot2_f32_bf16 %3, %12, %7, %3" : "=&v"(kt[0][0]), "=&v"(kt[0][1]), "=&v"(kt[1][0]), "=&v"(kt[1][1]), "=&v"(ku[0]), "=&v"(ku[1]), "=&v"(ku[2]), "=&v"(ku[3]), "=&v"(ku[4]) : "v"(kp[1][0]), "v"(kp[1][1]), "v"(kc[1][0]), "v"(kc[1][1]), "v"(sw[1][0]), "v"(sw[1][1]) : ); SB();
  asm volatile("buffer_load_dwordx4 %0, %1, %2 offen offset:2048 lds" :  : "v"(voS), "s"(rA), "s"(ao[6]) : "memory"); SB();
  asm volatile("v_lshlrev_b32 %0, 16, %4\n\tv_and_b32 %1, 0xffff0000, %4\n\tv_lshlrev_b32 %2, 16, %5\n\tv_and_b32 %3, 0xffff0000, %5" : "=&v"(kf[0][0]), "=&v"(kf[0][1]), "=&v"(kf[1][0]), "=&v"(kf[1][1]) : "v"(vv[1][0]), "v"(vv[1][1]) : ); SB();
  kt[0] *= kf[0]; kt[1] *= kf[1]; SB();
  ko[1] = (u32x2){pk2(kt[0]), pk2(kt[1])};
  asm volatile("" : "+v"(ko[1]));
  SB();
  asm volatile("ds_write_b64 %0, %1 offset:2304" :  : "v"(kw), "v"(ko[1]) : "memory"); SB();
  asm volatile("" : "+v"(kp[2]), "+v"(kc[2]), "+v"(vv[2]), "+v"(sw[2]) :  : "memory"); SB();
  asm volatile("v_alignbit_b32 %4, %14, %14, 16\n\tv_alignbit_b32 %5, %10, %9, 16\n\tv_alignbit_b32 %6, %11, %10, 16\n\tv_alignbit_b32 %7, %13, %13, 16\n\tv_alignbit_b32 %8, %12, %11, 16\n\tv_dot2_f32_bf16 %0, %5, %4, 0\n\tv_dot2_f32_bf16 %1, %10, %4, 0\n\tv_dot2_f32_bf16 %2, %6, %4, 0\n\tv_dot2_f32_bf16 %3, %11, %4, 0\n\tv_dot2_f32_bf16 %0, %6, %7, %0\n\tv_dot2_f32_bf16 %1, %11, %7, %1\n\tv_dot2_f32_bf16 %2, %8, %7, %2\n\tv_dot2_f32_bf16 %3, %12, %7, %3" : "=&v"(kt[0][0]), "=&v"(kt[0][1]), "=&v"(kt[1][0]), "=&v"(kt[1][1]), "=&v"(ku[0]), "=&v"(ku[1]), "=&v"(ku[2]), "=&v"(ku[3]), "=&v"(ku[4]) : "v"(kp[2][0]), "v"(kp[2][1]), "v"(kc[2][0]), "v"(kc[2][1]), "v"(sw[2][0]), "v"(sw[2][1]) : ); SB();
  asm volatile("buffer_load_dwordx4 %0, %1, %2 offen offset:3072 lds" :  : "v"(voS), "s"(rA), "s"(ao[7]) : "memory"); SB();
  asm volatile("v_xor_b32 %0, 128, %0" : "+v"(voS) :  : "memory"); SB();
  asm volatile("v_lshlrev_b32 %0, 16, %4\n\tv_and_b32 %1, 0xffff0000, %4\n\tv_lshlrev_b32 %2, 16, %5\n\tv_and_b32 %3, 0xffff0000, %5" : "=&v"(kf[0][0]), "=&v"(kf[0][1]), "=&v"(kf[1][0]), "=&v"(kf[1][1]) : "v"(vv[2][0]), "v"(vv[2][1]) : ); SB();
  kt[0] *= kf[0]; kt[1] *= kf[1]; SB();
  ko[2] = (u32x2){pk2(kt[0]), pk2(kt[1])};
  asm volatile("" : "+v"(ko[2]));
  SB();
  asm volatile("ds_write_b64 %0, %1 offset:4608" :  : "v"(kw), "v"(ko[2]) : "memory"); SB();
  asm volatile("s_waitcnt lgkmcnt(2)" : "+v"(kp[3]), "+v"(kc[3]), "+v"(vv[3]), "+v"(sw[3]) :  : "memory"); SB();
  asm volatile("v_alignbit_b32 %4, %14, %14, 16\n\tv_alignbit_b32 %5, %10, %9, 16\n\tv_alignbit_b32 %6, %11, %10, 16\n\tv_alignbit_b32 %7, %13, %13, 16\n\tv_alignbit_b32 %8, %12, %11, 16\n\tv_dot2_f32_bf16 %0, %5, %4, 0\n\tv_dot2_f32_bf16 %1, %10, %4, 0\n\tv_dot2_f32_bf16 %2, %6, %4, 0\n\tv_dot2_f32_bf16 %3, %11, %4, 0\n\tv_dot2_f32_bf16 %0, %6, %7, %0\n\tv_dot2_f32_bf16 %1, %11, %7, %1\n\tv_dot2_f32_bf16 %2, %8, %7, %2\n\tv_dot2_f32_bf16 %3, %12, %7, %3" : "=&v"(kt[0][0]), "=&v"(kt[0][1]), "=&v"(kt[1][0]), "=&v"(kt[1][1]), "=&v"(ku[0]), "=&v"(ku[1]), "=&v"(ku[2]), "=&v"(ku[3]), "=&v"(ku[4]) : "v"(kp[3][0]), "v"(kp[3][1]), "v"(kc[3][0]), "v"(kc[3][1]), "v"(sw[3][0]), "v"(sw[3][1]) : ); SB();
  asm volatile("s_add_u32 m0, %0, 98304" :  : "s"(mA) : "memory", "scc"); SB();
  asm volatile("s_nop 0\n\tbuffer_load_dwordx4 %0, %1, %2 offen lds" :  : "v"(voS), "s"(rA), "s"(ao[0]) : "memory"); SB();
  asm volatile("buffer_load_dwordx4 %0, %1, %2 offen offset:1024 lds" :  : "v"(voS), "s"(rA), "s"(ao[1]) : "memory"); SB();
  asm volatile("v_lshlrev_b32 %0, 16, %4\n\tv_and_b32 %1, 0xffff0000, %4\n\tv_lshlrev_b32 %2, 16, %5\n\tv_and_b32 %3, 0xffff0000, %5" : "=&v"(kf[0][0]), "=&v"(kf[0][1]), "=&v"(kf[1][0]), "=&v"(kf[1][1]) : "v"(vv[3][0]), "v"(vv[3][1]) : ); SB();
  kt[0] *= kf[0]; kt[1] *= kf[1]; SB();
  ko[3] = (u32x2){pk2(kt[0]), pk2(kt[1])};
  asm volatile("" : "+v"(ko[3]));
  SB();
  asm volatile("buffer_load_dwordx4 %0, %1, %2 offen offset:2048 lds" :  : "v"(voS), "s"(rA), "s"(ao[2]) : "memory"); SB();
  asm volatile("ds_write_b64 %0, %1 offset:6912" :  : "v"(kw), "v"(ko[3]) : "memory"); SB();
  asm volatile("" : "+v"(kp[4]), "+v"(kc[4]), "+v"(vv[4]), "+v"(sw[4]) :  : "memory"); SB();
  asm volatile("v_alignbit_b32 %4, %14, %14, 16\n\tv_alignbit_b32 %5, %10, %9, 16\n\tv_alignbit_b32 %6, %11, %10, 16\n\tv_alignbit_b32 %7, %13, %13, 16\n\tv_alignbit_b32 %8, %12, %11, 16\n\tv_dot2_f32_bf16 %0, %5, %4, 0\n\tv_dot2_f32_bf16 %1, %10, %4, 0\n\tv_dot2_f32_bf16 %2, %6, %4, 0\n\tv_dot2_f32_bf16 %3, %11, %4, 0\n\tv_dot2_f32_bf16 %0, %6, %7, %0\n\tv_dot2_f32_bf16 %1, %11, %7, %1\n\tv_dot2_f32_bf16 %2, %8, %7, %2\n\tv_dot2_f32_bf16 %3, %12, %7, %3" : "=&v"(kt[0][0]), "=&v"(kt[0][1]), "=&v"(kt[1][0]), "=&v"(kt[1][1]), "=&v"(ku[0]), "=&v"(ku[1]), "=&v"(ku[2]), "=&v"(ku[3]), "=&v"(ku[4]) : "v"(kp[4][0]), "v"(kp[4][1]), "v"(kc[4][0]), "v"(kc[4][1]), "v"(sw[4][0]), "v"(sw[4][1]) : ); SB();
  asm volatile("v_lshlrev_b32 %0, 16, %4\n\tv_and_b32 %1, 0xffff0000, %4\n\tv_lshlrev_b32 %2, 16, %5\n\tv_and_b32 %3, 0xffff0000, %5" : "=&v"(kf[0][0]), "=&v"(kf[0][1]), "=&v"(kf[1][0]), "=&v"(kf[1][1]) : "v"(vv[4][0]), "v"(vv[4][1]) : ); SB();
  kt[0] *= kf[0]; kt[1] *= kf[1]; SB();
  ko[4] = (u32x2){pk2(kt[0]), pk2(kt[1])};
  asm volatile("" : "+v"(ko[4]));
  SB();
  asm volatile("ds_write_b64 %0, %1 offset:9216" :  : "v"(kw), "v"(ko[4]) : "memory"); SB();
  ST(16);
  asm volatile("s_waitcnt vmcnt(27) lgkmcnt(0)\n\ts_barrier" :  :  : "memory"); SB();
  asm volatile("ds_read_b128 %0, %1 offset:0" : "=&v"(kq[0]) : "v"(kr) : "memory"); SB();
  asm volatile("ds_read_b128 %0, %1 offset:1152" : "=&v"(kq[1]) : "v"(kr) : "memory"); SB();
  asm volatile("ds_read_b128 %0, %1 offset:2304" : "=&v"(kq[2]) : "v"(kr) : "memory"); SB();
  asm volatile("ds_read_b64_tr_b16 v[184:185], %0 offset:24576" :  : "v"(aW[0][0][0]) : "memory"); SB();
  asm volatile("ds_read_b64_tr_b16 v[186:187], %0 offset:25344" :  : "v"(aW[0][0][0]) : "memory"); SB();
  asm volatile("ds_read_b64_tr_b16 v[188:189], %0 offset:24576" :  : "v"(aW[0][0][1]) : "memory"); SB();
  asm volatile("ds_read_b64_tr_b16 v[190:191], %0 offset:25344" :  : "v"(aW[0][0][1]) : "memory"); SB();
  asm volatile("ds_read_b64_tr_b16 v[192:193], %0 offset:24576" :  : "v"(aW[0][0][2]) : "memory"); SB();
  asm volatile("ds_read_b64_tr_b16 v[194:195], %0 offset:25344" :  : "v"(aW[0][0][2]) : "memory"); SB();
  ST(7);
  asm volatile("s_waitcnt lgkmcnt(6)" : "+v"(kq[0]), "+v"(kq[1]), "+v"(kq[2]) :  : "memory"); SB();
  unsigned kz[3] = {kq[0][3], kq[1][3] | (w + 1) >> 2, kq[2][3] | (w + 1) >> 2};
  asm volatile("" : "+v"(kz[0]), "+v"(kz[1]), "+v"(kz[2])); SB();
  asm volatile("buffer_load_dwordx4 v[196:199], %0, %1, %2 offen offset:0 sc1" :  : "v"(vS), "s"(rWS), "s"(0u) : "memory"); SB();
  asm volatile("buffer_load_dwordx4 v[200:203], %0, %1, %2 offen offset:1024 sc1" :  : "v"(vS), "s"(rWS), "s"(0u) : "memory"); SB();
  asm volatile("s_waitcnt vmcnt(11)" :  :  : "memory"); SB();
  asm volatile("ds_read_b128 a[160:163], %0 offset:32768" :  : "v"(aA[0]) : "memory"); SB();
  asm volatile("s_waitcnt lgkmcnt(2)\n\tv_mfma_f32_16x16x32_bf16 %0, v[184:187], a[0:3], 0" : "=&v"(a1[0][0]) :  : ); SB();
  asm volatile("ds_read_b64_tr_b16 v[160:161], %0 offset:24576" :  : "v"(aW[0][1][0]) : "memory"); SB();
  asm volatile("v_mfma_f32_16x16x32_bf16 %0, v[184:187], a[4:7], 0" : "=&v"(a1[1][0]) :  : ); SB();
  asm volatile("ds_read_b64_tr_b16 v[162:163], %0 offset:25344" :  : "v"(aW[0][1][0]) : "memory"); SB();
  asm volatile("v_mfma_f32_16x16x32_bf16 %0, v[184:187], %1, 0" : "=&v"(c1[0]) : "v"(ones) : ); SB();
  asm volatile("ds_read_b64_tr_b16 v[164:165], %0 offset:24576" :  : "v"(aW[0][1][1]) : "memory"); SB();
  asm volatile("v_mfma_f32_16x16x32_bf16 %0, v[188:191], a[0:3], 0" : "=&v"(a1[0][1]) :  : ); SB();
  asm volatile("ds_read_b64_tr_b16 v[166:167], %0 offset:25344" :  : "v"(aW[0][1][1]) : "memory"); SB();
  asm volatile("v_mfma_f32_16x16x32_bf16 %0, v[188:191], a[4:7], 0" : "=&v"(a1[1][1]) :  : ); SB();
  asm volatile("ds_read_b64_tr_b16 v[168:169], %0 offset:24576" :  : "v"(aW[0][1][2]) : "memory"); SB();
  asm volatile("v_mfma_f32_16x16x32_bf16 %0, v[188:191], %1, 0" : "=&v"(c1[1]) : "v"(ones) : ); SB();
  asm volatile("ds_read_b64_tr_b16 v[170:171], %0 offset:25344" :  : "v"(aW[0][1][2]) : "memory"); SB();
  asm volatile("s_waitcnt lgkmcnt(7)\n\tv_mfma_f32_16x16x32_bf16 %0, v[192:195], a[0:3], 0" : "=&v"(a1[0][2]) :  : ); SB();
  asm volatile("ds_read_b64_tr_b16 v[172:173], %0 offset:36864" :  : "v"(aW[0][0][0]) : "memory"); SB();
  asm volatile("buffer_load_dwordx4 v[204:207], %0, %1, %2 offen offset:2048 sc1" :  : "v"(vS), "s"(rWS), "s"(0u) : "memory"); SB();
  asm volatile("v_mfma_f32_16x16x32_bf16 %0, v[192:195], a[4:7], 0" : "=&v"(a1[1][2]) :  : ); SB();
  asm volatile("ds_read_b64_tr_b16 v[174:175], %0 offset:37632" :  : "v"(aW[0][0][0]) : "memory"); SB();
  asm volatile("v_mfma_f32_16x16x32_bf16 %0, v[192:195], %1, 0" : "=&v"(c1[2]) : "v"(ones) : ); SB();
  asm volatile("ds_read_b64_tr_b16 v[176:177], %0 offset:36864" :  : "v"(aW[0][0][1]) : "memory"); SB();
  asm volatile("s_waitcnt lgkmcnt(7)\n\tv_mfma_f32_16x16x32_bf16 %0, v[160:163], a[8:11], %0" : "+v"(a1[0][0]) :  : ); SB();
  asm volatile("ds_read_b64_tr_b16 v[178:179], %0 offset:37632" :  : "v"(aW[0][0][1]) : "memory"); SB();
  asm volatile("buffer_load_dwordx4 v[208:211], %0, %1, %2 offen offset:3072 sc1" :  : "v"(vS), "s"(rWS), "s"(0u) : "memory"); SB();
  asm volatile("v_mfma_f32_16x16x32_bf16 %0, v[160:163], a[12:15], %0" : "+v"(a1[1][0]) :  : ); SB();
  asm volatile("ds_read_b64_tr_b16 v[180:181], %0 offset:36864" :  : "v"(aW[0][0][2]) : "memory"); SB();
  asm volatile("s_waitcnt lgkmcnt(7)\n\tv_mfma_f32_16x16x32_bf16 %0, v[164:167], a[8:11], %0" : "+v"(a1[0][1]) :  : ); SB();
  asm volatile("ds_read_b64_tr_b16 v[182:183], %0 offset:37632" :  : "v"(aW[0][0][2]) : "memory"); SB();
  asm volatile("v_mfma_f32_16x16x32_bf16 %0, v[164:167], a[12:15], %0" : "+v"(a1[1][1]) :  : ); SB();
  asm volatile("ds_read_b64_tr_b16 v[184:185], %0 offset:36864" :  : "v"(aW[0][1][0]) : "memory"); SB();
  asm volatile("s_waitcnt lgkmcnt(7)\n\tv_mfma_f32_16x16x32_bf16 %0, v[168:171], a[8:11], %0" : "+v"(a1[0][2]) :  : ); SB();
  asm volatile("ds_read_b64_tr_b16 v[186:187], %0 offset:37632" :  : "v"(aW[0][1][0]) : "memory"); SB();
  asm volatile("v_mfma_f32_16x16x32_bf16 %0, v[168:171], a[12:15], %0" : "+v"(a1[1][2]) :  : ); SB();
  asm volatile("ds_read_b64_tr_b16 v[188:189], %0 offset:36864" :  : "v"(aW[0][1][1]) : "memory"); SB();
  asm volatile("buffer_load_dwordx4 v[212:215], %0, %1, %2 offen offset:0 sc1" :  : "v"(vS), "s"(rWS), "s"(4096u) : "memory"); SB();
  asm volatile("s_waitcnt lgkmcnt(7)\n\tv_mfma_f32_16x16x32_bf16 %0, v[172:175], a[16:19], %0" : "+v"(a1[0][0]) :  : ); SB();
  asm volatile("ds_read_b64_tr_b16 v[190:191], %0 offset:37632" :  : "v"(aW[0][1][1]) : "memory"); SB();
  asm volatile("v_mfma_f32_16x16x32_bf16 %0, v[172:175], a[20:23], %0" : "+v"(a1[1][0]) :  : ); SB();
  asm volatile("ds_read_b64_tr_b16 v[192:193], %0 offset:36864" :  : "v"(aW[0][1][2]) : "memory"); SB();
  asm volatile("v_mfma_f32_16x16x32_bf16 %0, v[172:175], %1, %0" : "+v"(c1[0]) : "v"(ones) : ); SB();
  asm volatile("ds_read_b64_tr_b16 v[194:195], %0 offset:37632" :  : "v"(aW[0][1][2]) : "memory"); SB();
  asm volatile("s_waitcnt lgkmcnt(8)\n\tv_mfma_f32_16x16x32_bf16 %0, v[176:179], a[16:19], %0" : "+v"(a1[0][1]) :  : ); SB();
  asm volatile("s_barrier" :  :  : "memory"); SB();
  asm volatile("ds_read_b64_tr_b16 v[160:161], %0 offset:0" :  : "v"(aW[1][0][0]) : "memory"); SB();
  asm volatile("v_mfma_f32_16x16x32_bf16 %0, v[176:179], a[20:23], %0" : "+v"(a1[1][1]) :  : ); SB();
  asm volatile("ds_read_b64_tr_b16 v[162:163], %0 offset:768" :  : "v"(aW[1][0][0]) : "memory"); SB();
  asm volatile("buffer_load_dwordx4 v[216:219], %0, %1, %2 offen offset:1024 sc1" :  : "v"(vS), "s"(rWS), "s"(4096u) : "memory"); SB();
  asm volatile("v_mfma_f32_16x16x32_bf16 %0, v[176:179], %1, %0" : "+v"(c1[1]) : "v"(ones) : ); SB();
  asm volatile("ds_read_b64_tr_b16 v[164:165], %0 offset:0" :  : "v"(aW[1][0][1]) : "memory"); SB();
  asm volatile("s_waitcnt lgkmcnt(7)\n\tv_mfma_f32_16x16x32_bf16 %0, v[180:183], a[16:19], %0" : "+v"(a1[0][2]) :  : ); SB();
  asm volatile("ds_read_b64_tr_b16 v[166:167], %0 offset:768" :  : "v"(aW[1][0][1]) : "memory"); SB();
  asm volatile("v_mfma_f32_16x16x32_bf16 %0, v[180:183], a[20:23], %0" : "+v"(a1[1][2]) :  : ); SB();
  asm volatile("ds_read_b64_tr_b16 v[168:169], %0 offset:0" :  : "v"(aW[1][0][2]) : "memory"); SB();
  asm volatile("v_mfma_f32_16x16x32_bf16 %0, v[180:183], %1, %0" : "+v"(c1[2]) : "v"(ones) : ); SB();
  asm volatile("ds_read_b64_tr_b16 v[170:171], %0 offset:768" :  : "v"(aW[1][0][2]) : "memory"); SB();
  asm volatile("buffer_load_dwordx4 v[220:223], %0, %1, %2 offen offset:2048 sc1" :  : "v"(vS), "s"(rWS), "s"(4096u) : "memory"); SB();
  asm volatile("v_mfma_f32_16x16x32_bf16 %0, v[184:187], a[24:27], %0" : "+v"(a1[0][0]) :  : ); SB();
  asm volatile("ds_read_b64_tr_b16 v[172:173], %0 offset:0" :  : "v"(aW[1][1][0]) : "memory"); SB();
  asm volatile("v_mfma_f32_16x16x32_bf16 %0, v[184:187], a[28:31], %0" : "+v"(a1[1][0]) :  : ); SB();
  asm volatile("ds_read_b64_tr_b16 v[174:175], %0 offset:768" :  : "v"(aW[1][1][0]) : "memory"); SB();
  asm volatile("s_waitcnt lgkmcnt(8)\n\tv_mfma_f32_16x16x32_bf16 %0, v[188:191], a[24:27], %0" : "+v"(a1[0][1]) :  : ); SB();
  asm volatile("ds_read_b64_tr_b16 v[176:177], %0 offset:0" :  : "v"(aW[1][1][1]) : "memory"); SB();
  asm volatile("buffer_load_dwordx4 v[224:227], %0, %1, %2 offen offset:3072 sc1" :  : "v"(vS), "s"(rWS), "s"(4096u) : "memory"); SB();
  asm volatile("v_mfma_f32_16x16x32_bf16 %0, v[188:191], a[28:31], %0" : "+v"(a1[1][1]) :  : ); SB();
  asm volatile("ds_read_b64_tr_b16 v[178:179], %0 offset:768" :  : "v"(aW[1][1][1]) : "memory"); SB();
  asm volatile("v_mfma_f32_16x16x32_bf16 %0, v[192:195], a[24:27], %0" : "+v"(a1[0][2]) :  : ); SB();
  asm volatile("ds_read_b64_tr_b16 v[180:181], %0 offset:0" :  : "v"(aW[1][1][2]) : "memory"); SB();
  asm volatile("v_mfma_f32_16x16x32_bf16 %0, v[192:195], a[28:31], %0" : "+v"(a1[1][2]) :  : ); SB();
  asm volatile("ds_read_b64_tr_b16 v[182:183], %0 offset:768" :  : "v"(aW[1][1][2]) : "memory"); SB();
  asm volatile("buffer_store_dwordx4 %1, %0, %2, %3 offen offset:0 sc1\n\ts_nop 1" :  : "v"(vKV), "v"(kq[0]), "s"(rWS), "s"(0u) : "memory"); SB();
  ST(8);
  asm volatile("ds_write_b64 %0, %1 offset:0" :  : "v"(zwa[0]), "v"(((u32x2){zp[0][0][0], zp[0][0][1]})) : "memory"); SB();
  asm volatile("ds_write_b64 %0, %1 offset:32" :  : "v"(zwa[0]), "v"(((u32x2){zp[0][1][0], zp[0][1][1]})) : "memory"); SB();
  asm volatile("ds_write_b64 %0, %1 offset:64" :  : "v"(zwa[0]), "v"(((u32x2){zp[0][2][0], zp[0][2][1]})) : "memory"); SB();
  asm volatile("buffer_store_dwordx4 %1, %0, %2, %3 offen offset:0 sc1\n\ts_nop 1" :  : "v"(vKV), "v"(kq[1]), "s"(rWS), "s"(w3k[0]) : "memory"); SB();
  asm volatile("buffer_store_dwordx4 %1, %0, %2, %3 offen offset:0 sc1\n\ts_nop 1" :  : "v"(vKV), "v"(kq[2]), "s"(rWS), "s"(w3k[1]) : "memory"); SB();
  asm volatile("ds_read_b128 a[176:179], %0 offset:32768" :  : "v"(aA[1]) : "memory"); SB();
  asm volatile("s_waitcnt vmcnt(18)" :  :  : "memory"); SB();
  asm volatile("ds_read_b128 a[164:167], %0 offset:34816" :  : "v"(aA[0]) : "memory"); SB();
  asm volatile("ds_read_b128 a[180:183], %0 offset:34816" :  : "v"(aA[1]) : "memory"); SB();
  asm volatile("ds_write_b64 %0, %1 offset:0" :  : "v"(zwa[1]), "v"(((u32x2){zp[1][0][0], zp[1][0][1]})) : "memory"); SB();
  asm volatile("ds_write_b64 %0, %1 offset:32" :  : "v"(zwa[1]), "v"(((u32x2){zp[1][1][0], zp[1][1][1]})) : "memory"); SB();
  asm volatile("ds_write_b64 %0, %1 offset:64" :  : "v"(zwa[1]), "v"(((u32x2){zp[1][2][0], zp[1][2][1]})) : "memory"); SB();
  asm volatile("buffer_store_dwordx4 %1, %0, %2, %3 offen offset:0 sc1\n\ts_nop 1" :  : "v"(vWO), "v"(((u32x4){wt[0][0], wt[0][1], wt[1][0], wt[1][1]})), "s"(rWS), "s"(0u) : "memory"); SB();
  asm volatile("s_waitcnt vmcnt(17)" :  :  : "memory"); SB();
  asm volatile("ds_read_b128 a[168:171], %0 offset:36864" :  : "v"(aA[0]) : "memory"); SB();
  asm volatile("ds_read_b128 a[184:187], %0 offset:36864" :  : "v"(aA[1]) : "memory"); SB();
  asm volatile("s_waitcnt vmcnt(15)" :  :  : "memory"); SB();
  asm volatile("ds_read_b128 a[172:175], %0 offset:38912" :  : "v"(aA[0]) : "memory"); SB();
  asm volatile("ds_read_b64_tr_b16 %0, %1 offset:0" : "=&v"(zt[0][0]) : "v"(zra) : "memory"); SB();
  asm volatile("ds_read_b64_tr_b16 %0, %1 offset:416" : "=&v"(zt[0][1]) : "v"(zra) : "memory"); SB();
  asm volatile("ds_read_b64_tr_b16 %0, %1 offset:32" : "=&v"(zt[1][0]) : "v"(zra) : "memory"); SB();
  asm volatile("ds_read_b64_tr_b16 %0, %1 offset:448" : "=&v"(zt[1][1]) : "v"(zra) : "memory"); SB();
  asm volatile("ds_read_b64_tr_b16 %0, %1 offset:64" : "=&v"(zt[2][0]) : "v"(zra) : "memory"); SB();
  asm volatile("ds_read_b64_tr_b16 %0, %1 offset:480" : "=&v"(zt[2][1]) : "v"(zra) : "memory"); SB();
  asm volatile("s_waitcnt lgkmcnt(0)" : "+v"(zt[0][0]), "+v"(zt[0][1]), "+v"(zt[1][0]), "+v"(zt[1][1]), "+v"(zt[2][0]), "+v"(zt[2][1]) :  : "memory"); SB();
  asm volatile("ds_read_b128 a[188:191], %0 offset:38912" :  : "v"(aA[1]) : "memory"); SB();
  asm volatile("s_waitcnt vmcnt(13)" :  :  : "memory"); SB();
  asm volatile("ds_read_b128 a[192:195], %0 offset:0" :  : "v"(aA[0]) : "memory"); SB();
  asm volatile("ds_read_b128 a[208:211], %0 offset:0" :  : "v"(aA[1]) : "memory"); SB();
  asm volatile("s_add_u32 m0, %0, 12288" :  : "s"(mW) : "memory", "scc"); SB();
  asm volatile("s_nop 0\n\tbuffer_load_dwordx4 %0, %1, %2 offen lds" :  : "v"(voB[0]), "s"(rW1), "s"(kw_[7]) : "memory"); SB();
  asm volatile("buffer_load_dwordx4 %0, %1, %2 offen offset:1024 lds" :  : "v"(voB[1]), "s"(rW1), "s"(kw_[7]) : "memory"); SB();
  asm volatile("buffer_load_dwordx4 %0, %1, %2 offen offset:2048 lds" :  : "v"(voB[2]), "s"(rW1), "s"(kw_[7]) : "memory"); SB();
  asm volatile("s_add_u32 m0, %0, 24576" :  : "s"(mW) : "memory", "scc"); SB();
  asm volatile("s_nop 0\n\tbuffer_load_dwordx4 %0, %1, %2 offen lds" :  : "v"(voB[0]), "s"(rW1), "s"(kw_[8]) : "memory"); SB();
  asm volatile("buffer_load_dwordx4 %0, %1, %2 offen offset:1024 lds" :  : "v"(voB[1]), "s"(rW1), "s"(kw_[8]) : "memory"); SB();
  asm volatile("buffer_load_dwordx4 %0, %1, %2 offen offset:2048 lds" :  : "v"(voB[2]), "s"(rW1), "s"(kw_[8]) : "memory"); SB();
  asm volatile("s_waitcnt vmcnt(10)\n\t.Lc_%=:\n\tv_cmpx_eq_u64 vcc, %1, v[198:199]\n\tv_cmpx_eq_u64 vcc, %1, v[202:203]\n\tv_cmpx_eq_u64 vcc, %1, v[206:207]\n\tv_cmpx_eq_u64 vcc, %1, v[210:211]\n\tv_cmpx_eq_u64 vcc, %1, v[214:215]\n\tv_cmpx_eq_u64 vcc, %1, v[218:219]\n\tv_cmpx_eq_u64 vcc, %1, v[222:223]\n\tv_cmpx_eq_u64 vcc, %1, v[226:227]\n\ts_cmp_eq_u64 vcc, -1\n\ts_mov_b64 exec, -1\n\ts_cbranch_scc0 .Lr_%=\n\t.Ld_%=:\n\t.subsection 1\n\t.Lr_%=:\n\tv_add_u32 %0, -1, %0\n\ts_nop 1\n\tv_readfirstlane_b32 vcc_hi, %0\n\ts_nop 3\n\ts_cmp_lt_i32 vcc_hi, 1\n\ts_cbranch_scc1 .Ld_%=\n\ts_sleep 2\n\ts_movk_i32 vcc_lo, 0x1000\n\tbuffer_load_dwordx4 v[196:199], %2, %3, 0 offen offset:0 sc1\n\tbuffer_load_dwordx4 v[200:203], %2, %3, 0 offen offset:1024 sc1\n\tbuffer_load_dwordx4 v[204:207], %2, %3, 0 offen offset:2048 sc1\n\tbuffer_load_dwordx4 v[208:211], %2, %3, 0 offen offset:3072 sc1\n\tbuffer_load_dwordx4 v[212:215], %2, %3, vcc_lo offen offset:0 sc1\n\tbuffer_load_dwordx4 v[216:219], %2, %3, vcc_lo offen offset:1024 sc1\n\tbuffer_load_dwordx4 v[220:223], %2, %3, vcc_lo offen offset:2048 sc1\n\tbuffer_load_dwordx4 v[224:227], %2, %3, vcc_lo offen offset:3072 sc1\n\ts_waitcnt vmcnt(0)\n\ts_branch .Lc_%=\n\t.subsection 0" : "+v"(tmo) : "s"(tg), "v"(vS), "s"(rWS) : "memory", "vcc", "scc"); SB();
  ST(9);
  {
    f32x2 s4[4], dm[4];
  asm volatile("v_pk_add_f32 %0, v[196:197], v[200:201]\n\tv_pk_add_f32 %1, v[204:205], v[208:209]\n\tv_pk_add_f32 %2, v[212:213], v[216:217]\n\tv_pk_add_f32 %3, v[220:221], v[224:225]" : "=&v"(s4[0]), "=&v"(s4[1]), "=&v"(s4[2]), "=&v"(s4[3]) :  : ); SB();
    const f32x2 sm = (s4[0] + s4[1]) + (s4[2] + s4[3]);
    const float mean = (sm[0] + dppf<0x128>(sm[0])) * (1.0f / 16.0f);
  asm volatile("v_sub_f32 %0, v196, %8\n\tv_sub_f32 %1, v200, %8\n\tv_sub_f32 %2, v204, %8\n\tv_sub_f32 %3, v208, %8\n\tv_sub_f32 %4, v212, %8\n\tv_sub_f32 %5, v216, %8\n\tv_sub_f32 %6, v220, %8\n\tv_sub_f32 %7, v224, %8" : "=&v"(dm[0][0]), "=&v"(dm[0][1]), "=&v"(dm[1][0]), "=&v"(dm[1][1]), "=&v"(dm[2][0]), "=&v"(dm[2][1]), "=&v"(dm[3][0]), "=&v"(dm[3][1]) : "v"(mean) : ); SB();
    const f32x2 dq = __builtin_elementwise_fma(dm[0], dm[0], dm[1] * dm[1]) + __builtin_elementwise_fma(dm[2], dm[2], dm[3] * dm[3]);
    float qs = __builtin_fmaf(dq[0] + dq[1], 48.0f, sm[1]);
    qs += dppf<0x128>(qs);
    float r = __builtin_amdgcn_rcpf(rbf(__builtin_amdgcn_sqrtf(rbf(rbf(qs * (1.0f / 768.0f)) + 1e-5f))));
    float mr = -rbf(mean) * r;
    asm volatile("" : "+v"(r), "+v"(mr)); SB();
  asm volatile("s_add_u32 m0, %0, 98304" :  : "s"(mA) : "memory", "scc"); SB();
  asm volatile("s_nop 0\n\tbuffer_load_dwordx4 %0, %1, %2 offen offset:3072 lds" :  : "v"(voS), "s"(rA), "s"(ao[3]) : "memory"); SB();
  asm volatile("s_add_u32 m0, %0, 102400" :  : "s"(mA) : "memory", "scc"); SB();
  asm volatile("s_nop 0\n\tbuffer_load_dwordx4 %0, %1, %2 offen lds" :  : "v"(voS), "s"(rA), "s"(ao[4]) : "memory"); SB();
    const f32x2 re[4] = {{dppf<0x150>(r), dppf<0x151>(r)}, {dppf<0x152>(r), dppf<0x153>(r)}, {dppf<0x154>(r), dppf<0x155>(r)}, {dppf<0x156>(r), dppf<0x157>(r)}};
    const f32x2 me[4] = {{dppf<0x150>(mr), dppf<0x151>(mr)}, {dppf<0x152>(mr), dppf<0x153>(mr)}, {dppf<0x154>(mr), dppf<0x155>(mr)}, {dppf<0x156>(mr), dppf<0x157>(mr)}};
    u32x4 zo[3];
#pragma unroll
    for (int e = 0; e < 4; ++e) zo[0][e] = pk2(__builtin_elementwise_fma(unpk(zt[0][e >> 1][e & 1]), re[e], me[e]));
    SB();
  asm volatile("buffer_store_dwordx4 %1, %0, %2, %3 offen offset:0 sc1\n\ts_nop 1" :  : "v"(vZ_), "v"(zo[0]), "s"(rWS), "s"(0u) : "memory"); SB();
#pragma unroll
    for (int e = 0; e < 4; ++e) zo[1][e] = pk2(__builtin_elementwise_fma(unpk(zt[1][e >> 1][e & 1]), re[e], me[e]));
    SB();
  asm volatile("buffer_store_dwordx4 %1, %0, %2, %3 offen offset:1024 sc1\n\ts_nop 1" :  : "v"(vZ_), "v"(zo[1]), "s"(rWS), "s"(0u) : "memory"); SB();
#pragma unroll
    for (int e = 0; e < 4; ++e) zo[2][e] = pk2(__builtin_elementwise_fma(unpk(zt[2][e >> 1][e & 1]), re[e], me[e]));
    SB();
  asm volatile("buffer_store_dwordx4 %1, %0, %2, %3 offen offset:2048 sc1\n\ts_nop 1" :  : "v"(vZ_), "v"(zo[2]), "s"(rWS), "s"(0u) : "memory"); SB();
  }
  ST(10);
  asm volatile("v_mfma_f32_16x16x32_bf16 %0, v[160:163], a[32:35], %0" : "+v"(a1[0][0]) :  : ); SB();
  asm volatile("ds_read_b64_tr_b16 v[184:185], %0 offset:12288" :  : "v"(aW[1][0][0]) : "memory"); SB();
  asm volatile("v_mfma_f32_16x16x32_bf16 %0, v[160:163], a[36:39], %0" : "+v"(a1[1][0]) :  : ); SB();
  asm volatile("ds_read_b64_tr_b16 v[186:187], %0 offset:13056" :  : "v"(aW[1][0][0]) : "memory"); SB();
  asm volatile("v_mfma_f32_16x16x32_bf16 %0, v[160:163], %1, %0" : "+v"(c1[0]) : "v"(ones) : ); SB();
  asm volatile("ds_read_b64_tr_b16 v[188:189], %0 offset:12288" :  : "v"(aW[1][0][1]) : "memory"); SB();
  asm volatile("v_mfma_f32_16x16x32_bf16 %0, v[164:167], a[32:35], %0" : "+v"(a1[0][1]) :  : ); SB();
  asm volatile("ds_read_b64_tr_b16 v[190:191], %0 offset:13056" :  : "v"(aW[1][0][1]) : "memory"); SB();
  asm volatile("v_mfma_f32_16x16x32_bf16 %0, v[164:167], a[36:39], %0" : "+v"(a1[1][1]) :  : ); SB();
  asm volatile("ds_read_b64_tr_b16 v[192:193], %0 offset:12288" :  : "v"(aW[1][0][2]) : "memory"); SB();
  asm volatile("v_mfma_f32_16x16x32_bf16 %0, v[164:167], %1, %0" : "+v"(c1[1]) : "v"(ones) : ); SB();
  asm volatile("ds_read_b64_tr_b16 v[194:195], %0 offset:13056" :  : "v"(aW[1][0][2]) : "memory"); SB();
  asm volatile("v_mfma_f32_16x16x32_bf16 %0, v[168:171], a[32:35], %0" : "+v"(a1[0][2]) :  : ); SB();
  ca = lb + 57344 + w * 192 + g4 * 16; asm volatile("" : "+v"(ca)); SB();
  asm volatile("v_mfma_f32_16x16x32_bf16 %0, v[168:171], a[36:39], %0" : "+v"(a1[1][2]) :  : ); SB();
  cr = lb + 57344 + (w ^ 2) * 192 + g4 * 16; asm volatile("" : "+v"(cr)); SB();
  asm volatile("v_mfma_f32_16x16x32_bf16 %0, v[168:171], %1, %0" : "+v"(c1[2]) : "v"(ones) : ); SB();
  asm volatile("buffer_load_dwordx4 %0, %1, %2 offen offset:1024 lds" :  : "v"(voS), "s"(rA), "s"(ao[5]) : "memory"); SB();
  asm volatile("v_mfma_f32_16x16x32_bf16 %0, v[172:175], a[40:43], %0" : "+v"(a1[0][0]) :  : ); SB();
  asm volatile("ds_read_b64_tr_b16 v[160:161], %0 offset:12288" :  : "v"(aW[1][1][0]) : "memory"); SB();
  asm volatile("s_waitcnt vmcnt(12)\n\ts_mov_b64 exec, 1\n\tbuffer_store_dwordx2 %0, off, %1, %2 sc1\n\ts_mov_b64 exec, -1" :  : "v"(((u32x2){tg0, tg1})), "s"(rWS), "s"(sFK) : "memory"); SB();
  asm volatile("v_mfma_f32_16x16x32_bf16 %0, v[172:175], a[44:47], %0" : "+v"(a1[1][0]) :  : ); SB();
  asm volatile("ds_read_b64_tr_b16 v[162:163], %0 offset:13056" :  : "v"(aW[1][1][0]) : "memory"); SB();
  asm volatile("v_lshlrev_b32 %0, 16, v245" : "=v"(dD[0]) :  : ); SB();
  asm volatile("v_mfma_f32_16x16x32_bf16 %0, v[176:179], a[40:43], %0" : "+v"(a1[0][1]) :  : ); SB();
  asm volatile("ds_read_b64_tr_b16 v[164:165], %0 offset:12288" :  : "v"(aW[1][1][1]) : "memory"); SB();
  asm volatile("v_lshlrev_b32 %0, 16, v246" : "=v"(dD[1]) :  : ); SB();
  asm volatile("v_mfma_f32_16x16x32_bf16 %0, v[176:179], a[44:47], %0" : "+v"(a1[1][1]) :  : ); SB();
  asm volatile("ds_read_b64_tr_b16 v[166:167], %0 offset:13056" :  : "v"(aW[1][1][1]) : "memory"); SB();
  asm volatile("v_lshlrev_b32 %0, 16, v247" : "=v"(dD[2]) :  : ); SB();
  asm volatile("v_mfma_f32_16x16x32_bf16 %0, v[180:183], a[40:43], %0" : "+v"(a1[0][2]) :  : ); SB();
  asm volatile("ds_read_b64_tr_b16 v[168:169], %0 offset:12288" :  : "v"(aW[1][1][2]) : "memory"); SB();
  asm volatile("v_lshlrev_b32 %0, 16, v248" : "=v"(bs) :  : ); SB();
  asm volatile("v_mfma_f32_16x16x32_bf16 %0, v[180:183], a[44:47], %0" : "+v"(a1[1][2]) :  : ); SB();
  asm volatile("ds_read_b64_tr_b16 v[170:171], %0 offset:13056" :  : "v"(aW[1][1][2]) : "memory"); SB();
  asm volatile("s_waitcnt lgkmcnt(6)\n\tv_mfma_f32_16x16x32_bf16 %0, v[184:187], a[48:51], %0" : "+v"(a1[0][0]) :  : ); SB();
  asm volatile("s_barrier" :  :  : "memory"); SB();
  asm volatile("ds_read_b64_tr_b16 v[172:173], %0 offset:24576" :  : "v"(aW[1][0][0]) : "memory"); SB();
  asm volatile("s_add_u32 m0, %0, 36864" :  : "s"(mW) : "memory", "scc"); SB();
  asm volatile("v_mfma_f32_16x16x32_bf16 %0, v[184:187], a[52:55], %0" : "+v"(a1[1][0]) :  : ); SB();
  asm volatile("ds_read_b64_tr_b16 v[174:175], %0 offset:25344" :  : "v"(aW[1][0][0]) : "memory"); SB();
  asm volatile("buffer_load_dwordx4 %0, %1, %2 offen lds" :  : "v"(voB[0]), "s"(rW1), "s"(kw_[9]) : "memory"); SB();
  asm volatile("v_mfma_f32_16x16x32_bf16 %0, v[184:187], %1, %0" : "+v"(c1[0]) : "v"(ones) : ); SB();
  asm volatile("ds_read_b64_tr_b16 v[176:177], %0 offset:24576" :  : "v"(aW[1][0][1]) : "memory"); SB();
  asm volatile("v_mov_b32_dpp v242, v231 wave_shr:1 row_mask:0xf bank_mask:0xf" :  :  : ); SB();
  asm volatile("v_mfma_f32_16x16x32_bf16 %0, v[188:191], a[48:51], %0" : "+v"(a1[0][1]) :  : ); SB();
  asm volatile("ds_read_b64_tr_b16 v[178:179], %0 offset:25344" :  : "v"(aW[1][0][1]) : "memory"); SB();
  asm volatile("v_alignbit_b32 %0, v231, v231, 16" : "=v"(im[0][0]) :  : ); SB();
  asm volatile("v_mfma_f32_16x16x32_bf16 %0, v[188:191], a[52:55], %0" : "+v"(a1[1][1]) :  : ); SB();
  asm volatile("ds_read_b64_tr_b16 v[180:181], %0 offset:24576" :  : "v"(aW[1][0][2]) : "memory"); SB();
  asm volatile("buffer_load_dwordx4 %0, %1, %2 offen offset:1024 lds" :  : "v"(voB[1]), "s"(rW1), "s"(kw_[9]) : "memory"); SB();
  asm volatile("v_mfma_f32_16x16x32_bf16 %0, v[188:191], %1, %0" : "+v"(c1[1]) : "v"(ones) : ); SB();
  asm volatile("ds_read_b64_tr_b16 v[182:183], %0 offset:25344" :  : "v"(aW[1][0][2]) : "memory"); SB();
  asm volatile("v_alignbit_b32 %0, v230, v230, 16" : "=v"(im[0][1]) :  : ); SB();
  asm volatile("v_mfma_f32_16x16x32_bf16 %0, v[192:195], a[48:51], %0" : "+v"(a1[0][2]) :  : ); SB();
  asm volatile("buffer_load_dwordx4 %0, %1, %2 offen offset:2048 lds" :  : "v"(voB[2]), "s"(rW1), "s"(kw_[9]) : "memory"); SB();
  asm volatile("v_alignbit_b32 %0, v229, v229, 16" : "=v"(im[0][2]) :  : ); SB();
  asm volatile("v_mfma_f32_16x16x32_bf16 %0, v[192:195], a[52:55], %0" : "+v"(a1[1][2]) :  : ); SB();
  asm volatile("v_alignbit_b32 %0, v228, v228, 16" : "=v"(im[0][3]) :  : ); SB();
  asm volatile("v_mfma_f32_16x16x32_bf16 %0, v[192:195], %1, %0" : "+v"(c1[2]) : "v"(ones) : ); SB();
  asm volatile("v_bfi_b32 %0, %1, v231, v230" : "=v"(im[1][0]) : "s"(0xFFFFu) : ); SB();
  asm volatile("s_waitcnt lgkmcnt(6)\n\tv_mfma_f32_16x16x32_bf16 %0, v[160:163], a[56:59], %0" : "+v"(a1[0][0]) :  : ); SB();
  asm volatile("ds_read_b64_tr_b16 v[184:185], %0 offset:24576" :  : "v"(aW[1][1][0]) : "memory"); SB();
  asm volatile("s_add_u32 m0, %0, 102400" :  : "s"(mA) : "memory", "scc"); SB();
  asm volatile("v_mfma_f32_16x16x32_bf16 %0, v[160:163], a[60:63], %0" : "+v"(a1[1][0]) :  : ); SB();
  asm volatile("ds_read_b64_tr_b16 v[186:187], %0 offset:25344" :  : "v"(aW[1][1][0]) : "memory"); SB();
  asm volatile("v_bfi_b32 %0, %1, v230, v229" : "=v"(im[1][1]) : "s"(0xFFFFu) : ); SB();
  asm volatile("v_mfma_f32_16x16x32_bf16 %0, v[164:167], a[56:59], %0" : "+v"(a1[0][1]) :  : ); SB();
  asm volatile("ds_read_b64_tr_b16 v[188:189], %0 offset:24576" :  : "v"(aW[1][1][1]) : "memory"); SB();
  asm volatile("v_bfi_b32 %0, %1, v229, v228" : "=v"(im[1][2]) : "s"(0xFFFFu) : ); SB();
  asm volatile("v_mfma_f32_16x16x32_bf16 %0, v[164:167], a[60:63], %0" : "+v"(a1[1][1]) :  : ); SB();
  asm volatile("ds_read_b64_tr_b16 v[190:191], %0 offset:25344" :  : "v"(aW[1][1][1]) : "memory"); SB();
  asm volatile("v_bfi_b32 %0, %1, v228, v242" : "=v"(im[1][3]) : "s"(0xFFFFu) : ); SB();
  asm volatile("v_mfma_f32_16x16x32_bf16 %0, v[168:171], a[56:59], %0" : "+v"(a1[0][2]) :  : ); SB();
  asm volatile("ds_read_b64_tr_b16 v[192:193], %0 offset:24576" :  : "v"(aW[1][1][2]) : "memory"); SB();
  asm volatile("buffer_load_dwordx4 %0, %1, %2 offen offset:2048 lds" :  : "v"(voS), "s"(rA), "s"(ao[6]) : "memory"); SB();
  asm volatile("v_mfma_f32_16x16x32_bf16 %0, v[168:171], a[60:63], %0" : "+v"(a1[1][2]) :  : ); SB();
  asm volatile("ds_read_b64_tr_b16 v[194:195], %0 offset:25344" :  : "v"(aW[1][1][2]) : "memory"); SB();
  asm volatile("s_waitcnt lgkmcnt(6)\n\tv_mfma_f32_16x16x32_bf16 %0, v[172:175], a[64:67], %0" : "+v"(a1[0][0]) :  : ); SB();
  asm volatile("ds_read_b64_tr_b16 v[160:161], %0 offset:36864" :  : "v"(aW[1][0][0]) : "memory"); SB();
  asm volatile("v_mov_b32_dpp v243, v235 wave_shr:1 row_mask:0xf bank_mask:0xf" :  :  : ); SB();
  asm volatile("v_mfma_f32_16x16x32_bf16 %0, v[172:175], a[68:71], %0" : "+v"(a1[1][0]) :  : ); SB();
  asm volatile("ds_read_b64_tr_b16 v[162:163], %0 offset:37632" :  : "v"(aW[1][0][0]) : "memory"); SB();
  asm volatile("v_alignbit_b32 %0, v235, v235, 16" : "=v"(im[2][0]) :  : ); SB();
  asm volatile("v_mfma_f32_16x16x32_bf16 %0, v[172:175], %1, %0" : "+v"(c1[0]) : "v"(ones) : ); SB();
  asm volatile("ds_read_b64_tr_b16 v[164:165], %0 offset:36864" :  : "v"(aW[1][0][1]) : "memory"); SB();
  asm volatile("v_alignbit_b32 %0, v234, v234, 16" : "=v"(im[2][1]) :  : ); SB();
  asm volatile("v_mfma_f32_16x16x32_bf16 %0, v[176:179], a[64:67], %0" : "+v"(a1[0][1]) :  : ); SB();
  asm volatile("ds_read_b64_tr_b16 v[166:167], %0 offset:37632" :  : "v"(aW[1][0][1]) : "memory"); SB();
  asm volatile("buffer_load_dwordx4 %0, %1, %2 offen offset:3072 lds" :  : "v"(voS), "s"(rA), "s"(ao[7]) : "memory"); SB();
  asm volatile("v_mfma_f32_16x16x32_bf16 %0, v[176:179], a[68:71], %0" : "+v"(a1[1][1]) :  : ); SB();
  asm volatile("ds_read_b64_tr_b16 v[168:169], %0 offset:36864" :  : "v"(aW[1][0][2]) : "memory"); SB();
  asm volatile("v_alignbit_b32 %0, v233, v233, 16" : "=v"(im[2][2]) :  : ); SB();
  asm volatile("v_mfma_f32_16x16x32_bf16 %0, v[176:179], %1, %0" : "+v"(c1[1]) : "v"(ones) : ); SB();
  asm volatile("ds_read_b64_tr_b16 v[170:171], %0 offset:37632" :  : "v"(aW[1][0][2]) : "memory"); SB();
  asm volatile("v_alignbit_b32 %0, v232, v232, 16" : "=v"(im[2][3]) :  : ); SB();
  asm volatile("v_mfma_f32_16x16x32_bf16 %0, v[180:183], a[64:67], %0" : "+v"(a1[0][2]) :  : ); SB();
  asm volatile("s_waitcnt vmcnt(11)" :  :  : "memory"); SB();
  asm volatile("ds_read_b128 a[196:199], %0 offset:2048" :  : "v"(aA[0]) : "memory"); SB();
  asm volatile("v_mfma_f32_16x16x32_bf16 %0, v[180:183], a[68:71], %0" : "+v"(a1[1][2]) :  : ); SB();
  asm volatile("ds_read_b128 a[212:215], %0 offset:2048" :  : "v"(aA[1]) : "memory"); SB();
  asm volatile("v_bfi_b32 %0, %1, v235, v234" : "=v"(im[3][0]) : "s"(0xFFFFu) : ); SB();
  asm volatile("v_mfma_f32_16x16x32_bf16 %0, v[180:183], %1, %0" : "+v"(c1[2]) : "v"(ones) : ); SB();
  asm volatile("v_xor_b32 %0, 384, %0" : "+v"(voS) :  : "memory"); SB();
  asm volatile("s_waitcnt lgkmcnt(8)\n\tv_mfma_f32_16x16x32_bf16 %0, v[184:187], a[72:75], %0" : "+v"(a1[0][0]) :  : ); SB();
  asm volatile("ds_read_b64_tr_b16 v[172:173], %0 offset:36864" :  : "v"(aW[1][1][0]) : "memory"); SB();
  asm volatile("s_add_u32 m0, %0, 131072" :  : "s"(mA) : "memory", "scc"); SB();
  asm volatile("v_mfma_f32_16x16x32_bf16 %0, v[184:187], a[76:79], %0" : "+v"(a1[1][0]) :  : ); SB();
  asm volatile("ds_read_b64_tr_b16 v[174:175], %0 offset:37632" :  : "v"(aW[1][1][0]) : "memory"); SB();
  asm volatile("buffer_load_dwordx4 %0, %1, %2 offen lds" :  : "v"(voS), "s"(rA), "s"(ao[0]) : "memory"); SB();
  asm volatile("v_mfma_f32_16x16x32_bf16 %0, v[188:191], a[72:75], %0" : "+v"(a1[0][1]) :  : ); SB();
  asm volatile("ds_read_b64_tr_b16 v[176:177], %0 offset:36864" :  : "v"(aW[1][1][1]) : "memory"); SB();
  asm volatile("buffer_load_dwordx4 %0, %1, %2 offen offset:1024 lds" :  : "v"(voS), "s"(rA), "s"(ao[1]) : "memory"); SB();
  asm volatile("v_mfma_f32_16x16x32_bf16 %0, v[188:191], a[76:79], %0" : "+v"(a1[1][1]) :  : ); SB();
  asm volatile("ds_read_b64_tr_b16 v[178:179], %0 offset:37632" :  : "v"(aW[1][1][1]) : "memory"); SB();
  asm volatile("v_bfi_b32 %0, %1, v234, v233" : "=v"(im[3][1]) : "s"(0xFFFFu) : ); SB();
  asm volatile("v_mfma_f32_16x16x32_bf16 %0, v[192:195], a[72:75], %0" : "+v"(a1[0][2]) :  : ); SB();
  asm volatile("ds_read_b64_tr_b16 v[180:181], %0 offset:36864" :  : "v"(aW[1][1][2]) : "memory"); SB();
  asm volatile("v_bfi_b32 %0, %1, v233, v232" : "=v"(im[3][2]) : "s"(0xFFFFu) : ); SB();
  asm volatile("v_mfma_f32_16x16x32_bf16 %0, v[192:195], a[76:79], %0" : "+v"(a1[1][2]) :  : ); SB();
  asm volatile("ds_read_b64_tr_b16 v[182:183], %0 offset:37632" :  : "v"(aW[1][1][2]) : "memory"); SB();
  asm volatile("s_waitcnt lgkmcnt(8)\n\tv_mfma_f32_16x16x32_bf16 %0, v[160:163], a[80:83], %0" : "+v"(a1[0][0]) :  : ); SB();
  asm volatile("s_barrier" :  :  : "memory"); SB();
  asm volatile("ds_read_b64_tr_b16 v[184:185], %0 offset:0" :  : "v"(aW[0][0][0]) : "memory"); SB();
  asm volatile("v_bfi_b32 %0, %1, v232, v243" : "=v"(im[3][3]) : "s"(0xFFFFu) : ); SB();
  asm volatile("v_mfma_f32_16x16x32_bf16 %0, v[160:163], a[84:87], %0" : "+v"(a1[1][0]) :  : ); SB();
  asm volatile("ds_read_b64_tr_b16 v[186:187], %0 offset:768" :  : "v"(aW[0][0][0]) : "memory"); SB();
  asm volatile("v_mov_b32_dpp v244, v239 wave_shr:1 row_mask:0xf bank_mask:0xf" :  :  : ); SB();
  asm volatile("v_mfma_f32_16x16x32_bf16 %0, v[160:163], %1, %0" : "+v"(c1[0]) : "v"(ones) : ); SB();
  asm volatile("ds_read_b64_tr_b16 v[188:189], %0 offset:0" :  : "v"(aW[0][0][1]) : "memory"); SB();
  asm volatile("v_alignbit_b32 %0, v239, v239, 16" : "=v"(im[4][0]) :  : ); SB();
  asm volatile("v_mfma_f32_16x16x32_bf16 %0, v[164:167], a[80:83], %0" : "+v"(a1[0][1]) :  : ); SB();
  asm volatile("ds_read_b64_tr_b16 v[190:191], %0 offset:768" :  : "v"(aW[0][0][1]) : "memory"); SB();
  asm volatile("v_alignbit_b32 %0, v238, v238, 16" : "=v"(im[4][1]) :  : ); SB();
  asm volatile("v_mfma_f32_16x16x32_bf16 %0, v[164:167], a[84:87], %0" : "+v"(a1[1][1]) :  : ); SB();
  asm volatile("ds_read_b64_tr_b16 v[192:193], %0 offset:0" :  : "v"(aW[0][0][2]) : "memory"); SB();
  asm volatile("v_alignbit_b32 %0, v237, v237, 16" : "=v"(im[4][2]) :  : ); SB();
  asm volatile("v_mfma_f32_16x16x32_bf16 %0, v[164:167], %1, %0" : "+v"(c1[1]) : "v"(ones) : ); SB();
  asm volatile("ds_read_b64_tr_b16 v[194:195], %0 offset:768" :  : "v"(aW[0][0][2]) : "memory"); SB();
  asm volatile("buffer_load_dwordx4 %0, %1, %2 offen offset:2048 lds" :  : "v"(voS), "s"(rA), "s"(ao[2]) : "memory"); SB();
  asm volatile("v_mfma_f32_16x16x32_bf16 %0, v[168:171], a[80:83], %0" : "+v"(a1[0][2]) :  : ); SB();
  asm volatile("buffer_load_dwordx4 %0, %1, %2 offen offset:3072 lds" :  : "v"(voS), "s"(rA), "s"(ao[3]) : "memory"); SB();
  asm volatile("v_alignbit_b32 %0, v236, v236, 16" : "=v"(im[4][3]) :  : ); SB();
  asm volatile("v_mfma_f32_16x16x32_bf16 %0, v[168:171], a[84:87], %0" : "+v"(a1[1][2]) :  : ); SB();
  asm volatile("v_bfi_b32 %0, %1, v239, v238" : "=v"(im[5][0]) : "s"(0xFFFFu) : ); SB();
  asm volatile("v_mfma_f32_16x16x32_bf16 %0, v[168:171], %1, %0" : "+v"(c1[2]) : "v"(ones) : ); SB();
  asm volatile("v_bfi_b32 %0, %1, v238, v237" : "=v"(im[5][1]) : "s"(0xFFFFu) : ); SB();
  asm volatile("s_waitcnt lgkmcnt(5)\n\tv_mfma_f32_16x16x32_bf16 %0, v[172:175], a[88:91], %0" : "+v"(a1[0][0]) :  : ); SB();
  asm volatile("ds_read_b64_tr_b16 v[160:161], %0 offset:0" :  : "v"(aW[0][1][0]) : "memory"); SB();
  asm volatile("s_add_u32 m0, %0, 135168" :  : "s"(mA) : "memory", "scc"); SB();
  asm volatile("v_mfma_f32_16x16x32_bf16 %0, v[172:175], a[92:95], %0" : "+v"(a1[1][0]) :  : ); SB();
  asm volatile("ds_read_b64_tr_b16 v[162:163], %0 offset:768" :  : "v"(aW[0][1][0]) : "memory"); SB();
  asm volatile("v_bfi_b32 %0, %1, v237, v236" : "=v"(im[5][2]) : "s"(0xFFFFu) : ); SB();
  asm volatile("v_mfma_f32_16x16x32_bf16 %0, v[176:179], a[88:91], %0" : "+v"(a1[0][1]) :  : ); SB();
  asm volatile("ds_read_b64_tr_b16 v[164:165], %0 offset:0" :  : "v"(aW[0][1][1]) : "memory"); SB();
  asm volatile("v_bfi_b32 %0, %1, v236, v244" : "=v"(im[5][3]) : "s"(0xFFFFu) : ); SB();
  asm volatile("v_mfma_f32_16x16x32_bf16 %0, v[176:179], a[92:95], %0" : "+v"(a1[1][1]) :  : ); SB();
  asm volatile("ds_read_b64_tr_b16 v[166:167], %0 offset:768" :  : "v"(aW[0][1][1]) : "memory"); SB();
  asm volatile("v_mov_b32 %0, 0" : "=v"(zero4[0]) :  : ); SB();
  asm volatile("v_mfma_f32_16x16x32_bf16 %0, v[180:183], a[88:91], %0" : "+v"(a1[0][2]) :  : ); SB();
  asm volatile("ds_read_b64_tr_b16 v[168:169], %0 offset:0" :  : "v"(aW[0][1][2]) : "memory"); SB();
  asm volatile("v_mov_b32 %0, 0" : "=v"(zero4[1]) :  : ); SB();
  asm volatile("v_mfma_f32_16x16x32_bf16 %0, v[180:183], a[92:95], %0" : "+v"(a1[1][2]) :  : ); SB();
  asm volatile("ds_read_b64_tr_b16 v[170:171], %0 offset:768" :  : "v"(aW[0][1][2]) : "memory"); SB();
  asm volatile("s_waitcnt lgkmcnt(6)\n\tv_mfma_f32_16x16x32_bf16 %0, v[184:187], a[96:99], %0" : "+v"(a1[0][0]) :  : ); SB();
  asm volatile("ds_read_b64_tr_b16 v[172:173], %0 offset:12288" :  : "v"(aW[0][0][0]) : "memory"); SB();
  asm volatile("v_mov_b32 %0, 0" : "=v"(zero4[2]) :  : ); SB();
  asm volatile("v_mfma_f32_16x16x32_bf16 %0, v[184:187], a[100:103], %0" : "+v"(a1[1][0]) :  : ); SB();
  asm volatile("ds_read_b64_tr_b16 v[174:175], %0 offset:13056" :  : "v"(aW[0][0][0]) : "memory"); SB();
  asm volatile("v_mov_b32 %0, 0" : "=v"(zero4[3]) :  : ); SB();
  asm volatile("v_mfma_f32_16x16x32_bf16 %0, v[184:187], %1, %0" : "+v"(c1[0]) : "v"(ones) : ); SB();
  asm volatile("ds_read_b64_tr_b16 v[176:177], %0 offset:12288" :  : "v"(aW[0][0][1]) : "memory"); SB();
  asm volatile("v_mfma_f32_16x16x32_bf16 %0, v[188:191], a[96:99], %0" : "+v"(a1[0][1]) :  : ); SB();
  asm volatile("ds_read_b64_tr_b16 v[178:179], %0 offset:13056" :  : "v"(aW[0][0][1]) : "memory"); SB();
  asm volatile("v_mfma_f32_16x16x32_bf16 %0, v[188:191], a[100:103], %0" : "+v"(a1[1][1]) :  : ); SB();
  asm volatile("ds_read_b64_tr_b16 v[180:181], %0 offset:12288" :  : "v"(aW[0][0][2]) : "memory"); SB();
  asm volatile("v_mfma_f32_16x16x32_bf16 %0, v[188:191], %1, %0" : "+v"(c1[1]) : "v"(ones) : ); SB();
  asm volatile("ds_read_b64_tr_b16 v[182:183], %0 offset:13056" :  : "v"(aW[0][0][2]) : "memory"); SB();
  asm volatile("v_mfma_f32_16x16x32_bf16 %0, v[192:195], a[96:99], %0" : "+v"(a1[0][2]) :  : ); SB();
  asm volatile("buffer_load_dwordx4 %0, %1, %2 offen lds" :  : "v"(voS), "s"(rA), "s"(ao[4]) : "memory"); SB();
  asm volatile("v_mfma_f32_16x16x32_bf16 %0, v[192:195], a[100:103], %0" : "+v"(a1[1][2]) :  : ); SB();
  asm volatile("buffer_load_dwordx4 %0, %1, %2 offen offset:1024 lds" :  : "v"(voS), "s"(rA), "s"(ao[5]) : "memory"); SB();
  asm volatile("v_mfma_f32_16x16x32_bf16 %0, v[192:195], %1, %0" : "+v"(c1[2]) : "v"(ones) : ); SB();
  asm volatile("s_waitcnt lgkmcnt(6)\n\tv_mfma_f32_16x16x32_bf16 %0, v[160:163], a[104:107], %0" : "+v"(a1[0][0]) :  : ); SB();
  asm volatile("ds_read_b64_tr_b16 v[184:185], %0 offset:12288" :  : "v"(aW[0][1][0]) : "memory"); SB();
  asm volatile("v_mfma_f32_16x16x32_bf16 %0, v[160:163], a[108:111], %0" : "+v"(a1[1][0]) :  : ); SB();
  asm volatile("s_mov_b32 exec_lo, 0x10001\n\ts_mov_b32 exec_hi, 0x10001\n\tds_write_b128 %0, %1 offset:0\n\ts_mov_b64 exec, -1" :  : "v"(ca), "v"(c1[0]) : "memory"); SB();
  asm volatile("ds_read_b64_tr_b16 v[186:187], %0 offset:13056" :  : "v"(aW[0][1][0]) : "memory"); SB();
  asm volatile("v_mfma_f32_16x16x32_bf16 %0, v[164:167], a[104:107], %0" : "+v"(a1[0][1]) :  : ); SB();
  asm volatile("ds_read_b64_tr_b16 v[188:189], %0 offset:12288" :  : "v"(aW[0][1][1]) : "memory"); SB();
  asm volatile("v_mfma_f32_16x16x32_bf16 %0, v[164:167], a[108:111], %0" : "+v"(a1[1][1]) :  : ); SB();
  asm volatile("s_mov_b32 exec_lo, 0x10001\n\ts_mov_b32 exec_hi, 0x10001\n\tds_write_b128 %0, %1 offset:64\n\ts_mov_b64 exec, -1" :  : "v"(ca), "v"(c1[1]) : "memory"); SB();
  asm volatile("ds_read_b64_tr_b16 v[190:191], %0 offset:13056" :  : "v"(aW[0][1][1]) : "memory"); SB();
  asm volatile("s_waitcnt vmcnt(13)\n\ts_mov_b64 exec, 1\n\tbuffer_store_dwordx2 %0, off, %1, %2 sc1\n\ts_mov_b64 exec, -1" :  : "v"(((u32x2){tg0, tg1})), "s"(rWS), "s"(sFZ) : "memory"); SB();
  asm volatile("v_mfma_f32_16x16x32_bf16 %0, v[168:171], a[104:107], %0" : "+v"(a1[0][2]) :  : ); SB();
  asm volatile("ds_read_b64_tr_b16 v[192:193], %0 offset:12288" :  : "v"(aW[0][1][2]) : "memory"); SB();
  asm volatile("s_waitcnt vmcnt(13)" :  :  : "memory"); SB();
  asm volatile("v_mfma_f32_16x16x32_bf16 %0, v[168:171], a[108:111], %0" : "+v"(a1[1][2]) :  : ); SB();
  asm volatile("s_mov_b32 exec_lo, 0x10001\n\ts_mov_b32 exec_hi, 0x10001\n\tds_write_b128 %0, %1 offset:128\n\ts_mov_b64 exec, -1" :  : "v"(ca), "v"(c1[2]) : "memory"); SB();
  asm volatile("ds_read_b64_tr_b16 v[194:195], %0 offset:13056" :  : "v"(aW[0][1][2]) : "memory"); SB();
  asm volatile("s_waitcnt lgkmcnt(5)\n\tv_mfma_f32_16x16x32_bf16 %0, v[172:175], a[112:115], %0" : "+v"(a1[0][0]) :  : ); SB();
  asm volatile("ds_read_b128 a[200:203], %0 offset:4096" :  : "v"(aA[0]) : "memory"); SB();
  asm volatile("v_mfma_f32_16x16x32_bf16 %0, v[172:175], a[116:119], %0" : "+v"(a1[1][0]) :  : ); SB();
  asm volatile("ds_read_b128 a[216:219], %0 offset:4096" :  : "v"(aA[1]) : "memory"); SB();
  asm volatile("v_mfma_f32_16x16x32_bf16 %0, v[172:175], %1, %0" : "+v"(c1[0]) : "v"(ones) : ); SB();
  asm volatile("buffer_load_dwordx4 %0, %1, %2 offen offset:2048 lds" :  : "v"(voS), "s"(rA), "s"(ao[6]) : "memory"); SB();
  asm volatile("v_mfma_f32_16x16x32_bf16 %0, v[176:179], a[112:115], %0" : "+v"(a1[0][1]) :  : ); SB();
  asm volatile("buffer_load_dwordx4 %0, %1, %2 offen offset:3072 lds" :  : "v"(voS), "s"(rA), "s"(ao[7]) : "memory"); SB();
  asm volatile("v_xor_b32 %0, 128, %0" : "+v"(voS) :  : "memory"); SB();
  asm volatile("s_add_u32 m0, %0, 98304" :  : "s"(mA) : "memory", "scc"); SB();
  asm volatile("s_nop 0\n\tbuffer_load_dwordx4 %0, %1, %2 offen lds" :  : "v"(voS), "s"(rA), "s"(ao[0]) : "memory"); SB();
  asm volatile("buffer_load_dwordx4 %0, %1, %2 offen offset:1024 lds" :  : "v"(voS), "s"(rA), "s"(ao[1]) : "memory"); SB();
  asm volatile("s_waitcnt vmcnt(13) lgkmcnt(0)\n\ts_barrier" :  :  : "memory"); SB();
  asm volatile("ds_read_b64_tr_b16 v[160:161], %0 offset:24576" :  : "v"(aW[0][0][0]) : "memory"); SB();
  asm volatile("v_mfma_f32_16x16x32_bf16 %0, v[176:179], a[116:119], %0" : "+v"(a1[1][1]) :  : ); SB();
  asm volatile("ds_read_b64_tr_b16 v[162:163], %0 offset:25344" :  : "v"(aW[0][0][0]) : "memory"); SB();
  asm volatile("v_mfma_f32_16x16x32_bf16 %0, v[176:179], %1, %0" : "+v"(c1[1]) : "v"(ones) : ); SB();
  asm volatile("ds_read_b64_tr_b16 v[164:165], %0 offset:24576" :  : "v"(aW[0][0][1]) : "memory"); SB();
  asm volatile("v_mfma_f32_16x16x32_bf16 %0, v[180:183], a[112:115], %0" : "+v"(a1[0][2]) :  : ); SB();
  asm volatile("ds_read_b64_tr_b16 v[166:167], %0 offset:25344" :  : "v"(aW[0][0][1]) : "memory"); SB();
  asm volatile("v_mfma_f32_16x16x32_bf16 %0, v[180:183], a[116:119], %0" : "+v"(a1[1][2]) :  : ); SB();
  asm volatile("ds_read_b64_tr_b16 v[168:169], %0 offset:24576" :  : "v"(aW[0][0][2]) : "memory"); SB();
  asm volatile("v_mfma_f32_16x16x32_bf16 %0, v[180:183], %1, %0" : "+v"(c1[2]) : "v"(ones) : ); SB();
  asm volatile("ds_read_b64_tr_b16 v[170:171], %0 offset:25344" :  : "v"(aW[0][0][2]) : "memory"); SB();
  asm volatile("s_waitcnt vmcnt(11)" :  :  : "memory"); SB();
  asm volatile("v_mfma_f32_16x16x32_bf16 %0, v[184:187], a[120:123], %0" : "+v"(a1[0][0]) :  : ); SB();
  asm volatile("ds_read_b128 a[204:207], %0 offset:6144" :  : "v"(aA[0]) : "memory"); SB();
  asm volatile("v_mfma_f32_16x16x32_bf16 %0, v[184:187], a[124:127], %0" : "+v"(a1[1][0]) :  : ); SB();
  asm volatile("ds_read_b128 a[220:223], %0 offset:6144" :  : "v"(aA[1]) : "memory"); SB();
  asm volatile("v_mfma_f32_16x16x32_bf16 %0, v[184:187], %1, %0" : "+v"(c1[0]) : "v"(ones) : ); SB();
  asm volatile("buffer_load_dwordx4 %0, %1, %2 offen offset:2048 lds" :  : "v"(voS), "s"(rA), "s"(ao[2]) : "memory"); SB();
  asm volatile("v_mfma_f32_16x16x32_bf16 %0, v[188:191], a[120:123], %0" : "+v"(a1[0][1]) :  : ); SB();
  asm volatile("ds_read_b64_tr_b16 v[172:173], %0 offset:24576" :  : "v"(aW[0][1][0]) : "memory"); SB();
  asm volatile("buffer_load_dwordx4 %0, %1, %2 offen offset:3072 lds" :  : "v"(voS), "s"(rA), "s"(ao[3]) : "memory"); SB();
  asm volatile("v_mfma_f32_16x16x32_bf16 %0, v[188:191], a[124:127], %0" : "+v"(a1[1][1]) :  : ); SB();
  asm volatile("ds_read_b64_tr_b16 v[174:175], %0 offset:25344" :  : "v"(aW[0][1][0]) : "memory"); SB();
  asm volatile("v_mfma_f32_16x16x32_bf16 %0, v[188:191], %1, %0" : "+v"(c1[1]) : "v"(ones) : ); SB();
  asm volatile("ds_read_b64_tr_b16 v[176:177], %0 offset:24576" :  : "v"(aW[0][1][1]) : "memory"); SB();
  asm volatile("v_mfma_f32_16x16x32_bf16 %0, v[192:195], a[120:123], %0" : "+v"(a1[0][2]) :  : ); SB();
  asm volatile("ds_read_b64_tr_b16 v[178:179], %0 offset:25344" :  : "v"(aW[0][1][1]) : "memory"); SB();
  asm volatile("s_waitcnt vmcnt(11)" :  :  : "memory"); SB();
  asm volatile("v_mfma_f32_16x16x32_bf16 %0, v[192:195], a[124:127], %0" : "+v"(a1[1][2]) :  : ); SB();
  asm volatile("ds_read_b64_tr_b16 v[180:181], %0 offset:24576" :  : "v"(aW[0][1][2]) : "memory"); SB();
  asm volatile("ds_read_b128 a[224:227], %0 offset:32768" :  : "v"(aA[0]) : "memory"); SB();
  asm volatile("v_mfma_f32_16x16x32_bf16 %0, v[192:195], %1, %0" : "+v"(c1[2]) : "v"(ones) : ); SB();
  asm volatile("ds_read_b64_tr_b16 v[182:183], %0 offset:25344" :  : "v"(aW[0][1][2]) : "memory"); SB();
  asm volatile("ds_read_b128 a[240:243], %0 offset:32768" :  : "v"(aA[1]) : "memory"); SB();
  asm volatile("s_waitcnt lgkmcnt(9)\n\tv_mfma_f32_16x16x32_bf16 %0, v[160:163], a[128:131], %0" : "+v"(a1[0][0]) :  : ); SB();
  asm volatile("s_add_u32 m0, %0, 102400" :  : "s"(mA) : "memory", "scc"); SB();
  asm volatile("ds_read_b128 %0, %1 offset:0" : "=&v"(co[0]) : "v"(cr) : "memory"); SB();
  asm volatile("v_mfma_f32_16x16x32_bf16 %0, v[160:163], a[132:135], %0" : "+v"(a1[1][0]) :  : ); SB();
  asm volatile("buffer_load_dwordx4 %0, %1, %2 offen lds" :  : "v"(voS), "s"(rA), "s"(ao[4]) : "memory"); SB();
  asm volatile("v_mfma_f32_16x16x32_bf16 %0, v[160:163], %1, %0" : "+v"(c1[0]) : "v"(ones) : ); SB();
  asm volatile("buffer_load_dwordx4 %0, %1, %2 offen offset:1024 lds" :  : "v"(voS), "s"(rA), "s"(ao[5]) : "memory"); SB();
  asm volatile("s_waitcnt lgkmcnt(8)" :  :  : "memory"); SB();
  asm volatile("v_mfma_f32_16x16x32_bf16 %0, v[164:167], a[128:131], %0" : "+v"(a1[0][1]) :  : ); SB();
  asm volatile("ds_read_b64_tr_b16 v[184:185], %0 offset:36864" :  : "v"(aW[0][0][0]) : "memory"); SB();
  asm volatile("s_waitcnt vmcnt(11)" :  :  : "memory"); SB();
  asm volatile("v_mfma_f32_16x16x32_bf16 %0, v[164:167], a[132:135], %0" : "+v"(a1[1][1]) :  : ); SB();
  asm volatile("ds_read_b64_tr_b16 v[186:187], %0 offset:37632" :  : "v"(aW[0][0][0]) : "memory"); SB();
  asm volatile("ds_read_b128 a[228:231], %0 offset:34816" :  : "v"(aA[0]) : "memory"); SB();
  asm volatile("v_mfma_f32_16x16x32_bf16 %0, v[164:167], %1, %0" : "+v"(c1[1]) : "v"(ones) : ); SB();
  asm volatile("ds_read_b64_tr_b16 v[188:189], %0 offset:36864" :  : "v"(aW[0][0][1]) : "memory"); SB();
  asm volatile("ds_read_b128 a[244:247], %0 offset:34816" :  : "v"(aA[1]) : "memory"); SB();
  asm volatile("v_mfma_f32_16x16x32_bf16 %0, v[168:171], a[128:131], %0" : "+v"(a1[0][2]) :  : ); SB();
  asm volatile("ds_read_b64_tr_b16 v[190:191], %0 offset:37632" :  : "v"(aW[0][0][1]) : "memory"); SB();
  asm volatile("buffer_load_dwordx4 %0, %1, %2 offen offset:2048 lds" :  : "v"(voS), "s"(rA), "s"(ao[6]) : "memory"); SB();
  asm volatile("v_mfma_f32_16x16x32_bf16 %0, v[168:171], a[132:135], %0" : "+v"(a1[1][2]) :  : ); SB();
  asm volatile("ds_read_b64_tr_b16 v[192:193], %0 offset:36864" :  : "v"(aW[0][0][2]) : "memory"); SB();
  asm volatile("buffer_load_dwordx4 %0, %1, %2 offen offset:3072 lds" :  : "v"(voS), "s"(rA), "s"(ao[7]) : "memory"); SB();
  asm volatile("v_mfma_f32_16x16x32_bf16 %0, v[168:171], %1, %0" : "+v"(c1[2]) : "v"(ones) : ); SB();
  asm volatile("ds_read_b64_tr_b16 v[194:195], %0 offset:37632" :  : "v"(aW[0][0][2]) : "memory"); SB();
  asm volatile("s_waitcnt lgkmcnt(8)\n\tv_mfma_f32_16x16x32_bf16 %0, v[172:175], a[136:139], %0" : "+v"(a1[0][0]) :  : ); SB();
  asm volatile("ds_read_b128 %0, %1 offset:64" : "=&v"(co[1]) : "v"(cr) : "memory"); SB();
  asm volatile("s_waitcnt vmcnt(11)" :  :  : "memory"); SB();
  asm volatile("v_mfma_f32_16x16x32_bf16 %0, v[172:175], a[140:143], %0" : "+v"(a1[1][0]) :  : ); SB();
  asm volatile("ds_read_b128 a[232:235], %0 offset:36864" :  : "v"(aA[0]) : "memory"); SB();
  asm volatile("v_mfma_f32_16x16x32_bf16 %0, v[172:175], %1, %0" : "+v"(c1[0]) : "v"(ones) : ); SB();
  asm volatile("ds_read_b128 a[248:251], %0 offset:36864" :  : "v"(aA[1]) : "memory"); SB();
  asm volatile("v_mfma_f32_16x16x32_bf16 %0, v[176:179], a[136:139], %0" : "+v"(a1[0][1]) :  : ); SB();
  asm volatile("ds_read_b64_tr_b16 v[160:161], %0 offset:36864" :  : "v"(aW[0][1][0]) : "memory"); SB();
  asm volatile("v_xor_b32 %0, 896, %0" : "+v"(voS) :  : "memory"); SB();
  asm volatile("v_mfma_f32_16x16x32_bf16 %0, v[176:179], a[140:143], %0" : "+v"(a1[1][1]) :  : ); SB();
  asm volatile("ds_read_b64_tr_b16 v[162:163], %0 offset:37632" :  : "v"(aW[0][1][0]) : "memory"); SB();
  asm volatile("buffer_load_dwordx2 %0, %1, %2, %3 offen sc1" : "=&v"(fk) : "v"(vFZ), "s"(rWS), "s"(8192u) : "memory"); SB();
  asm volatile("v_mfma_f32_16x16x32_bf16 %0, v[176:179], %1, %0" : "+v"(c1[1]) : "v"(ones) : ); SB();
  asm volatile("ds_read_b64_tr_b16 v[164:165], %0 offset:36864" :  : "v"(aW[0][1][1]) : "memory"); SB();
  asm volatile("s_add_u32 m0, %0, 131072" :  : "s"(mA) : "memory", "scc"); SB();
  asm volatile("v_mfma_f32_16x16x32_bf16 %0, v[180:183], a[136:139], %0" : "+v"(a1[0][2]) :  : ); SB();
  asm volatile("ds_read_b64_tr_b16 v[166:167], %0 offset:37632" :  : "v"(aW[0][1][1]) : "memory"); SB();
  asm volatile("buffer_load_dwordx4 %0, %1, %2 offen lds" :  : "v"(voS), "s"(rA), "s"(ao[0]) : "memory"); SB();
  asm volatile("v_mfma_f32_16x16x32_bf16 %0, v[180:183], a[140:143], %0" : "+v"(a1[1][2]) :  : ); SB();
  asm volatile("ds_read_b64_tr_b16 v[168:169], %0 offset:36864" :  : "v"(aW[0][1][2]) : "memory"); SB();
  asm volatile("v_mfma_f32_16x16x32_bf16 %0, v[180:183], %1, %0" : "+v"(c1[2]) : "v"(ones) : ); SB();
  asm volatile("ds_read_b64_tr_b16 v[170:171], %0 offset:37632" :  : "v"(aW[0][1][2]) : "memory"); SB();
  asm volatile("s_waitcnt lgkmcnt(9)\n\tv_mfma_f32_16x16x32_bf16 %0, v[184:187], a[144:147], %0" : "+v"(a1[0][0]) :  : ); SB();
  asm volatile("ds_read_b128 %0, %1 offset:128" : "=&v"(co[2]) : "v"(cr) : "memory"); SB();
  asm volatile("v_mfma_f32_16x16x32_bf16 %0, v[184:187], a[148:151], %0" : "+v"(a1[1][0]) :  : ); SB();
  asm volatile("ds_write_b128 %0, %1 offset:0" :  : "v"(ha), "v"(im[0]) : "memory"); SB();
  asm volatile("ds_write_b128 %0, %1 offset:4160" :  : "v"(ha), "v"(im[1]) : "memory"); SB();
  asm volatile("buffer_load_dwordx4 %0, %1, %2 offen offset:1024 lds" :  : "v"(voS), "s"(rA), "s"(ao[1]) : "memory"); SB();
  asm volatile("v_mfma_f32_16x16x32_bf16 %0, v[184:187], %1, %0" : "+v"(c1[0]) : "v"(ones) : ); SB();
  asm volatile("ds_write_b128 %0, %1 offset:12928" :  : "v"(ha), "v"(im[2]) : "memory"); SB();
  asm volatile("ds_write_b128 %0, %1 offset:17088" :  : "v"(ha), "v"(im[3]) : "memory"); SB();
  asm volatile("s_waitcnt vmcnt(11)" :  :  : "memory"); SB();
  asm volatile("ds_read_b128 a[236:239], %0 offset:38912" :  : "v"(aA[0]) : "memory"); SB();
  asm volatile("v_mfma_f32_16x16x32_bf16 %0, v[188:191], a[144:147], %0" : "+v"(a1[0][1]) :  : ); SB();
  asm volatile("ds_write_b128 %0, %1 offset:25856" :  : "v"(ha), "v"(im[4]) : "memory"); SB();
  asm volatile("ds_write_b128 %0, %1 offset:30016" :  : "v"(ha), "v"(im[5]) : "memory"); SB();
  asm volatile("ds_read_b128 a[252:255], %0 offset:38912" :  : "v"(aA[1]) : "memory"); SB();
  asm volatile("s_waitcnt vmcnt(9)" :  :  : "memory"); SB();
  asm volatile("v_mfma_f32_16x16x32_bf16 %0, v[188:191], a[148:151], %0" : "+v"(a1[1][1]) :  : ); SB();
  asm volatile("ds_read_b128 a[0:3], %0 offset:0" :  : "v"(aA[0]) : "memory"); SB();
  asm volatile("v_mfma_f32_16x16x32_bf16 %0, v[188:191], %1, %0" : "+v"(c1[1]) : "v"(ones) : ); SB();
  asm volatile("ds_read_b128 a[16:19], %0 offset:0" :  : "v"(aA[1]) : "memory"); SB();
  asm volatile("v_mfma_f32_16x16x32_bf16 %0, v[192:195], a[144:147], %0" : "+v"(a1[0][2]) :  : ); SB();
  asm volatile("ds_write_b128 %0, %1 offset:0" :  : "v"(za), "v"(zero4) : "memory"); SB();
  asm volatile("buffer_load_dwordx4 %0, %1, %2 offen offset:2048 lds" :  : "v"(voS), "s"(rA), "s"(ao[2]) : "memory"); SB();
  asm volatile("v_mfma_f32_16x16x32_bf16 %0, v[192:195], a[148:151], %0" : "+v"(a1[1][2]) :  : ); SB();
  asm volatile("buffer_load_dwordx4 %0, %1, %2 offen offset:3072 lds" :  : "v"(voS), "s"(rA), "s"(ao[3]) : "memory"); SB();
  asm volatile("s_waitcnt lgkmcnt(5)" :  :  : "memory"); SB();
  asm volatile("v_mfma_f32_16x16x32_bf16 %0, v[192:195], %1, %0" : "+v"(c1[2]) : "v"(ones) : ); SB();
  asm volatile("v_mfma_f32_16x16x32_bf16 %0, v[160:163], a[152:155], %0" : "+v"(a1[0][0]) :  : ); SB();
  asm volatile("v_mfma_f32_16x16x32_bf16 %0, v[160:163], a[156:159], %0" : "+v"(a1[1][0]) :  : ); SB();
  asm volatile("s_add_u32 m0, %0, 135168" :  : "s"(mA) : "memory", "scc"); SB();
  asm volatile("v_mfma_f32_16x16x32_bf16 %0, v[160:163], %1, %0" : "+v"(c1[0]) : "v"(ones) : ); SB();
  asm volatile("buffer_load_dwordx4 %0, %1, %2 offen lds" :  : "v"(voS), "s"(rA), "s"(ao[4]) : "memory"); SB();
  asm volatile("v_mfma_f32_16x16x32_bf16 %0, v[164:167], a[152:155], %0" : "+v"(a1[0][1]) :  : ); SB();
  asm volatile("v_mfma_f32_16x16x32_bf16 %0, v[164:167], a[156:159], %0" : "+v"(a1[1][1]) :  : ); SB();
  asm volatile("v_mfma_f32_16x16x32_bf16 %0, v[164:167], %1, %0" : "+v"(c1[1]) : "v"(ones) : ); SB();
  asm volatile("buffer_load_dwordx4 %0, %1, %2 offen offset:1024 lds" :  : "v"(voS), "s"(rA), "s"(ao[5]) : "memory"); SB();
  asm volatile("s_waitcnt lgkmcnt(0)" :  :  : "memory"); SB();
  asm volatile("v_mfma_f32_16x16x32_bf16 %0, v[168:171], a[152:155], %0" : "+v"(a1[0][2]) :  : ); SB();
  asm volatile("v_mfma_f32_16x16x32_bf16 %0, v[168:171], a[156:159], %0" : "+v"(a1[1][2]) :  : ); SB();
  asm volatile("v_mfma_f32_16x16x32_bf16 %0, v[168:171], %1, %0" : "+v"(c1[2]) : "v"(ones) : ); SB();
  asm volatile("buffer_load_dwordx4 %0, %1, %2 offen offset:2048 lds" :  : "v"(voS), "s"(rA), "s"(ao[6]) : "memory"); SB();
  asm volatile("s_waitcnt vmcnt(12)" :  :  : "memory"); SB();
  ST(11);
  f32x4 ac[8];
  u32x2 cA[2][8][2];
  asm volatile("ds_read_b128 a[4:7], %0 offset:2048" :  : "v"(aA[0]) : "memory"); SB();
  asm volatile("ds_read_b128 a[20:23], %0 offset:2048" :  : "v"(aA[1]) : "memory"); SB();
  asm volatile("buffer_load_dwordx4 %0, %1, %2 offen offset:3072 lds" :  : "v"(voS), "s"(rA), "s"(ao[7]) : "memory"); SB();
  asm volatile("v_xor_b32 %0, 128, %0" : "+v"(voS) :  : "memory"); SB();
  asm volatile("s_add_u32 m0, %0, 98304" :  : "s"(mA) : "memory", "scc"); SB();
  asm volatile("s_nop 0\n\tbuffer_load_dwordx4 %0, %1, %2 offen lds" :  : "v"(voS), "s"(rA), "s"(ao[0]) : "memory"); SB();
  asm volatile("buffer_load_dwordx4 %0, %1, %2 offen offset:1024 lds" :  : "v"(voS), "s"(rA), "s"(ao[1]) : "memory"); SB();
  asm volatile("buffer_load_dwordx2 %0, %1, %2, %3 offen sc1" : "=&v"(fz) : "v"(vFZ), "s"(rWS), "s"(0u) : "memory"); SB();
  asm volatile("s_waitcnt vmcnt(14)" :  :  : "memory"); SB();
  asm volatile("ds_read_b128 a[8:11], %0 offset:4096" :  : "v"(aA[0]) : "memory"); SB();
  asm volatile("ds_read_b128 a[24:27], %0 offset:4096" :  : "v"(aA[1]) : "memory"); SB();
  asm volatile("s_waitcnt lgkmcnt(2)" :  :  : "memory"); SB();
  asm volatile("buffer_load_dwordx4 %0, %1, %2 offen offset:2048 lds" :  : "v"(voS), "s"(rA), "s"(ao[2]) : "memory"); SB();
  asm volatile("buffer_load_dwordx4 %0, %1, %2 offen offset:3072 lds" :  : "v"(voS), "s"(rA), "s"(ao[3]) : "memory"); SB();
  asm volatile("s_waitcnt vmcnt(14)" :  :  : "memory"); SB();
  asm volatile("ds_read_b128 a[12:15], %0 offset:6144" :  : "v"(aA[0]) : "memory"); SB();
  asm volatile("ds_read_b128 a[28:31], %0 offset:6144" :  : "v"(aA[1]) : "memory"); SB();
  asm volatile("s_waitcnt lgkmcnt(2)" :  :  : "memory"); SB();
  asm volatile("s_add_u32 m0, %0, 102400" :  : "s"(mA) : "memory", "scc"); SB();
  asm volatile("s_nop 0\n\tbuffer_load_dwordx4 %0, %1, %2 offen lds" :  : "v"(voS), "s"(rA), "s"(ao[4]) : "memory"); SB();
  asm volatile("s_waitcnt vmcnt(14)\n\t.Lc_%=:\n\tv_cmp_ne_u64 vcc, %2, %1\n\ts_cbranch_vccnz .Lr_%=\n\t.Ld_%=:\n\t.subsection 1\n\t.Lr_%=:\n\tv_add_u32 %0, -1, %0\n\ts_nop 1\n\tv_readfirstlane_b32 vcc_hi, %0\n\ts_nop 3\n\ts_cmp_lt_i32 vcc_hi, 1\n\ts_cbranch_scc1 .Ld_%=\n\ts_sleep 2\n\ts_mov_b32 vcc_lo, 8192\n\tbuffer_load_dwordx2 %1, %3, %4, vcc_lo offen sc1\n\ts_waitcnt vmcnt(0)\n\ts_branch .Lc_%=\n\t.subsection 0" : "+v"(tmo), "+v"(fk) : "s"(tg), "v"(vFZ), "s"(rWS) : "memory", "vcc", "scc"); SB();
  asm volatile("s_add_u32 m0, %0, 85888" :  : "s"(mU) : "memory", "scc"); SB();
  asm volatile("s_nop 0\n\tbuffer_load_dwordx4 %0, %1, %2 offen sc1 lds" :  : "v"(vF), "s"(rWS), "s"(uso) : "memory"); SB();
  asm volatile("s_add_u32 m0, %0, 102400" :  : "s"(mA) : "memory", "scc"); SB();
  asm volatile("s_nop 0\n\tbuffer_load_dwordx4 %0, %1, %2 offen offset:1024 lds" :  : "v"(voS), "s"(rA), "s"(ao[5]) : "memory"); SB();
  asm volatile("s_waitcnt lgkmcnt(0)" :  :  : "memory"); SB();
  asm volatile("s_waitcnt vmcnt(14)" :  :  : "memory"); SB();
  asm volatile("ds_read_b128 a[32:35], %0 offset:32768" :  : "v"(aA[0]) : "memory"); SB();
  asm volatile("ds_read_b128 a[48:51], %0 offset:32768" :  : "v"(aA[1]) : "memory"); SB();
  asm volatile("buffer_load_dwordx4 %0, %1, %2 offen offset:2048 lds" :  : "v"(voS), "s"(rA), "s"(ao[6]) : "memory"); SB();
  asm volatile("buffer_load_dwordx4 %0, %1, %2 offen offset:3072 lds" :  : "v"(voS), "s"(rA), "s"(ao[7]) : "memory"); SB();
  asm volatile("s_waitcnt lgkmcnt(0)" :  :  : "memory"); SB();
  asm volatile("v_xor_b32 %0, 384, %0" : "+v"(voS) :  : "memory"); SB();
  asm volatile("s_waitcnt vmcnt(14)" :  :  : "memory"); SB();
  asm volatile("ds_read_b128 a[36:39], %0 offset:34816" :  : "v"(aA[0]) : "memory"); SB();
  asm volatile("ds_read_b128 a[52:55], %0 offset:34816" :  : "v"(aA[1]) : "memory"); SB();
  asm volatile("s_add_u32 m0, %0, 131072" :  : "s"(mA) : "memory", "scc"); SB();
  asm volatile("s_nop 0\n\tbuffer_load_dwordx4 %0, %1, %2 offen lds" :  : "v"(voS), "s"(rA), "s"(ao[0]) : "memory"); SB();
  asm volatile("buffer_load_dwordx4 %0, %1, %2 offen offset:1024 lds" :  : "v"(voS), "s"(rA), "s"(ao[1]) : "memory"); SB();
  asm volatile("s_waitcnt vmcnt(14)" :  :  : "memory"); SB();
  asm volatile("ds_read_b128 a[40:43], %0 offset:36864" :  : "v"(aA[0]) : "memory"); SB();
  asm volatile("ds_read_b128 a[56:59], %0 offset:36864" :  : "v"(aA[1]) : "memory"); SB();
  asm volatile("s_waitcnt lgkmcnt(2)" :  :  : "memory"); SB();
  asm volatile("buffer_load_dwordx4 %0, %1, %2 offen offset:2048 lds" :  : "v"(voS), "s"(rA), "s"(ao[2]) : "memory"); SB();
  asm volatile("buffer_load_dwordx4 %0, %1, %2 offen offset:3072 lds" :  : "v"(voS), "s"(rA), "s"(ao[3]) : "memory"); SB();
  asm volatile("s_waitcnt vmcnt(14)" :  :  : "memory"); SB();
  asm volatile("ds_read_b128 a[44:47], %0 offset:38912" :  : "v"(aA[0]) : "memory"); SB();
  asm volatile("ds_read_b128 a[60:63], %0 offset:38912" :  : "v"(aA[1]) : "memory"); SB();
  asm volatile("s_waitcnt lgkmcnt(2)" :  :  : "memory"); SB();
  asm volatile("s_add_u32 m0, %0, 135168" :  : "s"(mA) : "memory", "scc"); SB();
  asm volatile("s_nop 0\n\tbuffer_load_dwordx4 %0, %1, %2 offen lds" :  : "v"(voS), "s"(rA), "s"(ao[4]) : "memory"); SB();
  asm volatile("buffer_load_dwordx4 %0, %1, %2 offen offset:1024 lds" :  : "v"(voS), "s"(rA), "s"(ao[5]) : "memory"); SB();
  asm volatile("s_waitcnt vmcnt(14)" :  :  : "memory"); SB();
  asm volatile("ds_read_b128 a[64:67], %0 offset:0" :  : "v"(aA[0]) : "memory"); SB();
  asm volatile("ds_read_b128 a[80:83], %0 offset:0" :  : "v"(aA[1]) : "memory"); SB();
  asm volatile("s_waitcnt vmcnt(13)\n\t.Lc_%=:\n\tv_cmp_ne_u64 vcc, %2, %1\n\ts_cbranch_vccnz .Lr_%=\n\t.Ld_%=:\n\t.subsection 1\n\t.Lr_%=:\n\tv_add_u32 %0, -1, %0\n\ts_nop 1\n\tv_readfirstlane_b32 vcc_hi, %0\n\ts_nop 3\n\ts_cmp_lt_i32 vcc_hi, 1\n\ts_cbranch_scc1 .Ld_%=\n\ts_sleep 2\n\ts_mov_b32 vcc_lo, 0\n\tbuffer_load_dwordx2 %1, %3, %4, vcc_lo offen sc1\n\ts_waitcnt vmcnt(0)\n\ts_branch .Lc_%=\n\t.subsection 0" : "+v"(tmo), "+v"(fz) : "s"(tg), "v"(vFZ), "s"(rWS) : "memory", "vcc", "scc"); SB();
  asm volatile("buffer_load_dwordx4 v[160:163], %0, %1, %2 offen offset:0 sc1" :  : "v"(vB), "s"(rWS), "s"(0u) : "memory"); SB();
  asm volatile("s_waitcnt lgkmcnt(2)" :  :  : "memory"); SB();
  asm volatile("buffer_load_dwordx4 v[164:167], %0, %1, %2 offen offset:1024 sc1" :  : "v"(vB), "s"(rWS), "s"(0u) : "memory"); SB();
  asm volatile("s_waitcnt vmcnt(13)" :  :  : "memory"); SB();
  asm volatile("ds_read_b128 a[68:71], %0 offset:2048" :  : "v"(aA[0]) : "memory"); SB();
  asm volatile("ds_read_b128 a[84:87], %0 offset:2048" :  : "v"(aA[1]) : "memory"); SB();
  asm volatile("buffer_load_dwordx4 v[168:171], %0, %1, %2 offen offset:0 sc1" :  : "v"(vB), "s"(rWS), "s"(65536u) : "memory"); SB();
  asm volatile("s_waitcnt vmcnt(12) lgkmcnt(0)\n\ts_barrier" :  :  : "memory"); SB();
  ST(14);
  asm volatile("ds_read2_b32 %0, %1 offset0:176 offset1:177" : "=&v"(cA[0][0][0]) : "v"(aa[0]) : "memory"); SB();
  asm volatile("ds_read2_b32 %0, %1 offset0:178 offset1:179" : "=&v"(cA[0][0][1]) : "v"(aa[0]) : "memory"); SB();
  asm volatile("ds_read_b128 a[124:127], %0 offset:3776" :  : "v"(bb) : "memory"); SB();
  asm volatile("ds_read_b128 a[120:123], %0 offset:3264" :  : "v"(bb) : "memory"); SB();
  asm volatile("ds_read_b128 a[116:119], %0 offset:2752" :  : "v"(bb) : "memory"); SB();
  asm volatile("ds_read_b128 a[112:115], %0 offset:2240" :  : "v"(bb) : "memory"); SB();
  asm volatile("ds_read2_b32 %0, %1 offset0:48 offset1:49" : "=&v"(cA[0][1][0]) : "v"(aa[0]) : "memory"); SB();
  asm volatile("ds_read2_b32 %0, %1 offset0:50 offset1:51" : "=&v"(cA[0][1][1]) : "v"(aa[0]) : "memory"); SB();
  asm volatile("ds_read_b128 a[108:111], %0 offset:1728" :  : "v"(bb) : "memory"); SB();
  asm volatile("ds_read2_b32 %0, %1 offset0:176 offset1:177" : "=&v"(cA[0][2][0]) : "v"(aa[1]) : "memory"); SB();
  asm volatile("" : "+v"(a1[0][0]), "+v"(a1[0][1]), "+v"(a1[0][2]), "+v"(a1[1][0]), "+v"(a1[1][1]), "+v"(a1[1][2]), "+v"(c1[0]), "+v"(c1[1]), "+v"(c1[2]), "+v"(co[0]), "+v"(co[1]), "+v"(co[2]) :  : ); SB();
  c1[0] += co[0]; c1[1] += co[1]; c1[2] += co[2];
#pragma unroll
  for (int n = 0; n < 6; ++n) { a1[n / 3][n % 3] = __builtin_elementwise_fma((f32x4){-xm[n / 3], -xm[n / 3], -xm[n / 3], -xm[n / 3]}, c1[n % 3], a1[n / 3][n % 3]); asm volatile("" : "+v"(a1[n / 3][n % 3])); }
  SB();
  asm volatile("s_waitcnt vmcnt(11)" :  :  : "memory"); SB();
  asm volatile("ds_read_b128 a[72:75], %0 offset:4096" :  : "v"(aA[0]) : "memory"); SB();
  asm volatile("ds_read_b128 a[88:91], %0 offset:4096" :  : "v"(aA[1]) : "memory"); SB();
  asm volatile("buffer_load_dwordx4 v[172:175], %0, %1, %2 offen offset:1024 sc1" :  : "v"(vB), "s"(rWS), "s"(65536u) : "memory"); SB();
  asm volatile("v_xor_b32 %0, 131072, %0" : "+v"(vB) :  : "memory"); SB();
  asm volatile("buffer_load_dwordx4 v[176:179], %0, %1, %2 offen offset:0 sc1" :  : "v"(vB), "s"(rWS), "s"(0u) : "memory"); SB();
  asm volatile("s_waitcnt vmcnt(11)" :  :  : "memory"); SB();
  asm volatile("ds_read_b128 a[76:79], %0 offset:6144" :  : "v"(aA[0]) : "memory"); SB();
  asm volatile("ds_read_b128 a[92:95], %0 offset:6144" :  : "v"(aA[1]) : "memory"); SB();
  if (nch == 3) {
  asm volatile("s_waitcnt lgkmcnt(5)\n\tv_mfma_f32_16x16x32_bf16 %0, %1, a[124:127], 0" : "=&v"(ac[7]) : "v"(((u32x4){cA[0][0][0][0], cA[0][0][0][1], cA[0][0][1][0], cA[0][0][1][1]})) : ); SB();
  asm volatile("ds_read2_b32 %0, %1 offset0:178 offset1:179" : "=&v"(cA[0][2][1]) : "v"(aa[1]) : "memory"); SB();
  asm volatile("v_mfma_f32_16x16x32_bf16 %0, %1, a[120:123], 0" : "=&v"(ac[6]) : "v"(((u32x4){cA[0][0][0][0], cA[0][0][0][1], cA[0][0][1][0], cA[0][0][1][1]})) : ); SB();
  asm volatile("ds_read2_b32 %0, %1 offset0:48 offset1:49" : "=&v"(cA[0][3][0]) : "v"(aa[1]) : "memory"); SB();
  asm volatile("v_mfma_f32_16x16x32_bf16 %0, %1, a[116:119], 0" : "=&v"(ac[5]) : "v"(((u32x4){cA[0][0][0][0], cA[0][0][0][1], cA[0][0][1][0], cA[0][0][1][1]})) : ); SB();
  asm volatile("ds_read2_b32 %0, %1 offset0:50 offset1:51" : "=&v"(cA[0][3][1]) : "v"(aa[1]) : "memory"); SB();
  asm volatile("v_mfma_f32_16x16x32_bf16 %0, %1, a[112:115], 0" : "=&v"(ac[4]) : "v"(((u32x4){cA[0][0][0][0], cA[0][0][0][1], cA[0][0][1][0], cA[0][0][1][1]})) : ); SB();
  asm volatile("ds_read_b128 a[104:107], %0 offset:1216" :  : "v"(bb) : "memory"); SB();
  asm volatile("buffer_load_dwordx4 v[180:183], %0, %1, %2 offen offset:1024 sc1" :  : "v"(vB), "s"(rWS), "s"(0u) : "memory"); SB();
  asm volatile("v_mfma_f32_16x16x32_bf16 %0, %1, a[120:123], %0" : "+v"(ac[7]) : "v"(((u32x4){cA[0][1][0][0], cA[0][1][0][1], cA[0][1][1][0], cA[0][1][1][1]})) : ); SB();
  asm volatile("ds_read2_b32 %0, %1 offset0:176 offset1:177" : "=&v"(cA[0][4][0]) : "v"(aa[2]) : "memory"); SB();
  asm volatile("v_mfma_f32_16x16x32_bf16 %0, %1, a[116:119], %0" : "+v"(ac[6]) : "v"(((u32x4){cA[0][1][0][0], cA[0][1][0][1], cA[0][1][1][0], cA[0][1][1][1]})) : ); SB();
  asm volatile("ds_read2_b32 %0, %1 offset0:178 offset1:179" : "=&v"(cA[0][4][1]) : "v"(aa[2]) : "memory"); SB();
  asm volatile("v_mfma_f32_16x16x32_bf16 %0, %1, a[112:115], %0" : "+v"(ac[5]) : "v"(((u32x4){cA[0][1][0][0], cA[0][1][0][1], cA[0][1][1][0], cA[0][1][1][1]})) : ); SB();
  asm volatile("ds_read2_b32 %0, %1 offset0:48 offset1:49" : "=&v"(cA[0][5][0]) : "v"(aa[2]) : "memory"); SB();
  asm volatile("buffer_load_dwordx4 v[184:187], %0, %1, %2 offen offset:0 sc1" :  : "v"(vB), "s"(rWS), "s"(65536u) : "memory"); SB();
  asm volatile("v_mfma_f32_16x16x32_bf16 %0, %1, a[108:111], %0" : "+v"(ac[4]) : "v"(((u32x4){cA[0][1][0][0], cA[0][1][0][1], cA[0][1][1][0], cA[0][1][1][1]})) : ); SB();
  asm volatile("ds_read2_b32 %0, %1 offset0:50 offset1:51" : "=&v"(cA[0][5][1]) : "v"(aa[2]) : "memory"); SB();
  asm volatile("s_waitcnt lgkmcnt(7)\n\tv_mfma_f32_16x16x32_bf16 %0, %1, a[116:119], %0" : "+v"(ac[7]) : "v"(((u32x4){cA[0][2][0][0], cA[0][2][0][1], cA[0][2][1][0], cA[0][2][1][1]})) : ); SB();
  asm volatile("ds_read_b128 a[100:103], %0 offset:704" :  : "v"(bb) : "memory"); SB();
  asm volatile("v_mfma_f32_16x16x32_bf16 %0, %1, a[112:115], %0" : "+v"(ac[6]) : "v"(((u32x4){cA[0][2][0][0], cA[0][2][0][1], cA[0][2][1][0], cA[0][2][1][1]})) : ); SB();
  asm volatile("ds_read2_b32 %0, %1 offset0:176 offset1:177" : "=&v"(cA[0][6][0]) : "v"(aa[3]) : "memory"); SB();
  asm volatile("v_mfma_f32_16x16x32_bf16 %0, %1, a[108:111], %0" : "+v"(ac[5]) : "v"(((u32x4){cA[0][2][0][0], cA[0][2][0][1], cA[0][2][1][0], cA[0][2][1][1]})) : ); SB();
  asm volatile("ds_read2_b32 %0, %1 offset0:178 offset1:179" : "=&v"(cA[0][6][1]) : "v"(aa[3]) : "memory"); SB();
  asm volatile("buffer_load_dwordx4 v[188:191], %0, %1, %2 offen offset:1024 sc1" :  : "v"(vB), "s"(rWS), "s"(65536u) : "memory"); SB();
  asm volatile("v_mfma_f32_16x16x32_bf16 %0, %1, a[108:111], 0" : "=&v"(ac[3]) : "v"(((u32x4){cA[0][0][0][0], cA[0][0][0][1], cA[0][0][1][0], cA[0][0][1][1]})) : ); SB();
  asm volatile("ds_read2_b32 %0, %1 offset0:48 offset1:49" : "=&v"(cA[0][7][0]) : "v"(aa[3]) : "memory"); SB();
  asm volatile("s_waitcnt lgkmcnt(9)\n\tv_mfma_f32_16x16x32_bf16 %0, %1, a[112:115], %0" : "+v"(ac[7]) : "v"(((u32x4){cA[0][3][0][0], cA[0][3][0][1], cA[0][3][1][0], cA[0][3][1][1]})) : ); SB();
  asm volatile("ds_read2_b32 %0, %1 offset0:50 offset1:51" : "=&v"(cA[0][7][1]) : "v"(aa[3]) : "memory"); SB();
  asm volatile("v_mfma_f32_16x16x32_bf16 %0, %1, a[108:111], %0" : "+v"(ac[6]) : "v"(((u32x4){cA[0][3][0][0], cA[0][3][0][1], cA[0][3][1][0], cA[0][3][1][1]})) : ); SB();
  asm volatile("ds_read_b128 a[96:99], %0 offset:192" :  : "v"(bb) : "memory"); SB();
  asm volatile("v_xor_b32 %0, 393216, %0" : "+v"(vB) :  : "memory"); SB();
  asm volatile("s_waitcnt lgkmcnt(8)\n\tv_mfma_f32_16x16x32_bf16 %0, %1, a[104:107], %0" : "+v"(ac[5]) : "v"(((u32x4){cA[0][3][0][0], cA[0][3][0][1], cA[0][3][1][0], cA[0][3][1][1]})) : ); SB();
  asm volatile("ds_read2_b32 %0, %1 offset0:160 offset1:161" : "=&v"(cA[1][0][0]) : "v"(aa[0]) : "memory"); SB();
  asm volatile("buffer_load_dwordx4 v[192:195], %0, %1, %2 offen offset:0 sc1" :  : "v"(vB), "s"(rWS), "s"(0u) : "memory"); SB();
  asm volatile("v_mfma_f32_16x16x32_bf16 %0, %1, a[104:107], %0" : "+v"(ac[4]) : "v"(((u32x4){cA[0][2][0][0], cA[0][2][0][1], cA[0][2][1][0], cA[0][2][1][1]})) : ); SB();
  asm volatile("buffer_load_dwordx4 v[196:199], %0, %1, %2 offen offset:1024 sc1" :  : "v"(vB), "s"(rWS), "s"(0u) : "memory"); SB();
  asm volatile("v_mfma_f32_16x16x32_bf16 %0, %1, a[108:111], %0" : "+v"(ac[7]) : "v"(((u32x4){cA[0][4][0][0], cA[0][4][0][1], cA[0][4][1][0], cA[0][4][1][1]})) : ); SB();
  asm volatile("ds_read2_b32 %0, %1 offset0:162 offset1:163" : "=&v"(cA[1][0][1]) : "v"(aa[0]) : "memory"); SB();
  asm volatile("v_mfma_f32_16x16x32_bf16 %0, %1, a[104:107], %0" : "+v"(ac[6]) : "v"(((u32x4){cA[0][4][0][0], cA[0][4][0][1], cA[0][4][1][0], cA[0][4][1][1]})) : ); SB();
  asm volatile("ds_read_b128 a[156:159], %0 offset:3712" :  : "v"(bb) : "memory"); SB();
  asm volatile("v_mfma_f32_16x16x32_bf16 %0, %1, a[104:107], %0" : "+v"(ac[3]) : "v"(((u32x4){cA[0][1][0][0], cA[0][1][0][1], cA[0][1][1][0], cA[0][1][1][1]})) : ); SB();
  asm volatile("ds_read_b128 a[152:155], %0 offset:3200" :  : "v"(bb) : "memory"); SB();
  asm volatile("v_mfma_f32_16x16x32_bf16 %0, %1, a[104:107], 0" : "=&v"(ac[2]) : "v"(((u32x4){cA[0][0][0][0], cA[0][0][0][1], cA[0][0][1][0], cA[0][0][1][1]})) : ); SB();
  asm volatile("buffer_load_dwordx4 v[200:203], %0, %1, %2 offen offset:0 sc1" :  : "v"(vB), "s"(rWS), "s"(65536u) : "memory"); SB();
  asm volatile("s_waitcnt lgkmcnt(6)\n\tv_mfma_f32_16x16x32_bf16 %0, %1, a[104:107], %0" : "+v"(ac[7]) : "v"(((u32x4){cA[0][5][0][0], cA[0][5][0][1], cA[0][5][1][0], cA[0][5][1][1]})) : ); SB();
  asm volatile("ds_read_b128 a[128:131], %0 offset:128" :  : "v"(bb) : "memory"); SB();
  asm volatile("v_mfma_f32_16x16x32_bf16 %0, %1, a[100:103], %0" : "+v"(ac[6]) : "v"(((u32x4){cA[0][5][0][0], cA[0][5][0][1], cA[0][5][1][0], cA[0][5][1][1]})) : ); SB();
  asm volatile("ds_read_b128 a[132:135], %0 offset:640" :  : "v"(bb) : "memory"); SB();
  asm volatile("v_mfma_f32_16x16x32_bf16 %0, %1, a[100:103], %0" : "+v"(ac[5]) : "v"(((u32x4){cA[0][4][0][0], cA[0][4][0][1], cA[0][4][1][0], cA[0][4][1][1]})) : ); SB();
  asm volatile("ds_read2_b32 %0, %1 offset0:32 offset1:33" : "=&v"(cA[1][1][0]) : "v"(aa[0]) : "memory"); SB();
  asm volatile("v_mfma_f32_16x16x32_bf16 %0, %1, a[100:103], %0" : "+v"(ac[4]) : "v"(((u32x4){cA[0][3][0][0], cA[0][3][0][1], cA[0][3][1][0], cA[0][3][1][1]})) : ); SB();
  asm volatile("buffer_load_dwordx4 v[204:207], %0, %1, %2 offen offset:1024 sc1" :  : "v"(vB), "s"(rWS), "s"(65536u) : "memory"); SB();
  asm volatile("v_mfma_f32_16x16x32_bf16 %0, %1, a[100:103], %0" : "+v"(ac[7]) : "v"(((u32x4){cA[0][6][0][0], cA[0][6][0][1], cA[0][6][1][0], cA[0][6][1][1]})) : ); SB();
  asm volatile("ds_read2_b32 %0, %1 offset0:34 offset1:35" : "=&v"(cA[1][1][1]) : "v"(aa[0]) : "memory"); SB();
  asm volatile("v_mfma_f32_16x16x32_bf16 %0, %1, a[100:103], %0" : "+v"(ac[3]) : "v"(((u32x4){cA[0][2][0][0], cA[0][2][0][1], cA[0][2][1][0], cA[0][2][1][1]})) : ); SB();
  asm volatile("ds_read_b128 a[148:151], %0 offset:2688" :  : "v"(bb) : "memory"); SB();
  asm volatile("v_mfma_f32_16x16x32_bf16 %0, %1, a[100:103], %0" : "+v"(ac[2]) : "v"(((u32x4){cA[0][1][0][0], cA[0][1][0][1], cA[0][1][1][0], cA[0][1][1][1]})) : ); SB();
  asm volatile("ds_read_b128 a[136:139], %0 offset:1152" :  : "v"(bb) : "memory"); SB();
  asm volatile("v_mfma_f32_16x16x32_bf16 %0, %1, a[100:103], 0" : "=&v"(ac[1]) : "v"(((u32x4){cA[0][0][0][0], cA[0][0][0][1], cA[0][0][1][0], cA[0][0][1][1]})) : ); SB();
  asm volatile("v_xor_b32 %0, 262144, %0" : "+v"(vB) :  : "memory"); SB();
  asm volatile("s_waitcnt lgkmcnt(7)\n\tv_mfma_f32_16x16x32_bf16 %0, %1, a[96:99], %0" : "+v"(ac[7]) : "v"(((u32x4){cA[0][7][0][0], cA[0][7][0][1], cA[0][7][1][0], cA[0][7][1][1]})) : ); SB();
  asm volatile("ds_read2_b32 %0, %1 offset0:160 offset1:161" : "=&v"(cA[1][2][0]) : "v"(aa[1]) : "memory"); SB();
  asm volatile("buffer_load_dwordx4 v[208:211], %0, %1, %2 offen offset:0 sc1" :  : "v"(vB), "s"(rWS), "s"(2048u) : "memory"); SB();
  asm volatile("v_mfma_f32_16x16x32_bf16 %0, %1, a[96:99], %0" : "+v"(ac[6]) : "v"(((u32x4){cA[0][6][0][0], cA[0][6][0][1], cA[0][6][1][0], cA[0][6][1][1]})) : ); SB();
  asm volatile("ds_read2_b32 %0, %1 offset0:162 offset1:163" : "=&v"(cA[1][2][1]) : "v"(aa[1]) : "memory"); SB();
  asm volatile("v_mfma_f32_16x16x32_bf16 %0, %1, a[96:99], %0" : "+v"(ac[5]) : "v"(((u32x4){cA[0][5][0][0], cA[0][5][0][1], cA[0][5][1][0], cA[0][5][1][1]})) : ); SB();
  asm volatile("ds_read_b128 a[140:143], %0 offset:1664" :  : "v"(bb) : "memory"); SB();
  asm volatile("buffer_load_dwordx4 v[212:215], %0, %1, %2 offen offset:1024 sc1" :  : "v"(vB), "s"(rWS), "s"(2048u) : "memory"); SB();
  asm volatile("v_mfma_f32_16x16x32_bf16 %0, %1, a[96:99], %0" : "+v"(ac[4]) : "v"(((u32x4){cA[0][4][0][0], cA[0][4][0][1], cA[0][4][1][0], cA[0][4][1][1]})) : ); SB();
  asm volatile("ds_write_b128 %0, %1 offset:0" :  : "v"(ew[0]), "v"(a1[0][0]) : "memory"); SB();
  asm volatile("v_mfma_f32_16x16x32_bf16 %0, %1, a[96:99], %0" : "+v"(ac[3]) : "v"(((u32x4){cA[0][3][0][0], cA[0][3][0][1], cA[0][3][1][0], cA[0][3][1][1]})) : ); SB();
  asm volatile("ds_read_b128 a[144:147], %0 offset:2176" :  : "v"(bb) : "memory"); SB();
  asm volatile("v_mfma_f32_16x16x32_bf16 %0, %1, a[96:99], %0" : "+v"(ac[2]) : "v"(((u32x4){cA[0][2][0][0], cA[0][2][0][1], cA[0][2][1][0], cA[0][2][1][1]})) : ); SB();
  asm volatile("ds_read2_b32 %0, %1 offset0:32 offset1:33" : "=&v"(cA[1][3][0]) : "v"(aa[1]) : "memory"); SB();
  asm volatile("v_mfma_f32_16x16x32_bf16 %0, %1, a[96:99], %0" : "+v"(ac[1]) : "v"(((u32x4){cA[0][1][0][0], cA[0][1][0][1], cA[0][1][1][0], cA[0][1][1][1]})) : ); SB();
  asm volatile("ds_read2_b32 %0, %1 offset0:34 offset1:35" : "=&v"(cA[1][3][1]) : "v"(aa[1]) : "memory"); SB();
  asm volatile("v_mfma_f32_16x16x32_bf16 %0, %1, a[96:99], 0" : "=&v"(ac[0]) : "v"(((u32x4){cA[0][0][0][0], cA[0][0][0][1], cA[0][0][1][0], cA[0][0][1][1]})) : ); SB();
  asm volatile("buffer_load_dwordx4 v[216:219], %0, %1, %2 offen offset:0 sc1" :  : "v"(vB), "s"(rWS), "s"(67584u) : "memory"); SB();
  asm volatile("v_mfma_f32_16x16x32_bf16 %0, %1, a[156:159], %0" : "+v"(ac[7]) : "v"(((u32x4){cA[1][0][0][0], cA[1][0][0][1], cA[1][0][1][0], cA[1][0][1][1]})) : ); SB();
  asm volatile("ds_read2_b32 %0, %1 offset0:160 offset1:161" : "=&v"(cA[1][4][0]) : "v"(aa[2]) : "memory"); SB();
  asm volatile("s_waitcnt lgkmcnt(7)\n\tv_mfma_f32_16x16x32_bf16 %0, %1, a[152:155], %0" : "+v"(ac[6]) : "v"(((u32x4){cA[1][0][0][0], cA[1][0][0][1], cA[1][0][1][0], cA[1][0][1][1]})) : ); SB();
  asm volatile("ds_read2_b32 %0, %1 offset0:162 offset1:163" : "=&v"(cA[1][4][1]) : "v"(aa[2]) : "memory"); SB();
  asm volatile("v_mfma_f32_16x16x32_bf16 %0, %1, a[128:131], %0" : "+v"(ac[0]) : "v"(((u32x4){cA[1][0][0][0], cA[1][0][0][1], cA[1][0][1][0], cA[1][0][1][1]})) : ); SB();
  asm volatile("ds_read2_b32 %0, %1 offset0:32 offset1:33" : "=&v"(cA[1][5][0]) : "v"(aa[2]) : "memory"); SB();
  asm volatile("v_mfma_f32_16x16x32_bf16 %0, %1, a[132:135], %0" : "+v"(ac[1]) : "v"(((u32x4){cA[1][0][0][0], cA[1][0][0][1], cA[1][0][1][0], cA[1][0][1][1]})) : ); SB();
  asm volatile("buffer_load_dwordx4 v[220:223], %0, %1, %2 offen offset:1024 sc1" :  : "v"(vB), "s"(rWS), "s"(67584u) : "memory"); SB();
  asm volatile("v_mfma_f32_16x16x32_bf16 %0, %1, a[152:155], %0" : "+v"(ac[7]) : "v"(((u32x4){cA[1][1][0][0], cA[1][1][0][1], cA[1][1][1][0], cA[1][1][1][1]})) : ); SB();
  asm volatile("ds_read2_b32 %0, %1 offset0:34 offset1:35" : "=&v"(cA[1][5][1]) : "v"(aa[2]) : "memory"); SB();
  asm volatile("v_mfma_f32_16x16x32_bf16 %0, %1, a[148:151], %0" : "+v"(ac[6]) : "v"(((u32x4){cA[1][1][0][0], cA[1][1][0][1], cA[1][1][1][0], cA[1][1][1][1]})) : ); SB();
  asm volatile("ds_read2_b32 %0, %1 offset0:160 offset1:161" : "=&v"(cA[1][6][0]) : "v"(aa[3]) : "memory"); SB();
  asm volatile("v_mfma_f32_16x16x32_bf16 %0, %1, a[136:139], %0" : "+v"(ac[2]) : "v"(((u32x4){cA[1][0][0][0], cA[1][0][0][1], cA[1][0][1][0], cA[1][0][1][1]})) : ); SB();
  asm volatile("ds_read2_b32 %0, %1 offset0:162 offset1:163" : "=&v"(cA[1][6][1]) : "v"(aa[3]) : "memory"); SB();
  asm volatile("v_mfma_f32_16x16x32_bf16 %0, %1, a[128:131], %0" : "+v"(ac[1]) : "v"(((u32x4){cA[1][1][0][0], cA[1][1][0][1], cA[1][1][1][0], cA[1][1][1][1]})) : ); SB();
  asm volatile("ds_write_b128 %0, %1 offset:0" :  : "v"(dd), "v"(ac[0]) : "memory"); SB();
  asm volatile("s_waitcnt lgkmcnt(7)\n\tv_mfma_f32_16x16x32_bf16 %0, %1, a[148:151], %0" : "+v"(ac[7]) : "v"(((u32x4){cA[1][2][0][0], cA[1][2][0][1], cA[1][2][1][0], cA[1][2][1][1]})) : ); SB();
  asm volatile("ds_read2_b32 %0, %1 offset0:32 offset1:33" : "=&v"(cA[1][7][0]) : "v"(aa[3]) : "memory"); SB();
  asm volatile("v_mfma_f32_16x16x32_bf16 %0, %1, a[140:143], %0" : "+v"(ac[3]) : "v"(((u32x4){cA[1][0][0][0], cA[1][0][0][1], cA[1][0][1][0], cA[1][0][1][1]})) : ); SB();
  asm volatile("ds_read2_b32 %0, %1 offset0:34 offset1:35" : "=&v"(cA[1][7][1]) : "v"(aa[3]) : "memory"); SB();
  asm volatile("v_mfma_f32_16x16x32_bf16 %0, %1, a[132:135], %0" : "+v"(ac[2]) : "v"(((u32x4){cA[1][1][0][0], cA[1][1][0][1], cA[1][1][1][0], cA[1][1][1][1]})) : ); SB();
  asm volatile("v_xor_b32 %0, 131072, %0" : "+v"(vB) :  : "memory"); SB();
  asm volatile("v_mfma_f32_16x16x32_bf16 %0, %1, a[144:147], %0" : "+v"(ac[4]) : "v"(((u32x4){cA[1][0][0][0], cA[1][0][0][1], cA[1][0][1][0], cA[1][0][1][1]})) : ); SB();
  asm volatile("ds_write_b128 %0, %1 offset:1024" :  : "v"(dd), "v"(ac[1]) : "memory"); SB();
  asm volatile("v_mfma_f32_16x16x32_bf16 %0, %1, a[144:147], %0" : "+v"(ac[7]) : "v"(((u32x4){cA[1][3][0][0], cA[1][3][0][1], cA[1][3][1][0], cA[1][3][1][1]})) : ); SB();
  asm volatile("buffer_load_dwordx4 v[224:227], %0, %1, %2 offen offset:0 sc1" :  : "v"(vB), "s"(rWS), "s"(2048u) : "memory"); SB();
  asm volatile("v_mfma_f32_16x16x32_bf16 %0, %1, a[136:139], %0" : "+v"(ac[3]) : "v"(((u32x4){cA[1][1][0][0], cA[1][1][0][1], cA[1][1][1][0], cA[1][1][1][1]})) : ); SB();
  asm volatile("buffer_load_dwordx4 v[228:231], %0, %1, %2 offen offset:1024 sc1" :  : "v"(vB), "s"(rWS), "s"(2048u) : "memory"); SB();
  asm volatile("v_mfma_f32_16x16x32_bf16 %0, %1, a[128:131], %0" : "+v"(ac[2]) : "v"(((u32x4){cA[1][2][0][0], cA[1][2][0][1], cA[1][2][1][0], cA[1][2][1][1]})) : ); SB();
  asm volatile("ds_write_b128 %0, %1 offset:1024" :  : "v"(ew[0]), "v"(a1[0][1]) : "memory"); SB();
  asm volatile("v_mfma_f32_16x16x32_bf16 %0, %1, a[140:143], %0" : "+v"(ac[4]) : "v"(((u32x4){cA[1][1][0][0], cA[1][1][0][1], cA[1][1][1][0], cA[1][1][1][1]})) : ); SB();
  asm volatile("buffer_load_dwordx4 v[232:235], %0, %1, %2 offen offset:0 sc1" :  : "v"(vB), "s"(rWS), "s"(67584u) : "memory"); SB();
  asm volatile("v_mfma_f32_16x16x32_bf16 %0, %1, a[148:151], %0" : "+v"(ac[5]) : "v"(((u32x4){cA[1][0][0][0], cA[1][0][0][1], cA[1][0][1][0], cA[1][0][1][1]})) : ); SB();
  asm volatile("ds_write_b128 %0, %1 offset:2048" :  : "v"(ew[0]), "v"(a1[0][2]) : "memory"); SB();
  asm volatile("v_mfma_f32_16x16x32_bf16 %0, %1, a[132:135], %0" : "+v"(ac[3]) : "v"(((u32x4){cA[1][2][0][0], cA[1][2][0][1], cA[1][2][1][0], cA[1][2][1][1]})) : ); SB();
  asm volatile("buffer_load_dwordx4 v[236:239], %0, %1, %2 offen offset:1024 sc1" :  : "v"(vB), "s"(rWS), "s"(67584u) : "memory"); SB();
  asm volatile("v_mfma_f32_16x16x32_bf16 %0, %1, a[144:147], %0" : "+v"(ac[6]) : "v"(((u32x4){cA[1][2][0][0], cA[1][2][0][1], cA[1][2][1][0], cA[1][2][1][1]})) : ); SB();
  asm volatile("ds_write_b128 %0, %1 offset:2048" :  : "v"(dd), "v"(ac[2]) : "memory"); SB();
  asm volatile("v_mfma_f32_16x16x32_bf16 %0, %1, a[136:139], %0" : "+v"(ac[4]) : "v"(((u32x4){cA[1][2][0][0], cA[1][2][0][1], cA[1][2][1][0], cA[1][2][1][1]})) : ); SB();
  asm volatile("v_xor_b32 %0, 393216, %0" : "+v"(vB) :  : "memory"); SB();
  asm volatile("v_mfma_f32_16x16x32_bf16 %0, %1, a[144:147], %0" : "+v"(ac[5]) : "v"(((u32x4){cA[1][1][0][0], cA[1][1][0][1], cA[1][1][1][0], cA[1][1][1][1]})) : ); SB();
  asm volatile("buffer_load_dwordx4 v[240:243], %0, %1, %2 offen offset:0 sc1" :  : "v"(vB), "s"(rWS), "s"(2048u) : "memory"); SB();
  asm volatile("v_mfma_f32_16x16x32_bf16 %0, %1, a[128:131], %0" : "+v"(ac[3]) : "v"(((u32x4){cA[1][3][0][0], cA[1][3][0][1], cA[1][3][1][0], cA[1][3][1][1]})) : ); SB();
  asm volatile("ds_write_b128 %0, %1 offset:0" :  : "v"(ew[1]), "v"(a1[1][0]) : "memory"); SB();
  asm volatile("v_mfma_f32_16x16x32_bf16 %0, %1, a[140:143], %0" : "+v"(ac[6]) : "v"(((u32x4){cA[1][3][0][0], cA[1][3][0][1], cA[1][3][1][0], cA[1][3][1][1]})) : ); SB();
  asm volatile("buffer_load_dwordx4 v[244:247], %0, %1, %2 offen offset:1024 sc1" :  : "v"(vB), "s"(rWS), "s"(2048u) : "memory"); SB();
  asm volatile("v_mfma_f32_16x16x32_bf16 %0, %1, a[132:135], %0" : "+v"(ac[4]) : "v"(((u32x4){cA[1][3][0][0], cA[1][3][0][1], cA[1][3][1][0], cA[1][3][1][1]})) : ); SB();
  asm volatile("ds_write_b128 %0, %1 offset:1024" :  : "v"(ew[1]), "v"(a1[1][1]) : "memory"); SB();
  asm volatile("v_mfma_f32_16x16x32_bf16 %0, %1, a[140:143], %0" : "+v"(ac[5]) : "v"(((u32x4){cA[1][2][0][0], cA[1][2][0][1], cA[1][2][1][0], cA[1][2][1][1]})) : ); SB();
  asm volatile("s_waitcnt lgkmcnt(3)\n\tv_mfma_f32_16x16x32_bf16 %0, %1, a[140:143], %0" : "+v"(ac[7]) : "v"(((u32x4){cA[1][4][0][0], cA[1][4][0][1], cA[1][4][1][0], cA[1][4][1][1]})) : ); SB();
  asm volatile("ds_write_b128 %0, %1 offset:3072" :  : "v"(dd), "v"(ac[3]) : "memory"); SB();
  asm volatile("v_mfma_f32_16x16x32_bf16 %0, %1, a[136:139], %0" : "+v"(ac[6]) : "v"(((u32x4){cA[1][4][0][0], cA[1][4][0][1], cA[1][4][1][0], cA[1][4][1][1]})) : ); SB();
  asm volatile("buffer_load_dwordx4 v[248:251], %0, %1, %2 offen offset:0 sc1" :  : "v"(vB), "s"(rWS), "s"(67584u) : "memory"); SB();
  asm volatile("v_mfma_f32_16x16x32_bf16 %0, %1, a[128:131], %0" : "+v"(ac[4]) : "v"(((u32x4){cA[1][4][0][0], cA[1][4][0][1], cA[1][4][1][0], cA[1][4][1][1]})) : ); SB();
  asm volatile("buffer_load_dwordx4 v[252:255], %0, %1, %2 offen offset:1024 sc1" :  : "v"(vB), "s"(rWS), "s"(67584u) : "memory"); SB();
  asm volatile("v_mfma_f32_16x16x32_bf16 %0, %1, a[136:139], %0" : "+v"(ac[5]) : "v"(((u32x4){cA[1][3][0][0], cA[1][3][0][1], cA[1][3][1][0], cA[1][3][1][1]})) : ); SB();
  asm volatile("v_xor_b32 %0, 131072, %0" : "+v"(vB) :  : "memory"); SB();
  asm volatile("v_mfma_f32_16x16x32_bf16 %0, %1, a[136:139], %0" : "+v"(ac[7]) : "v"(((u32x4){cA[1][5][0][0], cA[1][5][0][1], cA[1][5][1][0], cA[1][5][1][1]})) : ); SB();
  asm volatile("ds_write_b128 %0, %1 offset:2048" :  : "v"(ew[1]), "v"(a1[1][2]) : "memory"); SB();
  asm volatile("v_mfma_f32_16x16x32_bf16 %0, %1, a[132:135], %0" : "+v"(ac[6]) : "v"(((u32x4){cA[1][5][0][0], cA[1][5][0][1], cA[1][5][1][0], cA[1][5][1][1]})) : ); SB();
  asm volatile("buffer_load_dwordx4 %0, %1, %2 offen offset:2048 lds" :  : "v"(voS), "s"(rA), "s"(ao[6]) : "memory"); SB();
  asm volatile("v_mfma_f32_16x16x32_bf16 %0, %1, a[132:135], %0" : "+v"(ac[5]) : "v"(((u32x4){cA[1][4][0][0], cA[1][4][0][1], cA[1][4][1][0], cA[1][4][1][1]})) : ); SB();
  asm volatile("ds_write_b128 %0, %1 offset:4096" :  : "v"(dd), "v"(ac[4]) : "memory"); SB();
  asm volatile("v_mfma_f32_16x16x32_bf16 %0, %1, a[132:135], %0" : "+v"(ac[7]) : "v"(((u32x4){cA[1][6][0][0], cA[1][6][0][1], cA[1][6][1][0], cA[1][6][1][1]})) : ); SB();
  asm volatile("buffer_load_dwordx4 %0, %1, %2 offen offset:3072 lds" :  : "v"(voS), "s"(rA), "s"(ao[7]) : "memory"); SB();
  asm volatile("v_mfma_f32_16x16x32_bf16 %0, %1, a[128:131], %0" : "+v"(ac[6]) : "v"(((u32x4){cA[1][6][0][0], cA[1][6][0][1], cA[1][6][1][0], cA[1][6][1][1]})) : ); SB();
  asm volatile("v_xor_b32 %0, 128, %0" : "+v"(voS) :  : "memory"); SB();
  asm volatile("v_mfma_f32_16x16x32_bf16 %0, %1, a[128:131], %0" : "+v"(ac[5]) : "v"(((u32x4){cA[1][5][0][0], cA[1][5][0][1], cA[1][5][1][0], cA[1][5][1][1]})) : ); SB();
  asm volatile("v_mfma_f32_16x16x32_bf16 %0, %1, a[128:131], %0" : "+v"(ac[7]) : "v"(((u32x4){cA[1][7][0][0], cA[1][7][0][1], cA[1][7][1][0], cA[1][7][1][1]})) : ); SB();
  asm volatile("s_add_u32 m0, %0, 98304" :  : "s"(mA) : "memory", "scc"); SB();
  asm volatile("s_nop 0\n\tbuffer_load_dwordx4 %0, %1, %2 offen lds" :  : "v"(voS), "s"(rA), "s"(ao[0]) : "memory"); SB();
  asm volatile("s_nop 5" : "+v"(ac[5]), "+v"(ac[6]), "+v"(ac[7]) :  : ); SB();
  asm volatile("ds_write_b128 %0, %1 offset:6144" :  : "v"(dd), "v"(ac[6]) : "memory"); SB();
  asm volatile("ds_write_b128 %0, %1 offset:5120" :  : "v"(dd), "v"(ac[5]) : "memory"); SB();
  asm volatile("ds_write_b128 %0, %1 offset:7168" :  : "v"(dd), "v"(ac[7]) : "memory"); SB();
  } else {
  asm volatile("; ELSE_BEGIN"); SB();
  asm volatile("s_waitcnt lgkmcnt(0)" ::: "memory"); SB();
  asm volatile("buffer_load_dwordx4 v[180:183], %0, %1, %2 offen offset:1024 sc1" :  : "v"(vB), "s"(rWS), "s"(0u) : "memory"); SB();
  asm volatile("s_waitcnt lgkmcnt(0)" ::: "memory"); SB();
  asm volatile("buffer_load_dwordx4 v[184:187], %0, %1, %2 offen offset:0 sc1" :  : "v"(vB), "s"(rWS), "s"(65536u) : "memory"); SB();
  asm volatile("s_waitcnt lgkmcnt(0)" ::: "memory"); SB();
  asm volatile("buffer_load_dwordx4 v[188:191], %0, %1, %2 offen offset:1024 sc1" :  : "v"(vB), "s"(rWS), "s"(65536u) : "memory"); SB();
  asm volatile("s_waitcnt lgkmcnt(0)" ::: "memory"); SB();
  asm volatile("v_xor_b32 %0, 393216, %0" : "+v"(vB) :  : "memory"); SB();
  asm volatile("s_waitcnt lgkmcnt(0)" ::: "memory"); SB();
  asm volatile("buffer_load_dwordx4 v[192:195], %0, %1, %2 offen offset:0 sc1" :  : "v"(vB), "s"(rWS), "s"(0u) : "memory"); SB();
  asm volatile("s_waitcnt lgkmcnt(0)" ::: "memory"); SB();
  asm volatile("buffer_load_dwordx4 v[196:199], %0, %1, %2 offen offset:1024 sc1" :  : "v"(vB), "s"(rWS), "s"(0u) : "memory"); SB();
  asm volatile("s_waitcnt lgkmcnt(0)" ::: "memory"); SB();
  asm volatile("buffer_load_dwordx4 v[200:203], %0, %1, %2 offen offset:0 sc1" :  : "v"(vB), "s"(rWS), "s"(65536u) : "memory"); SB();
  asm volatile("s_waitcnt lgkmcnt(0)" ::: "memory"); SB();
  asm volatile("buffer_load_dwordx4 v[204:207], %0, %1, %2 offen offset:1024 sc1" :  : "v"(vB), "s"(rWS), "s"(65536u) : "memory"); SB();
  asm volatile("s_waitcnt lgkmcnt(0)" ::: "memory"); SB();
  asm volatile("v_xor_b32 %0, 262144, %0" : "+v"(vB) :  : "memory"); SB();
  asm volatile("s_waitcnt lgkmcnt(0)" ::: "memory"); SB();
  asm volatile("buffer_load_dwordx4 v[208:211], %0, %1, %2 offen offset:0 sc1" :  : "v"(vB), "s"(rWS), "s"(2048u) : "memory"); SB();
  asm volatile("s_waitcnt lgkmcnt(0)" ::: "memory"); SB();
  asm volatile("buffer_load_dwordx4 v[212:215], %0, %1, %2 offen offset:1024 sc1" :  : "v"(vB), "s"(rWS), "s"(2048u) : "memory"); SB();
  asm volatile("s_waitcnt lgkmcnt(0)" ::: "memory"); SB();
  asm volatile("ds_write_b128 %0, %1 offset:0" :  : "v"(ew[0]), "v"(a1[0][0]) : "memory"); SB();
  asm volatile("s_waitcnt lgkmcnt(0)" ::: "memory"); SB();
  asm volatile("buffer_load_dwordx4 v[216:219], %0, %1, %2 offen offset:0 sc1" :  : "v"(vB), "s"(rWS), "s"(67584u) : "memory"); SB();
  asm volatile("s_waitcnt lgkmcnt(0)" ::: "memory"); SB();
  asm volatile("buffer_load_dwordx4 v[220:223], %0, %1, %2 offen offset:1024 sc1" :  : "v"(vB), "s"(rWS), "s"(67584u) : "memory"); SB();
  asm volatile("s_waitcnt lgkmcnt(0)" ::: "memory"); SB();
  asm volatile("v_xor_b32 %0, 131072, %0" : "+v"(vB) :  : "memory"); SB();
  asm volatile("s_waitcnt lgkmcnt(0)" ::: "memory"); SB();
  asm volatile("buffer_load_dwordx4 v[224:227], %0, %1, %2 offen offset:0 sc1" :  : "v"(vB), "s"(rWS), "s"(2048u) : "memory"); SB();
  asm volatile("s_waitcnt lgkmcnt(0)" ::: "memory"); SB();
  asm volatile("buffer_load_dwordx4 v[228:231], %0, %1, %2 offen offset:1024 sc1" :  : "v"(vB), "s"(rWS), "s"(2048u) : "memory"); SB();
  asm volatile("s_waitcnt lgkmcnt(0)" ::: "memory"); SB();
  asm volatile("ds_write_b128 %0, %1 offset:1024" :  : "v"(ew[0]), "v"(a1[0][1]) : "memory"); SB();
  asm volatile("s_waitcnt lgkmcnt(0)" ::: "memory"); SB();
  asm volatile("buffer_load_dwordx4 v[232:235], %0, %1, %2 offen offset:0 sc1" :  : "v"(vB), "s"(rWS), "s"(67584u) : "memory"); SB();
  asm volatile("s_waitcnt lgkmcnt(0)" ::: "memory"); SB();
  asm volatile("ds_write_b128 %0, %1 offset:2048" :  : "v"(ew[0]), "v"(a1[0][2]) : "memory"); SB();
  asm volatile("s_waitcnt lgkmcnt(0)" ::: "memory"); SB();
  asm volatile("buffer_load_dwordx4 v[236:239], %0, %1, %2 offen offset:1024 sc1" :  : "v"(vB), "s"(rWS), "s"(67584u) : "memory"); SB();
  asm volatile("s_waitcnt lgkmcnt(0)" ::: "memory"); SB();
  asm volatile("v_xor_b32 %0, 393216, %0" : "+v"(vB) :  : "memory"); SB();
  asm volatile("s_waitcnt lgkmcnt(0)" ::: "memory"); SB();
  asm volatile("buffer_load_dwordx4 v[240:243], %0, %1, %2 offen offset:0 sc1" :  : "v"(vB), "s"(rWS), "s"(2048u) : "memory"); SB();
  asm volatile("s_waitcnt lgkmcnt(0)" ::: "memory"); SB();
  asm volatile("ds_write_b128 %0, %1 offset:0" :  : "v"(ew[1]), "v"(a1[1][0]) : "memory"); SB();
  asm volatile("s_waitcnt lgkmcnt(0)" ::: "memory"); SB();
  asm volatile("buffer_load_dwordx4 v[244:247], %0, %1, %2 offen offset:1024 sc1" :  : "v"(vB), "s"(rWS), "s"(2048u) : "memory"); SB();
  asm volatile("s_waitcnt lgkmcnt(0)" ::: "memory"); SB();
  asm volatile("ds_write_b128 %0, %1 offset:1024" :  : "v"(ew[1]), "v"(a1[1][1]) : "memory"); SB();
  asm volatile("s_waitcnt lgkmcnt(0)" ::: "memory"); SB();
  asm volatile("buffer_load_dwordx4 v[248:251], %0, %1, %2 offen offset:0 sc1" :  : "v"(vB), "s"(rWS), "s"(67584u) : "memory"); SB();
  asm volatile("s_waitcnt lgkmcnt(0)" ::: "memory"); SB();
  asm volatile("buffer_load_dwordx4 v[252:255], %0, %1, %2 offen offset:1024 sc1" :  : "v"(vB), "s"(rWS), "s"(67584u) : "memory"); SB();
  asm volatile("s_waitcnt lgkmcnt(0)" ::: "memory"); SB();
  asm volatile("v_xor_b32 %0, 131072, %0" : "+v"(vB) :  : "memory"); SB();
  asm volatile("s_waitcnt lgkmcnt(0)" ::: "memory"); SB();
  asm volatile("ds_write_b128 %0, %1 offset:2048" :  : "v"(ew[1]), "v"(a1[1][2]) : "memory"); SB();
  asm volatile("s_waitcnt lgkmcnt(0)" ::: "memory"); SB();
  asm volatile("s_waitcnt lgkmcnt(0)" ::: "memory"); SB();
  asm volatile("buffer_load_dwordx4 %0, %1, %2 offen offset:2048 lds" :  : "v"(voS), "s"(rA), "s"(ao[6]) : "memory"); SB();
  asm volatile("s_waitcnt lgkmcnt(0)" ::: "memory"); SB();
  asm volatile("s_waitcnt lgkmcnt(0)" ::: "memory"); SB();
  asm volatile("buffer_load_dwordx4 %0, %1, %2 offen offset:3072 lds" :  : "v"(voS), "s"(rA), "s"(ao[7]) : "memory"); SB();
  asm volatile("s_waitcnt lgkmcnt(0)" ::: "memory"); SB();
  asm volatile("s_waitcnt lgkmcnt(0)" ::: "memory"); SB();
  asm volatile("v_xor_b32 %0, 128, %0" : "+v"(voS) :  : "memory"); SB();
  asm volatile("s_waitcnt lgkmcnt(0)" ::: "memory"); SB();
  asm volatile("s_add_u32 m0, %0, 98304" :  : "s"(mA) : "memory", "scc"); SB();
  asm volatile("s_waitcnt lgkmcnt(0)" ::: "memory"); SB();
  asm volatile("s_nop 0\n\tbuffer_load_dwordx4 %0, %1, %2 offen lds" :  : "v"(voS), "s"(rA), "s"(ao[0]) : "memory"); SB();
  asm volatile("s_waitcnt lgkmcnt(0) ; ELSE_END" ::: "memory"); SB();
  }
  ST(15);
  f32x4 g[2][4][2], pr[6], yq[8], bs4 = {bs, bs, bs, bs};
  u32x4 ho[3], ykv, y2;
  u32x2 xv[5];
  float gt[4], ge[4];
  unsigned vH_, pw2, pe[3], vX, vO, vO2, vFH, vFY, vHr, hso[8], ys2, ydr, ykr, er, sFH, sFY;
  int dl;
  asm volatile("" : "+v"(bs4)); SB();
  asm volatile("s_waitcnt vmcnt(25)" :  :  : "memory"); SB();
  asm volatile("v_mfma_f32_16x16x32_bf16 %0, v[160:163], a[160:163], %1" : "=&v"(g[0][0][0]) : "v"(bs4) : ); SB();
  { unsigned lane = lane0, w = w0, rb = rb0, cb = cb0;
  asm volatile("" : "+v"(lane)); asm volatile("" : "+s"(w), "+s"(rb), "+s"(cb)); SB();

  asm volatile("v_mfma_f32_16x16x32_bf16 %0, v[160:163], a[164:167], 0" : "=&v"(g[0][1][0]) :  : ); SB();
  er = lb + 32768 + w * 6144 + l16; asm volatile("" : "+v"(er)); SB();
  asm volatile("v_mfma_f32_16x16x32_bf16 %0, v[160:163], a[168:171], 0" : "=&v"(g[0][2][0]) :  : ); SB();
  asm volatile("s_waitcnt lgkmcnt(0)\n\ts_barrier" :  :  : "memory"); SB();
  asm volatile("ds_read_b128 %0, %1 offset:0" : "=&v"(a1[0][0]) : "v"(er) : "memory"); SB();
  asm volatile("v_mfma_f32_16x16x32_bf16 %0, v[160:163], a[172:175], 0" : "=&v"(g[0][3][0]) :  : ); SB();
  asm volatile("ds_read_b128 %0, %1 offset:1024" : "=&v"(a1[0][1]) : "v"(er) : "memory"); SB();
  asm volatile("v_mfma_f32_16x16x32_bf16 %0, v[164:167], a[160:163], %1" : "=&v"(g[0][0][1]) : "v"(bs4) : ); SB();
  asm volatile("ds_read_b128 %0, %1 offset:2048" : "=&v"(a1[0][2]) : "v"(er) : "memory"); SB();
  asm volatile("v_mfma_f32_16x16x32_bf16 %0, v[164:167], a[164:167], 0" : "=&v"(g[0][1][1]) :  : ); SB();
  asm volatile("ds_read_b128 %0, %1 offset:3072" : "=&v"(a1[1][0]) : "v"(er) : "memory"); SB();
  asm volatile("v_mfma_f32_16x16x32_bf16 %0, v[164:167], a[168:171], 0" : "=&v"(g[0][2][1]) :  : ); SB();
  asm volatile("ds_read_b128 %0, %1 offset:4096" : "=&v"(a1[1][1]) : "v"(er) : "memory"); SB();
  asm volatile("v_mfma_f32_16x16x32_bf16 %0, v[164:167], a[172:175], 0" : "=&v"(g[0][3][1]) :  : ); SB();
  asm volatile("ds_read_b128 %0, %1 offset:5120" : "=&v"(a1[1][2]) : "v"(er) : "memory"); SB();
  ST(25);
  asm volatile("s_waitcnt vmcnt(23)" :  :  : "memory"); SB();
  asm volatile("v_mfma_f32_16x16x32_bf16 %0, v[168:171], a[176:179], %0" : "+v"(g[0][0][0]) :  : ); SB();
  ydr = lb + 0 + (w * 2 + (lane >> 5)) * 1024 + (lane & 1) * 512; asm volatile("" : "+v"(ydr)); SB();
  asm volatile("v_mfma_f32_16x16x32_bf16 %0, v[168:171], a[180:183], %0" : "+v"(g[0][1][0]) :  : ); SB();
  asm volatile("ds_read_b128 a[96:99], %0 offset:32768" :  : "v"(aA[0]) : "memory"); SB();
  asm volatile("v_mfma_f32_16x16x32_bf16 %0, v[168:171], a[184:187], %0" : "+v"(g[0][2][0]) :  : ); SB();
  asm volatile("ds_read_b128 a[112:115], %0 offset:32768" :  : "v"(aA[1]) : "memory"); SB();
  asm volatile("v_mfma_f32_16x16x32_bf16 %0, v[168:171], a[188:191], %0" : "+v"(g[0][3][0]) :  : ); SB();
  asm volatile("ds_read_b128 a[100:103], %0 offset:34816" :  : "v"(aA[0]) : "memory"); SB();
  asm volatile("v_mfma_f32_16x16x32_bf16 %0, v[172:175], a[176:179], %0" : "+v"(g[0][0][1]) :  : ); SB();
  asm volatile("ds_read_b128 a[116:119], %0 offset:34816" :  : "v"(aA[1]) : "memory"); SB();
  asm volatile("v_mfma_f32_16x16x32_bf16 %0, v[172:175], a[180:183], %0" : "+v"(g[0][1][1]) :  : ); SB();
  asm volatile("ds_read_b128 a[104:107], %0 offset:36864" :  : "v"(aA[0]) : "memory"); SB();
  asm volatile("v_mfma_f32_16x16x32_bf16 %0, v[172:175], a[184:187], %0" : "+v"(g[0][2][1]) :  : ); SB();
  asm volatile("ds_read_b128 a[120:123], %0 offset:36864" :  : "v"(aA[1]) : "memory"); SB();
  asm volatile("v_mfma_f32_16x16x32_bf16 %0, v[172:175], a[188:191], %0" : "+v"(g[0][3][1]) :  : ); SB();
  asm volatile("buffer_load_dwordx4 v[160:163], %0, %1, %2 offen offset:0 sc1" :  : "v"(vB), "s"(rWS), "s"(0u) : "memory"); SB();
  asm volatile("s_waitcnt vmcnt(22)" :  :  : "memory"); SB();
  asm volatile("v_mfma_f32_16x16x32_bf16 %0, v[176:179], a[192:195], %0" : "+v"(g[0][0][0]) :  : ); SB();
  asm volatile("buffer_load_dwordx4 v[164:167], %0, %1, %2 offen offset:1024 sc1" :  : "v"(vB), "s"(rWS), "s"(0u) : "memory"); SB();
  asm volatile("v_mfma_f32_16x16x32_bf16 %0, v[176:179], a[196:199], %0" : "+v"(g[0][1][0]) :  : ); SB();
  ydr = ydr + ((((lane >> 1) & 15) ^ ((lane & 1) << 3)) << 4); asm volatile("" : "+v"(ydr)); SB();
  asm volatile("v_mfma_f32_16x16x32_bf16 %0, v[176:179], a[200:203], %0" : "+v"(g[0][2][0]) :  : ); SB();
  ykr = lb + 59520 + 512 + w * 1024 + l16; asm volatile("" : "+v"(ykr)); SB();
  asm volatile("v_mfma_f32_16x16x32_bf16 %0, v[176:179], a[204:207], %0" : "+v"(g[0][3][0]) :  : ); SB();
  asm volatile("buffer_load_dwordx4 v[168:171], %0, %1, %2 offen offset:0 sc1" :  : "v"(vB), "s"(rWS), "s"(65536u) : "memory"); SB();
  pw2 = er + (122880 - 32768); asm volatile("" : "+v"(pw2)); SB();
  asm volatile("v_mfma_f32_16x16x32_bf16 %0, v[180:183], a[192:195], %0" : "+v"(g[0][0][1]) :  : ); SB();
  pe[0] = lb + 122880 + (w ^ 1) * 6144 + l16; asm volatile("" : "+v"(pe[0])); SB();
  asm volatile("v_mfma_f32_16x16x32_bf16 %0, v[180:183], a[196:199], %0" : "+v"(g[0][1][1]) :  : ); SB();
  pe[1] = lb + 122880 + (w ^ 2) * 6144 + l16; asm volatile("" : "+v"(pe[1])); SB();
  asm volatile("v_mfma_f32_16x16x32_bf16 %0, v[180:183], a[200:203], %0" : "+v"(g[0][2][1]) :  : ); SB();
  pe[2] = lb + 122880 + (w ^ 3) * 6144 + l16; asm volatile("" : "+v"(pe[2])); SB();
  asm volatile("v_mfma_f32_16x16x32_bf16 %0, v[180:183], a[204:207], %0" : "+v"(g[0][3][1]) :  : ); SB();
  asm volatile("buffer_load_dwordx4 v[172:175], %0, %1, %2 offen offset:1024 sc1" :  : "v"(vB), "s"(rWS), "s"(65536u) : "memory"); SB();
  asm volatile("s_waitcnt vmcnt(23)" :  :  : "memory"); SB();
  asm volatile("v_mfma_f32_16x16x32_bf16 %0, v[184:187], a[208:211], %0" : "+v"(g[0][0][0]) :  : ); SB();
  asm volatile("v_xor_b32 %0, 917504, %0" : "+v"(vB) :  : "memory"); SB();
  asm volatile("s_waitcnt lgkmcnt(0)" : "+v"(a1[0][0]), "+v"(a1[0][1]), "+v"(a1[0][2]), "+v"(a1[1][0]), "+v"(a1[1][1]), "+v"(a1[1][2]) :  : "memory"); SB();
  asm volatile("v_mfma_f32_16x16x32_bf16 %0, v[184:187], a[212:215], %0" : "+v"(g[0][1][0]) :  : ); SB();
  dl = (int)(wm * 12928 - wn * 256 + w * 128) - 2 * 12928; asm volatile("" : "+s"(dl)); SB();
  asm volatile("v_mfma_f32_16x16x32_bf16 %0, v[184:187], a[216:219], %0" : "+v"(g[0][2][0]) :  : ); SB();
  asm volatile("buffer_load_dwordx4 v[176:179], %0, %1, %2 offen offset:0 sc1" :  : "v"(vB), "s"(rWS), "s"(0u) : "memory"); SB();
  asm volatile("v_add_u32 %0, %1, %0" : "+v"(aa[0]) : "s"(dl) : ); SB();
  asm volatile("v_mfma_f32_16x16x32_bf16 %0, v[184:187], a[220:223], %0" : "+v"(g[0][3][0]) :  : ); SB();
  asm volatile("v_add_u32 %0, %1, %0" : "+v"(aa[1]) : "s"(dl) : ); SB();
  asm volatile("v_mfma_f32_16x16x32_bf16 %0, v[188:191], a[208:211], %0" : "+v"(g[0][0][1]) :  : ); SB();
  asm volatile("v_add_u32 %0, %1, %0" : "+v"(aa[2]) : "s"(dl) : ); SB();
  asm volatile("v_mfma_f32_16x16x32_bf16 %0, v[188:191], a[212:215], %0" : "+v"(g[0][1][1]) :  : ); SB();
  asm volatile("v_add_u32 %0, %1, %0" : "+v"(aa[3]) : "s"(dl) : ); SB();
  asm volatile("v_mfma_f32_16x16x32_bf16 %0, v[188:191], a[216:219], %0" : "+v"(g[0][2][1]) :  : ); SB();
  asm volatile("buffer_load_dwordx4 v[180:183], %0, %1, %2 offen offset:1024 sc1" :  : "v"(vB), "s"(rWS), "s"(0u) : "memory"); SB();
  asm volatile("v_add_u32 %0, %1, %0" : "+v"(bb) : "s"(dl) : ); SB();
  asm volatile("v_mfma_f32_16x16x32_bf16 %0, v[188:191], a[220:223], %0" : "+v"(g[0][3][1]) :  : ); SB();
  asm volatile("v_mul_f32 %0, %1, %1" : "=v"(gt[0]) : "v"(a1[0][0][0]) : ); SB();
  asm volatile("s_waitcnt vmcnt(23)" :  :  : "memory"); SB();
  asm volatile("v_mfma_f32_16x16x32_bf16 %0, v[192:195], a[224:227], %0" : "+v"(g[0][0][0]) :  : ); SB();
  asm volatile("v_mul_f32 %0, %1, %1" : "=v"(gt[1]) : "v"(a1[0][0][1]) : ); SB();
  asm volatile("v_mfma_f32_16x16x32_bf16 %0, v[192:195], a[228:231], %0" : "+v"(g[0][1][0]) :  : ); SB();
  asm volatile("buffer_load_dwordx4 v[184:187], %0, %1, %2 offen offset:0 sc1" :  : "v"(vB), "s"(rWS), "s"(65536u) : "memory"); SB();
  asm volatile("v_mul_f32 %0, %1, %1" : "=v"(gt[2]) : "v"(a1[0][0][2]) : ); SB();
  asm volatile("v_mfma_f32_16x16x32_bf16 %0, v[192:195], a[232:235], %0" : "+v"(g[0][2][0]) :  : ); SB();
  asm volatile("v_mul_f32 %0, %1, %1" : "=v"(gt[3]) : "v"(a1[0][0][3]) : ); SB();
  asm volatile("v_mfma_f32_16x16x32_bf16 %0, v[192:195], a[236:239], %0" : "+v"(g[0][3][0]) :  : ); SB();
  asm volatile("v_fma_f32 %0, %0, %1, %2" : "+v"(gt[0]) : "v"(k2[0]), "v"(k1[0]) : ); SB();
  asm volatile("v_mfma_f32_16x16x32_bf16 %0, v[196:199], a[224:227], %0" : "+v"(g[0][0][1]) :  : ); SB();
  asm volatile("v_fma_f32 %0, %0, %1, %2" : "+v"(gt[1]) : "v"(k2[0]), "v"(k1[0]) : ); SB();
  asm volatile("v_mfma_f32_16x16x32_bf16 %0, v[196:199], a[228:231], %0" : "+v"(g[0][1][1]) :  : ); SB();
  asm volatile("buffer_load_dwordx4 v[188:191], %0, %1, %2 offen offset:1024 sc1" :  : "v"(vB), "s"(rWS), "s"(65536u) : "memory"); SB();
  asm volatile("v_fma_f32 %0, %0, %1, %2" : "+v"(gt[2]) : "v"(k2[0]), "v"(k1[0]) : ); SB();
  asm volatile("v_mfma_f32_16x16x32_bf16 %0, v[196:199], a[232:235], %0" : "+v"(g[0][2][1]) :  : ); SB();
  asm volatile("v_xor_b32 %0, 131072, %0" : "+v"(vB) :  : "memory"); SB();
  asm volatile("v_fma_f32 %0, %0, %1, %2" : "+v"(gt[3]) : "v"(k2[0]), "v"(k1[0]) : ); SB();
  asm volatile("v_mfma_f32_16x16x32_bf16 %0, v[196:199], a[236:239], %0" : "+v"(g[0][3][1]) :  : ); SB();
  asm volatile("v_mul_f32 %0, %0, %1" : "+v"(gt[0]) : "v"(a1[0][0][0]) : ); SB();
  asm volatile("s_waitcnt vmcnt(23)" :  :  : "memory"); SB();
  asm volatile("v_mfma_f32_16x16x32_bf16 %0, v[200:203], a[240:243], %0" : "+v"(g[0][0][0]) :  : ); SB();
  asm volatile("buffer_load_dwordx4 v[192:195], %0, %1, %2 offen offset:0 sc1" :  : "v"(vB), "s"(rWS), "s"(0u) : "memory"); SB();
  asm volatile("v_mul_f32 %0, %0, %1" : "+v"(gt[1]) : "v"(a1[0][0][1]) : ); SB();
  asm volatile("v_mfma_f32_16x16x32_bf16 %0, v[200:203], a[244:247], %0" : "+v"(g[0][1][0]) :  : ); SB();
  asm volatile("s_waitcnt vmcnt(10)" :  :  : "memory"); SB();
  asm volatile("ds_read_b128 a[108:111], %0 offset:38912" :  : "v"(aA[0]) : "memory"); SB();
  asm volatile("v_mfma_f32_16x16x32_bf16 %0, v[200:203], a[248:251], %0" : "+v"(g[0][2][0]) :  : ); SB();
  asm volatile("ds_read_b128 a[124:127], %0 offset:38912" :  : "v"(aA[1]) : "memory"); SB();
  asm volatile("v_mul_f32 %0, %0, %1" : "+v"(gt[2]) : "v"(a1[0][0][2]) : ); SB();
  asm volatile("v_mfma_f32_16x16x32_bf16 %0, v[200:203], a[252:255], %0" : "+v"(g[0][3][0]) :  : ); SB();
  asm volatile("v_mul_f32 %0, %0, %1" : "+v"(gt[3]) : "v"(a1[0][0][3]) : ); SB();
  asm volatile("v_mfma_f32_16x16x32_bf16 %0, v[204:207], a[240:243], %0" : "+v"(g[0][0][1]) :  : ); SB();
  asm volatile("buffer_load_dwordx4 v[196:199], %0, %1, %2 offen offset:1024 sc1" :  : "v"(vB), "s"(rWS), "s"(0u) : "memory"); SB();
  asm volatile("v_exp_f32 %0, %1" : "=v"(ge[0]) : "v"(gt[0]) : ); SB();
  asm volatile("v_mfma_f32_16x16x32_bf16 %0, v[204:207], a[244:247], %0" : "+v"(g[0][1][1]) :  : ); SB();
  asm volatile("v_exp_f32 %0, %1" : "=v"(ge[1]) : "v"(gt[1]) : ); SB();
  asm volatile("v_mfma_f32_16x16x32_bf16 %0, v[204:207], a[248:251], %0" : "+v"(g[0][2][1]) :  : ); SB();
  asm volatile("v_exp_f32 %0, %1" : "=v"(ge[2]) : "v"(gt[2]) : ); SB();
  asm volatile("v_mfma_f32_16x16x32_bf16 %0, v[204:207], a[252:255], %0" : "+v"(g[0][3][1]) :  : ); SB();
  asm volatile("buffer_load_dwordx4 %0, %1, %2 offen offset:1024 lds" :  : "v"(voS), "s"(rA), "s"(ao[1]) : "memory"); SB();
  asm volatile("v_mfma_f32_16x16x32_bf16 %0, v[208:211], a[160:163], %1" : "=&v"(g[1][0][0]) : "v"(bs4) : ); SB();
  asm volatile("v_exp_f32 %0, %1" : "=v"(ge[3]) : "v"(gt[3]) : ); SB();
  asm volatile("v_mfma_f32_16x16x32_bf16 %0, v[208:211], a[164:167], 0" : "=&v"(g[1][1][0]) :  : ); SB();
  asm volatile("v_fma_f32 %0, %0, %1, %1" : "+v"(ge[0]) : "v"(xd[0]) : ); SB();
  asm volatile("v_mfma_f32_16x16x32_bf16 %0, v[208:211], a[168:171], 0" : "=&v"(g[1][2][0]) :  : ); SB();
  asm volatile("buffer_load_dwordx4 v[200:203], %0, %1, %2 offen offset:0 sc1" :  : "v"(vB), "s"(rWS), "s"(65536u) : "memory"); SB();
  asm volatile("v_fma_f32 %0, %0, %1, %1" : "+v"(ge[1]) : "v"(xd[0]) : ); SB();
  asm volatile("v_mfma_f32_16x16x32_bf16 %0, v[208:211], a[172:175], 0" : "=&v"(g[1][3][0]) :  : ); SB();
  asm volatile("v_fma_f32 %0, %0, %1, %1" : "+v"(ge[2]) : "v"(xd[0]) : ); SB();
  asm volatile("v_mfma_f32_16x16x32_bf16 %0, v[212:215], a[160:163], %1" : "=&v"(g[1][0][1]) : "v"(bs4) : ); SB();
  asm volatile("v_fma_f32 %0, %0, %1, %1" : "+v"(ge[3]) : "v"(xd[0]) : ); SB();
  asm volatile("v_mfma_f32_16x16x32_bf16 %0, v[212:215], a[164:167], 0" : "=&v"(g[1][1][1]) :  : ); SB();
  asm volatile("v_rcp_f32 %0, %1" : "=v"(gt[0]) : "v"(ge[0]) : ); SB();
  asm volatile("v_mfma_f32_16x16x32_bf16 %0, v[212:215], a[168:171], 0" : "=&v"(g[1][2][1]) :  : ); SB();
  asm volatile("buffer_load_dwordx4 v[204:207], %0, %1, %2 offen offset:1024 sc1" :  : "v"(vB), "s"(rWS), "s"(65536u) : "memory"); SB();
  asm volatile("v_rcp_f32 %0, %1" : "=v"(gt[1]) : "v"(ge[1]) : ); SB();
  asm volatile("v_mfma_f32_16x16x32_bf16 %0, v[212:215], a[172:175], 0" : "=&v"(g[1][3][1]) :  : ); SB();
  asm volatile("v_xor_b32 %0, 393216, %0" : "+v"(vB) :  : "memory"); SB();
  asm volatile("v_mfma_f32_16x16x32_bf16 %0, v[216:219], a[176:179], %0" : "+v"(g[1][0][0]) :  : ); SB();
  asm volatile("v_rcp_f32 %0, %1" : "=v"(gt[2]) : "v"(ge[2]) : ); SB();
  asm volatile("v_mfma_f32_16x16x32_bf16 %0, v[216:219], a[180:183], %0" : "+v"(g[1][1][0]) :  : ); SB();
  asm volatile("buffer_load_dwordx4 v[208:211], %0, %1, %2 offen offset:0 sc1" :  : "v"(vB), "s"(rWS), "s"(0u) : "memory"); SB();
  asm volatile("v_rcp_f32 %0, %1" : "=v"(gt[3]) : "v"(ge[3]) : ); SB();
  asm volatile("v_mfma_f32_16x16x32_bf16 %0, v[216:219], a[184:187], %0" : "+v"(g[1][2][0]) :  : ); SB();
  asm volatile("v_mul_f32 %0, %0, %1" : "+v"(a1[0][0][0]) : "v"(gt[0]) : ); SB();
  asm volatile("v_mfma_f32_16x16x32_bf16 %0, v[216:219], a[188:191], %0" : "+v"(g[1][3][0]) :  : ); SB();
  asm volatile("v_mul_f32 %0, %0, %1" : "+v"(a1[0][0][1]) : "v"(gt[1]) : ); SB();
  asm volatile("v_mfma_f32_16x16x32_bf16 %0, v[220:223], a[176:179], %0" : "+v"(g[1][0][1]) :  : ); SB();
  asm volatile("v_mul_f32 %0, %0, %1" : "+v"(a1[0][0][2]) : "v"(gt[2]) : ); SB();
  asm volatile("v_mfma_f32_16x16x32_bf16 %0, v[220:223], a[180:183], %0" : "+v"(g[1][1][1]) :  : ); SB();
  asm volatile("v_mul_f32 %0, %0, %1" : "+v"(a1[0][0][3]) : "v"(gt[3]) : ); SB();
  asm volatile("v_mfma_f32_16x16x32_bf16 %0, v[220:223], a[184:187], %0" : "+v"(g[1][2][1]) :  : ); SB();
  asm volatile("v_mul_f32 %0, %1, %1" : "=v"(gt[0]) : "v"(a1[0][1][0]) : ); SB();
  asm volatile("v_mfma_f32_16x16x32_bf16 %0, v[220:223], a[188:191], %0" : "+v"(g[1][3][1]) :  : ); SB();
  asm volatile("v_mul_f32 %0, %1, %1" : "=v"(gt[1]) : "v"(a1[0][1][1]) : ); SB();
  ST(26);
  asm volatile("v_mfma_f32_16x16x32_bf16 %0, v[224:227], a[192:195], %0" : "+v"(g[1][0][0]) :  : ); SB();
  vH_ = vH; asm volatile("" : "+v"(vH_)); SB();
  asm volatile("v_mfma_f32_16x16x32_bf16 %0, v[224:227], a[196:199], %0" : "+v"(g[1][1][0]) :  : ); SB();
  vO = W_WO + (cb * 120 + w * 30 + 0u * 15) * 1024 + l16; asm volatile("" : "+v"(vO)); SB();
  asm volatile("v_mfma_f32_16x16x32_bf16 %0, v[224:227], a[200:203], %0" : "+v"(g[1][2][0]) :  : ); SB();
  vO2 = vO + 15360 - 0u * 30720; asm volatile("" : "+v"(vO2)); SB();
  asm volatile("v_mfma_f32_16x16x32_bf16 %0, v[224:227], a[204:207], %0" : "+v"(g[1][3][0]) :  : ); SB();
  vFH = W_FH + rb * 256 + w * 64 + (lane & 7) * 8; asm volatile("" : "+v"(vFH)); SB();
  asm volatile("v_mfma_f32_16x16x32_bf16 %0, v[228:231], a[192:195], %0" : "+v"(g[1][0][1]) :  : ); SB();
  vHr = W_HH + (rb * 96 + w * 6) * 1024 + l16; asm volatile("" : "+v"(vHr)); SB();
  asm volatile("v_mfma_f32_16x16x32_bf16 %0, v[228:231], a[196:199], %0" : "+v"(g[1][1][1]) :  : ); SB();
  vX = (rb * 64 + w * 16 + l15) * 1280; asm volatile("" : "+v"(vX)); SB();
  asm volatile("v_mfma_f32_16x16x32_bf16 %0, v[228:231], a[200:203], %0" : "+v"(g[1][2][1]) :  : ); SB();
  vX = vX + cb * 160 + 8 * g4; asm volatile("" : "+v"(vX)); SB();
  asm volatile("v_mfma_f32_16x16x32_bf16 %0, v[228:231], a[204:207], %0" : "+v"(g[1][3][1]) :  : ); SB();
  sFH = W_FH + ((rb * 8 + cb) * 4 + w) * 8; asm volatile("" : "+s"(sFH)); SB();
  asm volatile("v_mfma_f32_16x16x32_bf16 %0, v[232:235], a[208:211], %0" : "+v"(g[1][0][0]) :  : ); SB();
  hso[0] = (w ^ 0) * 24576 + 0u * 3072; asm volatile("" : "+s"(hso[0])); SB();
  hso[1] = hso[0] ^ 3072; asm volatile("" : "+s"(hso[1])); SB();
  asm volatile("v_mfma_f32_16x16x32_bf16 %0, v[232:235], a[212:215], %0" : "+v"(g[1][1][0]) :  : ); SB();
  hso[2] = (w ^ 1) * 24576 + 0u * 3072; asm volatile("" : "+s"(hso[2])); SB();
  hso[3] = hso[2] ^ 3072; asm volatile("" : "+s"(hso[3])); SB();
  asm volatile("v_mfma_f32_16x16x32_bf16 %0, v[232:235], a[216:219], %0" : "+v"(g[1][2][0]) :  : ); SB();
  hso[4] = (w ^ 2) * 24576 + 0u * 3072; asm volatile("" : "+s"(hso[4])); SB();
  hso[5] = hso[4] ^ 3072; asm volatile("" : "+s"(hso[5])); SB();
  asm volatile("v_mfma_f32_16x16x32_bf16 %0, v[232:235], a[220:223], %0" : "+v"(g[1][3][0]) :  : ); SB();
  asm volatile("buffer_load_dwordx4 v[212:215], %0, %1, %2 offen offset:1024 sc1" :  : "v"(vB), "s"(rWS), "s"(0u) : "memory"); SB();
  asm volatile("v_mul_f32 %0, %1, %1" : "=v"(gt[2]) : "v"(a1[0][1][2]) : ); SB();
  asm volatile("v_mfma_f32_16x16x32_bf16 %0, v[236:239], a[208:211], %0" : "+v"(g[1][0][1]) :  : ); SB();
  hso[6] = (w ^ 3) * 24576 + 0u * 3072; asm volatile("" : "+s"(hso[6])); SB();
  hso[7] = hso[6] ^ 3072; asm volatile("" : "+s"(hso[7])); SB();
  asm volatile("v_mfma_f32_16x16x32_bf16 %0, v[236:239], a[212:215], %0" : "+v"(g[1][1][1]) :  : ); SB();
  asm volatile("buffer_load_dwordx4 %0, %1, %2 offen offset:2048 lds" :  : "v"(voS), "s"(rA), "s"(ao[2]) : "memory"); SB();
  asm volatile("v_mul_f32 %0, %1, %1" : "=v"(gt[3]) : "v"(a1[0][1][3]) : ); SB();
  asm volatile("v_mfma_f32_16x16x32_bf16 %0, v[236:239], a[216:219], %0" : "+v"(g[1][2][1]) :  : ); SB();
  asm volatile("v_fma_f32 %0, %0, %1, %2" : "+v"(gt[0]) : "v"(k2[0]), "v"(k1[0]) : ); SB();
  asm volatile("v_mfma_f32_16x16x32_bf16 %0, v[236:239], a[220:223], %0" : "+v"(g[1][3][1]) :  : ); SB();
  asm volatile("buffer_load_dwordx4 %0, %1, %2 offen offset:3072 lds" :  : "v"(voS), "s"(rA), "s"(ao[3]) : "memory"); SB();
  asm volatile("v_mfma_f32_16x16x32_bf16 %0, v[240:243], a[224:227], %0" : "+v"(g[1][0][0]) :  : ); SB();
  asm volatile("v_fma_f32 %0, %0, %1, %2" : "+v"(gt[1]) : "v"(k2[0]), "v"(k1[0]) : ); SB();
  asm volatile("v_mfma_f32_16x16x32_bf16 %0, v[240:243], a[228:231], %0" : "+v"(g[1][1][0]) :  : ); SB();
  asm volatile("v_fma_f32 %0, %0, %1, %2" : "+v"(gt[2]) : "v"(k2[0]), "v"(k1[0]) : ); SB();
  asm volatile("v_mfma_f32_16x16x32_bf16 %0, v[240:243], a[232:235], %0" : "+v"(g[1][2][0]) :  : ); SB();
  asm volatile("v_fma_f32 %0, %0, %1, %2" : "+v"(gt[3]) : "v"(k2[0]), "v"(k1[0]) : ); SB();
  asm volatile("v_mfma_f32_16x16x32_bf16 %0, v[240:243], a[236:239], %0" : "+v"(g[1][3][0]) :  : ); SB();
  asm volatile("s_add_u32 m0, %0, 102400" :  : "s"(mA) : "memory", "scc"); SB();
  asm volatile("v_mul_f32 %0, %0, %1" : "+v"(gt[0]) : "v"(a1[0][1][0]) : ); SB();
  asm volatile("v_mfma_f32_16x16x32_bf16 %0, v[244:247], a[224:227], %0" : "+v"(g[1][0][1]) :  : ); SB();
  asm volatile("buffer_load_dwordx4 %0, %1, %2 offen lds" :  : "v"(voS), "s"(rA), "s"(ao[4]) : "memory"); SB();
  asm volatile("v_mul_f32 %0, %0, %1" : "+v"(gt[1]) : "v"(a1[0][1][1]) : ); SB();
  asm volatile("v_mfma_f32_16x16x32_bf16 %0, v[244:247], a[228:231], %0" : "+v"(g[1][1][1]) :  : ); SB();
  asm volatile("v_mul_f32 %0, %0, %1" : "+v"(gt[2]) : "v"(a1[0][1][2]) : ); SB();
  asm volatile("v_mfma_f32_16x16x32_bf16 %0, v[244:247], a[232:235], %0" : "+v"(g[1][2][1]) :  : ); SB();
  asm volatile("buffer_load_dwordx4 %0, %1, %2 offen offset:1024 lds" :  : "v"(voS), "s"(rA), "s"(ao[5]) : "memory"); SB();
  asm volatile("v_mul_f32 %0, %0, %1" : "+v"(gt[3]) : "v"(a1[0][1][3]) : ); SB();
  asm volatile("v_mfma_f32_16x16x32_bf16 %0, v[244:247], a[236:239], %0" : "+v"(g[1][3][1]) :  : ); SB();
  asm volatile("v_exp_f32 %0, %1" : "=v"(ge[0]) : "v"(gt[0]) : ); SB();
  asm volatile("v_mfma_f32_16x16x32_bf16 %0, v[248:251], a[240:243], %0" : "+v"(g[1][0][0]) :  : ); SB();
  asm volatile("v_exp_f32 %0, %1" : "=v"(ge[1]) : "v"(gt[1]) : ); SB();
  asm volatile("v_mfma_f32_16x16x32_bf16 %0, v[248:251], a[244:247], %0" : "+v"(g[1][1][0]) :  : ); SB();
  asm volatile("v_exp_f32 %0, %1" : "=v"(ge[2]) : "v"(gt[2]) : ); SB();
  asm volatile("v_mfma_f32_16x16x32_bf16 %0, v[248:251], a[248:251], %0" : "+v"(g[1][2][0]) :  : ); SB();
  asm volatile("buffer_load_dwordx4 %0, %1, %2 offen offset:2048 lds" :  : "v"(voS), "s"(rA), "s"(ao[6]) : "memory"); SB();
  asm volatile("v_exp_f32 %0, %1" : "=v"(ge[3]) : "v"(gt[3]) : ); SB();
  asm volatile("v_mfma_f32_16x16x32_bf16 %0, v[248:251], a[252:255], %0" : "+v"(g[1][3][0]) :  : ); SB();
  asm volatile("v_fma_f32 %0, %0, %1, %1" : "+v"(ge[0]) : "v"(xd[0]) : ); SB();
  asm volatile("v_mfma_f32_16x16x32_bf16 %0, v[252:255], a[240:243], %0" : "+v"(g[1][0][1]) :  : ); SB();
  asm volatile("v_fma_f32 %0, %0, %1, %1" : "+v"(ge[1]) : "v"(xd[0]) : ); SB();
  asm volatile("v_mfma_f32_16x16x32_bf16 %0, v[252:255], a[244:247], %0" : "+v"(g[1][1][1]) :  : ); SB();
  asm volatile("buffer_load_dwordx4 %0, %1, %2 offen offset:3072 lds" :  : "v"(voS), "s"(rA), "s"(ao[7]) : "memory"); SB();
  asm volatile("v_fma_f32 %0, %0, %1, %1" : "+v"(ge[2]) : "v"(xd[0]) : ); SB();
  asm volatile("v_mfma_f32_16x16x32_bf16 %0, v[252:255], a[248:251], %0" : "+v"(g[1][2][1]) :  : ); SB();
  asm volatile("v_fma_f32 %0, %0, %1, %1" : "+v"(ge[3]) : "v"(xd[0]) : ); SB();
  asm volatile("v_mfma_f32_16x16x32_bf16 %0, v[252:255], a[252:255], %0" : "+v"(g[1][3][1]) :  : ); SB();
  asm volatile("v_rcp_f32 %0, %1" : "=v"(gt[0]) : "v"(ge[0]) : ); SB();
  asm volatile("s_waitcnt vmcnt(19)" :  :  : "memory"); SB();
  asm volatile("v_mfma_f32_16x16x32_bf16 %0, v[160:163], a[0:3], %0" : "+v"(g[0][0][0]) :  : ); SB();
  asm volatile("v_rcp_f32 %0, %1" : "=v"(gt[1]) : "v"(ge[1]) : ); SB();
  asm volatile("v_mfma_f32_16x16x32_bf16 %0, v[160:163], a[4:7], %0" : "+v"(g[0][1][0]) :  : ); SB();
  asm volatile("buffer_load_dwordx4 v[216:219], %0, %1, %2 offen offset:0 sc1" :  : "v"(vB), "s"(rWS), "s"(65536u) : "memory"); SB();
  asm volatile("v_rcp_f32 %0, %1" : "=v"(gt[2]) : "v"(ge[2]) : ); SB();
  asm volatile("v_mfma_f32_16x16x32_bf16 %0, v[160:163], a[8:11], %0" : "+v"(g[0][2][0]) :  : ); SB();
  asm volatile("v_rcp_f32 %0, %1" : "=v"(gt[3]) : "v"(ge[3]) : ); SB();
  asm volatile("v_mfma_f32_16x16x32_bf16 %0, v[160:163], a[12:15], %0" : "+v"(g[0][3][0]) :  : ); SB();
  asm volatile("v_mul_f32 %0, %0, %1" : "+v"(a1[0][1][0]) : "v"(gt[0]) : ); SB();
  asm volatile("v_mfma_f32_16x16x32_bf16 %0, v[164:167], a[0:3], %0" : "+v"(g[0][0][1]) :  : ); SB();
  asm volatile("s_waitcnt vmcnt(11)" :  :  : "memory"); SB();
  asm volatile("ds_read_b128 a[128:131], %0 offset:0" :  : "v"(aA[0]) : "memory"); SB();
  asm volatile("v_mfma_f32_16x16x32_bf16 %0, v[164:167], a[4:7], %0" : "+v"(g[0][1][1]) :  : ); SB();
  asm volatile("ds_read_b128 a[144:147], %0 offset:0" :  : "v"(aA[1]) : "memory"); SB();
  asm volatile("v_mul_f32 %0, %0, %1" : "+v"(a1[0][1][1]) : "v"(gt[1]) : ); SB();
  asm volatile("v_mfma_f32_16x16x32_bf16 %0, v[164:167], a[8:11], %0" : "+v"(g[0][2][1]) :  : ); SB();
  asm volatile("buffer_load_dwordx4 v[220:223], %0, %1, %2 offen offset:1024 sc1" :  : "v"(vB), "s"(rWS), "s"(65536u) : "memory"); SB();
  asm volatile("v_mul_f32 %0, %0, %1" : "+v"(a1[0][1][2]) : "v"(gt[2]) : ); SB();
  asm volatile("v_mfma_f32_16x16x32_bf16 %0, v[164:167], a[12:15], %0" : "+v"(g[0][3][1]) :  : ); SB();
  asm volatile("v_xor_b32 %0, 131072, %0" : "+v"(vB) :  : "memory"); SB();
  asm volatile("v_mfma_f32_16x16x32_bf16 %0, v[168:171], a[16:19], %0" : "+v"(g[0][0][0]) :  : ); SB();
  asm volatile("buffer_load_dwordx4 v[224:227], %0, %1, %2 offen offset:0 sc1" :  : "v"(vB), "s"(rWS), "s"(0u) : "memory"); SB();
  asm volatile("v_mul_f32 %0, %0, %1" : "+v"(a1[0][1][3]) : "v"(gt[3]) : ); SB();
  asm volatile("v_mfma_f32_16x16x32_bf16 %0, v[168:171], a[20:23], %0" : "+v"(g[0][1][0]) :  : ); SB();
  asm volatile("v_mul_f32 %0, %1, %1" : "=v"(gt[0]) : "v"(a1[0][2][0]) : ); SB();
  asm volatile("v_mfma_f32_16x16x32_bf16 %0, v[168:171], a[24:27], %0" : "+v"(g[0][2][0]) :  : ); SB();
  asm volatile("v_mul_f32 %0, %1, %1" : "=v"(gt[1]) : "v"(a1[0][2][1]) : ); SB();
  asm volatile("v_mfma_f32_16x16x32_bf16 %0, v[168:171], a[28:31], %0" : "+v"(g[0][3][0]) :  : ); SB();
  asm volatile("buffer_load_dwordx4 v[228:231], %0, %1, %2 offen offset:1024 sc1" :  : "v"(vB), "s"(rWS), "s"(0u) : "memory"); SB();
  asm volatile("v_mul_f32 %0, %1, %1" : "=v"(gt[2]) : "v"(a1[0][2][2]) : ); SB();
  asm volatile("v_mfma_f32_16x16x32_bf16 %0, v[172:175], a[16:19], %0" : "+v"(g[0][0][1]) :  : ); SB();
  asm volatile("v_mul_f32 %0, %1, %1" : "=v"(gt[3]) : "v"(a1[0][2][3]) : ); SB();
  asm volatile("v_mfma_f32_16x16x32_bf16 %0, v[172:175], a[20:23], %0" : "+v"(g[0][1][1]) :  : ); SB();
  asm volatile("v_fma_f32 %0, %0, %1, %2" : "+v"(gt[0]) : "v"(k2[0]), "v"(k1[0]) : ); SB();
  asm volatile("v_mfma_f32_16x16x32_bf16 %0, v[172:175], a[24:27], %0" : "+v"(g[0][2][1]) :  : ); SB();
  asm volatile("buffer_load_dwordx4 v[232:235], %0, %1, %2 offen offset:0 sc1" :  : "v"(vB), "s"(rWS), "s"(65536u) : "memory"); SB();
  asm volatile("v_fma_f32 %0, %0, %1, %2" : "+v"(gt[1]) : "v"(k2[0]), "v"(k1[0]) : ); SB();
  asm volatile("v_mfma_f32_16x16x32_bf16 %0, v[172:175], a[28:31], %0" : "+v"(g[0][3][1]) :  : ); SB();
  asm volatile("v_fma_f32 %0, %0, %1, %2" : "+v"(gt[2]) : "v"(k2[0]), "v"(k1[0]) : ); SB();
  asm volatile("v_mfma_f32_16x16x32_bf16 %0, v[176:179], a[32:35], %0" : "+v"(g[0][0][0]) :  : ); SB();
  asm volatile("v_fma_f32 %0, %0, %1, %2" : "+v"(gt[3]) : "v"(k2[0]), "v"(k1[0]) : ); SB();
  asm volatile("v_mfma_f32_16x16x32_bf16 %0, v[176:179], a[36:39], %0" : "+v"(g[0][1][0]) :  : ); SB();
  asm volatile("v_mul_f32 %0, %0, %1" : "+v"(gt[0]) : "v"(a1[0][2][0]) : ); SB();
  asm volatile("v_mfma_f32_16x16x32_bf16 %0, v[176:179], a[40:43], %0" : "+v"(g[0][2][0]) :  : ); SB();
  asm volatile("buffer_load_dwordx4 v[236:239], %0, %1, %2 offen offset:1024 sc1" :  : "v"(vB), "s"(rWS), "s"(65536u) : "memory"); SB();
  asm volatile("v_mul_f32 %0, %0, %1" : "+v"(gt[1]) : "v"(a1[0][2][1]) : ); SB();
  asm volatile("v_mfma_f32_16x16x32_bf16 %0, v[176:179], a[44:47], %0" : "+v"(g[0][3][0]) :  : ); SB();
  asm volatile("v_xor_b32 %0, 524288, %0" : "+v"(vB) :  : "memory"); SB();
  asm volatile("v_mul_f32 %0, %0, %1" : "+v"(gt[2]) : "v"(a1[0][2][2]) : ); SB();
  asm volatile("v_mfma_f32_16x16x32_bf16 %0, v[180:183], a[32:35], %0" : "+v"(g[0][0][1]) :  : ); SB();
  asm volatile("v_mul_f32 %0, %0, %1" : "+v"(gt[3]) : "v"(a1[0][2][3]) : ); SB();
  asm volatile("v_mfma_f32_16x16x32_bf16 %0, v[180:183], a[36:39], %0" : "+v"(g[0][1][1]) :  : ); SB();
  asm volatile("buffer_load_dwordx4 v[240:243], %0, %1, %2 offen offset:0 sc1" :  : "v"(vB), "s"(rWS), "s"(2048u) : "memory"); SB();
  asm volatile("v_exp_f32 %0, %1" : "=v"(ge[0]) : "v"(gt[0]) : ); SB();
  asm volatile("v_mfma_f32_16x16x32_bf16 %0, v[180:183], a[40:43], %0" : "+v"(g[0][2][1]) :  : ); SB();
  asm volatile("v_exp_f32 %0, %1" : "=v"(ge[1]) : "v"(gt[1]) : ); SB();
  asm volatile("v_mfma_f32_16x16x32_bf16 %0, v[180:183], a[44:47], %0" : "+v"(g[0][3][1]) :  : ); SB();
  asm volatile("v_exp_f32 %0, %1" : "=v"(ge[2]) : "v"(gt[2]) : ); SB();
  asm volatile("v_mfma_f32_16x16x32_bf16 %0, v[184:187], a[48:51], %0" : "+v"(g[0][0][0]) :  : ); SB();
  asm volatile("v_exp_f32 %0, %1" : "=v"(ge[3]) : "v"(gt[3]) : ); SB();
  asm volatile("v_mfma_f32_16x16x32_bf16 %0, v[184:187], a[52:55], %0" : "+v"(g[0][1][0]) :  : ); SB();
  asm volatile("v_fma_f32 %0, %0, %1, %1" : "+v"(ge[0]) : "v"(xd[0]) : ); SB();
  asm volatile("v_mfma_f32_16x16x32_bf16 %0, v[184:187], a[56:59], %0" : "+v"(g[0][2][0]) :  : ); SB();
  asm volatile("v_fma_f32 %0, %0, %1, %1" : "+v"(ge[1]) : "v"(xd[0]) : ); SB();
  asm volatile("v_mfma_f32_16x16x32_bf16 %0, v[184:187], a[60:63], %0" : "+v"(g[0][3][0]) :  : ); SB();
  asm volatile("v_fma_f32 %0, %0, %1, %1" : "+v"(ge[2]) : "v"(xd[0]) : ); SB();
  asm volatile("v_mfma_f32_16x16x32_bf16 %0, v[188:191], a[48:51], %0" : "+v"(g[0][0][1]) :  : ); SB();
  asm volatile("buffer_load_dwordx4 v[244:247], %0, %1, %2 offen offset:1024 sc1" :  : "v"(vB), "s"(rWS), "s"(2048u) : "memory"); SB();
  asm volatile("v_fma_f32 %0, %0, %1, %1" : "+v"(ge[3]) : "v"(xd[0]) : ); SB();
  asm volatile("v_mfma_f32_16x16x32_bf16 %0, v[188:191], a[52:55], %0" : "+v"(g[0][1][1]) :  : ); SB();
  asm volatile("v_rcp_f32 %0, %1" : "=v"(gt[0]) : "v"(ge[0]) : ); SB();
  asm volatile("v_mfma_f32_16x16x32_bf16 %0, v[188:191], a[56:59], %0" : "+v"(g[0][2][1]) :  : ); SB();
  asm volatile("v_rcp_f32 %0, %1" : "=v"(gt[1]) : "v"(ge[1]) : ); SB();
  asm volatile("v_mfma_f32_16x16x32_bf16 %0, v[188:191], a[60:63], %0" : "+v"(g[0][3][1]) :  : ); SB();
  asm volatile("v_rcp_f32 %0, %1" : "=v"(gt[2]) : "v"(ge[2]) : ); SB();
  ST(27);
  asm volatile("v_mfma_f32_16x16x32_bf16 %0, v[192:195], a[64:67], %0" : "+v"(g[0][0][0]) :  : ); SB();
  asm volatile("buffer_load_dwordx4 v[248:251], %0, %1, %2 offen offset:0 sc1" :  : "v"(vB), "s"(rWS), "s"(67584u) : "memory"); SB();
  asm volatile("v_rcp_f32 %0, %1" : "=v"(gt[3]) : "v"(ge[3]) : ); SB();
  asm volatile("v_mfma_f32_16x16x32_bf16 %0, v[192:195], a[68:71], %0" : "+v"(g[0][1][0]) :  : ); SB();
  asm volatile("v_mul_f32 %0, %0, %1" : "+v"(a1[0][2][0]) : "v"(gt[0]) : ); SB();
  asm volatile("v_mfma_f32_16x16x32_bf16 %0, v[192:195], a[72:75], %0" : "+v"(g[0][2][0]) :  : ); SB();
  asm volatile("v_mul_f32 %0, %0, %1" : "+v"(a1[0][2][1]) : "v"(gt[1]) : ); SB();
  asm volatile("v_mfma_f32_16x16x32_bf16 %0, v[192:195], a[76:79], %0" : "+v"(g[0][3][0]) :  : ); SB();
  asm volatile("s_waitcnt vmcnt(13)" :  :  : "memory"); SB();
  asm volatile("ds_read_b128 a[132:135], %0 offset:2048" :  : "v"(aA[0]) : "memory"); SB();
  asm volatile("v_mfma_f32_16x16x32_bf16 %0, v[196:199], a[64:67], %0" : "+v"(g[0][0][1]) :  : ); SB();
  asm volatile("ds_read_b128 a[148:151], %0 offset:2048" :  : "v"(aA[1]) : "memory"); SB();
  asm volatile("v_mul_f32 %0, %0, %1" : "+v"(a1[0][2][2]) : "v"(gt[2]) : ); SB();
  asm volatile("v_mfma_f32_16x16x32_bf16 %0, v[196:199], a[68:71], %0" : "+v"(g[0][1][1]) :  : ); SB();
  asm volatile("buffer_load_dwordx4 v[252:255], %0, %1, %2 offen offset:1024 sc1" :  : "v"(vB), "s"(rWS), "s"(67584u) : "memory"); SB();
  asm volatile("v_mul_f32 %0, %0, %1" : "+v"(a1[0][2][3]) : "v"(gt[3]) : ); SB();
  asm volatile("v_mfma_f32_16x16x32_bf16 %0, v[196:199], a[72:75], %0" : "+v"(g[0][2][1]) :  : ); SB();
  asm volatile("v_xor_b32 %0, 917504, %0" : "+v"(vB) :  : "memory"); SB();
  asm volatile("v_mul_f32 %0, %1, %1" : "=v"(gt[0]) : "v"(a1[1][0][0]) : ); SB();
  asm volatile("v_mfma_f32_16x16x32_bf16 %0, v[196:199], a[76:79], %0" : "+v"(g[0][3][1]) :  : ); SB();
  asm volatile("buffer_load_dwordx4 v[160:163], %0, %1, %2 offen offset:0 sc1" :  : "v"(vB), "s"(rWS), "s"(2048u) : "memory"); SB();
  asm volatile("v_mfma_f32_16x16x32_bf16 %0, v[200:203], a[80:83], %0" : "+v"(g[0][0][0]) :  : ); SB();
  asm volatile("v_mul_f32 %0, %1, %1" : "=v"(gt[1]) : "v"(a1[1][0][1]) : ); SB();
  asm volatile("v_mfma_f32_16x16x32_bf16 %0, v[200:203], a[84:87], %0" : "+v"(g[0][1][0]) :  : ); SB();
  asm volatile("v_mul_f32 %0, %1, %1" : "=v"(gt[2]) : "v"(a1[1][0][2]) : ); SB();
  asm volatile("v_mfma_f32_16x16x32_bf16 %0, v[200:203], a[88:91], %0" : "+v"(g[0][2][0]) :  : ); SB();
  asm volatile("s_waitcnt vmcnt(13)" :  :  : "memory"); SB();
  asm volatile("ds_read_b128 a[136:139], %0 offset:4096" :  : "v"(aA[0]) : "memory"); SB();
  asm volatile("v_mfma_f32_16x16x32_bf16 %0, v[200:203], a[92:95], %0" : "+v"(g[0][3][0]) :  : ); SB();
  asm volatile("ds_read_b128 a[152:155], %0 offset:4096" :  : "v"(aA[1]) : "memory"); SB();
  asm volatile("v_mul_f32 %0, %1, %1" : "=v"(gt[3]) : "v"(a1[1][0][3]) : ); SB();
  asm volatile("v_mfma_f32_16x16x32_bf16 %0, v[204:207], a[80:83], %0" : "+v"(g[0][0][1]) :  : ); SB();
  asm volatile("buffer_load_dwordx4 v[164:167], %0, %1, %2 offen offset:1024 sc1" :  : "v"(vB), "s"(rWS), "s"(2048u) : "memory"); SB();
  asm volatile("v_fma_f32 %0, %0, %1, %2" : "+v"(gt[0]) : "v"(k2[0]), "v"(k1[0]) : ); SB();
  asm volatile("v_mfma_f32_16x16x32_bf16 %0, v[204:207], a[84:87], %0" : "+v"(g[0][1][1]) :  : ); SB();
  asm volatile("v_fma_f32 %0, %0, %1, %2" : "+v"(gt[1]) : "v"(k2[0]), "v"(k1[0]) : ); SB();
  asm volatile("v_mfma_f32_16x16x32_bf16 %0, v[204:207], a[88:91], %0" : "+v"(g[0][2][1]) :  : ); SB();
  asm volatile("buffer_load_dwordx4 v[168:171], %0, %1, %2 offen offset:0 sc1" :  : "v"(vB), "s"(rWS), "s"(67584u) : "memory"); SB();
  asm volatile("v_fma_f32 %0, %0, %1, %2" : "+v"(gt[2]) : "v"(k2[0]), "v"(k1[0]) : ); SB();
  asm volatile("v_mfma_f32_16x16x32_bf16 %0, v[204:207], a[92:95], %0" : "+v"(g[0][3][1]) :  : ); SB();
  asm volatile("v_fma_f32 %0, %0, %1, %2" : "+v"(gt[3]) : "v"(k2[0]), "v"(k1[0]) : ); SB();
  asm volatile("v_mfma_f32_16x16x32_bf16 %0, v[208:211], a[96:99], %0" : "+v"(g[0][0][0]) :  : ); SB();
  asm volatile("v_mul_f32 %0, %0, %1" : "+v"(gt[0]) : "v"(a1[1][0][0]) : ); SB();
  asm volatile("v_mfma_f32_16x16x32_bf16 %0, v[208:211], a[100:103], %0" : "+v"(g[0][1][0]) :  : ); SB();
  asm volatile("s_waitcnt vmcnt(13)" :  :  : "memory"); SB();
  asm volatile("ds_read_b128 a[140:143], %0 offset:6144" :  : "v"(aA[0]) : "memory"); SB();
  asm volatile("v_mfma_f32_16x16x32_bf16 %0, v[208:211], a[104:107], %0" : "+v"(g[0][2][0]) :  : ); SB();
  asm volatile("ds_read_b128 a[156:159], %0 offset:6144" :  : "v"(aA[1]) : "memory"); SB();
  asm volatile("s_waitcnt lgkmcnt(4)\n\tv_mfma_f32_16x16x32_bf16 %0, v[208:211], a[108:111], %0" : "+v"(g[0][3][0]) :  : ); SB();
  asm volatile("buffer_load_dwordx4 v[172:175], %0, %1, %2 offen offset:1024 sc1" :  : "v"(vB), "s"(rWS), "s"(67584u) : "memory"); SB();
  asm volatile("v_mul_f32 %0, %0, %1" : "+v"(gt[1]) : "v"(a1[1][0][1]) : ); SB();
  asm volatile("v_mfma_f32_16x16x32_bf16 %0, v[212:215], a[96:99], %0" : "+v"(g[0][0][1]) :  : ); SB();
  asm volatile("v_xor_b32 %0, 131072, %0" : "+v"(vB) :  : "memory"); SB();
  asm volatile("v_mul_f32 %0, %0, %1" : "+v"(gt[2]) : "v"(a1[1][0][2]) : ); SB();
  asm volatile("v_mfma_f32_16x16x32_bf16 %0, v[212:215], a[100:103], %0" : "+v"(g[0][1][1]) :  : ); SB();
  asm volatile("buffer_load_dwordx4 v[176:179], %0, %1, %2 offen offset:0 sc1" :  : "v"(vB), "s"(rWS), "s"(2048u) : "memory"); SB();
  asm volatile("v_mul_f32 %0, %0, %1" : "+v"(gt[3]) : "v"(a1[1][0][3]) : ); SB();
  asm volatile("v_mfma_f32_16x16x32_bf16 %0, v[212:215], a[104:107], %0" : "+v"(g[0][2][1]) :  : ); SB();
  asm volatile("v_exp_f32 %0, %1" : "=v"(ge[0]) : "v"(gt[0]) : ); SB();
  asm volatile("v_mfma_f32_16x16x32_bf16 %0, v[212:215], a[108:111], %0" : "+v"(g[0][3][1]) :  : ); SB();
  asm volatile("v_exp_f32 %0, %1" : "=v"(ge[1]) : "v"(gt[1]) : ); SB();
  asm volatile("s_waitcnt vmcnt(13)" :  :  : "memory"); SB();
  asm volatile("v_mfma_f32_16x16x32_bf16 %0, v[216:219], a[112:115], %0" : "+v"(g[0][0][0]) :  : ); SB();
  asm volatile("buffer_load_dwordx4 v[180:183], %0, %1, %2 offen offset:1024 sc1" :  : "v"(vB), "s"(rWS), "s"(2048u) : "memory"); SB();
  asm volatile("v_exp_f32 %0, %1" : "=v"(ge[2]) : "v"(gt[2]) : ); SB();
  asm volatile("v_mfma_f32_16x16x32_bf16 %0, v[216:219], a[116:119], %0" : "+v"(g[0][1][0]) :  : ); SB();
  asm volatile("v_exp_f32 %0, %1" : "=v"(ge[3]) : "v"(gt[3]) : ); SB();
  asm volatile("v_mfma_f32_16x16x32_bf16 %0, v[216:219], a[120:123], %0" : "+v"(g[0][2][0]) :  : ); SB();
  asm volatile("v_fma_f32 %0, %0, %1, %1" : "+v"(ge[0]) : "v"(xd[0]) : ); SB();
  asm volatile("v_mfma_f32_16x16x32_bf16 %0, v[216:219], a[124:127], %0" : "+v"(g[0][3][0]) :  : ); SB();
  asm volatile("buffer_load_dwordx4 v[184:187], %0, %1, %2 offen offset:0 sc1" :  : "v"(vB), "s"(rWS), "s"(67584u) : "memory"); SB();
  asm volatile("v_fma_f32 %0, %0, %1, %1" : "+v"(ge[1]) : "v"(xd[0]) : ); SB();
  asm volatile("v_mfma_f32_16x16x32_bf16 %0, v[220:223], a[112:115], %0" : "+v"(g[0][0][1]) :  : ); SB();
  asm volatile("v_fma_f32 %0, %0, %1, %1" : "+v"(ge[2]) : "v"(xd[0]) : ); SB();
  asm volatile("v_mfma_f32_16x16x32_bf16 %0, v[220:223], a[116:119], %0" : "+v"(g[0][1][1]) :  : ); SB();
  asm volatile("v_fma_f32 %0, %0, %1, %1" : "+v"(ge[3]) : "v"(xd[0]) : ); SB();
  asm volatile("v_mfma_f32_16x16x32_bf16 %0, v[220:223], a[120:123], %0" : "+v"(g[0][2][1]) :  : ); SB();
  asm volatile("v_rcp_f32 %0, %1" : "=v"(gt[0]) : "v"(ge[0]) : ); SB();
  asm volatile("v_mfma_f32_16x16x32_bf16 %0, v[220:223], a[124:127], %0" : "+v"(g[0][3][1]) :  : ); SB();
  asm volatile("buffer_load_dwordx4 v[188:191], %0, %1, %2 offen offset:1024 sc1" :  : "v"(vB), "s"(rWS), "s"(67584u) : "memory"); SB();
  asm volatile("s_waitcnt vmcnt(14)" :  :  : "memory"); SB();
  asm volatile("v_mfma_f32_16x16x32_bf16 %0, v[224:227], a[128:131], %0" : "+v"(g[0][0][0]) :  : ); SB();
  asm volatile("v_xor_b32 %0, 393216, %0" : "+v"(vB) :  : "memory"); SB();
  asm volatile("v_rcp_f32 %0, %1" : "=v"(gt[1]) : "v"(ge[1]) : ); SB();
  asm volatile("v_mfma_f32_16x16x32_bf16 %0, v[224:227], a[132:135], %0" : "+v"(g[0][1][0]) :  : ); SB();
  asm volatile("v_rcp_f32 %0, %1" : "=v"(gt[2]) : "v"(ge[2]) : ); SB();
  asm volatile("s_waitcnt lgkmcnt(0)\n\tv_mfma_f32_16x16x32_bf16 %0, v[224:227], a[136:139], %0" : "+v"(g[0][2][0]) :  : ); SB();
  asm volatile("buffer_load_dwordx4 v[192:195], %0, %1, %2 offen offset:0 sc1" :  : "v"(vB), "s"(rWS), "s"(2048u) : "memory"); SB();
  asm volatile("v_rcp_f32 %0, %1" : "=v"(gt[3]) : "v"(ge[3]) : ); SB();
  asm volatile("v_mfma_f32_16x16x32_bf16 %0, v[224:227], a[140:143], %0" : "+v"(g[0][3][0]) :  : ); SB();
  asm volatile("v_mul_f32 %0, %0, %1" : "+v"(a1[1][0][0]) : "v"(gt[0]) : ); SB();
  asm volatile("v_mfma_f32_16x16x32_bf16 %0, v[228:231], a[128:131], %0" : "+v"(g[0][0][1]) :  : ); SB();
  asm volatile("v_mul_f32 %0, %0, %1" : "+v"(a1[1][0][1]) : "v"(gt[1]) : ); SB();
  asm volatile("v_mfma_f32_16x16x32_bf16 %0, v[228:231], a[132:135], %0" : "+v"(g[0][1][1]) :  : ); SB();
  asm volatile("v_mul_f32 %0, %0, %1" : "+v"(a1[1][0][2]) : "v"(gt[2]) : ); SB();
  asm volatile("v_mfma_f32_16x16x32_bf16 %0, v[228:231], a[136:139], %0" : "+v"(g[0][2][1]) :  : ); SB();
  asm volatile("buffer_load_dwordx4 v[196:199], %0, %1, %2 offen offset:1024 sc1" :  : "v"(vB), "s"(rWS), "s"(2048u) : "memory"); SB();
  asm volatile("v_mul_f32 %0, %0, %1" : "+v"(a1[1][0][3]) : "v"(gt[3]) : ); SB();
  asm volatile("v_mfma_f32_16x16x32_bf16 %0, v[228:231], a[140:143], %0" : "+v"(g[0][3][1]) :  : ); SB();
  asm volatile("v_mul_f32 %0, %1, %1" : "=v"(gt[0]) : "v"(a1[1][1][0]) : ); SB();
  asm volatile("s_waitcnt vmcnt(14)" :  :  : "memory"); SB();
  asm volatile("v_mfma_f32_16x16x32_bf16 %0, v[232:235], a[144:147], %0" : "+v"(g[0][0][0]) :  : ); SB();
  asm volatile("v_mul_f32 %0, %1, %1" : "=v"(gt[1]) : "v"(a1[1][1][1]) : ); SB();
  asm volatile("v_mfma_f32_16x16x32_bf16 %0, v[232:235], a[148:151], %0" : "+v"(g[0][1][0]) :  : ); SB();
  asm volatile("buffer_load_dwordx4 v[200:203], %0, %1, %2 offen offset:0 sc1" :  : "v"(vB), "s"(rWS), "s"(67584u) : "memory"); SB();
  asm volatile("v_mul_f32 %0, %1, %1" : "=v"(gt[2]) : "v"(a1[1][1][2]) : ); SB();
  asm volatile("v_mfma_f32_16x16x32_bf16 %0, v[232:235], a[152:155], %0" : "+v"(g[0][2][0]) :  : ); SB();
  asm volatile("v_mul_f32 %0, %1, %1" : "=v"(gt[3]) : "v"(a1[1][1][3]) : ); SB();
  asm volatile("v_mfma_f32_16x16x32_bf16 %0, v[232:235], a[156:159], %0" : "+v"(g[0][3][0]) :  : ); SB();
  asm volatile("v_fma_f32 %0, %0, %1, %2" : "+v"(gt[0]) : "v"(k2[0]), "v"(k1[0]) : ); SB();
  asm volatile("v_mfma_f32_16x16x32_bf16 %0, v[236:239], a[144:147], %0" : "+v"(g[0][0][1]) :  : ); SB();
  asm volatile("v_fma_f32 %0, %0, %1, %2" : "+v"(gt[1]) : "v"(k2[0]), "v"(k1[0]) : ); SB();
  asm volatile("v_mfma_f32_16x16x32_bf16 %0, v[236:239], a[148:151], %0" : "+v"(g[0][1][1]) :  : ); SB();
  asm volatile("buffer_load_dwordx4 v[204:207], %0, %1, %2 offen offset:1024 sc1" :  : "v"(vB), "s"(rWS), "s"(67584u) : "memory"); SB();
  asm volatile("v_fma_f32 %0, %0, %1, %2" : "+v"(gt[2]) : "v"(k2[0]), "v"(k1[0]) : ); SB();
  asm volatile("v_mfma_f32_16x16x32_bf16 %0, v[236:239], a[152:155], %0" : "+v"(g[0][2][1]) :  : ); SB();
  asm volatile("v_xor_b32 %0, 131072, %0" : "+v"(vB) :  : "memory"); SB();
  asm volatile("v_fma_f32 %0, %0, %1, %2" : "+v"(gt[3]) : "v"(k2[0]), "v"(k1[0]) : ); SB();
  asm volatile("v_mfma_f32_16x16x32_bf16 %0, v[236:239], a[156:159], %0" : "+v"(g[0][3][1]) :  : ); SB();
  asm volatile("v_mul_f32 %0, %0, %1" : "+v"(gt[0]) : "v"(a1[1][1][0]) : ); SB();
  asm volatile("s_waitcnt vmcnt(14)" :  :  : "memory"); SB();
  asm volatile("v_mfma_f32_16x16x32_bf16 %0, v[240:243], a[0:3], %0" : "+v"(g[1][0][0]) :  : ); SB();
  asm volatile("s_barrier" :  :  : "memory"); SB();
  asm volatile("buffer_load_dwordx4 v[208:211], %0, %1, %2 offen offset:0 sc1" :  : "v"(vB), "s"(rWS), "s"(2048u) : "memory"); SB();
  asm volatile("v_mfma_f32_16x16x32_bf16 %0, v[240:243], a[4:7], %0" : "+v"(g[1][1][0]) :  : ); SB();
  asm volatile("v_mul_f32 %0, %0, %1" : "+v"(gt[1]) : "v"(a1[1][1][1]) : ); SB();
  asm volatile("v_mfma_f32_16x16x32_bf16 %0, v[240:243], a[8:11], %0" : "+v"(g[1][2][0]) :  : ); SB();
  asm volatile("ds_write_b128 %0, %1 offset:0" :  : "v"(pw2), "v"(g[0][1][0]) : "memory"); SB();
  asm volatile("v_mfma_f32_16x16x32_bf16 %0, v[240:243], a[12:15], %0" : "+v"(g[1][3][0]) :  : ); SB();
  asm volatile("buffer_load_dwordx4 v[212:215], %0, %1, %2 offen offset:1024 sc1" :  : "v"(vB), "s"(rWS), "s"(2048u) : "memory"); SB();
  asm volatile("v_mul_f32 %0, %0, %1" : "+v"(gt[2]) : "v"(a1[1][1][2]) : ); SB();
  asm volatile("v_mfma_f32_16x16x32_bf16 %0, v[244:247], a[0:3], %0" : "+v"(g[1][0][1]) :  : ); SB();
  asm volatile("v_mul_f32 %0, %0, %1" : "+v"(gt[3]) : "v"(a1[1][1][3]) : ); SB();
  asm volatile("v_mfma_f32_16x16x32_bf16 %0, v[244:247], a[4:7], %0" : "+v"(g[1][1][1]) :  : ); SB();
  asm volatile("ds_write_b128 %0, %1 offset:1024" :  : "v"(pw2), "v"(g[0][1][1]) : "memory"); SB();
  asm volatile("v_mfma_f32_16x16x32_bf16 %0, v[244:247], a[8:11], %0" : "+v"(g[1][2][1]) :  : ); SB();
  asm volatile("buffer_load_dwordx4 v[216:219], %0, %1, %2 offen offset:0 sc1" :  : "v"(vB), "s"(rWS), "s"(67584u) : "memory"); SB();
  asm volatile("v_exp_f32 %0, %1" : "=v"(ge[0]) : "v"(gt[0]) : ); SB();
  asm volatile("v_mfma_f32_16x16x32_bf16 %0, v[244:247], a[12:15], %0" : "+v"(g[1][3][1]) :  : ); SB();
  asm volatile("v_exp_f32 %0, %1" : "=v"(ge[1]) : "v"(gt[1]) : ); SB();
  asm volatile("s_waitcnt vmcnt(15)" :  :  : "memory"); SB();
  asm volatile("v_mfma_f32_16x16x32_bf16 %0, v[248:251], a[16:19], %0" : "+v"(g[1][0][0]) :  : ); SB();
  asm volatile("ds_write_b128 %0, %1 offset:2048" :  : "v"(pw2), "v"(g[0][2][0]) : "memory"); SB();
  asm volatile("v_mfma_f32_16x16x32_bf16 %0, v[248:251], a[20:23], %0" : "+v"(g[1][1][0]) :  : ); SB();
  asm volatile("buffer_load_dwordx4 v[220:223], %0, %1, %2 offen offset:1024 sc1" :  : "v"(vB), "s"(rWS), "s"(67584u) : "memory"); SB();
  asm volatile("v_exp_f32 %0, %1" : "=v"(ge[2]) : "v"(gt[2]) : ); SB();
  asm volatile("v_mfma_f32_16x16x32_bf16 %0, v[248:251], a[24:27], %0" : "+v"(g[1][2][0]) :  : ); SB();
  asm volatile("v_xor_b32 %0, 917504, %0" : "+v"(vB) :  : "memory"); SB();
  asm volatile("v_exp_f32 %0, %1" : "=v"(ge[3]) : "v"(gt[3]) : ); SB();
  asm volatile("v_mfma_f32_16x16x32_bf16 %0, v[248:251], a[28:31], %0" : "+v"(g[1][3][0]) :  : ); SB();
  asm volatile("ds_write_b128 %0, %1 offset:3072" :  : "v"(pw2), "v"(g[0][2][1]) : "memory"); SB();
  asm volatile("v_mfma_f32_16x16x32_bf16 %0, v[252:255], a[16:19], %0" : "+v"(g[1][0][1]) :  : ); SB();
  asm volatile("buffer_load_dwordx4 v[224:227], %0, %1, %2 offen offset:0 sc1" :  : "v"(vB), "s"(rWS), "s"(4096u) : "memory"); SB();
  asm volatile("v_fma_f32 %0, %0, %1, %1" : "+v"(ge[0]) : "v"(xd[0]) : ); SB();
  asm volatile("v_mfma_f32_16x16x32_bf16 %0, v[252:255], a[20:23], %0" : "+v"(g[1][1][1]) :  : ); SB();
  asm volatile("buffer_load_dwordx4 v[228:231], %0, %1, %2 offen offset:1024 sc1" :  : "v"(vB), "s"(rWS), "s"(4096u) : "memory"); SB();
  asm volatile("v_fma_f32 %0, %0, %1, %1" : "+v"(ge[1]) : "v"(xd[0]) : ); SB();
  asm volatile("v_mfma_f32_16x16x32_bf16 %0, v[252:255], a[24:27], %0" : "+v"(g[1][2][1]) :  : ); SB();
  asm volatile("ds_write_b128 %0, %1 offset:4096" :  : "v"(pw2), "v"(g[0][3][0]) : "memory"); SB();
  asm volatile("v_mfma_f32_16x16x32_bf16 %0, v[252:255], a[28:31], %0" : "+v"(g[1][3][1]) :  : ); SB();
  asm volatile("buffer_load_dwordx4 v[232:235], %0, %1, %2 offen offset:0 sc1" :  : "v"(vB), "s"(rWS), "s"(69632u) : "memory"); SB();
  ST(28);
  asm volatile("s_waitcnt vmcnt(17)" :  :  : "memory"); SB();
  asm volatile("v_mfma_f32_16x16x32_bf16 %0, v[160:163], a[32:35], %0" : "+v"(g[1][0][0]) :  : ); SB();
  asm volatile("v_fma_f32 %0, %0, %1, %1" : "+v"(ge[2]) : "v"(xd[0]) : ); SB();
  asm volatile("v_mfma_f32_16x16x32_bf16 %0, v[160:163], a[36:39], %0" : "+v"(g[1][1][0]) :  : ); SB();
  asm volatile("ds_write_b128 %0, %1 offset:5120" :  : "v"(pw2), "v"(g[0][3][1]) : "memory"); SB();
  asm volatile("v_mfma_f32_16x16x32_bf16 %0, v[160:163], a[40:43], %0" : "+v"(g[1][2][0]) :  : ); SB();
  asm volatile("buffer_load_dwordx4 v[236:239], %0, %1, %2 offen offset:1024 sc1" :  : "v"(vB), "s"(rWS), "s"(69632u) : "memory"); SB();
  asm volatile("v_fma_f32 %0, %0, %1, %1" : "+v"(ge[3]) : "v"(xd[0]) : ); SB();
  asm volatile("v_mfma_f32_16x16x32_bf16 %0, v[160:163], a[44:47], %0" : "+v"(g[1][3][0]) :  : ); SB();
  asm volatile("v_xor_b32 %0, 131072, %0" : "+v"(vB) :  : "memory"); SB();
  asm volatile("v_rcp_f32 %0, %1" : "=v"(gt[0]) : "v"(ge[0]) : ); SB();
  asm volatile("v_mfma_f32_16x16x32_bf16 %0, v[164:167], a[32:35], %0" : "+v"(g[1][0][1]) :  : ); SB();
  asm volatile("s_waitcnt lgkmcnt(0)\n\ts_barrier" :  :  : "memory"); SB();
  asm volatile("ds_read_b128 %0, %1 offset:0" : "=&v"(pr[0]) : "v"(pe[0]) : "memory"); SB();
  asm volatile("v_mfma_f32_16x16x32_bf16 %0, v[164:167], a[36:39], %0" : "+v"(g[1][1][1]) :  : ); SB();
  asm volatile("ds_read_b128 %0, %1 offset:1024" : "=&v"(pr[1]) : "v"(pe[0]) : "memory"); SB();
  asm volatile("v_rcp_f32 %0, %1" : "=v"(gt[1]) : "v"(ge[1]) : ); SB();
  asm volatile("v_mfma_f32_16x16x32_bf16 %0, v[164:167], a[40:43], %0" : "+v"(g[1][2][1]) :  : ); SB();
  asm volatile("ds_read_b128 %0, %1 offset:2048" : "=&v"(pr[2]) : "v"(pe[1]) : "memory"); SB();
  asm volatile("v_rcp_f32 %0, %1" : "=v"(gt[2]) : "v"(ge[2]) : ); SB();
  asm volatile("v_mfma_f32_16x16x32_bf16 %0, v[164:167], a[44:47], %0" : "+v"(g[1][3][1]) :  : ); SB();
  asm volatile("ds_read_b128 %0, %1 offset:3072" : "=&v"(pr[3]) : "v"(pe[1]) : "memory"); SB();
  asm volatile("s_waitcnt vmcnt(16)" :  :  : "memory"); SB();
  asm volatile("v_mfma_f32_16x16x32_bf16 %0, v[168:171], a[48:51], %0" : "+v"(g[1][0][0]) :  : ); SB();
  asm volatile("ds_read_b128 %0, %1 offset:4096" : "=&v"(pr[4]) : "v"(pe[2]) : "memory"); SB();
  asm volatile("v_rcp_f32 %0, %1" : "=v"(gt[3]) : "v"(ge[3]) : ); SB();
  asm volatile("v_mfma_f32_16x16x32_bf16 %0, v[168:171], a[52:55], %0" : "+v"(g[1][1][0]) :  : ); SB();
  asm volatile("buffer_load_dwordx4 v[240:243], %0, %1, %2 offen offset:0 sc1" :  : "v"(vB), "s"(rWS), "s"(4096u) : "memory"); SB();
  asm volatile("v_mul_f32 %0, %0, %1" : "+v"(a1[1][1][0]) : "v"(gt[0]) : ); SB();
  asm volatile("v_mfma_f32_16x16x32_bf16 %0, v[168:171], a[56:59], %0" : "+v"(g[1][2][0]) :  : ); SB();
  asm volatile("ds_read_b128 %0, %1 offset:5120" : "=&v"(pr[5]) : "v"(pe[2]) : "memory"); SB();
  asm volatile("v_mul_f32 %0, %0, %1" : "+v"(a1[1][1][1]) : "v"(gt[1]) : ); SB();
  asm volatile("v_mfma_f32_16x16x32_bf16 %0, v[168:171], a[60:63], %0" : "+v"(g[1][3][0]) :  : ); SB();
  asm volatile("buffer_load_dwordx4 v[244:247], %0, %1, %2 offen offset:1024 sc1" :  : "v"(vB), "s"(rWS), "s"(4096u) : "memory"); SB();
  asm volatile("v_mul_f32 %0, %0, %1" : "+v"(a1[1][1][2]) : "v"(gt[2]) : ); SB();
  asm volatile("v_mfma_f32_16x16x32_bf16 %0, v[172:175], a[48:51], %0" : "+v"(g[1][0][1]) :  : ); SB();
  asm volatile("v_mul_f32 %0, %0, %1" : "+v"(a1[1][1][3]) : "v"(gt[3]) : ); SB();
  asm volatile("v_mfma_f32_16x16x32_bf16 %0, v[172:175], a[52:55], %0" : "+v"(g[1][1][1]) :  : ); SB();
  asm volatile("buffer_load_dwordx4 v[248:251], %0, %1, %2 offen offset:0 sc1" :  : "v"(vB), "s"(rWS), "s"(69632u) : "memory"); SB();
  asm volatile("v_mul_f32 %0, %1, %1" : "=v"(gt[0]) : "v"(a1[1][2][0]) : ); SB();
  asm volatile("v_mfma_f32_16x16x32_bf16 %0, v[172:175], a[56:59], %0" : "+v"(g[1][2][1]) :  : ); SB();
  asm volatile("v_mul_f32 %0, %1, %1" : "=v"(gt[1]) : "v"(a1[1][2][1]) : ); SB();
  asm volatile("v_mfma_f32_16x16x32_bf16 %0, v[172:175], a[60:63], %0" : "+v"(g[1][3][1]) :  : ); SB();
  asm volatile("v_mul_f32 %0, %1, %1" : "=v"(gt[2]) : "v"(a1[1][2][2]) : ); SB();
  asm volatile("s_waitcnt vmcnt(17)" :  :  : "memory"); SB();
  asm volatile("v_mfma_f32_16x16x32_bf16 %0, v[176:179], a[64:67], %0" : "+v"(g[1][0][0]) :  : ); SB();
  asm volatile("s_waitcnt lgkmcnt(0)" : "+v"(pr[0]), "+v"(pr[1]), "+v"(pr[2]), "+v"(pr[3]), "+v"(pr[4]), "+v"(pr[5]) :  : "memory"); SB();
  asm volatile("buffer_load_dwordx4 v[252:255], %0, %1, %2 offen offset:1024 sc1" :  : "v"(vB), "s"(rWS), "s"(69632u) : "memory"); SB();
  asm volatile("v_mfma_f32_16x16x32_bf16 %0, v[176:179], a[68:71], %0" : "+v"(g[1][1][0]) :  : ); SB();
  asm volatile("v_xor_b32 %0, 393216, %0" : "+v"(vB) :  : "memory"); SB();
  asm volatile("v_add_f32 %0, %0, %1" : "+v"(pr[0][0]) : "v"(g[0][0][0][0]) : ); SB();
  asm volatile("v_mfma_f32_16x16x32_bf16 %0, v[176:179], a[72:75], %0" : "+v"(g[1][2][0]) :  : ); SB();
  asm volatile("v_add_f32 %0, %0, %1" : "+v"(pr[0][1]) : "v"(g[0][0][0][1]) : ); SB();
  asm volatile("v_mfma_f32_16x16x32_bf16 %0, v[176:179], a[76:79], %0" : "+v"(g[1][3][0]) :  : ); SB();
  asm volatile("buffer_load_dwordx4 v[160:163], %0, %1, %2 offen offset:0 sc1" :  : "v"(vB), "s"(rWS), "s"(4096u) : "memory"); SB();
  asm volatile("v_add_f32 %0, %0, %1" : "+v"(pr[0][2]) : "v"(g[0][0][0][2]) : ); SB();
  asm volatile("v_mfma_f32_16x16x32_bf16 %0, v[180:183], a[64:67], %0" : "+v"(g[1][0][1]) :  : ); SB();
  asm volatile("v_add_f32 %0, %0, %1" : "+v"(pr[0][3]) : "v"(g[0][0][0][3]) : ); SB();
  asm volatile("v_mfma_f32_16x16x32_bf16 %0, v[180:183], a[68:71], %0" : "+v"(g[1][1][1]) :  : ); SB();
  asm volatile("v_add_f32 %0, %0, %1" : "+v"(pr[1][0]) : "v"(g[0][0][1][0]) : ); SB();
  asm volatile("v_mfma_f32_16x16x32_bf16 %0, v[180:183], a[72:75], %0" : "+v"(g[1][2][1]) :  : ); SB();
  asm volatile("buffer_load_dwordx4 v[164:167], %0, %1, %2 offen offset:1024 sc1" :  : "v"(vB), "s"(rWS), "s"(4096u) : "memory"); SB();
  asm volatile("v_add_f32 %0, %0, %1" : "+v"(pr[1][1]) : "v"(g[0][0][1][1]) : ); SB();
  asm volatile("v_mfma_f32_16x16x32_bf16 %0, v[180:183], a[76:79], %0" : "+v"(g[1][3][1]) :  : ); SB();
  asm volatile("v_add_f32 %0, %0, %1" : "+v"(pr[1][2]) : "v"(g[0][0][1][2]) : ); SB();
  asm volatile("s_waitcnt vmcnt(18)" :  :  : "memory"); SB();
  asm volatile("v_mfma_f32_16x16x32_bf16 %0, v[184:187], a[80:83], %0" : "+v"(g[1][0][0]) :  : ); SB();
  asm volatile("v_add_f32 %0, %0, %1" : "+v"(pr[1][3]) : "v"(g[0][0][1][3]) : ); SB();
  asm volatile("v_mfma_f32_16x16x32_bf16 %0, v[184:187], a[84:87], %0" : "+v"(g[1][1][0]) :  : ); SB();
  asm volatile("v_add_f32 %0, %0, %1" : "+v"(pr[2][0]) : "v"(pr[4][0]) : ); SB();
  asm volatile("v_mfma_f32_16x16x32_bf16 %0, v[184:187], a[88:91], %0" : "+v"(g[1][2][0]) :  : ); SB();
  asm volatile("buffer_load_dwordx4 v[168:171], %0, %1, %2 offen offset:0 sc1" :  : "v"(vB), "s"(rWS), "s"(69632u) : "memory"); SB();
  asm volatile("v_add_f32 %0, %0, %1" : "+v"(pr[2][1]) : "v"(pr[4][1]) : ); SB();
  asm volatile("v_mfma_f32_16x16x32_bf16 %0, v[184:187], a[92:95], %0" : "+v"(g[1][3][0]) :  : ); SB();
  asm volatile("v_add_f32 %0, %0, %1" : "+v"(pr[2][2]) : "v"(pr[4][2]) : ); SB();
  asm volatile("v_mfma_f32_16x16x32_bf16 %0, v[188:191], a[80:83], %0" : "+v"(g[1][0][1]) :  : ); SB();
  asm volatile("v_add_f32 %0, %0, %1" : "+v"(pr[2][3]) : "v"(pr[4][3]) : ); SB();
  asm volatile("v_mfma_f32_16x16x32_bf16 %0, v[188:191], a[84:87], %0" : "+v"(g[1][1][1]) :  : ); SB();
  asm volatile("buffer_load_dwordx4 v[172:175], %0, %1, %2 offen offset:1024 sc1" :  : "v"(vB), "s"(rWS), "s"(69632u) : "memory"); SB();
  asm volatile("v_add_f32 %0, %0, %1" : "+v"(pr[3][0]) : "v"(pr[5][0]) : ); SB();
  asm volatile("v_mfma_f32_16x16x32_bf16 %0, v[188:191], a[88:91], %0" : "+v"(g[1][2][1]) :  : ); SB();
  asm volatile("v_xor_b32 %0, 131072, %0" : "+v"(vB) :  : "memory"); SB();
  asm volatile("v_add_f32 %0, %0, %1" : "+v"(pr[3][1]) : "v"(pr[5][1]) : ); SB();
  asm volatile("v_mfma_f32_16x16x32_bf16 %0, v[188:191], a[92:95], %0" : "+v"(g[1][3][1]) :  : ); SB();
  asm volatile("v_add_f32 %0, %0, %1" : "+v"(pr[3][2]) : "v"(pr[5][2]) : ); SB();
  asm volatile("s_waitcnt vmcnt(18)" :  :  : "memory"); SB();
  asm volatile("v_mfma_f32_16x16x32_bf16 %0, v[192:195], a[96:99], %0" : "+v"(g[1][0][0]) :  : ); SB();
  asm volatile("v_add_f32 %0, %0, %1" : "+v"(pr[3][3]) : "v"(pr[5][3]) : ); SB();
  asm volatile("v_mfma_f32_16x16x32_bf16 %0, v[192:195], a[100:103], %0" : "+v"(g[1][1][0]) :  : ); SB();
  asm volatile("buffer_load_dwordx4 v[176:179], %0, %1, %2 offen offset:0 sc1" :  : "v"(vB), "s"(rWS), "s"(4096u) : "memory"); SB();
  asm volatile("v_add_f32 %0, %0, %1" : "+v"(pr[0][0]) : "v"(pr[2][0]) : ); SB();
  asm volatile("v_mfma_f32_16x16x32_bf16 %0, v[192:195], a[104:107], %0" : "+v"(g[1][2][0]) :  : ); SB();
  asm volatile("v_add_f32 %0, %0, %1" : "+v"(pr[0][1]) : "v"(pr[2][1]) : ); SB();
  asm volatile("v_mfma_f32_16x16x32_bf16 %0, v[192:195], a[108:111], %0" : "+v"(g[1][3][0]) :  : ); SB();
  asm volatile("v_add_f32 %0, %0, %1" : "+v"(pr[0][2]) : "v"(pr[2][2]) : ); SB();
  asm volatile("v_mfma_f32_16x16x32_bf16 %0, v[196:199], a[96:99], %0" : "+v"(g[1][0][1]) :  : ); SB();
  asm volatile("buffer_load_dwordx4 v[180:183], %0, %1, %2 offen offset:1024 sc1" :  : "v"(vB), "s"(rWS), "s"(4096u) : "memory"); SB();
  asm volatile("v_add_f32 %0, %0, %1" : "+v"(pr[0][3]) : "v"(pr[2][3]) : ); SB();
  asm volatile("v_mfma_f32_16x16x32_bf16 %0, v[196:199], a[100:103], %0" : "+v"(g[1][1][1]) :  : ); SB();
  asm volatile("v_add_f32 %0, %0, %1" : "+v"(pr[1][0]) : "v"(pr[3][0]) : ); SB();
  asm volatile("v_mfma_f32_16x16x32_bf16 %0, v[196:199], a[104:107], %0" : "+v"(g[1][2][1]) :  : ); SB();
  asm volatile("v_add_f32 %0, %0, %1" : "+v"(pr[1][1]) : "v"(pr[3][1]) : ); SB();
  asm volatile("v_mfma_f32_16x16x32_bf16 %0, v[196:199], a[108:111], %0" : "+v"(g[1][3][1]) :  : ); SB();
  asm volatile("v_add_f32 %0, %0, %1" : "+v"(pr[1][2]) : "v"(pr[3][2]) : ); SB();
  asm volatile("s_waitcnt vmcnt(18)" :  :  : "memory"); SB();
  asm volatile("v_mfma_f32_16x16x32_bf16 %0, v[200:203], a[112:115], %0" : "+v"(g[1][0][0]) :  : ); SB();
  asm volatile("buffer_load_dwordx4 v[184:187], %0, %1, %2 offen offset:0 sc1" :  : "v"(vB), "s"(rWS), "s"(69632u) : "memory"); SB();
  asm volatile("v_add_f32 %0, %0, %1" : "+v"(pr[1][3]) : "v"(pr[3][3]) : ); SB();
  asm volatile("v_mfma_f32_16x16x32_bf16 %0, v[200:203], a[116:119], %0" : "+v"(g[1][1][0]) :  : ); SB();
  asm volatile("v_mul_f32 %0, %0, %1" : "+v"(pr[0][0]) : "v"(a1[0][0][0]) : ); SB();
  asm volatile("v_mfma_f32_16x16x32_bf16 %0, v[200:203], a[120:123], %0" : "+v"(g[1][2][0]) :  : ); SB();
  asm volatile("v_mul_f32 %0, %0, %1" : "+v"(pr[0][1]) : "v"(a1[0][0][1]) : ); SB();
  asm volatile("v_mfma_f32_16x16x32_bf16 %0, v[200:203], a[124:127], %0" : "+v"(g[1][3][0]) :  : ); SB();
  asm volatile("buffer_load_dwordx4 v[188:191], %0, %1, %2 offen offset:1024 sc1" :  : "v"(vB), "s"(rWS), "s"(69632u) : "memory"); SB();
  asm volatile("v_mul_f32 %0, %0, %1" : "+v"(pr[0][2]) : "v"(a1[0][0][2]) : ); SB();
  asm volatile("v_mfma_f32_16x16x32_bf16 %0, v[204:207], a[112:115], %0" : "+v"(g[1][0][1]) :  : ); SB();
  asm volatile("v_xor_b32 %0, 917504, %0" : "+v"(vB) :  : "memory"); SB();
  asm volatile("v_mul_f32 %0, %0, %1" : "+v"(pr[0][3]) : "v"(a1[0][0][3]) : ); SB();
  asm volatile("v_mfma_f32_16x16x32_bf16 %0, v[204:207], a[116:119], %0" : "+v"(g[1][1][1]) :  : ); SB();
  asm volatile("v_mul_f32 %0, %0, %1" : "+v"(pr[1][0]) : "v"(a1[0][1][0]) : ); SB();
  asm volatile("v_mfma_f32_16x16x32_bf16 %0, v[204:207], a[120:123], %0" : "+v"(g[1][2][1]) :  : ); SB();
  asm volatile("v_mul_f32 %0, %0, %1" : "+v"(pr[1][1]) : "v"(a1[0][1][1]) : ); SB();
  asm volatile("v_mfma_f32_16x16x32_bf16 %0, v[204:207], a[124:127], %0" : "+v"(g[1][3][1]) :  : ); SB();
  asm volatile("buffer_load_dwordx4 v[192:195], %0, %1, %2 offen offset:0 sc1" :  : "v"(vB), "s"(rWS), "s"(4096u) : "memory"); SB();
  asm volatile("s_waitcnt vmcnt(19)" :  :  : "memory"); SB();
  asm volatile("v_mfma_f32_16x16x32_bf16 %0, v[208:211], a[128:131], %0" : "+v"(g[1][0][0]) :  : ); SB();
  asm volatile("v_mul_f32 %0, %0, %1" : "+v"(pr[1][2]) : "v"(a1[0][1][2]) : ); SB();
  asm volatile("v_mfma_f32_16x16x32_bf16 %0, v[208:211], a[132:135], %0" : "+v"(g[1][1][0]) :  : ); SB();
  asm volatile("v_mul_f32 %0, %0, %1" : "+v"(pr[1][3]) : "v"(a1[0][1][3]) : ); SB();
  asm volatile("v_mfma_f32_16x16x32_bf16 %0, v[208:211], a[136:139], %0" : "+v"(g[1][2][0]) :  : ); SB();
  asm volatile("buffer_load_dwordx4 v[196:199], %0, %1, %2 offen offset:1024 sc1" :  : "v"(vB), "s"(rWS), "s"(4096u) : "memory"); SB();
  asm volatile("v_cvt_pk_bf16_f32 %0, %1, %2" : "=v"(ho[0][0]) : "v"(pr[0][0]), "v"(pr[0][1]) : ); SB();
  asm volatile("v_mfma_f32_16x16x32_bf16 %0, v[208:211], a[140:143], %0" : "+v"(g[1][3][0]) :  : ); SB();
  asm volatile("v_cvt_pk_bf16_f32 %0, %1, %2" : "=v"(ho[0][1]) : "v"(pr[0][2]), "v"(pr[0][3]) : ); SB();
  asm volatile("v_mfma_f32_16x16x32_bf16 %0, v[212:215], a[128:131], %0" : "+v"(g[1][0][1]) :  : ); SB();
  asm volatile("v_cvt_pk_bf16_f32 %0, %1, %2" : "=v"(ho[0][2]) : "v"(pr[1][0]), "v"(pr[1][1]) : ); SB();
  asm volatile("v_mfma_f32_16x16x32_bf16 %0, v[212:215], a[132:135], %0" : "+v"(g[1][1][1]) :  : ); SB();
  asm volatile("v_cvt_pk_bf16_f32 %0, %1, %2" : "=v"(ho[0][3]) : "v"(pr[1][2]), "v"(pr[1][3]) : ); SB();
  asm volatile("v_mfma_f32_16x16x32_bf16 %0, v[212:215], a[136:139], %0" : "+v"(g[1][2][1]) :  : ); SB();
  asm volatile("v_mul_f32 %0, %1, %1" : "=v"(gt[3]) : "v"(a1[1][2][3]) : ); SB();
  asm volatile("v_mfma_f32_16x16x32_bf16 %0, v[212:215], a[140:143], %0" : "+v"(g[1][3][1]) :  : ); SB();
  asm volatile("v_fma_f32 %0, %0, %1, %2" : "+v"(gt[0]) : "v"(k2[0]), "v"(k1[0]) : ); SB();
  asm volatile("s_waitcnt vmcnt(18)" :  :  : "memory"); SB();
  asm volatile("v_mfma_f32_16x16x32_bf16 %0, v[216:219], a[144:147], %0" : "+v"(g[1][0][0]) :  : ); SB();
  asm volatile("v_fma_f32 %0, %0, %1, %2" : "+v"(gt[1]) : "v"(k2[0]), "v"(k1[0]) : ); SB();
  asm volatile("v_mfma_f32_16x16x32_bf16 %0, v[216:219], a[148:151], %0" : "+v"(g[1][1][0]) :  : ); SB();
  asm volatile("buffer_store_dwordx4 %1, %0, %2, %3 offen offset:0 sc1\n\ts_nop 1" :  : "v"(vH_), "v"(ho[0]), "s"(rWS), "s"(0u) : "memory"); SB();
  asm volatile("v_fma_f32 %0, %0, %1, %2" : "+v"(gt[2]) : "v"(k2[0]), "v"(k1[0]) : ); SB();
  asm volatile("v_mfma_f32_16x16x32_bf16 %0, v[216:219], a[152:155], %0" : "+v"(g[1][2][0]) :  : ); SB();
  asm volatile("v_fma_f32 %0, %0, %1, %2" : "+v"(gt[3]) : "v"(k2[0]), "v"(k1[0]) : ); SB();
  asm volatile("v_mfma_f32_16x16x32_bf16 %0, v[216:219], a[156:159], %0" : "+v"(g[1][3][0]) :  : ); SB();
  asm volatile("v_mul_f32 %0, %0, %1" : "+v"(gt[0]) : "v"(a1[1][2][0]) : ); SB();
  asm volatile("v_mfma_f32_16x16x32_bf16 %0, v[220:223], a[144:147], %0" : "+v"(g[1][0][1]) :  : ); SB();
  asm volatile("v_mul_f32 %0, %0, %1" : "+v"(gt[1]) : "v"(a1[1][2][1]) : ); SB();
  asm volatile("v_mfma_f32_16x16x32_bf16 %0, v[220:223], a[148:151], %0" : "+v"(g[1][1][1]) :  : ); SB();
  asm volatile("buffer_load_dwordx4 v[200:203], %0, %1, %2 offen offset:0 sc1" :  : "v"(vB), "s"(rWS), "s"(69632u) : "memory"); SB();
  asm volatile("v_mul_f32 %0, %0, %1" : "+v"(gt[2]) : "v"(a1[1][2][2]) : ); SB();
  asm volatile("v_mfma_f32_16x16x32_bf16 %0, v[220:223], a[152:155], %0" : "+v"(g[1][2][1]) :  : ); SB();
  asm volatile("v_mul_f32 %0, %0, %1" : "+v"(gt[3]) : "v"(a1[1][2][3]) : ); SB();
  asm volatile("v_mfma_f32_16x16x32_bf16 %0, v[220:223], a[156:159], %0" : "+v"(g[1][3][1]) :  : ); SB();
  asm volatile("v_exp_f32 %0, %1" : "=v"(ge[0]) : "v"(gt[0]) : ); SB();
  ST(29);
  asm volatile("s_waitcnt vmcnt(18)" :  :  : "memory"); SB();
  asm volatile("v_mfma_f32_16x16x32_bf16 %0, v[224:227], a[160:163], %1" : "=&v"(g[0][0][0]) : "v"(bs4) : ); SB();
  asm volatile("s_barrier" :  :  : "memory"); SB();
  asm volatile("buffer_load_dwordx4 v[204:207], %0, %1, %2 offen offset:1024 sc1" :  : "v"(vB), "s"(rWS), "s"(69632u) : "memory"); SB();
  asm volatile("v_mfma_f32_16x16x32_bf16 %0, v[224:227], a[164:167], 0" : "=&v"(g[0][1][0]) :  : ); SB();
  asm volatile("v_xor_b32 %0, 131072, %0" : "+v"(vB) :  : "memory"); SB();
  asm volatile("v_exp_f32 %0, %1" : "=v"(ge[1]) : "v"(gt[1]) : ); SB();
  asm volatile("v_mfma_f32_16x16x32_bf16 %0, v[224:227], a[168:171], 0" : "=&v"(g[0][2][0]) :  : ); SB();
  asm volatile("ds_write_b128 %0, %1 offset:0" :  : "v"(pw2), "v"(g[1][1][0]) : "memory"); SB();
  asm volatile("v_mfma_f32_16x16x32_bf16 %0, v[224:227], a[172:175], 0" : "=&v"(g[0][3][0]) :  : ); SB();
  asm volatile("buffer_load_dwordx4 v[208:211], %0, %1, %2 offen offset:0 sc1" :  : "v"(vB), "s"(rWS), "s"(4096u) : "memory"); SB();
  asm volatile("v_exp_f32 %0, %1" : "=v"(ge[2]) : "v"(gt[2]) : ); SB();
  asm volatile("v_mfma_f32_16x16x32_bf16 %0, v[228:231], a[160:163], %1" : "=&v"(g[0][0][1]) : "v"(bs4) : ); SB();
  asm volatile("buffer_load_dwordx4 v[212:215], %0, %1, %2 offen offset:1024 sc1" :  : "v"(vB), "s"(rWS), "s"(4096u) : "memory"); SB();
  asm volatile("v_exp_f32 %0, %1" : "=v"(ge[3]) : "v"(gt[3]) : ); SB();
  asm volatile("v_mfma_f32_16x16x32_bf16 %0, v[228:231], a[164:167], 0" : "=&v"(g[0][1][1]) :  : ); SB();
  asm volatile("ds_write_b128 %0, %1 offset:1024" :  : "v"(pw2), "v"(g[1][1][1]) : "memory"); SB();
  asm volatile("v_mfma_f32_16x16x32_bf16 %0, v[228:231], a[168:171], 0" : "=&v"(g[0][2][1]) :  : ); SB();
  asm volatile("buffer_load_dwordx4 v[216:219], %0, %1, %2 offen offset:0 sc1" :  : "v"(vB), "s"(rWS), "s"(69632u) : "memory"); SB();
  asm volatile("v_fma_f32 %0, %0, %1, %1" : "+v"(ge[0]) : "v"(xd[0]) : ); SB();
  asm volatile("v_mfma_f32_16x16x32_bf16 %0, v[228:231], a[172:175], 0" : "=&v"(g[0][3][1]) :  : ); SB();
  asm volatile("v_fma_f32 %0, %0, %1, %1" : "+v"(ge[1]) : "v"(xd[0]) : ); SB();
  asm volatile("s_waitcnt vmcnt(20)" :  :  : "memory"); SB();
  asm volatile("v_mfma_f32_16x16x32_bf16 %0, v[232:235], a[176:179], %0" : "+v"(g[0][0][0]) :  : ); SB();
  asm volatile("ds_write_b128 %0, %1 offset:2048" :  : "v"(pw2), "v"(g[1][2][0]) : "memory"); SB();
  asm volatile("v_mfma_f32_16x16x32_bf16 %0, v[232:235], a[180:183], %0" : "+v"(g[0][1][0]) :  : ); SB();
  asm volatile("buffer_load_dwordx4 v[220:223], %0, %1, %2 offen offset:1024 sc1" :  : "v"(vB), "s"(rWS), "s"(69632u) : "memory"); SB();
  asm volatile("v_fma_f32 %0, %0, %1, %1" : "+v"(ge[2]) : "v"(xd[0]) : ); SB();
  asm volatile("v_mfma_f32_16x16x32_bf16 %0, v[232:235], a[184:187], %0" : "+v"(g[0][2][0]) :  : ); SB();
  asm volatile("v_xor_b32 %0, 393216, %0" : "+v"(vB) :  : "memory"); SB();
  asm volatile("v_fma_f32 %0, %0, %1, %1" : "+v"(ge[3]) : "v"(xd[0]) : ); SB();
  asm volatile("v_mfma_f32_16x16x32_bf16 %0, v[232:235], a[188:191], %0" : "+v"(g[0][3][0]) :  : ); SB();
  asm volatile("ds_write_b128 %0, %1 offset:3072" :  : "v"(pw2), "v"(g[1][2][1]) : "memory"); SB();
  asm volatile("v_mfma_f32_16x16x32_bf16 %0, v[236:239], a[176:179], %0" : "+v"(g[0][0][1]) :  : ); SB();
  asm volatile("buffer_load_dwordx4 v[224:227], %0, %1, %2 offen offset:0 sc1" :  : "v"(vB), "s"(rWS), "s"(4096u) : "memory"); SB();
  asm volatile("v_rcp_f32 %0, %1" : "=v"(gt[0]) : "v"(ge[0]) : ); SB();
  asm volatile("v_mfma_f32_16x16x32_bf16 %0, v[236:239], a[180:183], %0" : "+v"(g[0][1][1]) :  : ); SB();
  asm volatile("v_rcp_f32 %0, %1" : "=v"(gt[1]) : "v"(ge[1]) : ); SB();
  asm volatile("v_mfma_f32_16x16x32_bf16 %0, v[236:239], a[184:187], %0" : "+v"(g[0][2][1]) :  : ); SB();
  asm volatile("ds_write_b128 %0, %1 offset:4096" :  : "v"(pw2), "v"(g[1][3][0]) : "memory"); SB();
  asm volatile("v_mfma_f32_16x16x32_bf16 %0, v[236:239], a[188:191], %0" : "+v"(g[0][3][1]) :  : ); SB();
  asm volatile("buffer_load_dwordx4 v[228:231], %0, %1, %2 offen offset:1024 sc1" :  : "v"(vB), "s"(rWS), "s"(4096u) : "memory"); SB();
  asm volatile("s_waitcnt vmcnt(21)" :  :  : "memory"); SB();
  asm volatile("v_mfma_f32_16x16x32_bf16 %0, v[240:243], a[192:195], %0" : "+v"(g[0][0][0]) :  : ); SB();
  asm volatile("v_rcp_f32 %0, %1" : "=v"(gt[2]) : "v"(ge[2]) : ); SB();
  asm volatile("v_mfma_f32_16x16x32_bf16 %0, v[240:243], a[196:199], %0" : "+v"(g[0][1][0]) :  : ); SB();
  asm volatile("ds_write_b128 %0, %1 offset:5120" :  : "v"(pw2), "v"(g[1][3][1]) : "memory"); SB();
  asm volatile("v_mfma_f32_16x16x32_bf16 %0, v[240:243], a[200:203], %0" : "+v"(g[0][2][0]) :  : ); SB();
  asm volatile("buffer_load_dwordx4 v[232:235], %0, %1, %2 offen offset:0 sc1" :  : "v"(vB), "s"(rWS), "s"(69632u) : "memory"); SB();
  asm volatile("v_rcp_f32 %0, %1" : "=v"(gt[3]) : "v"(ge[3]) : ); SB();
  asm volatile("v_mfma_f32_16x16x32_bf16 %0, v[240:243], a[204:207], %0" : "+v"(g[0][3][0]) :  : ); SB();
  asm volatile("buffer_load_dwordx4 v[236:239], %0, %1, %2 offen offset:1024 sc1" :  : "v"(vB), "s"(rWS), "s"(69632u) : "memory"); SB();
  asm volatile("v_mul_f32 %0, %0, %1" : "+v"(a1[1][2][0]) : "v"(gt[0]) : ); SB();
  asm volatile("v_mfma_f32_16x16x32_bf16 %0, v[244:247], a[192:195], %0" : "+v"(g[0][0][1]) :  : ); SB();
  asm volatile("s_waitcnt lgkmcnt(0)\n\ts_barrier" :  :  : "memory"); SB();
  asm volatile("ds_read_b128 %0, %1 offset:0" : "=&v"(pr[0]) : "v"(pe[0]) : "memory"); SB();
  asm volatile("v_mfma_f32_16x16x32_bf16 %0, v[244:247], a[196:199], %0" : "+v"(g[0][1][1]) :  : ); SB();
  asm volatile("ds_read_b128 %0, %1 offset:1024" : "=&v"(pr[1]) : "v"(pe[0]) : "memory"); SB();
  asm volatile("v_mul_f32 %0, %0, %1" : "+v"(a1[1][2][1]) : "v"(gt[1]) : ); SB();
  asm volatile("v_mfma_f32_16x16x32_bf16 %0, v[244:247], a[200:203], %0" : "+v"(g[0][2][1]) :  : ); SB();
  asm volatile("ds_read_b128 %0, %1 offset:2048" : "=&v"(pr[2]) : "v"(pe[1]) : "memory"); SB();
  asm volatile("v_mul_f32 %0, %0, %1" : "+v"(a1[1][2][2]) : "v"(gt[2]) : ); SB();
  asm volatile("v_mfma_f32_16x16x32_bf16 %0, v[244:247], a[204:207], %0" : "+v"(g[0][3][1]) :  : ); SB();
  asm volatile("ds_read_b128 %0, %1 offset:3072" : "=&v"(pr[3]) : "v"(pe[1]) : "memory"); SB();
  asm volatile("s_waitcnt vmcnt(21)" :  :  : "memory"); SB();
  asm volatile("v_mfma_f32_16x16x32_bf16 %0, v[248:251], a[208:211], %0" : "+v"(g[0][0][0]) :  : ); SB();
  asm volatile("ds_read_b128 %0, %1 offset:4096" : "=&v"(pr[4]) : "v"(pe[2]) : "memory"); SB();
  asm volatile("v_mul_f32 %0, %0, %1" : "+v"(a1[1][2][3]) : "v"(gt[3]) : ); SB();
  asm volatile("v_mfma_f32_16x16x32_bf16 %0, v[248:251], a[212:215], %0" : "+v"(g[0][1][0]) :  : ); SB();
  asm volatile("ds_read_b128 %0, %1 offset:5120" : "=&v"(pr[5]) : "v"(pe[2]) : "memory"); SB();
  asm volatile("v_mfma_f32_16x16x32_bf16 %0, v[248:251], a[216:219], %0" : "+v"(g[0][2][0]) :  : ); SB();
  asm volatile("v_xor_b32 %0, 131072, %0" : "+v"(vB) :  : "memory"); SB();
  asm volatile("v_mfma_f32_16x16x32_bf16 %0, v[248:251], a[220:223], %0" : "+v"(g[0][3][0]) :  : ); SB();
  asm volatile("buffer_load_dwordx4 v[240:243], %0, %1, %2 offen offset:0 sc1" :  : "v"(vB), "s"(rWS), "s"(4096u) : "memory"); SB();
  asm volatile("v_mfma_f32_16x16x32_bf16 %0, v[252:255], a[208:211], %0" : "+v"(g[0][0][1]) :  : ); SB();
  asm volatile("buffer_load_dwordx4 v[244:247], %0, %1, %2 offen offset:1024 sc1" :  : "v"(vB), "s"(rWS), "s"(4096u) : "memory"); SB();
  asm volatile("v_mfma_f32_16x16x32_bf16 %0, v[252:255], a[212:215], %0" : "+v"(g[0][1][1]) :  : ); SB();
  asm volatile("v_mfma_f32_16x16x32_bf16 %0, v[252:255], a[216:219], %0" : "+v"(g[0][2][1]) :  : ); SB();
  asm volatile("v_mfma_f32_16x16x32_bf16 %0, v[252:255], a[220:223], %0" : "+v"(g[0][3][1]) :  : ); SB();
  asm volatile("s_waitcnt lgkmcnt(0)" : "+v"(pr[0]), "+v"(pr[1]), "+v"(pr[2]), "+v"(pr[3]), "+v"(pr[4]), "+v"(pr[5]) :  : "memory"); SB();
  asm volatile("s_add_u32 m0, %0, 60032" :  : "s"(mU) : "memory", "scc"); SB();
  asm volatile("s_waitcnt vmcnt(21)" :  :  : "memory"); SB();
  asm volatile("v_mfma_f32_16x16x32_bf16 %0, v[160:163], a[224:227], %0" : "+v"(g[0][0][0]) :  : ); SB();
  asm volatile("buffer_load_dwordx4 %0, %1, %2 offen sc1 lds" :  : "v"(vF), "s"(rWS), "s"(W_KV) : "memory"); SB();
  asm volatile("v_add_f32 %0, %0, %1" : "+v"(pr[0][0]) : "v"(g[1][0][0][0]) : ); SB();
  asm volatile("v_mfma_f32_16x16x32_bf16 %0, v[160:163], a[228:231], %0" : "+v"(g[0][1][0]) :  : ); SB();
  asm volatile("v_add_f32 %0, %0, %1" : "+v"(pr[0][1]) : "v"(g[1][0][0][1]) : ); SB();
  asm volatile("v_mfma_f32_16x16x32_bf16 %0, v[160:163], a[232:235], %0" : "+v"(g[0][2][0]) :  : ); SB();
  asm volatile("v_add_f32 %0, %0, %1" : "+v"(pr[0][2]) : "v"(g[1][0][0][2]) : ); SB();
  asm volatile("v_mfma_f32_16x16x32_bf16 %0, v[160:163], a[236:239], %0" : "+v"(g[0][3][0]) :  : ); SB();
  asm volatile("v_add_f32 %0, %0, %1" : "+v"(pr[0][3]) : "v"(g[1][0][0][3]) : ); SB();
  asm volatile("v_mfma_f32_16x16x32_bf16 %0, v[164:167], a[224:227], %0" : "+v"(g[0][0][1]) :  : ); SB();
  asm volatile("v_add_f32 %0, %0, %1" : "+v"(pr[1][0]) : "v"(g[1][0][1][0]) : ); SB();
  asm volatile("v_mfma_f32_16x16x32_bf16 %0, v[164:167], a[228:231], %0" : "+v"(g[0][1][1]) :  : ); SB();
  asm volatile("v_add_f32 %0, %0, %1" : "+v"(pr[1][1]) : "v"(g[1][0][1][1]) : ); SB();
  asm volatile("v_mfma_f32_16x16x32_bf16 %0, v[164:167], a[232:235], %0" : "+v"(g[0][2][1]) :  : ); SB();
  asm volatile("v_add_f32 %0, %0, %1" : "+v"(pr[1][2]) : "v"(g[1][0][1][2]) : ); SB();
  asm volatile("v_mfma_f32_16x16x32_bf16 %0, v[164:167], a[236:239], %0" : "+v"(g[0][3][1]) :  : ); SB();
  asm volatile("v_add_f32 %0, %0, %1" : "+v"(pr[1][3]) : "v"(g[1][0][1][3]) : ); SB();
  asm volatile("s_waitcnt vmcnt(20)" :  :  : "memory"); SB();
  asm volatile("v_mfma_f32_16x16x32_bf16 %0, v[168:171], a[240:243], %0" : "+v"(g[0][0][0]) :  : ); SB();
  asm volatile("v_add_f32 %0, %0, %1" : "+v"(pr[2][0]) : "v"(pr[4][0]) : ); SB();
  asm volatile("v_mfma_f32_16x16x32_bf16 %0, v[168:171], a[244:247], %0" : "+v"(g[0][1][0]) :  : ); SB();
  asm volatile("s_add_u32 m0, %0, 72960" :  : "s"(mU) : "memory", "scc"); SB();
  asm volatile("v_add_f32 %0, %0, %1" : "+v"(pr[2][1]) : "v"(pr[4][1]) : ); SB();
  asm volatile("v_mfma_f32_16x16x32_bf16 %0, v[168:171], a[248:251], %0" : "+v"(g[0][2][0]) :  : ); SB();
  asm volatile("buffer_load_dwordx4 %0, %1, %2 offen sc1 lds" :  : "v"(vF), "s"(rWS), "s"(W_KV + 4096) : "memory"); SB();
  asm volatile("v_add_f32 %0, %0, %1" : "+v"(pr[2][2]) : "v"(pr[4][2]) : ); SB();
  asm volatile("v_mfma_f32_16x16x32_bf16 %0, v[168:171], a[252:255], %0" : "+v"(g[0][3][0]) :  : ); SB();
  asm volatile("v_add_f32 %0, %0, %1" : "+v"(pr[2][3]) : "v"(pr[4][3]) : ); SB();
  asm volatile("v_mfma_f32_16x16x32_bf16 %0, v[172:175], a[240:243], %0" : "+v"(g[0][0][1]) :  : ); SB();
  asm volatile("v_add_f32 %0, %0, %1" : "+v"(pr[3][0]) : "v"(pr[5][0]) : ); SB();
  asm volatile("v_mfma_f32_16x16x32_bf16 %0, v[172:175], a[244:247], %0" : "+v"(g[0][1][1]) :  : ); SB();
  asm volatile("buffer_load_dwordx4 v[248:251], %0, %1, %2 offen offset:0 sc1" :  : "v"(vB), "s"(rWS), "s"(69632u) : "memory"); SB();
  asm volatile("v_add_f32 %0, %0, %1" : "+v"(pr[3][1]) : "v"(pr[5][1]) : ); SB();
  asm volatile("v_mfma_f32_16x16x32_bf16 %0, v[172:175], a[248:251], %0" : "+v"(g[0][2][1]) :  : ); SB();
  asm volatile("v_add_f32 %0, %0, %1" : "+v"(pr[3][2]) : "v"(pr[5][2]) : ); SB();
  asm volatile("v_mfma_f32_16x16x32_bf16 %0, v[172:175], a[252:255], %0" : "+v"(g[0][3][1]) :  : ); SB();
  asm volatile("v_add_f32 %0, %0, %1" : "+v"(pr[3][3]) : "v"(pr[5][3]) : ); SB();
  asm volatile("s_waitcnt vmcnt(20)" :  :  : "memory"); SB();
  asm volatile("v_mfma_f32_16x16x32_bf16 %0, v[176:179], a[0:3], %0" : "+v"(g[0][0][0]) :  : ); SB();
  asm volatile("v_add_f32 %0, %0, %1" : "+v"(pr[0][0]) : "v"(pr[2][0]) : ); SB();
  asm volatile("v_mfma_f32_16x16x32_bf16 %0, v[176:179], a[4:7], %0" : "+v"(g[0][1][0]) :  : ); SB();
  asm volatile("buffer_load_dwordx4 v[252:255], %0, %1, %2 offen offset:1024 sc1" :  : "v"(vB), "s"(rWS), "s"(69632u) : "memory"); SB();
  asm volatile("v_add_f32 %0, %0, %1" : "+v"(pr[0][1]) : "v"(pr[2][1]) : ); SB();
  asm volatile("v_mfma_f32_16x16x32_bf16 %0, v[176:179], a[8:11], %0" : "+v"(g[0][2][0]) :  : ); SB();
  asm volatile("v_add_f32 %0, %0, %1" : "+v"(pr[0][2]) : "v"(pr[2][2]) : ); SB();
  asm volatile("v_mfma_f32_16x16x32_bf16 %0, v[176:179], a[12:15], %0" : "+v"(g[0][3][0]) :  : ); SB();
  asm volatile("v_add_f32 %0, %0, %1" : "+v"(pr[0][3]) : "v"(pr[2][3]) : ); SB();
  asm volatile("v_mfma_f32_16x16x32_bf16 %0, v[180:183], a[0:3], %0" : "+v"(g[0][0][1]) :  : ); SB();
  asm volatile("buffer_load_dwordx4 a[160:163], %0, %1, %2 offen offset:0 sc1" :  : "v"(vO), "s"(rWS), "s"(0u) : "memory"); SB();
  asm volatile("v_add_f32 %0, %0, %1" : "+v"(pr[1][0]) : "v"(pr[3][0]) : ); SB();
  asm volatile("v_mfma_f32_16x16x32_bf16 %0, v[180:183], a[4:7], %0" : "+v"(g[0][1][1]) :  : ); SB();
  asm volatile("v_add_f32 %0, %0, %1" : "+v"(pr[1][1]) : "v"(pr[3][1]) : ); SB();
  asm volatile("v_mfma_f32_16x16x32_bf16 %0, v[180:183], a[8:11], %0" : "+v"(g[0][2][1]) :  : ); SB();
  asm volatile("v_add_f32 %0, %0, %1" : "+v"(pr[1][2]) : "v"(pr[3][2]) : ); SB();
  asm volatile("v_mfma_f32_16x16x32_bf16 %0, v[180:183], a[12:15], %0" : "+v"(g[0][3][1]) :  : ); SB();
  asm volatile("v_add_f32 %0, %0, %1" : "+v"(pr[1][3]) : "v"(pr[3][3]) : ); SB();
  asm volatile("s_waitcnt vmcnt(20)" :  :  : "memory"); SB();
  asm volatile("v_mfma_f32_16x16x32_bf16 %0, v[184:187], a[16:19], %0" : "+v"(g[0][0][0]) :  : ); SB();
  asm volatile("buffer_load_dwordx4 a[164:167], %0, %1, %2 offen offset:1024 sc1" :  : "v"(vO), "s"(rWS), "s"(0u) : "memory"); SB();
  asm volatile("v_mul_f32 %0, %0, %1" : "+v"(pr[0][0]) : "v"(a1[0][2][0]) : ); SB();
  asm volatile("v_mfma_f32_16x16x32_bf16 %0, v[184:187], a[20:23], %0" : "+v"(g[0][1][0]) :  : ); SB();
  asm volatile("v_mul_f32 %0, %0, %1" : "+v"(pr[0][1]) : "v"(a1[0][2][1]) : ); SB();
  asm volatile("v_mfma_f32_16x16x32_bf16 %0, v[184:187], a[24:27], %0" : "+v"(g[0][2][0]) :  : ); SB();
  asm volatile("v_mul_f32 %0, %0, %1" : "+v"(pr[0][2]) : "v"(a1[0][2][2]) : ); SB();
  asm volatile("v_mfma_f32_16x16x32_bf16 %0, v[184:187], a[28:31], %0" : "+v"(g[0][3][0]) :  : ); SB();
  asm volatile("buffer_load_dwordx4 a[168:171], %0, %1, %2 offen offset:2048 sc1" :  : "v"(vO), "s"(rWS), "s"(0u) : "memory"); SB();
  asm volatile("v_mul_f32 %0, %0, %1" : "+v"(pr[0][3]) : "v"(a1[0][2][3]) : ); SB();
  asm volatile("v_mfma_f32_16x16x32_bf16 %0, v[188:191], a[16:19], %0" : "+v"(g[0][0][1]) :  : ); SB();
  asm volatile("v_mul_f32 %0, %0, %1" : "+v"(pr[1][0]) : "v"(a1[1][0][0]) : ); SB();
  asm volatile("v_mfma_f32_16x16x32_bf16 %0, v[188:191], a[20:23], %0" : "+v"(g[0][1][1]) :  : ); SB();
  asm volatile("v_mul_f32 %0, %0, %1" : "+v"(pr[1][1]) : "v"(a1[1][0][1]) : ); SB();
  asm volatile("v_mfma_f32_16x16x32_bf16 %0, v[188:191], a[24:27], %0" : "+v"(g[0][2][1]) :  : ); SB();
  asm volatile("v_mul_f32 %0, %0, %1" : "+v"(pr[1][2]) : "v"(a1[1][0][2]) : ); SB();
  asm volatile("v_mfma_f32_16x16x32_bf16 %0, v[188:191], a[28:31], %0" : "+v"(g[0][3][1]) :  : ); SB();
  asm volatile("buffer_load_dwordx4 a[172:175], %0, %1, %2 offen offset:3072 sc1" :  : "v"(vO), "s"(rWS), "s"(0u) : "memory"); SB();
  asm volatile("s_waitcnt vmcnt(21)" :  :  : "memory"); SB();
  asm volatile("v_mfma_f32_16x16x32_bf16 %0, v[192:195], a[32:35], %0" : "+v"(g[0][0][0]) :  : ); SB();
  asm volatile("v_mul_f32 %0, %0, %1" : "+v"(pr[1][3]) : "v"(a1[1][0][3]) : ); SB();
  asm volatile("v_mfma_f32_16x16x32_bf16 %0, v[192:195], a[36:39], %0" : "+v"(g[0][1][0]) :  : ); SB();
  asm volatile("v_cvt_pk_bf16_f32 %0, %1, %2" : "=v"(ho[1][0]) : "v"(pr[0][0]), "v"(pr[0][1]) : ); SB();
  asm volatile("v_mfma_f32_16x16x32_bf16 %0, v[192:195], a[40:43], %0" : "+v"(g[0][2][0]) :  : ); SB();
  asm volatile("buffer_load_dwordx4 a[176:179], %0, %1, %2 offen offset:0 sc1" :  : "v"(vO), "s"(rWS), "s"(4096u) : "memory"); SB();
  asm volatile("v_cvt_pk_bf16_f32 %0, %1, %2" : "=v"(ho[1][1]) : "v"(pr[0][2]), "v"(pr[0][3]) : ); SB();
  asm volatile("v_mfma_f32_16x16x32_bf16 %0, v[192:195], a[44:47], %0" : "+v"(g[0][3][0]) :  : ); SB();
  asm volatile("v_cvt_pk_bf16_f32 %0, %1, %2" : "=v"(ho[1][2]) : "v"(pr[1][0]), "v"(pr[1][1]) : ); SB();
  asm volatile("v_mfma_f32_16x16x32_bf16 %0, v[196:199], a[32:35], %0" : "+v"(g[0][0][1]) :  : ); SB();
  asm volatile("v_cvt_pk_bf16_f32 %0, %1, %2" : "=v"(ho[1][3]) : "v"(pr[1][2]), "v"(pr[1][3]) : ); SB();
  asm volatile("v_mfma_f32_16x16x32_bf16 %0, v[196:199], a[36:39], %0" : "+v"(g[0][1][1]) :  : ); SB();
  asm volatile("v_mfma_f32_16x16x32_bf16 %0, v[196:199], a[40:43], %0" : "+v"(g[0][2][1]) :  : ); SB();
  asm volatile("buffer_store_dwordx4 %1, %0, %2, %3 offen offset:1024 sc1\n\ts_nop 1" :  : "v"(vH_), "v"(ho[1]), "s"(rWS), "s"(0u) : "memory"); SB();
  asm volatile("v_mfma_f32_16x16x32_bf16 %0, v[196:199], a[44:47], %0" : "+v"(g[0][3][1]) :  : ); SB();
  asm volatile("s_waitcnt vmcnt(20)" :  :  : "memory"); SB();
  asm volatile("v_mfma_f32_16x16x32_bf16 %0, v[200:203], a[48:51], %0" : "+v"(g[0][0][0]) :  : ); SB();
  asm volatile("v_mfma_f32_16x16x32_bf16 %0, v[200:203], a[52:55], %0" : "+v"(g[0][1][0]) :  : ); SB();
  asm volatile("v_mfma_f32_16x16x32_bf16 %0, v[200:203], a[56:59], %0" : "+v"(g[0][2][0]) :  : ); SB();
  asm volatile("buffer_load_dwordx4 a[180:183], %0, %1, %2 offen offset:1024 sc1" :  : "v"(vO), "s"(rWS), "s"(4096u) : "memory"); SB();
  asm volatile("v_mfma_f32_16x16x32_bf16 %0, v[200:203], a[60:63], %0" : "+v"(g[0][3][0]) :  : ); SB();
  asm volatile("v_mfma_f32_16x16x32_bf16 %0, v[204:207], a[48:51], %0" : "+v"(g[0][0][1]) :  : ); SB();
  asm volatile("v_mfma_f32_16x16x32_bf16 %0, v[204:207], a[52:55], %0" : "+v"(g[0][1][1]) :  : ); SB();
  asm volatile("buffer_load_dwordx4 a[184:187], %0, %1, %2 offen offset:2048 sc1" :  : "v"(vO), "s"(rWS), "s"(4096u) : "memory"); SB();
  asm volatile("v_mfma_f32_16x16x32_bf16 %0, v[204:207], a[56:59], %0" : "+v"(g[0][2][1]) :  : ); SB();
  asm volatile("v_mfma_f32_16x16x32_bf16 %0, v[204:207], a[60:63], %0" : "+v"(g[0][3][1]) :  : ); SB();
  asm volatile("s_waitcnt vmcnt(20)" :  :  : "memory"); SB();
  asm volatile("v_mfma_f32_16x16x32_bf16 %0, v[208:211], a[64:67], %0" : "+v"(g[0][0][0]) :  : ); SB();
  asm volatile("ds_read_b128 %0, %1 offset:0" : "=&v"(yq[0]) : "v"(ydr) : "memory"); SB();
  asm volatile("v_mfma_f32_16x16x32_bf16 %0, v[208:211], a[68:71], %0" : "+v"(g[0][1][0]) :  : ); SB();
  asm volatile("ds_read_b128 %0, %1 offset:256" : "=&v"(yq[1]) : "v"(ydr) : "memory"); SB();
  asm volatile("v_mfma_f32_16x16x32_bf16 %0, v[208:211], a[72:75], %0" : "+v"(g[0][2][0]) :  : ); SB();
  asm volatile("ds_read_b128 %0, %1 offset:8192" : "=&v"(yq[2]) : "v"(ydr) : "memory"); SB();
  asm volatile("v_mfma_f32_16x16x32_bf16 %0, v[208:211], a[76:79], %0" : "+v"(g[0][3][0]) :  : ); SB();
  asm volatile("ds_read_b128 %0, %1 offset:8448" : "=&v"(yq[3]) : "v"(ydr) : "memory"); SB();
  asm volatile("v_mfma_f32_16x16x32_bf16 %0, v[212:215], a[64:67], %0" : "+v"(g[0][0][1]) :  : ); SB();
  asm volatile("ds_read_b128 %0, %1 offset:16384" : "=&v"(yq[4]) : "v"(ydr) : "memory"); SB();
  asm volatile("v_mfma_f32_16x16x32_bf16 %0, v[212:215], a[68:71], %0" : "+v"(g[0][1][1]) :  : ); SB();
  asm volatile("ds_read_b128 %0, %1 offset:16640" : "=&v"(yq[5]) : "v"(ydr) : "memory"); SB();
  asm volatile("v_mfma_f32_16x16x32_bf16 %0, v[212:215], a[72:75], %0" : "+v"(g[0][2][1]) :  : ); SB();
  asm volatile("ds_read_b128 %0, %1 offset:24576" : "=&v"(yq[6]) : "v"(ydr) : "memory"); SB();
  asm volatile("v_mfma_f32_16x16x32_bf16 %0, v[212:215], a[76:79], %0" : "+v"(g[0][3][1]) :  : ); SB();
  asm volatile("ds_read_b128 %0, %1 offset:24832" : "=&v"(yq[7]) : "v"(ydr) : "memory"); SB();
  asm volatile("s_waitcnt vmcnt(18)" :  :  : "memory"); SB();
  asm volatile("v_mfma_f32_16x16x32_bf16 %0, v[216:219], a[80:83], %0" : "+v"(g[0][0][0]) :  : ); SB();
  asm volatile("ds_read_b128 %0, %1 offset:25856" : "=&v"(ykv) : "v"(ykr) : "memory"); SB();
  asm volatile("v_mfma_f32_16x16x32_bf16 %0, v[216:219], a[84:87], %0" : "+v"(g[0][1][0]) :  : ); SB();
  asm volatile("buffer_load_dwordx4 a[188:191], %0, %1, %2 offen offset:3072 sc1" :  : "v"(vO), "s"(rWS), "s"(4096u) : "memory"); SB();
  asm volatile("v_mfma_f32_16x16x32_bf16 %0, v[216:219], a[88:91], %0" : "+v"(g[0][2][0]) :  : ); SB();
  asm volatile("buffer_load_dwordx4 a[192:195], %0, %1, %2 offen offset:0 sc1" :  : "v"(vO), "s"(rWS), "s"(8192u) : "memory"); SB();
  asm volatile("v_mfma_f32_16x16x32_bf16 %0, v[216:219], a[92:95], %0" : "+v"(g[0][3][0]) :  : ); SB();
  asm volatile("v_mfma_f32_16x16x32_bf16 %0, v[220:223], a[80:83], %0" : "+v"(g[0][0][1]) :  : ); SB();
  asm volatile("v_mfma_f32_16x16x32_bf16 %0, v[220:223], a[84:87], %0" : "+v"(g[0][1][1]) :  : ); SB();
  asm volatile("buffer_load_dwordx4 a[196:199], %0, %1, %2 offen offset:1024 sc1" :  : "v"(vO), "s"(rWS), "s"(8192u) : "memory"); SB();
  asm volatile("v_mfma_f32_16x16x32_bf16 %0, v[220:223], a[88:91], %0" : "+v"(g[0][2][1]) :  : ); SB();
  asm volatile("v_mfma_f32_16x16x32_bf16 %0, v[220:223], a[92:95], %0" : "+v"(g[0][3][1]) :  : ); SB();
  asm volatile("s_waitcnt vmcnt(13)\n\ts_barrier" :  :  : "memory"); SB();
  asm volatile("s_waitcnt vmcnt(11)" :  :  : "memory"); SB();
  asm volatile("v_mfma_f32_16x16x32_bf16 %0, v[224:227], a[100:103], %0" : "+v"(g[0][1][0]) :  : ); SB();
  asm volatile("ds_read2_b32 a[64:65], %0 offset0:176 offset1:177" :  : "v"(aa[0]) : "memory"); SB();
  asm volatile("v_mfma_f32_16x16x32_bf16 %0, v[228:231], a[100:103], %0" : "+v"(g[0][1][1]) :  : ); SB();
  asm volatile("ds_read2_b32 a[66:67], %0 offset0:178 offset1:179" :  : "v"(aa[0]) : "memory"); SB();
  asm volatile("v_mfma_f32_16x16x32_bf16 %0, v[232:235], a[116:119], %0" : "+v"(g[0][1][0]) :  : ); SB();
  asm volatile("ds_read_b128 a[60:63], %0 offset:3776" :  : "v"(bb) : "memory"); SB();
  asm volatile("v_mfma_f32_16x16x32_bf16 %0, v[236:239], a[116:119], %0" : "+v"(g[0][1][1]) :  : ); SB();
  asm volatile("ds_read_b128 a[56:59], %0 offset:3264" :  : "v"(bb) : "memory"); SB();
  asm volatile("v_mfma_f32_16x16x32_bf16 %0, v[240:243], a[132:135], %0" : "+v"(g[0][1][0]) :  : ); SB();
  asm volatile("buffer_load_dwordx4 a[200:203], %0, %1, %2 offen offset:0 sc1" :  : "v"(vO2), "s"(rWS), "s"(0u) : "memory"); SB();
  asm volatile("v_mfma_f32_16x16x32_bf16 %0, v[244:247], a[132:135], %0" : "+v"(g[0][1][1]) :  : ); SB();
  asm volatile("ds_read_b128 a[52:55], %0 offset:2752" :  : "v"(bb) : "memory"); SB();
  asm volatile("v_mfma_f32_16x16x32_bf16 %0, v[248:251], a[148:151], %0" : "+v"(g[0][1][0]) :  : ); SB();
  asm volatile("ds_read_b128 a[48:51], %0 offset:2240" :  : "v"(bb) : "memory"); SB();
  asm volatile("v_mfma_f32_16x16x32_bf16 %0, v[252:255], a[148:151], %0" : "+v"(g[0][1][1]) :  : ); SB();
  asm volatile("buffer_load_dwordx4 a[204:207], %0, %1, %2 offen offset:1024 sc1" :  : "v"(vO2), "s"(rWS), "s"(0u) : "memory"); SB();
  asm volatile("v_mfma_f32_16x16x32_bf16 %0, v[224:227], a[104:107], %0" : "+v"(g[0][2][0]) :  : ); SB();
  asm volatile("ds_read2_b32 a[68:69], %0 offset0:48 offset1:49" :  : "v"(aa[0]) : "memory"); SB();
  asm volatile("v_mfma_f32_16x16x32_bf16 %0, v[228:231], a[104:107], %0" : "+v"(g[0][2][1]) :  : ); SB();
  asm volatile("ds_read2_b32 a[70:71], %0 offset0:50 offset1:51" :  : "v"(aa[0]) : "memory"); SB();
  asm volatile("v_mfma_f32_16x16x32_bf16 %0, v[232:235], a[120:123], %0" : "+v"(g[0][2][0]) :  : ); SB();
  asm volatile("ds_write_b128 %0, %1 offset:0" :  : "v"(pw2), "v"(g[0][1][0]) : "memory"); SB();
  asm volatile("v_mfma_f32_16x16x32_bf16 %0, v[236:239], a[120:123], %0" : "+v"(g[0][2][1]) :  : ); SB();
  asm volatile("buffer_load_dwordx4 a[208:211], %0, %1, %2 offen offset:2048 sc1" :  : "v"(vO2), "s"(rWS), "s"(0u) : "memory"); SB();
  asm volatile("v_mfma_f32_16x16x32_bf16 %0, v[240:243], a[136:139], %0" : "+v"(g[0][2][0]) :  : ); SB();
  asm volatile("ds_read_b128 a[44:47], %0 offset:1728" :  : "v"(bb) : "memory"); SB();
  asm volatile("v_mfma_f32_16x16x32_bf16 %0, v[244:247], a[136:139], %0" : "+v"(g[0][2][1]) :  : ); SB();
  asm volatile("buffer_load_dwordx4 a[212:215], %0, %1, %2 offen offset:3072 sc1" :  : "v"(vO2), "s"(rWS), "s"(0u) : "memory"); SB();
  asm volatile("v_mfma_f32_16x16x32_bf16 %0, v[248:251], a[152:155], %0" : "+v"(g[0][2][0]) :  : ); SB();
  asm volatile("ds_write_b128 %0, %1 offset:1024" :  : "v"(pw2), "v"(g[0][1][1]) : "memory"); SB();
  asm volatile("v_mfma_f32_16x16x32_bf16 %0, v[252:255], a[152:155], %0" : "+v"(g[0][2][1]) :  : ); SB();
  asm volatile("ds_read2_b32 a[72:73], %0 offset0:176 offset1:177" :  : "v"(aa[1]) : "memory"); SB();
  asm volatile("v_mfma_f32_16x16x32_bf16 %0, v[224:227], a[108:111], %0" : "+v"(g[0][3][0]) :  : ); SB();
  asm volatile("buffer_load_dwordx4 a[216:219], %0, %1, %2 offen offset:0 sc1" :  : "v"(vO2), "s"(rWS), "s"(4096u) : "memory"); SB();
  asm volatile("v_mfma_f32_16x16x32_bf16 %0, v[228:231], a[108:111], %0" : "+v"(g[0][3][1]) :  : ); SB();
  asm volatile("ds_read2_b32 a[74:75], %0 offset0:178 offset1:179" :  : "v"(aa[1]) : "memory"); SB();
  asm volatile("v_mfma_f32_16x16x32_bf16 %0, v[232:235], a[124:127], %0" : "+v"(g[0][3][0]) :  : ); SB();
  asm volatile("ds_write_b128 %0, %1 offset:2048" :  : "v"(pw2), "v"(g[0][2][0]) : "memory"); SB();
  asm volatile("v_mfma_f32_16x16x32_bf16 %0, v[236:239], a[124:127], %0" : "+v"(g[0][3][1]) :  : ); SB();
  asm volatile("ds_read2_b32 a[76:77], %0 offset0:48 offset1:49" :  : "v"(aa[1]) : "memory"); SB();
  asm volatile("v_mfma_f32_16x16x32_bf16 %0, v[240:243], a[140:143], %0" : "+v"(g[0][3][0]) :  : ); SB();
  asm volatile("ds_read2_b32 a[78:79], %0 offset0:50 offset1:51" :  : "v"(aa[1]) : "memory"); SB();
  asm volatile("v_mfma_f32_16x16x32_bf16 %0, v[244:247], a[140:143], %0" : "+v"(g[0][3][1]) :  : ); SB();
  asm volatile("ds_write_b128 %0, %1 offset:3072" :  : "v"(pw2), "v"(g[0][2][1]) : "memory"); SB();
  asm volatile("v_mfma_f32_16x16x32_bf16 %0, v[248:251], a[156:159], %0" : "+v"(g[0][3][0]) :  : ); SB();
  asm volatile("ds_read_b128 a[40:43], %0 offset:1216" :  : "v"(bb) : "memory"); SB();
  asm volatile("v_mfma_f32_16x16x32_bf16 %0, v[252:255], a[156:159], %0" : "+v"(g[0][3][1]) :  : ); SB();
  asm volatile("ds_read2_b32 a[80:81], %0 offset0:176 offset1:177" :  : "v"(aa[2]) : "memory"); SB();
  asm volatile("v_mfma_f32_16x16x32_bf16 %0, v[224:227], a[96:99], %0" : "+v"(g[0][0][0]) :  : ); SB();
  asm volatile("ds_read2_b32 a[82:83], %0 offset0:178 offset1:179" :  : "v"(aa[2]) : "memory"); SB();
  asm volatile("v_mfma_f32_16x16x32_bf16 %0, v[228:231], a[96:99], %0" : "+v"(g[0][0][1]) :  : ); SB();
  asm volatile("ds_read2_b32 a[84:85], %0 offset0:48 offset1:49" :  : "v"(aa[2]) : "memory"); SB();
  asm volatile("v_mfma_f32_16x16x32_bf16 %0, v[232:235], a[112:115], %0" : "+v"(g[0][0][0]) :  : ); SB();
  asm volatile("ds_write_b128 %0, %1 offset:4096" :  : "v"(pw2), "v"(g[0][3][0]) : "memory"); SB();
  asm volatile("v_mfma_f32_16x16x32_bf16 %0, v[236:239], a[112:115], %0" : "+v"(g[0][0][1]) :  : ); SB();
  asm volatile("ds_read2_b32 a[86:87], %0 offset0:50 offset1:51" :  : "v"(aa[2]) : "memory"); SB();
  asm volatile("v_mfma_f32_16x16x32_bf16 %0, v[240:243], a[128:131], %0" : "+v"(g[0][0][0]) :  : ); SB();
  asm volatile("ds_read_b128 a[36:39], %0 offset:704" :  : "v"(bb) : "memory"); SB();
  asm volatile("v_mfma_f32_16x16x32_bf16 %0, v[244:247], a[128:131], %0" : "+v"(g[0][0][1]) :  : ); SB();
  asm volatile("ds_write_b128 %0, %1 offset:5120" :  : "v"(pw2), "v"(g[0][3][1]) : "memory"); SB();
  asm volatile("v_mfma_f32_16x16x32_bf16 %0, v[248:251], a[144:147], %0" : "+v"(g[0][0][0]) :  : ); SB();
  asm volatile("ds_read2_b32 a[88:89], %0 offset0:176 offset1:177" :  : "v"(aa[3]) : "memory"); SB();
  asm volatile("v_mfma_f32_16x16x32_bf16 %0, v[252:255], a[144:147], %0" : "+v"(g[0][0][1]) :  : ); SB();
  asm volatile("s_waitcnt vmcnt(10)\n\ts_mov_b64 exec, 1\n\tbuffer_store_dwordx2 %0, off, %1, %2 sc1\n\ts_mov_b64 exec, -1" :  : "v"(((u32x2){tg0, tg1})), "s"(rWS), "s"(sFH) : "memory"); SB();
  asm volatile("buffer_load_dwordx4 a[220:223], %0, %1, %2 offen offset:1024 sc1" :  : "v"(vO2), "s"(rWS), "s"(4096u) : "memory"); SB();
  if ((rb | cb | w) == 0) asm volatile("buffer_store_dwordx2 %0, off, %1, 0 sc1" :: "v"(((u32x2){tg0, tg1})), "s"(rWS) : "memory");
  SB();
  ST(17);
  unsigned pw3, p3[3], yw, ys[2], vYr, ya, po;
  asm volatile("s_waitcnt lgkmcnt(5)\n\tv_mfma_f32_16x16x32_bf16 %0, a[64:67], a[60:63], 0" : "=&v"(ac[7]) :  : ); SB();
  asm volatile("ds_read2_b32 a[90:91], %0 offset0:178 offset1:179" :  : "v"(aa[3]) : "memory"); SB();
  asm volatile("" : "+v"(yq[0]), "+v"(yq[1]), "+v"(yq[2]), "+v"(yq[3]), "+v"(yq[4]), "+v"(yq[5]), "+v"(yq[6]), "+v"(yq[7]), "+v"(ykv) :  : "memory"); SB();
  asm volatile("v_mfma_f32_16x16x32_bf16 %0, a[64:67], a[56:59], 0" : "=&v"(ac[6]) :  : ); SB();
  asm volatile("ds_read2_b32 a[92:93], %0 offset0:48 offset1:49" :  : "v"(aa[3]) : "memory"); SB();
  asm volatile("v_add_f32 %0, %0, %1" : "+v"(yq[0][0]) : "v"(yq[2][0]) : ); SB();
  asm volatile("v_mfma_f32_16x16x32_bf16 %0, a[64:67], a[52:55], 0" : "=&v"(ac[5]) :  : ); SB();
  asm volatile("ds_read2_b32 a[94:95], %0 offset0:50 offset1:51" :  : "v"(aa[3]) : "memory"); SB();
  asm volatile("v_add_f32 %0, %0, %1" : "+v"(yq[0][1]) : "v"(yq[2][1]) : ); SB();
  asm volatile("v_mfma_f32_16x16x32_bf16 %0, a[64:67], a[48:51], 0" : "=&v"(ac[4]) :  : ); SB();
  asm volatile("ds_read_b128 a[32:35], %0 offset:192" :  : "v"(bb) : "memory"); SB();
  asm volatile("v_add_f32 %0, %0, %1" : "+v"(yq[0][2]) : "v"(yq[2][2]) : ); SB();
  asm volatile("v_mfma_f32_16x16x32_bf16 %0, a[68:71], a[56:59], %0" : "+v"(ac[7]) :  : ); SB();
  asm volatile("ds_read2_b32 a[128:129], %0 offset0:160 offset1:161" :  : "v"(aa[0]) : "memory"); SB();
  asm volatile("s_waitcnt lgkmcnt(6)\n\ts_barrier" :  :  : "memory"); SB();
  asm volatile("v_mfma_f32_16x16x32_bf16 %0, a[68:71], a[52:55], %0" : "+v"(ac[6]) :  : ); SB();
  asm volatile("ds_read2_b32 a[130:131], %0 offset0:162 offset1:163" :  : "v"(aa[0]) : "memory"); SB();
  asm volatile("buffer_load_dwordx4 a[224:227], %0, %1, %2 offen offset:2048 sc1" :  : "v"(vO2), "s"(rWS), "s"(4096u) : "memory"); SB();
  asm volatile("v_mfma_f32_16x16x32_bf16 %0, a[68:71], a[48:51], %0" : "+v"(ac[5]) :  : ); SB();
  asm volatile("ds_read_b128 a[124:127], %0 offset:3712" :  : "v"(bb) : "memory"); SB();
  asm volatile("v_add_f32 %0, %0, %1" : "+v"(yq[0][3]) : "v"(yq[2][3]) : ); SB();
  asm volatile("v_mfma_f32_16x16x32_bf16 %0, a[68:71], a[44:47], %0" : "+v"(ac[4]) :  : ); SB();
  asm volatile("ds_read_b128 %0, %1 offset:0" : "=&v"(pr[0]) : "v"(pe[0]) : "memory"); SB();
  asm volatile("v_add_f32 %0, %0, %1" : "+v"(yq[1][0]) : "v"(yq[3][0]) : ); SB();
  asm volatile("v_mfma_f32_16x16x32_bf16 %0, a[72:75], a[52:55], %0" : "+v"(ac[7]) :  : ); SB();
  asm volatile("ds_read_b128 a[120:123], %0 offset:3200" :  : "v"(bb) : "memory"); SB();
  asm volatile("buffer_load_dwordx4 a[228:231], %0, %1, %2 offen offset:3072 sc1" :  : "v"(vO2), "s"(rWS), "s"(4096u) : "memory"); SB();
  asm volatile("v_mfma_f32_16x16x32_bf16 %0, a[72:75], a[48:51], %0" : "+v"(ac[6]) :  : ); SB();
  asm volatile("ds_read_b128 a[116:119], %0 offset:2688" :  : "v"(bb) : "memory"); SB();
  asm volatile("v_add_f32 %0, %0, %1" : "+v"(yq[1][1]) : "v"(yq[3][1]) : ); SB();
  asm volatile("v_mfma_f32_16x16x32_bf16 %0, a[72:75], a[44:47], %0" : "+v"(ac[5]) :  : ); SB();
  asm volatile("ds_read_b128 a[112:115], %0 offset:2176" :  : "v"(bb) : "memory"); SB();
  asm volatile("v_add_f32 %0, %0, %1" : "+v"(yq[1][2]) : "v"(yq[3][2]) : ); SB();
  asm volatile("v_mfma_f32_16x16x32_bf16 %0, a[64:67], a[44:47], 0" : "=&v"(ac[3]) :  : ); SB();
  asm volatile("ds_read_b128 %0, %1 offset:1024" : "=&v"(pr[1]) : "v"(pe[0]) : "memory"); SB();
  asm volatile("v_add_f32 %0, %0, %1" : "+v"(yq[1][3]) : "v"(yq[3][3]) : ); SB();
  asm volatile("v_mfma_f32_16x16x32_bf16 %0, a[76:79], a[48:51], %0" : "+v"(ac[7]) :  : ); SB();
  asm volatile("ds_read2_b32 a[132:133], %0 offset0:32 offset1:33" :  : "v"(aa[0]) : "memory"); SB();
  asm volatile("buffer_load_dwordx4 a[232:235], %0, %1, %2 offen offset:0 sc1" :  : "v"(vO2), "s"(rWS), "s"(8192u) : "memory"); SB();
  asm volatile("v_mfma_f32_16x16x32_bf16 %0, a[76:79], a[44:47], %0" : "+v"(ac[6]) :  : ); SB();
  asm volatile("ds_read2_b32 a[134:135], %0 offset0:34 offset1:35" :  : "v"(aa[0]) : "memory"); SB();
  asm volatile("v_add_f32 %0, %0, %1" : "+v"(yq[4][0]) : "v"(yq[6][0]) : ); SB();
  asm volatile("v_mfma_f32_16x16x32_bf16 %0, a[76:79], a[40:43], %0" : "+v"(ac[5]) :  : ); SB();
  asm volatile("ds_read_b128 a[108:111], %0 offset:1664" :  : "v"(bb) : "memory"); SB();
  asm volatile("v_add_f32 %0, %0, %1" : "+v"(yq[4][1]) : "v"(yq[6][1]) : ); SB();
  asm volatile("v_mfma_f32_16x16x32_bf16 %0, a[72:75], a[40:43], %0" : "+v"(ac[4]) :  : ); SB();
  asm volatile("ds_read_b128 %0, %1 offset:2048" : "=&v"(pr[2]) : "v"(pe[1]) : "memory"); SB();
  asm volatile("buffer_load_dwordx4 a[236:239], %0, %1, %2 offen offset:1024 sc1" :  : "v"(vO2), "s"(rWS), "s"(8192u) : "memory"); SB();
  asm volatile("v_mfma_f32_16x16x32_bf16 %0, a[80:83], a[44:47], %0" : "+v"(ac[7]) :  : ); SB();
  asm volatile("ds_read2_b32 a[136:137], %0 offset0:160 offset1:161" :  : "v"(aa[1]) : "memory"); SB();
  asm volatile("v_add_f32 %0, %0, %1" : "+v"(yq[4][2]) : "v"(yq[6][2]) : ); SB();
  asm volatile("v_mfma_f32_16x16x32_bf16 %0, a[80:83], a[40:43], %0" : "+v"(ac[6]) :  : ); SB();
  asm volatile("ds_read2_b32 a[138:139], %0 offset0:162 offset1:163" :  : "v"(aa[1]) : "memory"); SB();
  asm volatile("v_add_f32 %0, %0, %1" : "+v"(yq[4][3]) : "v"(yq[6][3]) : ); SB();
  asm volatile("v_mfma_f32_16x16x32_bf16 %0, a[68:71], a[40:43], %0" : "+v"(ac[3]) :  : ); SB();
  asm volatile("ds_read2_b32 a[140:141], %0 offset0:32 offset1:33" :  : "v"(aa[1]) : "memory"); SB();
  asm volatile("buffer_load_dwordx4 a[240:243], %0, %1, %2 offen offset:2048 sc1" :  : "v"(vO), "s"(rWS), "s"(8192u) : "memory"); SB();
  asm volatile("v_mfma_f32_16x16x32_bf16 %0, a[64:67], a[40:43], 0" : "=&v"(ac[2]) :  : ); SB();
  asm volatile("ds_read_b128 %0, %1 offset:3072" : "=&v"(pr[3]) : "v"(pe[1]) : "memory"); SB();
  asm volatile("v_add_f32 %0, %0, %1" : "+v"(yq[5][0]) : "v"(yq[7][0]) : ); SB();
  asm volatile("v_mfma_f32_16x16x32_bf16 %0, a[84:87], a[40:43], %0" : "+v"(ac[7]) :  : ); SB();
  asm volatile("ds_read2_b32 a[142:143], %0 offset0:34 offset1:35" :  : "v"(aa[1]) : "memory"); SB();
  asm volatile("v_add_f32 %0, %0, %1" : "+v"(yq[5][1]) : "v"(yq[7][1]) : ); SB();
  asm volatile("v_mfma_f32_16x16x32_bf16 %0, a[84:87], a[36:39], %0" : "+v"(ac[6]) :  : ); SB();
  asm volatile("ds_read_b128 a[104:107], %0 offset:1152" :  : "v"(bb) : "memory"); SB();
  asm volatile("buffer_load_dwordx4 a[244:247], %0, %1, %2 offen offset:3072 sc1" :  : "v"(vO), "s"(rWS), "s"(8192u) : "memory"); SB();
  asm volatile("v_mfma_f32_16x16x32_bf16 %0, a[80:83], a[36:39], %0" : "+v"(ac[5]) :  : ); SB();
  asm volatile("ds_read2_b32 a[144:145], %0 offset0:160 offset1:161" :  : "v"(aa[2]) : "memory"); SB();
  asm volatile("v_add_f32 %0, %0, %1" : "+v"(yq[5][2]) : "v"(yq[7][2]) : ); SB();
  asm volatile("v_mfma_f32_16x16x32_bf16 %0, a[76:79], a[36:39], %0" : "+v"(ac[4]) :  : ); SB();
  asm volatile("ds_read_b128 %0, %1 offset:4096" : "=&v"(pr[4]) : "v"(pe[2]) : "memory"); SB();
  asm volatile("s_waitcnt lgkmcnt(8)\n\tv_mfma_f32_16x16x32_bf16 %0, a[88:91], a[36:39], %0" : "+v"(ac[7]) :  : ); SB();
  asm volatile("ds_read2_b32 a[146:147], %0 offset0:162 offset1:163" :  : "v"(aa[2]) : "memory"); SB();
  asm volatile("buffer_load_dwordx4 a[248:251], %0, %1, %2 offen offset:0 sc1" :  : "v"(vO), "s"(rWS), "s"(12288u) : "memory"); SB();
  asm volatile("v_mfma_f32_16x16x32_bf16 %0, a[72:75], a[36:39], %0" : "+v"(ac[3]) :  : ); SB();
  asm volatile("ds_read2_b32 a[148:149], %0 offset0:32 offset1:33" :  : "v"(aa[2]) : "memory"); SB();
  asm volatile("v_add_f32 %0, %0, %1" : "+v"(yq[5][3]) : "v"(yq[7][3]) : ); SB();
  asm volatile("v_mfma_f32_16x16x32_bf16 %0, a[68:71], a[36:39], %0" : "+v"(ac[2]) :  : ); SB();
  asm volatile("ds_read2_b32 a[150:151], %0 offset0:34 offset1:35" :  : "v"(aa[2]) : "memory"); SB();
  asm volatile("v_add_f32 %0, %0, %1" : "+v"(yq[0][0]) : "v"(yq[4][0]) : ); SB();
  asm volatile("v_mfma_f32_16x16x32_bf16 %0, a[64:67], a[36:39], 0" : "=&v"(ac[1]) :  : ); SB();
  asm volatile("ds_read_b128 %0, %1 offset:5120" : "=&v"(pr[5]) : "v"(pe[2]) : "memory"); SB();
  asm volatile("v_add_f32 %0, %0, %1" : "+v"(yq[0][1]) : "v"(yq[4][1]) : ); SB();
  asm volatile("v_mfma_f32_16x16x32_bf16 %0, a[92:95], a[32:35], %0" : "+v"(ac[7]) :  : ); SB();
  asm volatile("ds_read_b128 a[100:103], %0 offset:640" :  : "v"(bb) : "memory"); SB();
  asm volatile("buffer_load_dwordx4 a[252:255], %0, %1, %2 offen offset:1024 sc1" :  : "v"(vO), "s"(rWS), "s"(12288u) : "memory"); SB();
  asm volatile("v_mfma_f32_16x16x32_bf16 %0, a[88:91], a[32:35], %0" : "+v"(ac[6]) :  : ); SB();
  asm volatile("ds_read2_b32 a[152:153], %0 offset0:160 offset1:161" :  : "v"(aa[3]) : "memory"); SB();
  asm volatile("v_add_f32 %0, %0, %1" : "+v"(yq[0][2]) : "v"(yq[4][2]) : ); SB();
  asm volatile("v_mfma_f32_16x16x32_bf16 %0, a[84:87], a[32:35], %0" : "+v"(ac[5]) :  : ); SB();
  asm volatile("ds_read2_b32 a[154:155], %0 offset0:162 offset1:163" :  : "v"(aa[3]) : "memory"); SB();
  asm volatile("v_add_f32 %0, %0, %1" : "+v"(yq[0][3]) : "v"(yq[4][3]) : ); SB();
  asm volatile("v_mfma_f32_16x16x32_bf16 %0, a[80:83], a[32:35], %0" : "+v"(ac[4]) :  : ); SB();
  pw3 = lb + 98304 + w * 15360 + l16; asm volatile("" : "+v"(pw3)); SB();
  asm volatile("v_mfma_f32_16x16x32_bf16 %0, a[76:79], a[32:35], %0" : "+v"(ac[3]) :  : ); SB();
  asm volatile("ds_read2_b32 a[156:157], %0 offset0:32 offset1:33" :  : "v"(aa[3]) : "memory"); SB();
  asm volatile("v_add_f32 %0, %0, %1" : "+v"(yq[1][0]) : "v"(yq[5][0]) : ); SB();
  asm volatile("v_mfma_f32_16x16x32_bf16 %0, a[72:75], a[32:35], %0" : "+v"(ac[2]) :  : ); SB();
  asm volatile("ds_read2_b32 a[158:159], %0 offset0:34 offset1:35" :  : "v"(aa[3]) : "memory"); SB();
  asm volatile("s_waitcnt lgkmcnt(5)" : "+v"(pr[0]), "+v"(pr[1]), "+v"(pr[2]), "+v"(pr[3]), "+v"(pr[4]), "+v"(pr[5]) :  : "memory"); SB();
  asm volatile("v_mfma_f32_16x16x32_bf16 %0, a[68:71], a[32:35], %0" : "+v"(ac[1]) :  : ); SB();
  asm volatile("ds_read_b128 a[96:99], %0 offset:128" :  : "v"(bb) : "memory"); SB();
  asm volatile("v_add_f32 %0, %0, %1" : "+v"(yq[1][1]) : "v"(yq[5][1]) : ); SB();
  asm volatile("v_mfma_f32_16x16x32_bf16 %0, a[64:67], a[32:35], 0" : "=&v"(ac[0]) :  : ); SB();
  {
    const f32x4 h0 = ((g[0][0][0] + pr[0]) + (pr[2] + pr[4])) * a1[1][1], h1 = ((g[0][0][1] + pr[1]) + (pr[3] + pr[5])) * a1[1][2];
    ho[2] = (u32x4){pack2(h0[0], h0[1]), pack2(h0[2], h0[3]), pack2(h1[0], h1[1]), nohole(pack2(h1[2], h1[3]))};
    SB();
  }
  asm volatile("v_mfma_f32_16x16x32_bf16 %0, a[128:131], a[124:127], %0" : "+v"(ac[7]) :  : ); SB();
  asm volatile("ds_read2_b32 a[64:65], %0 offset0:144 offset1:145" :  : "v"(aa[0]) : "memory"); SB();
  asm volatile("v_add_f32 %0, %0, %1" : "+v"(yq[1][2]) : "v"(yq[5][2]) : ); SB();
  asm volatile("v_mfma_f32_16x16x32_bf16 %0, a[128:131], a[120:123], %0" : "+v"(ac[6]) :  : ); SB();
  asm volatile("ds_read2_b32 a[66:67], %0 offset0:146 offset1:147" :  : "v"(aa[0]) : "memory"); SB();
  asm volatile("v_add_f32 %0, %0, %1" : "+v"(yq[1][3]) : "v"(yq[5][3]) : ); SB();
  asm volatile("v_mfma_f32_16x16x32_bf16 %0, a[128:131], a[116:119], %0" : "+v"(ac[5]) :  : ); SB();
  asm volatile("ds_read_b128 a[60:63], %0 offset:3648" :  : "v"(bb) : "memory"); SB();
  asm volatile("buffer_load_dwordx4 a[0:3], %0, %1, %2 offen offset:2048 sc1" :  : "v"(vO), "s"(rWS), "s"(12288u) : "memory"); SB();
  asm volatile("v_mfma_f32_16x16x32_bf16 %0, a[128:131], a[112:115], %0" : "+v"(ac[4]) :  : ); SB();
  asm volatile("buffer_store_dwordx4 %1, %0, %2, %3 offen offset:2048 sc1\n\ts_nop 1" :  : "v"(vH_), "v"(ho[2]), "s"(rWS), "s"(0u) : "memory"); SB();
  asm volatile("v_mfma_f32_16x16x32_bf16 %0, a[132:135], a[120:123], %0" : "+v"(ac[7]) :  : ); SB();
  asm volatile("ds_read_b128 a[56:59], %0 offset:3136" :  : "v"(bb) : "memory"); SB();
  asm volatile("v_lshlrev_b32 %0, 16, %1" : "=v"(yq[2][0]) : "v"(ykv[0]) : ); SB();
  asm volatile("v_mfma_f32_16x16x32_bf16 %0, a[132:135], a[116:119], %0" : "+v"(ac[6]) :  : ); SB();
  asm volatile("ds_read_b128 a[52:55], %0 offset:2624" :  : "v"(bb) : "memory"); SB();
  asm volatile("v_and_b32 %0, 0xffff0000, %1" : "=v"(yq[2][1]) : "v"(ykv[0]) : ); SB();
  asm volatile("v_mfma_f32_16x16x32_bf16 %0, a[132:135], a[112:115], %0" : "+v"(ac[5]) :  : ); SB();
  asm volatile("ds_read_b128 a[48:51], %0 offset:2112" :  : "v"(bb) : "memory"); SB();
  asm volatile("v_lshlrev_b32 %0, 16, %1" : "=v"(yq[2][2]) : "v"(ykv[1]) : ); SB();
  asm volatile("v_mfma_f32_16x16x32_bf16 %0, a[132:135], a[108:111], %0" : "+v"(ac[4]) :  : ); SB();
  p3[0] = lb + 98304 + (w ^ 1) * 15360 + l16; asm volatile("" : "+v"(p3[0])); SB();
  asm volatile("v_mfma_f32_16x16x32_bf16 %0, a[136:139], a[116:119], %0" : "+v"(ac[7]) :  : ); SB();
  asm volatile("ds_read2_b32 a[68:69], %0 offset0:16 offset1:17" :  : "v"(aa[0]) : "memory"); SB();
  asm volatile("v_and_b32 %0, 0xffff0000, %1" : "=v"(yq[2][3]) : "v"(ykv[1]) : ); SB();
  asm volatile("v_mfma_f32_16x16x32_bf16 %0, a[136:139], a[112:115], %0" : "+v"(ac[6]) :  : ); SB();
  asm volatile("ds_read2_b32 a[70:71], %0 offset0:18 offset1:19" :  : "v"(aa[0]) : "memory"); SB();
  asm volatile("v_lshlrev_b32 %0, 16, %1" : "=v"(yq[3][0]) : "v"(ykv[2]) : ); SB();
  asm volatile("v_mfma_f32_16x16x32_bf16 %0, a[136:139], a[108:111], %0" : "+v"(ac[5]) :  : ); SB();
  asm volatile("ds_read_b128 a[44:47], %0 offset:1600" :  : "v"(bb) : "memory"); SB();
  asm volatile("v_and_b32 %0, 0xffff0000, %1" : "=v"(yq[3][1]) : "v"(ykv[2]) : ); SB();
  asm volatile("v_mfma_f32_16x16x32_bf16 %0, a[128:131], a[108:111], %0" : "+v"(ac[3]) :  : ); SB();
  p3[1] = lb + 98304 + (w ^ 2) * 15360 + l16; asm volatile("" : "+v"(p3[1])); SB();
  asm volatile("v_mfma_f32_16x16x32_bf16 %0, a[140:143], a[112:115], %0" : "+v"(ac[7]) :  : ); SB();
  asm volatile("ds_read2_b32 a[72:73], %0 offset0:144 offset1:145" :  : "v"(aa[1]) : "memory"); SB();
  asm volatile("buffer_load_dwordx4 a[4:7], %0, %1, %2 offen offset:2048 sc1" :  : "v"(vO2), "s"(rWS), "s"(8192u) : "memory"); SB();
  asm volatile("v_mfma_f32_16x16x32_bf16 %0, a[140:143], a[108:111], %0" : "+v"(ac[6]) :  : ); SB();
  asm volatile("ds_read2_b32 a[74:75], %0 offset0:146 offset1:147" :  : "v"(aa[1]) : "memory"); SB();
  asm volatile("v_lshlrev_b32 %0, 16, %1" : "=v"(yq[3][2]) : "v"(ykv[3]) : ); SB();
  asm volatile("v_mfma_f32_16x16x32_bf16 %0, a[140:143], a[104:107], %0" : "+v"(ac[5]) :  : ); SB();
  asm volatile("ds_read2_b32 a[76:77], %0 offset0:16 offset1:17" :  : "v"(aa[1]) : "memory"); SB();
  asm volatile("v_and_b32 %0, 0xffff0000, %1" : "=v"(yq[3][3]) : "v"(ykv[3]) : ); SB();
  asm volatile("v_mfma_f32_16x16x32_bf16 %0, a[136:139], a[104:107], %0" : "+v"(ac[4]) :  : ); SB();
  p3[2] = lb + 98304 + (w ^ 3) * 15360 + l16; asm volatile("" : "+v"(p3[2])); SB();
  asm volatile("v_mfma_f32_16x16x32_bf16 %0, a[144:147], a[108:111], %0" : "+v"(ac[7]) :  : ); SB();
  asm volatile("ds_read2_b32 a[78:79], %0 offset0:18 offset1:19" :  : "v"(aa[1]) : "memory"); SB();
  asm volatile("buffer_load_dwordx4 a[8:11], %0, %1, %2 offen offset:3072 sc1" :  : "v"(vO2), "s"(rWS), "s"(8192u) : "memory"); SB();
  asm volatile("v_mfma_f32_16x16x32_bf16 %0, a[144:147], a[104:107], %0" : "+v"(ac[6]) :  : ); SB();
  asm volatile("ds_read_b128 a[40:43], %0 offset:1088" :  : "v"(bb) : "memory"); SB();
  asm volatile("v_fmac_f32 %0, %1, %2" : "+v"(yq[0][0]) : "v"(yq[2][0]), "v"(dD[2]) : ); SB();
  asm volatile("v_mfma_f32_16x16x32_bf16 %0, a[132:135], a[104:107], %0" : "+v"(ac[3]) :  : ); SB();
  asm volatile("ds_read2_b32 a[80:81], %0 offset0:144 offset1:145" :  : "v"(aa[2]) : "memory"); SB();
  asm volatile("v_fmac_f32 %0, %1, %2" : "+v"(yq[0][1]) : "v"(yq[2][1]), "v"(dD[2]) : ); SB();
  asm volatile("v_mfma_f32_16x16x32_bf16 %0, a[128:131], a[104:107], %0" : "+v"(ac[2]) :  : ); SB();
  yw = (lane ^ ((lane >> 3) & 6)) * 16; asm volatile("" : "+v"(yw)); SB();
  asm volatile("v_mfma_f32_16x16x32_bf16 %0, a[148:151], a[104:107], %0" : "+v"(ac[7]) :  : ); SB();
  asm volatile("ds_read2_b32 a[82:83], %0 offset0:146 offset1:147" :  : "v"(aa[2]) : "memory"); SB();
  asm volatile("buffer_load_dwordx4 a[12:15], %0, %1, %2 offen offset:0 sc1" :  : "v"(vO2), "s"(rWS), "s"(12288u) : "memory"); SB();
  asm volatile("s_waitcnt lgkmcnt(6)\n\tv_mfma_f32_16x16x32_bf16 %0, a[148:151], a[100:103], %0" : "+v"(ac[6]) :  : ); SB();
  asm volatile("ds_read2_b32 a[84:85], %0 offset0:16 offset1:17" :  : "v"(aa[2]) : "memory"); SB();
  asm volatile("v_fmac_f32 %0, %1, %2" : "+v"(yq[0][2]) : "v"(yq[2][2]), "v"(dD[2]) : ); SB();
  asm volatile("v_mfma_f32_16x16x32_bf16 %0, a[144:147], a[100:103], %0" : "+v"(ac[5]) :  : ); SB();
  asm volatile("ds_read2_b32 a[86:87], %0 offset0:18 offset1:19" :  : "v"(aa[2]) : "memory"); SB();
  asm volatile("buffer_load_dwordx4 a[16:19], %0, %1, %2 offen offset:1024 sc1" :  : "v"(vO2), "s"(rWS), "s"(12288u) : "memory"); SB();
  asm volatile("v_mfma_f32_16x16x32_bf16 %0, a[140:143], a[100:103], %0" : "+v"(ac[4]) :  : ); SB();
  yw = yw + lb + 32768 + w * 3072; asm volatile("" : "+v"(yw)); SB();
  asm volatile("v_mfma_f32_16x16x32_bf16 %0, a[152:155], a[100:103], %0" : "+v"(ac[7]) :  : ); SB();
  asm volatile("ds_read_b128 a[36:39], %0 offset:576" :  : "v"(bb) : "memory"); SB();
  asm volatile("v_fmac_f32 %0, %1, %2" : "+v"(yq[0][3]) : "v"(yq[2][3]), "v"(dD[2]) : ); SB();
  asm volatile("v_mfma_f32_16x16x32_bf16 %0, a[136:139], a[100:103], %0" : "+v"(ac[3]) :  : ); SB();
  asm volatile("ds_read2_b32 a[88:89], %0 offset0:144 offset1:145" :  : "v"(aa[3]) : "memory"); SB();
  asm volatile("buffer_load_dwordx4 a[20:23], %0, %1, %2 offen offset:2048 sc1" :  : "v"(vO2), "s"(rWS), "s"(12288u) : "memory"); SB();
  asm volatile("v_mfma_f32_16x16x32_bf16 %0, a[132:135], a[100:103], %0" : "+v"(ac[2]) :  : ); SB();
  asm volatile("ds_read2_b32 a[90:91], %0 offset0:146 offset1:147" :  : "v"(aa[3]) : "memory"); SB();
  asm volatile("v_fmac_f32 %0, %1, %2" : "+v"(yq[1][0]) : "v"(yq[3][0]), "v"(dD[2]) : ); SB();
  asm volatile("v_mfma_f32_16x16x32_bf16 %0, a[128:131], a[100:103], %0" : "+v"(ac[1]) :  : ); SB();
  ya = lb + 32768 + (4 * g4 + q4) * 128 + 8 * p4; asm volatile("" : "+v"(ya)); SB();
  asm volatile("v_mfma_f32_16x16x32_bf16 %0, a[156:159], a[96:99], %0" : "+v"(ac[7]) :  : ); SB();
  asm volatile("ds_read2_b32 a[92:93], %0 offset0:16 offset1:17" :  : "v"(aa[3]) : "memory"); SB();
  asm volatile("v_fmac_f32 %0, %1, %2" : "+v"(yq[1][1]) : "v"(yq[3][1]), "v"(dD[2]) : ); SB();
  asm volatile("v_mfma_f32_16x16x32_bf16 %0, a[152:155], a[96:99], %0" : "+v"(ac[6]) :  : ); SB();
  asm volatile("ds_read2_b32 a[94:95], %0 offset0:18 offset1:19" :  : "v"(aa[3]) : "memory"); SB();
  asm volatile("buffer_load_dwordx2 %0, %1, %2, %3 offen sc1" : "=&v"(fh) : "v"(vFH), "s"(rWS), "s"(0u) : "memory"); SB();
  asm volatile("v_mfma_f32_16x16x32_bf16 %0, a[148:151], a[96:99], %0" : "+v"(ac[5]) :  : ); SB();
  asm volatile("ds_read_b128 a[32:35], %0 offset:64" :  : "v"(bb) : "memory"); SB();
  asm volatile("v_fmac_f32 %0, %1, %2" : "+v"(yq[1][2]) : "v"(yq[3][2]), "v"(dD[2]) : ); SB();
  asm volatile("v_mfma_f32_16x16x32_bf16 %0, a[144:147], a[96:99], %0" : "+v"(ac[4]) :  : ); SB();
  po = (2 * g4 + (q4 >> 1)) & 3; asm volatile("" : "+v"(po)); SB();
  asm volatile("v_mfma_f32_16x16x32_bf16 %0, a[140:143], a[96:99], %0" : "+v"(ac[3]) :  : ); SB();
  ya = ya + ((w ^ po) << 5); asm volatile("" : "+v"(ya)); SB();
  asm volatile("v_mfma_f32_16x16x32_bf16 %0, a[136:139], a[96:99], %0" : "+v"(ac[2]) :  : ); SB();
  po = vX + 24 * (g4 & 1); asm volatile("" : "+v"(po)); SB();
  asm volatile("v_mfma_f32_16x16x32_bf16 %0, a[132:135], a[96:99], %0" : "+v"(ac[1]) :  : ); SB();
  asm volatile("v_fmac_f32 %0, %1, %2" : "+v"(yq[1][3]) : "v"(yq[3][3]), "v"(dD[2]) : ); SB();
  asm volatile("v_mfma_f32_16x16x32_bf16 %0, a[128:131], a[96:99], %0" : "+v"(ac[0]) :  : ); SB();
  asm volatile("v_cvt_pk_bf16_f32 %0, %1, %2" : "=v"(y2[0]) : "v"(yq[0][0]), "v"(yq[0][1]) : ); SB();
  asm volatile("v_mfma_f32_16x16x32_bf16 %0, a[64:67], a[60:63], %0" : "+v"(ac[7]) :  : ); SB();
  asm volatile("ds_read2_b32 a[128:129], %0 offset0:128 offset1:129" :  : "v"(aa[0]) : "memory"); SB();
  asm volatile("global_load_dwordx2 %0, %1, %2 offset:0" : "=&v"(xv[0]) : "v"(vX), "s"(x) : "memory"); SB();
  asm volatile("v_mfma_f32_16x16x32_bf16 %0, a[64:67], a[56:59], %0" : "+v"(ac[6]) :  : ); SB();
  asm volatile("ds_read2_b32 a[130:131], %0 offset0:130 offset1:131" :  : "v"(aa[0]) : "memory"); SB();
  asm volatile("global_load_dwordx2 %0, %1, %2 offset:32" : "=&v"(xv[1]) : "v"(vX), "s"(x) : "memory"); SB();
  asm volatile("v_mfma_f32_16x16x32_bf16 %0, a[64:67], a[52:55], %0" : "+v"(ac[5]) :  : ); SB();
  asm volatile("ds_read_b128 a[124:127], %0 offset:3584" :  : "v"(bb) : "memory"); SB();
  asm volatile("v_cvt_pk_bf16_f32 %0, %1, %2" : "=v"(y2[1]) : "v"(yq[0][2]), "v"(yq[0][3]) : ); SB();
  asm volatile("v_mfma_f32_16x16x32_bf16 %0, a[64:67], a[48:51], %0" : "+v"(ac[4]) :  : ); SB();
  asm volatile("v_cvt_pk_bf16_f32 %0, %1, %2" : "=v"(y2[2]) : "v"(yq[1][0]), "v"(yq[1][1]) : ); SB();
  asm volatile("v_mfma_f32_16x16x32_bf16 %0, a[68:71], a[56:59], %0" : "+v"(ac[7]) :  : ); SB();
  asm volatile("ds_read_b128 a[120:123], %0 offset:3072" :  : "v"(bb) : "memory"); SB();
  asm volatile("v_cvt_pk_bf16_f32 %0, %1, %2" : "=v"(y2[3]) : "v"(yq[1][2]), "v"(yq[1][3]) : ); SB();
  asm volatile("v_mfma_f32_16x16x32_bf16 %0, a[68:71], a[52:55], %0" : "+v"(ac[6]) :  : ); SB();
  asm volatile("ds_read_b128 a[96:99], %0 offset:0" :  : "v"(bb) : "memory"); SB();
  asm volatile("v_min_u32 %0, -2, %0" : "+v"(y2[3]) :  : ); SB();
  asm volatile("v_mfma_f32_16x16x32_bf16 %0, a[68:71], a[48:51], %0" : "+v"(ac[5]) :  : ); SB();
  asm volatile("ds_read_b128 a[100:103], %0 offset:512" :  : "v"(bb) : "memory"); SB();
  asm volatile("v_xnor_b32 %0, %0, %1" : "+v"(y2[3]) : "v"(ykv[3]) : ); SB();
  asm volatile("v_mfma_f32_16x16x32_bf16 %0, a[68:71], a[44:47], %0" : "+v"(ac[4]) :  : ); SB();
  asm volatile("buffer_store_dwordx4 %1, %0, %2, %3 offen offset:0 sc1\n\ts_nop 1" :  : "v"(vF), "v"(y2), "s"(rWS), "s"(uso) : "memory"); SB();
  asm volatile("s_waitcnt lgkmcnt(6)\n\tv_mfma_f32_16x16x32_bf16 %0, a[72:75], a[52:55], %0" : "+v"(ac[7]) :  : ); SB();
  asm volatile("ds_read2_b32 a[132:133], %0 offset0:0 offset1:1" :  : "v"(aa[0]) : "memory"); SB();
  asm volatile("v_mfma_f32_16x16x32_bf16 %0, a[72:75], a[48:51], %0" : "+v"(ac[6]) :  : ); SB();
  asm volatile("ds_read2_b32 a[134:135], %0 offset0:2 offset1:3" :  : "v"(aa[0]) : "memory"); SB();
  asm volatile("v_mfma_f32_16x16x32_bf16 %0, a[72:75], a[44:47], %0" : "+v"(ac[5]) :  : ); SB();
  asm volatile("ds_read_b128 a[116:119], %0 offset:2560" :  : "v"(bb) : "memory"); SB();
  asm volatile("v_mfma_f32_16x16x32_bf16 %0, a[64:67], a[44:47], %0" : "+v"(ac[3]) :  : ); SB();
  asm volatile("global_load_dwordx2 %0, %1, %2 offset:64" : "=&v"(xv[2]) : "v"(vX), "s"(x) : "memory"); SB();
  asm volatile("v_mfma_f32_16x16x32_bf16 %0, a[76:79], a[48:51], %0" : "+v"(ac[7]) :  : ); SB();
  asm volatile("ds_read_b128 a[104:107], %0 offset:1024" :  : "v"(bb) : "memory"); SB();
  asm volatile("v_mfma_f32_16x16x32_bf16 %0, a[76:79], a[44:47], %0" : "+v"(ac[6]) :  : ); SB();
  asm volatile("ds_read2_b32 a[136:137], %0 offset0:128 offset1:129" :  : "v"(aa[1]) : "memory"); SB();
  asm volatile("v_mfma_f32_16x16x32_bf16 %0, a[76:79], a[40:43], %0" : "+v"(ac[5]) :  : ); SB();
  asm volatile("ds_read2_b32 a[138:139], %0 offset0:130 offset1:131" :  : "v"(aa[1]) : "memory"); SB();
  asm volatile("v_mfma_f32_16x16x32_bf16 %0, a[72:75], a[40:43], %0" : "+v"(ac[4]) :  : ); SB();
  asm volatile("global_load_dwordx2 %0, %1, %2 offset:96" : "=&v"(xv[3]) : "v"(vX), "s"(x) : "memory"); SB();
  asm volatile("v_mfma_f32_16x16x32_bf16 %0, a[80:83], a[44:47], %0" : "+v"(ac[7]) :  : ); SB();
  asm volatile("ds_read_b128 a[108:111], %0 offset:1536" :  : "v"(bb) : "memory"); SB();
  asm volatile("v_mfma_f32_16x16x32_bf16 %0, a[80:83], a[40:43], %0" : "+v"(ac[6]) :  : ); SB();
  asm volatile("ds_read_b128 a[112:115], %0 offset:2048" :  : "v"(bb) : "memory"); SB();
  asm volatile("v_mfma_f32_16x16x32_bf16 %0, a[68:71], a[40:43], %0" : "+v"(ac[3]) :  : ); SB();
  asm volatile("ds_read2_b32 a[140:141], %0 offset0:0 offset1:1" :  : "v"(aa[1]) : "memory"); SB();
  asm volatile("v_mfma_f32_16x16x32_bf16 %0, a[64:67], a[40:43], %0" : "+v"(ac[2]) :  : ); SB();
  asm volatile("global_load_dwordx2 %0, %1, %2 offset:128" : "=&v"(xv[4]) : "v"(vX), "s"(x) : "memory"); SB();
  asm volatile("v_mfma_f32_16x16x32_bf16 %0, a[84:87], a[40:43], %0" : "+v"(ac[7]) :  : ); SB();
  asm volatile("ds_read2_b32 a[142:143], %0 offset0:2 offset1:3" :  : "v"(aa[1]) : "memory"); SB();
  asm volatile("v_mfma_f32_16x16x32_bf16 %0, a[84:87], a[36:39], %0" : "+v"(ac[6]) :  : ); SB();
  asm volatile("ds_read2_b32 a[144:145], %0 offset0:128 offset1:129" :  : "v"(aa[2]) : "memory"); SB();
  asm volatile("v_mfma_f32_16x16x32_bf16 %0, a[80:83], a[36:39], %0" : "+v"(ac[5]) :  : ); SB();
  asm volatile("ds_read2_b32 a[146:147], %0 offset0:130 offset1:131" :  : "v"(aa[2]) : "memory"); SB();
  asm volatile("v_mfma_f32_16x16x32_bf16 %0, a[76:79], a[36:39], %0" : "+v"(ac[4]) :  : ); SB();
  asm volatile("v_mfma_f32_16x16x32_bf16 %0, a[88:91], a[36:39], %0" : "+v"(ac[7]) :  : ); SB();
  asm volatile("ds_read2_b32 a[148:149], %0 offset0:0 offset1:1" :  : "v"(aa[2]) : "memory"); SB();
  asm volatile("v_mfma_f32_16x16x32_bf16 %0, a[72:75], a[36:39], %0" : "+v"(ac[3]) :  : ); SB();
  asm volatile("ds_read2_b32 a[150:151], %0 offset0:2 offset1:3" :  : "v"(aa[2]) : "memory"); SB();
  asm volatile("v_mfma_f32_16x16x32_bf16 %0, a[68:71], a[36:39], %0" : "+v"(ac[2]) :  : ); SB();
  asm volatile("ds_read2_b32 a[152:153], %0 offset0:128 offset1:129" :  : "v"(aa[3]) : "memory"); SB();
  asm volatile("v_mfma_f32_16x16x32_bf16 %0, a[64:67], a[36:39], %0" : "+v"(ac[1]) :  : ); SB();
  asm volatile("v_mfma_f32_16x16x32_bf16 %0, a[92:95], a[32:35], %0" : "+v"(ac[7]) :  : ); SB();
  asm volatile("ds_read2_b32 a[154:155], %0 offset0:130 offset1:131" :  : "v"(aa[3]) : "memory"); SB();
  asm volatile("v_mfma_f32_16x16x32_bf16 %0, a[88:91], a[32:35], %0" : "+v"(ac[6]) :  : ); SB();
  asm volatile("ds_read2_b32 a[156:157], %0 offset0:0 offset1:1" :  : "v"(aa[3]) : "memory"); SB();
  asm volatile("v_mfma_f32_16x16x32_bf16 %0, a[84:87], a[32:35], %0" : "+v"(ac[5]) :  : ); SB();
  asm volatile("ds_read2_b32 a[158:159], %0 offset0:2 offset1:3" :  : "v"(aa[3]) : "memory"); SB();
  asm volatile("s_waitcnt vmcnt(12)\n\ts_mov_b64 exec, 1\n\tbuffer_store_dwordx2 %0, off, %1, %2 sc1\n\ts_mov_b64 exec, -1" :  : "v"(((u32x2){tg0, tg1})), "s"(rWS), "s"(sFH + 8192) : "memory"); SB();
  asm volatile("v_mfma_f32_16x16x32_bf16 %0, a[80:83], a[32:35], %0" : "+v"(ac[4]) :  : ); SB();
  asm volatile("v_mfma_f32_16x16x32_bf16 %0, a[76:79], a[32:35], %0" : "+v"(ac[3]) :  : ); SB();
  asm volatile("v_mfma_f32_16x16x32_bf16 %0, a[72:75], a[32:35], %0" : "+v"(ac[2]) :  : ); SB();
  asm volatile("v_mfma_f32_16x16x32_bf16 %0, a[68:71], a[32:35], %0" : "+v"(ac[1]) :  : ); SB();
  asm volatile("v_mfma_f32_16x16x32_bf16 %0, a[64:67], a[32:35], %0" : "+v"(ac[0]) :  : ); SB();
  asm volatile("s_waitcnt lgkmcnt(3)\n\tv_mfma_f32_16x16x32_bf16 %0, a[128:131], a[124:127], %0" : "+v"(ac[7]) :  : ); SB();
  asm volatile("v_mfma_f32_16x16x32_bf16 %0, a[128:131], a[120:123], %0" : "+v"(ac[6]) :  : ); SB();
  asm volatile("v_mfma_f32_16x16x32_bf16 %0, a[128:131], a[96:99], %0" : "+v"(ac[0]) :  : ); SB();
  asm volatile("v_mfma_f32_16x16x32_bf16 %0, a[128:131], a[100:103], %0" : "+v"(ac[1]) :  : ); SB();
  asm volatile("v_mfma_f32_16x16x32_bf16 %0, a[132:135], a[120:123], %0" : "+v"(ac[7]) :  : ); SB();
  asm volatile("v_mfma_f32_16x16x32_bf16 %0, a[132:135], a[116:119], %0" : "+v"(ac[6]) :  : ); SB();
  asm volatile("v_mfma_f32_16x16x32_bf16 %0, a[128:131], a[104:107], %0" : "+v"(ac[2]) :  : ); SB();
  asm volatile("ds_write_b128 %0, %1 offset:0" :  : "v"(dd), "v"(ac[0]) : "memory"); SB();
  asm volatile("v_mfma_f32_16x16x32_bf16 %0, a[132:135], a[96:99], %0" : "+v"(ac[1]) :  : ); SB();
  asm volatile("v_mfma_f32_16x16x32_bf16 %0, a[136:139], a[116:119], %0" : "+v"(ac[7]) :  : ); SB();
  asm volatile("v_mfma_f32_16x16x32_bf16 %0, a[128:131], a[108:111], %0" : "+v"(ac[3]) :  : ); SB();
  asm volatile("v_mfma_f32_16x16x32_bf16 %0, a[132:135], a[100:103], %0" : "+v"(ac[2]) :  : ); SB();
  asm volatile("v_mfma_f32_16x16x32_bf16 %0, a[128:131], a[112:115], %0" : "+v"(ac[4]) :  : ); SB();
  asm volatile("s_waitcnt vmcnt(7)\n\t.Lc_%=:\n\tv_cmp_ne_u64 vcc, %2, %1\n\ts_cbranch_vccnz .Lr_%=\n\t.Ld_%=:\n\t.subsection 1\n\t.Lr_%=:\n\tv_add_u32 %0, -1, %0\n\ts_nop 1\n\tv_readfirstlane_b32 vcc_hi, %0\n\ts_nop 3\n\ts_cmp_lt_i32 vcc_hi, 1\n\ts_cbranch_scc1 .Ld_%=\n\ts_sleep 2\n\ts_mov_b32 vcc_lo, 0\n\tbuffer_load_dwordx2 %1, %3, %4, vcc_lo offen sc1\n\ts_waitcnt vmcnt(0)\n\ts_branch .Lc_%=\n\t.subsection 0" : "+v"(tmo), "+v"(fh) : "s"(tg), "v"(vFH), "s"(rWS) : "memory", "vcc", "scc"); SB();
  asm volatile("buffer_load_dwordx4 v[164:167], %0, %1, %2 offen offset:0 sc1" :  : "v"(vHr), "s"(rWS), "s"(hso[2]) : "memory"); SB();
  asm volatile("v_mfma_f32_16x16x32_bf16 %0, a[140:143], a[112:115], %0" : "+v"(ac[7]) :  : ); SB();
  asm volatile("buffer_load_dwordx4 v[168:171], %0, %1, %2 offen offset:0 sc1" :  : "v"(vHr), "s"(rWS), "s"(hso[4]) : "memory"); SB();
  asm volatile("v_mfma_f32_16x16x32_bf16 %0, a[132:135], a[104:107], %0" : "+v"(ac[3]) :  : ); SB();
  asm volatile("ds_write_b128 %0, %1 offset:1024" :  : "v"(dd), "v"(ac[1]) : "memory"); SB();
  asm volatile("v_mfma_f32_16x16x32_bf16 %0, a[136:139], a[96:99], %0" : "+v"(ac[2]) :  : ); SB();
  asm volatile("buffer_load_dwordx4 v[172:175], %0, %1, %2 offen offset:0 sc1" :  : "v"(vHr), "s"(rWS), "s"(hso[6]) : "memory"); SB();
  asm volatile("v_mfma_f32_16x16x32_bf16 %0, a[132:135], a[108:111], %0" : "+v"(ac[4]) :  : ); SB();
  asm volatile("v_mfma_f32_16x16x32_bf16 %0, a[128:131], a[116:119], %0" : "+v"(ac[5]) :  : ); SB();
  asm volatile("v_mfma_f32_16x16x32_bf16 %0, a[136:139], a[100:103], %0" : "+v"(ac[3]) :  : ); SB();
  asm volatile("v_mfma_f32_16x16x32_bf16 %0, a[136:139], a[112:115], %0" : "+v"(ac[6]) :  : ); SB();
  asm volatile("buffer_load_dwordx4 v[160:163], %0, %1, %2 offen offset:0 sc1" :  : "v"(vHr), "s"(rWS), "s"(hso[0]) : "memory"); SB();
  asm volatile("v_mfma_f32_16x16x32_bf16 %0, a[136:139], a[104:107], %0" : "+v"(ac[4]) :  : ); SB();
  asm volatile("ds_write_b128 %0, %1 offset:2048" :  : "v"(dd), "v"(ac[2]) : "memory"); SB();
  asm volatile("v_mfma_f32_16x16x32_bf16 %0, a[132:135], a[112:115], %0" : "+v"(ac[5]) :  : ); SB();
  asm volatile("buffer_load_dwordx4 v[180:183], %0, %1, %2 offen offset:1024 sc1" :  : "v"(vHr), "s"(rWS), "s"(hso[2]) : "memory"); SB();
  asm volatile("v_mfma_f32_16x16x32_bf16 %0, a[140:143], a[96:99], %0" : "+v"(ac[3]) :  : ); SB();
  asm volatile("v_mfma_f32_16x16x32_bf16 %0, a[140:143], a[108:111], %0" : "+v"(ac[6]) :  : ); SB();
  asm volatile("v_mfma_f32_16x16x32_bf16 %0, a[140:143], a[100:103], %0" : "+v"(ac[4]) :  : ); SB();
  asm volatile("v_mfma_f32_16x16x32_bf16 %0, a[136:139], a[108:111], %0" : "+v"(ac[5]) :  : ); SB();
  asm volatile("buffer_load_dwordx4 v[184:187], %0, %1, %2 offen offset:1024 sc1" :  : "v"(vHr), "s"(rWS), "s"(hso[4]) : "memory"); SB();
  asm volatile("v_mfma_f32_16x16x32_bf16 %0, a[144:147], a[108:111], %0" : "+v"(ac[7]) :  : ); SB();
  asm volatile("ds_write_b128 %0, %1 offset:3072" :  : "v"(dd), "v"(ac[3]) : "memory"); SB();
  asm volatile("v_mfma_f32_16x16x32_bf16 %0, a[144:147], a[104:107], %0" : "+v"(ac[6]) :  : ); SB();
  asm volatile("buffer_load_dwordx4 v[188:191], %0, %1, %2 offen offset:1024 sc1" :  : "v"(vHr), "s"(rWS), "s"(hso[6]) : "memory"); SB();
  asm volatile("v_mfma_f32_16x16x32_bf16 %0, a[144:147], a[96:99], %0" : "+v"(ac[4]) :  : ); SB();
  asm volatile("v_mfma_f32_16x16x32_bf16 %0, a[140:143], a[104:107], %0" : "+v"(ac[5]) :  : ); SB();
  asm volatile("v_mfma_f32_16x16x32_bf16 %0, a[148:151], a[104:107], %0" : "+v"(ac[7]) :  : ); SB();
  asm volatile("v_mfma_f32_16x16x32_bf16 %0, a[148:151], a[100:103], %0" : "+v"(ac[6]) :  : ); SB();
  asm volatile("buffer_load_dwordx4 v[176:179], %0, %1, %2 offen offset:1024 sc1" :  : "v"(vHr), "s"(rWS), "s"(hso[0]) : "memory"); SB();
  asm volatile("v_mfma_f32_16x16x32_bf16 %0, a[144:147], a[100:103], %0" : "+v"(ac[5]) :  : ); SB();
  asm volatile("ds_write_b128 %0, %1 offset:4096" :  : "v"(dd), "v"(ac[4]) : "memory"); SB();
  asm volatile("s_waitcnt lgkmcnt(2)\n\tv_mfma_f32_16x16x32_bf16 %0, a[152:155], a[100:103], %0" : "+v"(ac[7]) :  : ); SB();
  asm volatile("buffer_load_dwordx4 v[196:199], %0, %1, %2 offen offset:0 sc1" :  : "v"(vHr), "s"(rWS), "s"(hso[3]) : "memory"); SB();
  asm volatile("v_mfma_f32_16x16x32_bf16 %0, a[152:155], a[96:99], %0" : "+v"(ac[6]) :  : ); SB();
  asm volatile("v_mfma_f32_16x16x32_bf16 %0, a[148:151], a[96:99], %0" : "+v"(ac[5]) :  : ); SB();
  asm volatile("v_mfma_f32_16x16x32_bf16 %0, a[156:159], a[96:99], %0" : "+v"(ac[7]) :  : ); SB();
  asm volatile("buffer_load_dwordx4 v[200:203], %0, %1, %2 offen offset:0 sc1" :  : "v"(vHr), "s"(rWS), "s"(hso[5]) : "memory"); SB();
  asm volatile("s_nop 5" : "+v"(ac[5]), "+v"(ac[6]), "+v"(ac[7]) :  : ); SB();
  asm volatile("ds_write_b128 %0, %1 offset:6144" :  : "v"(dd), "v"(ac[6]) : "memory"); SB();
  asm volatile("ds_write_b128 %0, %1 offset:5120" :  : "v"(dd), "v"(ac[5]) : "memory"); SB();
  asm volatile("ds_write_b128 %0, %1 offset:7168" :  : "v"(dd), "v"(ac[7]) : "memory"); SB();
  ST(18);
  f32x4 yp[2][4], o3[4][5];
  u32x4 yk[2], yo[2];
  unsigned vtmp;
  asm volatile("ds_read_b128 %0, %1 offset:0" : "=&v"(yk[0]) : "v"(ykr) : "memory"); SB();
  asm volatile("ds_read_b128 %0, %1 offset:12928" : "=&v"(yk[1]) : "v"(ykr) : "memory"); SB();
  asm volatile("s_waitcnt lgkmcnt(0)\n\ts_barrier" :  :  : "memory"); SB();
  asm volatile("ds_read_b128 %0, %1 offset:0" : "=&v"(yp[0][0]) : "v"(ydr) : "memory"); SB();
  asm volatile("ds_read_b128 %0, %1 offset:256" : "=&v"(yp[0][1]) : "v"(ydr) : "memory"); SB();
  asm volatile("ds_read_b128 %0, %1 offset:8192" : "=&v"(yp[0][2]) : "v"(ydr) : "memory"); SB();
  asm volatile("ds_read_b128 %0, %1 offset:8448" : "=&v"(yp[0][3]) : "v"(ydr) : "memory"); SB();
  asm volatile("ds_read_b128 %0, %1 offset:16384" : "=&v"(yp[1][0]) : "v"(ydr) : "memory"); SB();
  asm volatile("ds_read_b128 %0, %1 offset:16640" : "=&v"(yp[1][1]) : "v"(ydr) : "memory"); SB();
  asm volatile("ds_read_b128 %0, %1 offset:24576" : "=&v"(yp[1][2]) : "v"(ydr) : "memory"); SB();
  asm volatile("ds_read_b128 %0, %1 offset:24832" : "=&v"(yp[1][3]) : "v"(ydr) : "memory"); SB();
  asm volatile("" : "+v"(yk[0]), "+v"(yk[1]) :  : ); SB();
  f32x2 yd[2][4];
#pragma unroll
  for (int n = 0; n < 8; ++n) { const float Dv = dD[n >> 2]; yd[n >> 2][n & 3] = unpk(yk[n >> 2][n & 3]) * (f32x2){Dv, Dv}; asm volatile("" : "+v"(yd[n >> 2][n & 3])); }
  SB();
  asm volatile("s_waitcnt lgkmcnt(3)" : "+v"(yp[0][0]), "+v"(yp[0][1]), "+v"(yp[0][2]), "+v"(yp[0][3]) :  : "memory"); SB();
  {
    const f32x4 s0 = yp[0][0] + yp[0][2], s1 = yp[0][1] + yp[0][3];
    yo[0] = (u32x4){pk2((f32x2){s0[0], s0[1]} + yd[0][0]), pk2((f32x2){s0[2], s0[3]} + yd[0][1]), pk2((f32x2){s1[0], s1[1]} + yd[0][2]), ~(nohole(pk2((f32x2){s1[2], s1[3]} + yd[0][3])) ^ yk[0][3])};
    SB();
  }
  asm volatile("buffer_store_dwordx4 %1, %0, %2, %3 offen offset:0 sc1\n\ts_nop 1" :  : "v"(vF), "v"(yo[0]), "s"(rWS), "s"(W_KV + 0) : "memory"); SB();
  asm volatile("s_waitcnt lgkmcnt(0)" : "+v"(yp[1][0]), "+v"(yp[1][1]), "+v"(yp[1][2]), "+v"(yp[1][3]) :  : "memory"); SB();
  {
    const f32x4 s0 = yp[1][0] + yp[1][2], s1 = yp[1][1] + yp[1][3];
    yo[1] = (u32x4){pk2((f32x2){s0[0], s0[1]} + yd[1][0]), pk2((f32x2){s0[2], s0[3]} + yd[1][1]), pk2((f32x2){s1[0], s1[1]} + yd[1][2]), ~(nohole(pk2((f32x2){s1[2], s1[3]} + yd[1][3])) ^ yk[1][3])};
    SB();
  }
  asm volatile("buffer_store_dwordx4 %1, %0, %2, %3 offen offset:0 sc1\n\ts_nop 1" :  : "v"(vF), "v"(yo[1]), "s"(rWS), "s"(W_KV + 4096) : "memory"); SB();
  ST(19);
  ST(20);
  f32x2 xf[5][2], sv[5][2];
  float rd, mr;
  asm volatile("s_waitcnt vmcnt(8)" :  :  : "memory"); SB();
  asm volatile("v_mfma_f32_16x16x32_bf16 %0, a[160:163], v[164:167], 0" : "=&v"(o3[1][0]) :  : ); SB();
  asm volatile("buffer_load_dwordx2 %0, %1, %2, %3 offen sc1" : "=&v"(fi) : "v"(vFH), "s"(rWS), "s"(8192u) : "memory"); SB();
  asm volatile("v_mfma_f32_16x16x32_bf16 %0, a[164:167], v[164:167], 0" : "=&v"(o3[1][1]) :  : ); SB();
  asm volatile("v_mfma_f32_16x16x32_bf16 %0, a[168:171], v[164:167], 0" : "=&v"(o3[1][2]) :  : ); SB();
  asm volatile("v_mfma_f32_16x16x32_bf16 %0, a[172:175], v[164:167], 0" : "=&v"(o3[1][3]) :  : ); SB();
  asm volatile("buffer_load_dwordx4 v[204:207], %0, %1, %2 offen offset:0 sc1" :  : "v"(vHr), "s"(rWS), "s"(hso[7]) : "memory"); SB();
  asm volatile("v_mfma_f32_16x16x32_bf16 %0, a[176:179], v[164:167], 0" : "=&v"(o3[1][4]) :  : ); SB();
  asm volatile("v_mfma_f32_16x16x32_bf16 %0, a[160:163], v[168:171], 0" : "=&v"(o3[2][0]) :  : ); SB();
  asm volatile("v_mfma_f32_16x16x32_bf16 %0, a[164:167], v[168:171], 0" : "=&v"(o3[2][1]) :  : ); SB();
  asm volatile("v_mfma_f32_16x16x32_bf16 %0, a[168:171], v[168:171], 0" : "=&v"(o3[2][2]) :  : ); SB();
  asm volatile("buffer_load_dwordx4 v[192:195], %0, %1, %2 offen offset:0 sc1" :  : "v"(vHr), "s"(rWS), "s"(hso[1]) : "memory"); SB();
  asm volatile("v_mfma_f32_16x16x32_bf16 %0, a[172:175], v[168:171], 0" : "=&v"(o3[2][3]) :  : ); SB();
  asm volatile("v_mfma_f32_16x16x32_bf16 %0, a[176:179], v[168:171], 0" : "=&v"(o3[2][4]) :  : ); SB();
  asm volatile("v_mfma_f32_16x16x32_bf16 %0, a[160:163], v[172:175], 0" : "=&v"(o3[3][0]) :  : ); SB();
  asm volatile("buffer_load_dwordx4 v[212:215], %0, %1, %2 offen offset:1024 sc1" :  : "v"(vHr), "s"(rWS), "s"(hso[3]) : "memory"); SB();
  asm volatile("v_mfma_f32_16x16x32_bf16 %0, a[164:167], v[172:175], 0" : "=&v"(o3[3][1]) :  : ); SB();
  asm volatile("v_mfma_f32_16x16x32_bf16 %0, a[168:171], v[172:175], 0" : "=&v"(o3[3][2]) :  : ); SB();
  asm volatile("v_mfma_f32_16x16x32_bf16 %0, a[172:175], v[172:175], 0" : "=&v"(o3[3][3]) :  : ); SB();
  asm volatile("v_mfma_f32_16x16x32_bf16 %0, a[176:179], v[172:175], 0" : "=&v"(o3[3][4]) :  : ); SB();
  asm volatile("buffer_load_dwordx4 v[216:219], %0, %1, %2 offen offset:1024 sc1" :  : "v"(vHr), "s"(rWS), "s"(hso[5]) : "memory"); SB();
  asm volatile("s_waitcnt vmcnt(9)" :  :  : "memory"); SB();
  asm volatile("v_mfma_f32_16x16x32_bf16 %0, a[180:183], v[180:183], %0" : "+v"(o3[1][0]) :  : ); SB();
  asm volatile("v_rcp_f32 %0, %1" : "=v"(rd) : "v"(xd[0]) : ); SB();
  asm volatile("v_mfma_f32_16x16x32_bf16 %0, a[184:187], v[180:183], %0" : "+v"(o3[1][1]) :  : ); SB();
  asm volatile("" : "+v"(xv[0]), "+v"(xv[1]), "+v"(xv[2]), "+v"(xv[3]), "+v"(xv[4]) :  : "memory"); SB();
  asm volatile("v_mul_f32 %0, -%1, %2" : "=v"(mr) : "v"(xm[0]), "v"(rd) : ); SB();
  asm volatile("v_mfma_f32_16x16x32_bf16 %0, a[188:191], v[180:183], %0" : "+v"(o3[1][2]) :  : ); SB();
  asm volatile("buffer_load_dwordx4 v[220:223], %0, %1, %2 offen offset:1024 sc1" :  : "v"(vHr), "s"(rWS), "s"(hso[7]) : "memory"); SB();
  asm volatile("v_lshlrev_b32 %0, 16, %1" : "=v"(xf[0][0][0]) : "v"(xv[0][0]) : ); SB();
  asm volatile("v_mfma_f32_16x16x32_bf16 %0, a[192:195], v[180:183], %0" : "+v"(o3[1][3]) :  : ); SB();
  asm volatile("v_and_b32 %0, 0xffff0000, %1" : "=v"(xf[0][0][1]) : "v"(xv[0][0]) : ); SB();
  asm volatile("v_mfma_f32_16x16x32_bf16 %0, a[196:199], v[180:183], %0" : "+v"(o3[1][4]) :  : ); SB();
  asm volatile("v_lshlrev_b32 %0, 16, %1" : "=v"(xf[0][1][0]) : "v"(xv[0][1]) : ); SB();
  asm volatile("v_mfma_f32_16x16x32_bf16 %0, a[180:183], v[184:187], %0" : "+v"(o3[2][0]) :  : ); SB();
  asm volatile("v_and_b32 %0, 0xffff0000, %1" : "=v"(xf[0][1][1]) : "v"(xv[0][1]) : ); SB();
  asm volatile("v_mfma_f32_16x16x32_bf16 %0, a[184:187], v[184:187], %0" : "+v"(o3[2][1]) :  : ); SB();
  asm volatile("buffer_load_dwordx4 v[208:211], %0, %1, %2 offen offset:1024 sc1" :  : "v"(vHr), "s"(rWS), "s"(hso[1]) : "memory"); SB();
  asm volatile("v_lshlrev_b32 %0, 16, %1" : "=v"(xf[1][0][0]) : "v"(xv[1][0]) : ); SB();
  asm volatile("v_mfma_f32_16x16x32_bf16 %0, a[188:191], v[184:187], %0" : "+v"(o3[2][2]) :  : ); SB();
  asm volatile("v_and_b32 %0, 0xffff0000, %1" : "=v"(xf[1][0][1]) : "v"(xv[1][0]) : ); SB();
  asm volatile("v_mfma_f32_16x16x32_bf16 %0, a[192:195], v[184:187], %0" : "+v"(o3[2][3]) :  : ); SB();
  asm volatile("v_lshlrev_b32 %0, 16, %1" : "=v"(xf[1][1][0]) : "v"(xv[1][1]) : ); SB();
  asm volatile("v_mfma_f32_16x16x32_bf16 %0, a[196:199], v[184:187], %0" : "+v"(o3[2][4]) :  : ); SB();
  asm volatile("v_and_b32 %0, 0xffff0000, %1" : "=v"(xf[1][1][1]) : "v"(xv[1][1]) : ); SB();
  asm volatile("v_mfma_f32_16x16x32_bf16 %0, a[180:183], v[188:191], %0" : "+v"(o3[3][0]) :  : ); SB();
  asm volatile("v_lshlrev_b32 %0, 16, %1" : "=v"(xf[2][0][0]) : "v"(xv[2][0]) : ); SB();
  asm volatile("v_mfma_f32_16x16x32_bf16 %0, a[184:187], v[188:191], %0" : "+v"(o3[3][1]) :  : ); SB();
  asm volatile("v_and_b32 %0, 0xffff0000, %1" : "=v"(xf[2][0][1]) : "v"(xv[2][0]) : ); SB();
  asm volatile("v_mfma_f32_16x16x32_bf16 %0, a[188:191], v[188:191], %0" : "+v"(o3[3][2]) :  : ); SB();
  asm volatile("v_lshlrev_b32 %0, 16, %1" : "=v"(xf[2][1][0]) : "v"(xv[2][1]) : ); SB();
  asm volatile("v_mfma_f32_16x16x32_bf16 %0, a[192:195], v[188:191], %0" : "+v"(o3[3][3]) :  : ); SB();
  asm volatile("v_and_b32 %0, 0xffff0000, %1" : "=v"(xf[2][1][1]) : "v"(xv[2][1]) : ); SB();
  asm volatile("v_mfma_f32_16x16x32_bf16 %0, a[196:199], v[188:191], %0" : "+v"(o3[3][4]) :  : ); SB();
  asm volatile("v_lshlrev_b32 %0, 16, %1" : "=v"(xf[3][0][0]) : "v"(xv[3][0]) : ); SB();
  asm volatile("s_waitcnt vmcnt(6)\n\t.Lc_%=:\n\tv_cmp_ne_u64 vcc, %2, %1\n\ts_cbranch_vccnz .Lr_%=\n\t.Ld_%=:\n\t.subsection 1\n\t.Lr_%=:\n\tv_add_u32 %0, -1, %0\n\ts_nop 1\n\tv_readfirstlane_b32 vcc_hi, %0\n\ts_nop 3\n\ts_cmp_lt_i32 vcc_hi, 1\n\ts_cbranch_scc1 .Ld_%=\n\ts_sleep 2\n\ts_mov_b32 vcc_lo, 8192\n\tbuffer_load_dwordx2 %1, %3, %4, vcc_lo offen sc1\n\ts_waitcnt vmcnt(0)\n\ts_branch .Lc_%=\n\t.subsection 0" : "+v"(tmo), "+v"(fi) : "s"(tg), "v"(vFH), "s"(rWS) : "memory", "vcc", "scc"); SB();
  asm volatile("buffer_load_dwordx4 v[228:231], %0, %1, %2 offen offset:2048 sc1" :  : "v"(vHr), "s"(rWS), "s"(hso[2]) : "memory"); SB();
  asm volatile("buffer_load_dwordx4 v[232:235], %0, %1, %2 offen offset:2048 sc1" :  : "v"(vHr), "s"(rWS), "s"(hso[4]) : "memory"); SB();
  asm volatile("buffer_load_dwordx4 v[236:239], %0, %1, %2 offen offset:2048 sc1" :  : "v"(vHr), "s"(rWS), "s"(hso[6]) : "memory"); SB();
  asm volatile("s_waitcnt vmcnt(7)" :  :  : "memory"); SB();
  asm volatile("v_mfma_f32_16x16x32_bf16 %0, a[200:203], v[196:199], %0" : "+v"(o3[1][0]) :  : ); SB();
  asm volatile("v_and_b32 %0, 0xffff0000, %1" : "=v"(xf[3][0][1]) : "v"(xv[3][0]) : ); SB();
  asm volatile("v_mfma_f32_16x16x32_bf16 %0, a[204:207], v[196:199], %0" : "+v"(o3[1][1]) :  : ); SB();
  asm volatile("buffer_load_dwordx4 v[224:227], %0, %1, %2 offen offset:2048 sc1" :  : "v"(vHr), "s"(rWS), "s"(hso[0]) : "memory"); SB();
  asm volatile("v_lshlrev_b32 %0, 16, %1" : "=v"(xf[3][1][0]) : "v"(xv[3][1]) : ); SB();
  asm volatile("v_mfma_f32_16x16x32_bf16 %0, a[208:211], v[196:199], %0" : "+v"(o3[1][2]) :  : ); SB();
  asm volatile("v_and_b32 %0, 0xffff0000, %1" : "=v"(xf[3][1][1]) : "v"(xv[3][1]) : ); SB();
  asm volatile("v_mfma_f32_16x16x32_bf16 %0, a[212:215], v[196:199], %0" : "+v"(o3[1][3]) :  : ); SB();
  asm volatile("v_lshlrev_b32 %0, 16, %1" : "=v"(xf[4][0][0]) : "v"(xv[4][0]) : ); SB();
  asm volatile("v_mfma_f32_16x16x32_bf16 %0, a[216:219], v[196:199], %0" : "+v"(o3[1][4]) :  : ); SB();
  asm volatile("v_and_b32 %0, 0xffff0000, %1" : "=v"(xf[4][0][1]) : "v"(xv[4][0]) : ); SB();
  asm volatile("v_mfma_f32_16x16x32_bf16 %0, a[200:203], v[200:203], %0" : "+v"(o3[2][0]) :  : ); SB();
  asm volatile("buffer_load_dwordx4 v[244:247], %0, %1, %2 offen offset:2048 sc1" :  : "v"(vHr), "s"(rWS), "s"(hso[3]) : "memory"); SB();
  asm volatile("v_lshlrev_b32 %0, 16, %1" : "=v"(xf[4][1][0]) : "v"(xv[4][1]) : ); SB();
  asm volatile("v_mfma_f32_16x16x32_bf16 %0, a[204:207], v[200:203], %0" : "+v"(o3[2][1]) :  : ); SB();
  asm volatile("v_and_b32 %0, 0xffff0000, %1" : "=v"(xf[4][1][1]) : "v"(xv[4][1]) : ); SB();
  asm volatile("v_mfma_f32_16x16x32_bf16 %0, a[208:211], v[200:203], %0" : "+v"(o3[2][2]) :  : ); SB();
  asm volatile("v_fma_f32 %0, %1, %2, %3" : "=v"(sv[0][0][0]) : "v"(xf[0][0][0]), "v"(rd), "v"(mr) : ); SB();
  asm volatile("v_mfma_f32_16x16x32_bf16 %0, a[212:215], v[200:203], %0" : "+v"(o3[2][3]) :  : ); SB();
  asm volatile("buffer_load_dwordx4 v[248:251], %0, %1, %2 offen offset:2048 sc1" :  : "v"(vHr), "s"(rWS), "s"(hso[5]) : "memory"); SB();
  asm volatile("v_fma_f32 %0, %1, %2, %3" : "=v"(sv[0][0][1]) : "v"(xf[0][0][1]), "v"(rd), "v"(mr) : ); SB();
  asm volatile("v_mfma_f32_16x16x32_bf16 %0, a[216:219], v[200:203], %0" : "+v"(o3[2][4]) :  : ); SB();
  asm volatile("v_fma_f32 %0, %1, %2, %3" : "=v"(sv[0][1][0]) : "v"(xf[0][1][0]), "v"(rd), "v"(mr) : ); SB();
  asm volatile("v_mfma_f32_16x16x32_bf16 %0, a[200:203], v[204:207], %0" : "+v"(o3[3][0]) :  : ); SB();
  asm volatile("v_fma_f32 %0, %1, %2, %3" : "=v"(sv[0][1][1]) : "v"(xf[0][1][1]), "v"(rd), "v"(mr) : ); SB();
  asm volatile("v_mfma_f32_16x16x32_bf16 %0, a[204:207], v[204:207], %0" : "+v"(o3[3][1]) :  : ); SB();
  asm volatile("v_fma_f32 %0, %1, %2, %3" : "=v"(sv[1][0][0]) : "v"(xf[1][0][0]), "v"(rd), "v"(mr) : ); SB();
  asm volatile("v_mfma_f32_16x16x32_bf16 %0, a[208:211], v[204:207], %0" : "+v"(o3[3][2]) :  : ); SB();
  asm volatile("buffer_load_dwordx4 v[252:255], %0, %1, %2 offen offset:2048 sc1" :  : "v"(vHr), "s"(rWS), "s"(hso[7]) : "memory"); SB();
  asm volatile("v_fma_f32 %0, %1, %2, %3" : "=v"(sv[1][0][1]) : "v"(xf[1][0][1]), "v"(rd), "v"(mr) : ); SB();
  asm volatile("v_mfma_f32_16x16x32_bf16 %0, a[212:215], v[204:207], %0" : "+v"(o3[3][3]) :  : ); SB();
  asm volatile("v_fma_f32 %0, %1, %2, %3" : "=v"(sv[1][1][0]) : "v"(xf[1][1][0]), "v"(rd), "v"(mr) : ); SB();
  asm volatile("v_mfma_f32_16x16x32_bf16 %0, a[216:219], v[204:207], %0" : "+v"(o3[3][4]) :  : ); SB();
  asm volatile("v_fma_f32 %0, %1, %2, %3" : "=v"(sv[1][1][1]) : "v"(xf[1][1][1]), "v"(rd), "v"(mr) : ); SB();
  asm volatile("s_waitcnt vmcnt(7)" :  :  : "memory"); SB();
  asm volatile("v_mfma_f32_16x16x32_bf16 %0, a[220:223], v[212:215], %0" : "+v"(o3[1][0]) :  : ); SB();
  asm volatile("buffer_load_dwordx4 v[240:243], %0, %1, %2 offen offset:2048 sc1" :  : "v"(vHr), "s"(rWS), "s"(hso[1]) : "memory"); SB();
  asm volatile("v_fma_f32 %0, %1, %2, %3" : "=v"(sv[2][0][0]) : "v"(xf[2][0][0]), "v"(rd), "v"(mr) : ); SB();
  asm volatile("v_mfma_f32_16x16x32_bf16 %0, a[224:227], v[212:215], %0" : "+v"(o3[1][1]) :  : ); SB();
  asm volatile("v_fma_f32 %0, %1, %2, %3" : "=v"(sv[2][0][1]) : "v"(xf[2][0][1]), "v"(rd), "v"(mr) : ); SB();
  asm volatile("v_mfma_f32_16x16x32_bf16 %0, a[228:231], v[212:215], %0" : "+v"(o3[1][2]) :  : ); SB();
  asm volatile("v_fma_f32 %0, %1, %2, %3" : "=v"(sv[2][1][0]) : "v"(xf[2][1][0]), "v"(rd), "v"(mr) : ); SB();
  asm volatile("v_mfma_f32_16x16x32_bf16 %0, a[232:235], v[212:215], %0" : "+v"(o3[1][3]) :  : ); SB();
  asm volatile("v_fma_f32 %0, %1, %2, %3" : "=v"(sv[2][1][1]) : "v"(xf[2][1][1]), "v"(rd), "v"(mr) : ); SB();
  asm volatile("v_mfma_f32_16x16x32_bf16 %0, a[236:239], v[212:215], %0" : "+v"(o3[1][4]) :  : ); SB();
  asm volatile("v_fma_f32 %0, %1, %2, %3" : "=v"(sv[3][0][0]) : "v"(xf[3][0][0]), "v"(rd), "v"(mr) : ); SB();
  asm volatile("v_mfma_f32_16x16x32_bf16 %0, a[220:223], v[216:219], %0" : "+v"(o3[2][0]) :  : ); SB();
  asm volatile("v_fma_f32 %0, %1, %2, %3" : "=v"(sv[3][0][1]) : "v"(xf[3][0][1]), "v"(rd), "v"(mr) : ); SB();
  asm volatile("v_mfma_f32_16x16x32_bf16 %0, a[224:227], v[216:219], %0" : "+v"(o3[2][1]) :  : ); SB();
  asm volatile("v_fma_f32 %0, %1, %2, %3" : "=v"(sv[3][1][0]) : "v"(xf[3][1][0]), "v"(rd), "v"(mr) : ); SB();
  asm volatile("v_mfma_f32_16x16x32_bf16 %0, a[228:231], v[216:219], %0" : "+v"(o3[2][2]) :  : ); SB();
  asm volatile("v_fma_f32 %0, %1, %2, %3" : "=v"(sv[3][1][1]) : "v"(xf[3][1][1]), "v"(rd), "v"(mr) : ); SB();
  asm volatile("v_mfma_f32_16x16x32_bf16 %0, a[232:235], v[216:219], %0" : "+v"(o3[2][3]) :  : ); SB();
  asm volatile("v_fma_f32 %0, %1, %2, %3" : "=v"(sv[4][0][0]) : "v"(xf[4][0][0]), "v"(rd), "v"(mr) : ); SB();
  asm volatile("v_mfma_f32_16x16x32_bf16 %0, a[236:239], v[216:219], %0" : "+v"(o3[2][4]) :  : ); SB();
  asm volatile("v_fma_f32 %0, %1, %2, %3" : "=v"(sv[4][0][1]) : "v"(xf[4][0][1]), "v"(rd), "v"(mr) : ); SB();
  asm volatile("v_mfma_f32_16x16x32_bf16 %0, a[220:223], v[220:223], %0" : "+v"(o3[3][0]) :  : ); SB();
  asm volatile("v_fma_f32 %0, %1, %2, %3" : "=v"(sv[4][1][0]) : "v"(xf[4][1][0]), "v"(rd), "v"(mr) : ); SB();
  asm volatile("v_mfma_f32_16x16x32_bf16 %0, a[224:227], v[220:223], %0" : "+v"(o3[3][1]) :  : ); SB();
  asm volatile("v_fma_f32 %0, %1, %2, %3" : "=v"(sv[4][1][1]) : "v"(xf[4][1][1]), "v"(rd), "v"(mr) : ); SB();
  asm volatile("v_mfma_f32_16x16x32_bf16 %0, a[228:231], v[220:223], %0" : "+v"(o3[3][2]) :  : ); SB();
  asm volatile("v_mfma_f32_16x16x32_bf16 %0, a[232:235], v[220:223], %0" : "+v"(o3[3][3]) :  : ); SB();
  asm volatile("v_mfma_f32_16x16x32_bf16 %0, a[236:239], v[220:223], %0" : "+v"(o3[3][4]) :  : ); SB();
  ST(30);
  asm volatile("buffer_load_dwordx4 v[164:167], %0, %1, %2 offen offset:0 sc1" :  : "v"(vKV), "s"(rWS), "s"(0u) : "memory"); SB();
  asm volatile("buffer_load_dwordx4 v[168:171], %0, %1, %2 offen offset:0 sc1" :  : "v"(vKV), "s"(rWS), "s"(w3k[0]) : "memory"); SB();
  asm volatile("buffer_load_dwordx4 v[172:175], %0, %1, %2 offen offset:0 sc1" :  : "v"(vKV), "s"(rWS), "s"(w3k[1]) : "memory"); SB();
  asm volatile("s_waitcnt vmcnt(3)" :  :  : "memory"); SB();
  asm volatile("v_mfma_f32_16x16x32_bf16 %0, a[240:243], v[228:231], %0" : "+v"(o3[1][0]) :  : ); SB();
  asm volatile("v_mfma_f32_16x16x32_bf16 %0, a[244:247], v[228:231], %0" : "+v"(o3[1][1]) :  : ); SB();
  asm volatile("v_mfma_f32_16x16x32_bf16 %0, a[248:251], v[228:231], %0" : "+v"(o3[1][2]) :  : ); SB();
  asm volatile("v_mfma_f32_16x16x32_bf16 %0, a[252:255], v[228:231], %0" : "+v"(o3[1][3]) :  : ); SB();
  asm volatile("v_mfma_f32_16x16x32_bf16 %0, a[0:3], v[228:231], %0" : "+v"(o3[1][4]) :  : ); SB();
  asm volatile("v_mfma_f32_16x16x32_bf16 %0, a[4:7], v[244:247], %0" : "+v"(o3[1][0]) :  : ); SB();
  asm volatile("v_mfma_f32_16x16x32_bf16 %0, a[8:11], v[244:247], %0" : "+v"(o3[1][1]) :  : ); SB();
  asm volatile("v_mfma_f32_16x16x32_bf16 %0, a[12:15], v[244:247], %0" : "+v"(o3[1][2]) :  : ); SB();
  asm volatile("v_mfma_f32_16x16x32_bf16 %0, a[16:19], v[244:247], %0" : "+v"(o3[1][3]) :  : ); SB();
  asm volatile("v_mfma_f32_16x16x32_bf16 %0, a[20:23], v[244:247], %0" : "+v"(o3[1][4]) :  : ); SB();
  asm volatile("v_mfma_f32_16x16x32_bf16 %0, a[240:243], v[232:235], %0" : "+v"(o3[2][0]) :  : ); SB();
  asm volatile("ds_write_b128 %0, %1 offset:0" :  : "v"(pw3), "v"(o3[1][0]) : "memory"); SB();
  asm volatile("v_mfma_f32_16x16x32_bf16 %0, a[244:247], v[232:235], %0" : "+v"(o3[2][1]) :  : ); SB();
  asm volatile("v_mfma_f32_16x16x32_bf16 %0, a[248:251], v[232:235], %0" : "+v"(o3[2][2]) :  : ); SB();
  asm volatile("v_mfma_f32_16x16x32_bf16 %0, a[252:255], v[232:235], %0" : "+v"(o3[2][3]) :  : ); SB();
  asm volatile("ds_write_b128 %0, %1 offset:1024" :  : "v"(pw3), "v"(o3[1][1]) : "memory"); SB();
  asm volatile("v_mfma_f32_16x16x32_bf16 %0, a[0:3], v[232:235], %0" : "+v"(o3[2][4]) :  : ); SB();
  asm volatile("v_mfma_f32_16x16x32_bf16 %0, a[4:7], v[248:251], %0" : "+v"(o3[2][0]) :  : ); SB();
  asm volatile("v_mfma_f32_16x16x32_bf16 %0, a[8:11], v[248:251], %0" : "+v"(o3[2][1]) :  : ); SB();
  asm volatile("ds_write_b128 %0, %1 offset:2048" :  : "v"(pw3), "v"(o3[1][2]) : "memory"); SB();
  asm volatile("v_mfma_f32_16x16x32_bf16 %0, a[12:15], v[248:251], %0" : "+v"(o3[2][2]) :  : ); SB();
  asm volatile("v_mfma_f32_16x16x32_bf16 %0, a[16:19], v[248:251], %0" : "+v"(o3[2][3]) :  : ); SB();
  asm volatile("v_mfma_f32_16x16x32_bf16 %0, a[20:23], v[248:251], %0" : "+v"(o3[2][4]) :  : ); SB();
  asm volatile("ds_write_b128 %0, %1 offset:3072" :  : "v"(pw3), "v"(o3[1][3]) : "memory"); SB();
  asm volatile("v_mfma_f32_16x16x32_bf16 %0, a[240:243], v[236:239], %0" : "+v"(o3[3][0]) :  : ); SB();
  asm volatile("v_mfma_f32_16x16x32_bf16 %0, a[244:247], v[236:239], %0" : "+v"(o3[3][1]) :  : ); SB();
  asm volatile("v_mfma_f32_16x16x32_bf16 %0, a[248:251], v[236:239], %0" : "+v"(o3[3][2]) :  : ); SB();
  asm volatile("ds_write_b128 %0, %1 offset:4096" :  : "v"(pw3), "v"(o3[1][4]) : "memory"); SB();
  asm volatile("v_mfma_f32_16x16x32_bf16 %0, a[252:255], v[236:239], %0" : "+v"(o3[3][3]) :  : ); SB();
  asm volatile("v_mfma_f32_16x16x32_bf16 %0, a[0:3], v[236:239], %0" : "+v"(o3[3][4]) :  : ); SB();
  asm volatile("v_mfma_f32_16x16x32_bf16 %0, a[4:7], v[252:255], %0" : "+v"(o3[3][0]) :  : ); SB();
  asm volatile("ds_write_b128 %0, %1 offset:5120" :  : "v"(pw3), "v"(o3[2][0]) : "memory"); SB();
  asm volatile("v_mfma_f32_16x16x32_bf16 %0, a[8:11], v[252:255], %0" : "+v"(o3[3][1]) :  : ); SB();
  asm volatile("v_mfma_f32_16x16x32_bf16 %0, a[12:15], v[252:255], %0" : "+v"(o3[3][2]) :  : ); SB();
  asm volatile("v_mfma_f32_16x16x32_bf16 %0, a[16:19], v[252:255], %0" : "+v"(o3[3][3]) :  : ); SB();
  asm volatile("ds_write_b128 %0, %1 offset:6144" :  : "v"(pw3), "v"(o3[2][1]) : "memory"); SB();
  asm volatile("v_mfma_f32_16x16x32_bf16 %0, a[20:23], v[252:255], %0" : "+v"(o3[3][4]) :  : ); SB();
  asm volatile("v_mfma_f32_16x16x32_bf16 %0, a[160:163], v[160:163], 0" : "=&v"(o3[0][0]) :  : ); SB();
  asm volatile("v_mfma_f32_16x16x32_bf16 %0, a[164:167], v[160:163], 0" : "=&v"(o3[0][1]) :  : ); SB();
  asm volatile("ds_write_b128 %0, %1 offset:7168" :  : "v"(pw3), "v"(o3[2][2]) : "memory"); SB();
  asm volatile("v_mfma_f32_16x16x32_bf16 %0, a[168:171], v[160:163], 0" : "=&v"(o3[0][2]) :  : ); SB();
  asm volatile("v_mfma_f32_16x16x32_bf16 %0, a[172:175], v[160:163], 0" : "=&v"(o3[0][3]) :  : ); SB();
  asm volatile("v_mfma_f32_16x16x32_bf16 %0, a[176:179], v[160:163], 0" : "=&v"(o3[0][4]) :  : ); SB();
  asm volatile("ds_write_b128 %0, %1 offset:8192" :  : "v"(pw3), "v"(o3[2][3]) : "memory"); SB();
  asm volatile("v_mfma_f32_16x16x32_bf16 %0, a[180:183], v[176:179], %0" : "+v"(o3[0][0]) :  : ); SB();
  asm volatile("v_mfma_f32_16x16x32_bf16 %0, a[184:187], v[176:179], %0" : "+v"(o3[0][1]) :  : ); SB();
  asm volatile("v_mfma_f32_16x16x32_bf16 %0, a[188:191], v[176:179], %0" : "+v"(o3[0][2]) :  : ); SB();
  asm volatile("ds_write_b128 %0, %1 offset:9216" :  : "v"(pw3), "v"(o3[2][4]) : "memory"); SB();
  asm volatile("v_mfma_f32_16x16x32_bf16 %0, a[192:195], v[176:179], %0" : "+v"(o3[0][3]) :  : ); SB();
  asm volatile("v_mfma_f32_16x16x32_bf16 %0, a[196:199], v[176:179], %0" : "+v"(o3[0][4]) :  : ); SB();
  asm volatile("v_mfma_f32_16x16x32_bf16 %0, a[200:203], v[192:195], %0" : "+v"(o3[0][0]) :  : ); SB();
  asm volatile("ds_write_b128 %0, %1 offset:10240" :  : "v"(pw3), "v"(o3[3][0]) : "memory"); SB();
  asm volatile("v_mfma_f32_16x16x32_bf16 %0, a[204:207], v[192:195], %0" : "+v"(o3[0][1]) :  : ); SB();
  asm volatile("v_mfma_f32_16x16x32_bf16 %0, a[208:211], v[192:195], %0" : "+v"(o3[0][2]) :  : ); SB();
  asm volatile("v_mfma_f32_16x16x32_bf16 %0, a[212:215], v[192:195], %0" : "+v"(o3[0][3]) :  : ); SB();
  asm volatile("ds_write_b128 %0, %1 offset:11264" :  : "v"(pw3), "v"(o3[3][1]) : "memory"); SB();
  asm volatile("v_mfma_f32_16x16x32_bf16 %0, a[216:219], v[192:195], %0" : "+v"(o3[0][4]) :  : ); SB();
  asm volatile("v_mfma_f32_16x16x32_bf16 %0, a[220:223], v[208:211], %0" : "+v"(o3[0][0]) :  : ); SB();
  asm volatile("v_mfma_f32_16x16x32_bf16 %0, a[224:227], v[208:211], %0" : "+v"(o3[0][1]) :  : ); SB();
  asm volatile("ds_write_b128 %0, %1 offset:12288" :  : "v"(pw3), "v"(o3[3][2]) : "memory"); SB();
  asm volatile("v_mfma_f32_16x16x32_bf16 %0, a[228:231], v[208:211], %0" : "+v"(o3[0][2]) :  : ); SB();
  asm volatile("v_mfma_f32_16x16x32_bf16 %0, a[232:235], v[208:211], %0" : "+v"(o3[0][3]) :  : ); SB();
  asm volatile("v_mfma_f32_16x16x32_bf16 %0, a[236:239], v[208:211], %0" : "+v"(o3[0][4]) :  : ); SB();
  asm volatile("ds_write_b128 %0, %1 offset:13312" :  : "v"(pw3), "v"(o3[3][3]) : "memory"); SB();
  asm volatile("v_mfma_f32_16x16x32_bf16 %0, a[240:243], v[224:227], %0" : "+v"(o3[0][0]) :  : ); SB();
  asm volatile("v_mfma_f32_16x16x32_bf16 %0, a[244:247], v[224:227], %0" : "+v"(o3[0][1]) :  : ); SB();
  asm volatile("v_mfma_f32_16x16x32_bf16 %0, a[248:251], v[224:227], %0" : "+v"(o3[0][2]) :  : ); SB();
  asm volatile("ds_write_b128 %0, %1 offset:14336" :  : "v"(pw3), "v"(o3[3][4]) : "memory"); SB();
  asm volatile("v_mfma_f32_16x16x32_bf16 %0, a[252:255], v[224:227], %0" : "+v"(o3[0][3]) :  : ); SB();
  asm volatile("v_mfma_f32_16x16x32_bf16 %0, a[0:3], v[224:227], %0" : "+v"(o3[0][4]) :  : ); SB();
  asm volatile("v_mfma_f32_16x16x32_bf16 %0, a[4:7], v[240:243], %0" : "+v"(o3[0][0]) :  : ); SB();
  asm volatile("v_mfma_f32_16x16x32_bf16 %0, a[8:11], v[240:243], %0" : "+v"(o3[0][1]) :  : ); SB();
  asm volatile("v_mfma_f32_16x16x32_bf16 %0, a[12:15], v[240:243], %0" : "+v"(o3[0][2]) :  : ); SB();
  asm volatile("v_mfma_f32_16x16x32_bf16 %0, a[16:19], v[240:243], %0" : "+v"(o3[0][3]) :  : ); SB();
  asm volatile("v_mfma_f32_16x16x32_bf16 %0, a[20:23], v[240:243], %0" : "+v"(o3[0][4]) :  : ); SB();
  asm volatile("s_waitcnt lgkmcnt(0)\n\ts_barrier" :  :  : "memory"); SB();
  asm volatile("ds_read_b128 %0, %1 offset:0" : "=&v"(o3[1][0]) : "v"(p3[0]) : "memory"); SB();
  asm volatile("ds_read_b128 %0, %1 offset:5120" : "=&v"(o3[2][0]) : "v"(p3[1]) : "memory"); SB();
  asm volatile("ds_read_b128 %0, %1 offset:10240" : "=&v"(o3[3][0]) : "v"(p3[2]) : "memory"); SB();
  asm volatile("ds_read_b128 %0, %1 offset:1024" : "=&v"(o3[1][1]) : "v"(p3[0]) : "memory"); SB();
  asm volatile("ds_read_b128 %0, %1 offset:6144" : "=&v"(o3[2][1]) : "v"(p3[1]) : "memory"); SB();
  asm volatile("ds_read_b128 %0, %1 offset:11264" : "=&v"(o3[3][1]) : "v"(p3[2]) : "memory"); SB();
  asm volatile("ds_read_b128 %0, %1 offset:2048" : "=&v"(o3[1][2]) : "v"(p3[0]) : "memory"); SB();
  asm volatile("ds_read_b128 %0, %1 offset:7168" : "=&v"(o3[2][2]) : "v"(p3[1]) : "memory"); SB();
  asm volatile("ds_read_b128 %0, %1 offset:12288" : "=&v"(o3[3][2]) : "v"(p3[2]) : "memory"); SB();
  asm volatile("ds_read_b128 %0, %1 offset:3072" : "=&v"(o3[1][3]) : "v"(p3[0]) : "memory"); SB();
  asm volatile("ds_read_b128 %0, %1 offset:8192" : "=&v"(o3[2][3]) : "v"(p3[1]) : "memory"); SB();
  asm volatile("ds_read_b128 %0, %1 offset:13312" : "=&v"(o3[3][3]) : "v"(p3[2]) : "memory"); SB();
  asm volatile("ds_read_b128 %0, %1 offset:4096" : "=&v"(o3[1][4]) : "v"(p3[0]) : "memory"); SB();
  asm volatile("ds_read_b128 %0, %1 offset:9216" : "=&v"(o3[2][4]) : "v"(p3[1]) : "memory"); SB();
  asm volatile("ds_read_b128 %0, %1 offset:14336" : "=&v"(o3[3][4]) : "v"(p3[2]) : "memory"); SB();
  ST(21);
  u32x2 yr[5];
  asm volatile("s_waitcnt lgkmcnt(9)" : "+v"(o3[0][0]), "+v"(o3[0][1]), "+v"(o3[1][0]), "+v"(o3[1][1]), "+v"(o3[2][0]), "+v"(o3[2][1]), "+v"(o3[3][0]), "+v"(o3[3][1]) :  : "memory"); SB();
#pragma unroll
  for (int j = 0; j <= 1; ++j) {
    const f32x4 o4 = (o3[0][j] + o3[1][j]) + (o3[2][j] + o3[3][j]);
#pragma unroll
    for (int h = 0; h < 2; ++h) {
      const f32x2 o = {o4[2 * h], o4[2 * h + 1]};
      const f32x2 a = o * (f32x2){-1.4426950f, -1.4426950f};
      const f32x2 e = (f32x2){__builtin_amdgcn_exp2f(a[0]), __builtin_amdgcn_exp2f(a[1])} + (f32x2){1.0f, 1.0f};
      const f32x2 r = {__builtin_amdgcn_rcpf(e[0]), __builtin_amdgcn_rcpf(e[1])};
      sv[j][h] = (sv[j][h] * o) * r;
    }
  }
  asm volatile("" : "+v"(sv[0][0]), "+v"(sv[0][1]), "+v"(sv[1][0]), "+v"(sv[1][1]));
  SB();
  asm volatile("s_waitcnt lgkmcnt(0)" : "+v"(o3[0][2]), "+v"(o3[0][3]), "+v"(o3[1][2]), "+v"(o3[1][3]), "+v"(o3[2][2]), "+v"(o3[2][3]), "+v"(o3[3][2]), "+v"(o3[3][3]) :  : "memory"); SB();
#pragma unroll
  for (int j = 2; j <= 3; ++j) {
    const f32x4 o4 = (o3[0][j] + o3[1][j]) + (o3[2][j] + o3[3][j]);
#pragma unroll
    for (int h = 0; h < 2; ++h) {
      const f32x2 o = {o4[2 * h], o4[2 * h + 1]};
      const f32x2 a = o * (f32x2){-1.4426950f, -1.4426950f};
      const f32x2 e = (f32x2){__builtin_amdgcn_exp2f(a[0]), __builtin_amdgcn_exp2f(a[1])} + (f32x2){1.0f, 1.0f};
      const f32x2 r = {__builtin_amdgcn_rcpf(e[0]), __builtin_amdgcn_rcpf(e[1])};
      sv[j][h] = (sv[j][h] * o) * r;
    }
  }
  asm volatile("" : "+v"(sv[2][0]), "+v"(sv[2][1]), "+v"(sv[3][0]), "+v"(sv[3][1]));
  SB();
  ST(22);
  asm volatile("s_waitcnt vmcnt(0)\n\t.Lc_%=:\n\tv_xnor_b32 v167, v167, %6\n\tv_xnor_b32 v171, v171, %7\n\tv_xnor_b32 v175, v175, %8\n\tv_max3_u32 %1, v167, v171, v175\n\tv_cmp_eq_u32 vcc, -1, %1\n\ts_cbranch_vccnz .Lr_%=\n\t.Ld_%=:\n\t.subsection 1\n\t.Lr_%=:\n\tv_add_u32 %0, -1, %0\n\ts_nop 1\n\tv_readfirstlane_b32 vcc_hi, %0\n\ts_nop 3\n\ts_cmp_lt_i32 vcc_hi, 1\n\ts_cbranch_scc1 .Ld_%=\n\ts_sleep 2\n\tbuffer_load_dwordx4 v[164:167], %2, %3, 0 offen sc1\n\tbuffer_load_dwordx4 v[168:171], %2, %3, %4 offen sc1\n\tbuffer_load_dwordx4 v[172:175], %2, %3, %5 offen sc1\n\ts_waitcnt vmcnt(0)\n\ts_branch .Lc_%=\n\t.subsection 0" : "+v"(tmo), "=&v"(vtmp) : "v"(vKV), "s"(rWS), "s"(w3k[0]), "s"(w3k[1]), "v"(kz[0]), "v"(kz[1]), "v"(kz[2]) : "memory", "vcc", "scc"); SB();
  ST(31);
  asm volatile("ds_write_b128 %0, v[164:167] offset:0" :  : "v"(yw) : "memory"); SB();
  asm volatile("ds_write_b128 %0, v[168:171] offset:1024" :  : "v"(yw) : "memory"); SB();
  asm volatile("ds_write_b128 %0, v[172:175] offset:2048" :  : "v"(yw) : "memory"); SB();
  asm volatile("s_waitcnt lgkmcnt(0)\n\ts_barrier" :  :  : "memory"); SB();
  asm volatile("ds_read_b64_tr_b16 %0, %1 offset:0" : "=&v"(yr[0]) : "v"(ya) : "memory"); SB();
  asm volatile("ds_read_b64_tr_b16 %0, %1 offset:2048" : "=&v"(yr[1]) : "v"(ya) : "memory"); SB();
  asm volatile("ds_read_b64_tr_b16 %0, %1 offset:4096" : "=&v"(yr[2]) : "v"(ya) : "memory"); SB();
  asm volatile("ds_read_b64_tr_b16 %0, %1 offset:6144" : "=&v"(yr[3]) : "v"(ya) : "memory"); SB();
  asm volatile("ds_read_b64_tr_b16 %0, %1 offset:8192" : "=&v"(yr[4]) : "v"(ya) : "memory"); SB();
  asm volatile("" : "+v"(o3[0][4]), "+v"(o3[1][4]), "+v"(o3[2][4]), "+v"(o3[3][4]) :  : "memory"); SB();
#pragma unroll
  for (int j = 4; j <= 4; ++j) {
    const f32x4 o4 = (o3[0][j] + o3[1][j]) + (o3[2][j] + o3[3][j]);
#pragma unroll
    for (int h = 0; h < 2; ++h) {
      const f32x2 o = {o4[2 * h], o4[2 * h + 1]};
      const f32x2 a = o * (f32x2){-1.4426950f, -1.4426950f};
      const f32x2 e = (f32x2){__builtin_amdgcn_exp2f(a[0]), __builtin_amdgcn_exp2f(a[1])} + (f32x2){1.0f, 1.0f};
      const f32x2 r = {__builtin_amdgcn_rcpf(e[0]), __builtin_amdgcn_rcpf(e[1])};
      sv[j][h] = (sv[j][h] * o) * r;
    }
  }
  asm volatile("" : "+v"(sv[4][0]), "+v"(sv[4][1]));
  SB();
  asm volatile("s_waitcnt lgkmcnt(0)" : "+v"(yr[0]), "+v"(yr[1]), "+v"(yr[2]), "+v"(yr[3]), "+v"(yr[4]) :  : "memory"); SB();
  {
    unsigned pk[5][2];
#pragma unroll
    for (int n = 0; n < 10; ++n) pk[n >> 1][n & 1] = pk2(__builtin_elementwise_fma(sv[n >> 1][n & 1], unpk(yr[n >> 1][n & 1]), xf[n >> 1][n & 1]));
#pragma unroll
    for (int q = 0; q < 2; ++q) {
      const auto s0 = __builtin_amdgcn_permlane16_swap(pk[2 * q][0], pk[2 * q + 1][0], false, false);
      const auto s1 = __builtin_amdgcn_permlane16_swap(pk[2 * q][1], pk[2 * q + 1][1], false, false);
      asm volatile("s_nop 1\n\tglobal_store_dwordx4 %0, %1, %2 offset:%3 " :: "v"(po), "v"((u32x4){s0[0], s1[0], s0[1], s1[1]}), "s"(out), "i"(64 * q) : "memory");
    }
    asm volatile("global_store_dwordx2 %0, %1, %2 offset:128 " :: "v"(vX), "v"((u32x2){pk[4][0], pk[4][1]}), "s"(out) : "memory");
  }
  ST(23);
  ST_OUT();
  }}}
}

extern "C" __global__ void k_end() { asm volatile(".subsection 2\n.Lcode_end:\n\ts_endpgm\n\t.subsection 0"); }

extern "C" void kernel_launch(void* const* d_in, const int* in_sizes, int n_in,
                              void* d_out, int out_size, void* d_ws, size_t ws_size,
                              hipStream_t stream) {
  (void)hipFuncSetAttribute((const void*)k_all, hipFuncAttributeMaxDynamicSharedMemorySize, 163840);
  k_all<<<256, 256, 163840, stream>>>((const char*)d_in[0], (const char*)d_in[1], (const char*)d_in[2], (const char*)d_in[3], (const char*)d_in[4],
                                      (const char*)d_in[5], (const char*)d_in[7], (const char*)d_in[8], (const char*)d_in[9], (const char*)d_in[10],
                                      (char*)d_out, (char*)d_ws);
}
```

```cpp
#include <hip/hip_runtime.h>
#include <stdint.h>

typedef unsigned short u16;
typedef __attribute__((ext_vector_type(4))) float f32x4;
typedef __attribute__((ext_vector_type(2))) float f32x2;
typedef __attribute__((ext_vector_type(4))) unsigned int u32x4;
typedef __attribute__((ext_vector_type(2))) unsigned int u32x2;
typedef __attribute__((ext_vector_type(2))) __bf16 bf16x2n;
typedef __attribute__((ext_vector_type(8))) unsigned long long u64x8;
typedef __attribute__((ext_vector_type(4))) unsigned long long u64x4;
#define LDS_AS __attribute__((address_space(3)))
#define GAS __attribute__((address_space(1)))
#define SB() __builtin_amdgcn_sched_barrier(0)
#define DI __device__ __forceinline__

#define SGA
#define ST_DECL do {} while (0)
#define ST(i) do {} while (0)
#define ST_OUT() do {} while (0)

constexpr int S_ = 2048, D_ = 640;
constexpr unsigned HOLE = 0xFFFFFFFFu;
constexpr int W_ST = 4096, W_FZ = 768 << 10, W_FK = W_FZ + 8192, W_FH = W_FK + 8192, W_KV = 1 << 20, W_Z2 = 4 << 20, W_HH = 11 << 20, W_WO = 14 << 20;

#define wm (w >> 1)
#define wn (w & 1)
#define g4 (lane >> 4)
#define l15 (lane & 15)
#define q4 ((lane >> 2) & 3)
#define p4 (lane & 3)
#define l16 (lane * 16)
#define nch (3 - (rb >> 4))
#define ch0 (cb * 80 + rb * 2 + min(rb, 16u))
#define c2o ((nch - 2) * 2)
#define c2s (8192u + ((rb >> 4) << 30))
#define w3s ((w + 1) >> 2 << 30)
#define vZ (W_Z2 + ((rb * 2 + wm) * 64 + cb * 6 + wn * 3) * 1024 + l16)
#define vH (W_HH + ((rb * 4 + w) * 24 + cb * 3) * 1024 + l16)

DI unsigned pack2(float lo, float hi) {
  f32x2 v = {lo, hi};
  return __builtin_bit_cast(unsigned, __builtin_convertvector(v, bf16x2n));
}
DI float lo16(unsigned w) { return __uint_as_float(w << 16); }
DI float hi16(unsigned w) { return __uint_as_float(w & 0xFFFF0000u); }
DI float rbf(float f) { return lo16(pack2(f, f)); }
DI f32x2 unpk(unsigned w) { f32x2 r = {lo16(w), hi16(w)}; return r; }
DI unsigned pk2(f32x2 v) { return pack2(v[0], v[1]); }
DI unsigned nohole(unsigned a) { return a < HOLE - 1 ? a : HOLE - 1; }
template <int CTRL> DI float dppf(float v) {
  return __builtin_bit_cast(float, __builtin_amdgcn_update_dpp(0, __builtin_bit_cast(int, v), CTRL, 0xF, 0xF, true));
}
DI float red16(float v) {
  const auto a = __builtin_amdgcn_permlane16_swap(__float_as_uint(v), __float_as_uint(v), false, false);
  return __uint_as_float(a[0]) + __uint_as_float(a[1]);
}
DI float red32(float v) {
  const auto c = __builtin_amdgcn_permlane32_swap(__float_as_uint(v), __float_as_uint(v), false, false);
  return __uint_as_float(c[0]) + __uint_as_float(c[1]);
}
DI float sel32(float a, float b) {
  const auto c = __builtin_amdgcn_permlane32_swap(__float_as_uint(a), __float_as_uint(b), false, false);
  return __uint_as_float(c[0]) + __uint_as_float(c[1]);
}
DI unsigned lds_addr(const void* p) { return (unsigned)(uintptr_t)(LDS_AS const char*)p; }

__global__ __launch_bounds__(256, 1) __attribute__((amdgpu_num_vgpr(160))) SGA void k_all(
    const char*, const char*, const char*, const char*, const char*, const char*, const char*, const char*, const char*, const char*, char*, char*) {
  extern __shared__ __attribute__((aligned(16))) char smem[];
  ST_DECL;
  ST(0);
  u64x8 ka0; u64x4 ka1;
  asm volatile("s_load_dwordx16 %0, %2, 0x0\n\ts_load_dwordx8 %1, %2, 0x40" : "=&s"(ka0), "=&s"(ka1) : "s"(__builtin_amdgcn_kernarg_segment_ptr()) : "memory"); SB();
  const unsigned tid = threadIdx.x, lane0 = tid & 63, w0 = __builtin_amdgcn_readfirstlane(tid >> 6);
  const unsigned b = blockIdx.x, xcd = b & 7, idx = b >> 3;
  const unsigned rb0 = 8 * (xcd >> 1) + (idx >> 2), cb0 = 4 * (xcd & 1) + (idx & 3);
  const unsigned lb = lds_addr(smem);
  const unsigned lane = lane0, w = w0, rb = rb0, cb = cb0;
  {
    unsigned long long pc; unsigned clen;
    asm volatile("s_getpc_b64 %0\n.Lpc_here_%=:\n\ts_mov_b32 %1, .Lcode_end-.Lpc_here_%=" : "=s"(pc), "=s"(clen));
    const unsigned o = min((((lane & 3) * 4 + w) * 32 + idx) * 128, clen - 4u) & ~3u;
    asm volatile("s_mov_b64 exec, 15\n\tglobal_load_dword v254, %0, %1\n\ts_mov_b64 exec, -1" :: "v"(o), "s"(pc) : "memory");
    SB();
  }
  unsigned voA = (rb * 64 + w * 16) * 1280 + __umul24(lane >> 3, 1280) + (((lane & 7) ^ (lane >> 3)) << 4), voB[3];
#pragma unroll
  for (int i = 0; i < 3; ++i) { const unsigned r = __umul24(i * 64 + lane, 171) >> 11; voB[i] = w * 49152 + cb * 192 + __umul24(r, 2880) + ((lane ^ ((r >> 3) << 1)) << 4); }
  unsigned mX = lb + w * 2048, mW = lb + w * 3072, mA;
  const unsigned rot = ((rb ^ cb) & 3);
  unsigned kx[10], kw_[10], voA2 = voA + 9216;
  auto kofs = [&](int s) {
    asm volatile("s_xor_b32 %0, %2, %3\n\ts_mul_i32 %1, %0, 0x30000\n\ts_lshl_b32 %0, %0, 7" : "=&s"(kx[s]), "=&s"(kw_[s]) : "s"(s < 8 ? rot : rot & 1), "i"(s) : "scc");
  };
  kofs(0); kofs(1);
  asm volatile("" : "+v"(voA), "+v"(voA2), "+v"(voB[0]), "+v"(voB[1]), "+v"(voB[2]));
  asm volatile("" : "+s"(mX), "+s"(mW)); SB();
  asm volatile("s_waitcnt lgkmcnt(0)" : "+s"(ka0), "+s"(ka1) :: "memory"); SB();
  const char *x = (const char*)ka0[0], *kin = (const char*)ka0[1], *vin = (const char*)ka0[2], *shift_k = (const char*)ka0[3], *ssm_k = (const char*)ka0[4];
  const char *ssm_D = (const char*)ka0[5], *w_in = (const char*)ka0[6], *w_sp = (const char*)ka0[7], *b_sp = (const char*)ka1[0], *w_out = (const char*)ka1[1];
  char *out = (char*)ka1[2], *ws = (char*)ka1[3];
  const auto rX = __builtin_amdgcn_make_buffer_rsrc((void*)x, 0, S_ * D_ * 2, 0x00020000);
  const auto rW = __builtin_amdgcn_make_buffer_rsrc((void*)(w_in + 1536), 0, 640 * 3072 - 1536, 0x00020000);
  asm volatile("; named registers" :  :  : "v255", "a255"); SB();
  asm volatile("s_add_u32 m0, %0, 81920" :  : "s"(mX) : "memory", "scc"); SB();
  asm volatile("s_nop 0\n\tbuffer_load_dwordx4 %0, %1, %2 offen lds" :  : "v"(voA), "s"(rX), "s"(kx[0]) : "memory"); SB();
  asm volatile("buffer_load_dwordx4 %0, %1, %2 offen offset:1024 lds" :  : "v"(voA2), "s"(rX), "s"(kx[0]) : "memory"); SB();
  asm volatile("s_add_u32 m0, %0, 0" :  : "s"(mW) : "memory", "scc"); SB();
  asm volatile("s_nop 0\n\tbuffer_load_dwordx4 %0, %1, %2 offen lds" :  : "v"(voB[0]), "s"(rW), "s"(kw_[0]) : "memory"); SB();
  asm volatile("buffer_load_dwordx4 %0, %1, %2 offen offset:1024 lds" :  : "v"(voB[1]), "s"(rW), "s"(kw_[0]) : "memory"); SB();
  asm volatile("buffer_load_dwordx4 %0, %1, %2 offen offset:2048 lds" :  : "v"(voB[2]), "s"(rW), "s"(kw_[0]) : "memory"); SB();
  asm volatile("s_add_u32 m0, %0, 90112" :  : "s"(mX) : "memory", "scc"); SB();
  asm volatile("s_nop 0\n\tbuffer_load_dwordx4 %0, %1, %2 offen lds" :  : "v"(voA), "s"(rX), "s"(kx[1]) : "memory"); SB();
  asm volatile("buffer_load_dwordx4 %0, %1, %2 offen offset:1024 lds" :  : "v"(voA2), "s"(rX), "s"(kx[1]) : "memory"); SB();
  kofs(2); kofs(3); kofs(4); kofs(5); kofs(6); kofs(7); kofs(8); kofs(9);
  const auto rWS = __builtin_amdgcn_make_buffer_rsrc((void*)ws, 0, 32 << 20, 0x00020000);
  const auto rW1 = __builtin_amdgcn_make_buffer_rsrc((void*)w_in, 0, 640 * 3072, 0x00020000);
  mA = lb + w * 8192; asm volatile("" : "+s"(mA));
  { unsigned lane = lane0, w = w0, rb = rb0, cb = cb0;
  asm volatile("" : "+v"(lane)); asm volatile("" : "+s"(w), "+s"(rb), "+s"(cb)); SB();

  unsigned ao[8];
  u32x2 kp[5], kc[5], vv[5], sw[5], wt[2], ko[5];
  u32x4 kq[3];
  f32x2 kt[2], kf[2];
  unsigned ku[5];
  unsigned aX[2][2], aW[2][2][3];
#pragma unroll
  for (int n = 0; n < 4; ++n) aX[n >> 1][n & 1] = lb + 98304 + (wm * 32 + ((n & 1) ^ wn) * 16 + l15) * 128 + (((((n >> 1) ^ wm) * 4 + g4) ^ (l15 & 7)) << 4);
#pragma unroll
  for (int n = 0; n < 6; ++n) {
    aW[0][n / 3][n % 3] = lb + ((n / 3 ^ wm) * 32 + 8 * g4 + q4) * 192 + (((wn * 3 + n % 3) ^ (g4 & 1)) << 5) + 8 * p4;
    aW[1][n / 3][n % 3] = aW[0][n / 3][n % 3] + 49152;
    asm volatile("" : "+v"(aW[0][n / 3][n % 3]), "+v"(aW[1][n / 3][n % 3]));
  }
  f32x4 co[3];
  u32x4 ones = {0x3F803F80u, 0x3F803F80u, 0x3F803F80u, 0x3F803F80u};
  f32x4 a2[2][3], a1[2][3], rs, gr, c2[3], c1[3];
  u32x2 zt[3][2];
  asm volatile("" : "+v"(ones), "+v"(aX[0][0]), "+v"(aX[0][1]), "+v"(aX[1][0]), "+v"(aX[1][1]));
  int tmo = (1 << 18);
  u32x2 fk, fz, fh, fi;
  unsigned tg0, tg1;
  float xm[2], xd[2], k1[2], k2[2];
  f32x4 xs[2];
  float xt[4];
  u32x2 xv[5];
  unsigned mU, uso, w3k[2], sFK, sFZ;
  float dD[3], bs;
  u32x4 im[6], zero4;
  SB();
  asm volatile("s_add_u32 m0, %0, 12288" :  : "s"(mW) : "memory", "scc"); SB();
  asm volatile("s_nop 0\n\tbuffer_load_dwordx4 %0, %1, %2 offen lds" :  : "v"(voB[0]), "s"(rW), "s"(kw_[1]) : "memory"); SB();
  asm volatile("buffer_load_dwordx4 %0, %1, %2 offen offset:1024 lds" :  : "v"(voB[1]), "s"(rW), "s"(kw_[1]) : "memory"); SB();
  asm volatile("buffer_load_dwordx4 %0, %1, %2 offen offset:2048 lds" :  : "v"(voB[2]), "s"(rW), "s"(kw_[1]) : "memory"); SB();
  const auto rK = __builtin_amdgcn_make_buffer_rsrc((void*)kin, 0, S_ * D_ * 2, 0x00020000);
  const auto rV = __builtin_amdgcn_make_buffer_rsrc((void*)vin, 0, S_ * D_ * 2, 0x00020000);
  const auto rS = __builtin_amdgcn_make_buffer_rsrc((void*)(shift_k - 3328), 0, D_ * 8 + 3328, 0x00020000);
  const auto rO = __builtin_amdgcn_make_buffer_rsrc((void*)(w_out - 3072), 0, 768 * 1280 + 3072, 0x00020000);
  unsigned voK[4], voV[3], voH = cb * 640 + l16;
#pragma unroll
  for (int n = 0; n < 4; ++n) { const unsigned o = n * 64 + lane, r = (o * 205) >> 11; voK[n] = (rb * 64 + w * 16 - 4) * 1280 + cb * 160 + r * 1120 + lane * 16; }
#pragma unroll
  for (int n = 0; n < 3; ++n) voV[n] = voK[n] + 5120;
  const unsigned wof = (rb >> 3) * 30 + min((rb & 7) * 4 + w, 29u), wok = (wof * 205) >> 10;
  unsigned voO = (lane >> 1) * 1280 + (lane & 1) * 16 + wok * 40960 + (wof - 5 * wok) * 32 + cb * 160, vWO = W_WO + (cb * 120 + wof) * 1024 + l16;
  asm volatile("" : "+v"(vWO), "+v"(voK[0]), "+v"(voK[1]), "+v"(voK[2]), "+v"(voK[3]), "+v"(voV[0]), "+v"(voV[1]), "+v"(voV[2]), "+v"(voH), "+v"(voO));
  SB();
  const auto rF = __builtin_amdgcn_make_buffer_rsrc((void*)ssm_k, 0, S_ * D_ * 2, 0x00020000);
  unsigned vF = ch0 * 4096 + w * 1024 + l16, c2s_ = c2s;
  unsigned hpo[3], vD = ch0 * 2, vD2 = vD + c2o * 2 - 4, vBs = (rb * 64 + w * 16 + l15) * 2;
  hpo[0] = w ? ch0 * 4096 + w * 1024 - 4 : 0x40000000u; hpo[1] = hpo[0] + 4096; hpo[2] = hpo[0] + c2s;
  asm volatile("" : "+s"(hpo[0]), "+s"(hpo[1]), "+s"(hpo[2]), "+v"(vD), "+v"(vD2), "+v"(vBs), "+v"(vF), "+s"(c2s_));
  SB();
  unsigned ka = lb + 98304 + w * 8192 + (4 * p4 + q4) * 160 + 8 * g4, oa = lb + 98304 + 7168 + w * 8192 + (4 * g4 + q4) * 32 + 8 * p4;
  unsigned sa = lb + 98304 + 3328 + w * 8192 + (4 * g4 + p4) * 8;
  unsigned kw = lb + 12288 + (4 * g4 + p4) * 144 + w * 32 + q4 * 8, kr = lb + 12288 + (w * 24 + (lane >> 3)) * 144 + (lane & 7) * 16;
  unsigned vKV = W_KV + (cb * 80 + w * 24 + (lane >> 3)) * 4096 + rb * 128 + (lane & 7) * 16;
  unsigned vS = W_ST + (rb * 2 + wm) * 8192 + l16;
  asm volatile("" : "+v"(ka), "+v"(oa), "+v"(sa), "+v"(kw), "+v"(kr), "+v"(vKV), "+v"(vS));
  SB();
  ST(1);
  SB();
  asm volatile("s_add_u32 m0, %0, 98304" :  : "s"(mX) : "memory", "scc"); SB();
  asm volatile("s_nop 0\n\tbuffer_load_dwordx4 %0, %1, %2 offen lds" :  : "v"(voA), "s"(rX), "s"(kx[2]) : "memory"); SB();
  asm volatile("buffer_load_dwordx4 %0, %1, %2 offen offset:1024 lds" :  : "v"(voA2), "s"(rX), "s"(kx[2]) : "memory"); SB();
  asm volatile("buffer_load_dwordx2 v[252:253], off, %0, 0 sc1" :  : "s"(rWS) : "memory"); SB();
  asm volatile("s_add_u32 m0, %0, 106496" :  : "s"(mX) : "memory", "scc"); SB();
  asm volatile("s_nop 0\n\tbuffer_load_dwordx4 %0, %1, %2 offen lds" :  : "v"(voA), "s"(rX), "s"(kx[3]) : "memory"); SB();
  asm volatile("buffer_load_dwordx4 %0, %1, %2 offen offset:1024 lds" :  : "v"(voA2), "s"(rX), "s"(kx[3]) : "memory"); SB();
  asm volatile("s_add_u32 m0, %0, 24576" :  : "s"(mW) : "memory", "scc"); SB();
  asm volatile("s_nop 0\n\tbuffer_load_dwordx4 %0, %1, %2 offen lds" :  : "v"(voB[0]), "s"(rW), "s"(kw_[2]) : "memory"); SB();
  asm volatile("s_waitcnt vmcnt(11) lgkmcnt(0)\n\ts_barrier" :  :  : "memory"); SB();
  ST(4);
  asm volatile("ds_read_b128 a[0:3], %0 offset:0" :  : "v"((aX[0][0] - 16384)) : "memory"); SB();
  asm volatile("ds_read_b128 a[4:7], %0 offset:0" :  : "v"((aX[0][1] - 16384)) : "memory"); SB();
  asm volatile("ds_read_b64_tr_b16 v[160:161], %0 offset:0" :  : "v"(aW[0][0][0]) : "memory"); SB();
  asm volatile("ds_read_b64_tr_b16 v[162:163], %0 offset:768" :  : "v"(aW[0][0][0]) : "memory"); SB();
  asm volatile("ds_read_b64_tr_b16 v[164:165], %0 offset:0" :  : "v"(aW[0][0][1]) : "memory"); SB();
  asm volatile("ds_read_b64_tr_b16 v[166:167], %0 offset:768" :  : "v"(aW[0][0][1]) : "memory"); SB();
  asm volatile("ds_read_b64_tr_b16 v[168:169], %0 offset:0" :  : "v"(aW[0][0][2]) : "memory"); SB();
  asm volatile("ds_read_b64_tr_b16 v[170:171], %0 offset:768" :  : "v"(aW[0][0][2]) : "memory"); SB();
  asm volatile("ds_read_b128 a[8:11], %0 offset:0" :  : "v"((aX[1][0] - 16384)) : "memory"); SB();
  asm volatile("ds_read_b128 a[12:15], %0 offset:0" :  : "v"((aX[1][1] - 16384)) : "memory"); SB();
  asm volatile("s_waitcnt lgkmcnt(6)\n\tv_mfma_f32_16x16x32_bf16 %0, v[160:163], a[0:3], 0" : "=&v"(a2[0][0]) :  : ); SB();
  asm volatile("ds_read_b64_tr_b16 v[172:173], %0 offset:0" :  : "v"(aW[0][1][0]) : "memory"); SB();
  asm volatile("buffer_load_dword v242, off, %0, %1" :  : "s"(rF), "s"(hpo[0]) : "memory"); SB();
  asm volatile("v_mfma_f32_16x16x32_bf16 %0, v[160:163], a[4:7], 0" : "=&v"(a2[1][0]) :  : ); SB();
  asm volatile("ds_read_b64_tr_b16 v[174:175], %0 offset:768" :  : "v"(aW[0][1][0]) : "memory"); SB();
  asm volatile("buffer_load_dwordx4 %0, %1, %2 offen offset:1024 lds" :  : "v"(voB[1]), "s"(rW), "s"(kw_[2]) : "memory"); SB();
  asm volatile("v_mfma_f32_16x16x32_bf16 %0, v[160:163], %1, 0" : "=&v"(c2[0]) : "v"(ones) : ); SB();
  asm volatile("ds_read_b64_tr_b16 v[176:177], %0 offset:0" :  : "v"(aW[0][1][1]) : "memory"); SB();
  asm volatile("s_waitcnt lgkmcnt(5)\n\tv_mfma_f32_16x16x32_bf16 %0, v[164:167], a[0:3], 0" : "=&v"(a2[0][1]) :  : ); SB();
  asm volatile("ds_read_b64_tr_b16 v[178:179], %0 offset:768" :  : "v"(aW[0][1][1]) : "memory"); SB();
  asm volatile("v_mfma_f32_16x16x32_bf16 %0, v[164:167], a[4:7], 0" : "=&v"(a2[1][1]) :  : ); SB();
  asm volatile("ds_read_b64_tr_b16 v[180:181], %0 offset:0" :  : "v"(aW[0][1][2]) : "memory"); SB();
  asm volatile("buffer_load_dwordx4 %0, %1, %2 offen offset:2048 lds" :  : "v"(voB[2]), "s"(rW), "s"(kw_[2]) : "memory"); SB();
  asm volatile("v_mfma_f32_16x16x32_bf16 %0, v[164:167], %1, 0" : "=&v"(c2[1]) : "v"(ones) : ); SB();
  asm volatile("ds_read_b64_tr_b16 v[182:183], %0 offset:768" :  : "v"(aW[0][1][2]) : "memory"); SB();
  asm volatile("v_mfma_f32_16x16x32_bf16 %0, v[168:171], a[0:3], 0" : "=&v"(a2[0][2]) :  : ); SB();
  asm volatile("s_waitcnt vmcnt(9)\n\ts_barrier" :  :  : "memory"); SB();
  asm volatile("ds_read_b64_tr_b16 v[184:185], %0 offset:12288" :  : "v"(aW[0][0][0]) : "memory"); SB();
  asm volatile("global_load_ushort v245, %0, %1 offset:0" :  : "v"(vD), "s"(ssm_D) : "memory"); SB();
  asm volatile("v_mfma_f32_16x16x32_bf16 %0, v[168:171], a[4:7], 0" : "=&v"(a2[1][2]) :  : ); SB();
  asm volatile("ds_read_b64_tr_b16 v[186:187], %0 offset:13056" :  : "v"(aW[0][0][0]) : "memory"); SB();
  asm volatile("s_add_u32 m0, %0, 114688" :  : "s"(mX) : "memory", "scc"); SB();
  asm volatile("v_mfma_f32_16x16x32_bf16 %0, v[168:171], %1, 0" : "=&v"(c2[2]) : "v"(ones) : ); SB();
  asm volatile("ds_read_b64_tr_b16 v[188:189], %0 offset:12288" :  : "v"(aW[0][0][1]) : "memory"); SB();
  asm volatile("buffer_load_dwordx4 %0, %1, %2 offen lds" :  : "v"(voA), "s"(rX), "s"(kx[4]) : "memory"); SB();
  asm volatile("v_mfma_f32_16x16x32_bf16 %0, %1, a[0:3], 0" : "=&v"(rs) : "v"(ones) : ); SB();
  asm volatile("ds_read_b128 a[16:19], %0 offset:8192" :  : "v"((aX[0][0] - 16384)) : "memory"); SB();
  asm volatile("ds_read_b128 a[20:23], %0 offset:8192" :  : "v"((aX[0][1] - 16384)) : "memory"); SB();
  asm volatile("ds_read_b64_tr_b16 v[190:191], %0 offset:13056" :  : "v"(aW[0][0][1]) : "memory"); SB();
  asm volatile("buffer_load_dwordx4 %0, %1, %2 offen offset:1024 lds" :  : "v"(voA2), "s"(rX), "s"(kx[4]) : "memory"); SB();
  asm volatile("v_mfma_f32_16x16x32_bf16 %0, a[0:3], a[0:3], 0" : "=&v"(gr) :  : ); SB();
  asm volatile("ds_read_b64_tr_b16 v[192:193], %0 offset:12288" :  : "v"(aW[0][0][2]) : "memory"); SB();
  asm volatile("s_waitcnt lgkmcnt(7)\n\tv_mfma_f32_16x16x32_bf16 %0, v[172:175], a[8:11], %0" : "+v"(a2[0][0]) :  : ); SB();
  asm volatile("ds_read_b64_tr_b16 v[194:195], %0 offset:13056" :  : "v"(aW[0][0][2]) : "memory"); SB();
  asm volatile("v_mfma_f32_16x16x32_bf16 %0, v[172:175], a[12:15], %0" : "+v"(a2[1][0]) :  : ); SB();
  asm volatile("ds_read_b128 a[24:27], %0 offset:8192" :  : "v"((aX[1][0] - 16384)) : "memory"); SB();
  asm volatile("s_add_u32 m0, %0, 122880" :  : "s"(mX) : "memory", "scc"); SB();
  asm volatile("v_mfma_f32_16x16x32_bf16 %0, v[176:179], a[8:11], %0" : "+v"(a2[0][1]) :  : ); SB();
  asm volatile("ds_read_b128 a[28:31], %0 offset:8192" :  : "v"((aX[1][1] - 16384)) : "memory"); SB();
  asm volatile("v_mfma_f32_16x16x32_bf16 %0, v[176:179], a[12:15], %0" : "+v"(a2[1][1]) :  : ); SB();
  asm volatile("buffer_load_dwordx4 %0, %1, %2 offen lds" :  : "v"(voA), "s"(rX), "s"(kx[5]) : "memory"); SB();
  asm volatile("v_mfma_f32_16x16x32_bf16 %0, v[180:183], a[8:11], %0" : "+v"(a2[0][2]) :  : ); SB();
  asm volatile("ds_read_b64_tr_b16 v[160:161], %0 offset:12288" :  : "v"(aW[0][1][0]) : "memory"); SB();
  asm volatile("v_mfma_f32_16x16x32_bf16 %0, v[180:183], a[12:15], %0" : "+v"(a2[1][2]) :  : ); SB();
  asm volatile("ds_read_b64_tr_b16 v[162:163], %0 offset:13056" :  : "v"(aW[0][1][0]) : "memory"); SB();
  asm volatile("v_mfma_f32_16x16x32_bf16 %0, %1, a[8:11], %0" : "+v"(rs) : "v"(ones) : ); SB();
  asm volatile("ds_read_b64_tr_b16 v[164:165], %0 offset:12288" :  : "v"(aW[0][1][1]) : "memory"); SB();
  asm volatile("v_mfma_f32_16x16x32_bf16 %0, a[8:11], a[8:11], %0" : "+v"(gr) :  : ); SB();
  asm volatile("ds_read_b64_tr_b16 v[166:167], %0 offset:13056" :  : "v"(aW[0][1][1]) : "memory"); SB();
  asm volatile("s_waitcnt lgkmcnt(8)\n\tv_mfma_f32_16x16x32_bf16 %0, v[184:187], a[16:19], %0" : "+v"(a2[0][0]) :  : ); SB();
  asm volatile("ds_read_b64_tr_b16 v[168:169], %0 offset:12288" :  : "v"(aW[0][1][2]) : "memory"); SB();
  asm volatile("v_mfma_f32_16x16x32_bf16 %0, v[184:187], a[20:23], %0" : "+v"(a2[1][0]) :  : ); SB();
  asm volatile("ds_read_b64_tr_b16 v[170:171], %0 offset:13056" :  : "v"(aW[0][1][2]) : "memory"); SB();
  asm volatile("v_mfma_f32_16x16x32_bf16 %0, v[184:187], %1, %0" : "+v"(c2[0]) : "v"(ones) : ); SB();
  asm volatile("buffer_load_dword v243, off, %0, %1" :  : "s"(rF), "s"(hpo[1]) : "memory"); SB();
  asm volatile("v_mfma_f32_16x16x32_bf16 %0, v[188:191], a[16:19], %0" : "+v"(a2[0][1]) :  : ); SB();
  asm volatile("buffer_load_dwordx4 %0, %1, %2 offen offset:1024 lds" :  : "v"(voA2), "s"(rX), "s"(kx[5]) : "memory"); SB();
  asm volatile("v_mfma_f32_16x16x32_bf16 %0, v[188:191], a[20:23], %0" : "+v"(a2[1][1]) :  : ); SB();
  asm volatile("s_add_u32 m0, %0, 36864" :  : "s"(mW) : "memory", "scc"); SB();
  asm volatile("s_nop 0\n\tbuffer_load_dwordx4 %0, %1, %2 offen lds" :  : "v"(voB[0]), "s"(rW), "s"(kw_[3]) : "memory"); SB();
  asm volatile("buffer_load_dwordx4 %0, %1, %2 offen offset:1024 lds" :  : "v"(voB[1]), "s"(rW), "s"(kw_[3]) : "memory"); SB();
  asm volatile("buffer_load_dwordx4 %0, %1, %2 offen offset:2048 lds" :  : "v"(voB[2]), "s"(rW), "s"(kw_[3]) : "memory"); SB();
  asm volatile("global_load_ushort v246, %0, %1 offset:2" :  : "v"(vD), "s"(ssm_D) : "memory"); SB();
  asm volatile("s_add_u32 m0, %0, 131072" :  : "s"(mX) : "memory", "scc"); SB();
  asm volatile("s_nop 0\n\tbuffer_load_dwordx4 %0, %1, %2 offen lds" :  : "v"(voA), "s"(rX), "s"(kx[6]) : "memory"); SB();
  asm volatile("buffer_load_dwordx4 %0, %1, %2 offen offset:1024 lds" :  : "v"(voA2), "s"(rX), "s"(kx[6]) : "memory"); SB();
  asm volatile("s_add_u32 m0, %0, 139264" :  : "s"(mX) : "memory", "scc"); SB();
  asm volatile("s_nop 0\n\tbuffer_load_dwordx4 %0, %1, %2 offen lds" :  : "v"(voA), "s"(rX), "s"(kx[7]) : "memory"); SB();
  asm volatile("buffer_load_dword v244, off, %0, %1" :  : "s"(rF), "s"(hpo[2]) : "memory"); SB();
  asm volatile("global_load_ushort v247, %0, %1 offset:4" :  : "v"(vD2), "s"(ssm_D) : "memory"); SB();
  asm volatile("s_waitcnt vmcnt(15)\n\ts_barrier" :  :  : "memory"); SB();
  asm volatile("ds_read_b64_tr_b16 v[172:173], %0 offset:24576" :  : "v"(aW[0][0][0]) : "memory"); SB();
  asm volatile("v_mfma_f32_16x16x32_bf16 %0, v[188:191], %1, %0" : "+v"(c2[1]) : "v"(ones) : ); SB();
  asm volatile("ds_read_b64_tr_b16 v[174:175], %0 offset:25344" :  : "v"(aW[0][0][0]) : "memory"); SB();
  asm volatile("s_waitcnt lgkmcnt(2)\n\tv_mfma_f32_16x16x32_bf16 %0, v[192:195], a[16:19], %0" : "+v"(a2[0][2]) :  : ); SB();
  asm volatile("ds_read_b64_tr_b16 v[176:177], %0 offset:24576" :  : "v"(aW[0][0][1]) : "memory"); SB();
  asm volatile("v_mfma_f32_16x16x32_bf16 %0, v[192:195], a[20:23], %0" : "+v"(a2[1][2]) :  : ); SB();
  asm volatile("ds_read_b64_tr_b16 v[178:179], %0 offset:25344" :  : "v"(aW[0][0][1]) : "memory"); SB();
  asm volatile("v_mfma_f32_16x16x32_bf16 %0, v[192:195], %1, %0" : "+v"(c2[2]) : "v"(ones) : ); SB();
  asm volatile("ds_read_b64_tr_b16 v[180:181], %0 offset:24576" :  : "v"(aW[0][0][2]) : "memory"); SB();
  asm volatile("v_mfma_f32_16x16x32_bf16 %0, %1, a[16:19], %0" : "+v"(rs) : "v"(ones) : ); SB();
  asm volatile("ds_read_b128 a[32:35], %0 offset:0" :  : "v"(aX[0][0]) : "memory"); SB();
  asm volatile("ds_read_b128 a[36:39], %0 offset:0" :  : "v"(aX[0][1]) : "memory"); SB();
  asm volatile("ds_read_b64_tr_b16 v[182:183], %0 offset:25344" :  : "v"(aW[0][0][2]) : "memory"); SB();
  asm volatile("v_mfma_f32_16x16x32_bf16 %0, a[16:19], a[16:19], %0" : "+v"(gr) :  : ); SB();
  asm volatile("ds_read_b128 a[40:43], %0 offset:0" :  : "v"(aX[1][0]) : "memory"); SB();
  asm volatile("v_mfma_f32_16x16x32_bf16 %0, v[160:163], a[24:27], %0" : "+v"(a2[0][0]) :  : ); SB();
  asm volatile("ds_read_b128 a[44:47], %0 offset:0" :  : "v"(aX[1][1]) : "memory"); SB();
  asm volatile("v_mfma_f32_16x16x32_bf16 %0, v[160:163], a[28:31], %0" : "+v"(a2[1][0]) :  : ); SB();
  asm volatile("global_load_ushort v248, %0, %1" :  : "v"(vBs), "s"(b_sp) : "memory"); SB();
  asm volatile("v_mfma_f32_16x16x32_bf16 %0, v[164:167], a[24:27], %0" : "+v"(a2[0][1]) :  : ); SB();
  asm volatile("buffer_load_dwordx4 %0, %1, %2 offen offset:1024 lds" :  : "v"(voA2), "s"(rX), "s"(kx[7]) : "memory"); SB();
  asm volatile("v_mfma_f32_16x16x32_bf16 %0, v[164:167], a[28:31], %0" : "+v"(a2[1][1]) :  : ); SB();
  asm volatile("ds_read_b128 a[48:51], %0 offset:8192" :  : "v"(aX[0][0]) : "memory"); SB();
  asm volatile("v_mfma_f32_16x16x32_bf16 %0, v[168:171], a[24:27], %0" : "+v"(a2[0][2]) :  : ); SB();
  asm volatile("ds_read_b128 a[52:55], %0 offset:8192" :  : "v"(aX[0][1]) : "memory"); SB();
  asm volatile("v_mfma_f32_16x16x32_bf16 %0, v[168:171], a[28:31], %0" : "+v"(a2[1][2]) :  : ); SB();
  asm volatile("s_add_u32 m0, %0, 49152" :  : "s"(mW) : "memory", "scc"); SB();
  asm volatile("ds_read_b128 a[56:59], %0 offset:8192" :  : "v"(aX[1][0]) : "memory"); SB();
  asm volatile("v_mfma_f32_16x16x32_bf16 %0, %1, a[24:27], %0" : "+v"(rs) : "v"(ones) : ); SB();
  asm volatile("ds_read_b64_tr_b16 v[184:185], %0 offset:24576" :  : "v"(aW[0][1][0]) : "memory"); SB();
  asm volatile("buffer_load_dwordx4 %0, %1, %2 offen lds" :  : "v"(voB[0]), "s"(rW), "s"(kw_[4]) : "memory"); SB();
  asm volatile("v_mfma_f32_16x16x32_bf16 %0, a[24:27], a[24:27], %0" : "+v"(gr) :  : ); SB();
  asm volatile("ds_read_b64_tr_b16 v[186:187], %0 offset:25344" :  : "v"(aW[0][1][0]) : "memory"); SB();
  asm volatile("s_waitcnt lgkmcnt(7)\n\tv_mfma_f32_16x16x32_bf16 %0, v[172:175], a[32:35], %0" : "+v"(a2[0][0]) :  : ); SB();
  asm volatile("ds_read_b64_tr_b16 v[188:189], %0 offset:24576" :  : "v"(aW[0][1][1]) : "memory"); SB();
  asm volatile("v_mfma_f32_16x16x32_bf16 %0, v[172:175], a[36:39], %0" : "+v"(a2[1][0]) :  : ); SB();
  asm volatile("ds_read_b64_tr_b16 v[190:191], %0 offset:25344" :  : "v"(aW[0][1][1]) : "memory"); SB();
  asm volatile("v_mfma_f32_16x16x32_bf16 %0, v[172:175], %1, %0" : "+v"(c2[0]) : "v"(ones) : ); SB();
  asm volatile("ds_read_b64_tr_b16 v[192:193], %0 offset:24576" :  : "v"(aW[0][1][2]) : "memory"); SB();
  asm volatile("v_mfma_f32_16x16x32_bf16 %0, v[176:179], a[32:35], %0" : "+v"(a2[0][1]) :  : ); SB();
  asm volatile("ds_read_b64_tr_b16 v[194:195], %0 offset:25344" :  : "v"(aW[0][1][2]) : "memory"); SB();
  asm volatile("v_mfma_f32_16x16x32_bf16 %0, v[176:179], a[36:39], %0" : "+v"(a2[1][1]) :  : ); SB();
  asm volatile("ds_read_b128 a[60:63], %0 offset:8192" :  : "v"(aX[1][1]) : "memory"); SB();
  asm volatile("v_mfma_f32_16x16x32_bf16 %0, v[176:179], %1, %0" : "+v"(c2[1]) : "v"(ones) : ); SB();
  asm volatile("buffer_load_dwordx4 %0, %1, %2 offen offset:1024 lds" :  : "v"(voB[1]), "s"(rW), "s"(kw_[4]) : "memory"); SB();
  asm volatile("v_mfma_f32_16x16x32_bf16 %0, v[180:183], a[32:35], %0" : "+v"(a2[0][2]) :  : ); SB();
  asm volatile("buffer_load_dwordx4 %0, %1, %2 offen offset:2048 lds" :  : "v"(voB[2]), "s"(rW), "s"(kw_[4]) : "memory"); SB();
  asm volatile("v_mfma_f32_16x16x32_bf16 %0, v[180:183], a[36:39], %0" : "+v"(a2[1][2]) :  : ); SB();
  asm volatile("v_mfma_f32_16x16x32_bf16 %0, v[180:183], %1, %0" : "+v"(c2[2]) : "v"(ones) : ); SB();
  asm volatile("s_add_u32 m0, %0, 147456" :  : "s"(mX) : "memory", "scc"); SB();
  asm volatile("s_nop 0\n\tbuffer_load_dwordx4 %0, %1, %2 offen lds" :  : "v"(voA), "s"(rX), "s"(kx[8]) : "memory"); SB();
  asm volatile("buffer_load_dwordx4 %0, %1, %2 offen offset:1024 lds" :  : "v"(voA2), "s"(rX), "s"(kx[8]) : "memory"); SB();
  asm volatile("s_add_u32 m0, %0, 155648" :  : "s"(mX) : "memory", "scc"); SB();
  asm volatile("s_nop 0\n\tbuffer_load_dwordx4 %0, %1, %2 offen lds" :  : "v"(voA), "s"(rX), "s"(kx[9]) : "memory"); SB();
  asm volatile("s_waitcnt vmcnt(14)\n\ts_barrier" :  :  : "memory"); SB();
  asm volatile("ds_read_b64_tr_b16 v[160:161], %0 offset:36864" :  : "v"(aW[0][0][0]) : "memory"); SB();
  asm volatile("v_mfma_f32_16x16x32_bf16 %0, %1, a[32:35], %0" : "+v"(rs) : "v"(ones) : ); SB();
  asm volatile("ds_read_b64_tr_b16 v[162:163], %0 offset:37632" :  : "v"(aW[0][0][0]) : "memory"); SB();
  asm volatile("v_mfma_f32_16x16x32_bf16 %0, a[32:35], a[32:35], %0" : "+v"(gr) :  : ); SB();
  asm volatile("ds_read_b64_tr_b16 v[164:165], %0 offset:36864" :  : "v"(aW[0][0][1]) : "memory"); SB();
  asm volatile("s_waitcnt lgkmcnt(3)\n\tv_mfma_f32_16x16x32_bf16 %0, %1, a[40:43], %0" : "+v"(rs) : "v"(ones) : ); SB();
  asm volatile("ds_read_b64_tr_b16 v[166:167], %0 offset:37632" :  : "v"(aW[0][0][1]) : "memory"); SB();
  asm volatile("v_mfma_f32_16x16x32_bf16 %0, a[40:43], a[40:43], %0" : "+v"(gr) :  : ); SB();
  asm volatile("ds_read_b64_tr_b16 v[168:169], %0 offset:36864" :  : "v"(aW[0][0][2]) : "memory"); SB();
  asm volatile("v_mfma_f32_16x16x32_bf16 %0, v[184:187], a[40:43], %0" : "+v"(a2[0][0]) :  : ); SB();
  asm volatile("ds_read_b64_tr_b16 v[170:171], %0 offset:37632" :  : "v"(aW[0][0][2]) : "memory"); SB();
  asm volatile("v_mfma_f32_16x16x32_bf16 %0, v[184:187], a[44:47], %0" : "+v"(a2[1][0]) :  : ); SB();
  asm volatile("ds_read_b128 a[64:67], %0 offset:16384" :  : "v"(aX[0][0]) : "memory"); SB();
  asm volatile("v_mfma_f32_16x16x32_bf16 %0, v[188:191], a[40:43], %0" : "+v"(a2[0][1]) :  : ); SB();
  asm volatile("buffer_load_dwordx4 %0, %1, %2 offen offset:1024 lds" :  : "v"(voA2), "s"(rX), "s"(kx[9]) : "memory"); SB();
  asm volatile("s_add_u32 m0, %0, 61440" :  : "s"(mW) : "memory", "scc"); SB();
  asm volatile("v_mfma_f32_16x16x32_bf16 %0, v[188:191], a[44:47], %0" : "+v"(a2[1][1]) :  : ); SB();
  asm volatile("ds_read_b128 a[68:71], %0 offset:16384" :  : "v"(aX[0][1]) : "memory"); SB();
  asm volatile("v_mfma_f32_16x16x32_bf16 %0, v[192:195], a[40:43], %0" : "+v"(a2[0][2]) :  : ); SB();
  asm volatile("ds_read_b128 a[72:75], %0 offset:16384" :  : "v"(aX[1][0]) : "memory"); SB();
  asm volatile("v_mfma_f32_16x16x32_bf16 %0, v[192:195], a[44:47], %0" : "+v"(a2[1][2]) :  : ); SB();
  asm volatile("ds_read_b128 a[76:79], %0 offset:16384" :  : "v"(aX[1][1]) : "memory"); SB();
  asm volatile("v_mfma_f32_16x16x32_bf16 %0, %1, a[48:51], %0" : "+v"(rs) : "v"(ones) : ); SB();
  asm volatile("buffer_load_dwordx4 %0, %1, %2 offen lds" :  : "v"(voB[0]), "s"(rW), "s"(kw_[5]) : "memory"); SB();
  asm volatile("v_mfma_f32_16x16x32_bf16 %0, a[48:51], a[48:51], %0" : "+v"(gr) :  : ); SB();
  asm volatile("ds_read_b128 a[80:83], %0 offset:24576" :  : "v"(aX[0][0]) : "memory"); SB();
  asm volatile("v_mfma_f32_16x16x32_bf16 %0, %1, a[56:59], %0" : "+v"(rs) : "v"(ones) : ); SB();
  asm volatile("ds_read_b64_tr_b16 v[172:173], %0 offset:36864" :  : "v"(aW[0][1][0]) : "memory"); SB();
  asm volatile("v_mfma_f32_16x16x32_bf16 %0, a[56:59], a[56:59], %0" : "+v"(gr) :  : ); SB();
  asm volatile("ds_read_b64_tr_b16 v[174:175], %0 offset:37632" :  : "v"(aW[0][1][0]) : "memory"); SB();
  asm volatile("s_waitcnt lgkmcnt(7)\n\tv_mfma_f32_16x16x32_bf16 %0, v[160:163], a[48:51], %0" : "+v"(a2[0][0]) :  : ); SB();
  asm volatile("ds_read_b64_tr_b16 v[176:177], %0 offset:36864" :  : "v"(aW[0][1][1]) : "memory"); SB();
  asm volatile("v_mfma_f32_16x16x32_bf16 %0, v[160:163], a[52:55], %0" : "+v"(a2[1][0]) :  : ); SB();
  asm volatile("ds_read_b64_tr_b16 v[178:179], %0 offset:37632" :  : "v"(aW[0][1][1]) : "memory"); SB();
  asm volatile("v_mfma_f32_16x16x32_bf16 %0, v[160:163], %1, %0" : "+v"(c2[0]) : "v"(ones) : ); SB();
  asm volatile("ds_read_b64_tr_b16 v[180:181], %0 offset:36864" :  : "v"(aW[0][1][2]) : "memory"); SB();
  asm volatile("v_mfma_f32_16x16x32_bf16 %0, v[164:167], a[48:51], %0" : "+v"(a2[0][1]) :  : ); SB();
  asm volatile("ds_read_b64_tr_b16 v[182:183], %0 offset:37632" :  : "v"(aW[0][1][2]) : "memory"); SB();
  asm volatile("v_mfma_f32_16x16x32_bf16 %0, v[164:167], a[52:55], %0" : "+v"(a2[1][1]) :  : ); SB();
  asm volatile("ds_read_b128 a[84:87], %0 offset:24576" :  : "v"(aX[0][1]) : "memory"); SB();
  asm volatile("v_mfma_f32_16x16x32_bf16 %0, v[164:167], %1, %0" : "+v"(c2[1]) : "v"(ones) : ); SB();
  asm volatile("ds_read_b128 a[88:91], %0 offset:24576" :  : "v"(aX[1][0]) : "memory"); SB();
  asm volatile("v_mfma_f32_16x16x32_bf16 %0, v[168:171], a[48:51], %0" : "+v"(a2[0][2]) :  : ); SB();
  asm volatile("ds_read_b128 a[92:95], %0 offset:24576" :  : "v"(aX[1][1]) : "memory"); SB();
  asm volatile("v_mfma_f32_16x16x32_bf16 %0, v[168:171], a[52:55], %0" : "+v"(a2[1][2]) :  : ); SB();
  asm volatile("buffer_load_dwordx4 %0, %1, %2 offen offset:1024 lds" :  : "v"(voB[1]), "s"(rW), "s"(kw_[5]) : "memory"); SB();
  asm volatile("v_mfma_f32_16x16x32_bf16 %0, v[168:171], %1, %0" : "+v"(c2[2]) : "v"(ones) : ); SB();
  const auto rA = __builtin_amdgcn_make_buffer_rsrc((void*)(w_sp - 3072), 0, S_ * S_ * 2 + 3072, 0x00020000);
  unsigned ca = lb + 98304 + 6912 + w * 8192 + g4 * 16, cr = lb + 98304 + 6912 + (w ^ 2) * 8192 + g4 * 16;
  unsigned voS = (rb * 64 + (lane >> 3)) * 4096 + w * 1024 + (cb & 1) * 128 + (((lane & 7) ^ (lane >> 3)) << 4), aA[2];
#pragma unroll
  for (int k = 0; k < 2; ++k) aA[k] = lb + 98304 + w * 8192 + l15 * 128 + (((k * 4 + g4) ^ (l15 & 7)) << 4);
  unsigned xw = lb + 98304 + 6656 + w * 8192 + l15 * 16, xr_ = lb + 98304 + 6656 + (w ^ 1) * 8192 + l15 * 16;
  unsigned vo = W_ST + ((rb * 2 + wm) * 8 + cb) * 1024 + wn * 128 + ((lane >> 5) ^ wn) * 512 + (l15 >> 3) * 256 + (l15 & 7) * 16;
  asm volatile("" : "+v"(ca), "+v"(cr), "+v"(voS), "+v"(aA[0]), "+v"(aA[1]), "+v"(xw), "+v"(xr_), "+v"(vo));
  SB();
  asm volatile("buffer_load_dwordx4 %0, %1, %2 offen offset:2048 lds" :  : "v"(voB[2]), "s"(rW), "s"(kw_[5]) : "memory"); SB();
  asm volatile("s_add_u32 m0, %0, 73728" :  : "s"(mW) : "memory", "scc"); SB();
  asm volatile("s_nop 0\n\tbuffer_load_dwordx4 %0, %1, %2 offen lds" :  : "v"(voB[0]), "s"(rW), "s"(kw_[6]) : "memory"); SB();
  asm volatile("buffer_load_dwordx4 %0, %1, %2 offen offset:1024 lds" :  : "v"(voB[1]), "s"(rW), "s"(kw_[6]) : "memory"); SB();
  asm volatile("buffer_load_dwordx4 %0, %1, %2 offen offset:2048 lds" :  : "v"(voB[2]), "s"(rW), "s"(kw_[6]) : "memory"); SB();
  asm volatile("s_waitcnt vmcnt(10)\n\ts_barrier" :  :  : "memory"); SB();
  asm volatile("ds_read_b64_tr_b16 v[184:185], %0 offset:0" :  : "v"(aW[1][0][0]) : "memory"); SB();
  asm volatile("s_waitcnt lgkmcnt(1)\n\tv_mfma_f32_16x16x32_bf16 %0, %1, a[64:67], %0" : "+v"(rs) : "v"(ones) : ); SB();
  asm volatile("ds_read_b64_tr_b16 v[186:187], %0 offset:768" :  : "v"(aW[1][0][0]) : "memory"); SB();
  asm volatile("s_add_u32 m0, %0, 86016" :  : "s"(mW) : "memory", "scc"); SB();
  asm volatile("v_mfma_f32_16x16x32_bf16 %0, a[64:67], a[64:67], %0" : "+v"(gr) :  : ); SB();
  asm volatile("ds_read_b64_tr_b16 v[188:189], %0 offset:0" :  : "v"(aW[1][0][1]) : "memory"); SB();
  asm volatile("v_mfma_f32_16x16x32_bf16 %0, %1, a[72:75], %0" : "+v"(rs) : "v"(ones) : ); SB();
  asm volatile("ds_read_b64_tr_b16 v[190:191], %0 offset:768" :  : "v"(aW[1][0][1]) : "memory"); SB();
  asm volatile("v_mfma_f32_16x16x32_bf16 %0, a[72:75], a[72:75], %0" : "+v"(gr) :  : ); SB();
  asm volatile("ds_read_b64_tr_b16 v[192:193], %0 offset:0" :  : "v"(aW[1][0][2]) : "memory"); SB();
  asm volatile("v_mfma_f32_16x16x32_bf16 %0, v[172:175], a[56:59], %0" : "+v"(a2[0][0]) :  : ); SB();
  asm volatile("ds_read_b64_tr_b16 v[194:195], %0 offset:768" :  : "v"(aW[1][0][2]) : "memory"); SB();
  asm volatile("buffer_load_dwordx4 %0, %1, %2 offen lds" :  : "v"(voB[0]), "s"(rW), "s"(kw_[7]) : "memory"); SB();
  asm volatile("v_mfma_f32_16x16x32_bf16 %0, v[172:175], a[60:63], %0" : "+v"(a2[1][0]) :  : ); SB();
  asm volatile("ds_read_b128 a[96:99], %0 offset:32768" :  : "v"(aX[0][0]) : "memory"); SB();
  asm volatile("v_mfma_f32_16x16x32_bf16 %0, v[176:179], a[56:59], %0" : "+v"(a2[0][1]) :  : ); SB();
  asm volatile("ds_read_b128 a[100:103], %0 offset:32768" :  : "v"(aX[0][1]) : "memory"); SB();
  asm volatile("v_mfma_f32_16x16x32_bf16 %0, v[176:179], a[60:63], %0" : "+v"(a2[1][1]) :  : ); SB();
  asm volatile("ds_read_b128 a[104:107], %0 offset:32768" :  : "v"(aX[1][0]) : "memory"); SB();
  asm volatile("v_mfma_f32_16x16x32_bf16 %0, v[180:183], a[56:59], %0" : "+v"(a2[0][2]) :  : ); SB();
  asm volatile("buffer_load_dwordx4 %0, %1, %2 offen offset:1024 lds" :  : "v"(voB[1]), "s"(rW), "s"(kw_[7]) : "memory"); SB();
  asm volatile("v_mfma_f32_16x16x32_bf16 %0, v[180:183], a[60:63], %0" : "+v"(a2[1][2]) :  : ); SB();
  asm volatile("ds_read_b128 a[108:111], %0 offset:32768" :  : "v"(aX[1][1]) : "memory"); SB();
  asm volatile("v_mfma_f32_16x16x32_bf16 %0, %1, a[80:83], %0" : "+v"(rs) : "v"(ones) : ); SB();
  asm volatile("ds_read_b128 a[112:115], %0 offset:40960" :  : "v"(aX[0][0]) : "memory"); SB();
  asm volatile("v_mfma_f32_16x16x32_bf16 %0, a[80:83], a[80:83], %0" : "+v"(gr) :  : ); SB();
  asm volatile("ds_read_b128 a[116:119], %0 offset:40960" :  : "v"(aX[0][1]) : "memory"); SB();
  asm volatile("v_mfma_f32_16x16x32_bf16 %0, %1, a[88:91], %0" : "+v"(rs) : "v"(ones) : ); SB();
  asm volatile("ds_read_b64_tr_b16 v[160:161], %0 offset:0" :  : "v"(aW[1][1][0]) : "memory"); SB();
  asm volatile("v_mfma_f32_16x16x32_bf16 %0, a[88:91], a[88:91], %0" : "+v"(gr) :  : ); SB();
  asm volatile("ds_read_b64_tr_b16 v[162:163], %0 offset:768" :  : "v"(aW[1][1][0]) : "memory"); SB();
  asm volatile("s_waitcnt lgkmcnt(8)\n\tv_mfma_f32_16x16x32_bf16 %0, v[184:187], a[64:67], %0" : "+v"(a2[0][0]) :  : ); SB();
  asm volatile("ds_read_b64_tr_b16 v[164:165], %0 offset:0" :  : "v"(aW[1][1][1]) : "memory"); SB();
  asm volatile("buffer_load_dwordx4 %0, %1, %2 offen offset:2048 lds" :  : "v"(voB[2]), "s"(rW), "s"(kw_[7]) : "memory"); SB();
  asm volatile("v_mfma_f32_16x16x32_bf16 %0, v[184:187], a[68:71], %0" : "+v"(a2[1][0]) :  : ); SB();
  asm volatile("ds_read_b64_tr_b16 v[166:167], %0 offset:768" :  : "v"(aW[1][1][1]) : "memory"); SB();
  asm volatile("s_add_u32 m0, %0, 0" :  : "s"(mW) : "memory", "scc"); SB();
  asm volatile("v_mfma_f32_16x16x32_bf16 %0, v[184:187], %1, %0" : "+v"(c2[0]) : "v"(ones) : ); SB();
  asm volatile("ds_read_b64_tr_b16 v[168:169], %0 offset:0" :  : "v"(aW[1][1][2]) : "memory"); SB();
  asm volatile("v_mfma_f32_16x16x32_bf16 %0, v[188:191], a[64:67], %0" : "+v"(a2[0][1]) :  : ); SB();
  asm volatile("ds_read_b64_tr_b16 v[170:171], %0 offset:768" :  : "v"(aW[1][1][2]) : "memory"); SB();
  asm volatile("v_mfma_f32_16x16x32_bf16 %0, v[188:191], a[68:71], %0" : "+v"(a2[1][1]) :  : ); SB();
  asm volatile("ds_read_b128 a[120:123], %0 offset:40960" :  : "v"(aX[1][0]) : "memory"); SB();
  asm volatile("v_mfma_f32_16x16x32_bf16 %0, v[188:191], %1, %0" : "+v"(c2[1]) : "v"(ones) : ); SB();
  asm volatile("buffer_load_dwordx4 %0, %1, %2 offen lds" :  : "v"(voB[0]), "s"(rW), "s"(kw_[8]) : "memory"); SB();
  asm volatile("v_mfma_f32_16x16x32_bf16 %0, v[192:195], a[64:67], %0" : "+v"(a2[0][2]) :  : ); SB();
  asm volatile("ds_read_b128 a[124:127], %0 offset:40960" :  : "v"(aX[1][1]) : "memory"); SB();
  asm volatile("v_mfma_f32_16x16x32_bf16 %0, v[192:195], a[68:71], %0" : "+v"(a2[1][2]) :  : ); SB();
  asm volatile("buffer_load_dwordx4 %0, %1, %2 offen offset:1024 lds" :  : "v"(voB[1]), "s"(rW), "s"(kw_[8]) : "memory"); SB();
  asm volatile("v_mfma_f32_16x16x32_bf16 %0, v[192:195], %1, %0" : "+v"(c2[2]) : "v"(ones) : ); SB();
  asm volatile("buffer_load_dwordx4 %0, %1, %2 offen offset:2048 lds" :  : "v"(voB[2]), "s"(rW), "s"(kw_[8]) : "memory"); SB();
  asm volatile("s_add_u32 m0, %0, 12288" :  : "s"(mW) : "memory", "scc"); SB();
  asm volatile("s_nop 0\n\tbuffer_load_dwordx4 %0, %1, %2 offen lds" :  : "v"(voB[0]), "s"(rW), "s"(kw_[9]) : "memory"); SB();
  asm volatile("buffer_load_dwordx4 %0, %1, %2 offen offset:1024 lds" :  : "v"(voB[1]), "s"(rW), "s"(kw_[9]) : "memory"); SB();
  asm volatile("s_waitcnt vmcnt(11)\n\ts_barrier" :  :  : "memory"); SB();
  asm volatile("ds_read_b64_tr_b16 v[172:173], %0 offset:12288" :  : "v"(aW[1][0][0]) : "memory"); SB();
  asm volatile("s_waitcnt lgkmcnt(1)\n\tv_mfma_f32_16x16x32_bf16 %0, %1, a[96:99], %0" : "+v"(rs) : "v"(ones) : ); SB();
  asm volatile("ds_read_b64_tr_b16 v[174:175], %0 offset:13056" :  : "v"(aW[1][0][0]) : "memory"); SB();
  asm volatile("v_mfma_f32_16x16x32_bf16 %0, a[96:99], a[96:99], %0" : "+v"(gr) :  : ); SB();
  asm volatile("ds_read_b64_tr_b16 v[176:177], %0 offset:12288" :  : "v"(aW[1][0][1]) : "memory"); SB();
  asm volatile("v_mfma_f32_16x16x32_bf16 %0, %1, a[104:107], %0" : "+v"(rs) : "v"(ones) : ); SB();
  asm volatile("ds_read_b64_tr_b16 v[178:179], %0 offset:13056" :  : "v"(aW[1][0][1]) : "memory"); SB();
  asm volatile("v_mfma_f32_16x16x32_bf16 %0, a[104:107], a[104:107], %0" : "+v"(gr) :  : ); SB();
  asm volatile("ds_read_b64_tr_b16 v[180:181], %0 offset:12288" :  : "v"(aW[1][0][2]) : "memory"); SB();
  asm volatile("v_mfma_f32_16x16x32_bf16 %0, v[160:163], a[72:75], %0" : "+v"(a2[0][0]) :  : ); SB();
  asm volatile("ds_read_b64_tr_b16 v[182:183], %0 offset:13056" :  : "v"(aW[1][0][2]) : "memory"); SB();
  asm volatile("v_mfma_f32_16x16x32_bf16 %0, v[160:163], a[76:79], %0" : "+v"(a2[1][0]) :  : ); SB();
  asm volatile("ds_read_b128 a[128:131], %0 offset:49152" :  : "v"(aX[0][0]) : "memory"); SB();
  asm volatile("v_mfma_f32_16x16x32_bf16 %0, v[164:167], a[72:75], %0" : "+v"(a2[0][1]) :  : ); SB();
  asm volatile("ds_read_b128 a[132:135], %0 offset:49152" :  : "v"(aX[0][1]) : "memory"); SB();
  asm volatile("v_mfma_f32_16x16x32_bf16 %0, v[164:167], a[76:79], %0" : "+v"(a2[1][1]) :  : ); SB();
  asm volatile("ds_read_b128 a[136:139], %0 offset:49152" :  : "v"(aX[1][0]) : "memory"); SB();
  asm volatile("v_mfma_f32_16x16x32_bf16 %0, v[168:171], a[72:75], %0" : "+v"(a2[0][2]) :  : ); SB();
  asm volatile("ds_read_b128 a[140:143], %0 offset:49152" :  : "v"(aX[1][1]) : "memory"); SB();
  asm volatile("v_mfma_f32_16x16x32_bf16 %0, v[168:171], a[76:79], %0" : "+v"(a2[1][2]) :  : ); SB();
  asm volatile("ds_read_b128 a[144:147], %0 offset:57344" :  : "v"(aX[0][0]) : "memory"); SB();
  asm volatile("v_mfma_f32_16x16x32_bf16 %0, %1, a[112:115], %0" : "+v"(rs) : "v"(ones) : ); SB();
  asm volatile("ds_read_b128 a[148:151], %0 offset:57344" :  : "v"(aX[0][1]) : "memory"); SB();
  asm volatile("v_mfma_f32_16x16x32_bf16 %0, a[112:115], a[112:115], %0" : "+v"(gr) :  : ); SB();
  asm volatile("ds_read_b128 a[152:155], %0 offset:57344" :  : "v"(aX[1][0]) : "memory"); SB();
  asm volatile("v_mfma_f32_16x16x32_bf16 %0, %1, a[120:123], %0" : "+v"(rs) : "v"(ones) : ); SB();
  asm volatile("ds_read_b64_tr_b16 v[184:185], %0 offset:12288" :  : "v"(aW[1][1][0]) : "memory"); SB();
  asm volatile("v_mfma_f32_16x16x32_bf16 %0, a[120:123], a[120:123], %0" : "+v"(gr) :  : ); SB();
  asm volatile("ds_read_b64_tr_b16 v[186:187], %0 offset:13056" :  : "v"(aW[1][1][0]) : "memory"); SB();
  asm volatile("s_waitcnt lgkmcnt(9)\n\tv_mfma_f32_16x16x32_bf16 %0, v[172:175], a[80:83], %0" : "+v"(a2[0][0]) :  : ); SB();
  asm volatile("ds_read_b64_tr_b16 v[188:189], %0 offset:12288" :  : "v"(aW[1][1][1]) : "memory"); SB();
  asm volatile("v_mfma_f32_16x16x32_bf16 %0, v[172:175], a[84:87], %0" : "+v"(a2[1][0]) :  : ); SB();
  asm volatile("ds_read_b64_tr_b16 v[190:191], %0 offset:13056" :  : "v"(aW[1][1][1]) : "memory"); SB();
  asm volatile("v_mfma_f32_16x16x32_bf16 %0, v[172:175], %1, %0" : "+v"(c2[0]) : "v"(ones) : ); SB();
  asm volatile("ds_read_b64_tr_b16 v[192:193], %0 offset:12288" :  : "v"(aW[1][1][2]) : "memory"); SB();
  asm volatile("v_mfma_f32_16x16x32_bf16 %0, v[176:179], a[80:83], %0" : "+v"(a2[0][1]) :  : ); SB();
  asm volatile("ds_read_b64_tr_b16 v[194:195], %0 offset:13056" :  : "v"(aW[1][1][2]) : "memory"); SB();
  asm volatile("v_mfma_f32_16x16x32_bf16 %0, v[176:179], a[84:87], %0" : "+v"(a2[1][1]) :  : ); SB();
  asm volatile("ds_read_b128 a[156:159], %0 offset:57344" :  : "v"(aX[1][1]) : "memory"); SB();
  asm volatile("v_mfma_f32_16x16x32_bf16 %0, v[176:179], %1, %0" : "+v"(c2[1]) : "v"(ones) : ); SB();
  asm volatile("buffer_load_dwordx4 %0, %1, %2 offen offset:2048 lds" :  : "v"(voB[2]), "s"(rW), "s"(kw_[9]) : "memory"); SB();
  asm volatile("s_add_u32 m0, %0, 98304" :  : "s"(mA) : "memory", "scc"); SB();
  asm volatile("v_mfma_f32_16x16x32_bf16 %0, v[180:183], a[80:83], %0" : "+v"(a2[0][2]) :  : ); SB();
  ao[0] = (w ^ 0) * 65536 + 3072; asm volatile("" : "+s"(ao[0])); SB();
  ao[1] = ao[0] + 31744; asm volatile("" : "+s"(ao[1])); SB();
  asm volatile("v_mfma_f32_16x16x32_bf16 %0, v[180:183], a[84:87], %0" : "+v"(a2[1][2]) :  : ); SB();
  ao[2] = (w ^ 1) * 65536 + 1024; asm volatile("" : "+s"(ao[2])); SB();
  ao[3] = ao[2] + 31744; asm volatile("" : "+s"(ao[3])); SB();
  asm volatile("v_mfma_f32_16x16x32_bf16 %0, v[180:183], %1, %0" : "+v"(c2[2]) : "v"(ones) : ); SB();
  asm volatile("s_waitcnt vmcnt(9)\n\ts_barrier" :  :  : "memory"); SB();
  asm volatile("ds_read_b64_tr_b16 v[160:161], %0 offset:24576" :  : "v"(aW[1][0][0]) : "memory"); SB();
  asm volatile("s_waitcnt lgkmcnt(4)\n\tv_mfma_f32_16x16x32_bf16 %0, %1, a[128:131], %0" : "+v"(rs) : "v"(ones) : ); SB();
  asm volatile("ds_read_b64_tr_b16 v[162:163], %0 offset:25344" :  : "v"(aW[1][0][0]) : "memory"); SB();
  asm volatile("v_mfma_f32_16x16x32_bf16 %0, a[128:131], a[128:131], %0" : "+v"(gr) :  : ); SB();
  asm volatile("ds_read_b64_tr_b16 v[164:165], %0 offset:24576" :  : "v"(aW[1][0][1]) : "memory"); SB();
  asm volatile("v_mfma_f32_16x16x32_bf16 %0, %1, a[136:139], %0" : "+v"(rs) : "v"(ones) : ); SB();
  asm volatile("ds_read_b64_tr_b16 v[166:167], %0 offset:25344" :  : "v"(aW[1][0][1]) : "memory"); SB();
  asm volatile("v_mfma_f32_16x16x32_bf16 %0, a[136:139], a[136:139], %0" : "+v"(gr) :  : ); SB();
  asm volatile("ds_read_b64_tr_b16 v[168:169], %0 offset:24576" :  : "v"(aW[1][0][2]) : "memory"); SB();
  asm volatile("v_mfma_f32_16x16x32_bf16 %0, v[184:187], a[88:91], %0" : "+v"(a2[0][0]) :  : ); SB();
  asm volatile("ds_read_b64_tr_b16 v[170:171], %0 offset:25344" :  : "v"(aW[1][0][2]) : "memory"); SB();
  asm volatile("v_mfma_f32_16x16x32_bf16 %0, v[184:187], a[92:95], %0" : "+v"(a2[1][0]) :  : ); SB();
  ao[4] = (w ^ 2) * 65536 + 3072; asm volatile("" : "+s"(ao[4])); SB();
  ao[5] = ao[4] + 31744; asm volatile("" : "+s"(ao[5])); SB();
  asm volatile("v_mfma_f32_16x16x32_bf16 %0, v[188:191], a[88:91], %0" : "+v"(a2[0][1]) :  : ); SB();
  ao[6] = (w ^ 3) * 65536 + 1024; asm volatile("" : "+s"(ao[6])); SB();
  ao[7] = ao[6] + 31744; asm volatile("" : "+s"(ao[7])); SB();
  asm volatile("v_mfma_f32_16x16x32_bf16 %0, v[188:191], a[92:95], %0" : "+v"(a2[1][1]) :  : ); SB();
  asm volatile("buffer_load_dwordx4 %0, %1, %2 offen lds" :  : "v"(voK[0]), "s"(rK), "s"(0u) : "memory"); SB();
  asm volatile("s_waitcnt lgkmcnt(6)\n\tv_mfma_f32_16x16x32_bf16 %0, v[192:195], a[88:91], %0" : "+v"(a2[0][2]) :  : ); SB();
  asm volatile("ds_read_b64_tr_b16 v[172:173], %0 offset:24576" :  : "v"(aW[1][1][0]) : "memory"); SB();
  asm volatile("v_mfma_f32_16x16x32_bf16 %0, v[192:195], a[92:95], %0" : "+v"(a2[1][2]) :  : ); SB();
  asm volatile("ds_read_b64_tr_b16 v[174:175], %0 offset:25344" :  : "v"(aW[1][1][0]) : "memory"); SB();
  asm volatile("v_mfma_f32_16x16x32_bf16 %0, %1, a[144:147], %0" : "+v"(rs) : "v"(ones) : ); SB();
  asm volatile("ds_read_b64_tr_b16 v[176:177], %0 offset:24576" :  : "v"(aW[1][1][1]) : "memory"); SB();
  asm volatile("v_mfma_f32_16x16x32_bf16 %0, a[144:147], a[144:147], %0" : "+v"(gr) :  : ); SB();
  asm volatile("ds_read_b64_tr_b16 v[178:179], %0 offset:25344" :  : "v"(aW[1][1][1]) : "memory"); SB();
  asm volatile("v_mfma_f32_16x16x32_bf16 %0, %1, a[152:155], %0" : "+v"(rs) : "v"(ones) : ); SB();
  asm volatile("ds_read_b64_tr_b16 v[180:181], %0 offset:24576" :  : "v"(aW[1][1][2]) : "memory"); SB();
  asm volatile("v_mfma_f32_16x16x32_bf16 %0, a[152:155], a[152:155], %0" : "+v"(gr) :  : ); SB();
  asm volatile("ds_read_b64_tr_b16 v[182:183], %0 offset:25344" :  : "v"(aW[1][1][2]) : "memory"); SB();
  asm volatile("s_waitcnt lgkmcnt(6)\n\tv_mfma_f32_16x16x32_bf16 %0, v[160:163], a[96:99], %0" : "+v"(a2[0][0]) :  : ); SB();
  asm volatile("buffer_load_dwordx4 %0, %1, %2 offen offset:1024 lds" :  : "v"(voK[1]), "s"(rK), "s"(0u) : "memory"); SB();
  asm volatile("buffer_load_dwordx4 %0, %1, %2 offen offset:2048 lds" :  : "v"(voK[2]), "s"(rK), "s"(0u) : "memory"); SB();
  asm volatile("s_waitcnt vmcnt(9)\n\ts_barrier" :  :  : "memory"); SB();
  asm volatile("ds_read_b64_tr_b16 v[184:185], %0 offset:36864" :  : "v"(aW[1][0][0]) : "memory"); SB();
  asm volatile("v_mfma_f32_16x16x32_bf16 %0, v[160:163], a[100:103], %0" : "+v"(a2[1][0]) :  : ); SB();
  asm volatile("ds_read_b64_tr_b16 v[186:187], %0 offset:37632" :  : "v"(aW[1][0][0]) : "memory"); SB();
  asm volatile("v_mfma_f32_16x16x32_bf16 %0, v[160:163], %1, %0" : "+v"(c2[0]) : "v"(ones) : ); SB();
  asm volatile("ds_read_b64_tr_b16 v[188:189], %0 offset:36864" :  : "v"(aW[1][0][1]) : "memory"); SB();
  asm volatile("v_mfma_f32_16x16x32_bf16 %0, v[164:167], a[96:99], %0" : "+v"(a2[0][1]) :  : ); SB();
  asm volatile("ds_read_b64_tr_b16 v[190:191], %0 offset:37632" :  : "v"(aW[1][0][1]) : "memory"); SB();
  asm volatile("s_mov_b32 exec_lo, 0xff\n\ts_mov_b32 exec_hi, 0\n\tbuffer_load_dwordx4 %0, %1, %2 offen offset:3072 lds\n\ts_mov_b64 exec, -1" :  : "v"(voK[3]), "s"(rK), "s"(0u) : "memory"); SB();
  asm volatile("v_mfma_f32_16x16x32_bf16 %0, v[164:167], a[100:103], %0" : "+v"(a2[1][1]) :  : ); SB();
  asm volatile("ds_read_b64_tr_b16 v[192:193], %0 offset:36864" :  : "v"(aW[1][0][2]) : "memory"); SB();
  asm volatile("v_mfma_f32_16x16x32_bf16 %0, v[164:167], %1, %0" : "+v"(c2[1]) : "v"(ones) : ); SB();
  asm volatile("ds_read_b64_tr_b16 v[194:195], %0 offset:37632" :  : "v"(aW[1][0][2]) : "memory"); SB();
  asm volatile("v_mfma_f32_16x16x32_bf16 %0, v[168:171], a[96:99], %0" : "+v"(a2[0][2]) :  : ); SB();
  asm volatile("s_mov_b32 exec_lo, -1\n\ts_mov_b32 exec_hi, 0xff\n\tbuffer_load_dwordx4 %0, %1, %2 offen offset:3328 lds\n\ts_mov_b64 exec, -1" :  : "v"(voH), "s"(rS), "s"(0u) : "memory"); SB();
  asm volatile("v_mfma_f32_16x16x32_bf16 %0, v[168:171], a[100:103], %0" : "+v"(a2[1][2]) :  : ); SB();
  asm volatile("s_add_u32 m0, %0, 102400" :  : "s"(mA) : "memory", "scc"); SB();
  asm volatile("v_mfma_f32_16x16x32_bf16 %0, v[168:171], %1, %0" : "+v"(c2[2]) : "v"(ones) : ); SB();
  asm volatile("buffer_load_dwordx4 %0, %1, %2 offen lds" :  : "v"(voV[0]), "s"(rV), "s"(0u) : "memory"); SB();
  asm volatile("s_waitcnt lgkmcnt(5)\n\tv_mfma_f32_16x16x32_bf16 %0, v[172:175], a[104:107], %0" : "+v"(a2[0][0]) :  : ); SB();
  asm volatile("ds_read_b64_tr_b16 v[160:161], %0 offset:36864" :  : "v"(aW[1][1][0]) : "memory"); SB();
  asm volatile("v_cndmask_b32 %0, %1, %2, %3" : "=v"(xt[0]) : "v"(gr[0]), "v"(gr[1]), "s"(0xAAAAAAAAAAAAAAAAull) : ); SB();
  asm volatile("v_mfma_f32_16x16x32_bf16 %0, v[172:175], a[108:111], %0" : "+v"(a2[1][0]) :  : ); SB();
  asm volatile("s_mov_b32 exec_lo, 0x10001\n\ts_mov_b32 exec_hi, 0x10001\n\tds_write_b128 %0, %1 offset:0\n\ts_mov_b64 exec, -1" :  : "v"(ca), "v"(c2[0]) : "memory"); SB();
  asm volatile("ds_read_b64_tr_b16 v[162:163], %0 offset:37632" :  : "v"(aW[1][1][0]) : "memory"); SB();
  asm volatile("v_cndmask_b32 %0, %1, %2, %3" : "=v"(xt[1]) : "v"(gr[2]), "v"(gr[3]), "s"(0xAAAAAAAAAAAAAAAAull) : ); SB();
  asm volatile("v_mfma_f32_16x16x32_bf16 %0, v[176:179], a[104:107], %0" : "+v"(a2[0][1]) :  : ); SB();
  asm volatile("ds_read_b64_tr_b16 v[164:165], %0 offset:36864" :  : "v"(aW[1][1][1]) : "memory"); SB();
  asm volatile("v_mul_f32 %0, 0x3acccccd, %1" : "=v"(xs[0][0]) : "v"(rs[0]) : ); SB();
  asm volatile("v_mfma_f32_16x16x32_bf16 %0, v[176:179], a[108:111], %0" : "+v"(a2[1][1]) :  : ); SB();
  asm volatile("s_mov_b32 exec_lo, 0x10001\n\ts_mov_b32 exec_hi, 0x10001\n\tds_write_b128 %0, %1 offset:64\n\ts_mov_b64 exec, -1" :  : "v"(ca), "v"(c2[1]) : "memory"); SB();
  asm volatile("ds_read_b64_tr_b16 v[166:167], %0 offset:37632" :  : "v"(aW[1][1][1]) : "memory"); SB();
  asm volatile("buffer_load_dwordx4 %0, %1, %2 offen offset:1024 lds" :  : "v"(voV[1]), "s"(rV), "s"(0u) : "memory"); SB();
  asm volatile("v_mfma_f32_16x16x32_bf16 %0, v[180:183], a[104:107], %0" : "+v"(a2[0][2]) :  : ); SB();
  asm volatile("ds_read_b64_tr_b16 v[168:169], %0 offset:36864" :  : "v"(aW[1][1][2]) : "memory"); SB();
  asm volatile("v_cndmask_b32 %0, %0, %1, %2" : "+v"(xt[0]) : "v"(xt[1]), "s"(0xCCCCCCCCCCCCCCCCull) : ); SB();
  asm volatile("v_mfma_f32_16x16x32_bf16 %0, v[180:183], a[108:111], %0" : "+v"(a2[1][2]) :  : ); SB();
  asm volatile("s_mov_b32 exec_lo, 0x10001\n\ts_mov_b32 exec_hi, 0x10001\n\tds_write_b128 %0, %1 offset:128\n\ts_mov_b64 exec, -1" :  : "v"(ca), "v"(c2[2]) : "memory"); SB();
  asm volatile("ds_read_b64_tr_b16 v[170:171], %0 offset:37632" :  : "v"(aW[1][1][2]) : "memory"); SB();
  asm volatile("s_mov_b32 exec_lo, -1\n\ts_mov_b32 exec_hi, 0\n\tbuffer_load_dwordx4 %0, %1, %2 offen offset:2048 lds\n\ts_mov_b64 exec, -1" :  : "v"(voV[2]), "s"(rV), "s"(0u) : "memory"); SB();
  asm volatile("s_waitcnt lgkmcnt(5)\n\tv_mfma_f32_16x16x32_bf16 %0, v[184:187], a[112:115], %0" : "+v"(a2[0][0]) :  : ); SB();
  asm volatile("buffer_load_dwordx4 %0, %1, %2 offen offset:3072 lds" :  : "v"(voO), "s"(rO), "s"(0u) : "memory"); SB();
  asm volatile("v_mul_f32 %0, 0x3a83126f, %1" : "=v"(xt[1]) : "v"(rs[0]) : ); SB();
  asm volatile("v_mfma_f32_16x16x32_bf16 %0, v[184:187], a[116:119], %0" : "+v"(a2[1][0]) :  : ); SB();
  asm volatile("v_fma_f32 %0, -%1, %2, %3" : "=v"(xs[0][1]) : "v"(rs[0]), "v"(xs[0][0]), "v"(xt[0]) : ); SB();
  asm volatile("v_mfma_f32_16x16x32_bf16 %0, v[184:187], %1, %0" : "+v"(c2[0]) : "v"(ones) : ); SB();
  asm volatile("v_mul_f32 %0, %0, %1" : "+v"(xt[1]) : "v"(xs[0][0]) : ); SB();
  asm volatile("v_mfma_f32_16x16x32_bf16 %0, v[188:191], a[112:115], %0" : "+v"(a2[0][1]) :  : ); SB();
  asm volatile("s_waitcnt vmcnt(12)\n\ts_barrier" :  :  : "memory"); SB();
  asm volatile("ds_read_b64_tr_b16 v[172:173], %0 offset:0" :  : "v"(aW[0][0][0]) : "memory"); SB();
  asm volatile("v_mfma_f32_16x16x32_bf16 %0, v[188:191], a[116:119], %0" : "+v"(a2[1][1]) :  : ); SB();
  asm volatile("ds_read_b64_tr_b16 v[174:175], %0 offset:768" :  : "v"(aW[0][0][0]) : "memory"); SB();
  asm volatile("v_mfma_f32_16x16x32_bf16 %0, v[188:191], %1, %0" : "+v"(c2[1]) : "v"(ones) : ); SB();
  asm volatile("ds_read_b64_tr_b16 v[176:177], %0 offset:0" :  : "v"(aW[0][0][1]) : "memory"); SB();
  asm volatile("v_mfma_f32_16x16x32_bf16 %0, v[192:195], a[112:115], %0" : "+v"(a2[0][2]) :  : ); SB();
  asm volatile("ds_read_b64_tr_b16 v[178:179], %0 offset:768" :  : "v"(aW[0][0][1]) : "memory"); SB();
  asm volatile("v_mfma_f32_16x16x32_bf16 %0, v[192:195], a[116:119], %0" : "+v"(a2[1][2]) :  : ); SB();
  asm volatile("ds_read_b64_tr_b16 v[180:181], %0 offset:0" :  : "v"(aW[0][0][2]) : "memory"); SB();
  asm volatile("v_mfma_f32_16x16x32_bf16 %0, v[192:195], %1, %0" : "+v"(c2[2]) : "v"(ones) : ); SB();
  asm volatile("ds_read_b64_tr_b16 v[182:183], %0 offset:768" :  : "v"(aW[0][0][2]) : "memory"); SB();
  asm volatile("v_mfma_f32_16x16x32_bf16 %0, v[160:163], a[120:123], %0" : "+v"(a2[0][0]) :  : ); SB();
  if (__builtin_expect((__builtin_amdgcn_ballot_w64(!(xs[0][1] >= xt[1])) & 0xF0000F0000F0000Full) != 0, 0)) {
    const f32x2 mm = {xs[0][0], xs[0][0]};
    f32x2 q2 = {0.f, 0.f};
    const u32x4* xp = (const u32x4*)(x + (size_t)(rb * 64 + w * 16 + l15) * 1280 + g4 * 320);
#pragma unroll 1
    for (int c = 0; c < 20; ++c) {
      u32x4 xq;
      asm volatile("global_load_dwordx4 %0, %1, off\n\ts_waitcnt vmcnt(0)" : "=&v"(xq) : "v"(xp + c) : "memory");
#pragma unroll
      for (int e = 0; e < 4; ++e) { const f32x2 d = unpk(xq[e]) - mm; q2 = __builtin_elementwise_fma(d, d, q2); }
    }
    xs[0][1] = red32(red16(q2[0] + q2[1]));
  }
  SB();
  asm volatile("v_mfma_f32_16x16x32_bf16 %0, v[160:163], a[124:127], %0" : "+v"(a2[1][0]) :  : ); SB();
  asm volatile("buffer_load_dwordx4 v[228:231], %0, %1, %2 offen" :  : "v"(vF), "s"(rF), "s"(0u) : "memory"); SB();
  asm volatile("v_max_f32 %0, 0, %0" : "+v"(xs[0][1]) :  : ); SB();
  asm volatile("v_mfma_f32_16x16x32_bf16 %0, v[160:163], %1, %0" : "+v"(c2[0]) : "v"(ones) : ); SB();
  asm volatile("buffer_load_dwordx4 v[232:235], %0, %1, %2 offen" :  : "v"(vF), "s"(rF), "s"(4096u) : "memory"); SB();
  asm volatile("s_waitcnt lgkmcnt(5)\n\tv_mfma_f32_16x16x32_bf16 %0, v[164:167], a[120:123], %0" : "+v"(a2[0][1]) :  : ); SB();
  asm volatile("ds_read_b64_tr_b16 v[184:185], %0 offset:0" :  : "v"(aW[0][1][0]) : "memory"); SB();
  asm volatile("v_mul_f32 %0, 0x3acccccd, %0" : "+v"(xs[0][1]) :  : ); SB();
  asm volatile("v_mfma_f32_16x16x32_bf16 %0, v[164:167], a[124:127], %0" : "+v"(a2[1][1]) :  : ); SB();
  asm volatile("ds_read_b64_tr_b16 v[186:187], %0 offset:768" :  : "v"(aW[0][1][0]) : "memory"); SB();
  asm volatile("v_cvt_pk_bf16_f32 %0, %0, %0" : "+v"(xs[0][1]) :  : ); SB();
  asm volatile("v_mfma_f32_16x16x32_bf16 %0, v[164:167], %1, %0" : "+v"(c2[1]) : "v"(ones) : ); SB();
  asm volatile("ds_read_b64_tr_b16 v[188:189], %0 offset:0" :  : "v"(aW[0][1][1]) : "memory"); SB();
  asm volatile("v_lshlrev_b32 %0, 16, %0" : "+v"(xs[0][1]) :  : ); SB();
  asm volatile("v_mfma_f32_16x16x32_bf16 %0, v[168:171], a[120:123], %0" : "+v"(a2[0][2]) :  : ); SB();
  asm volatile("ds_read_b64_tr_b16 v[190:191], %0 offset:768" :  : "v"(aW[0][1][1]) : "memory"); SB();
  asm volatile("v_add_f32 %0, 0x3727c5ac, %0" : "+v"(xs[0][1]) :  : ); SB();
  asm volatile("v_mfma_f32_16x16x32_bf16 %0, v[168:171], a[124:127], %0" : "+v"(a2[1][2]) :  : ); SB();
  asm volatile("ds_read_b64_tr_b16 v[192:193], %0 offset:0" :  : "v"(aW[0][1][2]) : "memory"); SB();
  asm volatile("v_cvt_pk_bf16_f32 %0, %0, %0" : "+v"(xs[0][1]) :  : ); SB();
  asm volatile("v_mfma_f32_16x16x32_bf16 %0, v[168:171], %1, %0" : "+v"(c2[2]) : "v"(ones) : ); SB();
  asm volatile("ds_read_b64_tr_b16 v[194:195], %0 offset:768" :  : "v"(aW[0][1][2]) : "memory"); SB();
  asm volatile("s_waitcnt lgkmcnt(6)\n\tv_mfma_f32_16x16x32_bf16 %0, v[172:175], a[128:131], %0" : "+v"(a2[0][0]) :  : ); SB();
  asm volatile("v_lshlrev_b32 %0, 16, %0" : "+v"(xs[0][1]) :  : ); SB();
  asm volatile("v_mfma_f32_16x16x32_bf16 %0, v[172:175], a[132:135], %0" : "+v"(a2[1][0]) :  : ); SB();
  asm volatile("buffer_load_dwordx4 v[236:239], %0, %1, %2 offen" :  : "v"(vF), "s"(rF), "s"(c2s_) : "memory"); SB();
  asm volatile("v_mfma_f32_16x16x32_bf16 %0, v[172:175], %1, %0" : "+v"(c2[0]) : "v"(ones) : ); SB();
  asm volatile("v_sqrt_f32 %0, %0" : "+v"(xs[0][1]) :  : ); SB();
  asm volatile("v_mfma_f32_16x16x32_bf16 %0, v[176:179], a[128:131], %0" : "+v"(a2[0][1]) :  : ); SB();
  asm volatile("v_cvt_pk_bf16_f32 %0, %0, %0" : "+v"(xs[0][0]) :  : ); SB();
  asm volatile("v_lshlrev_b32 %0, 16, %0" : "+v"(xs[0][0]) :  : ); SB();
  asm volatile("v_cvt_pk_bf16_f32 %0, %0, %0" : "+v"(xs[0][1]) :  : ); SB();
  asm volatile("v_lshlrev_b32 %0, 16, %0" : "+v"(xs[0][1]) :  : ); SB();
  asm volatile("v_rcp_f32 %0, %1" : "=v"(xt[0]) : "v"(xs[0][1]) : ); SB();
  asm volatile("s_nop 0\n\tv_mul_f32 %0, 0xc0135761, %1" : "=v"(xs[0][2]) : "v"(xt[0]) : ); SB();
  asm volatile("v_mul_f32 %0, %1, %1" : "=v"(xt[1]) : "v"(xt[0]) : ); SB();
  asm volatile("v_mul_f32 %0, 0xbdd2d3e8, %0" : "+v"(xt[0]) :  : ); SB();
  asm volatile("v_mul_f32 %0, %1, %2" : "=v"(xs[0][3]) : "v"(xt[0]), "v"(xt[1]) : ); SB();
  asm volatile("s_mov_b32 exec_lo, 0xf0000f\n\ts_mov_b32 exec_hi, 0xf0000f00\n\tds_write_b128 %0, %1\n\ts_mov_b64 exec, -1" :  : "v"(xw), "v"(xs[0]) : "memory"); SB();
  asm volatile("s_waitcnt vmcnt(12) lgkmcnt(0)\n\ts_barrier" :  :  : "memory"); SB();
  asm volatile("ds_read_b64_tr_b16 v[160:161], %0 offset:12288" :  : "v"(aW[0][0][0]) : "memory"); SB();
  asm volatile("v_mfma_f32_16x16x32_bf16 %0, v[176:179], a[132:135], %0" : "+v"(a2[1][1]) :  : ); SB();
  asm volatile("ds_read_b64_tr_b16 v[162:163], %0 offset:13056" :  : "v"(aW[0][0][0]) : "memory"); SB();
  asm volatile("s_add_u32 m0, %0, 24576" :  : "s"(mW) : "memory", "scc"); SB();
  asm volatile("v_mfma_f32_16x16x32_bf16 %0, v[176:179], %1, %0" : "+v"(c2[1]) : "v"(ones) : ); SB();
  asm volatile("ds_read_b64_tr_b16 v[164:165], %0 offset:12288" :  : "v"(aW[0][0][1]) : "memory"); SB();
  asm volatile("v_mfma_f32_16x16x32_bf16 %0, v[180:183], a[128:131], %0" : "+v"(a2[0][2]) :  : ); SB();
  asm volatile("ds_read_b64_tr_b16 v[166:167], %0 offset:13056" :  : "v"(aW[0][0][1]) : "memory"); SB();
  asm volatile("v_mfma_f32_16x16x32_bf16 %0, v[180:183], a[132:135], %0" : "+v"(a2[1][2]) :  : ); SB();
  asm volatile("ds_read_b64_tr_b16 v[168:169], %0 offset:12288" :  : "v"(aW[0][0][2]) : "memory"); SB();
  asm volatile("v_mfma_f32_16x16x32_bf16 %0, v[180:183], %1, %0" : "+v"(c2[2]) : "v"(ones) : ); SB();
  asm volatile("ds_read_b64_tr_b16 v[170:171], %0 offset:13056" :  : "v"(aW[0][0][2]) : "memory"); SB();
  asm volatile("v_mfma_f32_16x16x32_bf16 %0, v[184:187], a[136:139], %0" : "+v"(a2[0][0]) :  : ); SB();
  asm volatile("ds_read_b128 %0, %1 offset:0" : "=&v"(xs[0]) : "v"(xw) : "memory"); SB();
  asm volatile("v_mfma_f32_16x16x32_bf16 %0, v[184:187], a[140:143], %0" : "+v"(a2[1][0]) :  : ); SB();
  asm volatile("ds_read_b128 %0, %1 offset:0" : "=&v"(xs[1]) : "v"(xr_) : "memory"); SB();
  asm volatile("v_mfma_f32_16x16x32_bf16 %0, v[184:187], %1, %0" : "+v"(c2[0]) : "v"(ones) : ); SB();
  asm volatile("ds_read_b128 %0, %1 offset:0" : "=&v"(co[0]) : "v"(cr) : "memory"); SB();
  asm volatile("v_mfma_f32_16x16x32_bf16 %0, v[188:191], a[136:139], %0" : "+v"(a2[0][1]) :  : ); SB();
  asm volatile("ds_read_b64_tr_b16 v[172:173], %0 offset:12288" :  : "v"(aW[0][1][0]) : "memory"); SB();
  asm volatile("v_mfma_f32_16x16x32_bf16 %0, v[188:191], a[140:143], %0" : "+v"(a2[1][1]) :  : ); SB();
  asm volatile("ds_read_b64_tr_b16 v[174:175], %0 offset:13056" :  : "v"(aW[0][1][0]) : "memory"); SB();
  asm volatile("v_mfma_f32_16x16x32_bf16 %0, v[188:191], %1, %0" : "+v"(c2[1]) : "v"(ones) : ); SB();
  asm volatile("ds_read_b64_tr_b16 v[176:177], %0 offset:12288" :  : "v"(aW[0][1][1]) : "memory"); SB();
  asm volatile("v_mfma_f32_16x16x32_bf16 %0, v[192:195], a[136:139], %0" : "+v"(a2[0][2]) :  : ); SB();
  asm volatile("ds_read_b64_tr_b16 v[178:179], %0 offset:13056" :  : "v"(aW[0][1][1]) : "memory"); SB();
  asm volatile("v_mfma_f32_16x16x32_bf16 %0, v[192:195], a[140:143], %0" : "+v"(a2[1][2]) :  : ); SB();
  asm volatile("ds_read_b64_tr_b16 v[180:181], %0 offset:12288" :  : "v"(aW[0][1][2]) : "memory"); SB();
  asm volatile("v_mfma_f32_16x16x32_bf16 %0, v[192:195], %1, %0" : "+v"(c2[2]) : "v"(ones) : ); SB();
  asm volatile("ds_read_b64_tr_b16 v[182:183], %0 offset:13056" :  : "v"(aW[0][1][2]) : "memory"); SB();
  asm volatile("s_waitcnt lgkmcnt(9)\n\tv_mfma_f32_16x16x32_bf16 %0, v[160:163], a[144:147], %0" : "+v"(a2[0][0]) :  : ); SB();
  asm volatile("ds_read_b128 %0, %1 offset:64" : "=&v"(co[1]) : "v"(cr) : "memory"); SB();
  asm volatile("v_mfma_f32_16x16x32_bf16 %0, v[160:163], a[148:151], %0" : "+v"(a2[1][0]) :  : ); SB();
  asm volatile("ds_read_b128 %0, %1 offset:128" : "=&v"(co[2]) : "v"(cr) : "memory"); SB();
  asm volatile("v_mfma_f32_16x16x32_bf16 %0, v[160:163], %1, %0" : "+v"(c2[0]) : "v"(ones) : ); SB();
  asm volatile("v_readfirstlane_b32 %0, v252\n\tv_readfirstlane_b32 %1, v253\n\ts_nop 0\n\ts_add_u32 %0, %0, 1\n\ts_addc_u32 %1, %1, 0" : "=&s"(tg0), "=&s"(tg1) :  : "memory", "scc"); SB();
  asm volatile("v_mfma_f32_16x16x32_bf16 %0, v[164:167], a[144:147], %0" : "+v"(a2[0][1]) :  : ); SB();
  { unsigned lane = lane0, w = w0, rb = rb0, cb = cb0;
  asm volatile("" : "+v"(lane)); asm volatile("" : "+s"(w), "+s"(rb), "+s"(cb)); SB();

  asm volatile("v_mfma_f32_16x16x32_bf16 %0, v[164:167], a[148:151], %0" : "+v"(a2[1][1]) :  : ); SB();
  w3k[0] = w3s + 32768; asm volatile("" : "+s"(w3k[0])); SB();
  asm volatile("v_mfma_f32_16x16x32_bf16 %0, v[164:167], %1, %0" : "+v"(c2[1]) : "v"(ones) : ); SB();
  w3k[1] = w3s + 65536; asm volatile("" : "+s"(w3k[1])); SB();
  asm volatile("buffer_load_dwordx4 %0, %1, %2 offen lds" :  : "v"(voB[0]), "s"(rW1), "s"(kw_[0]) : "memory"); SB();
  asm volatile("v_mfma_f32_16x16x32_bf16 %0, v[168:171], a[144:147], %0" : "+v"(a2[0][2]) :  : ); SB();
  sFZ = W_FZ + ((cb * 32 + rb) * 4 + w) * 8; asm volatile("" : "+s"(sFZ)); SB();
  asm volatile("v_mfma_f32_16x16x32_bf16 %0, v[168:171], a[148:151], %0" : "+v"(a2[1][2]) :  : ); SB();
  sFK = sFZ + 8192; asm volatile("" : "+s"(sFK)); SB();
  mU = lb + w * 1024; asm volatile("" : "+s"(mU)); SB();
  asm volatile("v_mfma_f32_16x16x32_bf16 %0, v[168:171], %1, %0" : "+v"(c2[2]) : "v"(ones) : ); SB();
  uso = W_KV + c2s; asm volatile("" : "+s"(uso)); SB();
  asm volatile("s_waitcnt lgkmcnt(1)\n\tv_mfma_f32_16x16x32_bf16 %0, v[172:175], a[152:155], %0" : "+v"(a2[0][0]) :  : ); SB();
  asm volatile("buffer_load_dwordx4 %0, %1, %2 offen offset:1024 lds" :  : "v"(voB[1]), "s"(rW1), "s"(kw_[0]) : "memory"); SB();
  asm volatile("v_mfma_f32_16x16x32_bf16 %0, v[172:175], a[156:159], %0" : "+v"(a2[1][0]) :  : ); SB();
  asm volatile("buffer_load_dwordx4 %0, %1, %2 offen offset:2048 lds" :  : "v"(voB[2]), "s"(rW1), "s"(kw_[0]) : "memory"); SB();
  asm volatile("v_mfma_f32_16x16x32_bf16 %0, v[172:175], %1, %0" : "+v"(c2[0]) : "v"(ones) : ); SB();
  asm volatile("v_mfma_f32_16x16x32_bf16 %0, v[176:179], a[152:155], %0" : "+v"(a2[0][1]) :  : ); SB();
  asm volatile("v_mfma_f32_16x16x32_bf16 %0, v[176:179], a[156:159], %0" : "+v"(a2[1][1]) :  : ); SB();
  asm volatile("s_add_u32 m0, %0, 36864" :  : "s"(mW) : "memory", "scc"); SB();
  asm volatile("v_mfma_f32_16x16x32_bf16 %0, v[176:179], %1, %0" : "+v"(c2[1]) : "v"(ones) : ); SB();
  asm volatile("buffer_load_dwordx4 %0, %1, %2 offen lds" :  : "v"(voB[0]), "s"(rW1), "s"(kw_[1]) : "memory"); SB();
  asm volatile("v_mfma_f32_16x16x32_bf16 %0, v[180:183], a[152:155], %0" : "+v"(a2[0][2]) :  : ); SB();
  asm volatile("v_mfma_f32_16x16x32_bf16 %0, v[180:183], a[156:159], %0" : "+v"(a2[1][2]) :  : ); SB();
  asm volatile("v_mfma_f32_16x16x32_bf16 %0, v[180:183], %1, %0" : "+v"(c2[2]) : "v"(ones) : ); SB();
  asm volatile("buffer_load_dwordx4 %0, %1, %2 offen offset:1024 lds" :  : "v"(voB[1]), "s"(rW1), "s"(kw_[1]) : "memory"); SB();
  ST(3);
  ST(5);
  const unsigned long long tg = ((unsigned long long)tg1 << 32) | tg0;
  asm volatile("s_waitcnt lgkmcnt(0)" : "+v"(xs[0]), "+v"(xs[1]), "+v"(co[0]), "+v"(co[1]), "+v"(co[2]) :  : "memory"); SB();
  asm volatile("s_nop 3" : "+v"(a2[0][0]), "+v"(a2[0][1]), "+v"(a2[0][2]), "+v"(a2[1][0]), "+v"(a2[1][1]), "+v"(a2[1][2]), "+v"(c2[0]), "+v"(c2[1]), "+v"(c2[2]) :  : ); SB();
  for (int i = 0; i < 2; ++i) { xm[i] = xs[i][0]; xd[i] = xs[i][1]; k1[i] = xs[i][2]; k2[i] = xs[i][3]; }
  c2[0] += co[0]; c2[1] += co[1]; c2[2] += co[2];
  f32x2 ge2[2];
  auto act = [&](f32x2& t, f32x2& e, const f32x4& a, const f32x4& cs, int i, int h) {
    t = __builtin_elementwise_fma((f32x2){-xm[i], -xm[i]}, (f32x2){cs[2 * h], cs[2 * h + 1]}, (f32x2){a[2 * h], a[2 * h + 1]});
    const f32x2 u = t * __builtin_elementwise_fma(t * t, (f32x2){k2[i], k2[i]}, (f32x2){k1[i], k1[i]});
    e = (f32x2){__builtin_amdgcn_exp2f(u[0]), __builtin_amdgcn_exp2f(u[1])};
  };
  auto act2 = [&](f32x2& t, f32x2 e, int i) {
    e = __builtin_elementwise_fma(e, (f32x2){xd[i], xd[i]}, (f32x2){xd[i], xd[i]});
    t *= (f32x2){__builtin_amdgcn_rcpf(e[0]), __builtin_amdgcn_rcpf(e[1])};
  };
  f32x2 gv[2][3][2];
  float sv[2][2], pv[2];
  act(gv[0][0][0], ge2[0], a2[0][0], c2[0], 0, 0); act(gv[0][0][1], ge2[1], a2[0][0], c2[0], 0, 1);
  asm volatile("" : "+v"(gv[0][0][0]), "+v"(gv[0][0][1]), "+v"(ge2[0]), "+v"(ge2[1]));
  SB();
  asm volatile("buffer_load_dwordx4 %0, %1, %2 offen offset:2048 lds" :  : "v"(voB[2]), "s"(rW1), "s"(kw_[1]) : "memory"); SB();
  asm volatile("s_add_u32 m0, %0, 49152" :  : "s"(mW) : "memory", "scc"); SB();
  asm volatile("s_nop 0\n\tbuffer_load_dwordx4 %0, %1, %2 offen lds" :  : "v"(voB[0]), "s"(rW1), "s"(kw_[2]) : "memory"); SB();
  act2(gv[0][0][0], ge2[0], 0); act2(gv[0][0][1], ge2[1], 0);
  asm volatile("" : "+v"(gv[0][0][0]), "+v"(gv[0][0][1]));
  SB();
  asm volatile("buffer_load_dwordx4 %0, %1, %2 offen offset:1024 lds" :  : "v"(voB[1]), "s"(rW1), "s"(kw_[2]) : "memory"); SB();
  act(gv[0][1][0], ge2[0], a2[0][1], c2[1], 0, 0); act(gv[0][1][1], ge2[1], a2[0][1], c2[1], 0, 1);
  asm volatile("" : "+v"(gv[0][1][0]), "+v"(gv[0][1][1]), "+v"(ge2[0]), "+v"(ge2[1]));
  SB();
  asm volatile("buffer_load_dwordx4 %0, %1, %2 offen offset:2048 lds" :  : "v"(voB[2]), "s"(rW1), "s"(kw_[2]) : "memory"); SB();
  asm volatile("s_add_u32 m0, %0, 61440" :  : "s"(mW) : "memory", "scc"); SB();
  asm volatile("s_nop 0\n\tbuffer_load_dwordx4 %0, %1, %2 offen lds" :  : "v"(voB[0]), "s"(rW1), "s"(kw_[3]) : "memory"); SB();
  act2(gv[0][1][0], ge2[0], 0); act2(gv[0][1][1], ge2[1], 0);
  asm volatile("" : "+v"(gv[0][1][0]), "+v"(gv[0][1][1]));
  SB();
  asm volatile("buffer_load_dwordx4 %0, %1, %2 offen offset:1024 lds" :  : "v"(voB[1]), "s"(rW1), "s"(kw_[3]) : "memory"); SB();
  act(gv[0][2][0], ge2[0], a2[0][2], c2[2], 0, 0); act(gv[0][2][1], ge2[1], a2[0][2], c2[2], 0, 1);
  asm volatile("" : "+v"(gv[0][2][0]), "+v"(gv[0][2][1]), "+v"(ge2[0]), "+v"(ge2[1]));
  SB();
  asm volatile("buffer_load_dwordx4 %0, %1, %2 offen offset:2048 lds" :  : "v"(voB[2]), "s"(rW1), "s"(kw_[3]) : "memory"); SB();
  asm volatile("s_add_u32 m0, %0, 73728" :  : "s"(mW) : "memory", "scc"); SB();
  asm volatile("s_nop 0\n\tbuffer_load_dwordx4 %0, %1, %2 offen lds" :  : "v"(voB[0]), "s"(rW1), "s"(kw_[4]) : "memory"); SB();
  act2(gv[0][2][0], ge2[0], 0); act2(gv[0][2][1], ge2[1], 0);
  asm volatile("" : "+v"(gv[0][2][0]), "+v"(gv[0][2][1]));
  SB();
  asm volatile("buffer_load_dwordx4 %0, %1, %2 offen offset:1024 lds" :  : "v"(voB[1]), "s"(rW1), "s"(kw_[4]) : "memory"); SB();
  act(gv[1][0][0], ge2[0], a2[1][0], c2[0], 1, 0); act(gv[1][0][1], ge2[1], a2[1][0], c2[0], 1, 1);
  asm volatile("" : "+v"(gv[1][0][0]), "+v"(gv[1][0][1]), "+v"(ge2[0]), "+v"(ge2[1]));
  SB();
  asm volatile("buffer_load_dwordx4 %0, %1, %2 offen offset:2048 lds" :  : "v"(voB[2]), "s"(rW1), "s"(kw_[4]) : "memory"); SB();
  act2(gv[1][0][0], ge2[0], 1); act2(gv[1][0][1], ge2[1], 1);
  asm volatile("" : "+v"(gv[1][0][0]), "+v"(gv[1][0][1]));
  SB();
  act(gv[1][1][0], ge2[0], a2[1][1], c2[1], 1, 0); act(gv[1][1][1], ge2[1], a2[1][1], c2[1], 1, 1);
  asm volatile("" : "+v"(gv[1][1][0]), "+v"(gv[1][1][1]), "+v"(ge2[0]), "+v"(ge2[1]));
  SB();
  act2(gv[1][1][0], ge2[0], 1); act2(gv[1][1][1], ge2[1], 1);
  asm volatile("" : "+v"(gv[1][1][0]), "+v"(gv[1][1][1]));
  SB();
  act(gv[1][2][0], ge2[0], a2[1][2], c2[2], 1, 0); act(gv[1][2][1], ge2[1], a2[1][2], c2[2], 1, 1);
  asm volatile("" : "+v"(gv[1][2][0]), "+v"(gv[1][2][1]), "+v"(ge2[0]), "+v"(ge2[1]));
  SB();
  act2(gv[1][2][0], ge2[0], 1); act2(gv[1][2][1], ge2[1], 1);
  asm volatile("" : "+v"(gv[1][2][0]), "+v"(gv[1][2][1]));
  SB();
#pragma unroll
  for (int i = 0; i < 2; ++i) {
    const unsigned p0 = __float_as_uint(gv[i][0][0][0]);
    const auto pa = __builtin_amdgcn_permlane16_swap(p0, p0, false, false);
    const auto pb = __builtin_amdgcn_permlane32_swap(pa[0], pa[0], false, false);
    pv[i] = __uint_as_float(pb[0]);
  }
  asm volatile("" : "+v"(pv[0]), "+v"(pv[1]));
  f32x2 s1[2], s2[2];
  SB();
  asm volatile("s_add_u32 m0, %0, 86016" :  : "s"(mW) : "memory", "scc"); SB();
  asm volatile("s_nop 0\n\tbuffer_load_dwordx4 %0, %1, %2 offen lds" :  : "v"(voB[0]), "s"(rW1), "s"(kw_[5]) : "memory"); SB();
#pragma unroll
  for (int m = 0; m < 4; ++m) {
    const int i = m & 1, n = 0 + (m >> 1);
    const f32x2 d = gv[i][n >> 1][n & 1] - (f32x2){pv[i], pv[i]};
    s1[i] = n ? s1[i] + d : d; s2[i] = n ? __builtin_elementwise_fma(d, d, s2[i]) : d * d;
  }
  asm volatile("" : "+v"(s1[0]), "+v"(s1[1]), "+v"(s2[0]), "+v"(s2[1]));
  SB();
  asm volatile("buffer_load_dwordx4 %0, %1, %2 offen offset:1024 lds" :  : "v"(voB[1]), "s"(rW1), "s"(kw_[5]) : "memory"); SB();
#pragma unroll
  for (int m = 0; m < 4; ++m) {
    const int i = m & 1, n = 2 + (m >> 1);
    const f32x2 d = gv[i][n >> 1][n & 1] - (f32x2){pv[i], pv[i]};
    s1[i] = n ? s1[i] + d : d; s2[i] = n ? __builtin_elementwise_fma(d, d, s2[i]) : d * d;
  }
  asm volatile("" : "+v"(s1[0]), "+v"(s1[1]), "+v"(s2[0]), "+v"(s2[1]));
  SB();
  asm volatile("buffer_load_dwordx4 %0, %1, %2 offen offset:2048 lds" :  : "v"(voB[2]), "s"(rW1), "s"(kw_[5]) : "memory"); SB();
#pragma unroll
  for (int m = 0; m < 4; ++m) {
    const int i = m & 1, n = 4 + (m >> 1);
    const f32x2 d = gv[i][n >> 1][n & 1] - (f32x2){pv[i], pv[i]};
    s1[i] = n ? s1[i] + d : d; s2[i] = n ? __builtin_elementwise_fma(d, d, s2[i]) : d * d;
  }
  asm volatile("" : "+v"(s1[0]), "+v"(s1[1]), "+v"(s2[0]), "+v"(s2[1]));
  SB();
  asm volatile("s_add_u32 m0, %0, 0" :  : "s"(mW) : "memory", "scc"); SB();
  asm volatile("s_nop 0\n\tbuffer_load_dwordx4 %0, %1, %2 offen lds" :  : "v"(voB[0]), "s"(rW1), "s"(kw_[6]) : "memory"); SB();
  for (int i = 0; i < 2; ++i) { sv[i][0] = s1[i][0] + s1[i][1]; sv[i][1] = s2[i][0] + s2[i][1]; }
  {
    const float t1 = red16(sel32(sv[0][0], sv[1][0])), t2 = red16(sel32(sv[0][1], sv[1][1]));
    const float dl = t1 * (1.0f / 48.0f);
    const u32x4 so = {__float_as_uint((lane < 32 ? pv[0] : pv[1]) + dl), __float_as_uint(fmaxf(t2 - t1 * dl, 0.f)), tg0, tg1};
    SB();
  asm volatile("s_mov_b32 exec_lo, 0xffff\n\ts_mov_b32 exec_hi, 0xffff\n\tbuffer_store_dwordx4 %1, %0, %2, %3 offen offset:0 sc1\n\ts_mov_b64 exec, -1\n\ts_nop 0" :  : "v"(vo), "v"(so), "s"(rWS), "s"(0u) : "memory"); SB();
  }
  ST(6);
  unsigned zp[2][3][2];
#pragma unroll
  for (int n = 0; n < 12; ++n) { zp[n / 6][(n >> 1) % 3][n & 1] = pk2(gv[n / 6][(n >> 1) % 3][n & 1]); asm volatile("" : "+v"(zp[n / 6][(n >> 1) % 3][n & 1])); }
  SB();
  asm volatile("s_waitcnt vmcnt(23)\n\ts_barrier" :  :  : "memory"); SB();
  ST(2);
  asm volatile("ds_read_b64_tr_b16 %0, %1 offset:0" : "=&v"(wt[0]) : "v"(oa) : "memory"); SB();
  asm volatile("ds_read_b64_tr_b16 %0, %1 offset:512" : "=&v"(wt[1]) : "v"(oa) : "memory"); SB();
  asm volatile("ds_read_b64_tr_b16 %0, %1 offset:0" : "=&v"(kp[0]) : "v"(ka) : "memory"); SB();
  asm volatile("ds_read_b64_tr_b16 %0, %1 offset:640" : "=&v"(kc[0]) : "v"(ka) : "memory"); SB();
  asm volatile("ds_read_b64_tr_b16 %0, %1 offset:4096" : "=&v"(vv[0]) : "v"(ka) : "memory"); SB();
  asm volatile("ds_read_b64 %0, %1 offset:0" : "=&v"(sw[0]) : "v"(sa) : "memory"); SB();
  asm volatile("buffer_load_dwordx4 %0, %1, %2 offen offset:1024 lds" :  : "v"(voB[1]), "s"(rW1), "s"(kw_[6]) : "memory"); SB();
  asm volatile("ds_read_b64_tr_b16 %0, %1 offset:32" : "=&v"(kp[1]) : "v"(ka) : "memory"); SB();
  asm volatile("ds_read_b64_tr_b16 %0, %1 offset:672" : "=&v"(kc[1]) : "v"(ka) : "memory"); SB();
  asm volatile("ds_read_b64_tr_b16 %0, %1 offset:4128" : "=&v"(vv[1]) : "v"(ka) : "memory"); SB();
  asm volatile("ds_read_b64 %0, %1 offset:128" : "=&v"(sw[1]) : "v"(sa) : "memory"); SB();
  asm volatile("buffer_load_dwordx4 %0, %1, %2 offen offset:2048 lds" :  : "v"(voB[2]), "s"(rW1), "s"(kw_[6]) : "memory"); SB();
  asm volatile("ds_read_b64_tr_b16 %0, %1 offset:64" : "=&v"(kp[2]) : "v"(ka) : "memory"); SB();
  asm volatile("ds_read_b64_tr_b16 %0, %1 offset:704" : "=&v"(kc[2]) : "v"(ka) : "memory"); SB();
  asm volatile("ds_read_b64_tr_b16 %0, %1 offset:4160" : "=&v"(vv[2]) : "v"(ka) : "memory"); SB();
  asm volatile("ds_read_b64 %0, %1 offset:256" : "=&v"(sw[2]) : "v"(sa) : "memory"); SB();
  asm volatile("s_add_u32 m0, %0, 131072" :  : "s"(mA) : "memory", "scc"); SB();
  asm volatile("s_nop 0\n\tbuffer_load_dwordx4 %0, %1, %2 offen lds" :  : "v"(voS), "s"(rA), "s"(ao[0]) : "memory"); SB();
  asm volatile("ds_read_b64_tr_b16 %0, %1 offset:96" : "=&v"(kp[3]) : "v"(ka) : "memory"); SB();
  asm volatile("ds_read_b64_tr_b16 %0, %1 offset:736" : "=&v"(kc[3]) : "v"(ka) : "memory"); SB();
  asm volatile("ds_read_b64_tr_b16 %0, %1 offset:4192" : "=&v"(vv[3]) : "v"(ka) : "memory"); SB();
  asm volatile("ds_read_b64 %0, %1 offset:384" : "=&v"(sw[3]) : "v"(sa) : "memory"); SB();
  asm volatile("ds_read_b64_tr_b16 %0, %1 offset:128" : "=&v"(kp[4]) : "v"(ka) : "memory"); SB();
  asm volatile("ds_read_b64_tr_b16 %0, %1 offset:768" : "=&v"(kc[4]) : "v"(ka) : "memory"); SB();
  asm volatile("ds_read_b64_tr_b16 %0, %1 offset:4224" : "=&v"(vv[4]) : "v"(ka) : "memory"); SB();
  asm volatile("ds_read_b64 %0, %1 offset:512" : "=&v"(sw[4]) : "v"(sa) : "memory"); SB();
  asm volatile("buffer_load_dwordx4 %0, %1, %2 offen offset:1024 lds" :  : "v"(voS), "s"(rA), "s"(ao[1]) : "memory"); SB();
  asm volatile("s_waitcnt lgkmcnt(8)" : "+v"(kp[0]), "+v"(kc[0]), "+v"(vv[0]), "+v"(sw[0]), "+v"(wt[0]), "+v"(wt[1]) :  : "memory"); SB();
  asm volatile("v_alignbit_b32 %4, %14, %14, 16\n\tv_alignbit_b32 %5, %10, %9, 16\n\tv_alignbit_b32 %6, %11, %10, 16\n\tv_alignbit_b32 %7, %13, %13, 16\n\tv_alignbit_b32 %8, %12, %11, 16\n\tv_dot2_f32_bf16 %0, %5, %4, 0\n\tv_dot2_f32_bf16 %1, %10, %4, 0\n\tv_dot2_f32_bf16 %2, %6, %4, 0\n\tv_dot2_f32_bf16 %3, %11, %4, 0\n\tv_dot2_f32_bf16 %0, %6, %7, %0\n\tv_dot2_f32_bf16 %1, %11, %7, %1\n\tv_dot2_f32_bf16 %2, %8, %7, %2\n\tv_dot2_f32_bf16 %3, %12, %7, %3" : "=&v"(kt[0][0]), "=&v"(kt[0][1]), "=&v"(kt[1][0]), "=&v"(kt[1][1]), "=&v"(ku[0]), "=&v"(ku[1]), "=&v"(ku[2]), "=&v"(ku[3]), "=&v"(ku[4]) : "v"(kp[0][0]), "v"(kp[0][1]), "v"(kc[0][0]), "v"(kc[0][1]), "v"(sw[0][0]), "v"(sw[0][1]) : ); SB();
  asm volatile("buffer_load_dwordx4 %0, %1, %2 offen offset:2048 lds" :  : "v"(voS), "s"(rA), "s"(ao[2]) : "memory"); SB();
  asm volatile("v_lshlrev_b32 %0, 16, %4\n\tv_and_b32 %1, 0xffff0000, %4\n\tv_lshlrev_b32 %2, 16, %5\n\tv_and_b32 %3, 0xffff0000, %5" : "=&v"(kf[0][0]), "=&v"(kf[0][1]), "=&v"(kf[1][0]), "=&v"(kf[1][1]) : "v"(vv[0][0]), "v"(vv[0][1]) : ); SB();
  kt[0] *= kf[0]; kt[1] *= kf[1]; SB();
  ko[0] = (u32x2){pk2(kt[0]), pk2(kt[1])};
  asm volatile("" : "+v"(ko[0]));
  SB();
  asm volatile("buffer_load_dwordx4 %0, %1, %2 offen offset:3072 lds" :  : "v"(voS), "s"(rA), "s"(ao[3]) : "memory"); SB();
  asm volatile("ds_write_b64 %0, %1 offset:0" :  : "v"(kw), "v"(ko[0]) : "memory"); SB();
  asm volatile("" : "+v"(kp[1]), "+v"(kc[1]), "+v"(vv[1]), "+v"(sw[1]) :  : "memory"); SB();
  asm volatile("v_alignbit_b32 %4, %14, %14, 16\n\tv_alignbit_b32 %5, %10, %9, 16\n\tv_alignbit_b32 %6, %11, %10, 16\n\tv_alignbit_b32 %7, %13, %13, 16\n\tv_alignbit_b32 %8, %12, %11, 16\n\tv_dot2_f32_bf16 %0, %5, %4, 0\n\tv_dot2_f32_bf16 %1, %10, %4, 0\n\tv_dot2_f32_bf16 %2, %6, %4, 0\n\tv_dot2_f32_bf16 %3, %11, %4, 0\n\tv_dot2_f32_bf16 %0, %6, %7, %0\n\tv_dot2_f32_bf16 %1, %11, %7, %1\n\tv_dot2_f32_bf16 %2, %8, %7, %2\n\tv_dot2_f32_bf16 %3, %12, %7, %3" : "=&v"(kt[0][0]), "=&v"(kt[0][1]), "=&v"(kt[1][0]), "=&v"(kt[1][1]), "=&v"(ku[0]), "=&v"(ku[1]), "=&v"(ku[2]), "=&v"(ku[3]), "=&v"(ku[4]) : "v"(kp[1][0]), "v"(kp[1][1]), "v"(kc[1][0]), "v"(kc[1][1]), "v"(sw[1][0]), "v"(sw[1][1]) : ); SB();
  asm volatile("s_add_u32 m0, %0, 135168" :  : "s"(mA) : "memory", "scc"); SB();
  asm volatile("s_nop 0\n\tbuffer_load_dwordx4 %0, %1, %2 offen lds" :  : "v"(voS), "s"(rA), "s"(ao[4]) : "memory"); SB();
  asm volatile("v_lshlrev_b32 %0, 16, %4\n\tv_and_b32 %1, 0xffff0000, %4\n\tv_lshlrev_b32 %2, 16, %5\n\tv_and_b32 %3, 0xffff0000, %5" : "=&v"(kf[0][0]), "=&v"(kf[0][1]), "=&v"(kf[1][0]), "=&v"(kf[1][1]) : "v"(vv[1][0]), "v"(vv[1][1]) : ); SB();
  kt[0] *= kf[0]; kt[1] *= kf[1]; SB();
  ko[1] = (u32x2){pk2(kt[0]), pk2(kt[1])};
  asm volatile("" : "+v"(ko[1]));
  SB();
  asm volatile("buffer_load_dwordx4 %0, %1, %2 offen offset:1024 lds" :  : "v"(voS), "s"(rA), "s"(ao[5]) : "memory"); SB();
  asm volatile("ds_write_b64 %0, %1 offset:2304" :  : "v"(kw), "v"(ko[1]) : "memory"); SB();
  asm volatile("" : "+v"(kp[2]), "+v"(kc[2]), "+v"(vv[2]), "+v"(sw[2]) :  : "memory"); SB();
  asm volatile("v_alignbit_b32 %4, %14, %14, 16\n\tv_alignbit_b32 %5, %10, %9, 16\n\tv_alignbit_b32 %6, %11, %10, 16\n\tv_alignbit_b32 %7, %13, %13, 16\n\tv_alignbit_b32 %8, %12, %11, 16\n\tv_dot2_f32_bf16 %0, %5, %4, 0\n\tv_dot2_f32_bf16 %1, %10, %4, 0\n\tv_dot2_f32_bf16 %2, %6, %4, 0\n\tv_dot2_f32_bf16 %3, %11, %4, 0\n\tv_dot2_f32_bf16 %0, %6, %7, %0\n\tv_dot2_f32_bf16 %1, %11, %7, %1\n\tv_dot2_f32_bf16 %2, %8, %7, %2\n\tv_dot2_f32_bf16 %3, %12, %7, %3" : "=&v"(kt[0][0]), "=&v"(kt[0][1]), "=&v"(kt[1][0]), "=&v"(kt[1][1]), "=&v"(ku[0]), "=&v"(ku[1]), "=&v"(ku[2]), "=&v"(ku[3]), "=&v"(ku[4]) : "v"(kp[2][0]), "v"(kp[2][1]), "v"(kc[2][0]), "v"(kc[2][1]), "v"(sw[2][0]), "v"(sw[2][1]) : ); SB();
  asm volatile("buffer_load_dwordx4 %0, %1, %2 offen offset:2048 lds" :  : "v"(voS), "s"(rA), "s"(ao[6]) : "memory"); SB();
  asm volatile("v_lshlrev_b32 %0, 16, %4\n\tv_and_b32 %1, 0xffff0000, %4\n\tv_lshlrev_b32 %2, 16, %5\n\tv_and_b32 %3, 0xffff0000, %5" : "=&v"(kf[0][0]), "=&v"(kf[0][1]), "=&v"(kf[1][0]), "=&v"(kf[1][1]) : "v"(vv[2][0]), "v"(vv[2][1]) : ); SB();
  kt[0] *= kf[0]; kt[1] *= kf[1]; SB();
  ko[2] = (u32x2){pk2(kt[0]), pk2(kt[1])};
  asm volatile("" : "+v"(ko[2]));
  SB();
  asm volatile("ds_write_b64 %0, %1 offset:4608" :  : "v"(kw), "v"(ko[2]) : "memory"); SB();
  asm volatile("s_waitcnt lgkmcnt(2)" : "+v"(kp[3]), "+v"(kc[3]), "+v"(vv[3]), "+v"(sw[3]) :  : "memory"); SB();
  asm volatile("v_alignbit_b32 %4, %14, %14, 16\n\tv_alignbit_b32 %5, %10, %9, 16\n\tv_alignbit_b32 %6, %11, %10, 16\n\tv_alignbit_b32 %7, %13, %13, 16\n\tv_alignbit_b32 %8, %12, %11, 16\n\tv_dot2_f32_bf16 %0, %5, %4, 0\n\tv_dot2_f32_bf16 %1, %10, %4, 0\n\tv_dot2_f32_bf16 %2, %6, %4, 0\n\tv_dot2_f32_bf16 %3, %11, %4, 0\n\tv_dot2_f32_bf16 %0, %6, %7, %0\n\tv_dot2_f32_bf16 %1, %11, %7, %1\n\tv_dot2_f32_bf16 %2, %8, %7, %2\n\tv_dot2_f32_bf16 %3, %12, %7, %3" : "=&v"(kt[0][0]), "=&v"(kt[0][1]), "=&v"(kt[1][0]), "=&v"(kt[1][1]), "=&v"(ku[0]), "=&v"(ku[1]), "=&v"(ku[2]), "=&v"(ku[3]), "=&v"(ku[4]) : "v"(kp[3][0]), "v"(kp[3][1]), "v"(kc[3][0]), "v"(kc[3][1]), "v"(sw[3][0]), "v"(sw[3][1]) : ); SB();
  asm volatile("buffer_load_dwordx4 %0, %1, %2 offen offset:3072 lds" :  : "v"(voS), "s"(rA), "s"(ao[7]) : "memory"); SB();
  asm volatile("v_xor_b32 %0, 128, %0" : "+v"(voS) :  : "memory"); SB();
  asm volatile("v_lshlrev_b32 %0, 16, %4\n\tv_and_b32 %1, 0xffff0000, %4\n\tv_lshlrev_b32 %2, 16, %5\n\tv_and_b32 %3, 0xffff0000, %5" : "=&v"(kf[0][0]), "=&v"(kf[0][1]), "=&v"(kf[1][0]), "=&v"(kf[1][1]) : "v"(vv[3][0]), "v"(vv[3][1]) : ); SB();
  kt[0] *= kf[0]; kt[1] *= kf[1]; SB();
  ko[3] = (u32x2){pk2(kt[0]), pk2(kt[1])};
  asm volatile("" : "+v"(ko[3]));
  SB();
  asm volatile("s_add_u32 m0, %0, 98304" :  : "s"(mA) : "memory", "scc"); SB();
  asm volatile("s_nop 0\n\tbuffer_load_dwordx4 %0, %1, %2 offen lds" :  : "v"(voS), "s"(rA), "s"(ao[0]) : "memory"); SB();
  asm volatile("ds_write_b64 %0, %1 offset:6912" :  : "v"(kw), "v"(ko[3]) : "memory"); SB();
  asm volatile("" : "+v"(kp[4]), "+v"(kc[4]), "+v"(vv[4]), "+v"(sw[4]) :  : "memory"); SB();
  asm volatile("v_alignbit_b32 %4, %14, %14, 16\n\tv_alignbit_b32 %5, %10, %9, 16\n\tv_alignbit_b32 %6, %11, %10, 16\n\tv_alignbit_b32 %7, %13, %13, 16\n\tv_alignbit_b32 %8, %12, %11, 16\n\tv_dot2_f32_bf16 %0, %5, %4, 0\n\tv_dot2_f32_bf16 %1, %10, %4, 0\n\tv_dot2_f32_bf16 %2, %6, %4, 0\n\tv_dot2_f32_bf16 %3, %11, %4, 0\n\tv_dot2_f32_bf16 %0, %6, %7, %0\n\tv_dot2_f32_bf16 %1, %11, %7, %1\n\tv_dot2_f32_bf16 %2, %8, %7, %2\n\tv_dot2_f32_bf16 %3, %12, %7, %3" : "=&v"(kt[0][0]), "=&v"(kt[0][1]), "=&v"(kt[1][0]), "=&v"(kt[1][1]), "=&v"(ku[0]), "=&v"(ku[1]), "=&v"(ku[2]), "=&v"(ku[3]), "=&v"(ku[4]) : "v"(kp[4][0]), "v"(kp[4][1]), "v"(kc[4][0]), "v"(kc[4][1]), "v"(sw[4][0]), "v"(sw[4][1]) : ); SB();
  asm volatile("buffer_load_dwordx4 %0, %1, %2 offen offset:1024 lds" :  : "v"(voS), "s"(rA), "s"(ao[1]) : "memory"); SB();
  asm volatile("v_lshlrev_b32 %0, 16, %4\n\tv_and_b32 %1, 0xffff0000, %4\n\tv_lshlrev_b32 %2, 16, %5\n\tv_and_b32 %3, 0xffff0000, %5" : "=&v"(kf[0][0]), "=&v"(kf[0][1]), "=&v"(kf[1][0]), "=&v"(kf[1][1]) : "v"(vv[4][0]), "v"(vv[4][1]) : ); SB();
  kt[0] *= kf[0]; kt[1] *= kf[1]; SB();
  ko[4] = (u32x2){pk2(kt[0]), pk2(kt[1])};
  asm volatile("" : "+v"(ko[4]));
  SB();
  asm volatile("ds_write_b64 %0, %1 offset:9216" :  : "v"(kw), "v"(ko[4]) : "memory"); SB();
  ST(16);
  asm volatile("s_waitcnt vmcnt(26) lgkmcnt(0)\n\ts_barrier" :  :  : "memory"); SB();
  asm volatile("ds_read_b128 %0, %1 offset:0" : "=&v"(kq[0]) : "v"(kr) : "memory"); SB();
  asm volatile("ds_read_b128 %0, %1 offset:1152" : "=&v"(kq[1]) : "v"(kr) : "memory"); SB();
  asm volatile("ds_read_b128 %0, %1 offset:2304" : "=&v"(kq[2]) : "v"(kr) : "memory"); SB();
  asm volatile("ds_read_b64_tr_b16 v[184:185], %0 offset:24576" :  : "v"(aW[0][0][0]) : "memory"); SB();
  asm volatile("ds_read_b64_tr_b16 v[186:187], %0 offset:25344" :  : "v"(aW[0][0][0]) : "memory"); SB();
  asm volatile("ds_read_b64_tr_b16 v[188:189], %0 offset:24576" :  : "v"(aW[0][0][1]) : "memory"); SB();
  asm volatile("ds_read_b64_tr_b16 v[190:191], %0 offset:25344" :  : "v"(aW[0][0][1]) : "memory"); SB();
  asm volatile("ds_read_b64_tr_b16 v[192:193], %0 offset:24576" :  : "v"(aW[0][0][2]) : "memory"); SB();
  asm volatile("ds_read_b64_tr_b16 v[194:195], %0 offset:25344" :  : "v"(aW[0][0][2]) : "memory"); SB();
  ST(7);
  asm volatile("s_waitcnt lgkmcnt(6)" : "+v"(kq[0]), "+v"(kq[1]), "+v"(kq[2]) :  : "memory"); SB();
  unsigned kz[3] = {kq[0][3], kq[1][3] | (w + 1) >> 2, kq[2][3] | (w + 1) >> 2};
  asm volatile("" : "+v"(kz[0]), "+v"(kz[1]), "+v"(kz[2])); SB();
  asm volatile("buffer_load_dwordx4 v[196:199], %0, %1, %2 offen offset:0 sc1" :  : "v"(vS), "s"(rWS), "s"(0u) : "memory"); SB();
  asm volatile("buffer_load_dwordx4 v[200:203], %0, %1, %2 offen offset:1024 sc1" :  : "v"(vS), "s"(rWS), "s"(0u) : "memory"); SB();
  asm volatile("s_waitcnt lgkmcnt(3)\n\tv_mfma_f32_16x16x32_bf16 %0, v[184:187], a[0:3], 0" : "=&v"(a1[0][0]) :  : ); SB();
  asm volatile("ds_read_b64_tr_b16 v[160:161], %0 offset:24576" :  : "v"(aW[0][1][0]) : "memory"); SB();
  asm volatile("v_mfma_f32_16x16x32_bf16 %0, v[184:187], a[4:7], 0" : "=&v"(a1[1][0]) :  : ); SB();
  asm volatile("ds_read_b64_tr_b16 v[162:163], %0 offset:25344" :  : "v"(aW[0][1][0]) : "memory"); SB();
  asm volatile("v_mfma_f32_16x16x32_bf16 %0, v[184:187], %1, 0" : "=&v"(c1[0]) : "v"(ones) : ); SB();
  asm volatile("ds_read_b64_tr_b16 v[164:165], %0 offset:24576" :  : "v"(aW[0][1][1]) : "memory"); SB();
  asm volatile("s_waitcnt lgkmcnt(3)\n\tv_mfma_f32_16x16x32_bf16 %0, v[188:191], a[0:3], 0" : "=&v"(a1[0][1]) :  : ); SB();
  asm volatile("ds_read_b64_tr_b16 v[166:167], %0 offset:25344" :  : "v"(aW[0][1][1]) : "memory"); SB();
  asm volatile("v_mfma_f32_16x16x32_bf16 %0, v[188:191], a[4:7], 0" : "=&v"(a1[1][1]) :  : ); SB();
  asm volatile("ds_read_b64_tr_b16 v[168:169], %0 offset:24576" :  : "v"(aW[0][1][2]) : "memory"); SB();
  asm volatile("v_mfma_f32_16x16x32_bf16 %0, v[188:191], %1, 0" : "=&v"(c1[1]) : "v"(ones) : ); SB();
  asm volatile("ds_read_b64_tr_b16 v[170:171], %0 offset:25344" :  : "v"(aW[0][1][2]) : "memory"); SB();
  asm volatile("v_mfma_f32_16x16x32_bf16 %0, v[192:195], a[0:3], 0" : "=&v"(a1[0][2]) :  : ); SB();
  asm volatile("ds_read_b64_tr_b16 v[172:173], %0 offset:36864" :  : "v"(aW[0][0][0]) : "memory"); SB();
  asm volatile("v_mfma_f32_16x16x32_bf16 %0, v[192:195], a[4:7], 0" : "=&v"(a1[1][2]) :  : ); SB();
  asm volatile("ds_read_b64_tr_b16 v[174:175], %0 offset:37632" :  : "v"(aW[0][0][0]) : "memory"); SB();
  asm volatile("v_mfma_f32_16x16x32_bf16 %0, v[192:195], %1, 0" : "=&v"(c1[2]) : "v"(ones) : ); SB();
  asm volatile("ds_read_b64_tr_b16 v[176:177], %0 offset:36864" :  : "v"(aW[0][0][1]) : "memory"); SB();
  asm volatile("s_waitcnt lgkmcnt(7)\n\tv_mfma_f32_16x16x32_bf16 %0, v[160:163], a[8:11], %0" : "+v"(a1[0][0]) :  : ); SB();
  asm volatile("ds_read_b64_tr_b16 v[178:179], %0 offset:37632" :  : "v"(aW[0][0][1]) : "memory"); SB();
  asm volatile("v_mfma_f32_16x16x32_bf16 %0, v[160:163], a[12:15], %0" : "+v"(a1[1][0]) :  : ); SB();
  asm volatile("ds_read_b64_tr_b16 v[180:181], %0 offset:36864" :  : "v"(aW[0][0][2]) : "memory"); SB();
  asm volatile("buffer_load_dwordx4 v[204:207], %0, %1, %2 offen offset:2048 sc1" :  : "v"(vS), "s"(rWS), "s"(0u) : "memory"); SB();
  asm volatile("s_waitcnt lgkmcnt(7)\n\tv_mfma_f32_16x16x32_bf16 %0, v[164:167], a[8:11], %0" : "+v"(a1[0][1]) :  : ); SB();
  asm volatile("ds_read_b64_tr_b16 v[182:183], %0 offset:37632" :  : "v"(aW[0][0][2]) : "memory"); SB();
  asm volatile("v_mfma_f32_16x16x32_bf16 %0, v[164:167], a[12:15], %0" : "+v"(a1[1][1]) :  : ); SB();
  asm volatile("ds_read_b64_tr_b16 v[184:185], %0 offset:36864" :  : "v"(aW[0][1][0]) : "memory"); SB();
  asm volatile("s_waitcnt lgkmcnt(7)\n\tv_mfma_f32_16x16x32_bf16 %0, v[168:171], a[8:11], %0" : "+v"(a1[0][2]) :  : ); SB();
  asm volatile("ds_read_b64_tr_b16 v[186:187], %0 offset:37632" :  : "v"(aW[0][1][0]) : "memory"); SB();
  asm volatile("v_mfma_f32_16x16x32_bf16 %0, v[168:171], a[12:15], %0" : "+v"(a1[1][2]) :  : ); SB();
  asm volatile("ds_read_b64_tr_b16 v[188:189], %0 offset:36864" :  : "v"(aW[0][1][1]) : "memory"); SB();
  asm volatile("s_waitcnt lgkmcnt(7)\n\tv_mfma_f32_16x16x32_bf16 %0, v[172:175], a[16:19], %0" : "+v"(a1[0][0]) :  : ); SB();
  asm volatile("ds_read_b64_tr_b16 v[190:191], %0 offset:37632" :  : "v"(aW[0][1][1]) : "memory"); SB();
  asm volatile("buffer_load_dwordx4 v[208:211], %0, %1, %2 offen offset:3072 sc1" :  : "v"(vS), "s"(rWS), "s"(0u) : "memory"); SB();
  asm volatile("v_mfma_f32_16x16x32_bf16 %0, v[172:175], a[20:23], %0" : "+v"(a1[1][0]) :  : ); SB();
  asm volatile("ds_read_b64_tr_b16 v[192:193], %0 offset:36864" :  : "v"(aW[0][1][2]) : "memory"); SB();
  asm volatile("v_mfma_f32_16x16x32_bf16 %0, v[172:175], %1, %0" : "+v"(c1[0]) : "v"(ones) : ); SB();
  asm volatile("ds_read_b64_tr_b16 v[194:195], %0 offset:37632" :  : "v"(aW[0][1][2]) : "memory"); SB();
  asm volatile("s_waitcnt lgkmcnt(8)\n\tv_mfma_f32_16x16x32_bf16 %0, v[176:179], a[16:19], %0" : "+v"(a1[0][1]) :  : ); SB();
  asm volatile("s_waitcnt vmcnt(24)\n\ts_barrier" :  :  : "memory"); SB();
  asm volatile("ds_read_b64_tr_b16 v[160:161], %0 offset:0" :  : "v"(aW[1][0][0]) : "memory"); SB();
  asm volatile("buffer_load_dwordx4 v[212:215], %0, %1, %2 offen offset:0 sc1" :  : "v"(vS), "s"(rWS), "s"(4096u) : "memory"); SB();
  asm volatile("v_mfma_f32_16x16x32_bf16 %0, v[176:179], a[20:23], %0" : "+v"(a1[1][1]) :  : ); SB();
  asm volatile("ds_read_b64_tr_b16 v[162:163], %0 offset:768" :  : "v"(aW[1][0][0]) : "memory"); SB();
  asm volatile("v_mfma_f32_16x16x32_bf16 %0, v[176:179], %1, %0" : "+v"(c1[1]) : "v"(ones) : ); SB();
  asm volatile("ds_read_b64_tr_b16 v[164:165], %0 offset:0" :  : "v"(aW[1][0][1]) : "memory"); SB();
  asm volatile("buffer_load_dwordx4 v[216:219], %0, %1, %2 offen offset:1024 sc1" :  : "v"(vS), "s"(rWS), "s"(4096u) : "memory"); SB();
  asm volatile("s_waitcnt lgkmcnt(6)\n\tv_mfma_f32_16x16x32_bf16 %0, v[180:183], a[16:19], %0" : "+v"(a1[0][2]) :  : ); SB();
  asm volatile("ds_read_b64_tr_b16 v[166:167], %0 offset:768" :  : "v"(aW[1][0][1]) : "memory"); SB();
  asm volatile("s_waitcnt vmcnt(14)" :  :  : "memory"); SB();
  asm volatile("v_mfma_f32_16x16x32_bf16 %0, v[180:183], a[20:23], %0" : "+v"(a1[1][2]) :  : ); SB();
  asm volatile("ds_read_b64_tr_b16 v[168:169], %0 offset:0" :  : "v"(aW[1][0][2]) : "memory"); SB();
  asm volatile("v_mfma_f32_16x16x32_bf16 %0, v[180:183], %1, %0" : "+v"(c1[2]) : "v"(ones) : ); SB();
  asm volatile("ds_read_b64_tr_b16 v[170:171], %0 offset:768" :  : "v"(aW[1][0][2]) : "memory"); SB();
  asm volatile("v_mfma_f32_16x16x32_bf16 %0, v[184:187], a[24:27], %0" : "+v"(a1[0][0]) :  : ); SB();
  asm volatile("ds_read_b64_tr_b16 v[172:173], %0 offset:0" :  : "v"(aW[1][1][0]) : "memory"); SB();
  asm volatile("v_mfma_f32_16x16x32_bf16 %0, v[184:187], a[28:31], %0" : "+v"(a1[1][0]) :  : ); SB();
  asm volatile("ds_read_b64_tr_b16 v[174:175], %0 offset:768" :  : "v"(aW[1][1][0]) : "memory"); SB();
  asm volatile("s_waitcnt lgkmcnt(8)\n\tv_mfma_f32_16x16x32_bf16 %0, v[188:191], a[24:27], %0" : "+v"(a1[0][1]) :  : ); SB();
  asm volatile("ds_read_b64_tr_b16 v[176:177], %0 offset:0" :  : "v"(aW[1][1][1]) : "memory"); SB();
  asm volatile("v_mfma_f32_16x16x32_bf16 %0, v[188:191], a[28:31], %0" : "+v"(a1[1][1]) :  : ); SB();
  asm volatile("ds_read_b64_tr_b16 v[178:179], %0 offset:768" :  : "v"(aW[1][1][1]) : "memory"); SB();
  asm volatile("v_mfma_f32_16x16x32_bf16 %0, v[192:195], a[24:27], %0" : "+v"(a1[0][2]) :  : ); SB();
  asm volatile("ds_read_b64_tr_b16 v[180:181], %0 offset:0" :  : "v"(aW[1][1][2]) : "memory"); SB();
  asm volatile("buffer_load_dwordx4 v[220:223], %0, %1, %2 offen offset:2048 sc1" :  : "v"(vS), "s"(rWS), "s"(4096u) : "memory"); SB();
  asm volatile("v_mfma_f32_16x16x32_bf16 %0, v[192:195], a[28:31], %0" : "+v"(a1[1][2]) :  : ); SB();
  asm volatile("ds_read_b64_tr_b16 v[182:183], %0 offset:768" :  : "v"(aW[1][1][2]) : "memory"); SB();
  ST(8);
  unsigned zwa[2] = {lb + 98304 + 4096 + w * 8192 + (wn * 16 + l15) * 104 + g4 * 8, lb + 98304 + 4096 + w * 8192 + ((wn ^ 1) * 16 + l15) * 104 + g4 * 8};
  unsigned zra = lb + 98304 + 4096 + w * 8192 + (8 * g4 + q4) * 104 + 8 * p4, vZ_ = vZ;
  asm volatile("" : "+v"(zwa[0]), "+v"(zwa[1]), "+v"(zra), "+v"(vZ_));
  SB();
  asm volatile("buffer_load_dwordx4 v[224:227], %0, %1, %2 offen offset:3072 sc1" :  : "v"(vS), "s"(rWS), "s"(4096u) : "memory"); SB();
  asm volatile("buffer_store_dwordx4 %1, %0, %2, %3 offen offset:0 sc1\n\ts_nop 1" :  : "v"(vKV), "v"(kq[0]), "s"(rWS), "s"(0u) : "memory"); SB();
  asm volatile("ds_read_b128 a[160:163], %0 offset:32768" :  : "v"(aA[0]) : "memory"); SB();
  asm volatile("ds_read_b128 a[176:179], %0 offset:32768" :  : "v"(aA[1]) : "memory"); SB();
  asm volatile("s_waitcnt vmcnt(15)" :  :  : "memory"); SB();
  asm volatile("ds_read_b128 a[164:167], %0 offset:34816" :  : "v"(aA[0]) : "memory"); SB();
  asm volatile("ds_read_b128 a[180:183], %0 offset:34816" :  : "v"(aA[1]) : "memory"); SB();
  asm volatile("buffer_store_dwordx4 %1, %0, %2, %3 offen offset:0 sc1\n\ts_nop 1" :  : "v"(vKV), "v"(kq[1]), "s"(rWS), "s"(w3k[0]) : "memory"); SB();
  asm volatile("s_waitcnt vmcnt(14)" :  :  : "memory"); SB();
  asm volatile("ds_read_b128 a[168:171], %0 offset:36864" :  : "v"(aA[0]) : "memory"); SB();
  asm volatile("ds_read_b128 a[184:187], %0 offset:36864" :  : "v"(aA[1]) : "memory"); SB();
  asm volatile("s_waitcnt vmcnt(12)" :  :  : "memory"); SB();
  asm volatile("ds_write_b64 %0, %1 offset:0" :  : "v"(zwa[0]), "v"(((u32x2){zp[0][0][0], zp[0][0][1]})) : "memory"); SB();
  asm volatile("ds_write_b64 %0, %1 offset:32" :  : "v"(zwa[0]), "v"(((u32x2){zp[0][1][0], zp[0][1][1]})) : "memory"); SB();
  asm volatile("ds_write_b64 %0, %1 offset:64" :  : "v"(zwa[0]), "v"(((u32x2){zp[0][2][0], zp[0][2][1]})) : "memory"); SB();
  asm volatile("buffer_store_dwordx4 %1, %0, %2, %3 offen offset:0 sc1\n\ts_nop 1" :  : "v"(vKV), "v"(kq[2]), "s"(rWS), "s"(w3k[1]) : "memory"); SB();
  asm volatile("buffer_store_dwordx4 %1, %0, %2, %3 offen offset:0 sc1\n\ts_nop 1" :  : "v"(vWO), "v"(((u32x4){wt[0][0], wt[0][1], wt[1][0], wt[1][1]})), "s"(rWS), "s"(0u) : "memory"); SB();
  asm volatile("ds_read_b128 a[172:175], %0 offset:38912" :  : "v"(aA[0]) : "memory"); SB();
  asm volatile("ds_read_b128 a[188:191], %0 offset:38912" :  : "v"(aA[1]) : "memory"); SB();
  asm volatile("s_waitcnt vmcnt(12)" :  :  : "memory"); SB();
  asm volatile("ds_read_b128 a[192:195], %0 offset:0" :  : "v"(aA[0]) : "memory"); SB();
  asm volatile("ds_write_b64 %0, %1 offset:0" :  : "v"(zwa[1]), "v"(((u32x2){zp[1][0][0], zp[1][0][1]})) : "memory"); SB();
  asm volatile("ds_write_b64 %0, %1 offset:32" :  : "v"(zwa[1]), "v"(((u32x2){zp[1][1][0], zp[1][1][1]})) : "memory"); SB();
  asm volatile("ds_write_b64 %0, %1 offset:64" :  : "v"(zwa[1]), "v"(((u32x2){zp[1][2][0], zp[1][2][1]})) : "memory"); SB();
  asm volatile("ds_read_b128 a[208:211], %0 offset:0" :  : "v"(aA[1]) : "memory"); SB();
  asm volatile("ds_read_b64_tr_b16 %0, %1 offset:0" : "=&v"(zt[0][0]) : "v"(zra) : "memory"); SB();
  asm volatile("ds_read_b64_tr_b16 %0, %1 offset:416" : "=&v"(zt[0][1]) : "v"(zra) : "memory"); SB();
  asm volatile("ds_read_b64_tr_b16 %0, %1 offset:32" : "=&v"(zt[1][0]) : "v"(zra) : "memory"); SB();
  asm volatile("ds_read_b64_tr_b16 %0, %1 offset:448" : "=&v"(zt[1][1]) : "v"(zra) : "memory"); SB();
  asm volatile("ds_read_b64_tr_b16 %0, %1 offset:64" : "=&v"(zt[2][0]) : "v"(zra) : "memory"); SB();
  asm volatile("ds_read_b64_tr_b16 %0, %1 offset:480" : "=&v"(zt[2][1]) : "v"(zra) : "memory"); SB();
  asm volatile("s_waitcnt lgkmcnt(0)" : "+v"(zt[0][0]), "+v"(zt[0][1]), "+v"(zt[1][0]), "+v"(zt[1][1]), "+v"(zt[2][0]), "+v"(zt[2][1]) :  : "memory"); SB();
  asm volatile("s_add_u32 m0, %0, 12288" :  : "s"(mW) : "memory", "scc"); SB();
  asm volatile("s_nop 0\n\tbuffer_load_dwordx4 %0, %1, %2 offen lds" :  : "v"(voB[0]), "s"(rW1), "s"(kw_[7]) : "memory"); SB();
  asm volatile("buffer_load_dwordx4 %0, %1, %2 offen offset:1024 lds" :  : "v"(voB[1]), "s"(rW1), "s"(kw_[7]) : "memory"); SB();
  asm volatile("buffer_load_dwordx4 %0, %1, %2 offen offset:2048 lds" :  : "v"(voB[2]), "s"(rW1), "s"(kw_[7]) : "memory"); SB();
  asm volatile("s_add_u32 m0, %0, 24576" :  : "s"(mW) : "memory", "scc"); SB();
  asm volatile("s_nop 0\n\tbuffer_load_dwordx4 %0, %1, %2 offen lds" :  : "v"(voB[0]), "s"(rW1), "s"(kw_[8]) : "memory"); SB();
  asm volatile("buffer_load_dwordx4 %0, %1, %2 offen offset:1024 lds" :  : "v"(voB[1]), "s"(rW1), "s"(kw_[8]) : "memory"); SB();
  asm volatile("buffer_load_dwordx4 %0, %1, %2 offen offset:2048 lds" :  : "v"(voB[2]), "s"(rW1), "s"(kw_[8]) : "memory"); SB();
  asm volatile("s_add_u32 m0, %0, 98304" :  : "s"(mA) : "memory", "scc"); SB();
  asm volatile("s_nop 0\n\tbuffer_load_dwordx4 %0, %1, %2 offen offset:2048 lds" :  : "v"(voS), "s"(rA), "s"(ao[2]) : "memory"); SB();
  asm volatile("s_waitcnt vmcnt(11)\n\t.Lc_%=:\n\tv_cmpx_eq_u64 vcc, %1, v[198:199]\n\tv_cmpx_eq_u64 vcc, %1, v[202:203]\n\tv_cmpx_eq_u64 vcc, %1, v[206:207]\n\tv_cmpx_eq_u64 vcc, %1, v[210:211]\n\tv_cmpx_eq_u64 vcc, %1, v[214:215]\n\tv_cmpx_eq_u64 vcc, %1, v[218:219]\n\tv_cmpx_eq_u64 vcc, %1, v[222:223]\n\tv_cmpx_eq_u64 vcc, %1, v[226:227]\n\ts_cmp_eq_u64 vcc, -1\n\ts_mov_b64 exec, -1\n\ts_cbranch_scc0 .Lr_%=\n\t.Ld_%=:\n\t.subsection 1\n\t.Lr_%=:\n\tv_add_u32 %0, -1, %0\n\ts_nop 1\n\tv_readfirstlane_b32 vcc_hi, %0\n\ts_nop 3\n\ts_cmp_lt_i32 vcc_hi, 1\n\ts_cbranch_scc1 .Ld_%=\n\ts_sleep 2\n\ts_movk_i32 vcc_lo, 0x1000\n\tbuffer_load_dwordx4 v[196:199], %2, %3, 0 offen offset:0 sc1\n\tbuffer_load_dwordx4 v[200:203], %2, %3, 0 offen offset:1024 sc1\n\tbuffer_load_dwordx4 v[204:207], %2, %3, 0 offen offset:2048 sc1\n\tbuffer_load_dwordx4 v[208:211], %2, %3, 0 offen offset:3072 sc1\n\tbuffer_load_dwordx4 v[212:215], %2, %3, vcc_lo offen offset:0 sc1\n\tbuffer_load_dwordx4 v[216:219], %2, %3, vcc_lo offen offset:1024 sc1\n\tbuffer_load_dwordx4 v[220:223], %2, %3, vcc_lo offen offset:2048 sc1\n\tbuffer_load_dwordx4 v[224:227], %2, %3, vcc_lo offen offset:3072 sc1\n\ts_waitcnt vmcnt(0)\n\ts_branch .Lc_%=\n\t.subsection 0" : "+v"(tmo) : "s"(tg), "v"(vS), "s"(rWS) : "memory", "vcc", "scc"); SB();
  ST(9);
  {
    f32x2 s4[4], dm[4];
  asm volatile("v_pk_add_f32 %0, v[196:197], v[200:201]\n\tv_pk_add_f32 %1, v[204:205], v[208:209]\n\tv_pk_add_f32 %2, v[212:213], v[216:217]\n\tv_pk_add_f32 %3, v[220:221], v[224:225]" : "=&v"(s4[0]), "=&v"(s4[1]), "=&v"(s4[2]), "=&v"(s4[3]) :  : ); SB();
    const f32x2 sm = (s4[0] + s4[1]) + (s4[2] + s4[3]);
    const float mean = (sm[0] + dppf<0x128>(sm[0])) * (1.0f / 16.0f);
  asm volatile("v_sub_f32 %0, v196, %8\n\tv_sub_f32 %1, v200, %8\n\tv_sub_f32 %2, v204, %8\n\tv_sub_f32 %3, v208, %8\n\tv_sub_f32 %4, v212, %8\n\tv_sub_f32 %5, v216, %8\n\tv_sub_f32 %6, v220, %8\n\tv_sub_f32 %7, v224, %8" : "=&v"(dm[0][0]), "=&v"(dm[0][1]), "=&v"(dm[1][0]), "=&v"(dm[1][1]), "=&v"(dm[2][0]), "=&v"(dm[2][1]), "=&v"(dm[3][0]), "=&v"(dm[3][1]) : "v"(mean) : ); SB();
    const f32x2 dq = __builtin_elementwise_fma(dm[0], dm[0], dm[1] * dm[1]) + __builtin_elementwise_fma(dm[2], dm[2], dm[3] * dm[3]);
    float qs = __builtin_fmaf(dq[0] + dq[1], 48.0f, sm[1]);
    qs += dppf<0x128>(qs);
    float r = __builtin_amdgcn_rcpf(rbf(__builtin_amdgcn_sqrtf(rbf(rbf(qs * (1.0f / 768.0f)) + 1e-5f))));
    float mr = -rbf(mean) * r;
    asm volatile("" : "+v"(r), "+v"(mr)); SB();
  asm volatile("buffer_load_dwordx4 %0, %1, %2 offen offset:3072 lds" :  : "v"(voS), "s"(rA), "s"(ao[3]) : "memory"); SB();
  asm volatile("s_add_u32 m0, %0, 102400" :  : "s"(mA) : "memory", "scc"); SB();
  asm volatile("s_nop 0\n\tbuffer_load_dwordx4 %0, %1, %2 offen lds" :  : "v"(voS), "s"(rA), "s"(ao[4]) : "memory"); SB();
    const f32x2 re[4] = {{dppf<0x150>(r), dppf<0x151>(r)}, {dppf<0x152>(r), dppf<0x153>(r)}, {dppf<0x154>(r), dppf<0x155>(r)}, {dppf<0x156>(r), dppf<0x157>(r)}};
    const f32x2 me[4] = {{dppf<0x150>(mr), dppf<0x151>(mr)}, {dppf<0x152>(mr), dppf<0x153>(mr)}, {dppf<0x154>(mr), dppf<0x155>(mr)}, {dppf<0x156>(mr), dppf<0x157>(mr)}};
    u32x4 zo[3];
#pragma unroll
    for (int e = 0; e < 4; ++e) zo[0][e] = pk2(__builtin_elementwise_fma(unpk(zt[0][e >> 1][e & 1]), re[e], me[e]));
    SB();
  asm volatile("buffer_store_dwordx4 %1, %0, %2, %3 offen offset:0 sc1\n\ts_nop 1" :  : "v"(vZ_), "v"(zo[0]), "s"(rWS), "s"(0u) : "memory"); SB();
#pragma unroll
    for (int e = 0; e < 4; ++e) zo[1][e] = pk2(__builtin_elementwise_fma(unpk(zt[1][e >> 1][e & 1]), re[e], me[e]));
    SB();
  asm volatile("buffer_store_dwordx4 %1, %0, %2, %3 offen offset:1024 sc1\n\ts_nop 1" :  : "v"(vZ_), "v"(zo[1]), "s"(rWS), "s"(0u) : "memory"); SB();
#pragma unroll
    for (int e = 0; e < 4; ++e) zo[2][e] = pk2(__builtin_elementwise_fma(unpk(zt[2][e >> 1][e & 1]), re[e], me[e]));
    SB();
  asm volatile("buffer_store_dwordx4 %1, %0, %2, %3 offen offset:2048 sc1\n\ts_nop 1" :  : "v"(vZ_), "v"(zo[2]), "s"(rWS), "s"(0u) : "memory"); SB();
  }
  ST(10);
  asm volatile("v_mfma_f32_16x16x32_bf16 %0, v[160:163], a[32:35], %0" : "+v"(a1[0][0]) :  : ); SB();
  asm volatile("ds_read_b64_tr_b16 v[184:185], %0 offset:12288" :  : "v"(aW[1][0][0]) : "memory"); SB();
  asm volatile("v_mfma_f32_16x16x32_bf16 %0, v[160:163], a[36:39], %0" : "+v"(a1[1][0]) :  : ); SB();
  asm volatile("ds_read_b64_tr_b16 v[186:187], %0 offset:13056" :  : "v"(aW[1][0][0]) : "memory"); SB();
  asm volatile("v_mfma_f32_16x16x32_bf16 %0, v[160:163], %1, %0" : "+v"(c1[0]) : "v"(ones) : ); SB();
  asm volatile("ds_read_b64_tr_b16 v[188:189], %0 offset:12288" :  : "v"(aW[1][0][1]) : "memory"); SB();
  asm volatile("v_mfma_f32_16x16x32_bf16 %0, v[164:167], a[32:35], %0" : "+v"(a1[0][1]) :  : ); SB();
  asm volatile("ds_read_b64_tr_b16 v[190:191], %0 offset:13056" :  : "v"(aW[1][0][1]) : "memory"); SB();
  asm volatile("v_mfma_f32_16x16x32_bf16 %0, v[164:167], a[36:39], %0" : "+v"(a1[1][1]) :  : ); SB();
  asm volatile("ds_read_b64_tr_b16 v[192:193], %0 offset:12288" :  : "v"(aW[1][0][2]) : "memory"); SB();
  asm volatile("v_mfma_f32_16x16x32_bf16 %0, v[164:167], %1, %0" : "+v"(c1[1]) : "v"(ones) : ); SB();
  asm volatile("ds_read_b64_tr_b16 v[194:195], %0 offset:13056" :  : "v"(aW[1][0][2]) : "memory"); SB();
  asm volatile("v_mfma_f32_16x16x32_bf16 %0, v[168:171], a[32:35], %0" : "+v"(a1[0][2]) :  : ); SB();
  ca = lb + 57344 + w * 192 + g4 * 16; asm volatile("" : "+v"(ca)); SB();
  asm volatile("v_mfma_f32_16x16x32_bf16 %0, v[168:171], a[36:39], %0" : "+v"(a1[1][2]) :  : ); SB();
  cr = lb + 57344 + (w ^ 2) * 192 + g4 * 16; asm volatile("" : "+v"(cr)); SB();
  asm volatile("v_mfma_f32_16x16x32_bf16 %0, v[168:171], %1, %0" : "+v"(c1[2]) : "v"(ones) : ); SB();
  asm volatile("buffer_load_dwordx4 %0, %1, %2 offen offset:1024 lds" :  : "v"(voS), "s"(rA), "s"(ao[5]) : "memory"); SB();
  asm volatile("v_mfma_f32_16x16x32_bf16 %0, v[172:175], a[40:43], %0" : "+v"(a1[0][0]) :  : ); SB();
  asm volatile("ds_read_b64_tr_b16 v[160:161], %0 offset:12288" :  : "v"(aW[1][1][0]) : "memory"); SB();
  asm volatile("s_waitcnt vmcnt(13)\n\ts_mov_b64 exec, 1\n\tbuffer_store_dwordx2 %0, off, %1, %2 sc1\n\ts_mov_b64 exec, -1" :  : "v"(((u32x2){tg0, tg1})), "s"(rWS), "s"(sFK) : "memory"); SB();
  asm volatile("v_mfma_f32_16x16x32_bf16 %0, v[172:175], a[44:47], %0" : "+v"(a1[1][0]) :  : ); SB();
  asm volatile("ds_read_b64_tr_b16 v[162:163], %0 offset:13056" :  : "v"(aW[1][1][0]) : "memory"); SB();
  asm volatile("v_lshlrev_b32 %0, 16, v245" : "=v"(dD[0]) :  : ); SB();
  asm volatile("v_mfma_f32_16x16x32_bf16 %0, v[176:179], a[40:43], %0" : "+v"(a1[0][1]) :  : ); SB();
  asm volatile("ds_read_b64_tr_b16 v[164:165], %0 offset:12288" :  : "v"(aW[1][1][1]) : "memory"); SB();
  asm volatile("v_lshlrev_b32 %0, 16, v246" : "=v"(dD[1]) :  : ); SB();
  asm volatile("v_mfma_f32_16x16x32_bf16 %0, v[176:179], a[44:47], %0" : "+v"(a1[1][1]) :  : ); SB();
  asm volatile("ds_read_b64_tr_b16 v[166:167], %0 offset:13056" :  : "v"(aW[1][1][1]) : "memory"); SB();
  asm volatile("v_lshlrev_b32 %0, 16, v247" : "=v"(dD[2]) :  : ); SB();
  asm volatile("v_mfma_f32_16x16x32_bf16 %0, v[180:183], a[40:43], %0" : "+v"(a1[0][2]) :  : ); SB();
  asm volatile("ds_read_b64_tr_b16 v[168:169], %0 offset:12288" :  : "v"(aW[1][1][2]) : "memory"); SB();
  asm volatile("v_lshlrev_b32 %0, 16, v248" : "=v"(bs) :  : ); SB();
  asm volatile("v_mfma_f32_16x16x32_bf16 %0, v[180:183], a[44:47], %0" : "+v"(a1[1][2]) :  : ); SB();
  asm volatile("ds_read_b64_tr_b16 v[170:171], %0 offset:13056" :  : "v"(aW[1][1][2]) : "memory"); SB();
  asm volatile("s_waitcnt lgkmcnt(6)\n\tv_mfma_f32_16x16x32_bf16 %0, v[184:187], a[48:51], %0" : "+v"(a1[0][0]) :  : ); SB();
  asm volatile("s_barrier" :  :  : "memory"); SB();
  asm volatile("ds_read_b64_tr_b16 v[172:173], %0 offset:24576" :  : "v"(aW[1][0][0]) : "memory"); SB();
  asm volatile("s_add_u32 m0, %0, 36864" :  : "s"(mW) : "memory", "scc"); SB();
  asm volatile("v_mfma_f32_16x16x32_bf16 %0, v[184:187], a[52:55], %0" : "+v"(a1[1][0]) :  : ); SB();
  asm volatile("ds_read_b64_tr_b16 v[174:175], %0 offset:25344" :  : "v"(aW[1][0][0]) : "memory"); SB();
  asm volatile("v_mov_b32_dpp v242, v231 wave_shr:1 row_mask:0xf bank_mask:0xf" :  :  : ); SB();
  asm volatile("v_mfma_f32_16x16x32_bf16 %0, v[184:187], %1, %0" : "+v"(c1[0]) : "v"(ones) : ); SB();
  asm volatile("ds_read_b64_tr_b16 v[176:177], %0 offset:24576" :  : "v"(aW[1][0][1]) : "memory"); SB();
  asm volatile("v_alignbit_b32 %0, v231, v231, 16" : "=v"(im[0][0]) :  : ); SB();
  asm volatile("v_mfma_f32_16x16x32_bf16 %0, v[188:191], a[48:51], %0" : "+v"(a1[0][1]) :  : ); SB();
  asm volatile("ds_read_b64_tr_b16 v[178:179], %0 offset:25344" :  : "v"(aW[1][0][1]) : "memory"); SB();
  asm volatile("v_alignbit_b32 %0, v230, v230, 16" : "=v"(im[0][1]) :  : ); SB();
  asm volatile("v_mfma_f32_16x16x32_bf16 %0, v[188:191], a[52:55], %0" : "+v"(a1[1][1]) :  : ); SB();
  asm volatile("ds_read_b64_tr_b16 v[180:181], %0 offset:24576" :  : "v"(aW[1][0][2]) : "memory"); SB();
  asm volatile("v_alignbit_b32 %0, v229, v229, 16" : "=v"(im[0][2]) :  : ); SB();
  asm volatile("v_mfma_f32_16x16x32_bf16 %0, v[188:191], %1, %0" : "+v"(c1[1]) : "v"(ones) : ); SB();
  asm volatile("ds_read_b64_tr_b16 v[182:183], %0 offset:25344" :  : "v"(aW[1][0][2]) : "memory"); SB();
  asm volatile("buffer_load_dwordx4 %0, %1, %2 offen lds" :  : "v"(voB[0]), "s"(rW1), "s"(kw_[9]) : "memory"); SB();
  asm volatile("v_mfma_f32_16x16x32_bf16 %0, v[192:195], a[48:51], %0" : "+v"(a1[0][2]) :  : ); SB();
  asm volatile("buffer_load_dwordx4 %0, %1, %2 offen offset:1024 lds" :  : "v"(voB[1]), "s"(rW1), "s"(kw_[9]) : "memory"); SB();
  asm volatile("v_alignbit_b32 %0, v228, v228, 16" : "=v"(im[0][3]) :  : ); SB();
  asm volatile("v_mfma_f32_16x16x32_bf16 %0, v[192:195], a[52:55], %0" : "+v"(a1[1][2]) :  : ); SB();
  asm volatile("v_bfi_b32 %0, %1, v231, v230" : "=v"(im[1][0]) : "s"(0xFFFFu) : ); SB();
  asm volatile("v_mfma_f32_16x16x32_bf16 %0, v[192:195], %1, %0" : "+v"(c1[2]) : "v"(ones) : ); SB();
  asm volatile("v_bfi_b32 %0, %1, v230, v229" : "=v"(im[1][1]) : "s"(0xFFFFu) : ); SB();
  asm volatile("s_waitcnt lgkmcnt(5)\n\tv_mfma_f32_16x16x32_bf16 %0, v[160:163], a[56:59], %0" : "+v"(a1[0][0]) :  : ); SB();
  asm volatile("ds_read_b64_tr_b16 v[184:185], %0 offset:24576" :  : "v"(aW[1][1][0]) : "memory"); SB();
  asm volatile("v_bfi_b32 %0, %1, v229, v228" : "=v"(im[1][2]) : "s"(0xFFFFu) : ); SB();
  asm volatile("v_mfma_f32_16x16x32_bf16 %0, v[160:163], a[60:63], %0" : "+v"(a1[1][0]) :  : ); SB();
  asm volatile("ds_read_b64_tr_b16 v[186:187], %0 offset:25344" :  : "v"(aW[1][1][0]) : "memory"); SB();
  asm volatile("v_bfi_b32 %0, %1, v228, v242" : "=v"(im[1][3]) : "s"(0xFFFFu) : ); SB();
  asm volatile("v_mfma_f32_16x16x32_bf16 %0, v[164:167], a[56:59], %0" : "+v"(a1[0][1]) :  : ); SB();
  asm volatile("ds_read_b64_tr_b16 v[188:189], %0 offset:24576" :  : "v"(aW[1][1][1]) : "memory"); SB();
  asm volatile("v_mov_b32_dpp v243, v235 wave_shr:1 row_mask:0xf bank_mask:0xf" :  :  : ); SB();
  asm volatile("v_mfma_f32_16x16x32_bf16 %0, v[164:167], a[60:63], %0" : "+v"(a1[1][1]) :  : ); SB();
  asm volatile("ds_read_b64_tr_b16 v[190:191], %0 offset:25344" :  : "v"(aW[1][1][1]) : "memory"); SB();
  asm volatile("v_alignbit_b32 %0, v235, v235, 16" : "=v"(im[2][0]) :  : ); SB();
  asm volatile("v_mfma_f32_16x16x32_bf16 %0, v[168:171], a[56:59], %0" : "+v"(a1[0][2]) :  : ); SB();
  asm volatile("ds_read_b64_tr_b16 v[192:193], %0 offset:24576" :  : "v"(aW[1][1][2]) : "memory"); SB();
  asm volatile("buffer_load_dwordx4 %0, %1, %2 offen offset:2048 lds" :  : "v"(voB[2]), "s"(rW1), "s"(kw_[9]) : "memory"); SB();
  asm volatile("v_mfma_f32_16x16x32_bf16 %0, v[168:171], a[60:63], %0" : "+v"(a1[1][2]) :  : ); SB();
  asm volatile("ds_read_b64_tr_b16 v[194:195], %0 offset:25344" :  : "v"(aW[1][1][2]) : "memory"); SB();
  asm volatile("s_waitcnt lgkmcnt(6)\n\tv_mfma_f32_16x16x32_bf16 %0, v[172:175], a[64:67], %0" : "+v"(a1[0][0]) :  : ); SB();
  asm volatile("ds_read_b64_tr_b16 v[160:161], %0 offset:36864" :  : "v"(aW[1][0][0]) : "memory"); SB();
  asm volatile("s_add_u32 m0, %0, 102400" :  : "s"(mA) : "memory", "scc"); SB();
  asm volatile("v_mfma_f32_16x16x32_bf16 %0, v[172:175], a[68:71], %0" : "+v"(a1[1][0]) :  : ); SB();
  asm volatile("ds_read_b64_tr_b16 v[162:163], %0 offset:37632" :  : "v"(aW[1][0][0]) : "memory"); SB();
  asm volatile("v_alignbit_b32 %0, v234, v234, 16" : "=v"(im[2][1]) :  : ); SB();
  asm volatile("v_mfma_f32_16x16x32_bf16 %0, v[172:175], %1, %0" : "+v"(c1[0]) : "v"(ones) : ); SB();
  asm volatile("ds_read_b64_tr_b16 v[164:165], %0 offset:36864" :  : "v"(aW[1][0][1]) : "memory"); SB();
  asm volatile("v_alignbit_b32 %0, v233, v233, 16" : "=v"(im[2][2]) :  : ); SB();
  asm volatile("v_mfma_f32_16x16x32_bf16 %0, v[176:179], a[64:67], %0" : "+v"(a1[0][1]) :  : ); SB();
  asm volatile("ds_read_b64_tr_b16 v[166:167], %0 offset:37632" :  : "v"(aW[1][0][1]) : "memory"); SB();
  asm volatile("buffer_load_dwordx4 %0, %1, %2 offen offset:2048 lds" :  : "v"(voS), "s"(rA), "s"(ao[6]) : "memory"); SB();
  asm volatile("v_mfma_f32_16x16x32_bf16 %0, v[176:179], a[68:71], %0" : "+v"(a1[1][1]) :  : ); SB();
  asm volatile("ds_read_b64_tr_b16 v[168:169], %0 offset:36864" :  : "v"(aW[1][0][2]) : "memory"); SB();
  asm volatile("v_alignbit_b32 %0, v232, v232, 16" : "=v"(im[2][3]) :  : ); SB();
  asm volatile("v_mfma_f32_16x16x32_bf16 %0, v[176:179], %1, %0" : "+v"(c1[1]) : "v"(ones) : ); SB();
  asm volatile("ds_read_b64_tr_b16 v[170:171], %0 offset:37632" :  : "v"(aW[1][0][2]) : "memory"); SB();
  asm volatile("v_bfi_b32 %0, %1, v235, v234" : "=v"(im[3][0]) : "s"(0xFFFFu) : ); SB();
  asm volatile("v_mfma_f32_16x16x32_bf16 %0, v[180:183], a[64:67], %0" : "+v"(a1[0][2]) :  : ); SB();
  asm volatile("buffer_load_dwordx4 %0, %1, %2 offen offset:3072 lds" :  : "v"(voS), "s"(rA), "s"(ao[7]) : "memory"); SB();
  asm volatile("v_bfi_b32 %0, %1, v234, v233" : "=v"(im[3][1]) : "s"(0xFFFFu) : ); SB();
  asm volatile("v_mfma_f32_16x16x32_bf16 %0, v[180:183], a[68:71], %0" : "+v"(a1[1][2]) :  : ); SB();
  asm volatile("v_xor_b32 %0, 384, %0" : "+v"(voS) :  : "memory"); SB();
  asm volatile("s_add_u32 m0, %0, 131072" :  : "s"(mA) : "memory", "scc"); SB();
  asm volatile("v_mfma_f32_16x16x32_bf16 %0, v[180:183], %1, %0" : "+v"(c1[2]) : "v"(ones) : ); SB();
  asm volatile("buffer_load_dwordx4 %0, %1, %2 offen lds" :  : "v"(voS), "s"(rA), "s"(ao[0]) : "memory"); SB();
  asm volatile("s_waitcnt lgkmcnt(6)\n\tv_mfma_f32_16x16x32_bf16 %0, v[184:187], a[72:75], %0" : "+v"(a1[0][0]) :  : ); SB();
  asm volatile("ds_read_b64_tr_b16 v[172:173], %0 offset:36864" :  : "v"(aW[1][1][0]) : "memory"); SB();
  asm volatile("s_waitcnt vmcnt(12)" :  :  : "memory"); SB();
  asm volatile("v_mfma_f32_16x16x32_bf16 %0, v[184:187], a[76:79], %0" : "+v"(a1[1][0]) :  : ); SB();
  asm volatile("ds_read_b64_tr_b16 v[174:175], %0 offset:37632" :  : "v"(aW[1][1][0]) : "memory"); SB();
  asm volatile("v_bfi_b32 %0, %1, v233, v232" : "=v"(im[3][2]) : "s"(0xFFFFu) : ); SB();
  asm volatile("v_mfma_f32_16x16x32_bf16 %0, v[188:191], a[72:75], %0" : "+v"(a1[0][1]) :  : ); SB();
  asm volatile("ds_read_b64_tr_b16 v[176:177], %0 offset:36864" :  : "v"(aW[1][1][1]) : "memory"); SB();
  asm volatile("v_bfi_b32 %0, %1, v232, v243" : "=v"(im[3][3]) : "s"(0xFFFFu) : ); SB();
  asm volatile("v_mfma_f32_16x16x32_bf16 %0, v[188:191], a[76:79], %0" : "+v"(a1[1][1]) :  : ); SB();
  asm volatile("ds_read_b64_tr_b16 v[178:179], %0 offset:37632" :  : "v"(aW[1][1][1]) : "memory"); SB();
  asm volatile("v_mov_b32_dpp v244, v239 wave_shr:1 row_mask:0xf bank_mask:0xf" :  :  : ); SB();
  asm volatile("v_mfma_f32_16x16x32_bf16 %0, v[192:195], a[72:75], %0" : "+v"(a1[0][2]) :  : ); SB();
  asm volatile("ds_read_b64_tr_b16 v[180:181], %0 offset:36864" :  : "v"(aW[1][1][2]) : "memory"); SB();
  asm volatile("ds_read_b128 a[196:199], %0 offset:2048" :  : "v"(aA[0]) : "memory"); SB();
  asm volatile("v_mfma_f32_16x16x32_bf16 %0, v[192:195], a[76:79], %0" : "+v"(a1[1][2]) :  : ); SB();
  asm volatile("ds_read_b64_tr_b16 v[182:183], %0 offset:37632" :  : "v"(aW[1][1][2]) : "memory"); SB();
  asm volatile("ds_read_b128 a[212:215], %0 offset:2048" :  : "v"(aA[1]) : "memory"); SB();
  asm volatile("s_waitcnt lgkmcnt(8)\n\tv_mfma_f32_16x16x32_bf16 %0, v[160:163], a[80:83], %0" : "+v"(a1[0][0]) :  : ); SB();
  unsigned vFZ = W_FZ + cb * 1024 + w * 256 + (lane & 31) * 8;
  unsigned vB = W_Z2 + (w * 1024 + (cb & 1) * 128 + cb * 6) * 1024 + l16;
  unsigned ha = lb + 59520 + 4608 + (2040 - 8 * (w * 64 + lane)) * 2;
  unsigned za = lb + 59520 + min(w, 2u) * 12928 + (lane < 32 ? lane * 16 : 4608 + ((lane >> 1) & 1) * 4160 + 4096 + (lane & 1) * 16);
  unsigned ew[2] = {lb + 32768 + w * 6144 + wn * 3072 + l16, lb + 32768 + (w ^ 1) * 6144 + wn * 3072 + l16};
  unsigned aa[4], bb;
  {
    const unsigned c = (l15 + 1) & 1;
    const unsigned o = lb + 59520 + 2 * 12928 - w * 128 - 192;
#pragma unroll
    for (int k = 0; k < 4; ++k) aa[k] = o + 4608 + c * 4160 + (2031 - l15 + 8 * g4 - c) * 2 - 512 - 1024 * k;
    bb = o + 512 + 32 * l15 - 32 + 16 * g4;
  }
  unsigned dd = lb + 0 + w * 8192 + g4 * 256 + ((l15 ^ ((g4 >> 1) << 3)) << 4);
  asm volatile("" : "+v"(aa[0]), "+v"(aa[1]), "+v"(aa[2]), "+v"(aa[3]), "+v"(bb), "+v"(dd), "+v"(vFZ), "+v"(vB), "+v"(ha), "+v"(za), "+v"(ew[0]), "+v"(ew[1]));
  SB();
  asm volatile("buffer_load_dwordx4 %0, %1, %2 offen offset:1024 lds" :  : "v"(voS), "s"(rA), "s"(ao[1]) : "memory"); SB();
  asm volatile("buffer_load_dwordx4 %0, %1, %2 offen offset:2048 lds" :  : "v"(voS), "s"(rA), "s"(ao[2]) : "memory"); SB();
  asm volatile("s_barrier" :  :  : "memory"); SB();
  asm volatile("ds_read_b64_tr_b16 v[184:185], %0 offset:0" :  : "v"(aW[0][0][0]) : "memory"); SB();
  asm volatile("v_alignbit_b32 %0, v239, v239, 16" : "=v"(im[4][0]) :  : ); SB();
  asm volatile("v_mfma_f32_16x16x32_bf16 %0, v[160:163], a[84:87], %0" : "+v"(a1[1][0]) :  : ); SB();
  asm volatile("ds_read_b64_tr_b16 v[186:187], %0 offset:768" :  : "v"(aW[0][0][0]) : "memory"); SB();
  asm volatile("buffer_load_dwordx4 %0, %1, %2 offen offset:3072 lds" :  : "v"(voS), "s"(rA), "s"(ao[3]) : "memory"); SB();
  asm volatile("v_mfma_f32_16x16x32_bf16 %0, v[160:163], %1, %0" : "+v"(c1[0]) : "v"(ones) : ); SB();
  asm volatile("ds_read_b64_tr_b16 v[188:189], %0 offset:0" :  : "v"(aW[0][0][1]) : "memory"); SB();
  asm volatile("v_alignbit_b32 %0, v238, v238, 16" : "=v"(im[4][1]) :  : ); SB();
  asm volatile("v_mfma_f32_16x16x32_bf16 %0, v[164:167], a[80:83], %0" : "+v"(a1[0][1]) :  : ); SB();
  asm volatile("ds_read_b64_tr_b16 v[190:191], %0 offset:768" :  : "v"(aW[0][0][1]) : "memory"); SB();
  asm volatile("s_add_u32 m0, %0, 135168" :  : "s"(mA) : "memory", "scc"); SB();
  asm volatile("v_mfma_f32_16x16x32_bf16 %0, v[164:167], a[84:87], %0" : "+v"(a1[1][1]) :  : ); SB();
  asm volatile("ds_read_b64_tr_b16 v[192:193], %0 offset:0" :  : "v"(aW[0][0][2]) : "memory"); SB();
  asm volatile("v_alignbit_b32 %0, v237, v237, 16" : "=v"(im[4][2]) :  : ); SB();
  asm volatile("v_mfma_f32_16x16x32_bf16 %0, v[164:167], %1, %0" : "+v"(c1[1]) : "v"(ones) : ); SB();
  asm volatile("ds_read_b64_tr_b16 v[194:195], %0 offset:768" :  : "v"(aW[0][0][2]) : "memory"); SB();
  asm volatile("v_alignbit_b32 %0, v236, v236, 16" : "=v"(im[4][3]) :  : ); SB();
  asm volatile("v_mfma_f32_16x16x32_bf16 %0, v[168:171], a[80:83], %0" : "+v"(a1[0][2]) :  : ); SB();
  asm volatile("buffer_load_dwordx4 %0, %1, %2 offen lds" :  : "v"(voS), "s"(rA), "s"(ao[4]) : "memory"); SB();
  asm volatile("v_bfi_b32 %0, %1, v239, v238" : "=v"(im[5][0]) : "s"(0xFFFFu) : ); SB();
  asm volatile("v_mfma_f32_16x16x32_bf16 %0, v[168:171], a[84:87], %0" : "+v"(a1[1][2]) :  : ); SB();
  asm volatile("buffer_load_dwordx4 %0, %1, %2 offen offset:1024 lds" :  : "v"(voS), "s"(rA), "s"(ao[5]) : "memory"); SB();
  asm volatile("v_bfi_b32 %0, %1, v238, v237" : "=v"(im[5][1]) : "s"(0xFFFFu) : ); SB();
  asm volatile("v_mfma_f32_16x16x32_bf16 %0, v[168:171], %1, %0" : "+v"(c1[2]) : "v"(ones) : ); SB();
  asm volatile("v_bfi_b32 %0, %1, v237, v236" : "=v"(im[5][2]) : "s"(0xFFFFu) : ); SB();
  asm volatile("s_waitcnt lgkmcnt(5)\n\tv_mfma_f32_16x16x32_bf16 %0, v[172:175], a[88:91], %0" : "+v"(a1[0][0]) :  : ); SB();
  asm volatile("ds_read_b64_tr_b16 v[160:161], %0 offset:0" :  : "v"(aW[0][1][0]) : "memory"); SB();
  asm volatile("v_bfi_b32 %0, %1, v236, v244" : "=v"(im[5][3]) : "s"(0xFFFFu) : ); SB();
  asm volatile("v_mfma_f32_16x16x32_bf16 %0, v[172:175], a[92:95], %0" : "+v"(a1[1][0]) :  : ); SB();
  asm volatile("ds_read_b64_tr_b16 v[162:163], %0 offset:768" :  : "v"(aW[0][1][0]) : "memory"); SB();
  asm volatile("v_mov_b32 %0, 0" : "=v"(zero4[0]) :  : ); SB();
  asm volatile("v_mfma_f32_16x16x32_bf16 %0, v[176:179], a[88:91], %0" : "+v"(a1[0][1]) :  : ); SB();
  asm volatile("ds_read_b64_tr_b16 v[164:165], %0 offset:0" :  : "v"(aW[0][1][1]) : "memory"); SB();
  asm volatile("v_mov_b32 %0, 0" : "=v"(zero4[1]) :  : ); SB();
  asm volatile("v_mfma_f32_16x16x32_bf16 %0, v[176:179], a[92:95], %0" : "+v"(a1[1][1]) :  : ); SB();
  asm volatile("ds_read_b64_tr_b16 v[166:167], %0 offset:768" :  : "v"(aW[0][1][1]) : "memory"); SB();
  asm volatile("v_mov_b32 %0, 0" : "=v"(zero4[2]) :  : ); SB();
  asm volatile("v_mfma_f32_16x16x32_bf16 %0, v[180:183], a[88:91], %0" : "+v"(a1[0][2]) :  : ); SB();
  asm volatile("ds_read_b64_tr_b16 v[168:169], %0 offset:0" :  : "v"(aW[0][1][2]) : "memory"); SB();
  asm volatile("v_mov_b32 %0, 0" : "=v"(zero4[3]) :  : ); SB();
  asm volatile("v_mfma_f32_16x16x32_bf16 %0, v[180:183], a[92:95], %0" : "+v"(a1[1][2]) :  : ); SB();
  asm volatile("ds_read_b64_tr_b16 v[170:171], %0 offset:768" :  : "v"(aW[0][1][2]) : "memory"); SB();
  asm volatile("s_waitcnt lgkmcnt(6)\n\tv_mfma_f32_16x16x32_bf16 %0, v[184:187], a[96:99], %0" : "+v"(a1[0][0]) :  : ); SB();
  asm volatile("ds_read_b64_tr_b16 v[172:173], %0 offset:12288" :  : "v"(aW[0][0][0]) : "memory"); SB();
  asm volatile("s_waitcnt vmcnt(13)\n\ts_mov_b64 exec, 1\n\tbuffer_store_dwordx2 %0, off, %1, %2 sc1\n\ts_mov_b64 exec, -1" :  : "v"(((u32x2){tg0, tg1})), "s"(rWS), "s"(sFZ) : "memory"); SB();
  asm volatile("v_mfma_f32_16x16x32_bf16 %0, v[184:187], a[100:103], %0" : "+v"(a1[1][0]) :  : ); SB();
  asm volatile("ds_read_b64_tr_b16 v[174:175], %0 offset:13056" :  : "v"(aW[0][0][0]) : "memory"); SB();
  asm volatile("s_waitcnt vmcnt(13)" :  :  : "memory"); SB();
  asm volatile("v_mfma_f32_16x16x32_bf16 %0, v[184:187], %1, %0" : "+v"(c1[0]) : "v"(ones) : ); SB();
  asm volatile("ds_read_b64_tr_b16 v[176:177], %0 offset:12288" :  : "v"(aW[0][0][1]) : "memory"); SB();
  asm volatile("v_mfma_f32_16x16x32_bf16 %0, v[188:191], a[96:99], %0" : "+v"(a1[0][1]) :  : ); SB();
  asm volatile("ds_read_b64_tr_b16 v[178:179], %0 offset:13056" :  : "v"(aW[0][0][1]) : "memory"); SB();
  asm volatile("v_mfma_f32_16x16x32_bf16 %0, v[188:191], a[100:103], %0" : "+v"(a1[1][1]) :  : ); SB();
  asm volatile("ds_read_b64_tr_b16 v[180:181], %0 offset:12288" :  : "v"(aW[0][0][2]) : "memory"); SB();
  asm volatile("v_mfma_f32_16x16x32_bf16 %0, v[188:191], %1, %0" : "+v"(c1[1]) : "v"(ones) : ); SB();
  asm volatile("ds_read_b64_tr_b16 v[182:183], %0 offset:13056" :  : "v"(aW[0][0][2]) : "memory"); SB();
  asm volatile("v_mfma_f32_16x16x32_bf16 %0, v[192:195], a[96:99], %0" : "+v"(a1[0][2]) :  : ); SB();
  asm volatile("ds_read_b128 a[200:203], %0 offset:4096" :  : "v"(aA[0]) : "memory"); SB();
  asm volatile("v_mfma_f32_16x16x32_bf16 %0, v[192:195], a[100:103], %0" : "+v"(a1[1][2]) :  : ); SB();
  asm volatile("ds_read_b128 a[216:219], %0 offset:4096" :  : "v"(aA[1]) : "memory"); SB();
  asm volatile("v_mfma_f32_16x16x32_bf16 %0, v[192:195], %1, %0" : "+v"(c1[2]) : "v"(ones) : ); SB();
  asm volatile("buffer_load_dwordx4 %0, %1, %2 offen offset:2048 lds" :  : "v"(voS), "s"(rA), "s"(ao[6]) : "memory"); SB();
  asm volatile("s_waitcnt lgkmcnt(7)\n\tv_mfma_f32_16x16x32_bf16 %0, v[160:163], a[104:107], %0" : "+v"(a1[0][0]) :  : ); SB();
  asm volatile("ds_read_b64_tr_b16 v[184:185], %0 offset:12288" :  : "v"(aW[0][1][0]) : "memory"); SB();
  asm volatile("v_mfma_f32_16x16x32_bf16 %0, v[160:163], a[108:111], %0" : "+v"(a1[1][0]) :  : ); SB();
  asm volatile("s_mov_b32 exec_lo, 0x10001\n\ts_mov_b32 exec_hi, 0x10001\n\tds_write_b128 %0, %1 offset:0\n\ts_mov_b64 exec, -1" :  : "v"(ca), "v"(c1[0]) : "memory"); SB();
  asm volatile("ds_read_b64_tr_b16 v[186:187], %0 offset:13056" :  : "v"(aW[0][1][0]) : "memory"); SB();
  asm volatile("v_mfma_f32_16x16x32_bf16 %0, v[164:167], a[104:107], %0" : "+v"(a1[0][1]) :  : ); SB();
  asm volatile("ds_read_b64_tr_b16 v[188:189], %0 offset:12288" :  : "v"(aW[0][1][1]) : "memory"); SB();
  asm volatile("v_mfma_f32_16x16x32_bf16 %0, v[164:167], a[108:111], %0" : "+v"(a1[1][1]) :  : ); SB();
  asm volatile("s_mov_b32 exec_lo, 0x10001\n\ts_mov_b32 exec_hi, 0x10001\n\tds_write_b128 %0, %1 offset:64\n\ts_mov_b64 exec, -1" :  : "v"(ca), "v"(c1[1]) : "memory"); SB();
  asm volatile("ds_read_b64_tr_b16 v[190:191], %0 offset:13056" :  : "v"(aW[0][1][1]) : "memory"); SB();
  asm volatile("buffer_load_dwordx4 %0, %1, %2 offen offset:3072 lds" :  : "v"(voS), "s"(rA), "s"(ao[7]) : "memory"); SB();
  asm volatile("v_mfma_f32_16x16x32_bf16 %0, v[168:171], a[104:107], %0" : "+v"(a1[0][2]) :  : ); SB();
  asm volatile("ds_read_b64_tr_b16 v[192:193], %0 offset:12288" :  : "v"(aW[0][1][2]) : "memory"); SB();
  asm volatile("v_mfma_f32_16x16x32_bf16 %0, v[168:171], a[108:111], %0" : "+v"(a1[1][2]) :  : ); SB();
  asm volatile("s_mov_b32 exec_lo, 0x10001\n\ts_mov_b32 exec_hi, 0x10001\n\tds_write_b128 %0, %1 offset:128\n\ts_mov_b64 exec, -1" :  : "v"(ca), "v"(c1[2]) : "memory"); SB();
  asm volatile("ds_read_b64_tr_b16 v[194:195], %0 offset:13056" :  : "v"(aW[0][1][2]) : "memory"); SB();
  asm volatile("v_xor_b32 %0, 128, %0" : "+v"(voS) :  : "memory"); SB();
  asm volatile("s_waitcnt lgkmcnt(5)\n\tv_mfma_f32_16x16x32_bf16 %0, v[172:175], a[112:115], %0" : "+v"(a1[0][0]) :  : ); SB();
  asm volatile("s_add_u32 m0, %0, 98304" :  : "s"(mA) : "memory", "scc"); SB();
  asm volatile("v_mfma_f32_16x16x32_bf16 %0, v[172:175], a[116:119], %0" : "+v"(a1[1][0]) :  : ); SB();
  asm volatile("buffer_load_dwordx4 %0, %1, %2 offen lds" :  : "v"(voS), "s"(rA), "s"(ao[0]) : "memory"); SB();
  asm volatile("v_mfma_f32_16x16x32_bf16 %0, v[172:175], %1, %0" : "+v"(c1[0]) : "v"(ones) : ); SB();
  asm volatile("buffer_load_dwordx4 %0, %1, %2 offen offset:1024 lds" :  : "v"(voS), "s"(rA), "s"(ao[1]) : "memory"); SB();
  asm volatile("v_mfma_f32_16x16x32_bf16 %0, v[176:179], a[112:115], %0" : "+v"(a1[0][1]) :  : ); SB();
  asm volatile("s_waitcnt vmcnt(13) lgkmcnt(1)\n\ts_barrier" :  :  : "memory"); SB();
  asm volatile("ds_read_b64_tr_b16 v[160:161], %0 offset:24576" :  : "v"(aW[0][0][0]) : "memory"); SB();
  asm volatile("s_waitcnt vmcnt(11)" :  :  : "memory"); SB();
  asm volatile("v_mfma_f32_16x16x32_bf16 %0, v[176:179], a[116:119], %0" : "+v"(a1[1][1]) :  : ); SB();
  asm volatile("ds_read_b64_tr_b16 v[162:163], %0 offset:25344" :  : "v"(aW[0][0][0]) : "memory"); SB();
  asm volatile("ds_read_b128 a[204:207], %0 offset:6144" :  : "v"(aA[0]) : "memory"); SB();
  asm volatile("v_mfma_f32_16x16x32_bf16 %0, v[176:179], %1, %0" : "+v"(c1[1]) : "v"(ones) : ); SB();
  asm volatile("ds_read_b64_tr_b16 v[164:165], %0 offset:24576" :  : "v"(aW[0][0][1]) : "memory"); SB();
  asm volatile("ds_read_b128 a[220:223], %0 offset:6144" :  : "v"(aA[1]) : "memory"); SB();
  asm volatile("v_mfma_f32_16x16x32_bf16 %0, v[180:183], a[112:115], %0" : "+v"(a1[0][2]) :  : ); SB();
  asm volatile("ds_read_b64_tr_b16 v[166:167], %0 offset:25344" :  : "v"(aW[0][0][1]) : "memory"); SB();
  asm volatile("buffer_load_dwordx4 %0, %1, %2 offen offset:2048 lds" :  : "v"(voS), "s"(rA), "s"(ao[2]) : "memory"); SB();
  asm volatile("v_mfma_f32_16x16x32_bf16 %0, v[180:183], a[116:119], %0" : "+v"(a1[1][2]) :  : ); SB();
  asm volatile("ds_read_b64_tr_b16 v[168:169], %0 offset:24576" :  : "v"(aW[0][0][2]) : "memory"); SB();
  asm volatile("s_waitcnt vmcnt(10)" :  :  : "memory"); SB();
  asm volatile("v_mfma_f32_16x16x32_bf16 %0, v[180:183], %1, %0" : "+v"(c1[2]) : "v"(ones) : ); SB();
  asm volatile("ds_read_b64_tr_b16 v[170:171], %0 offset:25344" :  : "v"(aW[0][0][2]) : "memory"); SB();
  asm volatile("ds_read_b128 a[224:227], %0 offset:32768" :  : "v"(aA[0]) : "memory"); SB();
  asm volatile("v_mfma_f32_16x16x32_bf16 %0, v[184:187], a[120:123], %0" : "+v"(a1[0][0]) :  : ); SB();
  asm volatile("ds_read_b128 a[240:243], %0 offset:32768" :  : "v"(aA[1]) : "memory"); SB();
  asm volatile("v_mfma_f32_16x16x32_bf16 %0, v[184:187], a[124:127], %0" : "+v"(a1[1][0]) :  : ); SB();
  asm volatile("buffer_load_dwordx4 %0, %1, %2 offen offset:3072 lds" :  : "v"(voS), "s"(rA), "s"(ao[3]) : "memory"); SB();
  asm volatile("s_add_u32 m0, %0, 102400" :  : "s"(mA) : "memory", "scc"); SB();
  asm volatile("v_mfma_f32_16x16x32_bf16 %0, v[184:187], %1, %0" : "+v"(c1[0]) : "v"(ones) : ); SB();
  asm volatile("buffer_load_dwordx4 %0, %1, %2 offen lds" :  : "v"(voS), "s"(rA), "s"(ao[4]) : "memory"); SB();
  asm volatile("v_mfma_f32_16x16x32_bf16 %0, v[188:191], a[120:123], %0" : "+v"(a1[0][1]) :  : ); SB();
  asm volatile("ds_read_b64_tr_b16 v[172:173], %0 offset:24576" :  : "v"(aW[0][1][0]) : "memory"); SB();
  asm volatile("v_mfma_f32_16x16x32_bf16 %0, v[188:191], a[124:127], %0" : "+v"(a1[1][1]) :  : ); SB();
  asm volatile("ds_read_b64_tr_b16 v[174:175], %0 offset:25344" :  : "v"(aW[0][1][0]) : "memory"); SB();
  asm volatile("v_mfma_f32_16x16x32_bf16 %0, v[188:191], %1, %0" : "+v"(c1[1]) : "v"(ones) : ); SB();
  asm volatile("ds_read_b64_tr_b16 v[176:177], %0 offset:24576" :  : "v"(aW[0][1][1]) : "memory"); SB();
  asm volatile("s_waitcnt vmcnt(10)" :  :  : "memory"); SB();
  asm volatile("s_waitcnt lgkmcnt(7)\n\tv_mfma_f32_16x16x32_bf16 %0, v[192:195], a[120:123], %0" : "+v"(a1[0][2]) :  : ); SB();
  asm volatile("ds_read_b64_tr_b16 v[178:179], %0 offset:25344" :  : "v"(aW[0][1][1]) : "memory"); SB();
  asm volatile("ds_read_b128 a[228:231], %0 offset:34816" :  : "v"(aA[0]) : "memory"); SB();
  asm volatile("v_mfma_f32_16x16x32_bf16 %0, v[192:195], a[124:127], %0" : "+v"(a1[1][2]) :  : ); SB();
  asm volatile("ds_read_b64_tr_b16 v[180:181], %0 offset:24576" :  : "v"(aW[0][1][2]) : "memory"); SB();
  asm volatile("ds_read_b128 a[244:247], %0 offset:34816" :  : "v"(aA[1]) : "memory"); SB();
  asm volatile("v_mfma_f32_16x16x32_bf16 %0, v[192:195], %1, %0" : "+v"(c1[2]) : "v"(ones) : ); SB();
  asm volatile("ds_read_b64_tr_b16 v[182:183], %0 offset:25344" :  : "v"(aW[0][1][2]) : "memory"); SB();
  asm volatile("buffer_load_dwordx4 %0, %1, %2 offen offset:1024 lds" :  : "v"(voS), "s"(rA), "s"(ao[5]) : "memory"); SB();
  asm volatile("v_mfma_f32_16x16x32_bf16 %0, v[160:163], a[128:131], %0" : "+v"(a1[0][0]) :  : ); SB();
  asm volatile("buffer_load_dwordx4 %0, %1, %2 offen offset:2048 lds" :  : "v"(voS), "s"(rA), "s"(ao[6]) : "memory"); SB();
  asm volatile("v_mfma_f32_16x16x32_bf16 %0, v[160:163], a[132:135], %0" : "+v"(a1[1][0]) :  : ); SB();
  asm volatile("ds_read_b128 %0, %1 offset:0" : "=&v"(co[0]) : "v"(cr) : "memory"); SB();
  asm volatile("v_mfma_f32_16x16x32_bf16 %0, v[160:163], %1, %0" : "+v"(c1[0]) : "v"(ones) : ); SB();
  asm volatile("buffer_load_dwordx2 %0, %1, %2, %3 offen sc1" : "=&v"(fk) : "v"(vFZ), "s"(rWS), "s"(8192u) : "memory"); SB();
  asm volatile("s_waitcnt vmcnt(11)" :  :  : "memory"); SB();
  asm volatile("v_mfma_f32_16x16x32_bf16 %0, v[164:167], a[128:131], %0" : "+v"(a1[0][1]) :  : ); SB();
  asm volatile("ds_read_b64_tr_b16 v[184:185], %0 offset:36864" :  : "v"(aW[0][0][0]) : "memory"); SB();
  asm volatile("ds_read_b128 a[232:235], %0 offset:36864" :  : "v"(aA[0]) : "memory"); SB();
  asm volatile("v_mfma_f32_16x16x32_bf16 %0, v[164:167], a[132:135], %0" : "+v"(a1[1][1]) :  : ); SB();
  asm volatile("ds_read_b64_tr_b16 v[186:187], %0 offset:37632" :  : "v"(aW[0][0][0]) : "memory"); SB();
  asm volatile("ds_read_b128 a[248:251], %0 offset:36864" :  : "v"(aA[1]) : "memory"); SB();
  asm volatile("v_mfma_f32_16x16x32_bf16 %0, v[164:167], %1, %0" : "+v"(c1[1]) : "v"(ones) : ); SB();
  asm volatile("ds_read_b64_tr_b16 v[188:189], %0 offset:36864" :  : "v"(aW[0][0][1]) : "memory"); SB();
  asm volatile("s_waitcnt lgkmcnt(9)\n\tv_mfma_f32_16x16x32_bf16 %0, v[168:171], a[128:131], %0" : "+v"(a1[0][2]) :  : ); SB();
  asm volatile("ds_read_b64_tr_b16 v[190:191], %0 offset:37632" :  : "v"(aW[0][0][1]) : "memory"); SB();
  asm volatile("buffer_load_dwordx4 %0, %1, %2 offen offset:3072 lds" :  : "v"(voS), "s"(rA), "s"(ao[7]) : "memory"); SB();
  asm volatile("v_mfma_f32_16x16x32_bf16 %0, v[168:171], a[132:135], %0" : "+v"(a1[1][2]) :  : ); SB();
  asm volatile("ds_read_b64_tr_b16 v[192:193], %0 offset:36864" :  : "v"(aW[0][0][2]) : "memory"); SB();
  asm volatile("v_xor_b32 %0, 896, %0" : "+v"(voS) :  : "memory"); SB();
  asm volatile("v_mfma_f32_16x16x32_bf16 %0, v[168:171], %1, %0" : "+v"(c1[2]) : "v"(ones) : ); SB();
  asm volatile("ds_read_b64_tr_b16 v[194:195], %0 offset:37632" :  : "v"(aW[0][0][2]) : "memory"); SB();
  asm volatile("v_mfma_f32_16x16x32_bf16 %0, v[172:175], a[136:139], %0" : "+v"(a1[0][0]) :  : ); SB();
  asm volatile("s_add_u32 m0, %0, 131072" :  : "s"(mA) : "memory", "scc"); SB();
  asm volatile("ds_read_b128 %0, %1 offset:64" : "=&v"(co[1]) : "v"(cr) : "memory"); SB();
  asm volatile("v_mfma_f32_16x16x32_bf16 %0, v[172:175], a[140:143], %0" : "+v"(a1[1][0]) :  : ); SB();
  asm volatile("buffer_load_dwordx4 %0, %1, %2 offen lds" :  : "v"(voS), "s"(rA), "s"(ao[0]) : "memory"); SB();
  asm volatile("v_mfma_f32_16x16x32_bf16 %0, v[172:175], %1, %0" : "+v"(c1[0]) : "v"(ones) : ); SB();
  asm volatile("ds_read_b128 %0, %1 offset:128" : "=&v"(co[2]) : "v"(cr) : "memory"); SB();
  asm volatile("v_mfma_f32_16x16x32_bf16 %0, v[176:179], a[136:139], %0" : "+v"(a1[0][1]) :  : ); SB();
  asm volatile("ds_write_b128 %0, %1 offset:0" :  : "v"(ha), "v"(im[0]) : "memory"); SB();
  asm volatile("ds_write_b128 %0, %1 offset:4160" :  : "v"(ha), "v"(im[1]) : "memory"); SB();
  asm volatile("ds_read_b64_tr_b16 v[160:161], %0 offset:36864" :  : "v"(aW[0][1][0]) : "memory"); SB();
  asm volatile("buffer_load_dwordx4 %0, %1, %2 offen offset:1024 lds" :  : "v"(voS), "s"(rA), "s"(ao[1]) : "memory"); SB();
  asm volatile("v_mfma_f32_16x16x32_bf16 %0, v[176:179], a[140:143], %0" : "+v"(a1[1][1]) :  : ); SB();
  asm volatile("ds_write_b128 %0, %1 offset:12928" :  : "v"(ha), "v"(im[2]) : "memory"); SB();
  asm volatile("ds_write_b128 %0, %1 offset:17088" :  : "v"(ha), "v"(im[3]) : "memory"); SB();
  asm volatile("ds_read_b64_tr_b16 v[162:163], %0 offset:37632" :  : "v"(aW[0][1][0]) : "memory"); SB();
  asm volatile("s_waitcnt lgkmcnt(6)" :  :  : "memory"); SB();
  asm volatile("v_mfma_f32_16x16x32_bf16 %0, v[176:179], %1, %0" : "+v"(c1[1]) : "v"(ones) : ); SB();
  asm volatile("ds_write_b128 %0, %1 offset:25856" :  : "v"(ha), "v"(im[4]) : "memory"); SB();
  asm volatile("ds_read_b64_tr_b16 v[164:165], %0 offset:36864" :  : "v"(aW[0][1][1]) : "memory"); SB();
  asm volatile("s_waitcnt vmcnt(11)" :  :  : "memory"); SB();
  asm volatile("v_mfma_f32_16x16x32_bf16 %0, v[180:183], a[136:139], %0" : "+v"(a1[0][2]) :  : ); SB();
  asm volatile("ds_read_b64_tr_b16 v[166:167], %0 offset:37632" :  : "v"(aW[0][1][1]) : "memory"); SB();
  asm volatile("ds_read_b128 a[236:239], %0 offset:38912" :  : "v"(aA[0]) : "memory"); SB();
  asm volatile("v_mfma_f32_16x16x32_bf16 %0, v[180:183], a[140:143], %0" : "+v"(a1[1][2]) :  : ); SB();
  asm volatile("ds_read_b64_tr_b16 v[168:169], %0 offset:36864" :  : "v"(aW[0][1][2]) : "memory"); SB();
  asm volatile("ds_read_b128 a[252:255], %0 offset:38912" :  : "v"(aA[1]) : "memory"); SB();
  asm volatile("v_mfma_f32_16x16x32_bf16 %0, v[180:183], %1, %0" : "+v"(c1[2]) : "v"(ones) : ); SB();
  asm volatile("ds_read_b64_tr_b16 v[170:171], %0 offset:37632" :  : "v"(aW[0][1][2]) : "memory"); SB();
  asm volatile("buffer_load_dwordx4 %0, %1, %2 offen offset:2048 lds" :  : "v"(voS), "s"(rA), "s"(ao[2]) : "memory"); SB();
  asm volatile("v_mfma_f32_16x16x32_bf16 %0, v[184:187], a[144:147], %0" : "+v"(a1[0][0]) :  : ); SB();
  asm volatile("buffer_load_dwordx4 %0, %1, %2 offen offset:3072 lds" :  : "v"(voS), "s"(rA), "s"(ao[3]) : "memory"); SB();
  asm volatile("v_mfma_f32_16x16x32_bf16 %0, v[184:187], a[148:151], %0" : "+v"(a1[1][0]) :  : ); SB();
  asm volatile("ds_write_b128 %0, %1 offset:30016" :  : "v"(ha), "v"(im[5]) : "memory"); SB();
  asm volatile("s_add_u32 m0, %0, 135168" :  : "s"(mA) : "memory", "scc"); SB();
  asm volatile("v_mfma_f32_16x16x32_bf16 %0, v[184:187], %1, %0" : "+v"(c1[0]) : "v"(ones) : ); SB();
  asm volatile("buffer_load_dwordx4 %0, %1, %2 offen lds" :  : "v"(voS), "s"(rA), "s"(ao[4]) : "memory"); SB();
  asm volatile("v_mfma_f32_16x16x32_bf16 %0, v[188:191], a[144:147], %0" : "+v"(a1[0][1]) :  : ); SB();
  asm volatile("s_waitcnt vmcnt(12)" :  :  : "memory"); SB();
  asm volatile("ds_read_b128 a[0:3], %0 offset:0" :  : "v"(aA[0]) : "memory"); SB();
  asm volatile("v_mfma_f32_16x16x32_bf16 %0, v[188:191], a[148:151], %0" : "+v"(a1[1][1]) :  : ); SB();
  asm volatile("ds_read_b128 a[16:19], %0 offset:0" :  : "v"(aA[1]) : "memory"); SB();
  asm volatile("v_mfma_f32_16x16x32_bf16 %0, v[188:191], %1, %0" : "+v"(c1[1]) : "v"(ones) : ); SB();
  asm volatile("buffer_load_dwordx4 %0, %1, %2 offen offset:1024 lds" :  : "v"(voS), "s"(rA), "s"(ao[5]) : "memory"); SB();
  asm volatile("s_waitcnt lgkmcnt(4)" :  :  : "memory"); SB();
  asm volatile("v_mfma_f32_16x16x32_bf16 %0, v[192:195], a[144:147], %0" : "+v"(a1[0][2]) :  : ); SB();
  asm volatile("ds_write_b128 %0, %1 offset:0" :  : "v"(za), "v"(zero4) : "memory"); SB();
  asm volatile("v_mfma_f32_16x16x32_bf16 %0, v[192:195], a[148:151], %0" : "+v"(a1[1][2]) :  : ); SB();
  asm volatile("v_mfma_f32_16x16x32_bf16 %0, v[192:195], %1, %0" : "+v"(c1[2]) : "v"(ones) : ); SB();
  asm volatile("buffer_load_dwordx4 %0, %1, %2 offen offset:2048 lds" :  : "v"(voS), "s"(rA), "s"(ao[6]) : "memory"); SB();
  asm volatile("v_mfma_f32_16x16x32_bf16 %0, v[160:163], a[152:155], %0" : "+v"(a1[0][0]) :  : ); SB();
  asm volatile("v_mfma_f32_16x16x32_bf16 %0, v[160:163], a[156:159], %0" : "+v"(a1[1][0]) :  : ); SB();
  asm volatile("v_mfma_f32_16x16x32_bf16 %0, v[160:163], %1, %0" : "+v"(c1[0]) : "v"(ones) : ); SB();
  asm volatile("buffer_load_dwordx4 %0, %1, %2 offen offset:3072 lds" :  : "v"(voS), "s"(rA), "s"(ao[7]) : "memory"); SB();
  asm volatile("s_waitcnt lgkmcnt(1)" :  :  : "memory"); SB();
  asm volatile("v_mfma_f32_16x16x32_bf16 %0, v[164:167], a[152:155], %0" : "+v"(a1[0][1]) :  : ); SB();
  asm volatile("v_xor_b32 %0, 128, %0" : "+v"(voS) :  : "memory"); SB();
  asm volatile("v_mfma_f32_16x16x32_bf16 %0, v[164:167], a[156:159], %0" : "+v"(a1[1][1]) :  : ); SB();
  asm volatile("v_mfma_f32_16x16x32_bf16 %0, v[164:167], %1, %0" : "+v"(c1[1]) : "v"(ones) : ); SB();
  asm volatile("s_waitcnt vmcnt(13)" :  :  : "memory"); SB();
  asm volatile("ds_read_b128 a[4:7], %0 offset:2048" :  : "v"(aA[0]) : "memory"); SB();
  asm volatile("v_mfma_f32_16x16x32_bf16 %0, v[168:171], a[152:155], %0" : "+v"(a1[0][2]) :  : ); SB();
  asm volatile("ds_read_b128 a[20:23], %0 offset:2048" :  : "v"(aA[1]) : "memory"); SB();
  asm volatile("s_add_u32 m0, %0, 98304" :  : "s"(mA) : "memory", "scc"); SB();
  asm volatile("v_mfma_f32_16x16x32_bf16 %0, v[168:171], a[156:159], %0" : "+v"(a1[1][2]) :  : ); SB();
  asm volatile("buffer_load_dwordx4 %0, %1, %2 offen lds" :  : "v"(voS), "s"(rA), "s"(ao[0]) : "memory"); SB();
  asm volatile("v_mfma_f32_16x16x32_bf16 %0, v[168:171], %1, %0" : "+v"(c1[2]) : "v"(ones) : ); SB();
  ST(11);
  f32x4 ac[8];
  u32x2 cA[2][8][2];
  asm volatile("buffer_load_dwordx4 %0, %1, %2 offen offset:1024 lds" :  : "v"(voS), "s"(rA), "s"(ao[1]) : "memory"); SB();
  asm volatile("buffer_load_dwordx2 %0, %1, %2, %3 offen sc1" : "=&v"(fz) : "v"(vFZ), "s"(rWS), "s"(0u) : "memory"); SB();
  asm volatile("s_waitcnt vmcnt(14)" :  :  : "memory"); SB();
  asm volatile("ds_read_b128 a[8:11], %0 offset:4096" :  : "v"(aA[0]) : "memory"); SB();
  asm volatile("ds_read_b128 a[24:27], %0 offset:4096" :  : "v"(aA[1]) : "memory"); SB();
  asm volatile("s_waitcnt lgkmcnt(2)" :  :  : "memory"); SB();
  asm volatile("buffer_load_dwordx4 %0, %1, %2 offen offset:2048 lds" :  : "v"(voS), "s"(rA), "s"(ao[2]) : "memory"); SB();
  asm volatile("buffer_load_dwordx4 %0, %1, %2 offen offset:3072 lds" :  : "v"(voS), "s"(rA), "s"(ao[3]) : "memory"); SB();
  asm volatile("s_waitcnt vmcnt(14)\n\t.Lc_%=:\n\tv_cmp_ne_u64 vcc, %2, %1\n\ts_cbranch_vccnz .Lr_%=\n\t.Ld_%=:\n\t.subsection 1\n\t.Lr_%=:\n\tv_add_u32 %0, -1, %0\n\ts_nop 1\n\tv_readfirstlane_b32 vcc_hi, %0\n\ts_nop 3\n\ts_cmp_lt_i32 vcc_hi, 1\n\ts_cbranch_scc1 .Ld_%=\n\ts_sleep 2\n\ts_mov_b32 vcc_lo, 8192\n\tbuffer_load_dwordx2 %1, %3, %4, vcc_lo offen sc1\n\ts_waitcnt vmcnt(0)\n\ts_branch .Lc_%=\n\t.subsection 0" : "+v"(tmo), "+v"(fk) : "s"(tg), "v"(vFZ), "s"(rWS) : "memory", "vcc", "scc"); SB();
  asm volatile("s_add_u32 m0, %0, 85888" :  : "s"(mU) : "memory", "scc"); SB();
  asm volatile("s_nop 0\n\tbuffer_load_dwordx4 %0, %1, %2 offen sc1 lds" :  : "v"(vF), "s"(rWS), "s"(uso) : "memory"); SB();
  asm volatile("s_waitcnt vmcnt(14)" :  :  : "memory"); SB();
  asm volatile("ds_read_b128 a[12:15], %0 offset:6144" :  : "v"(aA[0]) : "memory"); SB();
  asm volatile("ds_read_b128 a[28:31], %0 offset:6144" :  : "v"(aA[1]) : "memory"); SB();
  asm volatile("s_waitcnt lgkmcnt(0)" :  :  : "memory"); SB();
  asm volatile("s_add_u32 m0, %0, 102400" :  : "s"(mA) : "memory", "scc"); SB();
  asm volatile("s_nop 0\n\tbuffer_load_dwordx4 %0, %1, %2 offen lds" :  : "v"(voS), "s"(rA), "s"(ao[4]) : "memory"); SB();
  asm volatile("buffer_load_dwordx4 %0, %1, %2 offen offset:1024 lds" :  : "v"(voS), "s"(rA), "s"(ao[5]) : "memory"); SB();
  asm volatile("s_waitcnt vmcnt(14)" :  :  : "memory"); SB();
  asm volatile("ds_read_b128 a[32:35], %0 offset:32768" :  : "v"(aA[0]) : "memory"); SB();
  asm volatile("ds_read_b128 a[48:51], %0 offset:32768" :  : "v"(aA[1]) : "memory"); SB();
  asm volatile("buffer_load_dwordx4 %0, %1, %2 offen offset:2048 lds" :  : "v"(voS), "s"(rA), "s"(ao[6]) : "memory"); SB();
  asm volatile("buffer_load_dwordx4 %0, %1, %2 offen offset:3072 lds" :  : "v"(voS), "s"(rA), "s"(ao[7]) : "memory"); SB();
  asm volatile("s_waitcnt lgkmcnt(0)" :  :  : "memory"); SB();
  asm volatile("v_xor_b32 %0, 384, %0" : "+v"(voS) :  : "memory"); SB();
  asm volatile("s_waitcnt vmcnt(14)" :  :  : "memory"); SB();
  asm volatile("ds_read_b128 a[36:39], %0 offset:34816" :  : "v"(aA[0]) : "memory"); SB();
  asm volatile("ds_read_b128 a[52:55], %0 offset:34816" :  : "v"(aA[1]) : "memory"); SB();
  asm volatile("s_add_u32 m0, %0, 131072" :  : "s"(mA) : "memory", "scc"); SB();
  asm volatile("s_nop 0\n\tbuffer_load_dwordx4 %0, %1, %2 offen lds" :  : "v"(voS), "s"(rA), "s"(ao[0]) : "memory"); SB();
  asm volatile("buffer_load_dwordx4 %0, %1, %2 offen offset:1024 lds" :  : "v"(voS), "s"(rA), "s"(ao[1]) : "memory"); SB();
  asm volatile("s_waitcnt vmcnt(14)" :  :  : "memory"); SB();
  asm volatile("ds_read_b128 a[40:43], %0 offset:36864" :  : "v"(aA[0]) : "memory"); SB();
  asm volatile("ds_read_b128 a[56:59], %0 offset:36864" :  : "v"(aA[1]) : "memory"); SB();
  asm volatile("s_waitcnt lgkmcnt(2)" :  :  : "memory"); SB();
  asm volatile("buffer_load_dwordx4 %0, %1, %2 offen offset:2048 lds" :  : "v"(voS), "s"(rA), "s"(ao[2]) : "memory"); SB();
  asm volatile("buffer_load_dwordx4 %0, %1, %2 offen offset:3072 lds" :  : "v"(voS), "s"(rA), "s"(ao[3]) : "memory"); SB();
  asm volatile("s_waitcnt vmcnt(14)" :  :  : "memory"); SB();
  asm volatile("ds_read_b128 a[44:47], %0 offset:38912" :  : "v"(aA[0]) : "memory"); SB();
  asm volatile("ds_read_b128 a[60:63], %0 offset:38912" :  : "v"(aA[1]) : "memory"); SB();
  asm volatile("s_waitcnt lgkmcnt(2)" :  :  : "memory"); SB();
  asm volatile("s_add_u32 m0, %0, 135168" :  : "s"(mA) : "memory", "scc"); SB();
  asm volatile("s_nop 0\n\tbuffer_load_dwordx4 %0, %1, %2 offen lds" :  : "v"(voS), "s"(rA), "s"(ao[4]) : "memory"); SB();
  asm volatile("buffer_load_dwordx4 %0, %1, %2 offen offset:1024 lds" :  : "v"(voS), "s"(rA), "s"(ao[5]) : "memory"); SB();
  asm volatile("s_waitcnt vmcnt(14)" :  :  : "memory"); SB();
  asm volatile("ds_read_b128 a[64:67], %0 offset:0" :  : "v"(aA[0]) : "memory"); SB();
  asm volatile("ds_read_b128 a[80:83], %0 offset:0" :  : "v"(aA[1]) : "memory"); SB();
  asm volatile("s_waitcnt vmcnt(13)\n\t.Lc_%=:\n\tv_cmp_ne_u64 vcc, %2, %1\n\ts_cbranch_vccnz .Lr_%=\n\t.Ld_%=:\n\t.subsection 1\n\t.Lr_%=:\n\tv_add_u32 %0, -1, %0\n\ts_nop 1\n\tv_readfirstlane_b32 vcc_hi, %0\n\ts_nop 3\n\ts_cmp_lt_i32 vcc_hi, 1\n\ts_cbranch_scc1 .Ld_%=\n\ts_sleep 2\n\ts_mov_b32 vcc_lo, 0\n\tbuffer_load_dwordx2 %1, %3, %4, vcc_lo offen sc1\n\ts_waitcnt vmcnt(0)\n\ts_branch .Lc_%=\n\t.subsection 0" : "+v"(tmo), "+v"(fz) : "s"(tg), "v"(vFZ), "s"(rWS) : "memory", "vcc", "scc"); SB();
  asm volatile("buffer_load_dwordx4 v[160:163], %0, %1, %2 offen offset:0 sc1" :  : "v"(vB), "s"(rWS), "s"(0u) : "memory"); SB();
  asm volatile("s_waitcnt lgkmcnt(2)" :  :  : "memory"); SB();
  asm volatile("buffer_load_dwordx4 v[164:167], %0, %1, %2 offen offset:1024 sc1" :  : "v"(vB), "s"(rWS), "s"(0u) : "memory"); SB();
  asm volatile("s_waitcnt vmcnt(12) lgkmcnt(0)\n\ts_barrier" :  :  : "memory"); SB();
  ST(14);
  asm volatile("ds_read2_b32 %0, %1 offset0:176 offset1:177" : "=&v"(cA[0][0][0]) : "v"(aa[0]) : "memory"); SB();
  asm volatile("ds_read2_b32 %0, %1 offset0:178 offset1:179" : "=&v"(cA[0][0][1]) : "v"(aa[0]) : "memory"); SB();
  asm volatile("ds_read_b128 a[124:127], %0 offset:3776" :  : "v"(bb) : "memory"); SB();
  asm volatile("ds_read_b128 a[120:123], %0 offset:3264" :  : "v"(bb) : "memory"); SB();
  asm volatile("ds_read_b128 a[116:119], %0 offset:2752" :  : "v"(bb) : "memory"); SB();
  asm volatile("ds_read_b128 a[112:115], %0 offset:2240" :  : "v"(bb) : "memory"); SB();
  asm volatile("ds_read2_b32 %0, %1 offset0:48 offset1:49" : "=&v"(cA[0][1][0]) : "v"(aa[0]) : "memory"); SB();
  asm volatile("ds_read2_b32 %0, %1 offset0:50 offset1:51" : "=&v"(cA[0][1][1]) : "v"(aa[0]) : "memory"); SB();
  asm volatile("ds_read_b128 a[108:111], %0 offset:1728" :  : "v"(bb) : "memory"); SB();
  asm volatile("ds_read2_b32 %0, %1 offset0:176 offset1:177" : "=&v"(cA[0][2][0]) : "v"(aa[1]) : "memory"); SB();
  asm volatile("" : "+v"(a1[0][0]), "+v"(a1[0][1]), "+v"(a1[0][2]), "+v"(a1[1][0]), "+v"(a1[1][1]), "+v"(a1[1][2]), "+v"(c1[0]), "+v"(c1[1]), "+v"(c1[2]), "+v"(co[0]), "+v"(co[1]), "+v"(co[2]) :  : ); SB();
  c1[0] += co[0]; c1[1] += co[1]; c1[2] += co[2];
#pragma unroll
  for (int n = 0; n < 6; ++n) { a1[n / 3][n % 3] = __builtin_elementwise_fma((f32x4){-xm[n / 3], -xm[n / 3], -xm[n / 3], -xm[n / 3]}, c1[n % 3], a1[n / 3][n % 3]); asm volatile("" : "+v"(a1[n / 3][n % 3])); }
  SB();
  asm volatile("ds_read_b128 a[68:71], %0 offset:2048" :  : "v"(aA[0]) : "memory"); SB();
  asm volatile("ds_read_b128 a[84:87], %0 offset:2048" :  : "v"(aA[1]) : "memory"); SB();
  asm volatile("s_waitcnt vmcnt(10)" :  :  : "memory"); SB();
  asm volatile("ds_read_b128 a[72:75], %0 offset:4096" :  : "v"(aA[0]) : "memory"); SB();
  asm volatile("ds_read_b128 a[88:91], %0 offset:4096" :  : "v"(aA[1]) : "memory"); SB();
  asm volatile("buffer_load_dwordx4 v[168:171], %0, %1, %2 offen offset:0 sc1" :  : "v"(vB), "s"(rWS), "s"(65536u) : "memory"); SB();
  asm volatile("buffer_load_dwordx4 v[172:175], %0, %1, %2 offen offset:1024 sc1" :  : "v"(vB), "s"(rWS), "s"(65536u) : "memory"); SB();
  asm volatile("v_xor_b32 %0, 131072, %0" : "+v"(vB) :  : "memory"); SB();
  if (nch == 3) {
  asm volatile("s_waitcnt lgkmcnt(5)\n\tv_mfma_f32_16x16x32_bf16 %0, %1, a[124:127], 0" : "=&v"(ac[7]) : "v"(((u32x4){cA[0][0][0][0], cA[0][0][0][1], cA[0][0][1][0], cA[0][0][1][1]})) : ); SB();
  asm volatile("ds_read2_b32 %0, %1 offset0:178 offset1:179" : "=&v"(cA[0][2][1]) : "v"(aa[1]) : "memory"); SB();
  asm volatile("v_mfma_f32_16x16x32_bf16 %0, %1, a[120:123], 0" : "=&v"(ac[6]) : "v"(((u32x4){cA[0][0][0][0], cA[0][0][0][1], cA[0][0][1][0], cA[0][0][1][1]})) : ); SB();
  asm volatile("ds_read2_b32 %0, %1 offset0:48 offset1:49" : "=&v"(cA[0][3][0]) : "v"(aa[1]) : "memory"); SB();
  asm volatile("v_mfma_f32_16x16x32_bf16 %0, %1, a[116:119], 0" : "=&v"(ac[5]) : "v"(((u32x4){cA[0][0][0][0], cA[0][0][0][1], cA[0][0][1][0], cA[0][0][1][1]})) : ); SB();
  asm volatile("ds_read2_b32 %0, %1 offset0:50 offset1:51" : "=&v"(cA[0][3][1]) : "v"(aa[1]) : "memory"); SB();
  asm volatile("s_waitcnt vmcnt(10)" :  :  : "memory"); SB();
  asm volatile("v_mfma_f32_16x16x32_bf16 %0, %1, a[112:115], 0" : "=&v"(ac[4]) : "v"(((u32x4){cA[0][0][0][0], cA[0][0][0][1], cA[0][0][1][0], cA[0][0][1][1]})) : ); SB();
  asm volatile("ds_read_b128 a[104:107], %0 offset:1216" :  : "v"(bb) : "memory"); SB();
  asm volatile("ds_read_b128 a[76:79], %0 offset:6144" :  : "v"(aA[0]) : "memory"); SB();
  asm volatile("v_mfma_f32_16x16x32_bf16 %0, %1, a[120:123], %0" : "+v"(ac[7]) : "v"(((u32x4){cA[0][1][0][0], cA[0][1][0][1], cA[0][1][1][0], cA[0][1][1][1]})) : ); SB();
  asm volatile("ds_read2_b32 %0, %1 offset0:176 offset1:177" : "=&v"(cA[0][4][0]) : "v"(aa[2]) : "memory"); SB();
  asm volatile("ds_read_b128 a[92:95], %0 offset:6144" :  : "v"(aA[1]) : "memory"); SB();
  asm volatile("v_mfma_f32_16x16x32_bf16 %0, %1, a[116:119], %0" : "+v"(ac[6]) : "v"(((u32x4){cA[0][1][0][0], cA[0][1][0][1], cA[0][1][1][0], cA[0][1][1][1]})) : ); SB();
  asm volatile("ds_read2_b32 %0, %1 offset0:178 offset1:179" : "=&v"(cA[0][4][1]) : "v"(aa[2]) : "memory"); SB();
  asm volatile("buffer_load_dwordx4 v[176:179], %0, %1, %2 offen offset:0 sc1" :  : "v"(vB), "s"(rWS), "s"(0u) : "memory"); SB();
  asm volatile("v_mfma_f32_16x16x32_bf16 %0, %1, a[112:115], %0" : "+v"(ac[5]) : "v"(((u32x4){cA[0][1][0][0], cA[0][1][0][1], cA[0][1][1][0], cA[0][1][1][1]})) : ); SB();
  asm volatile("ds_read2_b32 %0, %1 offset0:48 offset1:49" : "=&v"(cA[0][5][0]) : "v"(aa[2]) : "memory"); SB();
  asm volatile("v_mfma_f32_16x16x32_bf16 %0, %1, a[108:111], %0" : "+v"(ac[4]) : "v"(((u32x4){cA[0][1][0][0], cA[0][1][0][1], cA[0][1][1][0], cA[0][1][1][1]})) : ); SB();
  asm volatile("ds_read2_b32 %0, %1 offset0:50 offset1:51" : "=&v"(cA[0][5][1]) : "v"(aa[2]) : "memory"); SB();
  asm volatile("buffer_load_dwordx4 v[180:183], %0, %1, %2 offen offset:1024 sc1" :  : "v"(vB), "s"(rWS), "s"(0u) : "memory"); SB();
  asm volatile("s_waitcnt lgkmcnt(9)\n\tv_mfma_f32_16x16x32_bf16 %0, %1, a[116:119], %0" : "+v"(ac[7]) : "v"(((u32x4){cA[0][2][0][0], cA[0][2][0][1], cA[0][2][1][0], cA[0][2][1][1]})) : ); SB();
  asm volatile("ds_read_b128 a[100:103], %0 offset:704" :  : "v"(bb) : "memory"); SB();
  asm volatile("v_mfma_f32_16x16x32_bf16 %0, %1, a[112:115], %0" : "+v"(ac[6]) : "v"(((u32x4){cA[0][2][0][0], cA[0][2][0][1], cA[0][2][1][0], cA[0][2][1][1]})) : ); SB();
  asm volatile("ds_read2_b32 %0, %1 offset0:176 offset1:177" : "=&v"(cA[0][6][0]) : "v"(aa[3]) : "memory"); SB();
  asm volatile("v_mfma_f32_16x16x32_bf16 %0, %1, a[108:111], %0" : "+v"(ac[5]) : "v"(((u32x4){cA[0][2][0][0], cA[0][2][0][1], cA[0][2][1][0], cA[0][2][1][1]})) : ); SB();
  asm volatile("ds_read2_b32 %0, %1 offset0:178 offset1:179" : "=&v"(cA[0][6][1]) : "v"(aa[3]) : "memory"); SB();
  asm volatile("buffer_load_dwordx4 v[184:187], %0, %1, %2 offen offset:0 sc1" :  : "v"(vB), "s"(rWS), "s"(65536u) : "memory"); SB();
  asm volatile("v_mfma_f32_16x16x32_bf16 %0, %1, a[108:111], 0" : "=&v"(ac[3]) : "v"(((u32x4){cA[0][0][0][0], cA[0][0][0][1], cA[0][0][1][0], cA[0][0][1][1]})) : ); SB();
  asm volatile("ds_read2_b32 %0, %1 offset0:48 offset1:49" : "=&v"(cA[0][7][0]) : "v"(aa[3]) : "memory"); SB();
  asm volatile("s_waitcnt lgkmcnt(9)\n\tv_mfma_f32_16x16x32_bf16 %0, %1, a[112:115], %0" : "+v"(ac[7]) : "v"(((u32x4){cA[0][3][0][0], cA[0][3][0][1], cA[0][3][1][0], cA[0][3][1][1]})) : ); SB();
  asm volatile("ds_read2_b32 %0, %1 offset0:50 offset1:51" : "=&v"(cA[0][7][1]) : "v"(aa[3]) : "memory"); SB();
  asm volatile("v_mfma_f32_16x16x32_bf16 %0, %1, a[108:111], %0" : "+v"(ac[6]) : "v"(((u32x4){cA[0][3][0][0], cA[0][3][0][1], cA[0][3][1][0], cA[0][3][1][1]})) : ); SB();
  asm volatile("ds_read_b128 a[96:99], %0 offset:192" :  : "v"(bb) : "memory"); SB();
  asm volatile("buffer_load_dwordx4 v[188:191], %0, %1, %2 offen offset:1024 sc1" :  : "v"(vB), "s"(rWS), "s"(65536u) : "memory"); SB();
  asm volatile("v_mfma_f32_16x16x32_bf16 %0, %1, a[104:107], %0" : "+v"(ac[5]) : "v"(((u32x4){cA[0][3][0][0], cA[0][3][0][1], cA[0][3][1][0], cA[0][3][1][1]})) : ); SB();
  asm volatile("ds_read2_b32 %0, %1 offset0:160 offset1:161" : "=&v"(cA[1][0][0]) : "v"(aa[0]) : "memory"); SB();
  asm volatile("v_mfma_f32_16x16x32_bf16 %0, %1, a[104:107], %0" : "+v"(ac[4]) : "v"(((u32x4){cA[0][2][0][0], cA[0][2][0][1], cA[0][2][1][0], cA[0][2][1][1]})) : ); SB();
  asm volatile("v_xor_b32 %0, 393216, %0" : "+v"(vB) :  : "memory"); SB();
  asm volatile("s_waitcnt lgkmcnt(7)\n\tv_mfma_f32_16x16x32_bf16 %0, %1, a[108:111], %0" : "+v"(ac[7]) : "v"(((u32x4){cA[0][4][0][0], cA[0][4][0][1], cA[0][4][1][0], cA[0][4][1][1]})) : ); SB();
  asm volatile("ds_read2_b32 %0, %1 offset0:162 offset1:163" : "=&v"(cA[1][0][1]) : "v"(aa[0]) : "memory"); SB();
  asm volatile("v_mfma_f32_16x16x32_bf16 %0, %1, a[104:107], %0" : "+v"(ac[6]) : "v"(((u32x4){cA[0][4][0][0], cA[0][4][0][1], cA[0][4][1][0], cA[0][4][1][1]})) : ); SB();
  asm volatile("ds_read_b128 a[156:159], %0 offset:3712" :  : "v"(bb) : "memory"); SB();
  asm volatile("buffer_load_dwordx4 v[192:195], %0, %1, %2 offen offset:0 sc1" :  : "v"(vB), "s"(rWS), "s"(0u) : "memory"); SB();
  asm volatile("v_mfma_f32_16x16x32_bf16 %0, %1, a[104:107], %0" : "+v"(ac[3]) : "v"(((u32x4){cA[0][1][0][0], cA[0][1][0][1], cA[0][1][1][0], cA[0][1][1][1]})) : ); SB();
  asm volatile("ds_read_b128 a[152:155], %0 offset:3200" :  : "v"(bb) : "memory"); SB();
  asm volatile("v_mfma_f32_16x16x32_bf16 %0, %1, a[104:107], 0" : "=&v"(ac[2]) : "v"(((u32x4){cA[0][0][0][0], cA[0][0][0][1], cA[0][0][1][0], cA[0][0][1][1]})) : ); SB();
  asm volatile("buffer_load_dwordx4 v[196:199], %0, %1, %2 offen offset:1024 sc1" :  : "v"(vB), "s"(rWS), "s"(0u) : "memory"); SB();
  asm volatile("v_mfma_f32_16x16x32_bf16 %0, %1, a[104:107], %0" : "+v"(ac[7]) : "v"(((u32x4){cA[0][5][0][0], cA[0][5][0][1], cA[0][5][1][0], cA[0][5][1][1]})) : ); SB();
  asm volatile("ds_read_b128 a[128:131], %0 offset:128" :  : "v"(bb) : "memory"); SB();
  asm volatile("s_waitcnt lgkmcnt(7)\n\tv_mfma_f32_16x16x32_bf16 %0, %1, a[100:103], %0" : "+v"(ac[6]) : "v"(((u32x4){cA[0][5][0][0], cA[0][5][0][1], cA[0][5][1][0], cA[0][5][1][1]})) : ); SB();
  asm volatile("ds_read_b128 a[132:135], %0 offset:640" :  : "v"(bb) : "memory"); SB();
  asm volatile("v_mfma_f32_16x16x32_bf16 %0, %1, a[100:103], %0" : "+v"(ac[5]) : "v"(((u32x4){cA[0][4][0][0], cA[0][4][0][1], cA[0][4][1][0], cA[0][4][1][1]})) : ); SB();
  asm volatile("ds_read2_b32 %0, %1 offset0:32 offset1:33" : "=&v"(cA[1][1][0]) : "v"(aa[0]) : "memory"); SB();
  asm volatile("v_mfma_f32_16x16x32_bf16 %0, %1, a[100:103], %0" : "+v"(ac[4]) : "v"(((u32x4){cA[0][3][0][0], cA[0][3][0][1], cA[0][3][1][0], cA[0][3][1][1]})) : ); SB();
  asm volatile("buffer_load_dwordx4 v[200:203], %0, %1, %2 offen offset:0 sc1" :  : "v"(vB), "s"(rWS), "s"(65536u) : "memory"); SB();
  asm volatile("v_mfma_f32_16x16x32_bf16 %0, %1, a[100:103], %0" : "+v"(ac[7]) : "v"(((u32x4){cA[0][6][0][0], cA[0][6][0][1], cA[0][6][1][0], cA[0][6][1][1]})) : ); SB();
  asm volatile("ds_read2_b32 %0, %1 offset0:34 offset1:35" : "=&v"(cA[1][1][1]) : "v"(aa[0]) : "memory"); SB();
  asm volatile("v_mfma_f32_16x16x32_bf16 %0, %1, a[100:103], %0" : "+v"(ac[3]) : "v"(((u32x4){cA[0][2][0][0], cA[0][2][0][1], cA[0][2][1][0], cA[0][2][1][1]})) : ); SB();
  asm volatile("ds_read_b128 a[148:151], %0 offset:2688" :  : "v"(bb) : "memory"); SB();
  asm volatile("v_mfma_f32_16x16x32_bf16 %0, %1, a[100:103], %0" : "+v"(ac[2]) : "v"(((u32x4){cA[0][1][0][0], cA[0][1][0][1], cA[0][1][1][0], cA[0][1][1][1]})) : ); SB();
  asm volatile("ds_read_b128 a[136:139], %0 offset:1152" :  : "v"(bb) : "memory"); SB();
  asm volatile("v_mfma_f32_16x16x32_bf16 %0, %1, a[100:103], 0" : "=&v"(ac[1]) : "v"(((u32x4){cA[0][0][0][0], cA[0][0][0][1], cA[0][0][1][0], cA[0][0][1][1]})) : ); SB();
  asm volatile("buffer_load_dwordx4 v[204:207], %0, %1, %2 offen offset:1024 sc1" :  : "v"(vB), "s"(rWS), "s"(65536u) : "memory"); SB();
  asm volatile("s_waitcnt lgkmcnt(6)\n\tv_mfma_f32_16x16x32_bf16 %0, %1, a[96:99], %0" : "+v"(ac[7]) : "v"(((u32x4){cA[0][7][0][0], cA[0][7][0][1], cA[0][7][1][0], cA[0][7][1][1]})) : ); SB();
  asm volatile("ds_read2_b32 %0, %1 offset0:160 offset1:161" : "=&v"(cA[1][2][0]) : "v"(aa[1]) : "memory"); SB();
  asm volatile("v_mfma_f32_16x16x32_bf16 %0, %1, a[96:99], %0" : "+v"(ac[6]) : "v"(((u32x4){cA[0][6][0][0], cA[0][6][0][1], cA[0][6][1][0], cA[0][6][1][1]})) : ); SB();
  asm volatile("ds_read2_b32 %0, %1 offset0:162 offset1:163" : "=&v"(cA[1][2][1]) : "v"(aa[1]) : "memory"); SB();
  asm volatile("v_mfma_f32_16x16x32_bf16 %0, %1, a[96:99], %0" : "+v"(ac[5]) : "v"(((u32x4){cA[0][5][0][0], cA[0][5][0][1], cA[0][5][1][0], cA[0][5][1][1]})) : ); SB();
  asm volatile("ds_read_b128 a[140:143], %0 offset:1664" :  : "v"(bb) : "memory"); SB();
  asm volatile("v_mfma_f32_16x16x32_bf16 %0, %1, a[96:99], %0" : "+v"(ac[4]) : "v"(((u32x4){cA[0][4][0][0], cA[0][4][0][1], cA[0][4][1][0], cA[0][4][1][1]})) : ); SB();
  asm volatile("v_xor_b32 %0, 262144, %0" : "+v"(vB) :  : "memory"); SB();
  asm volatile("v_mfma_f32_16x16x32_bf16 %0, %1, a[96:99], %0" : "+v"(ac[3]) : "v"(((u32x4){cA[0][3][0][0], cA[0][3][0][1], cA[0][3][1][0], cA[0][3][1][1]})) : ); SB();
  asm volatile("ds_read_b128 a[144:147], %0 offset:2176" :  : "v"(bb) : "memory"); SB();
  asm volatile("buffer_load_dwordx4 v[208:211], %0, %1, %2 offen offset:0 sc1" :  : "v"(vB), "s"(rWS), "s"(2048u) : "memory"); SB();
  asm volatile("v_mfma_f32_16x16x32_bf16 %0, %1, a[96:99], %0" : "+v"(ac[2]) : "v"(((u32x4){cA[0][2][0][0], cA[0][2][0][1], cA[0][2][1][0], cA[0][2][1][1]})) : ); SB();
  asm volatile("ds_read2_b32 %0, %1 offset0:32 offset1:33" : "=&v"(cA[1][3][0]) : "v"(aa[1]) : "memory"); SB();
  asm volatile("v_mfma_f32_16x16x32_bf16 %0, %1, a[96:99], %0" : "+v"(ac[1]) : "v"(((u32x4){cA[0][1][0][0], cA[0][1][0][1], cA[0][1][1][0], cA[0][1][1][1]})) : ); SB();
  asm volatile("ds_read2_b32 %0, %1 offset0:34 offset1:35" : "=&v"(cA[1][3][1]) : "v"(aa[1]) : "memory"); SB();
  asm volatile("v_mfma_f32_16x16x32_bf16 %0, %1, a[96:99], 0" : "=&v"(ac[0]) : "v"(((u32x4){cA[0][0][0][0], cA[0][0][0][1], cA[0][0][1][0], cA[0][0][1][1]})) : ); SB();
  asm volatile("buffer_load_dwordx4 v[212:215], %0, %1, %2 offen offset:1024 sc1" :  : "v"(vB), "s"(rWS), "s"(2048u) : "memory"); SB();
  asm volatile("v_mfma_f32_16x16x32_bf16 %0, %1, a[156:159], %0" : "+v"(ac[7]) : "v"(((u32x4){cA[1][0][0][0], cA[1][0][0][1], cA[1][0][1][0], cA[1][0][1][1]})) : ); SB();
  asm volatile("ds_read2_b32 %0, %1 offset0:160 offset1:161" : "=&v"(cA[1][4][0]) : "v"(aa[2]) : "memory"); SB();
  asm volatile("v_mfma_f32_16x16x32_bf16 %0, %1, a[152:155], %0" : "+v"(ac[6]) : "v"(((u32x4){cA[1][0][0][0], cA[1][0][0][1], cA[1][0][1][0], cA[1][0][1][1]})) : ); SB();
  asm volatile("ds_read2_b32 %0, %1 offset0:162 offset1:163" : "=&v"(cA[1][4][1]) : "v"(aa[2]) : "memory"); SB();
  asm volatile("s_waitcnt lgkmcnt(7)\n\tv_mfma_f32_16x16x32_bf16 %0, %1, a[128:131], %0" : "+v"(ac[0]) : "v"(((u32x4){cA[1][0][0][0], cA[1][0][0][1], cA[1][0][1][0], cA[1][0][1][1]})) : ); SB();
  asm volatile("ds_read2_b32 %0, %1 offset0:32 offset1:33" : "=&v"(cA[1][5][0]) : "v"(aa[2]) : "memory"); SB();
  asm volatile("v_mfma_f32_16x16x32_bf16 %0, %1, a[132:135], %0" : "+v"(ac[1]) : "v"(((u32x4){cA[1][0][0][0], cA[1][0][0][1], cA[1][0][1][0], cA[1][0][1][1]})) : ); SB();
  asm volatile("buffer_load_dwordx4 v[216:219], %0, %1, %2 offen offset:0 sc1" :  : "v"(vB), "s"(rWS), "s"(67584u) : "memory"); SB();
  asm volatile("v_mfma_f32_16x16x32_bf16 %0, %1, a[152:155], %0" : "+v"(ac[7]) : "v"(((u32x4){cA[1][1][0][0], cA[1][1][0][1], cA[1][1][1][0], cA[1][1][1][1]})) : ); SB();
  asm volatile("ds_read2_b32 %0, %1 offset0:34 offset1:35" : "=&v"(cA[1][5][1]) : "v"(aa[2]) : "memory"); SB();
  asm volatile("v_mfma_f32_16x16x32_bf16 %0, %1, a[148:151], %0" : "+v"(ac[6]) : "v"(((u32x4){cA[1][1][0][0], cA[1][1][0][1], cA[1][1][1][0], cA[1][1][1][1]})) : ); SB();
  asm volatile("ds_read2_b32 %0, %1 offset0:160 offset1:161" : "=&v"(cA[1][6][0]) : "v"(aa[3]) : "memory"); SB();
  asm volatile("v_mfma_f32_16x16x32_bf16 %0, %1, a[136:139], %0" : "+v"(ac[2]) : "v"(((u32x4){cA[1][0][0][0], cA[1][0][0][1], cA[1][0][1][0], cA[1][0][1][1]})) : ); SB();
  asm volatile("ds_read2_b32 %0, %1 offset0:162 offset1:163" : "=&v"(cA[1][6][1]) : "v"(aa[3]) : "memory"); SB();
  asm volatile("v_mfma_f32_16x16x32_bf16 %0, %1, a[128:131], %0" : "+v"(ac[1]) : "v"(((u32x4){cA[1][1][0][0], cA[1][1][0][1], cA[1][1][1][0], cA[1][1][1][1]})) : ); SB();
  asm volatile("ds_write_b128 %0, %1 offset:0" :  : "v"(dd), "v"(ac[0]) : "memory"); SB();
  asm volatile("s_waitcnt lgkmcnt(7)\n\tv_mfma_f32_16x16x32_bf16 %0, %1, a[148:151], %0" : "+v"(ac[7]) : "v"(((u32x4){cA[1][2][0][0], cA[1][2][0][1], cA[1][2][1][0], cA[1][2][1][1]})) : ); SB();
  asm volatile("ds_read2_b32 %0, %1 offset0:32 offset1:33" : "=&v"(cA[1][7][0]) : "v"(aa[3]) : "memory"); SB();
  asm volatile("v_mfma_f32_16x16x32_bf16 %0, %1, a[140:143], %0" : "+v"(ac[3]) : "v"(((u32x4){cA[1][0][0][0], cA[1][0][0][1], cA[1][0][1][0], cA[1][0][1][1]})) : ); SB();
  asm volatile("ds_read2_b32 %0, %1 offset0:34 offset1:35" : "=&v"(cA[1][7][1]) : "v"(aa[3]) : "memory"); SB();
  asm volatile("v_mfma_f32_16x16x32_bf16 %0, %1, a[132:135], %0" : "+v"(ac[2]) : "v"(((u32x4){cA[1][1][0][0], cA[1][1][0][1], cA[1][1][1][0], cA[1][1][1][1]})) : ); SB();
  asm volatile("buffer_load_dwordx4 v[220:223], %0, %1, %2 offen offset:1024 sc1" :  : "v"(vB), "s"(rWS), "s"(67584u) : "memory"); SB();
  asm volatile("v_mfma_f32_16x16x32_bf16 %0, %1, a[144:147], %0" : "+v"(ac[4]) : "v"(((u32x4){cA[1][0][0][0], cA[1][0][0][1], cA[1][0][1][0], cA[1][0][1][1]})) : ); SB();
  asm volatile("ds_write_b128 %0, %1 offset:1024" :  : "v"(dd), "v"(ac[1]) : "memory"); SB();
  asm volatile("v_mfma_f32_16x16x32_bf16 %0, %1, a[144:147], %0" : "+v"(ac[7]) : "v"(((u32x4){cA[1][3][0][0], cA[1][3][0][1], cA[1][3][1][0], cA[1][3][1][1]})) : ); SB();
  asm volatile("v_xor_b32 %0, 131072, %0" : "+v"(vB) :  : "memory"); SB();
  asm volatile("v_mfma_f32_16x16x32_bf16 %0, %1, a[136:139], %0" : "+v"(ac[3]) : "v"(((u32x4){cA[1][1][0][0], cA[1][1][0][1], cA[1][1][1][0], cA[1][1][1][1]})) : ); SB();
  asm volatile("buffer_load_dwordx4 v[224:227], %0, %1, %2 offen offset:0 sc1" :  : "v"(vB), "s"(rWS), "s"(2048u) : "memory"); SB();
  asm volatile("v_mfma_f32_16x16x32_bf16 %0, %1, a[128:131], %0" : "+v"(ac[2]) : "v"(((u32x4){cA[1][2][0][0], cA[1][2][0][1], cA[1][2][1][0], cA[1][2][1][1]})) : ); SB();
  asm volatile("ds_write_b128 %0, %1 offset:0" :  : "v"(ew[0]), "v"(a1[0][0]) : "memory"); SB();
  asm volatile("v_mfma_f32_16x16x32_bf16 %0, %1, a[140:143], %0" : "+v"(ac[4]) : "v"(((u32x4){cA[1][1][0][0], cA[1][1][0][1], cA[1][1][1][0], cA[1][1][1][1]})) : ); SB();
  asm volatile("buffer_load_dwordx4 v[228:231], %0, %1, %2 offen offset:1024 sc1" :  : "v"(vB), "s"(rWS), "s"(2048u) : "memory"); SB();
  asm volatile("v_mfma_f32_16x16x32_bf16 %0, %1, a[148:151], %0" : "+v"(ac[5]) : "v"(((u32x4){cA[1][0][0][0], cA[1][0][0][1], cA[1][0][1][0], cA[1][0][1][1]})) : ); SB();
  asm volatile("ds_write_b128 %0, %1 offset:1024" :  : "v"(ew[0]), "v"(a1[0][1]) : "memory"); SB();
  asm volatile("v_mfma_f32_16x16x32_bf16 %0, %1, a[132:135], %0" : "+v"(ac[3]) : "v"(((u32x4){cA[1][2][0][0], cA[1][2][0][1], cA[1][2][1][0], cA[1][2][1][1]})) : ); SB();
  asm volatile("ds_write_b128 %0, %1 offset:2048" :  : "v"(ew[0]), "v"(a1[0][2]) : "memory"); SB();
  asm volatile("v_mfma_f32_16x16x32_bf16 %0, %1, a[144:147], %0" : "+v"(ac[6]) : "v"(((u32x4){cA[1][2][0][0], cA[1][2][0][1], cA[1][2][1][0], cA[1][2][1][1]})) : ); SB();
  asm volatile("ds_write_b128 %0, %1 offset:2048" :  : "v"(dd), "v"(ac[2]) : "memory"); SB();
  asm volatile("v_mfma_f32_16x16x32_bf16 %0, %1, a[136:139], %0" : "+v"(ac[4]) : "v"(((u32x4){cA[1][2][0][0], cA[1][2][0][1], cA[1][2][1][0], cA[1][2][1][1]})) : ); SB();
  asm volatile("buffer_load_dwordx4 v[232:235], %0, %1, %2 offen offset:0 sc1" :  : "v"(vB), "s"(rWS), "s"(67584u) : "memory"); SB();
  asm volatile("v_mfma_f32_16x16x32_bf16 %0, %1, a[144:147], %0" : "+v"(ac[5]) : "v"(((u32x4){cA[1][1][0][0], cA[1][1][0][1], cA[1][1][1][0], cA[1][1][1][1]})) : ); SB();
  asm volatile("buffer_load_dwordx4 v[236:239], %0, %1, %2 offen offset:1024 sc1" :  : "v"(vB), "s"(rWS), "s"(67584u) : "memory"); SB();
  asm volatile("v_mfma_f32_16x16x32_bf16 %0, %1, a[128:131], %0" : "+v"(ac[3]) : "v"(((u32x4){cA[1][3][0][0], cA[1][3][0][1], cA[1][3][1][0], cA[1][3][1][1]})) : ); SB();
  asm volatile("v_xor_b32 %0, 393216, %0" : "+v"(vB) :  : "memory"); SB();
  asm volatile("v_mfma_f32_16x16x32_bf16 %0, %1, a[140:143], %0" : "+v"(ac[6]) : "v"(((u32x4){cA[1][3][0][0], cA[1][3][0][1], cA[1][3][1][0], cA[1][3][1][1]})) : ); SB();
  asm volatile("ds_write_b128 %0, %1 offset:0" :  : "v"(ew[1]), "v"(a1[1][0]) : "memory"); SB();
  asm volatile("v_mfma_f32_16x16x32_bf16 %0, %1, a[132:135], %0" : "+v"(ac[4]) : "v"(((u32x4){cA[1][3][0][0], cA[1][3][0][1], cA[1][3][1][0], cA[1][3][1][1]})) : ); SB();
  asm volatile("buffer_load_dwordx4 v[240:243], %0, %1, %2 offen offset:0 sc1" :  : "v"(vB), "s"(rWS), "s"(2048u) : "memory"); SB();
  asm volatile("v_mfma_f32_16x16x32_bf16 %0, %1, a[140:143], %0" : "+v"(ac[5]) : "v"(((u32x4){cA[1][2][0][0], cA[1][2][0][1], cA[1][2][1][0], cA[1][2][1][1]})) : ); SB();
  asm volatile("s_waitcnt lgkmcnt(2)\n\tv_mfma_f32_16x16x32_bf16 %0, %1, a[140:143], %0" : "+v"(ac[7]) : "v"(((u32x4){cA[1][4][0][0], cA[1][4][0][1], cA[1][4][1][0], cA[1][4][1][1]})) : ); SB();
  asm volatile("ds_write_b128 %0, %1 offset:3072" :  : "v"(dd), "v"(ac[3]) : "memory"); SB();
  asm volatile("v_mfma_f32_16x16x32_bf16 %0, %1, a[136:139], %0" : "+v"(ac[6]) : "v"(((u32x4){cA[1][4][0][0], cA[1][4][0][1], cA[1][4][1][0], cA[1][4][1][1]})) : ); SB();
  asm volatile("buffer_load_dwordx4 v[244:247], %0, %1, %2 offen offset:1024 sc1" :  : "v"(vB), "s"(rWS), "s"(2048u) : "memory"); SB();
  asm volatile("v_mfma_f32_16x16x32_bf16 %0, %1, a[128:131], %0" : "+v"(ac[4]) : "v"(((u32x4){cA[1][4][0][0], cA[1][4][0][1], cA[1][4][1][0], cA[1][4][1][1]})) : ); SB();
  asm volatile("ds_write_b128 %0, %1 offset:1024" :  : "v"(ew[1]), "v"(a1[1][1]) : "memory"); SB();
  asm volatile("v_mfma_f32_16x16x32_bf16 %0, %1, a[136:139], %0" : "+v"(ac[5]) : "v"(((u32x4){cA[1][3][0][0], cA[1][3][0][1], cA[1][3][1][0], cA[1][3][1][1]})) : ); SB();
  asm volatile("buffer_load_dwordx4 v[248:251], %0, %1, %2 offen offset:0 sc1" :  : "v"(vB), "s"(rWS), "s"(67584u) : "memory"); SB();
  asm volatile("v_mfma_f32_16x16x32_bf16 %0, %1, a[136:139], %0" : "+v"(ac[7]) : "v"(((u32x4){cA[1][5][0][0], cA[1][5][0][1], cA[1][5][1][0], cA[1][5][1][1]})) : ); SB();
  asm volatile("ds_write_b128 %0, %1 offset:2048" :  : "v"(ew[1]), "v"(a1[1][2]) : "memory"); SB();
  asm volatile("v_mfma_f32_16x16x32_bf16 %0, %1, a[132:135], %0" : "+v"(ac[6]) : "v"(((u32x4){cA[1][5][0][0], cA[1][5][0][1], cA[1][5][1][0], cA[1][5][1][1]})) : ); SB();
  asm volatile("buffer_load_dwordx4 v[252:255], %0, %1, %2 offen offset:1024 sc1" :  : "v"(vB), "s"(rWS), "s"(67584u) : "memory"); SB();
  asm volatile("v_mfma_f32_16x16x32_bf16 %0, %1, a[132:135], %0" : "+v"(ac[5]) : "v"(((u32x4){cA[1][4][0][0], cA[1][4][0][1], cA[1][4][1][0], cA[1][4][1][1]})) : ); SB();
  asm volatile("ds_write_b128 %0, %1 offset:4096" :  : "v"(dd), "v"(ac[4]) : "memory"); SB();
  asm volatile("v_mfma_f32_16x16x32_bf16 %0, %1, a[132:135], %0" : "+v"(ac[7]) : "v"(((u32x4){cA[1][6][0][0], cA[1][6][0][1], cA[1][6][1][0], cA[1][6][1][1]})) : ); SB();
  asm volatile("v_xor_b32 %0, 131072, %0" : "+v"(vB) :  : "memory"); SB();
  asm volatile("v_mfma_f32_16x16x32_bf16 %0, %1, a[128:131], %0" : "+v"(ac[6]) : "v"(((u32x4){cA[1][6][0][0], cA[1][6][0][1], cA[1][6][1][0], cA[1][6][1][1]})) : ); SB();
  asm volatile("buffer_load_dwordx4 %0, %1, %2 offen offset:2048 lds" :  : "v"(voS), "s"(rA), "s"(ao[6]) : "memory"); SB();
  asm volatile("v_mfma_f32_16x16x32_bf16 %0, %1, a[128:131], %0" : "+v"(ac[5]) : "v"(((u32x4){cA[1][5][0][0], cA[1][5][0][1], cA[1][5][1][0], cA[1][5][1][1]})) : ); SB();
  asm volatile("v_mfma_f32_16x16x32_bf16 %0, %1, a[128:131], %0" : "+v"(ac[7]) : "v"(((u32x4){cA[1][7][0][0], cA[1][7][0][1], cA[1][7][1][0], cA[1][7][1][1]})) : ); SB();
  asm volatile("s_nop 5" : "+v"(ac[5]), "+v"(ac[6]), "+v"(ac[7]) :  : ); SB();
  asm volatile("ds_write_b128 %0, %1 offset:6144" :  : "v"(dd), "v"(ac[6]) : "memory"); SB();
  asm volatile("ds_write_b128 %0, %1 offset:5120" :  : "v"(dd), "v"(ac[5]) : "memory"); SB();
  asm volatile("ds_write_b128 %0, %1 offset:7168" :  : "v"(dd), "v"(ac[7]) : "memory"); SB();
  } else {
  asm volatile("; ELSE_BEGIN"); SB();
  asm volatile("s_waitcnt lgkmcnt(0)" ::: "memory"); SB();
  asm volatile("s_waitcnt vmcnt(10)" :  :  : "memory"); SB();
  asm volatile("s_waitcnt lgkmcnt(0)" ::: "memory"); SB();
  asm volatile("ds_read_b128 a[76:79], %0 offset:6144" :  : "v"(aA[0]) : "memory"); SB();
  asm volatile("s_waitcnt lgkmcnt(0)" ::: "memory"); SB();
  asm volatile("ds_read_b128 a[92:95], %0 offset:6144" :  : "v"(aA[1]) : "memory"); SB();
  asm volatile("s_waitcnt lgkmcnt(0)" ::: "memory"); SB();
  asm volatile("buffer_load_dwordx4 v[176:179], %0, %1, %2 offen offset:0 sc1" :  : "v"(vB), "s"(rWS), "s"(0u) : "memory"); SB();
  asm volatile("s_waitcnt lgkmcnt(0)" ::: "memory"); SB();
  asm volatile("buffer_load_dwordx4 v[180:183], %0, %1, %2 offen offset:1024 sc1" :  : "v"(vB), "s"(rWS), "s"(0u) : "memory"); SB();
  asm volatile("s_waitcnt lgkmcnt(0)" ::: "memory"); SB();
  asm volatile("buffer_load_dwordx4 v[184:187], %0, %1, %2 offen offset:0 sc1" :  : "v"(vB), "s"(rWS), "s"(65536u) : "memory"); SB();
  asm volatile("s_waitcnt lgkmcnt(0)" ::: "memory"); SB();
  asm volatile("buffer_load_dwordx4 v[188:191], %0, %1, %2 offen offset:1024 sc1" :  : "v"(vB), "s"(rWS), "s"(65536u) : "memory"); SB();
  asm volatile("s_waitcnt lgkmcnt(0)" ::: "memory"); SB();
  asm volatile("v_xor_b32 %0, 393216, %0" : "+v"(vB) :  : "memory"); SB();
  asm volatile("s_waitcnt lgkmcnt(0)" ::: "memory"); SB();
  asm volatile("buffer_load_dwordx4 v[192:195], %0, %1, %2 offen offset:0 sc1" :  : "v"(vB), "s"(rWS), "s"(0u) : "memory"); SB();
  asm volatile("s_waitcnt lgkmcnt(0)" ::: "memory"); SB();
  asm volatile("buffer_load_dwordx4 v[196:199], %0, %1, %2 offen offset:1024 sc1" :  : "v"(vB), "s"(rWS), "s"(0u) : "memory"); SB();
  asm volatile("s_waitcnt lgkmcnt(0)" ::: "memory"); SB();
  asm volatile("buffer_load_dwordx4 v[200:203], %0, %1, %2 offen offset:0 sc1" :  : "v"(vB), "s"(rWS), "s"(65536u) : "memory"); SB();
  asm volatile("s_waitcnt lgkmcnt(0)" ::: "memory"); SB();
  asm volatile("buffer_load_dwordx4 v[204:207], %0, %1, %2 offen offset:1024 sc1" :  : "v"(vB), "s"(rWS), "s"(65536u) : "memory"); SB();
  asm volatile("s_waitcnt lgkmcnt(0)" ::: "memory"); SB();
  asm volatile("v_xor_b32 %0, 262144, %0" : "+v"(vB) :  : "memory"); SB();
  asm volatile("s_waitcnt lgkmcnt(0)" ::: "memory"); SB();
  asm volatile("buffer_load_dwordx4 v[208:211], %0, %1, %2 offen offset:0 sc1" :  : "v"(vB), "s"(rWS), "s"(2048u) : "memory"); SB();
  asm volatile("s_waitcnt lgkmcnt(0)" ::: "memory"); SB();
  asm volatile("buffer_load_dwordx4 v[212:215], %0, %1, %2 offen offset:1024 sc1" :  : "v"(vB), "s"(rWS), "s"(2048u) : "memory"); SB();
  asm volatile("s_waitcnt lgkmcnt(0)" ::: "memory"); SB();
  asm volatile("buffer_load_dwordx4 v[216:219], %0, %1, %2 offen offset:0 sc1" :  : "v"(vB), "s"(rWS), "s"(67584u) : "memory"); SB();
  asm volatile("s_waitcnt lgkmcnt(0)" ::: "memory"); SB();
  asm volatile("buffer_load_dwordx4 v[220:223], %0, %1, %2 offen offset:1024 sc1" :  : "v"(vB), "s"(rWS), "s"(67584u) : "memory"); SB();
  asm volatile("s_waitcnt lgkmcnt(0)" ::: "memory"); SB();
  asm volatile("v_xor_b32 %0, 131072, %0" : "+v"(vB) :  : "memory"); SB();
  asm volatile("s_waitcnt lgkmcnt(0)" ::: "memory"); SB();
  asm volatile("buffer_load_dwordx4 v[224:227], %0, %1, %2 offen offset:0 sc1" :  : "v"(vB), "s"(rWS), "s"(2048u) : "memory"); SB();
  asm volatile("s_waitcnt lgkmcnt(0)" ::: "memory"); SB();
  asm volatile("ds_write_b128 %0, %1 offset:0" :  : "v"(ew[0]), "v"(a1[0][0]) : "memory"); SB();
  asm volatile("s_waitcnt lgkmcnt(0)" ::: "memory"); SB();
  asm volatile("buffer_load_dwordx4 v[228:231], %0, %1, %2 offen offset:1024 sc1" :  : "v"(vB), "s"(rWS), "s"(2048u) : "memory"); SB();
  asm volatile("s_waitcnt lgkmcnt(0)" ::: "memory"); SB();
  asm volatile("ds_write_b128 %0, %1 offset:1024" :  : "v"(ew[0]), "v"(a1[0][1]) : "memory"); SB();
  asm volatile("s_waitcnt lgkmcnt(0)" ::: "memory"); SB();
  asm volatile("ds_write_b128 %0, %1 offset:2048" :  : "v"(ew[0]), "v"(a1[0][2]) : "memory"); SB();
  asm volatile("s_waitcnt lgkmcnt(0)" ::: "memory"); SB();
  asm volatile("buffer_load_dwordx4 v[232:235], %0, %1, %2 offen offset:0 sc1" :  : "v"(vB), "s"(rWS), "s"(67584u) : "memory"); SB();
  asm volatile("s_waitcnt lgkmcnt(0)" ::: "memory"); SB();
  asm volatile("buffer_load_dwordx4 v[236:239], %0, %1, %2 offen offset:1024 sc1" :  : "v"(vB), "s"(rWS), "s"(67584u) : "memory"); SB();
  asm volatile("s_waitcnt lgkmcnt(0)" ::: "memory"); SB();
  asm volatile("v_xor_b32 %0, 393216, %0" : "+v"(vB) :  : "memory"); SB();
  asm volatile("s_waitcnt lgkmcnt(0)" ::: "memory"); SB();
  asm volatile("ds_write_b128 %0, %1 offset:0" :  : "v"(ew[1]), "v"(a1[1][0]) : "memory"); SB();
  asm volatile("s_waitcnt lgkmcnt(0)" ::: "memory"); SB();
  asm volatile("buffer_load_dwordx4 v[240:243], %0, %1, %2 offen offset:0 sc1" :  : "v"(vB), "s"(rWS), "s"(2048u) : "memory"); SB();
  asm volatile("s_waitcnt lgkmcnt(0)" ::: "memory"); SB();
  asm volatile("buffer_load_dwordx4 v[244:247], %0, %1, %2 offen offset:1024 sc1" :  : "v"(vB), "s"(rWS), "s"(2048u) : "memory"); SB();
  asm volatile("s_waitcnt lgkmcnt(0)" ::: "memory"); SB();
  asm volatile("ds_write_b128 %0, %1 offset:1024" :  : "v"(ew[1]), "v"(a1[1][1]) : "memory"); SB();
  asm volatile("s_waitcnt lgkmcnt(0)" ::: "memory"); SB();
  asm volatile("buffer_load_dwordx4 v[248:251], %0, %1, %2 offen offset:0 sc1" :  : "v"(vB), "s"(rWS), "s"(67584u) : "memory"); SB();
  asm volatile("s_waitcnt lgkmcnt(0)" ::: "memory"); SB();
  asm volatile("ds_write_b128 %0, %1 offset:2048" :  : "v"(ew[1]), "v"(a1[1][2]) : "memory"); SB();
  asm volatile("s_waitcnt lgkmcnt(0)" ::: "memory"); SB();
  asm volatile("buffer_load_dwordx4 v[252:255], %0, %1, %2 offen offset:1024 sc1" :  : "v"(vB), "s"(rWS), "s"(67584u) : "memory"); SB();
  asm volatile("s_waitcnt lgkmcnt(0)" ::: "memory"); SB();
  asm volatile("v_xor_b32 %0, 131072, %0" : "+v"(vB) :  : "memory"); SB();
  asm volatile("s_waitcnt lgkmcnt(0)" ::: "memory"); SB();
  asm volatile("s_waitcnt lgkmcnt(0)" ::: "memory"); SB();
  asm volatile("buffer_load_dwordx4 %0, %1, %2 offen offset:2048 lds" :  : "v"(voS), "s"(rA), "s"(ao[6]) : "memory"); SB();
  asm volatile("s_waitcnt lgkmcnt(0) ; ELSE_END" ::: "memory"); SB();
  }
  ST(15);
  f32x4 g[2][4][2], pr[6], yq[8], bs4 = {bs, bs, bs, bs};
  u32x4 ho[3], ykv, y2;
  float gt[4], ge[4];
  unsigned vH_, pw2, pe[3], vX, vO, vO2, vP, vFH, vFY, vHr, hso[8], ys2, ydr, ykr, er, sFH, sFY;
  int dl;
  asm volatile("" : "+v"(bs4)); SB();
  asm volatile("s_waitcnt vmcnt(23)" :  :  : "memory"); SB();
  asm volatile("v_mfma_f32_16x16x32_bf16 %0, v[160:163], a[160:163], %1" : "=&v"(g[0][0][0]) : "v"(bs4) : ); SB();
  { unsigned lane = lane0, w = w0, rb = rb0, cb = cb0;
  asm volatile("" : "+v"(lane)); asm volatile("" : "+s"(w), "+s"(rb), "+s"(cb)); SB();

  asm volatile("v_mfma_f32_16x16x32_bf16 %0, v[160:163], a[164:167], 0" : "=&v"(g[0][1][0]) :  : ); SB();
  er = lb + 32768 + w * 6144 + l16; asm volatile("" : "+v"(er)); SB();
  asm volatile("v_mfma_f32_16x16x32_bf16 %0, v[160:163], a[168:171], 0" : "=&v"(g[0][2][0]) :  : ); SB();
  asm volatile("s_waitcnt lgkmcnt(0)\n\ts_barrier" :  :  : "memory"); SB();
  asm volatile("ds_read_b128 %0, %1 offset:0" : "=&v"(a1[0][0]) : "v"(er) : "memory"); SB();
  asm volatile("v_mfma_f32_16x16x32_bf16 %0, v[160:163], a[172:175], 0" : "=&v"(g[0][3][0]) :  : ); SB();
  asm volatile("ds_read_b128 a[96:99], %0 offset:32768" :  : "v"(aA[0]) : "memory"); SB();
  asm volatile("v_mfma_f32_16x16x32_bf16 %0, v[164:167], a[160:163], %1" : "=&v"(g[0][0][1]) : "v"(bs4) : ); SB();
  asm volatile("ds_read_b128 a[112:115], %0 offset:32768" :  : "v"(aA[1]) : "memory"); SB();
  asm volatile("v_mfma_f32_16x16x32_bf16 %0, v[164:167], a[164:167], 0" : "=&v"(g[0][1][1]) :  : ); SB();
  asm volatile("ds_read_b128 a[100:103], %0 offset:34816" :  : "v"(aA[0]) : "memory"); SB();
  asm volatile("v_mfma_f32_16x16x32_bf16 %0, v[164:167], a[168:171], 0" : "=&v"(g[0][2][1]) :  : ); SB();
  asm volatile("ds_read_b128 a[116:119], %0 offset:34816" :  : "v"(aA[1]) : "memory"); SB();
  asm volatile("v_mfma_f32_16x16x32_bf16 %0, v[164:167], a[172:175], 0" : "=&v"(g[0][3][1]) :  : ); SB();
  asm volatile("ds_read_b128 a[104:107], %0 offset:36864" :  : "v"(aA[0]) : "memory"); SB();
  ST(25);
  asm volatile("s_waitcnt vmcnt(21)" :  :  : "memory"); SB();
  asm volatile("v_mfma_f32_16x16x32_bf16 %0, v[168:171], a[176:179], %0" : "+v"(g[0][0][0]) :  : ); SB();
  asm volatile("ds_read_b128 a[120:123], %0 offset:36864" :  : "v"(aA[1]) : "memory"); SB();
  asm volatile("v_mfma_f32_16x16x32_bf16 %0, v[168:171], a[180:183], %0" : "+v"(g[0][1][0]) :  : ); SB();
  asm volatile("buffer_load_dwordx4 v[160:163], %0, %1, %2 offen offset:0 sc1" :  : "v"(vB), "s"(rWS), "s"(0u) : "memory"); SB();
  asm volatile("v_mfma_f32_16x16x32_bf16 %0, v[168:171], a[184:187], %0" : "+v"(g[0][2][0]) :  : ); SB();
  asm volatile("buffer_load_dwordx4 v[164:167], %0, %1, %2 offen offset:1024 sc1" :  : "v"(vB), "s"(rWS), "s"(0u) : "memory"); SB();
  asm volatile("v_mfma_f32_16x16x32_bf16 %0, v[168:171], a[188:191], %0" : "+v"(g[0][3][0]) :  : ); SB();
  asm volatile("ds_read_b128 %0, %1 offset:1024" : "=&v"(a1[0][1]) : "v"(er) : "memory"); SB();
  asm volatile("v_mfma_f32_16x16x32_bf16 %0, v[172:175], a[176:179], %0" : "+v"(g[0][0][1]) :  : ); SB();
  asm volatile("ds_read_b128 %0, %1 offset:2048" : "=&v"(a1[0][2]) : "v"(er) : "memory"); SB();
  asm volatile("v_mfma_f32_16x16x32_bf16 %0, v[172:175], a[180:183], %0" : "+v"(g[0][1][1]) :  : ); SB();
  asm volatile("buffer_load_dwordx4 %0, %1, %2 offen offset:3072 lds" :  : "v"(voS), "s"(rA), "s"(ao[7]) : "memory"); SB();
  asm volatile("v_mfma_f32_16x16x32_bf16 %0, v[172:175], a[184:187], %0" : "+v"(g[0][2][1]) :  : ); SB();
  asm volatile("v_xor_b32 %0, 128, %0" : "+v"(voS) :  : "memory"); SB();
  asm volatile("v_mfma_f32_16x16x32_bf16 %0, v[172:175], a[188:191], %0" : "+v"(g[0][3][1]) :  : ); SB();
  asm volatile("ds_read_b128 %0, %1 offset:3072" : "=&v"(a1[1][0]) : "v"(er) : "memory"); SB();
  asm volatile("s_waitcnt vmcnt(22)" :  :  : "memory"); SB();
  asm volatile("v_mfma_f32_16x16x32_bf16 %0, v[176:179], a[192:195], %0" : "+v"(g[0][0][0]) :  : ); SB();
  asm volatile("ds_read_b128 %0, %1 offset:4096" : "=&v"(a1[1][1]) : "v"(er) : "memory"); SB();
  asm volatile("v_mfma_f32_16x16x32_bf16 %0, v[176:179], a[196:199], %0" : "+v"(g[0][1][0]) :  : ); SB();
  asm volatile("buffer_load_dwordx4 v[168:171], %0, %1, %2 offen offset:0 sc1" :  : "v"(vB), "s"(rWS), "s"(65536u) : "memory"); SB();
  asm volatile("v_mfma_f32_16x16x32_bf16 %0, v[176:179], a[200:203], %0" : "+v"(g[0][2][0]) :  : ); SB();
  asm volatile("ds_read_b128 %0, %1 offset:5120" : "=&v"(a1[1][2]) : "v"(er) : "memory"); SB();
  asm volatile("v_mfma_f32_16x16x32_bf16 %0, v[176:179], a[204:207], %0" : "+v"(g[0][3][0]) :  : ); SB();
  ydr = lb + 0 + (w * 2 + (lane >> 5)) * 1024 + (lane & 1) * 512; asm volatile("" : "+v"(ydr)); SB();
  asm volatile("v_mfma_f32_16x16x32_bf16 %0, v[180:183], a[192:195], %0" : "+v"(g[0][0][1]) :  : ); SB();
  ydr = ydr + ((((lane >> 1) & 15) ^ ((lane & 1) << 3)) << 4); asm volatile("" : "+v"(ydr)); SB();
  asm volatile("v_mfma_f32_16x16x32_bf16 %0, v[180:183], a[196:199], %0" : "+v"(g[0][1][1]) :  : ); SB();
  asm volatile("buffer_load_dwordx4 v[172:175], %0, %1, %2 offen offset:1024 sc1" :  : "v"(vB), "s"(rWS), "s"(65536u) : "memory"); SB();
  asm volatile("v_mfma_f32_16x16x32_bf16 %0, v[180:183], a[200:203], %0" : "+v"(g[0][2][1]) :  : ); SB();
  ykr = lb + 59520 + 512 + w * 1024 + l16; asm volatile("" : "+v"(ykr)); SB();
  asm volatile("v_mfma_f32_16x16x32_bf16 %0, v[180:183], a[204:207], %0" : "+v"(g[0][3][1]) :  : ); SB();
  pw2 = er + (122880 - 32768); asm volatile("" : "+v"(pw2)); SB();
  asm volatile("s_waitcnt vmcnt(22)" :  :  : "memory"); SB();
  asm volatile("v_mfma_f32_16x16x32_bf16 %0, v[184:187], a[208:211], %0" : "+v"(g[0][0][0]) :  : ); SB();
  asm volatile("v_xor_b32 %0, 917504, %0" : "+v"(vB) :  : "memory"); SB();
  asm volatile("v_mfma_f32_16x16x32_bf16 %0, v[184:187], a[212:215], %0" : "+v"(g[0][1][0]) :  : ); SB();
  asm volatile("buffer_load_dwordx4 v[176:179], %0, %1, %2 offen offset:0 sc1" :  : "v"(vB), "s"(rWS), "s"(0u) : "memory"); SB();
  asm volatile("v_mfma_f32_16x16x32_bf16 %0, v[184:187], a[216:219], %0" : "+v"(g[0][2][0]) :  : ); SB();
  pe[0] = lb + 122880 + (w ^ 1) * 6144 + l16; asm volatile("" : "+v"(pe[0])); SB();
  asm volatile("v_mfma_f32_16x16x32_bf16 %0, v[184:187], a[220:223], %0" : "+v"(g[0][3][0]) :  : ); SB();
  pe[1] = lb + 122880 + (w ^ 2) * 6144 + l16; asm volatile("" : "+v"(pe[1])); SB();
  asm volatile("v_mfma_f32_16x16x32_bf16 %0, v[188:191], a[208:211], %0" : "+v"(g[0][0][1]) :  : ); SB();
  asm volatile("buffer_load_dwordx4 v[180:183], %0, %1, %2 offen offset:1024 sc1" :  : "v"(vB), "s"(rWS), "s"(0u) : "memory"); SB();
  asm volatile("v_mfma_f32_16x16x32_bf16 %0, v[188:191], a[212:215], %0" : "+v"(g[0][1][1]) :  : ); SB();
  pe[2] = lb + 122880 + (w ^ 3) * 6144 + l16; asm volatile("" : "+v"(pe[2])); SB();
  asm volatile("v_mfma_f32_16x16x32_bf16 %0, v[188:191], a[216:219], %0" : "+v"(g[0][2][1]) :  : ); SB();
  asm volatile("s_waitcnt lgkmcnt(0)" : "+v"(a1[0][0]), "+v"(a1[0][1]), "+v"(a1[0][2]), "+v"(a1[1][0]), "+v"(a1[1][1]), "+v"(a1[1][2]) :  : "memory"); SB();
  asm volatile("v_mul_f32 %0, %1, %1" : "=v"(gt[0]) : "v"(a1[0][0][0]) : ); SB();
  asm volatile("v_mfma_f32_16x16x32_bf16 %0, v[188:191], a[220:223], %0" : "+v"(g[0][3][1]) :  : ); SB();
  dl = (int)(wm * 12928 - wn * 256 + w * 128) - 2 * 12928; asm volatile("" : "+s"(dl)); SB();
  asm volatile("s_waitcnt vmcnt(22)" :  :  : "memory"); SB();
  asm volatile("v_mfma_f32_16x16x32_bf16 %0, v[192:195], a[224:227], %0" : "+v"(g[0][0][0]) :  : ); SB();
  asm volatile("buffer_load_dwordx4 v[184:187], %0, %1, %2 offen offset:0 sc1" :  : "v"(vB), "s"(rWS), "s"(65536u) : "memory"); SB();
  asm volatile("v_add_u32 %0, %1, %0" : "+v"(aa[0]) : "s"(dl) : ); SB();
  asm volatile("v_mfma_f32_16x16x32_bf16 %0, v[192:195], a[228:231], %0" : "+v"(g[0][1][0]) :  : ); SB();
  asm volatile("v_add_u32 %0, %1, %0" : "+v"(aa[1]) : "s"(dl) : ); SB();
  asm volatile("v_mfma_f32_16x16x32_bf16 %0, v[192:195], a[232:235], %0" : "+v"(g[0][2][0]) :  : ); SB();
  asm volatile("buffer_load_dwordx4 v[188:191], %0, %1, %2 offen offset:1024 sc1" :  : "v"(vB), "s"(rWS), "s"(65536u) : "memory"); SB();
  asm volatile("v_add_u32 %0, %1, %0" : "+v"(aa[2]) : "s"(dl) : ); SB();
  asm volatile("v_mfma_f32_16x16x32_bf16 %0, v[192:195], a[236:239], %0" : "+v"(g[0][3][0]) :  : ); SB();
  asm volatile("v_xor_b32 %0, 131072, %0" : "+v"(vB) :  : "memory"); SB();
  asm volatile("v_add_u32 %0, %1, %0" : "+v"(aa[3]) : "s"(dl) : ); SB();
  asm volatile("v_mfma_f32_16x16x32_bf16 %0, v[196:199], a[224:227], %0" : "+v"(g[0][0][1]) :  : ); SB();
  asm volatile("v_add_u32 %0, %1, %0" : "+v"(bb) : "s"(dl) : ); SB();
  asm volatile("v_mfma_f32_16x16x32_bf16 %0, v[196:199], a[228:231], %0" : "+v"(g[0][1][1]) :  : ); SB();
  asm volatile("v_mul_f32 %0, %1, %1" : "=v"(gt[1]) : "v"(a1[0][0][1]) : ); SB();
  asm volatile("v_mfma_f32_16x16x32_bf16 %0, v[196:199], a[232:235], %0" : "+v"(g[0][2][1]) :  : ); SB();
  asm volatile("s_add_u32 m0, %0, 98304" :  : "s"(mA) : "memory", "scc"); SB();
  asm volatile("v_mul_f32 %0, %1, %1" : "=v"(gt[2]) : "v"(a1[0][0][2]) : ); SB();
  asm volatile("v_mfma_f32_16x16x32_bf16 %0, v[196:199], a[236:239], %0" : "+v"(g[0][3][1]) :  : ); SB();
  asm volatile("buffer_load_dwordx4 %0, %1, %2 offen lds" :  : "v"(voS), "s"(rA), "s"(ao[0]) : "memory"); SB();
  asm volatile("s_waitcnt vmcnt(23)" :  :  : "memory"); SB();
  asm volatile("v_mfma_f32_16x16x32_bf16 %0, v[200:203], a[240:243], %0" : "+v"(g[0][0][0]) :  : ); SB();
  asm volatile("v_mul_f32 %0, %1, %1" : "=v"(gt[3]) : "v"(a1[0][0][3]) : ); SB();
  asm volatile("v_mfma_f32_16x16x32_bf16 %0, v[200:203], a[244:247], %0" : "+v"(g[0][1][0]) :  : ); SB();
  asm volatile("buffer_load_dwordx4 v[192:195], %0, %1, %2 offen offset:0 sc1" :  : "v"(vB), "s"(rWS), "s"(0u) : "memory"); SB();
  asm volatile("v_fma_f32 %0, %0, %1, %2" : "+v"(gt[0]) : "v"(k2[0]), "v"(k1[0]) : ); SB();
  asm volatile("v_mfma_f32_16x16x32_bf16 %0, v[200:203], a[248:251], %0" : "+v"(g[0][2][0]) :  : ); SB();
  asm volatile("v_fma_f32 %0, %0, %1, %2" : "+v"(gt[1]) : "v"(k2[0]), "v"(k1[0]) : ); SB();
  asm volatile("v_mfma_f32_16x16x32_bf16 %0, v[200:203], a[252:255], %0" : "+v"(g[0][3][0]) :  : ); SB();
  asm volatile("v_fma_f32 %0, %0, %1, %2" : "+v"(gt[2]) : "v"(k2[0]), "v"(k1[0]) : ); SB();
  asm volatile("v_mfma_f32_16x16x32_bf16 %0, v[204:207], a[240:243], %0" : "+v"(g[0][0][1]) :  : ); SB();
  asm volatile("v_fma_f32 %0, %0, %1, %2" : "+v"(gt[3]) : "v"(k2[0]), "v"(k1[0]) : ); SB();
  asm volatile("v_mfma_f32_16x16x32_bf16 %0, v[204:207], a[244:247], %0" : "+v"(g[0][1][1]) :  : ); SB();
  asm volatile("buffer_load_dwordx4 v[196:199], %0, %1, %2 offen offset:1024 sc1" :  : "v"(vB), "s"(rWS), "s"(0u) : "memory"); SB();
  asm volatile("v_mul_f32 %0, %0, %1" : "+v"(gt[0]) : "v"(a1[0][0][0]) : ); SB();
  asm volatile("v_mfma_f32_16x16x32_bf16 %0, v[204:207], a[248:251], %0" : "+v"(g[0][2][1]) :  : ); SB();
  asm volatile("v_mul_f32 %0, %0, %1" : "+v"(gt[1]) : "v"(a1[0][0][1]) : ); SB();
  asm volatile("v_mfma_f32_16x16x32_bf16 %0, v[204:207], a[252:255], %0" : "+v"(g[0][3][1]) :  : ); SB();
  asm volatile("v_mul_f32 %0, %0, %1" : "+v"(gt[2]) : "v"(a1[0][0][2]) : ); SB();
  asm volatile("s_waitcnt vmcnt(23)" :  :  : "memory"); SB();
  asm volatile("v_mfma_f32_16x16x32_bf16 %0, v[208:211], a[160:163], %1" : "=&v"(g[1][0][0]) : "v"(bs4) : ); SB();
  asm volatile("buffer_load_dwordx4 v[200:203], %0, %1, %2 offen offset:0 sc1" :  : "v"(vB), "s"(rWS), "s"(65536u) : "memory"); SB();
  asm volatile("v_mul_f32 %0, %0, %1" : "+v"(gt[3]) : "v"(a1[0][0][3]) : ); SB();
  asm volatile("v_mfma_f32_16x16x32_bf16 %0, v[208:211], a[164:167], 0" : "=&v"(g[1][1][0]) :  : ); SB();
  asm volatile("v_exp_f32 %0, %1" : "=v"(ge[0]) : "v"(gt[0]) : ); SB();
  asm volatile("v_mfma_f32_16x16x32_bf16 %0, v[208:211], a[168:171], 0" : "=&v"(g[1][2][0]) :  : ); SB();
  asm volatile("v_exp_f32 %0, %1" : "=v"(ge[1]) : "v"(gt[1]) : ); SB();
  asm volatile("v_mfma_f32_16x16x32_bf16 %0, v[208:211], a[172:175], 0" : "=&v"(g[1][3][0]) :  : ); SB();
  asm volatile("v_exp_f32 %0, %1" : "=v"(ge[2]) : "v"(gt[2]) : ); SB();
  asm volatile("v_mfma_f32_16x16x32_bf16 %0, v[212:215], a[160:163], %1" : "=&v"(g[1][0][1]) : "v"(bs4) : ); SB();
  asm volatile("buffer_load_dwordx4 v[204:207], %0, %1, %2 offen offset:1024 sc1" :  : "v"(vB), "s"(rWS), "s"(65536u) : "memory"); SB();
  asm volatile("v_exp_f32 %0, %1" : "=v"(ge[3]) : "v"(gt[3]) : ); SB();
  asm volatile("v_mfma_f32_16x16x32_bf16 %0, v[212:215], a[164:167], 0" : "=&v"(g[1][1][1]) :  : ); SB();
  asm volatile("v_xor_b32 %0, 393216, %0" : "+v"(vB) :  : "memory"); SB();
  asm volatile("v_fma_f32 %0, %0, %1, %1" : "+v"(ge[0]) : "v"(xd[0]) : ); SB();
  asm volatile("v_mfma_f32_16x16x32_bf16 %0, v[212:215], a[168:171], 0" : "=&v"(g[1][2][1]) :  : ); SB();
  asm volatile("v_fma_f32 %0, %0, %1, %1" : "+v"(ge[1]) : "v"(xd[0]) : ); SB();
  asm volatile("v_mfma_f32_16x16x32_bf16 %0, v[212:215], a[172:175], 0" : "=&v"(g[1][3][1]) :  : ); SB();
  asm volatile("v_fma_f32 %0, %0, %1, %1" : "+v"(ge[2]) : "v"(xd[0]) : ); SB();
  asm volatile("s_waitcnt vmcnt(23)" :  :  : "memory"); SB();
  asm volatile("v_mfma_f32_16x16x32_bf16 %0, v[216:219], a[176:179], %0" : "+v"(g[1][0][0]) :  : ); SB();
  asm volatile("v_fma_f32 %0, %0, %1, %1" : "+v"(ge[3]) : "v"(xd[0]) : ); SB();
  asm volatile("v_mfma_f32_16x16x32_bf16 %0, v[216:219], a[180:183], %0" : "+v"(g[1][1][0]) :  : ); SB();
  asm volatile("v_rcp_f32 %0, %1" : "=v"(gt[0]) : "v"(ge[0]) : ); SB();
  asm volatile("v_mfma_f32_16x16x32_bf16 %0, v[216:219], a[184:187], %0" : "+v"(g[1][2][0]) :  : ); SB();
  asm volatile("v_rcp_f32 %0, %1" : "=v"(gt[1]) : "v"(ge[1]) : ); SB();
  asm volatile("v_mfma_f32_16x16x32_bf16 %0, v[216:219], a[188:191], %0" : "+v"(g[1][3][0]) :  : ); SB();
  asm volatile("buffer_load_dwordx4 v[208:211], %0, %1, %2 offen offset:0 sc1" :  : "v"(vB), "s"(rWS), "s"(0u) : "memory"); SB();
  asm volatile("v_rcp_f32 %0, %1" : "=v"(gt[2]) : "v"(ge[2]) : ); SB();
  asm volatile("v_mfma_f32_16x16x32_bf16 %0, v[220:223], a[176:179], %0" : "+v"(g[1][0][1]) :  : ); SB();
  asm volatile("v_rcp_f32 %0, %1" : "=v"(gt[3]) : "v"(ge[3]) : ); SB();
  asm volatile("v_mfma_f32_16x16x32_bf16 %0, v[220:223], a[180:183], %0" : "+v"(g[1][1][1]) :  : ); SB();
  asm volatile("v_mul_f32 %0, %0, %1" : "+v"(a1[0][0][0]) : "v"(gt[0]) : ); SB();
  asm volatile("v_mfma_f32_16x16x32_bf16 %0, v[220:223], a[184:187], %0" : "+v"(g[1][2][1]) :  : ); SB();
  asm volatile("buffer_load_dwordx4 v[212:215], %0, %1, %2 offen offset:1024 sc1" :  : "v"(vB), "s"(rWS), "s"(0u) : "memory"); SB();
  asm volatile("v_mul_f32 %0, %0, %1" : "+v"(a1[0][0][1]) : "v"(gt[1]) : ); SB();
  asm volatile("v_mfma_f32_16x16x32_bf16 %0, v[220:223], a[188:191], %0" : "+v"(g[1][3][1]) :  : ); SB();
  asm volatile("v_mul_f32 %0, %0, %1" : "+v"(a1[0][0][2]) : "v"(gt[2]) : ); SB();
  ST(26);
  asm volatile("s_waitcnt vmcnt(23)" :  :  : "memory"); SB();
  asm volatile("v_mfma_f32_16x16x32_bf16 %0, v[224:227], a[192:195], %0" : "+v"(g[1][0][0]) :  : ); SB();
  vH_ = vH; asm volatile("" : "+v"(vH_)); SB();
  asm volatile("v_mfma_f32_16x16x32_bf16 %0, v[224:227], a[196:199], %0" : "+v"(g[1][1][0]) :  : ); SB();
  vO = W_WO + (cb * 120 + w * 30 + 0u * 15) * 1024 + l16; asm volatile("" : "+v"(vO)); SB();
  asm volatile("v_mfma_f32_16x16x32_bf16 %0, v[224:227], a[200:203], %0" : "+v"(g[1][2][0]) :  : ); SB();
  vO2 = vO + 15360 - 0u * 30720; asm volatile("" : "+v"(vO2)); SB();
  asm volatile("v_mfma_f32_16x16x32_bf16 %0, v[224:227], a[204:207], %0" : "+v"(g[1][3][0]) :  : ); SB();
  vFH = W_FH + rb * 256 + w * 64 + (lane & 7) * 8; asm volatile("" : "+v"(vFH)); SB();
  asm volatile("v_mfma_f32_16x16x32_bf16 %0, v[228:231], a[192:195], %0" : "+v"(g[1][0][1]) :  : ); SB();
  vHr = W_HH + (rb * 96 + w * 6) * 1024 + l16; asm volatile("" : "+v"(vHr)); SB();
  asm volatile("v_mfma_f32_16x16x32_bf16 %0, v[228:231], a[196:199], %0" : "+v"(g[1][1][1]) :  : ); SB();
  vX = (rb * 64 + w * 16 + l15) * 1280; asm volatile("" : "+v"(vX)); SB();
  asm volatile("v_mfma_f32_16x16x32_bf16 %0, v[228:231], a[200:203], %0" : "+v"(g[1][2][1]) :  : ); SB();
  vX = vX + cb * 160 + 8 * g4; asm volatile("" : "+v"(vX)); SB();
  asm volatile("v_mfma_f32_16x16x32_bf16 %0, v[228:231], a[204:207], %0" : "+v"(g[1][3][1]) :  : ); SB();
  sFH = W_FH + ((rb * 8 + cb) * 4 + w) * 8; asm volatile("" : "+s"(sFH)); SB();
  asm volatile("s_waitcnt vmcnt(21)" :  :  : "memory"); SB();
  asm volatile("v_mfma_f32_16x16x32_bf16 %0, v[232:235], a[208:211], %0" : "+v"(g[1][0][0]) :  : ); SB();
  hso[0] = (w ^ 0) * 24576 + 0u * 3072; asm volatile("" : "+s"(hso[0])); SB();
  hso[1] = hso[0] ^ 3072; asm volatile("" : "+s"(hso[1])); SB();
  asm volatile("v_mfma_f32_16x16x32_bf16 %0, v[232:235], a[212:215], %0" : "+v"(g[1][1][0]) :  : ); SB();
  hso[2] = (w ^ 1) * 24576 + 0u * 3072; asm volatile("" : "+s"(hso[2])); SB();
  hso[3] = hso[2] ^ 3072; asm volatile("" : "+s"(hso[3])); SB();
  asm volatile("v_mfma_f32_16x16x32_bf16 %0, v[232:235], a[216:219], %0" : "+v"(g[1][2][0]) :  : ); SB();
  hso[4] = (w ^ 2) * 24576 + 0u * 3072; asm volatile("" : "+s"(hso[4])); SB();
  hso[5] = hso[4] ^ 3072; asm volatile("" : "+s"(hso[5])); SB();
  asm volatile("v_mfma_f32_16x16x32_bf16 %0, v[232:235], a[220:223], %0" : "+v"(g[1][3][0]) :  : ); SB();
  hso[6] = (w ^ 3) * 24576 + 0u * 3072; asm volatile("" : "+s"(hso[6])); SB();
  hso[7] = hso[6] ^ 3072; asm volatile("" : "+s"(hso[7])); SB();
  asm volatile("v_mfma_f32_16x16x32_bf16 %0, v[236:239], a[208:211], %0" : "+v"(g[1][0][1]) :  : ); SB();
  asm volatile("s_waitcnt vmcnt(13)" :  :  : "memory"); SB();
  asm volatile("ds_read_b128 a[108:111], %0 offset:38912" :  : "v"(aA[0]) : "memory"); SB();
  asm volatile("v_mfma_f32_16x16x32_bf16 %0, v[236:239], a[212:215], %0" : "+v"(g[1][1][1]) :  : ); SB();
  asm volatile("ds_read_b128 a[124:127], %0 offset:38912" :  : "v"(aA[1]) : "memory"); SB();
  asm volatile("v_mul_f32 %0, %0, %1" : "+v"(a1[0][0][3]) : "v"(gt[3]) : ); SB();
  asm volatile("v_mfma_f32_16x16x32_bf16 %0, v[236:239], a[216:219], %0" : "+v"(g[1][2][1]) :  : ); SB();
  asm volatile("buffer_load_dwordx4 %0, %1, %2 offen offset:1024 lds" :  : "v"(voS), "s"(rA), "s"(ao[1]) : "memory"); SB();
  asm volatile("v_mul_f32 %0, %1, %1" : "=v"(gt[0]) : "v"(a1[0][1][0]) : ); SB();
  asm volatile("v_mfma_f32_16x16x32_bf16 %0, v[236:239], a[220:223], %0" : "+v"(g[1][3][1]) :  : ); SB();
  asm volatile("buffer_load_dwordx4 %0, %1, %2 offen offset:2048 lds" :  : "v"(voS), "s"(rA), "s"(ao[2]) : "memory"); SB();
  asm volatile("v_mfma_f32_16x16x32_bf16 %0, v[240:243], a[224:227], %0" : "+v"(g[1][0][0]) :  : ); SB();
  asm volatile("v_mul_f32 %0, %1, %1" : "=v"(gt[1]) : "v"(a1[0][1][1]) : ); SB();
  asm volatile("v_mfma_f32_16x16x32_bf16 %0, v[240:243], a[228:231], %0" : "+v"(g[1][1][0]) :  : ); SB();
  asm volatile("v_mul_f32 %0, %1, %1" : "=v"(gt[2]) : "v"(a1[0][1][2]) : ); SB();
  asm volatile("v_mfma_f32_16x16x32_bf16 %0, v[240:243], a[232:235], %0" : "+v"(g[1][2][0]) :  : ); SB();
  asm volatile("buffer_load_dwordx4 %0, %1, %2 offen offset:3072 lds" :  : "v"(voS), "s"(rA), "s"(ao[3]) : "memory"); SB();
  asm volatile("v_mul_f32 %0, %1, %1" : "=v"(gt[3]) : "v"(a1[0][1][3]) : ); SB();
  asm volatile("v_mfma_f32_16x16x32_bf16 %0, v[240:243], a[236:239], %0" : "+v"(g[1][3][0]) :  : ); SB();
  asm volatile("v_fma_f32 %0, %0, %1, %2" : "+v"(gt[0]) : "v"(k2[0]), "v"(k1[0]) : ); SB();
  asm volatile("v_mfma_f32_16x16x32_bf16 %0, v[244:247], a[224:227], %0" : "+v"(g[1][0][1]) :  : ); SB();
  asm volatile("v_fma_f32 %0, %0, %1, %2" : "+v"(gt[1]) : "v"(k2[0]), "v"(k1[0]) : ); SB();
  asm volatile("v_mfma_f32_16x16x32_bf16 %0, v[244:247], a[228:231], %0" : "+v"(g[1][1][1]) :  : ); SB();
  asm volatile("v_fma_f32 %0, %0, %1, %2" : "+v"(gt[2]) : "v"(k2[0]), "v"(k1[0]) : ); SB();
  asm volatile("v_mfma_f32_16x16x32_bf16 %0, v[244:247], a[232:235], %0" : "+v"(g[1][2][1]) :  : ); SB();
  asm volatile("s_add_u32 m0, %0, 102400" :  : "s"(mA) : "memory", "scc"); SB();
  asm volatile("v_fma_f32 %0, %0, %1, %2" : "+v"(gt[3]) : "v"(k2[0]), "v"(k1[0]) : ); SB();
  asm volatile("v_mfma_f32_16x16x32_bf16 %0, v[244:247], a[236:239], %0" : "+v"(g[1][3][1]) :  : ); SB();
  asm volatile("buffer_load_dwordx4 %0, %1, %2 offen lds" :  : "v"(voS), "s"(rA), "s"(ao[4]) : "memory"); SB();
  asm volatile("v_mfma_f32_16x16x32_bf16 %0, v[248:251], a[240:243], %0" : "+v"(g[1][0][0]) :  : ); SB();
  asm volatile("v_mul_f32 %0, %0, %1" : "+v"(gt[0]) : "v"(a1[0][1][0]) : ); SB();
  asm volatile("v_mfma_f32_16x16x32_bf16 %0, v[248:251], a[244:247], %0" : "+v"(g[1][1][0]) :  : ); SB();
  asm volatile("buffer_load_dwordx4 %0, %1, %2 offen offset:1024 lds" :  : "v"(voS), "s"(rA), "s"(ao[5]) : "memory"); SB();
  asm volatile("v_mul_f32 %0, %0, %1" : "+v"(gt[1]) : "v"(a1[0][1][1]) : ); SB();
  asm volatile("v_mfma_f32_16x16x32_bf16 %0, v[248:251], a[248:251], %0" : "+v"(g[1][2][0]) :  : ); SB();
  asm volatile("v_mul_f32 %0, %0, %1" : "+v"(gt[2]) : "v"(a1[0][1][2]) : ); SB();
  asm volatile("v_mfma_f32_16x16x32_bf16 %0, v[248:251], a[252:255], %0" : "+v"(g[1][3][0]) :  : ); SB();
  asm volatile("v_mul_f32 %0, %0, %1" : "+v"(gt[3]) : "v"(a1[0][1][3]) : ); SB();
  asm volatile("v_mfma_f32_16x16x32_bf16 %0, v[252:255], a[240:243], %0" : "+v"(g[1][0][1]) :  : ); SB();
  asm volatile("v_exp_f32 %0, %1" : "=v"(ge[0]) : "v"(gt[0]) : ); SB();
  asm volatile("v_mfma_f32_16x16x32_bf16 %0, v[252:255], a[244:247], %0" : "+v"(g[1][1][1]) :  : ); SB();
  asm volatile("buffer_load_dwordx4 %0, %1, %2 offen offset:2048 lds" :  : "v"(voS), "s"(rA), "s"(ao[6]) : "memory"); SB();
  asm volatile("v_exp_f32 %0, %1" : "=v"(ge[1]) : "v"(gt[1]) : ); SB();
  asm volatile("v_mfma_f32_16x16x32_bf16 %0, v[252:255], a[248:251], %0" : "+v"(g[1][2][1]) :  : ); SB();
  asm volatile("v_exp_f32 %0, %1" : "=v"(ge[2]) : "v"(gt[2]) : ); SB();
  asm volatile("v_mfma_f32_16x16x32_bf16 %0, v[252:255], a[252:255], %0" : "+v"(g[1][3][1]) :  : ); SB();
  asm volatile("v_exp_f32 %0, %1" : "=v"(ge[3]) : "v"(gt[3]) : ); SB();
  asm volatile("v_mfma_f32_16x16x32_bf16 %0, v[160:163], a[0:3], %0" : "+v"(g[0][0][0]) :  : ); SB();
  asm volatile("buffer_load_dwordx4 %0, %1, %2 offen offset:3072 lds" :  : "v"(voS), "s"(rA), "s"(ao[7]) : "memory"); SB();
  asm volatile("v_fma_f32 %0, %0, %1, %1" : "+v"(ge[0]) : "v"(xd[0]) : ); SB();
  asm volatile("v_mfma_f32_16x16x32_bf16 %0, v[160:163], a[4:7], %0" : "+v"(g[0][1][0]) :  : ); SB();
  asm volatile("v_fma_f32 %0, %0, %1, %1" : "+v"(ge[1]) : "v"(xd[0]) : ); SB();
  asm volatile("v_mfma_f32_16x16x32_bf16 %0, v[160:163], a[8:11], %0" : "+v"(g[0][2][0]) :  : ); SB();
  asm volatile("v_fma_f32 %0, %0, %1, %1" : "+v"(ge[2]) : "v"(xd[0]) : ); SB();
  asm volatile("v_mfma_f32_16x16x32_bf16 %0, v[160:163], a[12:15], %0" : "+v"(g[0][3][0]) :  : ); SB();
  asm volatile("buffer_load_dwordx4 v[216:219], %0, %1, %2 offen offset:0 sc1" :  : "v"(vB), "s"(rWS), "s"(65536u) : "memory"); SB();
  asm volatile("v_fma_f32 %0, %0, %1, %1" : "+v"(ge[3]) : "v"(xd[0]) : ); SB();
  asm volatile("v_mfma_f32_16x16x32_bf16 %0, v[164:167], a[0:3], %0" : "+v"(g[0][0][1]) :  : ); SB();
  asm volatile("v_rcp_f32 %0, %1" : "=v"(gt[0]) : "v"(ge[0]) : ); SB();
  asm volatile("v_mfma_f32_16x16x32_bf16 %0, v[164:167], a[4:7], %0" : "+v"(g[0][1][1]) :  : ); SB();
  asm volatile("v_rcp_f32 %0, %1" : "=v"(gt[1]) : "v"(ge[1]) : ); SB();
  asm volatile("v_mfma_f32_16x16x32_bf16 %0, v[164:167], a[8:11], %0" : "+v"(g[0][2][1]) :  : ); SB();
  asm volatile("v_rcp_f32 %0, %1" : "=v"(gt[2]) : "v"(ge[2]) : ); SB();
  asm volatile("v_mfma_f32_16x16x32_bf16 %0, v[164:167], a[12:15], %0" : "+v"(g[0][3][1]) :  : ); SB();
  asm volatile("buffer_load_dwordx4 v[220:223], %0, %1, %2 offen offset:1024 sc1" :  : "v"(vB), "s"(rWS), "s"(65536u) : "memory"); SB();
  asm volatile("s_waitcnt vmcnt(20)" :  :  : "memory"); SB();
  asm volatile("v_mfma_f32_16x16x32_bf16 %0, v[168:171], a[16:19], %0" : "+v"(g[0][0][0]) :  : ); SB();
  asm volatile("v_xor_b32 %0, 131072, %0" : "+v"(vB) :  : "memory"); SB();
  asm volatile("v_rcp_f32 %0, %1" : "=v"(gt[3]) : "v"(ge[3]) : ); SB();
  asm volatile("v_mfma_f32_16x16x32_bf16 %0, v[168:171], a[20:23], %0" : "+v"(g[0][1][0]) :  : ); SB();
  asm volatile("v_mul_f32 %0, %0, %1" : "+v"(a1[0][1][0]) : "v"(gt[0]) : ); SB();
  asm volatile("v_mfma_f32_16x16x32_bf16 %0, v[168:171], a[24:27], %0" : "+v"(g[0][2][0]) :  : ); SB();
  asm volatile("buffer_load_dwordx4 v[224:227], %0, %1, %2 offen offset:0 sc1" :  : "v"(vB), "s"(rWS), "s"(0u) : "memory"); SB();
  asm volatile("v_mul_f32 %0, %0, %1" : "+v"(a1[0][1][1]) : "v"(gt[1]) : ); SB();
  asm volatile("v_mfma_f32_16x16x32_bf16 %0, v[168:171], a[28:31], %0" : "+v"(g[0][3][0]) :  : ); SB();
  asm volatile("v_mul_f32 %0, %0, %1" : "+v"(a1[0][1][2]) : "v"(gt[2]) : ); SB();
  asm volatile("v_mfma_f32_16x16x32_bf16 %0, v[172:175], a[16:19], %0" : "+v"(g[0][0][1]) :  : ); SB();
  asm volatile("v_mul_f32 %0, %0, %1" : "+v"(a1[0][1][3]) : "v"(gt[3]) : ); SB();
  asm volatile("v_mfma_f32_16x16x32_bf16 %0, v[172:175], a[20:23], %0" : "+v"(g[0][1][1]) :  : ); SB();
  asm volatile("v_mul_f32 %0, %1, %1" : "=v"(gt[0]) : "v"(a1[0][2][0]) : ); SB();
  asm volatile("v_mfma_f32_16x16x32_bf16 %0, v[172:175], a[24:27], %0" : "+v"(g[0][2][1]) :  : ); SB();
  asm volatile("buffer_load_dwordx4 v[228:231], %0, %1, %2 offen offset:1024 sc1" :  : "v"(vB), "s"(rWS), "s"(0u) : "memory"); SB();
  asm volatile("v_mul_f32 %0, %1, %1" : "=v"(gt[1]) : "v"(a1[0][2][1]) : ); SB();
  asm volatile("v_mfma_f32_16x16x32_bf16 %0, v[172:175], a[28:31], %0" : "+v"(g[0][3][1]) :  : ); SB();
  asm volatile("v_mul_f32 %0, %1, %1" : "=v"(gt[2]) : "v"(a1[0][2][2]) : ); SB();
  asm volatile("s_waitcnt vmcnt(20)" :  :  : "memory"); SB();
  asm volatile("v_mfma_f32_16x16x32_bf16 %0, v[176:179], a[32:35], %0" : "+v"(g[0][0][0]) :  : ); SB();
  asm volatile("v_mul_f32 %0, %1, %1" : "=v"(gt[3]) : "v"(a1[0][2][3]) : ); SB();
  asm volatile("v_mfma_f32_16x16x32_bf16 %0, v[176:179], a[36:39], %0" : "+v"(g[0][1][0]) :  : ); SB();
  asm volatile("buffer_load_dwordx4 v[232:235], %0, %1, %2 offen offset:0 sc1" :  : "v"(vB), "s"(rWS), "s"(65536u) : "memory"); SB();
  asm volatile("v_fma_f32 %0, %0, %1, %2" : "+v"(gt[0]) : "v"(k2[0]), "v"(k1[0]) : ); SB();
  asm volatile("v_mfma_f32_16x16x32_bf16 %0, v[176:179], a[40:43], %0" : "+v"(g[0][2][0]) :  : ); SB();
  asm volatile("v_fma_f32 %0, %0, %1, %2" : "+v"(gt[1]) : "v"(k2[0]), "v"(k1[0]) : ); SB();
  asm volatile("v_mfma_f32_16x16x32_bf16 %0, v[176:179], a[44:47], %0" : "+v"(g[0][3][0]) :  : ); SB();
  asm volatile("v_fma_f32 %0, %0, %1, %2" : "+v"(gt[2]) : "v"(k2[0]), "v"(k1[0]) : ); SB();
  asm volatile("v_mfma_f32_16x16x32_bf16 %0, v[180:183], a[32:35], %0" : "+v"(g[0][0][1]) :  : ); SB();
  asm volatile("v_fma_f32 %0, %0, %1, %2" : "+v"(gt[3]) : "v"(k2[0]), "v"(k1[0]) : ); SB();
  asm volatile("v_mfma_f32_16x16x32_bf16 %0, v[180:183], a[36:39], %0" : "+v"(g[0][1][1]) :  : ); SB();
  asm volatile("buffer_load_dwordx4 v[236:239], %0, %1, %2 offen offset:1024 sc1" :  : "v"(vB), "s"(rWS), "s"(65536u) : "memory"); SB();
  asm volatile("v_mul_f32 %0, %0, %1" : "+v"(gt[0]) : "v"(a1[0][2][0]) : ); SB();
  asm volatile("v_mfma_f32_16x16x32_bf16 %0, v[180:183], a[40:43], %0" : "+v"(g[0][2][1]) :  : ); SB();
  asm volatile("v_xor_b32 %0, 524288, %0" : "+v"(vB) :  : "memory"); SB();
  asm volatile("v_mul_f32 %0, %0, %1" : "+v"(gt[1]) : "v"(a1[0][2][1]) : ); SB();
  asm volatile("v_mfma_f32_16x16x32_bf16 %0, v[180:183], a[44:47], %0" : "+v"(g[0][3][1]) :  : ); SB();
  asm volatile("v_mul_f32 %0, %0, %1" : "+v"(gt[2]) : "v"(a1[0][2][2]) : ); SB();
  asm volatile("s_waitcnt vmcnt(20)" :  :  : "memory"); SB();
  asm volatile("v_mfma_f32_16x16x32_bf16 %0, v[184:187], a[48:51], %0" : "+v"(g[0][0][0]) :  : ); SB();
  asm volatile("buffer_load_dwordx4 v[240:243], %0, %1, %2 offen offset:0 sc1" :  : "v"(vB), "s"(rWS), "s"(2048u) : "memory"); SB();
  asm volatile("v_mul_f32 %0, %0, %1" : "+v"(gt[3]) : "v"(a1[0][2][3]) : ); SB();
  asm volatile("v_mfma_f32_16x16x32_bf16 %0, v[184:187], a[52:55], %0" : "+v"(g[0][1][0]) :  : ); SB();
  asm volatile("v_exp_f32 %0, %1" : "=v"(ge[0]) : "v"(gt[0]) : ); SB();
  asm volatile("v_mfma_f32_16x16x32_bf16 %0, v[184:187], a[56:59], %0" : "+v"(g[0][2][0]) :  : ); SB();
  asm volatile("v_exp_f32 %0, %1" : "=v"(ge[1]) : "v"(gt[1]) : ); SB();
  asm volatile("v_mfma_f32_16x16x32_bf16 %0, v[184:187], a[60:63], %0" : "+v"(g[0][3][0]) :  : ); SB();
  asm volatile("v_exp_f32 %0, %1" : "=v"(ge[2]) : "v"(gt[2]) : ); SB();
  asm volatile("v_mfma_f32_16x16x32_bf16 %0, v[188:191], a[48:51], %0" : "+v"(g[0][0][1]) :  : ); SB();
  asm volatile("v_exp_f32 %0, %1" : "=v"(ge[3]) : "v"(gt[3]) : ); SB();
  asm volatile("v_mfma_f32_16x16x32_bf16 %0, v[188:191], a[52:55], %0" : "+v"(g[0][1][1]) :  : ); SB();
  asm volatile("v_fma_f32 %0, %0, %1, %1" : "+v"(ge[0]) : "v"(xd[0]) : ); SB();
  asm volatile("v_mfma_f32_16x16x32_bf16 %0, v[188:191], a[56:59], %0" : "+v"(g[0][2][1]) :  : ); SB();
  asm volatile("v_fma_f32 %0, %0, %1, %1" : "+v"(ge[1]) : "v"(xd[0]) : ); SB();
  asm volatile("v_mfma_f32_16x16x32_bf16 %0, v[188:191], a[60:63], %0" : "+v"(g[0][3][1]) :  : ); SB();
  asm volatile("v_fma_f32 %0, %0, %1, %1" : "+v"(ge[2]) : "v"(xd[0]) : ); SB();
  ST(27);
  asm volatile("s_waitcnt vmcnt(18)" :  :  : "memory"); SB();
  asm volatile("v_mfma_f32_16x16x32_bf16 %0, v[192:195], a[64:67], %0" : "+v"(g[0][0][0]) :  : ); SB();
  asm volatile("s_waitcnt vmcnt(13)" :  :  : "memory"); SB();
  asm volatile("ds_read_b128 a[128:131], %0 offset:0" :  : "v"(aA[0]) : "memory"); SB();
  asm volatile("v_mfma_f32_16x16x32_bf16 %0, v[192:195], a[68:71], %0" : "+v"(g[0][1][0]) :  : ); SB();
  asm volatile("ds_read_b128 a[144:147], %0 offset:0" :  : "v"(aA[1]) : "memory"); SB();
  asm volatile("v_fma_f32 %0, %0, %1, %1" : "+v"(ge[3]) : "v"(xd[0]) : ); SB();
  asm volatile("v_mfma_f32_16x16x32_bf16 %0, v[192:195], a[72:75], %0" : "+v"(g[0][2][0]) :  : ); SB();
  asm volatile("buffer_load_dwordx4 v[244:247], %0, %1, %2 offen offset:1024 sc1" :  : "v"(vB), "s"(rWS), "s"(2048u) : "memory"); SB();
  asm volatile("v_rcp_f32 %0, %1" : "=v"(gt[0]) : "v"(ge[0]) : ); SB();
  asm volatile("v_mfma_f32_16x16x32_bf16 %0, v[192:195], a[76:79], %0" : "+v"(g[0][3][0]) :  : ); SB();
  asm volatile("buffer_load_dwordx4 v[248:251], %0, %1, %2 offen offset:0 sc1" :  : "v"(vB), "s"(rWS), "s"(67584u) : "memory"); SB();
  asm volatile("v_rcp_f32 %0, %1" : "=v"(gt[1]) : "v"(ge[1]) : ); SB();
  asm volatile("v_mfma_f32_16x16x32_bf16 %0, v[196:199], a[64:67], %0" : "+v"(g[0][0][1]) :  : ); SB();
  asm volatile("v_rcp_f32 %0, %1" : "=v"(gt[2]) : "v"(ge[2]) : ); SB();
  asm volatile("v_mfma_f32_16x16x32_bf16 %0, v[196:199], a[68:71], %0" : "+v"(g[0][1][1]) :  : ); SB();
  asm volatile("v_rcp_f32 %0, %1" : "=v"(gt[3]) : "v"(ge[3]) : ); SB();
  asm volatile("v_mfma_f32_16x16x32_bf16 %0, v[196:199], a[72:75], %0" : "+v"(g[0][2][1]) :  : ); SB();
  asm volatile("s_waitcnt vmcnt(13)" :  :  : "memory"); SB();
  asm volatile("ds_read_b128 a[132:135], %0 offset:2048" :  : "v"(aA[0]) : "memory"); SB();
  asm volatile("v_mfma_f32_16x16x32_bf16 %0, v[196:199], a[76:79], %0" : "+v"(g[0][3][1]) :  : ); SB();
  asm volatile("ds_read_b128 a[148:151], %0 offset:2048" :  : "v"(aA[1]) : "memory"); SB();
  asm volatile("v_mfma_f32_16x16x32_bf16 %0, v[200:203], a[80:83], %0" : "+v"(g[0][0][0]) :  : ); SB();
  asm volatile("buffer_load_dwordx4 v[252:255], %0, %1, %2 offen offset:1024 sc1" :  : "v"(vB), "s"(rWS), "s"(67584u) : "memory"); SB();
  asm volatile("v_mul_f32 %0, %0, %1" : "+v"(a1[0][2][0]) : "v"(gt[0]) : ); SB();
  asm volatile("v_mfma_f32_16x16x32_bf16 %0, v[200:203], a[84:87], %0" : "+v"(g[0][1][0]) :  : ); SB();
  asm volatile("v_xor_b32 %0, 917504, %0" : "+v"(vB) :  : "memory"); SB();
  asm volatile("v_mul_f32 %0, %0, %1" : "+v"(a1[0][2][1]) : "v"(gt[1]) : ); SB();
  asm volatile("v_mfma_f32_16x16x32_bf16 %0, v[200:203], a[88:91], %0" : "+v"(g[0][2][0]) :  : ); SB();
  asm volatile("buffer_load_dwordx4 v[160:163], %0, %1, %2 offen offset:0 sc1" :  : "v"(vB), "s"(rWS), "s"(2048u) : "memory"); SB();
  asm volatile("v_mul_f32 %0, %0, %1" : "+v"(a1[0][2][2]) : "v"(gt[2]) : ); SB();
  asm volatile("v_mfma_f32_16x16x32_bf16 %0, v[200:203], a[92:95], %0" : "+v"(g[0][3][0]) :  : ); SB();
  asm volatile("v_mul_f32 %0, %0, %1" : "+v"(a1[0][2][3]) : "v"(gt[3]) : ); SB();
  asm volatile("v_mfma_f32_16x16x32_bf16 %0, v[204:207], a[80:83], %0" : "+v"(g[0][0][1]) :  : ); SB();
  asm volatile("v_mul_f32 %0, %1, %1" : "=v"(gt[0]) : "v"(a1[1][0][0]) : ); SB();
  asm volatile("v_mfma_f32_16x16x32_bf16 %0, v[204:207], a[84:87], %0" : "+v"(g[0][1][1]) :  : ); SB();
  asm volatile("s_waitcnt vmcnt(13)" :  :  : "memory"); SB();
  asm volatile("ds_read_b128 a[136:139], %0 offset:4096" :  : "v"(aA[0]) : "memory"); SB();
  asm volatile("v_mfma_f32_16x16x32_bf16 %0, v[204:207], a[88:91], %0" : "+v"(g[0][2][1]) :  : ); SB();
  asm volatile("ds_read_b128 a[152:155], %0 offset:4096" :  : "v"(aA[1]) : "memory"); SB();
  asm volatile("v_mul_f32 %0, %1, %1" : "=v"(gt[1]) : "v"(a1[1][0][1]) : ); SB();
  asm volatile("v_mfma_f32_16x16x32_bf16 %0, v[204:207], a[92:95], %0" : "+v"(g[0][3][1]) :  : ); SB();
  asm volatile("buffer_load_dwordx4 v[164:167], %0, %1, %2 offen offset:1024 sc1" :  : "v"(vB), "s"(rWS), "s"(2048u) : "memory"); SB();
  asm volatile("v_mfma_f32_16x16x32_bf16 %0, v[208:211], a[96:99], %0" : "+v"(g[0][0][0]) :  : ); SB();
  asm volatile("v_mul_f32 %0, %1, %1" : "=v"(gt[2]) : "v"(a1[1][0][2]) : ); SB();
  asm volatile("v_mfma_f32_16x16x32_bf16 %0, v[208:211], a[100:103], %0" : "+v"(g[0][1][0]) :  : ); SB();
  asm volatile("buffer_load_dwordx4 v[168:171], %0, %1, %2 offen offset:0 sc1" :  : "v"(vB), "s"(rWS), "s"(67584u) : "memory"); SB();
  asm volatile("v_mul_f32 %0, %1, %1" : "=v"(gt[3]) : "v"(a1[1][0][3]) : ); SB();
  asm volatile("v_mfma_f32_16x16x32_bf16 %0, v[208:211], a[104:107], %0" : "+v"(g[0][2][0]) :  : ); SB();
  asm volatile("v_fma_f32 %0, %0, %1, %2" : "+v"(gt[0]) : "v"(k2[0]), "v"(k1[0]) : ); SB();
  asm volatile("s_waitcnt lgkmcnt(2)\n\tv_mfma_f32_16x16x32_bf16 %0, v[208:211], a[108:111], %0" : "+v"(g[0][3][0]) :  : ); SB();
  asm volatile("v_fma_f32 %0, %0, %1, %2" : "+v"(gt[1]) : "v"(k2[0]), "v"(k1[0]) : ); SB();
  asm volatile("v_mfma_f32_16x16x32_bf16 %0, v[212:215], a[96:99], %0" : "+v"(g[0][0][1]) :  : ); SB();
  asm volatile("s_waitcnt vmcnt(13)" :  :  : "memory"); SB();
  asm volatile("ds_read_b128 a[140:143], %0 offset:6144" :  : "v"(aA[0]) : "memory"); SB();
  asm volatile("v_mfma_f32_16x16x32_bf16 %0, v[212:215], a[100:103], %0" : "+v"(g[0][1][1]) :  : ); SB();
  asm volatile("ds_read_b128 a[156:159], %0 offset:6144" :  : "v"(aA[1]) : "memory"); SB();
  asm volatile("v_fma_f32 %0, %0, %1, %2" : "+v"(gt[2]) : "v"(k2[0]), "v"(k1[0]) : ); SB();
  asm volatile("v_mfma_f32_16x16x32_bf16 %0, v[212:215], a[104:107], %0" : "+v"(g[0][2][1]) :  : ); SB();
  asm volatile("buffer_load_dwordx4 v[172:175], %0, %1, %2 offen offset:1024 sc1" :  : "v"(vB), "s"(rWS), "s"(67584u) : "memory"); SB();
  asm volatile("v_fma_f32 %0, %0, %1, %2" : "+v"(gt[3]) : "v"(k2[0]), "v"(k1[0]) : ); SB();
  asm volatile("v_mfma_f32_16x16x32_bf16 %0, v[212:215], a[108:111], %0" : "+v"(g[0][3][1]) :  : ); SB();
  asm volatile("v_xor_b32 %0, 131072, %0" : "+v"(vB) :  : "memory"); SB();
  asm volatile("s_waitcnt vmcnt(12)" :  :  : "memory"); SB();
  asm volatile("v_mfma_f32_16x16x32_bf16 %0, v[216:219], a[112:115], %0" : "+v"(g[0][0][0]) :  : ); SB();
  asm volatile("buffer_load_dwordx4 v[176:179], %0, %1, %2 offen offset:0 sc1" :  : "v"(vB), "s"(rWS), "s"(2048u) : "memory"); SB();
  asm volatile("v_mul_f32 %0, %0, %1" : "+v"(gt[0]) : "v"(a1[1][0][0]) : ); SB();
  asm volatile("v_mfma_f32_16x16x32_bf16 %0, v[216:219], a[116:119], %0" : "+v"(g[0][1][0]) :  : ); SB();
  asm volatile("buffer_load_dwordx4 v[180:183], %0, %1, %2 offen offset:1024 sc1" :  : "v"(vB), "s"(rWS), "s"(2048u) : "memory"); SB();
  asm volatile("v_mul_f32 %0, %0, %1" : "+v"(gt[1]) : "v"(a1[1][0][1]) : ); SB();
  asm volatile("v_mfma_f32_16x16x32_bf16 %0, v[216:219], a[120:123], %0" : "+v"(g[0][2][0]) :  : ); SB();
  asm volatile("v_mul_f32 %0, %0, %1" : "+v"(gt[2]) : "v"(a1[1][0][2]) : ); SB();
  asm volatile("v_mfma_f32_16x16x32_bf16 %0, v[216:219], a[124:127], %0" : "+v"(g[0][3][0]) :  : ); SB();
  asm volatile("v_mul_f32 %0, %0, %1" : "+v"(gt[3]) : "v"(a1[1][0][3]) : ); SB();
  asm volatile("v_mfma_f32_16x16x32_bf16 %0, v[220:223], a[112:115], %0" : "+v"(g[0][0][1]) :  : ); SB();
  asm volatile("buffer_load_dwordx4 v[184:187], %0, %1, %2 offen offset:0 sc1" :  : "v"(vB), "s"(rWS), "s"(67584u) : "memory"); SB();
  asm volatile("v_exp_f32 %0, %1" : "=v"(ge[0]) : "v"(gt[0]) : ); SB();
  asm volatile("v_mfma_f32_16x16x32_bf16 %0, v[220:223], a[116:119], %0" : "+v"(g[0][1][1]) :  : ); SB();
  asm volatile("v_exp_f32 %0, %1" : "=v"(ge[1]) : "v"(gt[1]) : ); SB();
  asm volatile("v_mfma_f32_16x16x32_bf16 %0, v[220:223], a[120:123], %0" : "+v"(g[0][2][1]) :  : ); SB();
  asm volatile("v_exp_f32 %0, %1" : "=v"(ge[2]) : "v"(gt[2]) : ); SB();
  asm volatile("v_mfma_f32_16x16x32_bf16 %0, v[220:223], a[124:127], %0" : "+v"(g[0][3][1]) :  : ); SB();
  asm volatile("buffer_load_dwordx4 v[188:191], %0, %1, %2 offen offset:1024 sc1" :  : "v"(vB), "s"(rWS), "s"(67584u) : "memory"); SB();
  asm volatile("s_waitcnt vmcnt(14)" :  :  : "memory"); SB();
  asm volatile("v_mfma_f32_16x16x32_bf16 %0, v[224:227], a[128:131], %0" : "+v"(g[0][0][0]) :  : ); SB();
  asm volatile("v_xor_b32 %0, 393216, %0" : "+v"(vB) :  : "memory"); SB();
  asm volatile("v_exp_f32 %0, %1" : "=v"(ge[3]) : "v"(gt[3]) : ); SB();
  asm volatile("v_mfma_f32_16x16x32_bf16 %0, v[224:227], a[132:135], %0" : "+v"(g[0][1][0]) :  : ); SB();
  asm volatile("v_fma_f32 %0, %0, %1, %1" : "+v"(ge[0]) : "v"(xd[0]) : ); SB();
  asm volatile("s_waitcnt lgkmcnt(0)\n\tv_mfma_f32_16x16x32_bf16 %0, v[224:227], a[136:139], %0" : "+v"(g[0][2][0]) :  : ); SB();
  asm volatile("v_fma_f32 %0, %0, %1, %1" : "+v"(ge[1]) : "v"(xd[0]) : ); SB();
  asm volatile("v_mfma_f32_16x16x32_bf16 %0, v[224:227], a[140:143], %0" : "+v"(g[0][3][0]) :  : ); SB();
  asm volatile("buffer_load_dwordx4 v[192:195], %0, %1, %2 offen offset:0 sc1" :  : "v"(vB), "s"(rWS), "s"(2048u) : "memory"); SB();
  asm volatile("v_fma_f32 %0, %0, %1, %1" : "+v"(ge[2]) : "v"(xd[0]) : ); SB();
  asm volatile("v_mfma_f32_16x16x32_bf16 %0, v[228:231], a[128:131], %0" : "+v"(g[0][0][1]) :  : ); SB();
  asm volatile("v_fma_f32 %0, %0, %1, %1" : "+v"(ge[3]) : "v"(xd[0]) : ); SB();
  asm volatile("v_mfma_f32_16x16x32_bf16 %0, v[228:231], a[132:135], %0" : "+v"(g[0][1][1]) :  : ); SB();
  asm volatile("v_rcp_f32 %0, %1" : "=v"(gt[0]) : "v"(ge[0]) : ); SB();
  asm volatile("v_mfma_f32_16x16x32_bf16 %0, v[228:231], a[136:139], %0" : "+v"(g[0][2][1]) :  : ); SB();
  asm volatile("buffer_load_dwordx4 v[196:199], %0, %1, %2 offen offset:1024 sc1" :  : "v"(vB), "s"(rWS), "s"(2048u) : "memory"); SB();
  asm volatile("v_rcp_f32 %0, %1" : "=v"(gt[1]) : "v"(ge[1]) : ); SB();
  asm volatile("v_mfma_f32_16x16x32_bf16 %0, v[228:231], a[140:143], %0" : "+v"(g[0][3][1]) :  : ); SB();
  asm volatile("v_rcp_f32 %0, %1" : "=v"(gt[2]) : "v"(ge[2]) : ); SB();
  asm volatile("s_waitcnt vmcnt(14)" :  :  : "memory"); SB();
  asm volatile("v_mfma_f32_16x16x32_bf16 %0, v[232:235], a[144:147], %0" : "+v"(g[0][0][0]) :  : ); SB();
  asm volatile("v_rcp_f32 %0, %1" : "=v"(gt[3]) : "v"(ge[3]) : ); SB();
  asm volatile("v_mfma_f32_16x16x32_bf16 %0, v[232:235], a[148:151], %0" : "+v"(g[0][1][0]) :  : ); SB();
  asm volatile("buffer_load_dwordx4 v[200:203], %0, %1, %2 offen offset:0 sc1" :  : "v"(vB), "s"(rWS), "s"(67584u) : "memory"); SB();
  asm volatile("v_mul_f32 %0, %0, %1" : "+v"(a1[1][0][0]) : "v"(gt[0]) : ); SB();
  asm volatile("v_mfma_f32_16x16x32_bf16 %0, v[232:235], a[152:155], %0" : "+v"(g[0][2][0]) :  : ); SB();
  asm volatile("v_mul_f32 %0, %0, %1" : "+v"(a1[1][0][1]) : "v"(gt[1]) : ); SB();
  asm volatile("v_mfma_f32_16x16x32_bf16 %0, v[232:235], a[156:159], %0" : "+v"(g[0][3][0]) :  : ); SB();
  asm volatile("v_mul_f32 %0, %0, %1" : "+v"(a1[1][0][2]) : "v"(gt[2]) : ); SB();
  asm volatile("v_mfma_f32_16x16x32_bf16 %0, v[236:239], a[144:147], %0" : "+v"(g[0][0][1]) :  : ); SB();
  asm volatile("v_mul_f32 %0, %0, %1" : "+v"(a1[1][0][3]) : "v"(gt[3]) : ); SB();
  asm volatile("v_mfma_f32_16x16x32_bf16 %0, v[236:239], a[148:151], %0" : "+v"(g[0][1][1]) :  : ); SB();
  asm volatile("v_mul_f32 %0, %1, %1" : "=v"(gt[0]) : "v"(a1[1][1][0]) : ); SB();
  asm volatile("v_mfma_f32_16x16x32_bf16 %0, v[236:239], a[152:155], %0" : "+v"(g[0][2][1]) :  : ); SB();
  asm volatile("v_mul_f32 %0, %1, %1" : "=v"(gt[1]) : "v"(a1[1][1][1]) : ); SB();
  asm volatile("v_mfma_f32_16x16x32_bf16 %0, v[236:239], a[156:159], %0" : "+v"(g[0][3][1]) :  : ); SB();
  asm volatile("buffer_load_dwordx4 v[204:207], %0, %1, %2 offen offset:1024 sc1" :  : "v"(vB), "s"(rWS), "s"(67584u) : "memory"); SB();
  asm volatile("s_waitcnt vmcnt(14)" :  :  : "memory"); SB();
  asm volatile("v_mfma_f32_16x16x32_bf16 %0, v[240:243], a[0:3], %0" : "+v"(g[1][0][0]) :  : ); SB();
  asm volatile("s_barrier" :  :  : "memory"); SB();
  asm volatile("v_xor_b32 %0, 131072, %0" : "+v"(vB) :  : "memory"); SB();
  asm volatile("v_mfma_f32_16x16x32_bf16 %0, v[240:243], a[4:7], %0" : "+v"(g[1][1][0]) :  : ); SB();
  asm volatile("buffer_load_dwordx4 v[208:211], %0, %1, %2 offen offset:0 sc1" :  : "v"(vB), "s"(rWS), "s"(2048u) : "memory"); SB();
  asm volatile("v_mul_f32 %0, %1, %1" : "=v"(gt[2]) : "v"(a1[1][1][2]) : ); SB();
  asm volatile("v_mfma_f32_16x16x32_bf16 %0, v[240:243], a[8:11], %0" : "+v"(g[1][2][0]) :  : ); SB();
  asm volatile("ds_write_b128 %0, %1 offset:0" :  : "v"(pw2), "v"(g[0][1][0]) : "memory"); SB();
  asm volatile("v_mfma_f32_16x16x32_bf16 %0, v[240:243], a[12:15], %0" : "+v"(g[1][3][0]) :  : ); SB();
  asm volatile("buffer_load_dwordx4 v[212:215], %0, %1, %2 offen offset:1024 sc1" :  : "v"(vB), "s"(rWS), "s"(2048u) : "memory"); SB();
  asm volatile("v_mul_f32 %0, %1, %1" : "=v"(gt[3]) : "v"(a1[1][1][3]) : ); SB();
  asm volatile("v_mfma_f32_16x16x32_bf16 %0, v[244:247], a[0:3], %0" : "+v"(g[1][0][1]) :  : ); SB();
  asm volatile("v_fma_f32 %0, %0, %1, %2" : "+v"(gt[0]) : "v"(k2[0]), "v"(k1[0]) : ); SB();
  asm volatile("v_mfma_f32_16x16x32_bf16 %0, v[244:247], a[4:7], %0" : "+v"(g[1][1][1]) :  : ); SB();
  asm volatile("ds_write_b128 %0, %1 offset:1024" :  : "v"(pw2), "v"(g[0][1][1]) : "memory"); SB();
  asm volatile("v_mfma_f32_16x16x32_bf16 %0, v[244:247], a[8:11], %0" : "+v"(g[1][2][1]) :  : ); SB();
  asm volatile("buffer_load_dwordx4 v[216:219], %0, %1, %2 offen offset:0 sc1" :  : "v"(vB), "s"(rWS), "s"(67584u) : "memory"); SB();
  asm volatile("v_fma_f32 %0, %0, %1, %2" : "+v"(gt[1]) : "v"(k2[0]), "v"(k1[0]) : ); SB();
  asm volatile("v_mfma_f32_16x16x32_bf16 %0, v[244:247], a[12:15], %0" : "+v"(g[1][3][1]) :  : ); SB();
  asm volatile("v_fma_f32 %0, %0, %1, %2" : "+v"(gt[2]) : "v"(k2[0]), "v"(k1[0]) : ); SB();
  asm volatile("s_waitcnt vmcnt(15)" :  :  : "memory"); SB();
  asm volatile("v_mfma_f32_16x16x32_bf16 %0, v[248:251], a[16:19], %0" : "+v"(g[1][0][0]) :  : ); SB();
  asm volatile("ds_write_b128 %0, %1 offset:2048" :  : "v"(pw2), "v"(g[0][2][0]) : "memory"); SB();
  asm volatile("v_mfma_f32_16x16x32_bf16 %0, v[248:251], a[20:23], %0" : "+v"(g[1][1][0]) :  : ); SB();
  asm volatile("buffer_load_dwordx4 v[220:223], %0, %1, %2 offen offset:1024 sc1" :  : "v"(vB), "s"(rWS), "s"(67584u) : "memory"); SB();
  asm volatile("v_fma_f32 %0, %0, %1, %2" : "+v"(gt[3]) : "v"(k2[0]), "v"(k1[0]) : ); SB();
  asm volatile("v_mfma_f32_16x16x32_bf16 %0, v[248:251], a[24:27], %0" : "+v"(g[1][2][0]) :  : ); SB();
  asm volatile("v_xor_b32 %0, 917504, %0" : "+v"(vB) :  : "memory"); SB();
  asm volatile("v_mul_f32 %0, %0, %1" : "+v"(gt[0]) : "v"(a1[1][1][0]) : ); SB();
  asm volatile("v_mfma_f32_16x16x32_bf16 %0, v[248:251], a[28:31], %0" : "+v"(g[1][3][0]) :  : ); SB();
  asm volatile("ds_write_b128 %0, %1 offset:3072" :  : "v"(pw2), "v"(g[0][2][1]) : "memory"); SB();
  asm volatile("v_mfma_f32_16x16x32_bf16 %0, v[252:255], a[16:19], %0" : "+v"(g[1][0][1]) :  : ); SB();
  asm volatile("buffer_load_dwordx4 v[224:227], %0, %1, %2 offen offset:0 sc1" :  : "v"(vB), "s"(rWS), "s"(4096u) : "memory"); SB();
  asm volatile("v_mul_f32 %0, %0, %1" : "+v"(gt[1]) : "v"(a1[1][1][1]) : ); SB();
  asm volatile("v_mfma_f32_16x16x32_bf16 %0, v[252:255], a[20:23], %0" : "+v"(g[1][1][1]) :  : ); SB();
  asm volatile("v_mul_f32 %0, %0, %1" : "+v"(gt[2]) : "v"(a1[1][1][2]) : ); SB();
  asm volatile("v_mfma_f32_16x16x32_bf16 %0, v[252:255], a[24:27], %0" : "+v"(g[1][2][1]) :  : ); SB();
  asm volatile("ds_write_b128 %0, %1 offset:4096" :  : "v"(pw2), "v"(g[0][3][0]) : "memory"); SB();
  asm volatile("v_mfma_f32_16x16x32_bf16 %0, v[252:255], a[28:31], %0" : "+v"(g[1][3][1]) :  : ); SB();
  asm volatile("buffer_load_dwordx4 v[228:231], %0, %1, %2 offen offset:1024 sc1" :  : "v"(vB), "s"(rWS), "s"(4096u) : "memory"); SB();
  ST(28);
  asm volatile("s_waitcnt vmcnt(16)" :  :  : "memory"); SB();
  asm volatile("v_mfma_f32_16x16x32_bf16 %0, v[160:163], a[32:35], %0" : "+v"(g[1][0][0]) :  : ); SB();
  asm volatile("v_mul_f32 %0, %0, %1" : "+v"(gt[3]) : "v"(a1[1][1][3]) : ); SB();
  asm volatile("v_mfma_f32_16x16x32_bf16 %0, v[160:163], a[36:39], %0" : "+v"(g[1][1][0]) :  : ); SB();
  asm volatile("ds_write_b128 %0, %1 offset:5120" :  : "v"(pw2), "v"(g[0][3][1]) : "memory"); SB();
  asm volatile("v_mfma_f32_16x16x32_bf16 %0, v[160:163], a[40:43], %0" : "+v"(g[1][2][0]) :  : ); SB();
  asm volatile("buffer_load_dwordx4 v[232:235], %0, %1, %2 offen offset:0 sc1" :  : "v"(vB), "s"(rWS), "s"(69632u) : "memory"); SB();
  asm volatile("v_exp_f32 %0, %1" : "=v"(ge[0]) : "v"(gt[0]) : ); SB();
  asm volatile("v_mfma_f32_16x16x32_bf16 %0, v[160:163], a[44:47], %0" : "+v"(g[1][3][0]) :  : ); SB();
  asm volatile("v_exp_f32 %0, %1" : "=v"(ge[1]) : "v"(gt[1]) : ); SB();
  asm volatile("v_mfma_f32_16x16x32_bf16 %0, v[164:167], a[32:35], %0" : "+v"(g[1][0][1]) :  : ); SB();
  asm volatile("s_waitcnt lgkmcnt(0)\n\ts_barrier" :  :  : "memory"); SB();
  asm volatile("ds_read_b128 %0, %1 offset:0" : "=&v"(pr[0]) : "v"(pe[0]) : "memory"); SB();
  asm volatile("v_mfma_f32_16x16x32_bf16 %0, v[164:167], a[36:39], %0" : "+v"(g[1][1][1]) :  : ); SB();
  asm volatile("ds_read_b128 %0, %1 offset:1024" : "=&v"(pr[1]) : "v"(pe[0]) : "memory"); SB();
  asm volatile("v_exp_f32 %0, %1" : "=v"(ge[2]) : "v"(gt[2]) : ); SB();
  asm volatile("v_mfma_f32_16x16x32_bf16 %0, v[164:167], a[40:43], %0" : "+v"(g[1][2][1]) :  : ); SB();
  asm volatile("ds_read_b128 %0, %1 offset:2048" : "=&v"(pr[2]) : "v"(pe[1]) : "memory"); SB();
  asm volatile("v_exp_f32 %0, %1" : "=v"(ge[3]) : "v"(gt[3]) : ); SB();
  asm volatile("v_mfma_f32_16x16x32_bf16 %0, v[164:167], a[44:47], %0" : "+v"(g[1][3][1]) :  : ); SB();
  asm volatile("ds_read_b128 %0, %1 offset:3072" : "=&v"(pr[3]) : "v"(pe[1]) : "memory"); SB();
  asm volatile("s_waitcnt vmcnt(15)" :  :  : "memory"); SB();
  asm volatile("v_mfma_f32_16x16x32_bf16 %0, v[168:171], a[48:51], %0" : "+v"(g[1][0][0]) :  : ); SB();
  asm volatile("buffer_load_dwordx4 v[236:239], %0, %1, %2 offen offset:1024 sc1" :  : "v"(vB), "s"(rWS), "s"(69632u) : "memory"); SB();
  asm volatile("v_fma_f32 %0, %0, %1, %1" : "+v"(ge[0]) : "v"(xd[0]) : ); SB();
  asm volatile("v_mfma_f32_16x16x32_bf16 %0, v[168:171], a[52:55], %0" : "+v"(g[1][1][0]) :  : ); SB();
  asm volatile("ds_read_b128 %0, %1 offset:4096" : "=&v"(pr[4]) : "v"(pe[2]) : "memory"); SB();
  asm volatile("v_fma_f32 %0, %0, %1, %1" : "+v"(ge[1]) : "v"(xd[0]) : ); SB();
  asm volatile("v_mfma_f32_16x16x32_bf16 %0, v[168:171], a[56:59], %0" : "+v"(g[1][2][0]) :  : ); SB();
  asm volatile("ds_read_b128 %0, %1 offset:5120" : "=&v"(pr[5]) : "v"(pe[2]) : "memory"); SB();
  asm volatile("v_fma_f32 %0, %0, %1, %1" : "+v"(ge[2]) : "v"(xd[0]) : ); SB();
  asm volatile("v_mfma_f32_16x16x32_bf16 %0, v[168:171], a[60:63], %0" : "+v"(g[1][3][0]) :  : ); SB();
  asm volatile("v_xor_b32 %0, 131072, %0" : "+v"(vB) :  : "memory"); SB();
  asm volatile("v_fma_f32 %0, %0, %1, %1" : "+v"(ge[3]) : "v"(xd[0]) : ); SB();
  asm volatile("v_mfma_f32_16x16x32_bf16 %0, v[172:175], a[48:51], %0" : "+v"(g[1][0][1]) :  : ); SB();
  asm volatile("buffer_load_dwordx4 v[240:243], %0, %1, %2 offen offset:0 sc1" :  : "v"(vB), "s"(rWS), "s"(4096u) : "memory"); SB();
  asm volatile("v_rcp_f32 %0, %1" : "=v"(gt[0]) : "v"(ge[0]) : ); SB();
  asm volatile("v_mfma_f32_16x16x32_bf16 %0, v[172:175], a[52:55], %0" : "+v"(g[1][1][1]) :  : ); SB();
  asm volatile("buffer_load_dwordx4 v[244:247], %0, %1, %2 offen offset:1024 sc1" :  : "v"(vB), "s"(rWS), "s"(4096u) : "memory"); SB();
  asm volatile("v_rcp_f32 %0, %1" : "=v"(gt[1]) : "v"(ge[1]) : ); SB();
  asm volatile("v_mfma_f32_16x16x32_bf16 %0, v[172:175], a[56:59], %0" : "+v"(g[1][2][1]) :  : ); SB();
  asm volatile("v_rcp_f32 %0, %1" : "=v"(gt[2]) : "v"(ge[2]) : ); SB();
  asm volatile("v_mfma_f32_16x16x32_bf16 %0, v[172:175], a[60:63], %0" : "+v"(g[1][3][1]) :  : ); SB();
  asm volatile("v_rcp_f32 %0, %1" : "=v"(gt[3]) : "v"(ge[3]) : ); SB();
  asm volatile("s_waitcnt vmcnt(16)" :  :  : "memory"); SB();
  asm volatile("v_mfma_f32_16x16x32_bf16 %0, v[176:179], a[64:67], %0" : "+v"(g[1][0][0]) :  : ); SB();
  asm volatile("s_waitcnt lgkmcnt(0)" : "+v"(pr[0]), "+v"(pr[1]), "+v"(pr[2]), "+v"(pr[3]), "+v"(pr[4]), "+v"(pr[5]) :  : "memory"); SB();
  asm volatile("buffer_load_dwordx4 v[248:251], %0, %1, %2 offen offset:0 sc1" :  : "v"(vB), "s"(rWS), "s"(69632u) : "memory"); SB();
  asm volatile("v_mfma_f32_16x16x32_bf16 %0, v[176:179], a[68:71], %0" : "+v"(g[1][1][0]) :  : ); SB();
  asm volatile("v_add_f32 %0, %0, %1" : "+v"(pr[0][0]) : "v"(g[0][0][0][0]) : ); SB();
  asm volatile("v_mfma_f32_16x16x32_bf16 %0, v[176:179], a[72:75], %0" : "+v"(g[1][2][0]) :  : ); SB();
  asm volatile("buffer_load_dwordx4 v[252:255], %0, %1, %2 offen offset:1024 sc1" :  : "v"(vB), "s"(rWS), "s"(69632u) : "memory"); SB();
  asm volatile("v_add_f32 %0, %0, %1" : "+v"(pr[0][1]) : "v"(g[0][0][0][1]) : ); SB();
  asm volatile("v_mfma_f32_16x16x32_bf16 %0, v[176:179], a[76:79], %0" : "+v"(g[1][3][0]) :  : ); SB();
  asm volatile("v_xor_b32 %0, 393216, %0" : "+v"(vB) :  : "memory"); SB();
  asm volatile("v_add_f32 %0, %0, %1" : "+v"(pr[0][2]) : "v"(g[0][0][0][2]) : ); SB();
  asm volatile("v_mfma_f32_16x16x32_bf16 %0, v[180:183], a[64:67], %0" : "+v"(g[1][0][1]) :  : ); SB();
  asm volatile("v_add_f32 %0, %0, %1" : "+v"(pr[0][3]) : "v"(g[0][0][0][3]) : ); SB();
  asm volatile("v_mfma_f32_16x16x32_bf16 %0, v[180:183], a[68:71], %0" : "+v"(g[1][1][1]) :  : ); SB();
  asm volatile("v_add_f32 %0, %0, %1" : "+v"(pr[1][0]) : "v"(g[0][0][1][0]) : ); SB();
  asm volatile("v_mfma_f32_16x16x32_bf16 %0, v[180:183], a[72:75], %0" : "+v"(g[1][2][1]) :  : ); SB();
  asm volatile("buffer_load_dwordx4 v[160:163], %0, %1, %2 offen offset:0 sc1" :  : "v"(vB), "s"(rWS), "s"(4096u) : "memory"); SB();
  asm volatile("v_add_f32 %0, %0, %1" : "+v"(pr[1][1]) : "v"(g[0][0][1][1]) : ); SB();
  asm volatile("v_mfma_f32_16x16x32_bf16 %0, v[180:183], a[76:79], %0" : "+v"(g[1][3][1]) :  : ); SB();
  asm volatile("v_add_f32 %0, %0, %1" : "+v"(pr[1][2]) : "v"(g[0][0][1][2]) : ); SB();
  asm volatile("s_waitcnt vmcnt(17)" :  :  : "memory"); SB();
  asm volatile("v_mfma_f32_16x16x32_bf16 %0, v[184:187], a[80:83], %0" : "+v"(g[1][0][0]) :  : ); SB();
  asm volatile("v_add_f32 %0, %0, %1" : "+v"(pr[1][3]) : "v"(g[0][0][1][3]) : ); SB();
  asm volatile("v_mfma_f32_16x16x32_bf16 %0, v[184:187], a[84:87], %0" : "+v"(g[1][1][0]) :  : ); SB();
  asm volatile("buffer_load_dwordx4 v[164:167], %0, %1, %2 offen offset:1024 sc1" :  : "v"(vB), "s"(rWS), "s"(4096u) : "memory"); SB();
  asm volatile("v_add_f32 %0, %0, %1" : "+v"(pr[2][0]) : "v"(pr[4][0]) : ); SB();
  asm volatile("v_mfma_f32_16x16x32_bf16 %0, v[184:187], a[88:91], %0" : "+v"(g[1][2][0]) :  : ); SB();
  asm volatile("v_add_f32 %0, %0, %1" : "+v"(pr[2][1]) : "v"(pr[4][1]) : ); SB();
  asm volatile("v_mfma_f32_16x16x32_bf16 %0, v[184:187], a[92:95], %0" : "+v"(g[1][3][0]) :  : ); SB();
  asm volatile("v_add_f32 %0, %0, %1" : "+v"(pr[2][2]) : "v"(pr[4][2]) : ); SB();
  asm volatile("v_mfma_f32_16x16x32_bf16 %0, v[188:191], a[80:83], %0" : "+v"(g[1][0][1]) :  : ); SB();
  asm volatile("v_add_f32 %0, %0, %1" : "+v"(pr[2][3]) : "v"(pr[4][3]) : ); SB();
  asm volatile("v_mfma_f32_16x16x32_bf16 %0, v[188:191], a[84:87], %0" : "+v"(g[1][1][1]) :  : ); SB();
  asm volatile("buffer_load_dwordx4 v[168:171], %0, %1, %2 offen offset:0 sc1" :  : "v"(vB), "s"(rWS), "s"(69632u) : "memory"); SB();
  asm volatile("v_add_f32 %0, %0, %1" : "+v"(pr[3][0]) : "v"(pr[5][0]) : ); SB();
  asm volatile("v_mfma_f32_16x16x32_bf16 %0, v[188:191], a[88:91], %0" : "+v"(g[1][2][1]) :  : ); SB();
  asm volatile("v_add_f32 %0, %0, %1" : "+v"(pr[3][1]) : "v"(pr[5][1]) : ); SB();
  asm volatile("v_mfma_f32_16x16x32_bf16 %0, v[188:191], a[92:95], %0" : "+v"(g[1][3][1]) :  : ); SB();
  asm volatile("v_add_f32 %0, %0, %1" : "+v"(pr[3][2]) : "v"(pr[5][2]) : ); SB();
  asm volatile("s_waitcnt vmcnt(17)" :  :  : "memory"); SB();
  asm volatile("v_mfma_f32_16x16x32_bf16 %0, v[192:195], a[96:99], %0" : "+v"(g[1][0][0]) :  : ); SB();
  asm volatile("buffer_load_dwordx4 v[172:175], %0, %1, %2 offen offset:1024 sc1" :  : "v"(vB), "s"(rWS), "s"(69632u) : "memory"); SB();
  asm volatile("v_add_f32 %0, %0, %1" : "+v"(pr[3][3]) : "v"(pr[5][3]) : ); SB();
  asm volatile("v_mfma_f32_16x16x32_bf16 %0, v[192:195], a[100:103], %0" : "+v"(g[1][1][0]) :  : ); SB();
  asm volatile("v_xor_b32 %0, 131072, %0" : "+v"(vB) :  : "memory"); SB();
  asm volatile("v_add_f32 %0, %0, %1" : "+v"(pr[0][0]) : "v"(pr[2][0]) : ); SB();
  asm volatile("v_mfma_f32_16x16x32_bf16 %0, v[192:195], a[104:107], %0" : "+v"(g[1][2][0]) :  : ); SB();
  asm volatile("v_add_f32 %0, %0, %1" : "+v"(pr[0][1]) : "v"(pr[2][1]) : ); SB();
  asm volatile("v_mfma_f32_16x16x32_bf16 %0, v[192:195], a[108:111], %0" : "+v"(g[1][3][0]) :  : ); SB();
  asm volatile("v_add_f32 %0, %0, %1" : "+v"(pr[0][2]) : "v"(pr[2][2]) : ); SB();
  asm volatile("v_mfma_f32_16x16x32_bf16 %0, v[196:199], a[96:99], %0" : "+v"(g[1][0][1]) :  : ); SB();
  asm volatile("buffer_load_dwordx4 v[176:179], %0, %1, %2 offen offset:0 sc1" :  : "v"(vB), "s"(rWS), "s"(4096u) : "memory"); SB();
  asm volatile("v_add_f32 %0, %0, %1" : "+v"(pr[0][3]) : "v"(pr[2][3]) : ); SB();
  asm volatile("v_mfma_f32_16x16x32_bf16 %0, v[196:199], a[100:103], %0" : "+v"(g[1][1][1]) :  : ); SB();
  asm volatile("v_add_f32 %0, %0, %1" : "+v"(pr[1][0]) : "v"(pr[3][0]) : ); SB();
  asm volatile("v_mfma_f32_16x16x32_bf16 %0, v[196:199], a[104:107], %0" : "+v"(g[1][2][1]) :  : ); SB();
  asm volatile("v_add_f32 %0, %0, %1" : "+v"(pr[1][1]) : "v"(pr[3][1]) : ); SB();
  asm volatile("v_mfma_f32_16x16x32_bf16 %0, v[196:199], a[108:111], %0" : "+v"(g[1][3][1]) :  : ); SB();
  asm volatile("buffer_load_dwordx4 v[180:183], %0, %1, %2 offen offset:1024 sc1" :  : "v"(vB), "s"(rWS), "s"(4096u) : "memory"); SB();
  asm volatile("s_waitcnt vmcnt(18)" :  :  : "memory"); SB();
  asm volatile("v_mfma_f32_16x16x32_bf16 %0, v[200:203], a[112:115], %0" : "+v"(g[1][0][0]) :  : ); SB();
  asm volatile("v_add_f32 %0, %0, %1" : "+v"(pr[1][2]) : "v"(pr[3][2]) : ); SB();
  asm volatile("v_mfma_f32_16x16x32_bf16 %0, v[200:203], a[116:119], %0" : "+v"(g[1][1][0]) :  : ); SB();
  asm volatile("v_add_f32 %0, %0, %1" : "+v"(pr[1][3]) : "v"(pr[3][3]) : ); SB();
  asm volatile("v_mfma_f32_16x16x32_bf16 %0, v[200:203], a[120:123], %0" : "+v"(g[1][2][0]) :  : ); SB();
  asm volatile("v_mul_f32 %0, %0, %1" : "+v"(pr[0][0]) : "v"(a1[0][0][0]) : ); SB();
  asm volatile("v_mfma_f32_16x16x32_bf16 %0, v[200:203], a[124:127], %0" : "+v"(g[1][3][0]) :  : ); SB();
  asm volatile("buffer_load_dwordx4 v[184:187], %0, %1, %2 offen offset:0 sc1" :  : "v"(vB), "s"(rWS), "s"(69632u) : "memory"); SB();
  asm volatile("v_mul_f32 %0, %0, %1" : "+v"(pr[0][1]) : "v"(a1[0][0][1]) : ); SB();
  asm volatile("v_mfma_f32_16x16x32_bf16 %0, v[204:207], a[112:115], %0" : "+v"(g[1][0][1]) :  : ); SB();
  asm volatile("v_mul_f32 %0, %0, %1" : "+v"(pr[0][2]) : "v"(a1[0][0][2]) : ); SB();
  asm volatile("v_mfma_f32_16x16x32_bf16 %0, v[204:207], a[116:119], %0" : "+v"(g[1][1][1]) :  : ); SB();
  asm volatile("v_mul_f32 %0, %0, %1" : "+v"(pr[0][3]) : "v"(a1[0][0][3]) : ); SB();
  asm volatile("v_mfma_f32_16x16x32_bf16 %0, v[204:207], a[120:123], %0" : "+v"(g[1][2][1]) :  : ); SB();
  asm volatile("buffer_load_dwordx4 v[188:191], %0, %1, %2 offen offset:1024 sc1" :  : "v"(vB), "s"(rWS), "s"(69632u) : "memory"); SB();
  asm volatile("v_mul_f32 %0, %0, %1" : "+v"(pr[1][0]) : "v"(a1[0][1][0]) : ); SB();
  asm volatile("v_mfma_f32_16x16x32_bf16 %0, v[204:207], a[124:127], %0" : "+v"(g[1][3][1]) :  : ); SB();
  asm volatile("v_xor_b32 %0, 917504, %0" : "+v"(vB) :  : "memory"); SB();
  asm volatile("s_waitcnt vmcnt(18)" :  :  : "memory"); SB();
  asm volatile("v_mfma_f32_16x16x32_bf16 %0, v[208:211], a[128:131], %0" : "+v"(g[1][0][0]) :  : ); SB();
  asm volatile("v_mul_f32 %0, %0, %1" : "+v"(pr[1][1]) : "v"(a1[0][1][1]) : ); SB();
  asm volatile("v_mfma_f32_16x16x32_bf16 %0, v[208:211], a[132:135], %0" : "+v"(g[1][1][0]) :  : ); SB();
  asm volatile("v_mul_f32 %0, %0, %1" : "+v"(pr[1][2]) : "v"(a1[0][1][2]) : ); SB();
  asm volatile("v_mfma_f32_16x16x32_bf16 %0, v[208:211], a[136:139], %0" : "+v"(g[1][2][0]) :  : ); SB();
  asm volatile("buffer_load_dwordx4 v[192:195], %0, %1, %2 offen offset:0 sc1" :  : "v"(vB), "s"(rWS), "s"(4096u) : "memory"); SB();
  asm volatile("v_mul_f32 %0, %0, %1" : "+v"(pr[1][3]) : "v"(a1[0][1][3]) : ); SB();
  asm volatile("v_mfma_f32_16x16x32_bf16 %0, v[208:211], a[140:143], %0" : "+v"(g[1][3][0]) :  : ); SB();
  asm volatile("v_cvt_pk_bf16_f32 %0, %1, %2" : "=v"(ho[0][0]) : "v"(pr[0][0]), "v"(pr[0][1]) : ); SB();
  asm volatile("v_mfma_f32_16x16x32_bf16 %0, v[212:215], a[128:131], %0" : "+v"(g[1][0][1]) :  : ); SB();
  asm volatile("v_cvt_pk_bf16_f32 %0, %1, %2" : "=v"(ho[0][1]) : "v"(pr[0][2]), "v"(pr[0][3]) : ); SB();
  asm volatile("v_mfma_f32_16x16x32_bf16 %0, v[212:215], a[132:135], %0" : "+v"(g[1][1][1]) :  : ); SB();
  asm volatile("v_cvt_pk_bf16_f32 %0, %1, %2" : "=v"(ho[0][2]) : "v"(pr[1][0]), "v"(pr[1][1]) : ); SB();
  asm volatile("v_mfma_f32_16x16x32_bf16 %0, v[212:215], a[136:139], %0" : "+v"(g[1][2][1]) :  : ); SB();
  asm volatile("v_cvt_pk_bf16_f32 %0, %1, %2" : "=v"(ho[0][3]) : "v"(pr[1][2]), "v"(pr[1][3]) : ); SB();
  asm volatile("v_mfma_f32_16x16x32_bf16 %0, v[212:215], a[140:143], %0" : "+v"(g[1][3][1]) :  : ); SB();
  asm volatile("v_mul_f32 %0, %0, %1" : "+v"(a1[1][1][0]) : "v"(gt[0]) : ); SB();
  asm volatile("s_waitcnt vmcnt(17)" :  :  : "memory"); SB();
  asm volatile("v_mfma_f32_16x16x32_bf16 %0, v[216:219], a[144:147], %0" : "+v"(g[1][0][0]) :  : ); SB();
  asm volatile("buffer_store_dwordx4 %1, %0, %2, %3 offen offset:0 sc1\n\ts_nop 1" :  : "v"(vH_), "v"(ho[0]), "s"(rWS), "s"(0u) : "memory"); SB();
  asm volatile("v_mul_f32 %0, %0, %1" : "+v"(a1[1][1][1]) : "v"(gt[1]) : ); SB();
  asm volatile("v_mfma_f32_16x16x32_bf16 %0, v[216:219], a[148:151], %0" : "+v"(g[1][1][0]) :  : ); SB();
  asm volatile("v_mul_f32 %0, %0, %1" : "+v"(a1[1][1][2]) : "v"(gt[2]) : ); SB();
  asm volatile("v_mfma_f32_16x16x32_bf16 %0, v[216:219], a[152:155], %0" : "+v"(g[1][2][0]) :  : ); SB();
  asm volatile("v_mul_f32 %0, %0, %1" : "+v"(a1[1][1][3]) : "v"(gt[3]) : ); SB();
  asm volatile("v_mfma_f32_16x16x32_bf16 %0, v[216:219], a[156:159], %0" : "+v"(g[1][3][0]) :  : ); SB();
  asm volatile("v_mul_f32 %0, %1, %1" : "=v"(gt[0]) : "v"(a1[1][2][0]) : ); SB();
  asm volatile("v_mfma_f32_16x16x32_bf16 %0, v[220:223], a[144:147], %0" : "+v"(g[1][0][1]) :  : ); SB();
  asm volatile("buffer_load_dwordx4 v[196:199], %0, %1, %2 offen offset:1024 sc1" :  : "v"(vB), "s"(rWS), "s"(4096u) : "memory"); SB();
  asm volatile("v_mul_f32 %0, %1, %1" : "=v"(gt[1]) : "v"(a1[1][2][1]) : ); SB();
  asm volatile("v_mfma_f32_16x16x32_bf16 %0, v[220:223], a[148:151], %0" : "+v"(g[1][1][1]) :  : ); SB();
  asm volatile("v_mul_f32 %0, %1, %1" : "=v"(gt[2]) : "v"(a1[1][2][2]) : ); SB();
  asm volatile("v_mfma_f32_16x16x32_bf16 %0, v[220:223], a[152:155], %0" : "+v"(g[1][2][1]) :  : ); SB();
  asm volatile("v_mul_f32 %0, %1, %1" : "=v"(gt[3]) : "v"(a1[1][2][3]) : ); SB();
  asm volatile("v_mfma_f32_16x16x32_bf16 %0, v[220:223], a[156:159], %0" : "+v"(g[1][3][1]) :  : ); SB();
  asm volatile("v_fma_f32 %0, %0, %1, %2" : "+v"(gt[0]) : "v"(k2[0]), "v"(k1[0]) : ); SB();
  ST(29);
  asm volatile("s_waitcnt vmcnt(17)" :  :  : "memory"); SB();
  asm volatile("v_mfma_f32_16x16x32_bf16 %0, v[224:227], a[160:163], %1" : "=&v"(g[0][0][0]) : "v"(bs4) : ); SB();
  asm volatile("s_barrier" :  :  : "memory"); SB();
  asm volatile("buffer_load_dwordx4 v[200:203], %0, %1, %2 offen offset:0 sc1" :  : "v"(vB), "s"(rWS), "s"(69632u) : "memory"); SB();
  asm volatile("v_mfma_f32_16x16x32_bf16 %0, v[224:227], a[164:167], 0" : "=&v"(g[0][1][0]) :  : ); SB();
  asm volatile("v_fma_f32 %0, %0, %1, %2" : "+v"(gt[1]) : "v"(k2[0]), "v"(k1[0]) : ); SB();
  asm volatile("v_mfma_f32_16x16x32_bf16 %0, v[224:227], a[168:171], 0" : "=&v"(g[0][2][0]) :  : ); SB();
  asm volatile("ds_write_b128 %0, %1 offset:0" :  : "v"(pw2), "v"(g[1][1][0]) : "memory"); SB();
  asm volatile("v_mfma_f32_16x16x32_bf16 %0, v[224:227], a[172:175], 0" : "=&v"(g[0][3][0]) :  : ); SB();
  asm volatile("buffer_load_dwordx4 v[204:207], %0, %1, %2 offen offset:1024 sc1" :  : "v"(vB), "s"(rWS), "s"(69632u) : "memory"); SB();
  asm volatile("v_fma_f32 %0, %0, %1, %2" : "+v"(gt[2]) : "v"(k2[0]), "v"(k1[0]) : ); SB();
  asm volatile("v_mfma_f32_16x16x32_bf16 %0, v[228:231], a[160:163], %1" : "=&v"(g[0][0][1]) : "v"(bs4) : ); SB();
  asm volatile("v_xor_b32 %0, 131072, %0" : "+v"(vB) :  : "memory"); SB();
  asm volatile("v_fma_f32 %0, %0, %1, %2" : "+v"(gt[3]) : "v"(k2[0]), "v"(k1[0]) : ); SB();
  asm volatile("v_mfma_f32_16x16x32_bf16 %0, v[228:231], a[164:167], 0" : "=&v"(g[0][1][1]) :  : ); SB();
  asm volatile("ds_write_b128 %0, %1 offset:1024" :  : "v"(pw2), "v"(g[1][1][1]) : "memory"); SB();
  asm volatile("v_mfma_f32_16x16x32_bf16 %0, v[228:231], a[168:171], 0" : "=&v"(g[0][2][1]) :  : ); SB();
  asm volatile("buffer_load_dwordx4 v[208:211], %0, %1, %2 offen offset:0 sc1" :  : "v"(vB), "s"(rWS), "s"(4096u) : "memory"); SB();
  asm volatile("v_mul_f32 %0, %0, %1" : "+v"(gt[0]) : "v"(a1[1][2][0]) : ); SB();
  asm volatile("v_mfma_f32_16x16x32_bf16 %0, v[228:231], a[172:175], 0" : "=&v"(g[0][3][1]) :  : ); SB();
  asm volatile("buffer_load_dwordx4 v[212:215], %0, %1, %2 offen offset:1024 sc1" :  : "v"(vB), "s"(rWS), "s"(4096u) : "memory"); SB();
  asm volatile("s_waitcnt vmcnt(19)" :  :  : "memory"); SB();
  asm volatile("v_mfma_f32_16x16x32_bf16 %0, v[232:235], a[176:179], %0" : "+v"(g[0][0][0]) :  : ); SB();
  asm volatile("ds_write_b128 %0, %1 offset:2048" :  : "v"(pw2), "v"(g[1][2][0]) : "memory"); SB();
  asm volatile("v_mfma_f32_16x16x32_bf16 %0, v[232:235], a[180:183], %0" : "+v"(g[0][1][0]) :  : ); SB();
  asm volatile("buffer_load_dwordx4 v[216:219], %0, %1, %2 offen offset:0 sc1" :  : "v"(vB), "s"(rWS), "s"(69632u) : "memory"); SB();
  asm volatile("v_mul_f32 %0, %0, %1" : "+v"(gt[1]) : "v"(a1[1][2][1]) : ); SB();
  asm volatile("v_mfma_f32_16x16x32_bf16 %0, v[232:235], a[184:187], %0" : "+v"(g[0][2][0]) :  : ); SB();
  asm volatile("v_mul_f32 %0, %0, %1" : "+v"(gt[2]) : "v"(a1[1][2][2]) : ); SB();
  asm volatile("v_mfma_f32_16x16x32_bf16 %0, v[232:235], a[188:191], %0" : "+v"(g[0][3][0]) :  : ); SB();
  asm volatile("ds_write_b128 %0, %1 offset:3072" :  : "v"(pw2), "v"(g[1][2][1]) : "memory"); SB();
  asm volatile("v_mfma_f32_16x16x32_bf16 %0, v[236:239], a[176:179], %0" : "+v"(g[0][0][1]) :  : ); SB();
  asm volatile("buffer_load_dwordx4 v[220:223], %0, %1, %2 offen offset:1024 sc1" :  : "v"(vB), "s"(rWS), "s"(69632u) : "memory"); SB();
  asm volatile("v_mul_f32 %0, %0, %1" : "+v"(gt[3]) : "v"(a1[1][2][3]) : ); SB();
  asm volatile("v_mfma_f32_16x16x32_bf16 %0, v[236:239], a[180:183], %0" : "+v"(g[0][1][1]) :  : ); SB();
  asm volatile("v_xor_b32 %0, 393216, %0" : "+v"(vB) :  : "memory"); SB();
  asm volatile("v_exp_f32 %0, %1" : "=v"(ge[0]) : "v"(gt[0]) : ); SB();
  asm volatile("v_mfma_f32_16x16x32_bf16 %0, v[236:239], a[184:187], %0" : "+v"(g[0][2][1]) :  : ); SB();
  asm volatile("ds_write_b128 %0, %1 offset:4096" :  : "v"(pw2), "v"(g[1][3][0]) : "memory"); SB();
  asm volatile("v_mfma_f32_16x16x32_bf16 %0, v[236:239], a[188:191], %0" : "+v"(g[0][3][1]) :  : ); SB();
  asm volatile("buffer_load_dwordx4 v[224:227], %0, %1, %2 offen offset:0 sc1" :  : "v"(vB), "s"(rWS), "s"(4096u) : "memory"); SB();
  asm volatile("s_waitcnt vmcnt(20)" :  :  : "memory"); SB();
  asm volatile("v_mfma_f32_16x16x32_bf16 %0, v[240:243], a[192:195], %0" : "+v"(g[0][0][0]) :  : ); SB();
  asm volatile("v_exp_f32 %0, %1" : "=v"(ge[1]) : "v"(gt[1]) : ); SB();
  asm volatile("v_mfma_f32_16x16x32_bf16 %0, v[240:243], a[196:199], %0" : "+v"(g[0][1][0]) :  : ); SB();
  asm volatile("ds_write_b128 %0, %1 offset:5120" :  : "v"(pw2), "v"(g[1][3][1]) : "memory"); SB();
  asm volatile("v_mfma_f32_16x16x32_bf16 %0, v[240:243], a[200:203], %0" : "+v"(g[0][2][0]) :  : ); SB();
  asm volatile("buffer_load_dwordx4 v[228:231], %0, %1, %2 offen offset:1024 sc1" :  : "v"(vB), "s"(rWS), "s"(4096u) : "memory"); SB();
  asm volatile("v_exp_f32 %0, %1" : "=v"(ge[2]) : "v"(gt[2]) : ); SB();
  asm volatile("v_mfma_f32_16x16x32_bf16 %0, v[240:243], a[204:207], %0" : "+v"(g[0][3][0]) :  : ); SB();
  asm volatile("buffer_load_dwordx4 v[232:235], %0, %1, %2 offen offset:0 sc1" :  : "v"(vB), "s"(rWS), "s"(69632u) : "memory"); SB();
  asm volatile("v_exp_f32 %0, %1" : "=v"(ge[3]) : "v"(gt[3]) : ); SB();
  asm volatile("v_mfma_f32_16x16x32_bf16 %0, v[244:247], a[192:195], %0" : "+v"(g[0][0][1]) :  : ); SB();
  asm volatile("s_waitcnt lgkmcnt(0)\n\ts_barrier" :  :  : "memory"); SB();
  asm volatile("ds_read_b128 %0, %1 offset:0" : "=&v"(pr[0]) : "v"(pe[0]) : "memory"); SB();
  asm volatile("v_mfma_f32_16x16x32_bf16 %0, v[244:247], a[196:199], %0" : "+v"(g[0][1][1]) :  : ); SB();
  asm volatile("ds_read_b128 %0, %1 offset:1024" : "=&v"(pr[1]) : "v"(pe[0]) : "memory"); SB();
  asm volatile("v_fma_f32 %0, %0, %1, %1" : "+v"(ge[0]) : "v"(xd[0]) : ); SB();
  asm volatile("v_mfma_f32_16x16x32_bf16 %0, v[244:247], a[200:203], %0" : "+v"(g[0][2][1]) :  : ); SB();
  asm volatile("ds_read_b128 %0, %1 offset:2048" : "=&v"(pr[2]) : "v"(pe[1]) : "memory"); SB();
  asm volatile("v_fma_f32 %0, %0, %1, %1" : "+v"(ge[1]) : "v"(xd[0]) : ); SB();
  asm volatile("v_mfma_f32_16x16x32_bf16 %0, v[244:247], a[204:207], %0" : "+v"(g[0][3][1]) :  : ); SB();
  asm volatile("ds_read_b128 %0, %1 offset:3072" : "=&v"(pr[3]) : "v"(pe[1]) : "memory"); SB();
  asm volatile("s_waitcnt vmcnt(20)" :  :  : "memory"); SB();
  asm volatile("v_mfma_f32_16x16x32_bf16 %0, v[248:251], a[208:211], %0" : "+v"(g[0][0][0]) :  : ); SB();
  asm volatile("ds_read_b128 %0, %1 offset:4096" : "=&v"(pr[4]) : "v"(pe[2]) : "memory"); SB();
  asm volatile("v_fma_f32 %0, %0, %1, %1" : "+v"(ge[2]) : "v"(xd[0]) : ); SB();
  asm volatile("v_mfma_f32_16x16x32_bf16 %0, v[248:251], a[212:215], %0" : "+v"(g[0][1][0]) :  : ); SB();
  asm volatile("ds_read_b128 %0, %1 offset:5120" : "=&v"(pr[5]) : "v"(pe[2]) : "memory"); SB();
  asm volatile("v_fma_f32 %0, %0, %1, %1" : "+v"(ge[3]) : "v"(xd[0]) : ); SB();
  asm volatile("v_mfma_f32_16x16x32_bf16 %0, v[248:251], a[216:219], %0" : "+v"(g[0][2][0]) :  : ); SB();
  asm volatile("buffer_load_dwordx4 v[236:239], %0, %1, %2 offen offset:1024 sc1" :  : "v"(vB), "s"(rWS), "s"(69632u) : "memory"); SB();
  asm volatile("v_rcp_f32 %0, %1" : "=v"(gt[0]) : "v"(ge[0]) : ); SB();
  asm volatile("v_mfma_f32_16x16x32_bf16 %0, v[248:251], a[220:223], %0" : "+v"(g[0][3][0]) :  : ); SB();
  asm volatile("v_xor_b32 %0, 131072, %0" : "+v"(vB) :  : "memory"); SB();
  asm volatile("v_rcp_f32 %0, %1" : "=v"(gt[1]) : "v"(ge[1]) : ); SB();
  asm volatile("v_mfma_f32_16x16x32_bf16 %0, v[252:255], a[208:211], %0" : "+v"(g[0][0][1]) :  : ); SB();
  asm volatile("buffer_load_dwordx4 v[240:243], %0, %1, %2 offen offset:0 sc1" :  : "v"(vB), "s"(rWS), "s"(4096u) : "memory"); SB();
  asm volatile("v_rcp_f32 %0, %1" : "=v"(gt[2]) : "v"(ge[2]) : ); SB();
  asm volatile("v_mfma_f32_16x16x32_bf16 %0, v[252:255], a[212:215], %0" : "+v"(g[0][1][1]) :  : ); SB();
  asm volatile("v_rcp_f32 %0, %1" : "=v"(gt[3]) : "v"(ge[3]) : ); SB();
  asm volatile("v_mfma_f32_16x16x32_bf16 %0, v[252:255], a[216:219], %0" : "+v"(g[0][2][1]) :  : ); SB();
  asm volatile("buffer_load_dwordx4 v[244:247], %0, %1, %2 offen offset:1024 sc1" :  : "v"(vB), "s"(rWS), "s"(4096u) : "memory"); SB();
  asm volatile("v_mul_f32 %0, %0, %1" : "+v"(a1[1][2][0]) : "v"(gt[0]) : ); SB();
  asm volatile("v_mfma_f32_16x16x32_bf16 %0, v[252:255], a[220:223], %0" : "+v"(g[0][3][1]) :  : ); SB();
  asm volatile("s_waitcnt lgkmcnt(0)" : "+v"(pr[0]), "+v"(pr[1]), "+v"(pr[2]), "+v"(pr[3]), "+v"(pr[4]), "+v"(pr[5]) :  : "memory"); SB();
  asm volatile("v_add_f32 %0, %0, %1" : "+v"(pr[0][0]) : "v"(g[1][0][0][0]) : ); SB();
  asm volatile("s_waitcnt vmcnt(21)" :  :  : "memory"); SB();
  asm volatile("v_mfma_f32_16x16x32_bf16 %0, v[160:163], a[224:227], %0" : "+v"(g[0][0][0]) :  : ); SB();
  asm volatile("v_add_f32 %0, %0, %1" : "+v"(pr[0][1]) : "v"(g[1][0][0][1]) : ); SB();
  asm volatile("v_mfma_f32_16x16x32_bf16 %0, v[160:163], a[228:231], %0" : "+v"(g[0][1][0]) :  : ); SB();
  asm volatile("v_add_f32 %0, %0, %1" : "+v"(pr[0][2]) : "v"(g[1][0][0][2]) : ); SB();
  asm volatile("v_mfma_f32_16x16x32_bf16 %0, v[160:163], a[232:235], %0" : "+v"(g[0][2][0]) :  : ); SB();
  asm volatile("v_add_f32 %0, %0, %1" : "+v"(pr[0][3]) : "v"(g[1][0][0][3]) : ); SB();
  asm volatile("v_mfma_f32_16x16x32_bf16 %0, v[160:163], a[236:239], %0" : "+v"(g[0][3][0]) :  : ); SB();
  asm volatile("v_add_f32 %0, %0, %1" : "+v"(pr[1][0]) : "v"(g[1][0][1][0]) : ); SB();
  asm volatile("v_mfma_f32_16x16x32_bf16 %0, v[164:167], a[224:227], %0" : "+v"(g[0][0][1]) :  : ); SB();
  asm volatile("v_add_f32 %0, %0, %1" : "+v"(pr[1][1]) : "v"(g[1][0][1][1]) : ); SB();
  asm volatile("v_mfma_f32_16x16x32_bf16 %0, v[164:167], a[228:231], %0" : "+v"(g[0][1][1]) :  : ); SB();
  asm volatile("v_add_f32 %0, %0, %1" : "+v"(pr[1][2]) : "v"(g[1][0][1][2]) : ); SB();
  asm volatile("v_mfma_f32_16x16x32_bf16 %0, v[164:167], a[232:235], %0" : "+v"(g[0][2][1]) :  : ); SB();
  asm volatile("v_add_f32 %0, %0, %1" : "+v"(pr[1][3]) : "v"(g[1][0][1][3]) : ); SB();
  asm volatile("v_mfma_f32_16x16x32_bf16 %0, v[164:167], a[236:239], %0" : "+v"(g[0][3][1]) :  : ); SB();
  asm volatile("v_add_f32 %0, %0, %1" : "+v"(pr[2][0]) : "v"(pr[4][0]) : ); SB();
  asm volatile("s_waitcnt vmcnt(19)" :  :  : "memory"); SB();
  asm volatile("v_mfma_f32_16x16x32_bf16 %0, v[168:171], a[240:243], %0" : "+v"(g[0][0][0]) :  : ); SB();
  asm volatile("v_add_f32 %0, %0, %1" : "+v"(pr[2][1]) : "v"(pr[4][1]) : ); SB();
  asm volatile("v_mfma_f32_16x16x32_bf16 %0, v[168:171], a[244:247], %0" : "+v"(g[0][1][0]) :  : ); SB();
  asm volatile("s_add_u32 m0, %0, 60032" :  : "s"(mU) : "memory", "scc"); SB();
  asm volatile("v_add_f32 %0, %0, %1" : "+v"(pr[2][2]) : "v"(pr[4][2]) : ); SB();
  asm volatile("v_mfma_f32_16x16x32_bf16 %0, v[168:171], a[248:251], %0" : "+v"(g[0][2][0]) :  : ); SB();
  asm volatile("buffer_load_dwordx4 %0, %1, %2 offen sc1 lds" :  : "v"(vF), "s"(rWS), "s"(W_KV) : "memory"); SB();
  asm volatile("v_add_f32 %0, %0, %1" : "+v"(pr[2][3]) : "v"(pr[4][3]) : ); SB();
  asm volatile("v_mfma_f32_16x16x32_bf16 %0, v[168:171], a[252:255], %0" : "+v"(g[0][3][0]) :  : ); SB();
  asm volatile("v_add_f32 %0, %0, %1" : "+v"(pr[3][0]) : "v"(pr[5][0]) : ); SB();
  asm volatile("v_mfma_f32_16x16x32_bf16 %0, v[172:175], a[240:243], %0" : "+v"(g[0][0][1]) :  : ); SB();
  asm volatile("s_add_u32 m0, %0, 72960" :  : "s"(mU) : "memory", "scc"); SB();
  asm volatile("v_add_f32 %0, %0, %1" : "+v"(pr[3][1]) : "v"(pr[5][1]) : ); SB();
  asm volatile("v_mfma_f32_16x16x32_bf16 %0, v[172:175], a[244:247], %0" : "+v"(g[0][1][1]) :  : ); SB();
  asm volatile("buffer_load_dwordx4 %0, %1, %2 offen sc1 lds" :  : "v"(vF), "s"(rWS), "s"(W_KV + 4096) : "memory"); SB();
  asm volatile("v_add_f32 %0, %0, %1" : "+v"(pr[3][2]) : "v"(pr[5][2]) : ); SB();
  asm volatile("v_mfma_f32_16x16x32_bf16 %0, v[172:175], a[248:251], %0" : "+v"(g[0][2][1]) :  : ); SB();
  asm volatile("v_add_f32 %0, %0, %1" : "+v"(pr[3][3]) : "v"(pr[5][3]) : ); SB();
  asm volatile("v_mfma_f32_16x16x32_bf16 %0, v[172:175], a[252:255], %0" : "+v"(g[0][3][1]) :  : ); SB();
  asm volatile("v_add_f32 %0, %0, %1" : "+v"(pr[0][0]) : "v"(pr[2][0]) : ); SB();
  asm volatile("s_waitcnt vmcnt(19)" :  :  : "memory"); SB();
  asm volatile("v_mfma_f32_16x16x32_bf16 %0, v[176:179], a[0:3], %0" : "+v"(g[0][0][0]) :  : ); SB();
  asm volatile("buffer_load_dwordx4 v[248:251], %0, %1, %2 offen offset:0 sc1" :  : "v"(vB), "s"(rWS), "s"(69632u) : "memory"); SB();
  asm volatile("v_add_f32 %0, %0, %1" : "+v"(pr[0][1]) : "v"(pr[2][1]) : ); SB();
  asm volatile("v_mfma_f32_16x16x32_bf16 %0, v[176:179], a[4:7], %0" : "+v"(g[0][1][0]) :  : ); SB();
  asm volatile("v_add_f32 %0, %0, %1" : "+v"(pr[0][2]) : "v"(pr[2][2]) : ); SB();
  asm volatile("v_mfma_f32_16x16x32_bf16 %0, v[176:179], a[8:11], %0" : "+v"(g[0][2][0]) :  : ); SB();
  asm volatile("v_add_f32 %0, %0, %1" : "+v"(pr[0][3]) : "v"(pr[2][3]) : ); SB();
  asm volatile("v_mfma_f32_16x16x32_bf16 %0, v[176:179], a[12:15], %0" : "+v"(g[0][3][0]) :  : ); SB();
  asm volatile("v_add_f32 %0, %0, %1" : "+v"(pr[1][0]) : "v"(pr[3][0]) : ); SB();
  asm volatile("v_mfma_f32_16x16x32_bf16 %0, v[180:183], a[0:3], %0" : "+v"(g[0][0][1]) :  : ); SB();
  asm volatile("buffer_load_dwordx4 v[252:255], %0, %1, %2 offen offset:1024 sc1" :  : "v"(vB), "s"(rWS), "s"(69632u) : "memory"); SB();
  asm volatile("v_add_f32 %0, %0, %1" : "+v"(pr[1][1]) : "v"(pr[3][1]) : ); SB();
  asm volatile("v_mfma_f32_16x16x32_bf16 %0, v[180:183], a[4:7], %0" : "+v"(g[0][1][1]) :  : ); SB();
  asm volatile("v_add_f32 %0, %0, %1" : "+v"(pr[1][2]) : "v"(pr[3][2]) : ); SB();
  asm volatile("v_mfma_f32_16x16x32_bf16 %0, v[180:183], a[8:11], %0" : "+v"(g[0][2][1]) :  : ); SB();
  asm volatile("v_add_f32 %0, %0, %1" : "+v"(pr[1][3]) : "v"(pr[3][3]) : ); SB();
  asm volatile("v_mfma_f32_16x16x32_bf16 %0, v[180:183], a[12:15], %0" : "+v"(g[0][3][1]) :  : ); SB();
  asm volatile("buffer_load_dwordx4 a[160:163], %0, %1, %2 offen offset:0 sc1" :  : "v"(vO), "s"(rWS), "s"(0u) : "memory"); SB();
  asm volatile("s_waitcnt vmcnt(20)" :  :  : "memory"); SB();
  asm volatile("v_mfma_f32_16x16x32_bf16 %0, v[184:187], a[16:19], %0" : "+v"(g[0][0][0]) :  : ); SB();
  asm volatile("v_mul_f32 %0, %0, %1" : "+v"(pr[0][0]) : "v"(a1[0][2][0]) : ); SB();
  asm volatile("v_mfma_f32_16x16x32_bf16 %0, v[184:187], a[20:23], %0" : "+v"(g[0][1][0]) :  : ); SB();
  asm volatile("v_mul_f32 %0, %0, %1" : "+v"(pr[0][1]) : "v"(a1[0][2][1]) : ); SB();
  asm volatile("v_mfma_f32_16x16x32_bf16 %0, v[184:187], a[24:27], %0" : "+v"(g[0][2][0]) :  : ); SB();
  asm volatile("v_mul_f32 %0, %0, %1" : "+v"(pr[0][2]) : "v"(a1[0][2][2]) : ); SB();
  asm volatile("v_mfma_f32_16x16x32_bf16 %0, v[184:187], a[28:31], %0" : "+v"(g[0][3][0]) :  : ); SB();
  asm volatile("buffer_load_dwordx4 a[164:167], %0, %1, %2 offen offset:1024 sc1" :  : "v"(vO), "s"(rWS), "s"(0u) : "memory"); SB();
  asm volatile("v_mul_f32 %0, %0, %1" : "+v"(pr[0][3]) : "v"(a1[0][2][3]) : ); SB();
  asm volatile("v_mfma_f32_16x16x32_bf16 %0, v[188:191], a[16:19], %0" : "+v"(g[0][0][1]) :  : ); SB();
  asm volatile("v_mul_f32 %0, %0, %1" : "+v"(pr[1][0]) : "v"(a1[1][0][0]) : ); SB();
  asm volatile("v_mfma_f32_16x16x32_bf16 %0, v[188:191], a[20:23], %0" : "+v"(g[0][1][1]) :  : ); SB();
  asm volatile("v_mul_f32 %0, %0, %1" : "+v"(pr[1][1]) : "v"(a1[1][0][1]) : ); SB();
  asm volatile("v_mfma_f32_16x16x32_bf16 %0, v[188:191], a[24:27], %0" : "+v"(g[0][2][1]) :  : ); SB();
  asm volatile("buffer_load_dwordx4 a[168:171], %0, %1, %2 offen offset:2048 sc1" :  : "v"(vO), "s"(rWS), "s"(0u) : "memory"); SB();
  asm volatile("v_mul_f32 %0, %0, %1" : "+v"(pr[1][2]) : "v"(a1[1][0][2]) : ); SB();
  asm volatile("v_mfma_f32_16x16x32_bf16 %0, v[188:191], a[28:31], %0" : "+v"(g[0][3][1]) :  : ); SB();
  asm volatile("v_mul_f32 %0, %0, %1" : "+v"(pr[1][3]) : "v"(a1[1][0][3]) : ); SB();
  asm volatile("s_waitcnt vmcnt(19)" :  :  : "memory"); SB();
  asm volatile("v_mfma_f32_16x16x32_bf16 %0, v[192:195], a[32:35], %0" : "+v"(g[0][0][0]) :  : ); SB();
  asm volatile("v_cvt_pk_bf16_f32 %0, %1, %2" : "=v"(ho[1][0]) : "v"(pr[0][0]), "v"(pr[0][1]) : ); SB();
  asm volatile("v_mfma_f32_16x16x32_bf16 %0, v[192:195], a[36:39], %0" : "+v"(g[0][1][0]) :  : ); SB();
  asm volatile("v_cvt_pk_bf16_f32 %0, %1, %2" : "=v"(ho[1][1]) : "v"(pr[0][2]), "v"(pr[0][3]) : ); SB();
  asm volatile("v_mfma_f32_16x16x32_bf16 %0, v[192:195], a[40:43], %0" : "+v"(g[0][2][0]) :  : ); SB();
  asm volatile("buffer_load_dwordx4 a[172:175], %0, %1, %2 offen offset:3072 sc1" :  : "v"(vO), "s"(rWS), "s"(0u) : "memory"); SB();
  asm volatile("v_cvt_pk_bf16_f32 %0, %1, %2" : "=v"(ho[1][2]) : "v"(pr[1][0]), "v"(pr[1][1]) : ); SB();
  asm volatile("v_mfma_f32_16x16x32_bf16 %0, v[192:195], a[44:47], %0" : "+v"(g[0][3][0]) :  : ); SB();
  asm volatile("v_cvt_pk_bf16_f32 %0, %1, %2" : "=v"(ho[1][3]) : "v"(pr[1][2]), "v"(pr[1][3]) : ); SB();
  asm volatile("v_mfma_f32_16x16x32_bf16 %0, v[196:199], a[32:35], %0" : "+v"(g[0][0][1]) :  : ); SB();
  asm volatile("v_mul_f32 %0, %0, %1" : "+v"(a1[1][2][1]) : "v"(gt[1]) : ); SB();
  asm volatile("v_mfma_f32_16x16x32_bf16 %0, v[196:199], a[36:39], %0" : "+v"(g[0][1][1]) :  : ); SB();
  asm volatile("v_mul_f32 %0, %0, %1" : "+v"(a1[1][2][2]) : "v"(gt[2]) : ); SB();
  asm volatile("v_mfma_f32_16x16x32_bf16 %0, v[196:199], a[40:43], %0" : "+v"(g[0][2][1]) :  : ); SB();
  asm volatile("buffer_store_dwordx4 %1, %0, %2, %3 offen offset:1024 sc1\n\ts_nop 1" :  : "v"(vH_), "v"(ho[1]), "s"(rWS), "s"(0u) : "memory"); SB();
  asm volatile("v_mul_f32 %0, %0, %1" : "+v"(a1[1][2][3]) : "v"(gt[3]) : ); SB();
  asm volatile("v_mfma_f32_16x16x32_bf16 %0, v[196:199], a[44:47], %0" : "+v"(g[0][3][1]) :  : ); SB();
  asm volatile("s_waitcnt vmcnt(19)" :  :  : "memory"); SB();
  asm volatile("v_mfma_f32_16x16x32_bf16 %0, v[200:203], a[48:51], %0" : "+v"(g[0][0][0]) :  : ); SB();
  asm volatile("v_mfma_f32_16x16x32_bf16 %0, v[200:203], a[52:55], %0" : "+v"(g[0][1][0]) :  : ); SB();
  asm volatile("buffer_load_dwordx4 a[176:179], %0, %1, %2 offen offset:0 sc1" :  : "v"(vO), "s"(rWS), "s"(4096u) : "memory"); SB();
  asm volatile("v_mfma_f32_16x16x32_bf16 %0, v[200:203], a[56:59], %0" : "+v"(g[0][2][0]) :  : ); SB();
  asm volatile("v_mfma_f32_16x16x32_bf16 %0, v[200:203], a[60:63], %0" : "+v"(g[0][3][0]) :  : ); SB();
  asm volatile("v_mfma_f32_16x16x32_bf16 %0, v[204:207], a[48:51], %0" : "+v"(g[0][0][1]) :  : ); SB();
  asm volatile("buffer_load_dwordx4 a[180:183], %0, %1, %2 offen offset:1024 sc1" :  : "v"(vO), "s"(rWS), "s"(4096u) : "memory"); SB();
  asm volatile("v_mfma_f32_16x16x32_bf16 %0, v[204:207], a[52:55], %0" : "+v"(g[0][1][1]) :  : ); SB();
  asm volatile("v_mfma_f32_16x16x32_bf16 %0, v[204:207], a[56:59], %0" : "+v"(g[0][2][1]) :  : ); SB();
  asm volatile("v_mfma_f32_16x16x32_bf16 %0, v[204:207], a[60:63], %0" : "+v"(g[0][3][1]) :  : ); SB();
  asm volatile("s_waitcnt vmcnt(19)" :  :  : "memory"); SB();
  asm volatile("v_mfma_f32_16x16x32_bf16 %0, v[208:211], a[64:67], %0" : "+v"(g[0][0][0]) :  : ); SB();
  asm volatile("ds_read_b128 %0, %1 offset:0" : "=&v"(yq[0]) : "v"(ydr) : "memory"); SB();
  asm volatile("v_mfma_f32_16x16x32_bf16 %0, v[208:211], a[68:71], %0" : "+v"(g[0][1][0]) :  : ); SB();
  asm volatile("ds_read_b128 %0, %1 offset:256" : "=&v"(yq[1]) : "v"(ydr) : "memory"); SB();
  asm volatile("v_mfma_f32_16x16x32_bf16 %0, v[208:211], a[72:75], %0" : "+v"(g[0][2][0]) :  : ); SB();
  asm volatile("ds_read_b128 %0, %1 offset:8192" : "=&v"(yq[2]) : "v"(ydr) : "memory"); SB();
  asm volatile("v_mfma_f32_16x16x32_bf16 %0, v[208:211], a[76:79], %0" : "+v"(g[0][3][0]) :  : ); SB();
  asm volatile("ds_read_b128 %0, %1 offset:8448" : "=&v"(yq[3]) : "v"(ydr) : "memory"); SB();
  asm volatile("v_mfma_f32_16x16x32_bf16 %0, v[212:215], a[64:67], %0" : "+v"(g[0][0][1]) :  : ); SB();
  asm volatile("ds_read_b128 %0, %1 offset:16384" : "=&v"(yq[4]) : "v"(ydr) : "memory"); SB();
  asm volatile("v_mfma_f32_16x16x32_bf16 %0, v[212:215], a[68:71], %0" : "+v"(g[0][1][1]) :  : ); SB();
  asm volatile("ds_read_b128 %0, %1 offset:16640" : "=&v"(yq[5]) : "v"(ydr) : "memory"); SB();
  asm volatile("v_mfma_f32_16x16x32_bf16 %0, v[212:215], a[72:75], %0" : "+v"(g[0][2][1]) :  : ); SB();
  asm volatile("ds_read_b128 %0, %1 offset:24576" : "=&v"(yq[6]) : "v"(ydr) : "memory"); SB();
  asm volatile("v_mfma_f32_16x16x32_bf16 %0, v[212:215], a[76:79], %0" : "+v"(g[0][3][1]) :  : ); SB();
  asm volatile("ds_read_b128 %0, %1 offset:24832" : "=&v"(yq[7]) : "v"(ydr) : "memory"); SB();
  asm volatile("s_waitcnt vmcnt(17)" :  :  : "memory"); SB();
  asm volatile("v_mfma_f32_16x16x32_bf16 %0, v[216:219], a[80:83], %0" : "+v"(g[0][0][0]) :  : ); SB();
  asm volatile("ds_read_b128 %0, %1 offset:25856" : "=&v"(ykv) : "v"(ykr) : "memory"); SB();
  asm volatile("v_mfma_f32_16x16x32_bf16 %0, v[216:219], a[84:87], %0" : "+v"(g[0][1][0]) :  : ); SB();
  asm volatile("buffer_load_dwordx4 a[184:187], %0, %1, %2 offen offset:2048 sc1" :  : "v"(vO), "s"(rWS), "s"(4096u) : "memory"); SB();
  asm volatile("v_mfma_f32_16x16x32_bf16 %0, v[216:219], a[88:91], %0" : "+v"(g[0][2][0]) :  : ); SB();
  asm volatile("buffer_load_dwordx4 a[188:191], %0, %1, %2 offen offset:3072 sc1" :  : "v"(vO), "s"(rWS), "s"(4096u) : "memory"); SB();
  asm volatile("v_mfma_f32_16x16x32_bf16 %0, v[216:219], a[92:95], %0" : "+v"(g[0][3][0]) :  : ); SB();
  asm volatile("v_mfma_f32_16x16x32_bf16 %0, v[220:223], a[80:83], %0" : "+v"(g[0][0][1]) :  : ); SB();
  asm volatile("v_mfma_f32_16x16x32_bf16 %0, v[220:223], a[84:87], %0" : "+v"(g[0][1][1]) :  : ); SB();
  asm volatile("buffer_load_dwordx4 a[192:195], %0, %1, %2 offen offset:0 sc1" :  : "v"(vO), "s"(rWS), "s"(8192u) : "memory"); SB();
  asm volatile("v_mfma_f32_16x16x32_bf16 %0, v[220:223], a[88:91], %0" : "+v"(g[0][2][1]) :  : ); SB();
  asm volatile("v_mfma_f32_16x16x32_bf16 %0, v[220:223], a[92:95], %0" : "+v"(g[0][3][1]) :  : ); SB();
  asm volatile("s_waitcnt vmcnt(12)\n\ts_barrier" :  :  : "memory"); SB();
  asm volatile("s_waitcnt vmcnt(10)" :  :  : "memory"); SB();
  asm volatile("v_mfma_f32_16x16x32_bf16 %0, v[224:227], a[100:103], %0" : "+v"(g[0][1][0]) :  : ); SB();
  asm volatile("buffer_load_dwordx4 a[196:199], %0, %1, %2 offen offset:1024 sc1" :  : "v"(vO), "s"(rWS), "s"(8192u) : "memory"); SB();
  asm volatile("v_mfma_f32_16x16x32_bf16 %0, v[228:231], a[100:103], %0" : "+v"(g[0][1][1]) :  : ); SB();
  asm volatile("ds_read2_b32 a[64:65], %0 offset0:176 offset1:177" :  : "v"(aa[0]) : "memory"); SB();
  asm volatile("v_mfma_f32_16x16x32_bf16 %0, v[232:235], a[116:119], %0" : "+v"(g[0][1][0]) :  : ); SB();
  asm volatile("ds_read2_b32 a[66:67], %0 offset0:178 offset1:179" :  : "v"(aa[0]) : "memory"); SB();
  asm volatile("v_mfma_f32_16x16x32_bf16 %0, v[236:239], a[116:119], %0" : "+v"(g[0][1][1]) :  : ); SB();
  asm volatile("ds_read_b128 a[60:63], %0 offset:3776" :  : "v"(bb) : "memory"); SB();
  asm volatile("v_mfma_f32_16x16x32_bf16 %0, v[240:243], a[132:135], %0" : "+v"(g[0][1][0]) :  : ); SB();
  asm volatile("buffer_load_dwordx4 a[200:203], %0, %1, %2 offen offset:0 sc1" :  : "v"(vO2), "s"(rWS), "s"(0u) : "memory"); SB();
  asm volatile("v_mfma_f32_16x16x32_bf16 %0, v[244:247], a[132:135], %0" : "+v"(g[0][1][1]) :  : ); SB();
  asm volatile("ds_read_b128 a[56:59], %0 offset:3264" :  : "v"(bb) : "memory"); SB();
  asm volatile("v_mfma_f32_16x16x32_bf16 %0, v[248:251], a[148:151], %0" : "+v"(g[0][1][0]) :  : ); SB();
  asm volatile("ds_read_b128 a[52:55], %0 offset:2752" :  : "v"(bb) : "memory"); SB();
  asm volatile("v_mfma_f32_16x16x32_bf16 %0, v[252:255], a[148:151], %0" : "+v"(g[0][1][1]) :  : ); SB();
  asm volatile("buffer_load_dwordx4 a[204:207], %0, %1, %2 offen offset:1024 sc1" :  : "v"(vO2), "s"(rWS), "s"(0u) : "memory"); SB();
  asm volatile("v_mfma_f32_16x16x32_bf16 %0, v[224:227], a[104:107], %0" : "+v"(g[0][2][0]) :  : ); SB();
  asm volatile("ds_read_b128 a[48:51], %0 offset:2240" :  : "v"(bb) : "memory"); SB();
  asm volatile("v_mfma_f32_16x16x32_bf16 %0, v[228:231], a[104:107], %0" : "+v"(g[0][2][1]) :  : ); SB();
  asm volatile("ds_read2_b32 a[68:69], %0 offset0:48 offset1:49" :  : "v"(aa[0]) : "memory"); SB();
  asm volatile("v_mfma_f32_16x16x32_bf16 %0, v[232:235], a[120:123], %0" : "+v"(g[0][2][0]) :  : ); SB();
  asm volatile("ds_write_b128 %0, %1 offset:0" :  : "v"(pw2), "v"(g[0][1][0]) : "memory"); SB();
  asm volatile("v_mfma_f32_16x16x32_bf16 %0, v[236:239], a[120:123], %0" : "+v"(g[0][2][1]) :  : ); SB();
  asm volatile("buffer_load_dwordx4 a[208:211], %0, %1, %2 offen offset:2048 sc1" :  : "v"(vO2), "s"(rWS), "s"(0u) : "memory"); SB();
  asm volatile("v_mfma_f32_16x16x32_bf16 %0, v[240:243], a[136:139], %0" : "+v"(g[0][2][0]) :  : ); SB();
  asm volatile("ds_read2_b32 a[70:71], %0 offset0:50 offset1:51" :  : "v"(aa[0]) : "memory"); SB();
  asm volatile("v_mfma_f32_16x16x32_bf16 %0, v[244:247], a[136:139], %0" : "+v"(g[0][2][1]) :  : ); SB();
  asm volatile("buffer_load_dwordx4 a[212:215], %0, %1, %2 offen offset:3072 sc1" :  : "v"(vO2), "s"(rWS), "s"(0u) : "memory"); SB();
  asm volatile("v_mfma_f32_16x16x32_bf16 %0, v[248:251], a[152:155], %0" : "+v"(g[0][2][0]) :  : ); SB();
  asm volatile("ds_write_b128 %0, %1 offset:1024" :  : "v"(pw2), "v"(g[0][1][1]) : "memory"); SB();
  asm volatile("v_mfma_f32_16x16x32_bf16 %0, v[252:255], a[152:155], %0" : "+v"(g[0][2][1]) :  : ); SB();
  asm volatile("ds_read_b128 a[44:47], %0 offset:1728" :  : "v"(bb) : "memory"); SB();
  asm volatile("v_mfma_f32_16x16x32_bf16 %0, v[224:227], a[108:111], %0" : "+v"(g[0][3][0]) :  : ); SB();
  asm volatile("ds_read2_b32 a[72:73], %0 offset0:176 offset1:177" :  : "v"(aa[1]) : "memory"); SB();
  asm volatile("v_mfma_f32_16x16x32_bf16 %0, v[228:231], a[108:111], %0" : "+v"(g[0][3][1]) :  : ); SB();
  asm volatile("ds_read2_b32 a[74:75], %0 offset0:178 offset1:179" :  : "v"(aa[1]) : "memory"); SB();
  asm volatile("v_mfma_f32_16x16x32_bf16 %0, v[232:235], a[124:127], %0" : "+v"(g[0][3][0]) :  : ); SB();
  asm volatile("ds_write_b128 %0, %1 offset:2048" :  : "v"(pw2), "v"(g[0][2][0]) : "memory"); SB();
  asm volatile("v_mfma_f32_16x16x32_bf16 %0, v[236:239], a[124:127], %0" : "+v"(g[0][3][1]) :  : ); SB();
  asm volatile("ds_read2_b32 a[76:77], %0 offset0:48 offset1:49" :  : "v"(aa[1]) : "memory"); SB();
  asm volatile("v_mfma_f32_16x16x32_bf16 %0, v[240:243], a[140:143], %0" : "+v"(g[0][3][0]) :  : ); SB();
  asm volatile("ds_read2_b32 a[78:79], %0 offset0:50 offset1:51" :  : "v"(aa[1]) : "memory"); SB();
  asm volatile("v_mfma_f32_16x16x32_bf16 %0, v[244:247], a[140:143], %0" : "+v"(g[0][3][1]) :  : ); SB();
  asm volatile("ds_write_b128 %0, %1 offset:3072" :  : "v"(pw2), "v"(g[0][2][1]) : "memory"); SB();
  asm volatile("v_mfma_f32_16x16x32_bf16 %0, v[248:251], a[156:159], %0" : "+v"(g[0][3][0]) :  : ); SB();
  asm volatile("ds_read_b128 a[40:43], %0 offset:1216" :  : "v"(bb) : "memory"); SB();
  asm volatile("v_mfma_f32_16x16x32_bf16 %0, v[252:255], a[156:159], %0" : "+v"(g[0][3][1]) :  : ); SB();
  asm volatile("ds_read2_b32 a[80:81], %0 offset0:176 offset1:177" :  : "v"(aa[2]) : "memory"); SB();
  asm volatile("v_mfma_f32_16x16x32_bf16 %0, v[224:227], a[96:99], %0" : "+v"(g[0][0][0]) :  : ); SB();
  asm volatile("ds_read2_b32 a[82:83], %0 offset0:178 offset1:179" :  : "v"(aa[2]) : "memory"); SB();
  asm volatile("v_mfma_f32_16x16x32_bf16 %0, v[228:231], a[96:99], %0" : "+v"(g[0][0][1]) :  : ); SB();
  asm volatile("ds_read2_b32 a[84:85], %0 offset0:48 offset1:49" :  : "v"(aa[2]) : "memory"); SB();
  asm volatile("v_mfma_f32_16x16x32_bf16 %0, v[232:235], a[112:115], %0" : "+v"(g[0][0][0]) :  : ); SB();
  asm volatile("ds_write_b128 %0, %1 offset:4096" :  : "v"(pw2), "v"(g[0][3][0]) : "memory"); SB();
  asm volatile("v_mfma_f32_16x16x32_bf16 %0, v[236:239], a[112:115], %0" : "+v"(g[0][0][1]) :  : ); SB();
  asm volatile("ds_read2_b32 a[86:87], %0 offset0:50 offset1:51" :  : "v"(aa[2]) : "memory"); SB();
  asm volatile("v_mfma_f32_16x16x32_bf16 %0, v[240:243], a[128:131], %0" : "+v"(g[0][0][0]) :  : ); SB();
  asm volatile("ds_read_b128 a[36:39], %0 offset:704" :  : "v"(bb) : "memory"); SB();
  asm volatile("s_waitcnt vmcnt(10)\n\ts_mov_b64 exec, 1\n\tbuffer_store_dwordx2 %0, off, %1, %2 sc1\n\ts_mov_b64 exec, -1" :  : "v"(((u32x2){tg0, tg1})), "s"(rWS), "s"(sFH) : "memory"); SB();
  asm volatile("v_mfma_f32_16x16x32_bf16 %0, v[244:247], a[128:131], %0" : "+v"(g[0][0][1]) :  : ); SB();
  asm volatile("buffer_load_dwordx4 a[216:219], %0, %1, %2 offen offset:0 sc1" :  : "v"(vO2), "s"(rWS), "s"(4096u) : "memory"); SB();
  asm volatile("v_mfma_f32_16x16x32_bf16 %0, v[248:251], a[144:147], %0" : "+v"(g[0][0][0]) :  : ); SB();
  asm volatile("ds_write_b128 %0, %1 offset:5120" :  : "v"(pw2), "v"(g[0][3][1]) : "memory"); SB();
  asm volatile("v_mfma_f32_16x16x32_bf16 %0, v[252:255], a[144:147], %0" : "+v"(g[0][0][1]) :  : ); SB();
  asm volatile("buffer_load_dwordx4 a[220:223], %0, %1, %2 offen offset:1024 sc1" :  : "v"(vO2), "s"(rWS), "s"(4096u) : "memory"); SB();
  if ((rb | cb | w) == 0) asm volatile("buffer_store_dwordx2 %0, off, %1, 0 sc1" :: "v"(((u32x2){tg0, tg1})), "s"(rWS) : "memory");
  SB();
  ST(17);
  unsigned pw3, p3[3], yw, ys[2], vYr, ya, po;
  asm volatile("s_waitcnt lgkmcnt(4)\n\tv_mfma_f32_16x16x32_bf16 %0, a[64:67], a[60:63], 0" : "=&v"(ac[7]) :  : ); SB();
  asm volatile("ds_read2_b32 a[88:89], %0 offset0:176 offset1:177" :  : "v"(aa[3]) : "memory"); SB();
  asm volatile("" : "+v"(yq[0]), "+v"(yq[1]), "+v"(yq[2]), "+v"(yq[3]), "+v"(yq[4]), "+v"(yq[5]), "+v"(yq[6]), "+v"(yq[7]), "+v"(ykv) :  : "memory"); SB();
  asm volatile("v_mfma_f32_16x16x32_bf16 %0, a[64:67], a[56:59], 0" : "=&v"(ac[6]) :  : ); SB();
  asm volatile("ds_read2_b32 a[90:91], %0 offset0:178 offset1:179" :  : "v"(aa[3]) : "memory"); SB();
  asm volatile("v_add_f32 %0, %0, %1" : "+v"(yq[0][0]) : "v"(yq[2][0]) : ); SB();
  asm volatile("v_mfma_f32_16x16x32_bf16 %0, a[64:67], a[52:55], 0" : "=&v"(ac[5]) :  : ); SB();
  asm volatile("ds_read2_b32 a[92:93], %0 offset0:48 offset1:49" :  : "v"(aa[3]) : "memory"); SB();
  asm volatile("v_add_f32 %0, %0, %1" : "+v"(yq[0][1]) : "v"(yq[2][1]) : ); SB();
  asm volatile("v_mfma_f32_16x16x32_bf16 %0, a[64:67], a[48:51], 0" : "=&v"(ac[4]) :  : ); SB();
  asm volatile("ds_read2_b32 a[94:95], %0 offset0:50 offset1:51" :  : "v"(aa[3]) : "memory"); SB();
  asm volatile("v_add_f32 %0, %0, %1" : "+v"(yq[0][2]) : "v"(yq[2][2]) : ); SB();
  asm volatile("v_mfma_f32_16x16x32_bf16 %0, a[68:71], a[56:59], %0" : "+v"(ac[7]) :  : ); SB();
  asm volatile("ds_read_b128 a[32:35], %0 offset:192" :  : "v"(bb) : "memory"); SB();
  asm volatile("s_waitcnt lgkmcnt(5)\n\ts_barrier" :  :  : "memory"); SB();
  asm volatile("v_mfma_f32_16x16x32_bf16 %0, a[68:71], a[52:55], %0" : "+v"(ac[6]) :  : ); SB();
  asm volatile("ds_read2_b32 a[128:129], %0 offset0:160 offset1:161" :  : "v"(aa[0]) : "memory"); SB();
  asm volatile("buffer_load_dwordx4 a[224:227], %0, %1, %2 offen offset:2048 sc1" :  : "v"(vO2), "s"(rWS), "s"(4096u) : "memory"); SB();
  asm volatile("v_mfma_f32_16x16x32_bf16 %0, a[68:71], a[48:51], %0" : "+v"(ac[5]) :  : ); SB();
  asm volatile("ds_read2_b32 a[130:131], %0 offset0:162 offset1:163" :  : "v"(aa[0]) : "memory"); SB();
  asm volatile("v_add_f32 %0, %0, %1" : "+v"(yq[0][3]) : "v"(yq[2][3]) : ); SB();
  asm volatile("v_mfma_f32_16x16x32_bf16 %0, a[68:71], a[44:47], %0" : "+v"(ac[4]) :  : ); SB();
  asm volatile("ds_read_b128 %0, %1 offset:0" : "=&v"(pr[0]) : "v"(pe[0]) : "memory"); SB();
  asm volatile("v_add_f32 %0, %0, %1" : "+v"(yq[1][0]) : "v"(yq[3][0]) : ); SB();
  asm volatile("v_mfma_f32_16x16x32_bf16 %0, a[72:75], a[52:55], %0" : "+v"(ac[7]) :  : ); SB();
  asm volatile("ds_read_b128 a[124:127], %0 offset:3712" :  : "v"(bb) : "memory"); SB();
  asm volatile("buffer_load_dwordx4 a[228:231], %0, %1, %2 offen offset:3072 sc1" :  : "v"(vO2), "s"(rWS), "s"(4096u) : "memory"); SB();
  asm volatile("v_mfma_f32_16x16x32_bf16 %0, a[72:75], a[48:51], %0" : "+v"(ac[6]) :  : ); SB();
  asm volatile("ds_read_b128 a[120:123], %0 offset:3200" :  : "v"(bb) : "memory"); SB();
  asm volatile("v_add_f32 %0, %0, %1" : "+v"(yq[1][1]) : "v"(yq[3][1]) : ); SB();
  asm volatile("v_mfma_f32_16x16x32_bf16 %0, a[72:75], a[44:47], %0" : "+v"(ac[5]) :  : ); SB();
  asm volatile("ds_read_b128 a[116:119], %0 offset:2688" :  : "v"(bb) : "memory"); SB();
  asm volatile("v_add_f32 %0, %0, %1" : "+v"(yq[1][2]) : "v"(yq[3][2]) : ); SB();
  asm volatile("v_mfma_f32_16x16x32_bf16 %0, a[64:67], a[44:47], 0" : "=&v"(ac[3]) :  : ); SB();
  asm volatile("ds_read_b128 %0, %1 offset:1024" : "=&v"(pr[1]) : "v"(pe[0]) : "memory"); SB();
  asm volatile("v_add_f32 %0, %0, %1" : "+v"(yq[1][3]) : "v"(yq[3][3]) : ); SB();
  asm volatile("v_mfma_f32_16x16x32_bf16 %0, a[76:79], a[48:51], %0" : "+v"(ac[7]) :  : ); SB();
  asm volatile("ds_read_b128 a[112:115], %0 offset:2176" :  : "v"(bb) : "memory"); SB();
  asm volatile("buffer_load_dwordx4 a[232:235], %0, %1, %2 offen offset:0 sc1" :  : "v"(vO2), "s"(rWS), "s"(8192u) : "memory"); SB();
  asm volatile("v_mfma_f32_16x16x32_bf16 %0, a[76:79], a[44:47], %0" : "+v"(ac[6]) :  : ); SB();
  asm volatile("ds_read2_b32 a[132:133], %0 offset0:32 offset1:33" :  : "v"(aa[0]) : "memory"); SB();
  asm volatile("v_add_f32 %0, %0, %1" : "+v"(yq[4][0]) : "v"(yq[6][0]) : ); SB();
  asm volatile("v_mfma_f32_16x16x32_bf16 %0, a[76:79], a[40:43], %0" : "+v"(ac[5]) :  : ); SB();
  asm volatile("ds_read2_b32 a[134:135], %0 offset0:34 offset1:35" :  : "v"(aa[0]) : "memory"); SB();
  asm volatile("buffer_load_dwordx4 a[236:239], %0, %1, %2 offen offset:1024 sc1" :  : "v"(vO2), "s"(rWS), "s"(8192u) : "memory"); SB();
  asm volatile("v_mfma_f32_16x16x32_bf16 %0, a[72:75], a[40:43], %0" : "+v"(ac[4]) :  : ); SB();
  asm volatile("ds_read_b128 %0, %1 offset:2048" : "=&v"(pr[2]) : "v"(pe[1]) : "memory"); SB();
  asm volatile("v_add_f32 %0, %0, %1" : "+v"(yq[4][1]) : "v"(yq[6][1]) : ); SB();
  asm volatile("v_mfma_f32_16x16x32_bf16 %0, a[80:83], a[44:47], %0" : "+v"(ac[7]) :  : ); SB();
  asm volatile("ds_read_b128 a[108:111], %0 offset:1664" :  : "v"(bb) : "memory"); SB();
  asm volatile("v_add_f32 %0, %0, %1" : "+v"(yq[4][2]) : "v"(yq[6][2]) : ); SB();
  asm volatile("v_mfma_f32_16x16x32_bf16 %0, a[80:83], a[40:43], %0" : "+v"(ac[6]) :  : ); SB();
  asm volatile("ds_read2_b32 a[136:137], %0 offset0:160 offset1:161" :  : "v"(aa[1]) : "memory"); SB();
  asm volatile("v_add_f32 %0, %0, %1" : "+v"(yq[4][3]) : "v"(yq[6][3]) : ); SB();
  asm volatile("v_mfma_f32_16x16x32_bf16 %0, a[68:71], a[40:43], %0" : "+v"(ac[3]) :  : ); SB();
  asm volatile("ds_read2_b32 a[138:139], %0 offset0:162 offset1:163" :  : "v"(aa[1]) : "memory"); SB();
  asm volatile("buffer_load_dwordx4 a[240:243], %0, %1, %2 offen offset:2048 sc1" :  : "v"(vO), "s"(rWS), "s"(8192u) : "memory"); SB();
  asm volatile("v_mfma_f32_16x16x32_bf16 %0, a[64:67], a[40:43], 0" : "=&v"(ac[2]) :  : ); SB();
  asm volatile("ds_read_b128 %0, %1 offset:3072" : "=&v"(pr[3]) : "v"(pe[1]) : "memory"); SB();
  asm volatile("v_add_f32 %0, %0, %1" : "+v"(yq[5][0]) : "v"(yq[7][0]) : ); SB();
  asm volatile("v_mfma_f32_16x16x32_bf16 %0, a[84:87], a[40:43], %0" : "+v"(ac[7]) :  : ); SB();
  asm volatile("ds_read2_b32 a[140:141], %0 offset0:32 offset1:33" :  : "v"(aa[1]) : "memory"); SB();
  asm volatile("v_add_f32 %0, %0, %1" : "+v"(yq[5][1]) : "v"(yq[7][1]) : ); SB();
  asm volatile("v_mfma_f32_16x16x32_bf16 %0, a[84:87], a[36:39], %0" : "+v"(ac[6]) :  : ); SB();
  asm volatile("ds_read2_b32 a[142:143], %0 offset0:34 offset1:35" :  : "v"(aa[1]) : "memory"); SB();
  asm volatile("buffer_load_dwordx4 a[244:247], %0, %1, %2 offen offset:3072 sc1" :  : "v"(vO), "s"(rWS), "s"(8192u) : "memory"); SB();
  asm volatile("v_mfma_f32_16x16x32_bf16 %0, a[80:83], a[36:39], %0" : "+v"(ac[5]) :  : ); SB();
  asm volatile("ds_read_b128 a[104:107], %0 offset:1152" :  : "v"(bb) : "memory"); SB();
  asm volatile("v_add_f32 %0, %0, %1" : "+v"(yq[5][2]) : "v"(yq[7][2]) : ); SB();
  asm volatile("v_mfma_f32_16x16x32_bf16 %0, a[76:79], a[36:39], %0" : "+v"(ac[4]) :  : ); SB();
  asm volatile("ds_read_b128 %0, %1 offset:4096" : "=&v"(pr[4]) : "v"(pe[2]) : "memory"); SB();
  asm volatile("s_waitcnt lgkmcnt(8)\n\tv_mfma_f32_16x16x32_bf16 %0, a[88:91], a[36:39], %0" : "+v"(ac[7]) :  : ); SB();
  asm volatile("ds_read2_b32 a[144:145], %0 offset0:160 offset1:161" :  : "v"(aa[2]) : "memory"); SB();
  asm volatile("buffer_load_dwordx4 a[248:251], %0, %1, %2 offen offset:0 sc1" :  : "v"(vO), "s"(rWS), "s"(12288u) : "memory"); SB();
  asm volatile("v_mfma_f32_16x16x32_bf16 %0, a[72:75], a[36:39], %0" : "+v"(ac[3]) :  : ); SB();
  asm volatile("ds_read2_b32 a[146:147], %0 offset0:162 offset1:163" :  : "v"(aa[2]) : "memory"); SB();
  asm volatile("v_add_f32 %0, %0, %1" : "+v"(yq[5][3]) : "v"(yq[7][3]) : ); SB();
  asm volatile("v_mfma_f32_16x16x32_bf16 %0, a[68:71], a[36:39], %0" : "+v"(ac[2]) :  : ); SB();
  asm volatile("ds_read2_b32 a[148:149], %0 offset0:32 offset1:33" :  : "v"(aa[2]) : "memory"); SB();
  asm volatile("v_add_f32 %0, %0, %1" : "+v"(yq[0][0]) : "v"(yq[4][0]) : ); SB();
  asm volatile("v_mfma_f32_16x16x32_bf16 %0, a[64:67], a[36:39], 0" : "=&v"(ac[1]) :  : ); SB();
  asm volatile("ds_read_b128 %0, %1 offset:5120" : "=&v"(pr[5]) : "v"(pe[2]) : "memory"); SB();
  asm volatile("v_add_f32 %0, %0, %1" : "+v"(yq[0][1]) : "v"(yq[4][1]) : ); SB();
  asm volatile("v_mfma_f32_16x16x32_bf16 %0, a[92:95], a[32:35], %0" : "+v"(ac[7]) :  : ); SB();
  asm volatile("ds_read2_b32 a[150:151], %0 offset0:34 offset1:35" :  : "v"(aa[2]) : "memory"); SB();
  asm volatile("v_add_f32 %0, %0, %1" : "+v"(yq[0][2]) : "v"(yq[4][2]) : ); SB();
  asm volatile("v_mfma_f32_16x16x32_bf16 %0, a[88:91], a[32:35], %0" : "+v"(ac[6]) :  : ); SB();
  asm volatile("ds_read_b128 a[100:103], %0 offset:640" :  : "v"(bb) : "memory"); SB();
  asm volatile("v_add_f32 %0, %0, %1" : "+v"(yq[0][3]) : "v"(yq[4][3]) : ); SB();
  asm volatile("v_mfma_f32_16x16x32_bf16 %0, a[84:87], a[32:35], %0" : "+v"(ac[5]) :  : ); SB();
  asm volatile("ds_read2_b32 a[152:153], %0 offset0:160 offset1:161" :  : "v"(aa[3]) : "memory"); SB();
  asm volatile("v_add_f32 %0, %0, %1" : "+v"(yq[1][0]) : "v"(yq[5][0]) : ); SB();
  asm volatile("v_mfma_f32_16x16x32_bf16 %0, a[80:83], a[32:35], %0" : "+v"(ac[4]) :  : ); SB();
  pw3 = lb + 98304 + w * 15360 + l16; asm volatile("" : "+v"(pw3)); SB();
  asm volatile("v_mfma_f32_16x16x32_bf16 %0, a[76:79], a[32:35], %0" : "+v"(ac[3]) :  : ); SB();
  asm volatile("ds_read2_b32 a[154:155], %0 offset0:162 offset1:163" :  : "v"(aa[3]) : "memory"); SB();
  asm volatile("v_add_f32 %0, %0, %1" : "+v"(yq[1][1]) : "v"(yq[5][1]) : ); SB();
  asm volatile("v_mfma_f32_16x16x32_bf16 %0, a[72:75], a[32:35], %0" : "+v"(ac[2]) :  : ); SB();
  asm volatile("ds_read2_b32 a[156:157], %0 offset0:32 offset1:33" :  : "v"(aa[3]) : "memory"); SB();
  asm volatile("s_waitcnt lgkmcnt(5)" : "+v"(pr[0]), "+v"(pr[1]), "+v"(pr[2]), "+v"(pr[3]), "+v"(pr[4]), "+v"(pr[5]) :  : "memory"); SB();
  asm volatile("v_mfma_f32_16x16x32_bf16 %0, a[68:71], a[32:35], %0" : "+v"(ac[1]) :  : ); SB();
  asm volatile("ds_read2_b32 a[158:159], %0 offset0:34 offset1:35" :  : "v"(aa[3]) : "memory"); SB();
  asm volatile("v_add_f32 %0, %0, %1" : "+v"(yq[1][2]) : "v"(yq[5][2]) : ); SB();
  asm volatile("v_mfma_f32_16x16x32_bf16 %0, a[64:67], a[32:35], 0" : "=&v"(ac[0]) :  : ); SB();
  {
    const f32x4 h0 = ((g[0][0][0] + pr[0]) + (pr[2] + pr[4])) * a1[1][1], h1 = ((g[0][0][1] + pr[1]) + (pr[3] + pr[5])) * a1[1][2];
    ho[2] = (u32x4){pack2(h0[0], h0[1]), pack2(h0[2], h0[3]), pack2(h1[0], h1[1]), nohole(pack2(h1[2], h1[3]))};
    SB();
  }
  asm volatile("v_mfma_f32_16x16x32_bf16 %0, a[128:131], a[124:127], %0" : "+v"(ac[7]) :  : ); SB();
  asm volatile("ds_read_b128 a[96:99], %0 offset:128" :  : "v"(bb) : "memory"); SB();
  asm volatile("buffer_load_dwordx4 a[252:255], %0, %1, %2 offen offset:1024 sc1" :  : "v"(vO), "s"(rWS), "s"(12288u) : "memory"); SB();
  asm volatile("v_mfma_f32_16x16x32_bf16 %0, a[128:131], a[120:123], %0" : "+v"(ac[6]) :  : ); SB();
  asm volatile("ds_read2_b32 a[64:65], %0 offset0:144 offset1:145" :  : "v"(aa[0]) : "memory"); SB();
  asm volatile("v_add_f32 %0, %0, %1" : "+v"(yq[1][3]) : "v"(yq[5][3]) : ); SB();
  asm volatile("v_mfma_f32_16x16x32_bf16 %0, a[128:131], a[116:119], %0" : "+v"(ac[5]) :  : ); SB();
  asm volatile("ds_read2_b32 a[66:67], %0 offset0:146 offset1:147" :  : "v"(aa[0]) : "memory"); SB();
  asm volatile("buffer_load_dwordx4 a[0:3], %0, %1, %2 offen offset:2048 sc1" :  : "v"(vO), "s"(rWS), "s"(12288u) : "memory"); SB();
  asm volatile("v_mfma_f32_16x16x32_bf16 %0, a[128:131], a[112:115], %0" : "+v"(ac[4]) :  : ); SB();
  asm volatile("buffer_store_dwordx4 %1, %0, %2, %3 offen offset:2048 sc1\n\ts_nop 1" :  : "v"(vH_), "v"(ho[2]), "s"(rWS), "s"(0u) : "memory"); SB();
  asm volatile("v_mfma_f32_16x16x32_bf16 %0, a[132:135], a[120:123], %0" : "+v"(ac[7]) :  : ); SB();
  asm volatile("ds_read_b128 a[60:63], %0 offset:3648" :  : "v"(bb) : "memory"); SB();
  asm volatile("v_lshlrev_b32 %0, 16, %1" : "=v"(yq[2][0]) : "v"(ykv[0]) : ); SB();
  asm volatile("v_mfma_f32_16x16x32_bf16 %0, a[132:135], a[116:119], %0" : "+v"(ac[6]) :  : ); SB();
  asm volatile("ds_read_b128 a[56:59], %0 offset:3136" :  : "v"(bb) : "memory"); SB();
  asm volatile("v_and_b32 %0, 0xffff0000, %1" : "=v"(yq[2][1]) : "v"(ykv[0]) : ); SB();
  asm volatile("v_mfma_f32_16x16x32_bf16 %0, a[132:135], a[112:115], %0" : "+v"(ac[5]) :  : ); SB();
  asm volatile("ds_read_b128 a[52:55], %0 offset:2624" :  : "v"(bb) : "memory"); SB();
  asm volatile("v_lshlrev_b32 %0, 16, %1" : "=v"(yq[2][2]) : "v"(ykv[1]) : ); SB();
  asm volatile("v_mfma_f32_16x16x32_bf16 %0, a[132:135], a[108:111], %0" : "+v"(ac[4]) :  : ); SB();
  p3[0] = lb + 98304 + (w ^ 1) * 15360 + l16; asm volatile("" : "+v"(p3[0])); SB();
  asm volatile("v_mfma_f32_16x16x32_bf16 %0, a[136:139], a[116:119], %0" : "+v"(ac[7]) :  : ); SB();
  asm volatile("ds_read_b128 a[48:51], %0 offset:2112" :  : "v"(bb) : "memory"); SB();
  asm volatile("v_and_b32 %0, 0xffff0000, %1" : "=v"(yq[2][3]) : "v"(ykv[1]) : ); SB();
  asm volatile("v_mfma_f32_16x16x32_bf16 %0, a[136:139], a[112:115], %0" : "+v"(ac[6]) :  : ); SB();
  asm volatile("ds_read2_b32 a[68:69], %0 offset0:16 offset1:17" :  : "v"(aa[0]) : "memory"); SB();
  asm volatile("v_lshlrev_b32 %0, 16, %1" : "=v"(yq[3][0]) : "v"(ykv[2]) : ); SB();
  asm volatile("v_mfma_f32_16x16x32_bf16 %0, a[136:139], a[108:111], %0" : "+v"(ac[5]) :  : ); SB();
  asm volatile("ds_read2_b32 a[70:71], %0 offset0:18 offset1:19" :  : "v"(aa[0]) : "memory"); SB();
  asm volatile("v_and_b32 %0, 0xffff0000, %1" : "=v"(yq[3][1]) : "v"(ykv[2]) : ); SB();
  asm volatile("v_mfma_f32_16x16x32_bf16 %0, a[128:131], a[108:111], %0" : "+v"(ac[3]) :  : ); SB();
  p3[1] = lb + 98304 + (w ^ 2) * 15360 + l16; asm volatile("" : "+v"(p3[1])); SB();
  asm volatile("v_mfma_f32_16x16x32_bf16 %0, a[140:143], a[112:115], %0" : "+v"(ac[7]) :  : ); SB();
  asm volatile("ds_read_b128 a[44:47], %0 offset:1600" :  : "v"(bb) : "memory"); SB();
  asm volatile("buffer_load_dwordx4 a[4:7], %0, %1, %2 offen offset:2048 sc1" :  : "v"(vO2), "s"(rWS), "s"(8192u) : "memory"); SB();
  asm volatile("v_mfma_f32_16x16x32_bf16 %0, a[140:143], a[108:111], %0" : "+v"(ac[6]) :  : ); SB();
  asm volatile("ds_read2_b32 a[72:73], %0 offset0:144 offset1:145" :  : "v"(aa[1]) : "memory"); SB();
  asm volatile("v_lshlrev_b32 %0, 16, %1" : "=v"(yq[3][2]) : "v"(ykv[3]) : ); SB();
  asm volatile("v_mfma_f32_16x16x32_bf16 %0, a[140:143], a[104:107], %0" : "+v"(ac[5]) :  : ); SB();
  asm volatile("ds_read2_b32 a[74:75], %0 offset0:146 offset1:147" :  : "v"(aa[1]) : "memory"); SB();
  asm volatile("v_and_b32 %0, 0xffff0000, %1" : "=v"(yq[3][3]) : "v"(ykv[3]) : ); SB();
  asm volatile("v_mfma_f32_16x16x32_bf16 %0, a[136:139], a[104:107], %0" : "+v"(ac[4]) :  : ); SB();
  p3[2] = lb + 98304 + (w ^ 3) * 15360 + l16; asm volatile("" : "+v"(p3[2])); SB();
  asm volatile("v_mfma_f32_16x16x32_bf16 %0, a[144:147], a[108:111], %0" : "+v"(ac[7]) :  : ); SB();
  asm volatile("ds_read2_b32 a[76:77], %0 offset0:16 offset1:17" :  : "v"(aa[1]) : "memory"); SB();
  asm volatile("buffer_load_dwordx4 a[8:11], %0, %1, %2 offen offset:3072 sc1" :  : "v"(vO2), "s"(rWS), "s"(8192u) : "memory"); SB();
  asm volatile("v_mfma_f32_16x16x32_bf16 %0, a[144:147], a[104:107], %0" : "+v"(ac[6]) :  : ); SB();
  asm volatile("ds_read2_b32 a[78:79], %0 offset0:18 offset1:19" :  : "v"(aa[1]) : "memory"); SB();
  asm volatile("v_fmac_f32 %0, %1, %2" : "+v"(yq[0][0]) : "v"(yq[2][0]), "v"(dD[2]) : ); SB();
  asm volatile("v_mfma_f32_16x16x32_bf16 %0, a[132:135], a[104:107], %0" : "+v"(ac[3]) :  : ); SB();
  asm volatile("ds_read_b128 a[40:43], %0 offset:1088" :  : "v"(bb) : "memory"); SB();
  asm volatile("buffer_load_dwordx4 a[12:15], %0, %1, %2 offen offset:0 sc1" :  : "v"(vO2), "s"(rWS), "s"(12288u) : "memory"); SB();
  asm volatile("v_mfma_f32_16x16x32_bf16 %0, a[128:131], a[104:107], %0" : "+v"(ac[2]) :  : ); SB();
  yw = (lane ^ ((lane >> 3) & 6)) * 16; asm volatile("" : "+v"(yw)); SB();
  asm volatile("s_waitcnt lgkmcnt(6)\n\tv_mfma_f32_16x16x32_bf16 %0, a[148:151], a[104:107], %0" : "+v"(ac[7]) :  : ); SB();
  asm volatile("ds_read2_b32 a[80:81], %0 offset0:144 offset1:145" :  : "v"(aa[2]) : "memory"); SB();
  asm volatile("v_fmac_f32 %0, %1, %2" : "+v"(yq[0][1]) : "v"(yq[2][1]), "v"(dD[2]) : ); SB();
  asm volatile("v_mfma_f32_16x16x32_bf16 %0, a[148:151], a[100:103], %0" : "+v"(ac[6]) :  : ); SB();
  asm volatile("ds_read2_b32 a[82:83], %0 offset0:146 offset1:147" :  : "v"(aa[2]) : "memory"); SB();
  asm volatile("buffer_load_dwordx4 a[16:19], %0, %1, %2 offen offset:1024 sc1" :  : "v"(vO2), "s"(rWS), "s"(12288u) : "memory"); SB();
  asm volatile("v_mfma_f32_16x16x32_bf16 %0, a[144:147], a[100:103], %0" : "+v"(ac[5]) :  : ); SB();
  asm volatile("ds_read2_b32 a[84:85], %0 offset0:16 offset1:17" :  : "v"(aa[2]) : "memory"); SB();
  asm volatile("v_fmac_f32 %0, %1, %2" : "+v"(yq[0][2]) : "v"(yq[2][2]), "v"(dD[2]) : ); SB();
  asm volatile("v_mfma_f32_16x16x32_bf16 %0, a[140:143], a[100:103], %0" : "+v"(ac[4]) :  : ); SB();
  yw = yw + lb + 32768 + w * 3072; asm volatile("" : "+v"(yw)); SB();
  asm volatile("v_mfma_f32_16x16x32_bf16 %0, a[152:155], a[100:103], %0" : "+v"(ac[7]) :  : ); SB();
  asm volatile("ds_read2_b32 a[86:87], %0 offset0:18 offset1:19" :  : "v"(aa[2]) : "memory"); SB();
  asm volatile("v_fmac_f32 %0, %1, %2" : "+v"(yq[0][3]) : "v"(yq[2][3]), "v"(dD[2]) : ); SB();
  asm volatile("v_mfma_f32_16x16x32_bf16 %0, a[136:139], a[100:103], %0" : "+v"(ac[3]) :  : ); SB();
  asm volatile("ds_read_b128 a[36:39], %0 offset:576" :  : "v"(bb) : "memory"); SB();
  asm volatile("buffer_load_dwordx2 %0, %1, %2, %3 offen sc1" : "=&v"(fh) : "v"(vFH), "s"(rWS), "s"(0u) : "memory"); SB();
  asm volatile("v_mfma_f32_16x16x32_bf16 %0, a[132:135], a[100:103], %0" : "+v"(ac[2]) :  : ); SB();
  asm volatile("ds_read2_b32 a[88:89], %0 offset0:144 offset1:145" :  : "v"(aa[3]) : "memory"); SB();
  asm volatile("buffer_load_dwordx4 a[20:23], %0, %1, %2 offen offset:2048 sc1" :  : "v"(vO2), "s"(rWS), "s"(12288u) : "memory"); SB();
  asm volatile("v_mfma_f32_16x16x32_bf16 %0, a[128:131], a[100:103], %0" : "+v"(ac[1]) :  : ); SB();
  ya = lb + 32768 + (4 * g4 + q4) * 128 + 8 * p4; asm volatile("" : "+v"(ya)); SB();
  asm volatile("v_mfma_f32_16x16x32_bf16 %0, a[156:159], a[96:99], %0" : "+v"(ac[7]) :  : ); SB();
  asm volatile("ds_read2_b32 a[90:91], %0 offset0:146 offset1:147" :  : "v"(aa[3]) : "memory"); SB();
  asm volatile("v_fmac_f32 %0, %1, %2" : "+v"(yq[1][0]) : "v"(yq[3][0]), "v"(dD[2]) : ); SB();
  asm volatile("v_mfma_f32_16x16x32_bf16 %0, a[152:155], a[96:99], %0" : "+v"(ac[6]) :  : ); SB();
  asm volatile("ds_read2_b32 a[92:93], %0 offset0:16 offset1:17" :  : "v"(aa[3]) : "memory"); SB();
  asm volatile("v_fmac_f32 %0, %1, %2" : "+v"(yq[1][1]) : "v"(yq[3][1]), "v"(dD[2]) : ); SB();
  asm volatile("v_mfma_f32_16x16x32_bf16 %0, a[148:151], a[96:99], %0" : "+v"(ac[5]) :  : ); SB();
  asm volatile("ds_read2_b32 a[94:95], %0 offset0:18 offset1:19" :  : "v"(aa[3]) : "memory"); SB();
  asm volatile("v_fmac_f32 %0, %1, %2" : "+v"(yq[1][2]) : "v"(yq[3][2]), "v"(dD[2]) : ); SB();
  asm volatile("v_mfma_f32_16x16x32_bf16 %0, a[144:147], a[96:99], %0" : "+v"(ac[4]) :  : ); SB();
  po = (2 * g4 + (q4 >> 1)) & 3; asm volatile("" : "+v"(po)); SB();
  asm volatile("v_mfma_f32_16x16x32_bf16 %0, a[140:143], a[96:99], %0" : "+v"(ac[3]) :  : ); SB();
  asm volatile("ds_read_b128 a[32:35], %0 offset:64" :  : "v"(bb) : "memory"); SB();
  asm volatile("v_fmac_f32 %0, %1, %2" : "+v"(yq[1][3]) : "v"(yq[3][3]), "v"(dD[2]) : ); SB();
  asm volatile("v_mfma_f32_16x16x32_bf16 %0, a[136:139], a[96:99], %0" : "+v"(ac[2]) :  : ); SB();
  ya = ya + ((w ^ po) << 5); asm volatile("" : "+v"(ya)); SB();
  asm volatile("v_mfma_f32_16x16x32_bf16 %0, a[132:135], a[96:99], %0" : "+v"(ac[1]) :  : ); SB();
  po = vX + 24 * (g4 & 1); asm volatile("" : "+v"(po)); SB();
  asm volatile("v_mfma_f32_16x16x32_bf16 %0, a[128:131], a[96:99], %0" : "+v"(ac[0]) :  : ); SB();
  asm volatile("v_cvt_pk_bf16_f32 %0, %1, %2" : "=v"(y2[0]) : "v"(yq[0][0]), "v"(yq[0][1]) : ); SB();
  asm volatile("v_mfma_f32_16x16x32_bf16 %0, a[64:67], a[60:63], %0" : "+v"(ac[7]) :  : ); SB();
  asm volatile("ds_read2_b32 a[128:129], %0 offset0:128 offset1:129" :  : "v"(aa[0]) : "memory"); SB();
  asm volatile("global_load_dwordx2 %0, %1, %2 offset:0" : "=&v"(xv[0]) : "v"(vX), "s"(x) : "memory"); SB();
  asm volatile("v_mfma_f32_16x16x32_bf16 %0, a[64:67], a[56:59], %0" : "+v"(ac[6]) :  : ); SB();
  asm volatile("ds_read2_b32 a[130:131], %0 offset0:130 offset1:131" :  : "v"(aa[0]) : "memory"); SB();
  asm volatile("v_cvt_pk_bf16_f32 %0, %1, %2" : "=v"(y2[1]) : "v"(yq[0][2]), "v"(yq[0][3]) : ); SB();
  asm volatile("v_mfma_f32_16x16x32_bf16 %0, a[64:67], a[52:55], %0" : "+v"(ac[5]) :  : ); SB();
  asm volatile("ds_read_b128 a[124:127], %0 offset:3584" :  : "v"(bb) : "memory"); SB();
  asm volatile("v_cvt_pk_bf16_f32 %0, %1, %2" : "=v"(y2[2]) : "v"(yq[1][0]), "v"(yq[1][1]) : ); SB();
  asm volatile("v_mfma_f32_16x16x32_bf16 %0, a[64:67], a[48:51], %0" : "+v"(ac[4]) :  : ); SB();
  asm volatile("global_load_dwordx2 %0, %1, %2 offset:32" : "=&v"(xv[1]) : "v"(vX), "s"(x) : "memory"); SB();
  asm volatile("v_cvt_pk_bf16_f32 %0, %1, %2" : "=v"(y2[3]) : "v"(yq[1][2]), "v"(yq[1][3]) : ); SB();
  asm volatile("v_mfma_f32_16x16x32_bf16 %0, a[68:71], a[56:59], %0" : "+v"(ac[7]) :  : ); SB();
  asm volatile("ds_read_b128 a[120:123], %0 offset:3072" :  : "v"(bb) : "memory"); SB();
  asm volatile("v_min_u32 %0, -2, %0" : "+v"(y2[3]) :  : ); SB();
  asm volatile("v_mfma_f32_16x16x32_bf16 %0, a[68:71], a[52:55], %0" : "+v"(ac[6]) :  : ); SB();
  asm volatile("ds_read_b128 a[96:99], %0 offset:0" :  : "v"(bb) : "memory"); SB();
  asm volatile("v_xnor_b32 %0, %0, %1" : "+v"(y2[3]) : "v"(ykv[3]) : ); SB();
  asm volatile("v_mfma_f32_16x16x32_bf16 %0, a[68:71], a[48:51], %0" : "+v"(ac[5]) :  : ); SB();
  asm volatile("ds_read_b128 a[100:103], %0 offset:512" :  : "v"(bb) : "memory"); SB();
  asm volatile("s_waitcnt lgkmcnt(6)\n\tv_mfma_f32_16x16x32_bf16 %0, a[68:71], a[44:47], %0" : "+v"(ac[4]) :  : ); SB();
  asm volatile("buffer_store_dwordx4 %1, %0, %2, %3 offen offset:0 sc1\n\ts_nop 1" :  : "v"(vF), "v"(y2), "s"(rWS), "s"(uso) : "memory"); SB();
  asm volatile("v_mfma_f32_16x16x32_bf16 %0, a[72:75], a[52:55], %0" : "+v"(ac[7]) :  : ); SB();
  asm volatile("ds_read2_b32 a[132:133], %0 offset0:0 offset1:1" :  : "v"(aa[0]) : "memory"); SB();
  asm volatile("v_mfma_f32_16x16x32_bf16 %0, a[72:75], a[48:51], %0" : "+v"(ac[6]) :  : ); SB();
  asm volatile("ds_read2_b32 a[134:135], %0 offset0:2 offset1:3" :  : "v"(aa[0]) : "memory"); SB();
  asm volatile("v_mfma_f32_16x16x32_bf16 %0, a[72:75], a[44:47], %0" : "+v"(ac[5]) :  : ); SB();
  asm volatile("ds_read_b128 a[116:119], %0 offset:2560" :  : "v"(bb) : "memory"); SB();
  asm volatile("v_mfma_f32_16x16x32_bf16 %0, a[64:67], a[44:47], %0" : "+v"(ac[3]) :  : ); SB();
  asm volatile("global_load_dwordx2 %0, %1, %2 offset:64" : "=&v"(xv[2]) : "v"(vX), "s"(x) : "memory"); SB();
  asm volatile("v_mfma_f32_16x16x32_bf16 %0, a[76:79], a[48:51], %0" : "+v"(ac[7]) :  : ); SB();
  asm volatile("ds_read_b128 a[104:107], %0 offset:1024" :  : "v"(bb) : "memory"); SB();
  asm volatile("v_mfma_f32_16x16x32_bf16 %0, a[76:79], a[44:47], %0" : "+v"(ac[6]) :  : ); SB();
  asm volatile("ds_read2_b32 a[136:137], %0 offset0:128 offset1:129" :  : "v"(aa[1]) : "memory"); SB();
  asm volatile("v_mfma_f32_16x16x32_bf16 %0, a[76:79], a[40:43], %0" : "+v"(ac[5]) :  : ); SB();
  asm volatile("ds_read2_b32 a[138:139], %0 offset0:130 offset1:131" :  : "v"(aa[1]) : "memory"); SB();
  asm volatile("v_mfma_f32_16x16x32_bf16 %0, a[72:75], a[40:43], %0" : "+v"(ac[4]) :  : ); SB();
  asm volatile("global_load_dwordx2 %0, %1, %2 offset:96" : "=&v"(xv[3]) : "v"(vX), "s"(x) : "memory"); SB();
  asm volatile("v_mfma_f32_16x16x32_bf16 %0, a[80:83], a[44:47], %0" : "+v"(ac[7]) :  : ); SB();
  asm volatile("ds_read_b128 a[108:111], %0 offset:1536" :  : "v"(bb) : "memory"); SB();
  asm volatile("v_mfma_f32_16x16x32_bf16 %0, a[80:83], a[40:43], %0" : "+v"(ac[6]) :  : ); SB();
  asm volatile("ds_read_b128 a[112:115], %0 offset:2048" :  : "v"(bb) : "memory"); SB();
  asm volatile("v_mfma_f32_16x16x32_bf16 %0, a[68:71], a[40:43], %0" : "+v"(ac[3]) :  : ); SB();
  asm volatile("ds_read2_b32 a[140:141], %0 offset0:0 offset1:1" :  : "v"(aa[1]) : "memory"); SB();
  asm volatile("v_mfma_f32_16x16x32_bf16 %0, a[64:67], a[40:43], %0" : "+v"(ac[2]) :  : ); SB();
  asm volatile("global_load_dwordx2 %0, %1, %2 offset:128" : "=&v"(xv[4]) : "v"(vX), "s"(x) : "memory"); SB();
  asm volatile("v_mfma_f32_16x16x32_bf16 %0, a[84:87], a[40:43], %0" : "+v"(ac[7]) :  : ); SB();
  asm volatile("ds_read2_b32 a[142:143], %0 offset0:2 offset1:3" :  : "v"(aa[1]) : "memory"); SB();
  asm volatile("v_mfma_f32_16x16x32_bf16 %0, a[84:87], a[36:39], %0" : "+v"(ac[6]) :  : ); SB();
  asm volatile("ds_read2_b32 a[144:145], %0 offset0:128 offset1:129" :  : "v"(aa[2]) : "memory"); SB();
  asm volatile("v_mfma_f32_16x16x32_bf16 %0, a[80:83], a[36:39], %0" : "+v"(ac[5]) :  : ); SB();
  asm volatile("ds_read2_b32 a[146:147], %0 offset0:130 offset1:131" :  : "v"(aa[2]) : "memory"); SB();
  asm volatile("v_mfma_f32_16x16x32_bf16 %0, a[76:79], a[36:39], %0" : "+v"(ac[4]) :  : ); SB();
  asm volatile("v_mfma_f32_16x16x32_bf16 %0, a[88:91], a[36:39], %0" : "+v"(ac[7]) :  : ); SB();
  asm volatile("ds_read2_b32 a[148:149], %0 offset0:0 offset1:1" :  : "v"(aa[2]) : "memory"); SB();
  asm volatile("v_mfma_f32_16x16x32_bf16 %0, a[72:75], a[36:39], %0" : "+v"(ac[3]) :  : ); SB();
  asm volatile("ds_read2_b32 a[150:151], %0 offset0:2 offset1:3" :  : "v"(aa[2]) : "memory"); SB();
  asm volatile("v_mfma_f32_16x16x32_bf16 %0, a[68:71], a[36:39], %0" : "+v"(ac[2]) :  : ); SB();
  asm volatile("ds_read2_b32 a[152:153], %0 offset0:128 offset1:129" :  : "v"(aa[3]) : "memory"); SB();
  asm volatile("v_mfma_f32_16x16x32_bf16 %0, a[64:67], a[36:39], %0" : "+v"(ac[1]) :  : ); SB();
  asm volatile("v_mfma_f32_16x16x32_bf16 %0, a[92:95], a[32:35], %0" : "+v"(ac[7]) :  : ); SB();
  asm volatile("ds_read2_b32 a[154:155], %0 offset0:130 offset1:131" :  : "v"(aa[3]) : "memory"); SB();
  asm volatile("v_mfma_f32_16x16x32_bf16 %0, a[88:91], a[32:35], %0" : "+v"(ac[6]) :  : ); SB();
  asm volatile("ds_read2_b32 a[156:157], %0 offset0:0 offset1:1" :  : "v"(aa[3]) : "memory"); SB();
  asm volatile("v_mfma_f32_16x16x32_bf16 %0, a[84:87], a[32:35], %0" : "+v"(ac[5]) :  : ); SB();
  asm volatile("ds_read2_b32 a[158:159], %0 offset0:2 offset1:3" :  : "v"(aa[3]) : "memory"); SB();
  asm volatile("s_waitcnt vmcnt(12)\n\ts_mov_b64 exec, 1\n\tbuffer_store_dwordx2 %0, off, %1, %2 sc1\n\ts_mov_b64 exec, -1" :  : "v"(((u32x2){tg0, tg1})), "s"(rWS), "s"(sFH + 8192) : "memory"); SB();
  asm volatile("v_mfma_f32_16x16x32_bf16 %0, a[80:83], a[32:35], %0" : "+v"(ac[4]) :  : ); SB();
  asm volatile("v_mfma_f32_16x16x32_bf16 %0, a[76:79], a[32:35], %0" : "+v"(ac[3]) :  : ); SB();
  asm volatile("v_mfma_f32_16x16x32_bf16 %0, a[72:75], a[32:35], %0" : "+v"(ac[2]) :  : ); SB();
  asm volatile("v_mfma_f32_16x16x32_bf16 %0, a[68:71], a[32:35], %0" : "+v"(ac[1]) :  : ); SB();
  asm volatile("v_mfma_f32_16x16x32_bf16 %0, a[64:67], a[32:35], %0" : "+v"(ac[0]) :  : ); SB();
  asm volatile("s_waitcnt lgkmcnt(3)\n\tv_mfma_f32_16x16x32_bf16 %0, a[128:131], a[124:127], %0" : "+v"(ac[7]) :  : ); SB();
  asm volatile("v_mfma_f32_16x16x32_bf16 %0, a[128:131], a[120:123], %0" : "+v"(ac[6]) :  : ); SB();
  asm volatile("v_mfma_f32_16x16x32_bf16 %0, a[128:131], a[96:99], %0" : "+v"(ac[0]) :  : ); SB();
  asm volatile("v_mfma_f32_16x16x32_bf16 %0, a[128:131], a[100:103], %0" : "+v"(ac[1]) :  : ); SB();
  asm volatile("v_mfma_f32_16x16x32_bf16 %0, a[132:135], a[120:123], %0" : "+v"(ac[7]) :  : ); SB();
  asm volatile("v_mfma_f32_16x16x32_bf16 %0, a[132:135], a[116:119], %0" : "+v"(ac[6]) :  : ); SB();
  asm volatile("v_mfma_f32_16x16x32_bf16 %0, a[128:131], a[104:107], %0" : "+v"(ac[2]) :  : ); SB();
  asm volatile("ds_write_b128 %0, %1 offset:0" :  : "v"(dd), "v"(ac[0]) : "memory"); SB();
  asm volatile("v_mfma_f32_16x16x32_bf16 %0, a[132:135], a[96:99], %0" : "+v"(ac[1]) :  : ); SB();
  asm volatile("v_mfma_f32_16x16x32_bf16 %0, a[136:139], a[116:119], %0" : "+v"(ac[7]) :  : ); SB();
  asm volatile("s_waitcnt vmcnt(8)\n\t.Lc_%=:\n\tv_cmp_ne_u64 vcc, %2, %1\n\ts_cbranch_vccnz .Lr_%=\n\t.Ld_%=:\n\t.subsection 1\n\t.Lr_%=:\n\tv_add_u32 %0, -1, %0\n\ts_nop 1\n\tv_readfirstlane_b32 vcc_hi, %0\n\ts_nop 3\n\ts_cmp_lt_i32 vcc_hi, 1\n\ts_cbranch_scc1 .Ld_%=\n\ts_sleep 2\n\ts_mov_b32 vcc_lo, 0\n\tbuffer_load_dwordx2 %1, %3, %4, vcc_lo offen sc1\n\ts_waitcnt vmcnt(0)\n\ts_branch .Lc_%=\n\t.subsection 0" : "+v"(tmo), "+v"(fh) : "s"(tg), "v"(vFH), "s"(rWS) : "memory", "vcc", "scc"); SB();
  asm volatile("buffer_load_dwordx4 v[164:167], %0, %1, %2 offen offset:0 sc1" :  : "v"(vHr), "s"(rWS), "s"(hso[2]) : "memory"); SB();
  asm volatile("v_mfma_f32_16x16x32_bf16 %0, a[128:131], a[108:111], %0" : "+v"(ac[3]) :  : ); SB();
  asm volatile("buffer_load_dwordx4 v[168:171], %0, %1, %2 offen offset:0 sc1" :  : "v"(vHr), "s"(rWS), "s"(hso[4]) : "memory"); SB();
  asm volatile("v_mfma_f32_16x16x32_bf16 %0, a[132:135], a[100:103], %0" : "+v"(ac[2]) :  : ); SB();
  asm volatile("v_mfma_f32_16x16x32_bf16 %0, a[128:131], a[112:115], %0" : "+v"(ac[4]) :  : ); SB();
  asm volatile("ds_write_b128 %0, %1 offset:1024" :  : "v"(dd), "v"(ac[1]) : "memory"); SB();
  asm volatile("v_mfma_f32_16x16x32_bf16 %0, a[140:143], a[112:115], %0" : "+v"(ac[7]) :  : ); SB();
  asm volatile("buffer_load_dwordx4 v[172:175], %0, %1, %2 offen offset:0 sc1" :  : "v"(vHr), "s"(rWS), "s"(hso[6]) : "memory"); SB();
  asm volatile("v_mfma_f32_16x16x32_bf16 %0, a[132:135], a[104:107], %0" : "+v"(ac[3]) :  : ); SB();
  asm volatile("v_mfma_f32_16x16x32_bf16 %0, a[136:139], a[96:99], %0" : "+v"(ac[2]) :  : ); SB();
  asm volatile("v_mfma_f32_16x16x32_bf16 %0, a[132:135], a[108:111], %0" : "+v"(ac[4]) :  : ); SB();
  asm volatile("buffer_load_dwordx4 v[160:163], %0, %1, %2 offen offset:0 sc1" :  : "v"(vHr), "s"(rWS), "s"(hso[0]) : "memory"); SB();
  asm volatile("v_mfma_f32_16x16x32_bf16 %0, a[128:131], a[116:119], %0" : "+v"(ac[5]) :  : ); SB();
  asm volatile("v_mfma_f32_16x16x32_bf16 %0, a[136:139], a[100:103], %0" : "+v"(ac[3]) :  : ); SB();
  asm volatile("v_mfma_f32_16x16x32_bf16 %0, a[136:139], a[112:115], %0" : "+v"(ac[6]) :  : ); SB();
  asm volatile("buffer_load_dwordx4 v[180:183], %0, %1, %2 offen offset:1024 sc1" :  : "v"(vHr), "s"(rWS), "s"(hso[2]) : "memory"); SB();
  asm volatile("v_mfma_f32_16x16x32_bf16 %0, a[136:139], a[104:107], %0" : "+v"(ac[4]) :  : ); SB();
  asm volatile("ds_write_b128 %0, %1 offset:2048" :  : "v"(dd), "v"(ac[2]) : "memory"); SB();
  asm volatile("v_mfma_f32_16x16x32_bf16 %0, a[132:135], a[112:115], %0" : "+v"(ac[5]) :  : ); SB();
  asm volatile("v_mfma_f32_16x16x32_bf16 %0, a[140:143], a[96:99], %0" : "+v"(ac[3]) :  : ); SB();
  asm volatile("buffer_load_dwordx4 v[184:187], %0, %1, %2 offen offset:1024 sc1" :  : "v"(vHr), "s"(rWS), "s"(hso[4]) : "memory"); SB();
  asm volatile("v_mfma_f32_16x16x32_bf16 %0, a[140:143], a[108:111], %0" : "+v"(ac[6]) :  : ); SB();
  asm volatile("v_mfma_f32_16x16x32_bf16 %0, a[140:143], a[100:103], %0" : "+v"(ac[4]) :  : ); SB();
  asm volatile("v_mfma_f32_16x16x32_bf16 %0, a[136:139], a[108:111], %0" : "+v"(ac[5]) :  : ); SB();
  asm volatile("buffer_load_dwordx4 v[188:191], %0, %1, %2 offen offset:1024 sc1" :  : "v"(vHr), "s"(rWS), "s"(hso[6]) : "memory"); SB();
  asm volatile("v_mfma_f32_16x16x32_bf16 %0, a[144:147], a[108:111], %0" : "+v"(ac[7]) :  : ); SB();
  asm volatile("ds_write_b128 %0, %1 offset:3072" :  : "v"(dd), "v"(ac[3]) : "memory"); SB();
  asm volatile("v_mfma_f32_16x16x32_bf16 %0, a[144:147], a[104:107], %0" : "+v"(ac[6]) :  : ); SB();
  asm volatile("v_mfma_f32_16x16x32_bf16 %0, a[144:147], a[96:99], %0" : "+v"(ac[4]) :  : ); SB();
  asm volatile("buffer_load_dwordx4 v[176:179], %0, %1, %2 offen offset:1024 sc1" :  : "v"(vHr), "s"(rWS), "s"(hso[0]) : "memory"); SB();
  asm volatile("v_mfma_f32_16x16x32_bf16 %0, a[140:143], a[104:107], %0" : "+v"(ac[5]) :  : ); SB();
  asm volatile("v_mfma_f32_16x16x32_bf16 %0, a[148:151], a[104:107], %0" : "+v"(ac[7]) :  : ); SB();
  asm volatile("v_mfma_f32_16x16x32_bf16 %0, a[148:151], a[100:103], %0" : "+v"(ac[6]) :  : ); SB();
  asm volatile("v_mfma_f32_16x16x32_bf16 %0, a[144:147], a[100:103], %0" : "+v"(ac[5]) :  : ); SB();
  asm volatile("ds_write_b128 %0, %1 offset:4096" :  : "v"(dd), "v"(ac[4]) : "memory"); SB();
  asm volatile("s_waitcnt lgkmcnt(2)\n\tv_mfma_f32_16x16x32_bf16 %0, a[152:155], a[100:103], %0" : "+v"(ac[7]) :  : ); SB();
  asm volatile("buffer_load_dwordx4 v[196:199], %0, %1, %2 offen offset:0 sc1" :  : "v"(vHr), "s"(rWS), "s"(hso[3]) : "memory"); SB();
  asm volatile("v_mfma_f32_16x16x32_bf16 %0, a[152:155], a[96:99], %0" : "+v"(ac[6]) :  : ); SB();
  asm volatile("v_mfma_f32_16x16x32_bf16 %0, a[148:151], a[96:99], %0" : "+v"(ac[5]) :  : ); SB();
  asm volatile("buffer_load_dwordx4 v[200:203], %0, %1, %2 offen offset:0 sc1" :  : "v"(vHr), "s"(rWS), "s"(hso[5]) : "memory"); SB();
  asm volatile("v_mfma_f32_16x16x32_bf16 %0, a[156:159], a[96:99], %0" : "+v"(ac[7]) :  : ); SB();
  asm volatile("s_nop 5" : "+v"(ac[5]), "+v"(ac[6]), "+v"(ac[7]) :  : ); SB();
  asm volatile("ds_write_b128 %0, %1 offset:6144" :  : "v"(dd), "v"(ac[6]) : "memory"); SB();
  asm volatile("ds_write_b128 %0, %1 offset:5120" :  : "v"(dd), "v"(ac[5]) : "memory"); SB();
  asm volatile("ds_write_b128 %0, %1 offset:7168" :  : "v"(dd), "v"(ac[7]) : "memory"); SB();
  ST(18);
  f32x4 yp[2][4], o3[4][5];
  u32x4 yk[2], yo[2];
  unsigned vtmp;
  asm volatile("ds_read_b128 %0, %1 offset:0" : "=&v"(yk[0]) : "v"(ykr) : "memory"); SB();
  asm volatile("ds_read_b128 %0, %1 offset:12928" : "=&v"(yk[1]) : "v"(ykr) : "memory"); SB();
  asm volatile("s_waitcnt lgkmcnt(0)\n\ts_barrier" :  :  : "memory"); SB();
  asm volatile("ds_read_b128 %0, %1 offset:0" : "=&v"(yp[0][0]) : "v"(ydr) : "memory"); SB();
  asm volatile("ds_read_b128 %0, %1 offset:256" : "=&v"(yp[0][1]) : "v"(ydr) : "memory"); SB();
  asm volatile("ds_read_b128 %0, %1 offset:8192" : "=&v"(yp[0][2]) : "v"(ydr) : "memory"); SB();
  asm volatile("ds_read_b128 %0, %1 offset:8448" : "=&v"(yp[0][3]) : "v"(ydr) : "memory"); SB();
  asm volatile("ds_read_b128 %0, %1 offset:16384" : "=&v"(yp[1][0]) : "v"(ydr) : "memory"); SB();
  asm volatile("ds_read_b128 %0, %1 offset:16640" : "=&v"(yp[1][1]) : "v"(ydr) : "memory"); SB();
  asm volatile("ds_read_b128 %0, %1 offset:24576" : "=&v"(yp[1][2]) : "v"(ydr) : "memory"); SB();
  asm volatile("ds_read_b128 %0, %1 offset:24832" : "=&v"(yp[1][3]) : "v"(ydr) : "memory"); SB();
  asm volatile("" : "+v"(yk[0]), "+v"(yk[1]) :  : ); SB();
  f32x2 yd[2][4];
#pragma unroll
  for (int n = 0; n < 8; ++n) { const float Dv = dD[n >> 2]; yd[n >> 2][n & 3] = unpk(yk[n >> 2][n & 3]) * (f32x2){Dv, Dv}; asm volatile("" : "+v"(yd[n >> 2][n & 3])); }
  SB();
  asm volatile("s_waitcnt lgkmcnt(3)" : "+v"(yp[0][0]), "+v"(yp[0][1]), "+v"(yp[0][2]), "+v"(yp[0][3]) :  : "memory"); SB();
  {
    const f32x4 s0 = yp[0][0] + yp[0][2], s1 = yp[0][1] + yp[0][3];
    yo[0] = (u32x4){pk2((f32x2){s0[0], s0[1]} + yd[0][0]), pk2((f32x2){s0[2], s0[3]} + yd[0][1]), pk2((f32x2){s1[0], s1[1]} + yd[0][2]), ~(nohole(pk2((f32x2){s1[2], s1[3]} + yd[0][3])) ^ yk[0][3])};
    SB();
  }
  asm volatile("buffer_store_dwordx4 %1, %0, %2, %3 offen offset:0 sc1\n\ts_nop 1" :  : "v"(vF), "v"(yo[0]), "s"(rWS), "s"(W_KV + 0) : "memory"); SB();
  asm volatile("s_waitcnt lgkmcnt(0)" : "+v"(yp[1][0]), "+v"(yp[1][1]), "+v"(yp[1][2]), "+v"(yp[1][3]) :  : "memory"); SB();
  {
    const f32x4 s0 = yp[1][0] + yp[1][2], s1 = yp[1][1] + yp[1][3];
    yo[1] = (u32x4){pk2((f32x2){s0[0], s0[1]} + yd[1][0]), pk2((f32x2){s0[2], s0[3]} + yd[1][1]), pk2((f32x2){s1[0], s1[1]} + yd[1][2]), ~(nohole(pk2((f32x2){s1[2], s1[3]} + yd[1][3])) ^ yk[1][3])};
    SB();
  }
  asm volatile("buffer_store_dwordx4 %1, %0, %2, %3 offen offset:0 sc1\n\ts_nop 1" :  : "v"(vF), "v"(yo[1]), "s"(rWS), "s"(W_KV + 4096) : "memory"); SB();
  ST(19);
  ST(20);
  f32x2 xf[5][2], sv[5][2];
  float rd, mr;
  asm volatile("s_waitcnt vmcnt(8)" :  :  : "memory"); SB();
  asm volatile("v_mfma_f32_16x16x32_bf16 %0, a[160:163], v[164:167], 0" : "=&v"(o3[1][0]) :  : ); SB();
  asm volatile("buffer_load_dwordx2 %0, %1, %2, %3 offen sc1" : "=&v"(fi) : "v"(vFH), "s"(rWS), "s"(8192u) : "memory"); SB();
  asm volatile("v_mfma_f32_16x16x32_bf16 %0, a[164:167], v[164:167], 0" : "=&v"(o3[1][1]) :  : ); SB();
  asm volatile("v_mfma_f32_16x16x32_bf16 %0, a[168:171], v[164:167], 0" : "=&v"(o3[1][2]) :  : ); SB();
  asm volatile("v_mfma_f32_16x16x32_bf16 %0, a[172:175], v[164:167], 0" : "=&v"(o3[1][3]) :  : ); SB();
  asm volatile("buffer_load_dwordx4 v[204:207], %0, %1, %2 offen offset:0 sc1" :  : "v"(vHr), "s"(rWS), "s"(hso[7]) : "memory"); SB();
  asm volatile("v_mfma_f32_16x16x32_bf16 %0, a[176:179], v[164:167], 0" : "=&v"(o3[1][4]) :  : ); SB();
  asm volatile("v_mfma_f32_16x16x32_bf16 %0, a[160:163], v[168:171], 0" : "=&v"(o3[2][0]) :  : ); SB();
  asm volatile("v_mfma_f32_16x16x32_bf16 %0, a[164:167], v[168:171], 0" : "=&v"(o3[2][1]) :  : ); SB();
  asm volatile("v_mfma_f32_16x16x32_bf16 %0, a[168:171], v[168:171], 0" : "=&v"(o3[2][2]) :  : ); SB();
  asm volatile("buffer_load_dwordx4 v[192:195], %0, %1, %2 offen offset:0 sc1" :  : "v"(vHr), "s"(rWS), "s"(hso[1]) : "memory"); SB();
  asm volatile("v_mfma_f32_16x16x32_bf16 %0, a[172:175], v[168:171], 0" : "=&v"(o3[2][3]) :  : ); SB();
  asm volatile("v_mfma_f32_16x16x32_bf16 %0, a[176:179], v[168:171], 0" : "=&v"(o3[2][4]) :  : ); SB();
  asm volatile("v_mfma_f32_16x16x32_bf16 %0, a[160:163], v[172:175], 0" : "=&v"(o3[3][0]) :  : ); SB();
  asm volatile("buffer_load_dwordx4 v[212:215], %0, %1, %2 offen offset:1024 sc1" :  : "v"(vHr), "s"(rWS), "s"(hso[3]) : "memory"); SB();
  asm volatile("v_mfma_f32_16x16x32_bf16 %0, a[164:167], v[172:175], 0" : "=&v"(o3[3][1]) :  : ); SB();
  asm volatile("v_mfma_f32_16x16x32_bf16 %0, a[168:171], v[172:175], 0" : "=&v"(o3[3][2]) :  : ); SB();
  asm volatile("v_mfma_f32_16x16x32_bf16 %0, a[172:175], v[172:175], 0" : "=&v"(o3[3][3]) :  : ); SB();
  asm volatile("v_mfma_f32_16x16x32_bf16 %0, a[176:179], v[172:175], 0" : "=&v"(o3[3][4]) :  : ); SB();
  asm volatile("buffer_load_dwordx4 v[216:219], %0, %1, %2 offen offset:1024 sc1" :  : "v"(vHr), "s"(rWS), "s"(hso[5]) : "memory"); SB();
  asm volatile("s_waitcnt vmcnt(9)" :  :  : "memory"); SB();
  asm volatile("v_mfma_f32_16x16x32_bf16 %0, a[180:183], v[180:183], %0" : "+v"(o3[1][0]) :  : ); SB();
  asm volatile("v_rcp_f32 %0, %1" : "=v"(rd) : "v"(xd[0]) : ); SB();
  asm volatile("v_mfma_f32_16x16x32_bf16 %0, a[184:187], v[180:183], %0" : "+v"(o3[1][1]) :  : ); SB();
  asm volatile("" : "+v"(xv[0]), "+v"(xv[1]), "+v"(xv[2]), "+v"(xv[3]), "+v"(xv[4]) :  : "memory"); SB();
  asm volatile("v_mul_f32 %0, -%1, %2" : "=v"(mr) : "v"(xm[0]), "v"(rd) : ); SB();
  asm volatile("v_mfma_f32_16x16x32_bf16 %0, a[188:191], v[180:183], %0" : "+v"(o3[1][2]) :  : ); SB();
  asm volatile("buffer_load_dwordx4 v[220:223], %0, %1, %2 offen offset:1024 sc1" :  : "v"(vHr), "s"(rWS), "s"(hso[7]) : "memory"); SB();
  asm volatile("v_lshlrev_b32 %0, 16, %1" : "=v"(xf[0][0][0]) : "v"(xv[0][0]) : ); SB();
  asm volatile("v_mfma_f32_16x16x32_bf16 %0, a[192:195], v[180:183], %0" : "+v"(o3[1][3]) :  : ); SB();
  asm volatile("v_and_b32 %0, 0xffff0000, %1" : "=v"(xf[0][0][1]) : "v"(xv[0][0]) : ); SB();
  asm volatile("v_mfma_f32_16x16x32_bf16 %0, a[196:199], v[180:183], %0" : "+v"(o3[1][4]) :  : ); SB();
  asm volatile("v_lshlrev_b32 %0, 16, %1" : "=v"(xf[0][1][0]) : "v"(xv[0][1]) : ); SB();
  asm volatile("v_mfma_f32_16x16x32_bf16 %0, a[180:183], v[184:187], %0" : "+v"(o3[2][0]) :  : ); SB();
  asm volatile("v_and_b32 %0, 0xffff0000, %1" : "=v"(xf[0][1][1]) : "v"(xv[0][1]) : ); SB();
  asm volatile("v_mfma_f32_16x16x32_bf16 %0, a[184:187], v[184:187], %0" : "+v"(o3[2][1]) :  : ); SB();
  asm volatile("buffer_load_dwordx4 v[208:211], %0, %1, %2 offen offset:1024 sc1" :  : "v"(vHr), "s"(rWS), "s"(hso[1]) : "memory"); SB();
  asm volatile("v_lshlrev_b32 %0, 16, %1" : "=v"(xf[1][0][0]) : "v"(xv[1][0]) : ); SB();
  asm volatile("v_mfma_f32_16x16x32_bf16 %0, a[188:191], v[184:187], %0" : "+v"(o3[2][2]) :  : ); SB();
  asm volatile("v_and_b32 %0, 0xffff0000, %1" : "=v"(xf[1][0][1]) : "v"(xv[1][0]) : ); SB();
  asm volatile("v_mfma_f32_16x16x32_bf16 %0, a[192:195], v[184:187], %0" : "+v"(o3[2][3]) :  : ); SB();
  asm volatile("v_lshlrev_b32 %0, 16, %1" : "=v"(xf[1][1][0]) : "v"(xv[1][1]) : ); SB();
  asm volatile("v_mfma_f32_16x16x32_bf16 %0, a[196:199], v[184:187], %0" : "+v"(o3[2][4]) :  : ); SB();
  asm volatile("v_and_b32 %0, 0xffff0000, %1" : "=v"(xf[1][1][1]) : "v"(xv[1][1]) : ); SB();
  asm volatile("v_mfma_f32_16x16x32_bf16 %0, a[180:183], v[188:191], %0" : "+v"(o3[3][0]) :  : ); SB();
  asm volatile("v_lshlrev_b32 %0, 16, %1" : "=v"(xf[2][0][0]) : "v"(xv[2][0]) : ); SB();
  asm volatile("v_mfma_f32_16x16x32_bf16 %0, a[184:187], v[188:191], %0" : "+v"(o3[3][1]) :  : ); SB();
  asm volatile("v_and_b32 %0, 0xffff0000, %1" : "=v"(xf[2][0][1]) : "v"(xv[2][0]) : ); SB();
  asm volatile("v_mfma_f32_16x16x32_bf16 %0, a[188:191], v[188:191], %0" : "+v"(o3[3][2]) :  : ); SB();
  asm volatile("v_lshlrev_b32 %0, 16, %1" : "=v"(xf[2][1][0]) : "v"(xv[2][1]) : ); SB();
  asm volatile("v_mfma_f32_16x16x32_bf16 %0, a[192:195], v[188:191], %0" : "+v"(o3[3][3]) :  : ); SB();
  asm volatile("v_and_b32 %0, 0xffff0000, %1" : "=v"(xf[2][1][1]) : "v"(xv[2][1]) : ); SB();
  asm volatile("v_mfma_f32_16x16x32_bf16 %0, a[196:199], v[188:191], %0" : "+v"(o3[3][4]) :  : ); SB();
  asm volatile("v_lshlrev_b32 %0, 16, %1" : "=v"(xf[3][0][0]) : "v"(xv[3][0]) : ); SB();
  asm volatile("s_waitcnt vmcnt(6)\n\t.Lc_%=:\n\tv_cmp_ne_u64 vcc, %2, %1\n\ts_cbranch_vccnz .Lr_%=\n\t.Ld_%=:\n\t.subsection 1\n\t.Lr_%=:\n\tv_add_u32 %0, -1, %0\n\ts_nop 1\n\tv_readfirstlane_b32 vcc_hi, %0\n\ts_nop 3\n\ts_cmp_lt_i32 vcc_hi, 1\n\ts_cbranch_scc1 .Ld_%=\n\ts_sleep 2\n\ts_mov_b32 vcc_lo, 8192\n\tbuffer_load_dwordx2 %1, %3, %4, vcc_lo offen sc1\n\ts_waitcnt vmcnt(0)\n\ts_branch .Lc_%=\n\t.subsection 0" : "+v"(tmo), "+v"(fi) : "s"(tg), "v"(vFH), "s"(rWS) : "memory", "vcc", "scc"); SB();
  asm volatile("buffer_load_dwordx4 v[228:231], %0, %1, %2 offen offset:2048 sc1" :  : "v"(vHr), "s"(rWS), "s"(hso[2]) : "memory"); SB();
  asm volatile("buffer_load_dwordx4 v[232:235], %0, %1, %2 offen offset:2048 sc1" :  : "v"(vHr), "s"(rWS), "s"(hso[4]) : "memory"); SB();
  asm volatile("buffer_load_dwordx4 v[236:239], %0, %1, %2 offen offset:2048 sc1" :  : "v"(vHr), "s"(rWS), "s"(hso[6]) : "memory"); SB();
  asm volatile("s_waitcnt vmcnt(7)" :  :  : "memory"); SB();
  asm volatile("v_mfma_f32_16x16x32_bf16 %0, a[200:203], v[196:199], %0" : "+v"(o3[1][0]) :  : ); SB();
  asm volatile("v_and_b32 %0, 0xffff0000, %1" : "=v"(xf[3][0][1]) : "v"(xv[3][0]) : ); SB();
  asm volatile("v_mfma_f32_16x16x32_bf16 %0, a[204:207], v[196:199], %0" : "+v"(o3[1][1]) :  : ); SB();
  asm volatile("buffer_load_dwordx4 v[224:227], %0, %1, %2 offen offset:2048 sc1" :  : "v"(vHr), "s"(rWS), "s"(hso[0]) : "memory"); SB();
  asm volatile("v_lshlrev_b32 %0, 16, %1" : "=v"(xf[3][1][0]) : "v"(xv[3][1]) : ); SB();
  asm volatile("v_mfma_f32_16x16x32_bf16 %0, a[208:211], v[196:199], %0" : "+v"(o3[1][2]) :  : ); SB();
  asm volatile("v_and_b32 %0, 0xffff0000, %1" : "=v"(xf[3][1][1]) : "v"(xv[3][1]) : ); SB();
  asm volatile("v_mfma_f32_16x16x32_bf16 %0, a[212:215], v[196:199], %0" : "+v"(o3[1][3]) :  : ); SB();
  asm volatile("v_lshlrev_b32 %0, 16, %1" : "=v"(xf[4][0][0]) : "v"(xv[4][0]) : ); SB();
  asm volatile("v_mfma_f32_16x16x32_bf16 %0, a[216:219], v[196:199], %0" : "+v"(o3[1][4]) :  : ); SB();
  asm volatile("v_and_b32 %0, 0xffff0000, %1" : "=v"(xf[4][0][1]) : "v"(xv[4][0]) : ); SB();
  asm volatile("v_mfma_f32_16x16x32_bf16 %0, a[200:203], v[200:203], %0" : "+v"(o3[2][0]) :  : ); SB();
  asm volatile("buffer_load_dwordx4 v[244:247], %0, %1, %2 offen offset:2048 sc1" :  : "v"(vHr), "s"(rWS), "s"(hso[3]) : "memory"); SB();
  asm volatile("v_lshlrev_b32 %0, 16, %1" : "=v"(xf[4][1][0]) : "v"(xv[4][1]) : ); SB();
  asm volatile("v_mfma_f32_16x16x32_bf16 %0, a[204:207], v[200:203], %0" : "+v"(o3[2][1]) :  : ); SB();
  asm volatile("v_and_b32 %0, 0xffff0000, %1" : "=v"(xf[4][1][1]) : "v"(xv[4][1]) : ); SB();
  asm volatile("v_mfma_f32_16x16x32_bf16 %0, a[208:211], v[200:203], %0" : "+v"(o3[2][2]) :  : ); SB();
  asm volatile("v_fma_f32 %0, %1, %2, %3" : "=v"(sv[0][0][0]) : "v"(xf[0][0][0]), "v"(rd), "v"(mr) : ); SB();
  asm volatile("v_mfma_f32_16x16x32_bf16 %0, a[212:215], v[200:203], %0" : "+v"(o3[2][3]) :  : ); SB();
  asm volatile("buffer_load_dwordx4 v[248:251], %0, %1, %2 offen offset:2048 sc1" :  : "v"(vHr), "s"(rWS), "s"(hso[5]) : "memory"); SB();
  asm volatile("v_fma_f32 %0, %1, %2, %3" : "=v"(sv[0][0][1]) : "v"(xf[0][0][1]), "v"(rd), "v"(mr) : ); SB();
  asm volatile("v_mfma_f32_16x16x32_bf16 %0, a[216:219], v[200:203], %0" : "+v"(o3[2][4]) :  : ); SB();
  asm volatile("v_fma_f32 %0, %1, %2, %3" : "=v"(sv[0][1][0]) : "v"(xf[0][1][0]), "v"(rd), "v"(mr) : ); SB();
  asm volatile("v_mfma_f32_16x16x32_bf16 %0, a[200:203], v[204:207], %0" : "+v"(o3[3][0]) :  : ); SB();
  asm volatile("v_fma_f32 %0, %1, %2, %3" : "=v"(sv[0][1][1]) : "v"(xf[0][1][1]), "v"(rd), "v"(mr) : ); SB();
  asm volatile("v_mfma_f32_16x16x32_bf16 %0, a[204:207], v[204:207], %0" : "+v"(o3[3][1]) :  : ); SB();
  asm volatile("v_fma_f32 %0, %1, %2, %3" : "=v"(sv[1][0][0]) : "v"(xf[1][0][0]), "v"(rd), "v"(mr) : ); SB();
  asm volatile("v_mfma_f32_16x16x32_bf16 %0, a[208:211], v[204:207], %0" : "+v"(o3[3][2]) :  : ); SB();
  asm volatile("buffer_load_dwordx4 v[252:255], %0, %1, %2 offen offset:2048 sc1" :  : "v"(vHr), "s"(rWS), "s"(hso[7]) : "memory"); SB();
  asm volatile("v_fma_f32 %0, %1, %2, %3" : "=v"(sv[1][0][1]) : "v"(xf[1][0][1]), "v"(rd), "v"(mr) : ); SB();
  asm volatile("v_mfma_f32_16x16x32_bf16 %0, a[212:215], v[204:207], %0" : "+v"(o3[3][3]) :  : ); SB();
  asm volatile("v_fma_f32 %0, %1, %2, %3" : "=v"(sv[1][1][0]) : "v"(xf[1][1][0]), "v"(rd), "v"(mr) : ); SB();
  asm volatile("v_mfma_f32_16x16x32_bf16 %0, a[216:219], v[204:207], %0" : "+v"(o3[3][4]) :  : ); SB();
  asm volatile("v_fma_f32 %0, %1, %2, %3" : "=v"(sv[1][1][1]) : "v"(xf[1][1][1]), "v"(rd), "v"(mr) : ); SB();
  asm volatile("s_waitcnt vmcnt(7)" :  :  : "memory"); SB();
  asm volatile("v_mfma_f32_16x16x32_bf16 %0, a[220:223], v[212:215], %0" : "+v"(o3[1][0]) :  : ); SB();
  asm volatile("buffer_load_dwordx4 v[240:243], %0, %1, %2 offen offset:2048 sc1" :  : "v"(vHr), "s"(rWS), "s"(hso[1]) : "memory"); SB();
  asm volatile("v_fma_f32 %0, %1, %2, %3" : "=v"(sv[2][0][0]) : "v"(xf[2][0][0]), "v"(rd), "v"(mr) : ); SB();
  asm volatile("v_mfma_f32_16x16x32_bf16 %0, a[224:227], v[212:215], %0" : "+v"(o3[1][1]) :  : ); SB();
  asm volatile("v_fma_f32 %0, %1, %2, %3" : "=v"(sv[2][0][1]) : "v"(xf[2][0][1]), "v"(rd), "v"(mr) : ); SB();
  asm volatile("v_mfma_f32_16x16x32_bf16 %0, a[228:231], v[212:215], %0" : "+v"(o3[1][2]) :  : ); SB();
  asm volatile("v_fma_f32 %0, %1, %2, %3" : "=v"(sv[2][1][0]) : "v"(xf[2][1][0]), "v"(rd), "v"(mr) : ); SB();
  asm volatile("v_mfma_f32_16x16x32_bf16 %0, a[232:235], v[212:215], %0" : "+v"(o3[1][3]) :  : ); SB();
  asm volatile("v_fma_f32 %0, %1, %2, %3" : "=v"(sv[2][1][1]) : "v"(xf[2][1][1]), "v"(rd), "v"(mr) : ); SB();
  asm volatile("v_mfma_f32_16x16x32_bf16 %0, a[236:239], v[212:215], %0" : "+v"(o3[1][4]) :  : ); SB();
  asm volatile("v_fma_f32 %0, %1, %2, %3" : "=v"(sv[3][0][0]) : "v"(xf[3][0][0]), "v"(rd), "v"(mr) : ); SB();
  asm volatile("v_mfma_f32_16x16x32_bf16 %0, a[220:223], v[216:219], %0" : "+v"(o3[2][0]) :  : ); SB();
  asm volatile("v_fma_f32 %0, %1, %2, %3" : "=v"(sv[3][0][1]) : "v"(xf[3][0][1]), "v"(rd), "v"(mr) : ); SB();
  asm volatile("v_mfma_f32_16x16x32_bf16 %0, a[224:227], v[216:219], %0" : "+v"(o3[2][1]) :  : ); SB();
  asm volatile("v_fma_f32 %0, %1, %2, %3" : "=v"(sv[3][1][0]) : "v"(xf[3][1][0]), "v"(rd), "v"(mr) : ); SB();
  asm volatile("v_mfma_f32_16x16x32_bf16 %0, a[228:231], v[216:219], %0" : "+v"(o3[2][2]) :  : ); SB();
  asm volatile("v_fma_f32 %0, %1, %2, %3" : "=v"(sv[3][1][1]) : "v"(xf[3][1][1]), "v"(rd), "v"(mr) : ); SB();
  asm volatile("v_mfma_f32_16x16x32_bf16 %0, a[232:235], v[216:219], %0" : "+v"(o3[2][3]) :  : ); SB();
  asm volatile("v_fma_f32 %0, %1, %2, %3" : "=v"(sv[4][0][0]) : "v"(xf[4][0][0]), "v"(rd), "v"(mr) : ); SB();
  asm volatile("v_mfma_f32_16x16x32_bf16 %0, a[236:239], v[216:219], %0" : "+v"(o3[2][4]) :  : ); SB();
  asm volatile("v_fma_f32 %0, %1, %2, %3" : "=v"(sv[4][0][1]) : "v"(xf[4][0][1]), "v"(rd), "v"(mr) : ); SB();
  asm volatile("v_mfma_f32_16x16x32_bf16 %0, a[220:223], v[220:223], %0" : "+v"(o3[3][0]) :  : ); SB();
  asm volatile("v_fma_f32 %0, %1, %2, %3" : "=v"(sv[4][1][0]) : "v"(xf[4][1][0]), "v"(rd), "v"(mr) : ); SB();
  asm volatile("v_mfma_f32_16x16x32_bf16 %0, a[224:227], v[220:223], %0" : "+v"(o3[3][1]) :  : ); SB();
  asm volatile("v_fma_f32 %0, %1, %2, %3" : "=v"(sv[4][1][1]) : "v"(xf[4][1][1]), "v"(rd), "v"(mr) : ); SB();
  asm volatile("v_mfma_f32_16x16x32_bf16 %0, a[228:231], v[220:223], %0" : "+v"(o3[3][2]) :  : ); SB();
  asm volatile("v_mfma_f32_16x16x32_bf16 %0, a[232:235], v[220:223], %0" : "+v"(o3[3][3]) :  : ); SB();
  asm volatile("v_mfma_f32_16x16x32_bf16 %0, a[236:239], v[220:223], %0" : "+v"(o3[3][4]) :  : ); SB();
  ST(30);
  asm volatile("buffer_load_dwordx4 v[164:167], %0, %1, %2 offen offset:0 sc1" :  : "v"(vKV), "s"(rWS), "s"(0u) : "memory"); SB();
  asm volatile("buffer_load_dwordx4 v[168:171], %0, %1, %2 offen offset:0 sc1" :  : "v"(vKV), "s"(rWS), "s"(w3k[0]) : "memory"); SB();
  asm volatile("buffer_load_dwordx4 v[172:175], %0, %1, %2 offen offset:0 sc1" :  : "v"(vKV), "s"(rWS), "s"(w3k[1]) : "memory"); SB();
  asm volatile("s_waitcnt vmcnt(3)" :  :  : "memory"); SB();
  asm volatile("v_mfma_f32_16x16x32_bf16 %0, a[240:243], v[228:231], %0" : "+v"(o3[1][0]) :  : ); SB();
  asm volatile("v_mfma_f32_16x16x32_bf16 %0, a[244:247], v[228:231], %0" : "+v"(o3[1][1]) :  : ); SB();
  asm volatile("v_mfma_f32_16x16x32_bf16 %0, a[248:251], v[228:231], %0" : "+v"(o3[1][2]) :  : ); SB();
  asm volatile("v_mfma_f32_16x16x32_bf16 %0, a[252:255], v[228:231], %0" : "+v"(o3[1][3]) :  : ); SB();
  asm volatile("v_mfma_f32_16x16x32_bf16 %0, a[0:3], v[228:231], %0" : "+v"(o3[1][4]) :  : ); SB();
  asm volatile("v_mfma_f32_16x16x32_bf16 %0, a[4:7], v[244:247], %0" : "+v"(o3[1][0]) :  : ); SB();
  asm volatile("v_mfma_f32_16x16x32_bf16 %0, a[8:11], v[244:247], %0" : "+v"(o3[1][1]) :  : ); SB();
  asm volatile("v_mfma_f32_16x16x32_bf16 %0, a[12:15], v[244:247], %0" : "+v"(o3[1][2]) :  : ); SB();
  asm volatile("v_mfma_f32_16x16x32_bf16 %0, a[16:19], v[244:247], %0" : "+v"(o3[1][3]) :  : ); SB();
  asm volatile("v_mfma_f32_16x16x32_bf16 %0, a[20:23], v[244:247], %0" : "+v"(o3[1][4]) :  : ); SB();
  asm volatile("v_mfma_f32_16x16x32_bf16 %0, a[240:243], v[232:235], %0" : "+v"(o3[2][0]) :  : ); SB();
  asm volatile("ds_write_b128 %0, %1 offset:0" :  : "v"(pw3), "v"(o3[1][0]) : "memory"); SB();
  asm volatile("v_mfma_f32_16x16x32_bf16 %0, a[244:247], v[232:235], %0" : "+v"(o3[2][1]) :  : ); SB();
  asm volatile("v_mfma_f32_16x16x32_bf16 %0, a[248:251], v[232:235], %0" : "+v"(o3[2][2]) :  : ); SB();
  asm volatile("v_mfma_f32_16x16x32_bf16 %0, a[252:255], v[232:235], %0" : "+v"(o3[2][3]) :  : ); SB();
  asm volatile("ds_write_b128 %0, %1 offset:1024" :  : "v"(pw3), "v"(o3[1][1]) : "memory"); SB();
  asm volatile("v_mfma_f32_16x16x32_bf16 %0, a[0:3], v[232:235], %0" : "+v"(o3[2][4]) :  : ); SB();
  asm volatile("v_mfma_f32_16x16x32_bf16 %0, a[4:7], v[248:251], %0" : "+v"(o3[2][0]) :  : ); SB();
  asm volatile("v_mfma_f32_16x16x32_bf16 %0, a[8:11], v[248:251], %0" : "+v"(o3[2][1]) :  : ); SB();
  asm volatile("ds_write_b128 %0, %1 offset:2048" :  : "v"(pw3), "v"(o3[1][2]) : "memory"); SB();
  asm volatile("v_mfma_f32_16x16x32_bf16 %0, a[12:15], v[248:251], %0" : "+v"(o3[2][2]) :  : ); SB();
  asm volatile("v_mfma_f32_16x16x32_bf16 %0, a[16:19], v[248:251], %0" : "+v"(o3[2][3]) :  : ); SB();
  asm volatile("v_mfma_f32_16x16x32_bf16 %0, a[20:23], v[248:251], %0" : "+v"(o3[2][4]) :  : ); SB();
  asm volatile("ds_write_b128 %0, %1 offset:3072" :  : "v"(pw3), "v"(o3[1][3]) : "memory"); SB();
  asm volatile("v_mfma_f32_16x16x32_bf16 %0, a[240:243], v[236:239], %0" : "+v"(o3[3][0]) :  : ); SB();
  asm volatile("v_mfma_f32_16x16x32_bf16 %0, a[244:247], v[236:239], %0" : "+v"(o3[3][1]) :  : ); SB();
  asm volatile("v_mfma_f32_16x16x32_bf16 %0, a[248:251], v[236:239], %0" : "+v"(o3[3][2]) :  : ); SB();
  asm volatile("ds_write_b128 %0, %1 offset:4096" :  : "v"(pw3), "v"(o3[1][4]) : "memory"); SB();
  asm volatile("v_mfma_f32_16x16x32_bf16 %0, a[252:255], v[236:239], %0" : "+v"(o3[3][3]) :  : ); SB();
  asm volatile("v_mfma_f32_16x16x32_bf16 %0, a[0:3], v[236:239], %0" : "+v"(o3[3][4]) :  : ); SB();
  asm volatile("v_mfma_f32_16x16x32_bf16 %0, a[4:7], v[252:255], %0" : "+v"(o3[3][0]) :  : ); SB();
  asm volatile("ds_write_b128 %0, %1 offset:5120" :  : "v"(pw3), "v"(o3[2][0]) : "memory"); SB();
  asm volatile("v_mfma_f32_16x16x32_bf16 %0, a[8:11], v[252:255], %0" : "+v"(o3[3][1]) :  : ); SB();
  asm volatile("v_mfma_f32_16x16x32_bf16 %0, a[12:15], v[252:255], %0" : "+v"(o3[3][2]) :  : ); SB();
  asm volatile("v_mfma_f32_16x16x32_bf16 %0, a[16:19], v[252:255], %0" : "+v"(o3[3][3]) :  : ); SB();
  asm volatile("ds_write_b128 %0, %1 offset:6144" :  : "v"(pw3), "v"(o3[2][1]) : "memory"); SB();
  asm volatile("v_mfma_f32_16x16x32_bf16 %0, a[20:23], v[252:255], %0" : "+v"(o3[3][4]) :  : ); SB();
  asm volatile("v_mfma_f32_16x16x32_bf16 %0, a[160:163], v[160:163], 0" : "=&v"(o3[0][0]) :  : ); SB();
  asm volatile("v_mfma_f32_16x16x32_bf16 %0, a[164:167], v[160:163], 0" : "=&v"(o3[0][1]) :  : ); SB();
  asm volatile("ds_write_b128 %0, %1 offset:7168" :  : "v"(pw3), "v"(o3[2][2]) : "memory"); SB();
  asm volatile("v_mfma_f32_16x16x32_bf16 %0, a[168:171], v[160:163], 0" : "=&v"(o3[0][2]) :  : ); SB();
  asm volatile("v_mfma_f32_16x16x32_bf16 %0, a[172:175], v[160:163], 0" : "=&v"(o3[0][3]) :  : ); SB();
  asm volatile("v_mfma_f32_16x16x32_bf16 %0, a[176:179], v[160:163], 0" : "=&v"(o3[0][4]) :  : ); SB();
  asm volatile("ds_write_b128 %0, %1 offset:8192" :  : "v"(pw3), "v"(o3[2][3]) : "memory"); SB();
  asm volatile("v_mfma_f32_16x16x32_bf16 %0, a[180:183], v[176:179], %0" : "+v"(o3[0][0]) :  : ); SB();
  asm volatile("v_mfma_f32_16x16x32_bf16 %0, a[184:187], v[176:179], %0" : "+v"(o3[0][1]) :  : ); SB();
  asm volatile("v_mfma_f32_16x16x32_bf16 %0, a[188:191], v[176:179], %0" : "+v"(o3[0][2]) :  : ); SB();
  asm volatile("ds_write_b128 %0, %1 offset:9216" :  : "v"(pw3), "v"(o3[2][4]) : "memory"); SB();
  asm volatile("v_mfma_f32_16x16x32_bf16 %0, a[192:195], v[176:179], %0" : "+v"(o3[0][3]) :  : ); SB();
  asm volatile("v_mfma_f32_16x16x32_bf16 %0, a[196:199], v[176:179], %0" : "+v"(o3[0][4]) :  : ); SB();
  asm volatile("v_mfma_f32_16x16x32_bf16 %0, a[200:203], v[192:195], %0" : "+v"(o3[0][0]) :  : ); SB();
  asm volatile("ds_write_b128 %0, %1 offset:10240" :  : "v"(pw3), "v"(o3[3][0]) : "memory"); SB();
  asm volatile("v_mfma_f32_16x16x32_bf16 %0, a[204:207], v[192:195], %0" : "+v"(o3[0][1]) :  : ); SB();
  asm volatile("v_mfma_f32_16x16x32_bf16 %0, a[208:211], v[192:195], %0" : "+v"(o3[0][2]) :  : ); SB();
  asm volatile("v_mfma_f32_16x16x32_bf16 %0, a[212:215], v[192:195], %0" : "+v"(o3[0][3]) :  : ); SB();
  asm volatile("ds_write_b128 %0, %1 offset:11264" :  : "v"(pw3), "v"(o3[3][1]) : "memory"); SB();
  asm volatile("v_mfma_f32_16x16x32_bf16 %0, a[216:219], v[192:195], %0" : "+v"(o3[0][4]) :  : ); SB();
  asm volatile("v_mfma_f32_16x16x32_bf16 %0, a[220:223], v[208:211], %0" : "+v"(o3[0][0]) :  : ); SB();
  asm volatile("v_mfma_f32_16x16x32_bf16 %0, a[224:227], v[208:211], %0" : "+v"(o3[0][1]) :  : ); SB();
  asm volatile("ds_write_b128 %0, %1 offset:12288" :  : "v"(pw3), "v"(o3[3][2]) : "memory"); SB();
  asm volatile("v_mfma_f32_16x16x32_bf16 %0, a[228:231], v[208:211], %0" : "+v"(o3[0][2]) :  : ); SB();
  asm volatile("v_mfma_f32_16x16x32_bf16 %0, a[232:235], v[208:211], %0" : "+v"(o3[0][3]) :  : ); SB();
  asm volatile("v_mfma_f32_16x16x32_bf16 %0, a[236:239], v[208:211], %0" : "+v"(o3[0][4]) :  : ); SB();
  asm volatile("ds_write_b128 %0, %1 offset:13312" :  : "v"(pw3), "v"(o3[3][3]) : "memory"); SB();
  asm volatile("v_mfma_f32_16x16x32_bf16 %0, a[240:243], v[224:227], %0" : "+v"(o3[0][0]) :  : ); SB();
  asm volatile("v_mfma_f32_16x16x32_bf16 %0, a[244:247], v[224:227], %0" : "+v"(o3[0][1]) :  : ); SB();
  asm volatile("v_mfma_f32_16x16x32_bf16 %0, a[248:251], v[224:227], %0" : "+v"(o3[0][2]) :  : ); SB();
  asm volatile("ds_write_b128 %0, %1 offset:14336" :  : "v"(pw3), "v"(o3[3][4]) : "memory"); SB();
  asm volatile("v_mfma_f32_16x16x32_bf16 %0, a[252:255], v[224:227], %0" : "+v"(o3[0][3]) :  : ); SB();
  asm volatile("v_mfma_f32_16x16x32_bf16 %0, a[0:3], v[224:227], %0" : "+v"(o3[0][4]) :  : ); SB();
  asm volatile("v_mfma_f32_16x16x32_bf16 %0, a[4:7], v[240:243], %0" : "+v"(o3[0][0]) :  : ); SB();
  asm volatile("v_mfma_f32_16x16x32_bf16 %0, a[8:11], v[240:243], %0" : "+v"(o3[0][1]) :  : ); SB();
  asm volatile("v_mfma_f32_16x16x32_bf16 %0, a[12:15], v[240:243], %0" : "+v"(o3[0][2]) :  : ); SB();
  asm volatile("v_mfma_f32_16x16x32_bf16 %0, a[16:19], v[240:243], %0" : "+v"(o3[0][3]) :  : ); SB();
  asm volatile("v_mfma_f32_16x16x32_bf16 %0, a[20:23], v[240:243], %0" : "+v"(o3[0][4]) :  : ); SB();
  asm volatile("s_waitcnt lgkmcnt(0)\n\ts_barrier" :  :  : "memory"); SB();
  asm volatile("ds_read_b128 %0, %1 offset:0" : "=&v"(o3[1][0]) : "v"(p3[0]) : "memory"); SB();
  asm volatile("ds_read_b128 %0, %1 offset:5120" : "=&v"(o3[2][0]) : "v"(p3[1]) : "memory"); SB();
  asm volatile("ds_read_b128 %0, %1 offset:10240" : "=&v"(o3[3][0]) : "v"(p3[2]) : "memory"); SB();
  asm volatile("ds_read_b128 %0, %1 offset:1024" : "=&v"(o3[1][1]) : "v"(p3[0]) : "memory"); SB();
  asm volatile("ds_read_b128 %0, %1 offset:6144" : "=&v"(o3[2][1]) : "v"(p3[1]) : "memory"); SB();
  asm volatile("ds_read_b128 %0, %1 offset:11264" : "=&v"(o3[3][1]) : "v"(p3[2]) : "memory"); SB();
  asm volatile("ds_read_b128 %0, %1 offset:2048" : "=&v"(o3[1][2]) : "v"(p3[0]) : "memory"); SB();
  asm volatile("ds_read_b128 %0, %1 offset:7168" : "=&v"(o3[2][2]) : "v"(p3[1]) : "memory"); SB();
  asm volatile("ds_read_b128 %0, %1 offset:12288" : "=&v"(o3[3][2]) : "v"(p3[2]) : "memory"); SB();
  asm volatile("ds_read_b128 %0, %1 offset:3072" : "=&v"(o3[1][3]) : "v"(p3[0]) : "memory"); SB();
  asm volatile("ds_read_b128 %0, %1 offset:8192" : "=&v"(o3[2][3]) : "v"(p3[1]) : "memory"); SB();
  asm volatile("ds_read_b128 %0, %1 offset:13312" : "=&v"(o3[3][3]) : "v"(p3[2]) : "memory"); SB();
  asm volatile("ds_read_b128 %0, %1 offset:4096" : "=&v"(o3[1][4]) : "v"(p3[0]) : "memory"); SB();
  asm volatile("ds_read_b128 %0, %1 offset:9216" : "=&v"(o3[2][4]) : "v"(p3[1]) : "memory"); SB();
  asm volatile("ds_read_b128 %0, %1 offset:14336" : "=&v"(o3[3][4]) : "v"(p3[2]) : "memory"); SB();
  ST(21);
  u32x2 yr[5];
  asm volatile("s_waitcnt lgkmcnt(9)" : "+v"(o3[0][0]), "+v"(o3[0][1]), "+v"(o3[1][0]), "+v"(o3[1][1]), "+v"(o3[2][0]), "+v"(o3[2][1]), "+v"(o3[3][0]), "+v"(o3[3][1]) :  : "memory"); SB();
#pragma unroll
  for (int j = 0; j <= 1; ++j) {
    const f32x4 o4 = (o3[0][j] + o3[1][j]) + (o3[2][j] + o3[3][j]);
#pragma unroll
    for (int h = 0; h < 2; ++h) {
      const f32x2 o = {o4[2 * h], o4[2 * h + 1]};
      const f32x2 a = o * (f32x2){-1.4426950f, -1.4426950f};
      const f32x2 e = (f32x2){__builtin_amdgcn_exp2f(a[0]), __builtin_amdgcn_exp2f(a[1])} + (f32x2){1.0f, 1.0f};
      const f32x2 r = {__builtin_amdgcn_rcpf(e[0]), __builtin_amdgcn_rcpf(e[1])};
      sv[j][h] = (sv[j][h] * o) * r;
    }
  }
  asm volatile("" : "+v"(sv[0][0]), "+v"(sv[0][1]), "+v"(sv[1][0]), "+v"(sv[1][1]));
  SB();
  asm volatile("s_waitcnt lgkmcnt(0)" : "+v"(o3[0][2]), "+v"(o3[0][3]), "+v"(o3[1][2]), "+v"(o3[1][3]), "+v"(o3[2][2]), "+v"(o3[2][3]), "+v"(o3[3][2]), "+v"(o3[3][3]) :  : "memory"); SB();
#pragma unroll
  for (int j = 2; j <= 3; ++j) {
    const f32x4 o4 = (o3[0][j] + o3[1][j]) + (o3[2][j] + o3[3][j]);
#pragma unroll
    for (int h = 0; h < 2; ++h) {
      const f32x2 o = {o4[2 * h], o4[2 * h + 1]};
      const f32x2 a = o * (f32x2){-1.4426950f, -1.4426950f};
      const f32x2 e = (f32x2){__builtin_amdgcn_exp2f(a[0]), __builtin_amdgcn_exp2f(a[1])} + (f32x2){1.0f, 1.0f};
      const f32x2 r = {__builtin_amdgcn_rcpf(e[0]), __builtin_amdgcn_rcpf(e[1])};
      sv[j][h] = (sv[j][h] * o) * r;
    }
  }
  asm volatile("" : "+v"(sv[2][0]), "+v"(sv[2][1]), "+v"(sv[3][0]), "+v"(sv[3][1]));
  SB();
  ST(22);
  asm volatile("s_waitcnt vmcnt(0)\n\t.Lc_%=:\n\tv_xnor_b32 v167, v167, %6\n\tv_xnor_b32 v171, v171, %7\n\tv_xnor_b32 v175, v175, %8\n\tv_max3_u32 %1, v167, v171, v175\n\tv_cmp_eq_u32 vcc, -1, %1\n\ts_cbranch_vccnz .Lr_%=\n\t.Ld_%=:\n\t.subsection 1\n\t.Lr_%=:\n\tv_add_u32 %0, -1, %0\n\ts_nop 1\n\tv_readfirstlane_b32 vcc_hi, %0\n\ts_nop 3\n\ts_cmp_lt_i32 vcc_hi, 1\n\ts_cbranch_scc1 .Ld_%=\n\ts_sleep 2\n\tbuffer_load_dwordx4 v[164:167], %2, %3, 0 offen sc1\n\tbuffer_load_dwordx4 v[168:171], %2, %3, %4 offen sc1\n\tbuffer_load_dwordx4 v[172:175], %2, %3, %5 offen sc1\n\ts_waitcnt vmcnt(0)\n\ts_branch .Lc_%=\n\t.subsection 0" : "+v"(tmo), "=&v"(vtmp) : "v"(vKV), "s"(rWS), "s"(w3k[0]), "s"(w3k[1]), "v"(kz[0]), "v"(kz[1]), "v"(kz[2]) : "memory", "vcc", "scc"); SB();
  ST(31);
  asm volatile("ds_write_b128 %0, v[164:167] offset:0" :  : "v"(yw) : "memory"); SB();
  asm volatile("ds_write_b128 %0, v[168:171] offset:1024" :  : "v"(yw) : "memory"); SB();
  asm volatile("ds_write_b128 %0, v[172:175] offset:2048" :  : "v"(yw) : "memory"); SB();
  asm volatile("s_waitcnt lgkmcnt(0)\n\ts_barrier" :  :  : "memory"); SB();
  asm volatile("ds_read_b64_tr_b16 %0, %1 offset:0" : "=&v"(yr[0]) : "v"(ya) : "memory"); SB();
  asm volatile("ds_read_b64_tr_b16 %0, %1 offset:2048" : "=&v"(yr[1]) : "v"(ya) : "memory"); SB();
  asm volatile("ds_read_b64_tr_b16 %0, %1 offset:4096" : "=&v"(yr[2]) : "v"(ya) : "memory"); SB();
  asm volatile("ds_read_b64_tr_b16 %0, %1 offset:6144" : "=&v"(yr[3]) : "v"(ya) : "memory"); SB();
  asm volatile("ds_read_b64_tr_b16 %0, %1 offset:8192" : "=&v"(yr[4]) : "v"(ya) : "memory"); SB();
  asm volatile("" : "+v"(o3[0][4]), "+v"(o3[1][4]), "+v"(o3[2][4]), "+v"(o3[3][4]) :  : "memory"); SB();
#pragma unroll
  for (int j = 4; j <= 4; ++j) {
    const f32x4 o4 = (o3[0][j] + o3[1][j]) + (o3[2][j] + o3[3][j]);
#pragma unroll
    for (int h = 0; h < 2; ++h) {
      const f32x2 o = {o4[2 * h], o4[2 * h + 1]};
      const f32x2 a = o * (f32x2){-1.4426950f, -1.4426950f};
      const f32x2 e = (f32x2){__builtin_amdgcn_exp2f(a[0]), __builtin_amdgcn_exp2f(a[1])} + (f32x2){1.0f, 1.0f};
      const f32x2 r = {__builtin_amdgcn_rcpf(e[0]), __builtin_amdgcn_rcpf(e[1])};
      sv[j][h] = (sv[j][h] * o) * r;
    }
  }
  asm volatile("" : "+v"(sv[4][0]), "+v"(sv[4][1]));
  SB();
  asm volatile("s_waitcnt lgkmcnt(0)" : "+v"(yr[0]), "+v"(yr[1]), "+v"(yr[2]), "+v"(yr[3]), "+v"(yr[4]) :  : "memory"); SB();
  {
    unsigned pk[5][2];
#pragma unroll
    for (int n = 0; n < 10; ++n) pk[n >> 1][n & 1] = pk2(__builtin_elementwise_fma(sv[n >> 1][n & 1], unpk(yr[n >> 1][n & 1]), xf[n >> 1][n & 1]));
#pragma unroll
    for (int q = 0; q < 2; ++q) {
      const auto s0 = __builtin_amdgcn_permlane16_swap(pk[2 * q][0], pk[2 * q + 1][0], false, false);
      const auto s1 = __builtin_amdgcn_permlane16_swap(pk[2 * q][1], pk[2 * q + 1][1], false, false);
      asm volatile("s_nop 1\n\tglobal_store_dwordx4 %0, %1, %2 offset:%3 " :: "v"(po), "v"((u32x4){s0[0], s1[0], s0[1], s1[1]}), "s"(out), "i"(64 * q) : "memory");
    }
    asm volatile("global_store_dwordx2 %0, %1, %2 offset:128 " :: "v"(vX), "v"((u32x2){pk[4][0], pk[4][1]}), "s"(out) : "memory");
  }
  ST(23);
  ST_OUT();
  }}}
}

extern "C" __global__ void k_end() { asm volatile(".subsection 2\n.Lcode_end:\n\ts_endpgm\n\t.subsection 0"); }

extern "C" void kernel_launch(void* const* d_in, const int* in_sizes, int n_in,
                              void* d_out, int out_size, void* d_ws, size_t ws_size,
                              hipStream_t stream) {
  (void)hipFuncSetAttribute((const void*)k_all, hipFuncAttributeMaxDynamicSharedMemorySize, 163840);
  k_all<<<256, 256, 163840, stream>>>((const char*)d_in[0], (const char*)d_in[1], (const char*)d_in[2], (const char*)d_in[3], (const char*)d_in[4],
                                      (const char*)d_in[5], (const char*)d_in[7], (const char*)d_in[8], (const char*)d_in[9], (const char*)d_in[10],
                                      (char*)d_out, (char*)d_ws);
}
```

```cpp
#include <hip/hip_runtime.h>
#include <stdint.h>

typedef unsigned short u16;
typedef __attribute__((ext_vector_type(4))) float f32x4;
typedef __attribute__((ext_vector_type(2))) float f32x2;
typedef __attribute__((ext_vector_type(4))) unsigned int u32x4;
typedef __attribute__((ext_vector_type(2))) unsigned int u32x2;
typedef __attribute__((ext_vector_type(2))) __bf16 bf16x2n;
typedef __attribute__((ext_vector_type(8))) unsigned long long u64x8;
typedef __attribute__((ext_vector_type(4))) unsigned long long u64x4;
#define LDS_AS __attribute__((address_space(3)))
#define GAS __attribute__((address_space(1)))
#define SB() __builtin_amdgcn_sched_barrier(0)
#define DI __device__ __forceinline__

#define SGA
#define ST_DECL do {} while (0)
#define ST(i) do {} while (0)
#define ST_OUT() do {} while (0)

constexpr int S_ = 2048, D_ = 640;
constexpr unsigned HOLE = 0xFFFFFFFFu;
constexpr int W_ST = 4096, W_FZ = 768 << 10, W_FK = W_FZ + 8192, W_FH = W_FK + 8192, W_KV = 1 << 20, W_Z2 = 4 << 20, W_HH = 11 << 20, W_WO = 14 << 20;

#define wm (w >> 1)
#define wn (w & 1)
#define g4 (lane >> 4)
#define l15 (lane & 15)
#define q4 ((lane >> 2) & 3)
#define p4 (lane & 3)
#define l16 (lane * 16)
#define nch (3 - (rb >> 4))
#define ch0 (cb * 80 + rb * 2 + min(rb, 16u))
#define c2o ((nch - 2) * 2)
#define c2s (8192u + ((rb >> 4) << 30))
#define w3s ((w + 1) >> 2 << 30)
#define vZ (W_Z2 + ((rb * 2 + wm) * 64 + cb * 6 + wn * 3) * 1024 + l16)
#define vH (W_HH + ((rb * 4 + w) * 24 + cb * 3) * 1024 + l16)

DI unsigned pack2(float lo, float hi) {
  f32x2 v = {lo, hi};
  return __builtin_bit_cast(unsigned, __builtin_convertvector(v, bf16x2n));
}
DI float lo16(unsigned w) { return __uint_as_float(w << 16); }
DI float hi16(unsigned w) { return __uint_as_float(w & 0xFFFF0000u); }
DI float rbf(float f) { return lo16(pack2(f, f)); }
DI f32x2 unpk(unsigned w) { f32x2 r = {lo16(w), hi16(w)}; return r; }
DI unsigned pk2(f32x2 v) { return pack2(v[0], v[1]); }
DI unsigned nohole(unsigned a) { return a < HOLE - 1 ? a : HOLE - 1; }
template <int CTRL> DI float dppf(float v) {
  return __builtin_bit_cast(float, __builtin_amdgcn_update_dpp(0, __builtin_bit_cast(int, v), CTRL, 0xF, 0xF, true));
}
DI float red16(float v) {
  const auto a = __builtin_amdgcn_permlane16_swap(__float_as_uint(v), __float_as_uint(v), false, false);
  return __uint_as_float(a[0]) + __uint_as_float(a[1]);
}
DI float red32(float v) {
  const auto c = __builtin_amdgcn_permlane32_swap(__float_as_uint(v), __float_as_uint(v), false, false);
  return __uint_as_float(c[0]) + __uint_as_float(c[1]);
}
DI float sel32(float a, float b) {
  const auto c = __builtin_amdgcn_permlane32_swap(__float_as_uint(a), __float_as_uint(b), false, false);
  return __uint_as_float(c[0]) + __uint_as_float(c[1]);
}
DI unsigned lds_addr(const void* p) { return (unsigned)(uintptr_t)(LDS_AS const char*)p; }

__global__ __launch_bounds__(256, 1) __attribute__((amdgpu_num_vgpr(160))) SGA void k_all(
    const char*, const char*, const char*, const char*, const char*, const char*, const char*, const char*, const char*, const char*, char*, char*) {
  extern __shared__ __attribute__((aligned(16))) char smem[];
  ST_DECL;
  ST(0);
  u64x8 ka0; u64x4 ka1;
  asm volatile("s_load_dwordx16 %0, %2, 0x0\n\ts_load_dwordx8 %1, %2, 0x40" : "=&s"(ka0), "=&s"(ka1) : "s"(__builtin_amdgcn_kernarg_segment_ptr()) : "memory"); SB();
  const unsigned tid = threadIdx.x, lane0 = tid & 63, w0 = __builtin_amdgcn_readfirstlane(tid >> 6);
  const unsigned b = blockIdx.x, xcd = b & 7, idx = b >> 3;
  const unsigned rb0 = 8 * (xcd >> 1) + (idx >> 2), cb0 = 4 * (xcd & 1) + (idx & 3);
  const unsigned lb = lds_addr(smem);
  const unsigned lane = lane0, w = w0, rb = rb0, cb = cb0;
  {
    unsigned long long pc; unsigned clen;
    asm volatile("s_getpc_b64 %0\n.Lpc_here_%=:\n\ts_mov_b32 %1, .Lcode_end-.Lpc_here_%=" : "=s"(pc), "=s"(clen));
    const unsigned o = min((((lane & 3) * 4 + w) * 32 + idx) * 128, clen - 4u) & ~3u;
    asm volatile("s_mov_b64 exec, 15\n\tglobal_load_dword v254, %0, %1\n\ts_mov_b64 exec, -1" :: "v"(o), "s"(pc) : "memory");
    SB();
  }
  unsigned voA = (rb * 64 + w * 16) * 1280 + __umul24(lane >> 3, 1280) + (((lane & 7) ^ (lane >> 3)) << 4), voB[3];
#pragma unroll
  for (int i = 0; i < 3; ++i) { const unsigned r = __umul24(i * 64 + lane, 171) >> 11; voB[i] = w * 49152 + cb * 192 + __umul24(r, 2880) + ((lane ^ ((r >> 3) << 1)) << 4); }
  unsigned mX = lb + w * 2048, mW = lb + w * 3072, mA;
  const unsigned rot = ((rb ^ cb) & 3) | (cb < 3 ? 4 : cb == 3 ? (~rb & 2) << 1 : 0);
  unsigned kx[10], kw_[10], voA2 = voA + 9216;
  auto kofs = [&](int s) {
    asm volatile("s_xor_b32 %0, %2, %3\n\ts_mul_i32 %1, %0, 0x30000\n\ts_lshl_b32 %0, %0, 7" : "=&s"(kx[s]), "=&s"(kw_[s]) : "s"(s < 8 ? rot : rot & 1), "i"(s) : "scc");
  };
  kofs(0); kofs(1);
  asm volatile("" : "+v"(voA), "+v"(voA2), "+v"(voB[0]), "+v"(voB[1]), "+v"(voB[2]));
  asm volatile("" : "+s"(mX), "+s"(mW)); SB();
  asm volatile("s_waitcnt lgkmcnt(0)" : "+s"(ka0), "+s"(ka1) :: "memory"); SB();
  const char *x = (const char*)ka0[0], *kin = (const char*)ka0[1], *vin = (const char*)ka0[2], *shift_k = (const char*)ka0[3], *ssm_k = (const char*)ka0[4];
  const char *ssm_D = (const char*)ka0[5], *w_in = (const char*)ka0[6], *w_sp = (const char*)ka0[7], *b_sp = (const char*)ka1[0], *w_out = (const char*)ka1[1];
  char *out = (char*)ka1[2], *ws = (char*)ka1[3];
  const auto rX = __builtin_amdgcn_make_buffer_rsrc((void*)x, 0, S_ * D_ * 2, 0x00020000);
  const auto rW = __builtin_amdgcn_make_buffer_rsrc((void*)(w_in + 1536), 0, 640 * 3072 - 1536, 0x00020000);
  asm volatile("; named registers" :  :  : "v255", "a255"); SB();
  asm volatile("s_add_u32 m0, %0, 81920" :  : "s"(mX) : "memory", "scc"); SB();
  asm volatile("s_nop 0\n\tbuffer_load_dwordx4 %0, %1, %2 offen lds" :  : "v"(voA), "s"(rX), "s"(kx[0]) : "memory"); SB();
  asm volatile("buffer_load_dwordx4 %0, %1, %2 offen offset:1024 lds" :  : "v"(voA2), "s"(rX), "s"(kx[0]) : "memory"); SB();
  asm volatile("s_add_u32 m0, %0, 0" :  : "s"(mW) : "memory", "scc"); SB();
  asm volatile("s_nop 0\n\tbuffer_load_dwordx4 %0, %1, %2 offen lds" :  : "v"(voB[0]), "s"(rW), "s"(kw_[0]) : "memory"); SB();
  asm volatile("buffer_load_dwordx4 %0, %1, %2 offen offset:1024 lds" :  : "v"(voB[1]), "s"(rW), "s"(kw_[0]) : "memory"); SB();
  asm volatile("buffer_load_dwordx4 %0, %1, %2 offen offset:2048 lds" :  : "v"(voB[2]), "s"(rW), "s"(kw_[0]) : "memory"); SB();
  asm volatile("s_add_u32 m0, %0, 90112" :  : "s"(mX) : "memory", "scc"); SB();
  asm volatile("s_nop 0\n\tbuffer_load_dwordx4 %0, %1, %2 offen lds" :  : "v"(voA), "s"(rX), "s"(kx[1]) : "memory"); SB();
  asm volatile("buffer_load_dwordx4 %0, %1, %2 offen offset:1024 lds" :  : "v"(voA2), "s"(rX), "s"(kx[1]) : "memory"); SB();
  kofs(2); kofs(3); kofs(4); kofs(5); kofs(6); kofs(7); kofs(8); kofs(9);
  const auto rWS = __builtin_amdgcn_make_buffer_rsrc((void*)ws, 0, 32 << 20, 0x00020000);
  const auto rW1 = __builtin_amdgcn_make_buffer_rsrc((void*)w_in, 0, 640 * 3072, 0x00020000);
  mA = lb + w * 8192; asm volatile("" : "+s"(mA));
  { unsigned lane = lane0, w = w0, rb = rb0, cb = cb0;
  asm volatile("" : "+v"(lane)); asm volatile("" : "+s"(w), "+s"(rb), "+s"(cb)); SB();

  unsigned ao[8];
  u32x2 kp[5], kc[5], vv[5], sw[5], wt[2], ko[5];
  u32x4 kq[3];
  f32x2 kt[2], kf[2];
  unsigned ku[5];
  unsigned aX[2][2], aW[2][2][3];
#pragma unroll
  for (int n = 0; n < 4; ++n) aX[n >> 1][n & 1] = lb + 98304 + (wm * 32 + ((n & 1) ^ wn) * 16 + l15) * 128 + (((((n >> 1) ^ wm) * 4 + g4) ^ (l15 & 7)) << 4);
#pragma unroll
  for (int n = 0; n < 6; ++n) {
    aW[0][n / 3][n % 3] = lb + ((n / 3 ^ wm) * 32 + 8 * g4 + q4) * 192 + (((wn * 3 + n % 3) ^ (g4 & 1)) << 5) + 8 * p4;
    aW[1][n / 3][n % 3] = aW[0][n / 3][n % 3] + 49152;
    asm volatile("" : "+v"(aW[0][n / 3][n % 3]), "+v"(aW[1][n / 3][n % 3]));
  }
  f32x4 co[3];
  u32x4 ones = {0x3F803F80u, 0x3F803F80u, 0x3F803F80u, 0x3F803F80u};
  f32x4 a2[2][3], a1[2][3], rs, gr, c2[3], c1[3];
  u32x2 zt[3][2];
  asm volatile("" : "+v"(ones), "+v"(aX[0][0]), "+v"(aX[0][1]), "+v"(aX[1][0]), "+v"(aX[1][1]));
  int tmo = (1 << 18);
  u32x2 fk, fz, fh, fi;
  unsigned tg0, tg1;
  float xm[2], xd[2], k1[2], k2[2];
  f32x4 xs[2];
  float xt[4];
  u32x2 xv[5];
  unsigned mU, uso, w3k[2], sFK, sFZ;
  float dD[3], bs;
  u32x4 im[6], zero4;
  SB();
  asm volatile("s_add_u32 m0, %0, 12288" :  : "s"(mW) : "memory", "scc"); SB();
  asm volatile("s_nop 0\n\tbuffer_load_dwordx4 %0, %1, %2 offen lds" :  : "v"(voB[0]), "s"(rW), "s"(kw_[1]) : "memory"); SB();
  asm volatile("buffer_load_dwordx4 %0, %1, %2 offen offset:1024 lds" :  : "v"(voB[1]), "s"(rW), "s"(kw_[1]) : "memory"); SB();
  asm volatile("buffer_load_dwordx4 %0, %1, %2 offen offset:2048 lds" :  : "v"(voB[2]), "s"(rW), "s"(kw_[1]) : "memory"); SB();
  const auto rK = __builtin_amdgcn_make_buffer_rsrc((void*)kin, 0, S_ * D_ * 2, 0x00020000);
  const auto rV = __builtin_amdgcn_make_buffer_rsrc((void*)vin, 0, S_ * D_ * 2, 0x00020000);
  const auto rS = __builtin_amdgcn_make_buffer_rsrc((void*)(shift_k - 3328), 0, D_ * 8 + 3328, 0x00020000);
  const auto rO = __builtin_amdgcn_make_buffer_rsrc((void*)(w_out - 3072), 0, 768 * 1280 + 3072, 0x00020000);
  unsigned voK[4], voV[3], voH = cb * 640 + l16;
#pragma unroll
  for (int n = 0; n < 4; ++n) { const unsigned o = n * 64 + lane, r = (o * 205) >> 11; voK[n] = (rb * 64 + w * 16 - 4) * 1280 + cb * 160 + r * 1120 + lane * 16; }
#pragma unroll
  for (int n = 0; n < 3; ++n) voV[n] = voK[n] + 5120;
  const unsigned wof = (rb >> 3) * 30 + min((rb & 7) * 4 + w, 29u), wok = (wof * 205) >> 10;
  unsigned voO = (lane >> 1) * 1280 + (lane & 1) * 16 + wok * 40960 + (wof - 5 * wok) * 32 + cb * 160, vWO = W_WO + (cb * 120 + wof) * 1024 + l16;
  asm volatile("" : "+v"(vWO), "+v"(voK[0]), "+v"(voK[1]), "+v"(voK[2]), "+v"(voK[3]), "+v"(voV[0]), "+v"(voV[1]), "+v"(voV[2]), "+v"(voH), "+v"(voO));
  SB();
  const auto rF = __builtin_amdgcn_make_buffer_rsrc((void*)ssm_k, 0, S_ * D_ * 2, 0x00020000);
  unsigned vF = ch0 * 4096 + w * 1024 + l16, c2s_ = c2s;
  unsigned hpo[3], vD = ch0 * 2, vD2 = vD + c2o * 2 - 4, vBs = (rb * 64 + w * 16 + l15) * 2;
  hpo[0] = w ? ch0 * 4096 + w * 1024 - 4 : 0x40000000u; hpo[1] = hpo[0] + 4096; hpo[2] = hpo[0] + c2s;
  asm volatile("" : "+s"(hpo[0]), "+s"(hpo[1]), "+s"(hpo[2]), "+v"(vD), "+v"(vD2), "+v"(vBs), "+v"(vF), "+s"(c2s_));
  SB();
  unsigned ka = lb + 98304 + w * 8192 + (4 * p4 + q4) * 160 + 8 * g4, oa = lb + 98304 + 7168 + w * 8192 + (4 * g4 + q4) * 32 + 8 * p4;
  unsigned sa = lb + 98304 + 3328 + w * 8192 + (4 * g4 + p4) * 8;
  unsigned kw = lb + 12288 + (4 * g4 + p4) * 144 + w * 32 + q4 * 8, kr = lb + 12288 + (w * 24 + (lane >> 3)) * 144 + (lane & 7) * 16;
  unsigned vKV = W_KV + (cb * 80 + w * 24 + (lane >> 3)) * 4096 + rb * 128 + (lane & 7) * 16;
  unsigned vS = W_ST + (rb * 2 + wm) * 8192 + l16;
  asm volatile("" : "+v"(ka), "+v"(oa), "+v"(sa), "+v"(kw), "+v"(kr), "+v"(vKV), "+v"(vS));
  SB();
  ST(1);
  SB();
  asm volatile("s_add_u32 m0, %0, 98304" :  : "s"(mX) : "memory", "scc"); SB();
  asm volatile("s_nop 0\n\tbuffer_load_dwordx4 %0, %1, %2 offen lds" :  : "v"(voA), "s"(rX), "s"(kx[2]) : "memory"); SB();
  asm volatile("buffer_load_dwordx4 %0, %1, %2 offen offset:1024 lds" :  : "v"(voA2), "s"(rX), "s"(kx[2]) : "memory"); SB();
  asm volatile("buffer_load_dwordx2 v[252:253], off, %0, 0 sc1" :  : "s"(rWS) : "memory"); SB();
  asm volatile("s_add_u32 m0, %0, 106496" :  : "s"(mX) : "memory", "scc"); SB();
  asm volatile("s_nop 0\n\tbuffer_load_dwordx4 %0, %1, %2 offen lds" :  : "v"(voA), "s"(rX), "s"(kx[3]) : "memory"); SB();
  asm volatile("buffer_load_dwordx4 %0, %1, %2 offen offset:1024 lds" :  : "v"(voA2), "s"(rX), "s"(kx[3]) : "memory"); SB();
  asm volatile("s_add_u32 m0, %0, 24576" :  : "s"(mW) : "memory", "scc"); SB();
  asm volatile("s_nop 0\n\tbuffer_load_dwordx4 %0, %1, %2 offen lds" :  : "v"(voB[0]), "s"(rW), "s"(kw_[2]) : "memory"); SB();
  asm volatile("s_waitcnt vmcnt(11) lgkmcnt(0)\n\ts_barrier" :  :  : "memory"); SB();
  ST(4);
  asm volatile("ds_read_b128 a[0:3], %0 offset:0" :  : "v"((aX[0][0] - 16384)) : "memory"); SB();
  asm volatile("ds_read_b128 a[4:7], %0 offset:0" :  : "v"((aX[0][1] - 16384)) : "memory"); SB();
  asm volatile("ds_read_b64_tr_b16 v[160:161], %0 offset:0" :  : "v"(aW[0][0][0]) : "memory"); SB();
  asm volatile("ds_read_b64_tr_b16 v[162:163], %0 offset:768" :  : "v"(aW[0][0][0]) : "memory"); SB();
  asm volatile("ds_read_b64_tr_b16 v[164:165], %0 offset:0" :  : "v"(aW[0][0][1]) : "memory"); SB();
  asm volatile("ds_read_b64_tr_b16 v[166:167], %0 offset:768" :  : "v"(aW[0][0][1]) : "memory"); SB();
  asm volatile("ds_read_b64_tr_b16 v[168:169], %0 offset:0" :  : "v"(aW[0][0][2]) : "memory"); SB();
  asm volatile("ds_read_b64_tr_b16 v[170:171], %0 offset:768" :  : "v"(aW[0][0][2]) : "memory"); SB();
  asm volatile("ds_read_b128 a[8:11], %0 offset:0" :  : "v"((aX[1][0] - 16384)) : "memory"); SB();
  asm volatile("ds_read_b128 a[12:15], %0 offset:0" :  : "v"((aX[1][1] - 16384)) : "memory"); SB();
  asm volatile("s_waitcnt lgkmcnt(6)\n\tv_mfma_f32_16x16x32_bf16 %0, v[160:163], a[0:3], 0" : "=&v"(a2[0][0]) :  : ); SB();
  asm volatile("ds_read_b64_tr_b16 v[172:173], %0 offset:0" :  : "v"(aW[0][1][0]) : "memory"); SB();
  asm volatile("buffer_load_dword v242, off, %0, %1" :  : "s"(rF), "s"(hpo[0]) : "memory"); SB();
  asm volatile("v_mfma_f32_16x16x32_bf16 %0, v[160:163], a[4:7], 0" : "=&v"(a2[1][0]) :  : ); SB();
  asm volatile("ds_read_b64_tr_b16 v[174:175], %0 offset:768" :  : "v"(aW[0][1][0]) : "memory"); SB();
  asm volatile("buffer_load_dwordx4 %0, %1, %2 offen offset:1024 lds" :  : "v"(voB[1]), "s"(rW), "s"(kw_[2]) : "memory"); SB();
  asm volatile("v_mfma_f32_16x16x32_bf16 %0, v[160:163], %1, 0" : "=&v"(c2[0]) : "v"(ones) : ); SB();
  asm volatile("ds_read_b64_tr_b16 v[176:177], %0 offset:0" :  : "v"(aW[0][1][1]) : "memory"); SB();
  asm volatile("s_waitcnt lgkmcnt(5)\n\tv_mfma_f32_16x16x32_bf16 %0, v[164:167], a[0:3], 0" : "=&v"(a2[0][1]) :  : ); SB();
  asm volatile("ds_read_b64_tr_b16 v[178:179], %0 offset:768" :  : "v"(aW[0][1][1]) : "memory"); SB();
  asm volatile("v_mfma_f32_16x16x32_bf16 %0, v[164:167], a[4:7], 0" : "=&v"(a2[1][1]) :  : ); SB();
  asm volatile("ds_read_b64_tr_b16 v[180:181], %0 offset:0" :  : "v"(aW[0][1][2]) : "memory"); SB();
  asm volatile("buffer_load_dwordx4 %0, %1, %2 offen offset:2048 lds" :  : "v"(voB[2]), "s"(rW), "s"(kw_[2]) : "memory"); SB();
  asm volatile("v_mfma_f32_16x16x32_bf16 %0, v[164:167], %1, 0" : "=&v"(c2[1]) : "v"(ones) : ); SB();
  asm volatile("ds_read_b64_tr_b16 v[182:183], %0 offset:768" :  : "v"(aW[0][1][2]) : "memory"); SB();
  asm volatile("v_mfma_f32_16x16x32_bf16 %0, v[168:171], a[0:3], 0" : "=&v"(a2[0][2]) :  : ); SB();
  unsigned aV[5];
#pragma unroll
  for (int j = 0; j < 5; ++j) {
    const unsigned c = cb * 10 + 2 * j, t = (c >> 3) ^ (c < 64 ? rot : rot & 1);
    aV[j] = lb + 81920 + t * 8192 + w * 2048 + l15 * 128 + ((((c & 7) + (g4 >> 1)) ^ (l15 & 7)) << 4) + (g4 & 1) * 8;
    asm volatile("" : "+v"(aV[j]));
  }
  SB();
  asm volatile("global_load_ushort v245, %0, %1 offset:0" :  : "v"(vD), "s"(ssm_D) : "memory"); SB();
  asm volatile("s_add_u32 m0, %0, 114688" :  : "s"(mX) : "memory", "scc"); SB();
  asm volatile("s_nop 0\n\tbuffer_load_dwordx4 %0, %1, %2 offen lds" :  : "v"(voA), "s"(rX), "s"(kx[4]) : "memory"); SB();
  asm volatile("s_waitcnt vmcnt(11)\n\ts_barrier" :  :  : "memory"); SB();
  asm volatile("ds_read_b64_tr_b16 v[184:185], %0 offset:12288" :  : "v"(aW[0][0][0]) : "memory"); SB();
  asm volatile("buffer_load_dwordx4 %0, %1, %2 offen offset:1024 lds" :  : "v"(voA2), "s"(rX), "s"(kx[4]) : "memory"); SB();
  asm volatile("v_mfma_f32_16x16x32_bf16 %0, v[168:171], a[4:7], 0" : "=&v"(a2[1][2]) :  : ); SB();
  asm volatile("ds_read_b64_tr_b16 v[186:187], %0 offset:13056" :  : "v"(aW[0][0][0]) : "memory"); SB();
  asm volatile("v_mfma_f32_16x16x32_bf16 %0, v[168:171], %1, 0" : "=&v"(c2[2]) : "v"(ones) : ); SB();
  asm volatile("ds_read_b64_tr_b16 v[188:189], %0 offset:12288" :  : "v"(aW[0][0][1]) : "memory"); SB();
  asm volatile("v_mfma_f32_16x16x32_bf16 %0, %1, a[0:3], 0" : "=&v"(rs) : "v"(ones) : ); SB();
  asm volatile("ds_read_b128 a[16:19], %0 offset:8192" :  : "v"((aX[0][0] - 16384)) : "memory"); SB();
  asm volatile("ds_read_b128 a[20:23], %0 offset:8192" :  : "v"((aX[0][1] - 16384)) : "memory"); SB();
  asm volatile("ds_read_b64_tr_b16 v[190:191], %0 offset:13056" :  : "v"(aW[0][0][1]) : "memory"); SB();
  asm volatile("v_mfma_f32_16x16x32_bf16 %0, a[0:3], a[0:3], 0" : "=&v"(gr) :  : ); SB();
  asm volatile("ds_read_b64_tr_b16 v[192:193], %0 offset:12288" :  : "v"(aW[0][0][2]) : "memory"); SB();
  asm volatile("s_waitcnt lgkmcnt(7)\n\tv_mfma_f32_16x16x32_bf16 %0, v[172:175], a[8:11], %0" : "+v"(a2[0][0]) :  : ); SB();
  asm volatile("ds_read_b64_tr_b16 v[194:195], %0 offset:13056" :  : "v"(aW[0][0][2]) : "memory"); SB();
  asm volatile("v_mfma_f32_16x16x32_bf16 %0, v[172:175], a[12:15], %0" : "+v"(a2[1][0]) :  : ); SB();
  asm volatile("ds_read_b128 a[24:27], %0 offset:8192" :  : "v"((aX[1][0] - 16384)) : "memory"); SB();
  asm volatile("v_mfma_f32_16x16x32_bf16 %0, v[176:179], a[8:11], %0" : "+v"(a2[0][1]) :  : ); SB();
  asm volatile("buffer_load_dword v243, off, %0, %1" :  : "s"(rF), "s"(hpo[1]) : "memory"); SB();
  asm volatile("s_add_u32 m0, %0, 122880" :  : "s"(mX) : "memory", "scc"); SB();
  asm volatile("v_mfma_f32_16x16x32_bf16 %0, v[176:179], a[12:15], %0" : "+v"(a2[1][1]) :  : ); SB();
  asm volatile("buffer_load_dwordx4 %0, %1, %2 offen lds" :  : "v"(voA), "s"(rX), "s"(kx[5]) : "memory"); SB();
  asm volatile("v_mfma_f32_16x16x32_bf16 %0, v[180:183], a[8:11], %0" : "+v"(a2[0][2]) :  : ); SB();
  asm volatile("ds_read_b64_tr_b16 v[160:161], %0 offset:12288" :  : "v"(aW[0][1][0]) : "memory"); SB();
  asm volatile("v_mfma_f32_16x16x32_bf16 %0, v[180:183], a[12:15], %0" : "+v"(a2[1][2]) :  : ); SB();
  asm volatile("ds_read_b64_tr_b16 v[162:163], %0 offset:13056" :  : "v"(aW[0][1][0]) : "memory"); SB();
  asm volatile("v_mfma_f32_16x16x32_bf16 %0, %1, a[8:11], %0" : "+v"(rs) : "v"(ones) : ); SB();
  asm volatile("ds_read_b64_tr_b16 v[164:165], %0 offset:12288" :  : "v"(aW[0][1][1]) : "memory"); SB();
  asm volatile("buffer_load_dwordx4 %0, %1, %2 offen offset:1024 lds" :  : "v"(voA2), "s"(rX), "s"(kx[5]) : "memory"); SB();
  asm volatile("v_mfma_f32_16x16x32_bf16 %0, a[8:11], a[8:11], %0" : "+v"(gr) :  : ); SB();
  asm volatile("ds_read_b64_tr_b16 v[166:167], %0 offset:13056" :  : "v"(aW[0][1][1]) : "memory"); SB();
  asm volatile("s_waitcnt lgkmcnt(7)\n\tv_mfma_f32_16x16x32_bf16 %0, v[184:187], a[16:19], %0" : "+v"(a2[0][0]) :  : ); SB();
  asm volatile("ds_read_b64_tr_b16 v[168:169], %0 offset:12288" :  : "v"(aW[0][1][2]) : "memory"); SB();
  asm volatile("v_mfma_f32_16x16x32_bf16 %0, v[184:187], a[20:23], %0" : "+v"(a2[1][0]) :  : ); SB();
  asm volatile("ds_read_b128 a[28:31], %0 offset:8192" :  : "v"((aX[1][1] - 16384)) : "memory"); SB();
  asm volatile("ds_read_b64_tr_b16 v[170:171], %0 offset:13056" :  : "v"(aW[0][1][2]) : "memory"); SB();
  asm volatile("s_add_u32 m0, %0, 36864" :  : "s"(mW) : "memory", "scc"); SB();
  asm volatile("v_mfma_f32_16x16x32_bf16 %0, v[184:187], %1, %0" : "+v"(c2[0]) : "v"(ones) : ); SB();
  asm volatile("buffer_load_dwordx4 %0, %1, %2 offen lds" :  : "v"(voB[0]), "s"(rW), "s"(kw_[3]) : "memory"); SB();
  asm volatile("v_mfma_f32_16x16x32_bf16 %0, v[188:191], a[16:19], %0" : "+v"(a2[0][1]) :  : ); SB();
  asm volatile("global_load_ushort v246, %0, %1 offset:2" :  : "v"(vD), "s"(ssm_D) : "memory"); SB();
  asm volatile("v_mfma_f32_16x16x32_bf16 %0, v[188:191], a[20:23], %0" : "+v"(a2[1][1]) :  : ); SB();
  asm volatile("buffer_load_dwordx4 %0, %1, %2 offen offset:1024 lds" :  : "v"(voB[1]), "s"(rW), "s"(kw_[3]) : "memory"); SB();
  asm volatile("buffer_load_dwordx4 %0, %1, %2 offen offset:2048 lds" :  : "v"(voB[2]), "s"(rW), "s"(kw_[3]) : "memory"); SB();
  asm volatile("s_add_u32 m0, %0, 131072" :  : "s"(mX) : "memory", "scc"); SB();
  asm volatile("s_nop 0\n\tbuffer_load_dwordx4 %0, %1, %2 offen lds" :  : "v"(voA), "s"(rX), "s"(kx[6]) : "memory"); SB();
  asm volatile("buffer_load_dwordx4 %0, %1, %2 offen offset:1024 lds" :  : "v"(voA2), "s"(rX), "s"(kx[6]) : "memory"); SB();
  asm volatile("buffer_load_dword v244, off, %0, %1" :  : "s"(rF), "s"(hpo[2]) : "memory"); SB();
  asm volatile("s_add_u32 m0, %0, 139264" :  : "s"(mX) : "memory", "scc"); SB();
  asm volatile("s_nop 0\n\tbuffer_load_dwordx4 %0, %1, %2 offen lds" :  : "v"(voA), "s"(rX), "s"(kx[7]) : "memory"); SB();
  asm volatile("global_load_ushort v247, %0, %1 offset:4" :  : "v"(vD2), "s"(ssm_D) : "memory"); SB();
  asm volatile("s_waitcnt vmcnt(15)\n\ts_barrier" :  :  : "memory"); SB();
  asm volatile("ds_read_b64_tr_b16 v[172:173], %0 offset:24576" :  : "v"(aW[0][0][0]) : "memory"); SB();
  asm volatile("v_mfma_f32_16x16x32_bf16 %0, v[188:191], %1, %0" : "+v"(c2[1]) : "v"(ones) : ); SB();
  asm volatile("ds_read_b64_tr_b16 v[174:175], %0 offset:25344" :  : "v"(aW[0][0][0]) : "memory"); SB();
  asm volatile("s_waitcnt lgkmcnt(2)\n\tv_mfma_f32_16x16x32_bf16 %0, v[192:195], a[16:19], %0" : "+v"(a2[0][2]) :  : ); SB();
  asm volatile("ds_read_b64_tr_b16 v[176:177], %0 offset:24576" :  : "v"(aW[0][0][1]) : "memory"); SB();
  asm volatile("v_mfma_f32_16x16x32_bf16 %0, v[192:195], a[20:23], %0" : "+v"(a2[1][2]) :  : ); SB();
  asm volatile("ds_read_b64_tr_b16 v[178:179], %0 offset:25344" :  : "v"(aW[0][0][1]) : "memory"); SB();
  asm volatile("v_mfma_f32_16x16x32_bf16 %0, v[192:195], %1, %0" : "+v"(c2[2]) : "v"(ones) : ); SB();
  asm volatile("ds_read_b64_tr_b16 v[180:181], %0 offset:24576" :  : "v"(aW[0][0][2]) : "memory"); SB();
  asm volatile("v_mfma_f32_16x16x32_bf16 %0, %1, a[16:19], %0" : "+v"(rs) : "v"(ones) : ); SB();
  asm volatile("ds_read_b128 a[32:35], %0 offset:0" :  : "v"(aX[0][0]) : "memory"); SB();
  asm volatile("ds_read_b128 a[36:39], %0 offset:0" :  : "v"(aX[0][1]) : "memory"); SB();
  asm volatile("ds_read_b64_tr_b16 v[182:183], %0 offset:25344" :  : "v"(aW[0][0][2]) : "memory"); SB();
  asm volatile("v_mfma_f32_16x16x32_bf16 %0, a[16:19], a[16:19], %0" : "+v"(gr) :  : ); SB();
  asm volatile("ds_read_b128 a[40:43], %0 offset:0" :  : "v"(aX[1][0]) : "memory"); SB();
  asm volatile("v_mfma_f32_16x16x32_bf16 %0, v[160:163], a[24:27], %0" : "+v"(a2[0][0]) :  : ); SB();
  asm volatile("ds_read_b128 a[44:47], %0 offset:0" :  : "v"(aX[1][1]) : "memory"); SB();
  asm volatile("v_mfma_f32_16x16x32_bf16 %0, v[160:163], a[28:31], %0" : "+v"(a2[1][0]) :  : ); SB();
  asm volatile("ds_read_b128 a[48:51], %0 offset:8192" :  : "v"(aX[0][0]) : "memory"); SB();
  asm volatile("v_mfma_f32_16x16x32_bf16 %0, v[164:167], a[24:27], %0" : "+v"(a2[0][1]) :  : ); SB();
  asm volatile("ds_read_b128 a[52:55], %0 offset:8192" :  : "v"(aX[0][1]) : "memory"); SB();
  asm volatile("v_mfma_f32_16x16x32_bf16 %0, v[164:167], a[28:31], %0" : "+v"(a2[1][1]) :  : ); SB();
  asm volatile("ds_read_b128 a[56:59], %0 offset:8192" :  : "v"(aX[1][0]) : "memory"); SB();
  asm volatile("v_mfma_f32_16x16x32_bf16 %0, v[168:171], a[24:27], %0" : "+v"(a2[0][2]) :  : ); SB();
  asm volatile("ds_read_b128 a[60:63], %0 offset:8192" :  : "v"(aX[1][1]) : "memory"); SB();
  asm volatile("v_mfma_f32_16x16x32_bf16 %0, v[168:171], a[28:31], %0" : "+v"(a2[1][2]) :  : ); SB();
  asm volatile("global_load_ushort v248, %0, %1" :  : "v"(vBs), "s"(b_sp) : "memory"); SB();
  asm volatile("v_mfma_f32_16x16x32_bf16 %0, %1, a[24:27], %0" : "+v"(rs) : "v"(ones) : ); SB();
  asm volatile("ds_read_b64_tr_b16 v[184:185], %0 offset:24576" :  : "v"(aW[0][1][0]) : "memory"); SB();
  asm volatile("v_mfma_f32_16x16x32_bf16 %0, a[24:27], a[24:27], %0" : "+v"(gr) :  : ); SB();
  asm volatile("ds_read_b64_tr_b16 v[186:187], %0 offset:25344" :  : "v"(aW[0][1][0]) : "memory"); SB();
  asm volatile("s_waitcnt lgkmcnt(9)\n\tv_mfma_f32_16x16x32_bf16 %0, v[172:175], a[32:35], %0" : "+v"(a2[0][0]) :  : ); SB();
  asm volatile("ds_read_b64_tr_b16 v[188:189], %0 offset:24576" :  : "v"(aW[0][1][1]) : "memory"); SB();
  asm volatile("buffer_load_dwordx4 %0, %1, %2 offen offset:1024 lds" :  : "v"(voA2), "s"(rX), "s"(kx[7]) : "memory"); SB();
  asm volatile("v_mfma_f32_16x16x32_bf16 %0, v[172:175], a[36:39], %0" : "+v"(a2[1][0]) :  : ); SB();
  asm volatile("ds_read_b64_tr_b16 v[190:191], %0 offset:25344" :  : "v"(aW[0][1][1]) : "memory"); SB();
  asm volatile("s_add_u32 m0, %0, 49152" :  : "s"(mW) : "memory", "scc"); SB();
  asm volatile("v_mfma_f32_16x16x32_bf16 %0, v[172:175], %1, %0" : "+v"(c2[0]) : "v"(ones) : ); SB();
  asm volatile("ds_read_b64_tr_b16 v[192:193], %0 offset:24576" :  : "v"(aW[0][1][2]) : "memory"); SB();
  asm volatile("v_mfma_f32_16x16x32_bf16 %0, v[176:179], a[32:35], %0" : "+v"(a2[0][1]) :  : ); SB();
  asm volatile("ds_read_b64_tr_b16 v[194:195], %0 offset:25344" :  : "v"(aW[0][1][2]) : "memory"); SB();
  asm volatile("v_mfma_f32_16x16x32_bf16 %0, v[176:179], a[36:39], %0" : "+v"(a2[1][1]) :  : ); SB();
  asm volatile("buffer_load_dwordx4 %0, %1, %2 offen lds" :  : "v"(voB[0]), "s"(rW), "s"(kw_[4]) : "memory"); SB();
  asm volatile("v_mfma_f32_16x16x32_bf16 %0, v[176:179], %1, %0" : "+v"(c2[1]) : "v"(ones) : ); SB();
  asm volatile("buffer_load_dwordx4 %0, %1, %2 offen offset:1024 lds" :  : "v"(voB[1]), "s"(rW), "s"(kw_[4]) : "memory"); SB();
  asm volatile("s_waitcnt lgkmcnt(6)\n\tv_mfma_f32_16x16x32_bf16 %0, v[180:183], a[32:35], %0" : "+v"(a2[0][2]) :  : ); SB();
  asm volatile("v_mfma_f32_16x16x32_bf16 %0, v[180:183], a[36:39], %0" : "+v"(a2[1][2]) :  : ); SB();
  asm volatile("v_mfma_f32_16x16x32_bf16 %0, v[180:183], %1, %0" : "+v"(c2[2]) : "v"(ones) : ); SB();
  const auto rA = __builtin_amdgcn_make_buffer_rsrc((void*)(w_sp - 3072), 0, S_ * S_ * 2 + 3072, 0x00020000);
  unsigned ca = lb + 98304 + 6912 + w * 8192 + g4 * 16, cr = lb + 98304 + 6912 + (w ^ 2) * 8192 + g4 * 16;
  unsigned voS = (rb * 64 + (lane >> 3)) * 4096 + w * 1024 + (cb & 1) * 128 + (((lane & 7) ^ (lane >> 3)) << 4), aA[2];
#pragma unroll
  for (int k = 0; k < 2; ++k) aA[k] = lb + 98304 + w * 8192 + l15 * 128 + (((k * 4 + g4) ^ (l15 & 7)) << 4);
  unsigned xw = lb + 98304 + 6656 + w * 8192 + l15 * 16, xr_ = lb + 98304 + 6656 + (w ^ 1) * 8192 + l15 * 16;
  unsigned vo = W_ST + ((rb * 2 + wm) * 8 + cb) * 1024 + wn * 128 + ((lane >> 5) ^ wn) * 512 + (l15 >> 3) * 256 + (l15 & 7) * 16;
  asm volatile("" : "+v"(ca), "+v"(cr), "+v"(voS), "+v"(aA[0]), "+v"(aA[1]), "+v"(xw), "+v"(xr_), "+v"(vo));
  SB();
  asm volatile("buffer_load_dwordx4 %0, %1, %2 offen offset:2048 lds" :  : "v"(voB[2]), "s"(rW), "s"(kw_[4]) : "memory"); SB();
  asm volatile("s_add_u32 m0, %0, 147456" :  : "s"(mX) : "memory", "scc"); SB();
  asm volatile("s_nop 0\n\tbuffer_load_dwordx4 %0, %1, %2 offen lds" :  : "v"(voA), "s"(rX), "s"(kx[8]) : "memory"); SB();
  asm volatile("s_waitcnt vmcnt(11)\n\ts_barrier" :  :  : "memory"); SB();
  asm volatile("ds_read_b64 %0, %1 offset:0" : "=&v"(xv[0]) : "v"(aV[0]) : "memory"); SB();
  asm volatile("ds_read_b64 %0, %1 offset:0" : "=&v"(xv[1]) : "v"(aV[1]) : "memory"); SB();
  asm volatile("ds_read_b64_tr_b16 v[160:161], %0 offset:36864" :  : "v"(aW[0][0][0]) : "memory"); SB();
  asm volatile("buffer_load_dwordx4 %0, %1, %2 offen offset:1024 lds" :  : "v"(voA2), "s"(rX), "s"(kx[8]) : "memory"); SB();
  asm volatile("v_mfma_f32_16x16x32_bf16 %0, %1, a[32:35], %0" : "+v"(rs) : "v"(ones) : ); SB();
  asm volatile("ds_read_b64 %0, %1 offset:0" : "=&v"(xv[2]) : "v"(aV[2]) : "memory"); SB();
  asm volatile("ds_read_b64 %0, %1 offset:0" : "=&v"(xv[3]) : "v"(aV[3]) : "memory"); SB();
  asm volatile("ds_read_b64_tr_b16 v[162:163], %0 offset:37632" :  : "v"(aW[0][0][0]) : "memory"); SB();
  asm volatile("s_add_u32 m0, %0, 155648" :  : "s"(mX) : "memory", "scc"); SB();
  asm volatile("v_mfma_f32_16x16x32_bf16 %0, a[32:35], a[32:35], %0" : "+v"(gr) :  : ); SB();
  asm volatile("ds_read_b64 %0, %1 offset:0" : "=&v"(xv[4]) : "v"(aV[4]) : "memory"); SB();
  asm volatile("ds_read_b64_tr_b16 v[164:165], %0 offset:36864" :  : "v"(aW[0][0][1]) : "memory"); SB();
  asm volatile("buffer_load_dwordx4 %0, %1, %2 offen lds" :  : "v"(voA), "s"(rX), "s"(kx[9]) : "memory"); SB();
  asm volatile("v_mfma_f32_16x16x32_bf16 %0, %1, a[40:43], %0" : "+v"(rs) : "v"(ones) : ); SB();
  asm volatile("ds_read_b64_tr_b16 v[166:167], %0 offset:37632" :  : "v"(aW[0][0][1]) : "memory"); SB();
  asm volatile("buffer_load_dwordx4 %0, %1, %2 offen offset:1024 lds" :  : "v"(voA2), "s"(rX), "s"(kx[9]) : "memory"); SB();
  asm volatile("v_mfma_f32_16x16x32_bf16 %0, a[40:43], a[40:43], %0" : "+v"(gr) :  : ); SB();
  asm volatile("ds_read_b64_tr_b16 v[168:169], %0 offset:36864" :  : "v"(aW[0][0][2]) : "memory"); SB();
  asm volatile("s_waitcnt lgkmcnt(10)\n\tv_mfma_f32_16x16x32_bf16 %0, v[184:187], a[40:43], %0" : "+v"(a2[0][0]) :  : ); SB();
  asm volatile("ds_read_b64_tr_b16 v[170:171], %0 offset:37632" :  : "v"(aW[0][0][2]) : "memory"); SB();
  asm volatile("v_mfma_f32_16x16x32_bf16 %0, v[184:187], a[44:47], %0" : "+v"(a2[1][0]) :  : ); SB();
  asm volatile("s_add_u32 m0, %0, 61440" :  : "s"(mW) : "memory", "scc"); SB();
  asm volatile("ds_read_b128 a[64:67], %0 offset:16384" :  : "v"(aX[0][0]) : "memory"); SB();
  asm volatile("v_mfma_f32_16x16x32_bf16 %0, v[188:191], a[40:43], %0" : "+v"(a2[0][1]) :  : ); SB();
  asm volatile("buffer_load_dwordx4 %0, %1, %2 offen lds" :  : "v"(voB[0]), "s"(rW), "s"(kw_[5]) : "memory"); SB();
  asm volatile("v_mfma_f32_16x16x32_bf16 %0, v[188:191], a[44:47], %0" : "+v"(a2[1][1]) :  : ); SB();
  asm volatile("ds_read_b128 a[68:71], %0 offset:16384" :  : "v"(aX[0][1]) : "memory"); SB();
  asm volatile("v_mfma_f32_16x16x32_bf16 %0, v[192:195], a[40:43], %0" : "+v"(a2[0][2]) :  : ); SB();
  asm volatile("ds_read_b128 a[72:75], %0 offset:16384" :  : "v"(aX[1][0]) : "memory"); SB();
  asm volatile("v_mfma_f32_16x16x32_bf16 %0, v[192:195], a[44:47], %0" : "+v"(a2[1][2]) :  : ); SB();
  asm volatile("ds_read_b128 a[76:79], %0 offset:16384" :  : "v"(aX[1][1]) : "memory"); SB();
  asm volatile("v_mfma_f32_16x16x32_bf16 %0, %1, a[48:51], %0" : "+v"(rs) : "v"(ones) : ); SB();
  asm volatile("ds_read_b128 a[80:83], %0 offset:24576" :  : "v"(aX[0][0]) : "memory"); SB();
  asm volatile("v_mfma_f32_16x16x32_bf16 %0, a[48:51], a[48:51], %0" : "+v"(gr) :  : ); SB();
  asm volatile("ds_read_b128 a[84:87], %0 offset:24576" :  : "v"(aX[0][1]) : "memory"); SB();
  asm volatile("v_mfma_f32_16x16x32_bf16 %0, %1, a[56:59], %0" : "+v"(rs) : "v"(ones) : ); SB();
  asm volatile("ds_read_b64_tr_b16 v[172:173], %0 offset:36864" :  : "v"(aW[0][1][0]) : "memory"); SB();
  asm volatile("v_mfma_f32_16x16x32_bf16 %0, a[56:59], a[56:59], %0" : "+v"(gr) :  : ); SB();
  asm volatile("ds_read_b64_tr_b16 v[174:175], %0 offset:37632" :  : "v"(aW[0][1][0]) : "memory"); SB();
  asm volatile("s_waitcnt lgkmcnt(8)\n\tv_mfma_f32_16x16x32_bf16 %0, v[160:163], a[48:51], %0" : "+v"(a2[0][0]) :  : ); SB();
  asm volatile("ds_read_b64_tr_b16 v[176:177], %0 offset:36864" :  : "v"(aW[0][1][1]) : "memory"); SB();
  asm volatile("v_mfma_f32_16x16x32_bf16 %0, v[160:163], a[52:55], %0" : "+v"(a2[1][0]) :  : ); SB();
  asm volatile("ds_read_b64_tr_b16 v[178:179], %0 offset:37632" :  : "v"(aW[0][1][1]) : "memory"); SB();
  asm volatile("v_mfma_f32_16x16x32_bf16 %0, v[160:163], %1, %0" : "+v"(c2[0]) : "v"(ones) : ); SB();
  asm volatile("ds_read_b64_tr_b16 v[180:181], %0 offset:36864" :  : "v"(aW[0][1][2]) : "memory"); SB();
  asm volatile("v_mfma_f32_16x16x32_bf16 %0, v[164:167], a[48:51], %0" : "+v"(a2[0][1]) :  : ); SB();
  asm volatile("ds_read_b64_tr_b16 v[182:183], %0 offset:37632" :  : "v"(aW[0][1][2]) : "memory"); SB();
  asm volatile("v_mfma_f32_16x16x32_bf16 %0, v[164:167], a[52:55], %0" : "+v"(a2[1][1]) :  : ); SB();
  asm volatile("ds_read_b128 a[88:91], %0 offset:24576" :  : "v"(aX[1][0]) : "memory"); SB();
  asm volatile("v_mfma_f32_16x16x32_bf16 %0, v[164:167], %1, %0" : "+v"(c2[1]) : "v"(ones) : ); SB();
  asm volatile("ds_read_b128 a[92:95], %0 offset:24576" :  : "v"(aX[1][1]) : "memory"); SB();
  asm volatile("v_mfma_f32_16x16x32_bf16 %0, v[168:171], a[48:51], %0" : "+v"(a2[0][2]) :  : ); SB();
  asm volatile("buffer_load_dwordx4 %0, %1, %2 offen offset:1024 lds" :  : "v"(voB[1]), "s"(rW), "s"(kw_[5]) : "memory"); SB();
  asm volatile("v_mfma_f32_16x16x32_bf16 %0, v[168:171], a[52:55], %0" : "+v"(a2[1][2]) :  : ); SB();
  asm volatile("buffer_load_dwordx4 %0, %1, %2 offen offset:2048 lds" :  : "v"(voB[2]), "s"(rW), "s"(kw_[5]) : "memory"); SB();
  asm volatile("v_mfma_f32_16x16x32_bf16 %0, v[168:171], %1, %0" : "+v"(c2[2]) : "v"(ones) : ); SB();
#pragma unroll
  for (int n = 0; n < 8; ++n) { ao[n] = (n ^ (2 * w)) * 32768 + (3 - (n & 3)) * 1024; asm volatile("" : "+s"(ao[n])); }
  w3k[0] = w3s + 32768; w3k[1] = w3s + 65536; sFZ = W_FZ + ((cb * 32 + rb) * 4 + w) * 8; sFK = sFZ + 8192; mU = lb + w * 1024; uso = W_KV + c2s;
  asm volatile("" : "+s"(w3k[0]), "+s"(w3k[1]), "+s"(sFZ), "+s"(sFK), "+s"(mU), "+s"(uso));
  SB();
  asm volatile("s_add_u32 m0, %0, 73728" :  : "s"(mW) : "memory", "scc"); SB();
  asm volatile("s_nop 0\n\tbuffer_load_dwordx4 %0, %1, %2 offen lds" :  : "v"(voB[0]), "s"(rW), "s"(kw_[6]) : "memory"); SB();
  asm volatile("v_readfirstlane_b32 %0, v252\n\tv_readfirstlane_b32 %1, v253\n\ts_nop 0\n\ts_add_u32 %0, %0, 1\n\ts_addc_u32 %1, %1, 0" : "=&s"(tg0), "=&s"(tg1) :  : "memory", "scc"); SB();
  asm volatile("buffer_load_dwordx4 %0, %1, %2 offen offset:1024 lds" :  : "v"(voB[1]), "s"(rW), "s"(kw_[6]) : "memory"); SB();
  asm volatile("buffer_load_dwordx4 %0, %1, %2 offen offset:2048 lds" :  : "v"(voB[2]), "s"(rW), "s"(kw_[6]) : "memory"); SB();
  asm volatile("s_add_u32 m0, %0, 86016" :  : "s"(mW) : "memory", "scc"); SB();
  asm volatile("s_nop 0\n\tbuffer_load_dwordx4 %0, %1, %2 offen lds" :  : "v"(voB[0]), "s"(rW), "s"(kw_[7]) : "memory"); SB();
  asm volatile("s_waitcnt vmcnt(11)\n\ts_barrier" :  :  : "memory"); SB();
  asm volatile("ds_read_b64_tr_b16 v[184:185], %0 offset:0" :  : "v"(aW[1][0][0]) : "memory"); SB();
  asm volatile("s_waitcnt lgkmcnt(1)\n\tv_mfma_f32_16x16x32_bf16 %0, %1, a[64:67], %0" : "+v"(rs) : "v"(ones) : ); SB();
  asm volatile("ds_read_b64_tr_b16 v[186:187], %0 offset:768" :  : "v"(aW[1][0][0]) : "memory"); SB();
  asm volatile("v_mfma_f32_16x16x32_bf16 %0, a[64:67], a[64:67], %0" : "+v"(gr) :  : ); SB();
  asm volatile("ds_read_b64_tr_b16 v[188:189], %0 offset:0" :  : "v"(aW[1][0][1]) : "memory"); SB();
  asm volatile("v_mfma_f32_16x16x32_bf16 %0, %1, a[72:75], %0" : "+v"(rs) : "v"(ones) : ); SB();
  asm volatile("ds_read_b64_tr_b16 v[190:191], %0 offset:768" :  : "v"(aW[1][0][1]) : "memory"); SB();
  asm volatile("v_mfma_f32_16x16x32_bf16 %0, a[72:75], a[72:75], %0" : "+v"(gr) :  : ); SB();
  asm volatile("ds_read_b64_tr_b16 v[192:193], %0 offset:0" :  : "v"(aW[1][0][2]) : "memory"); SB();
  asm volatile("v_mfma_f32_16x16x32_bf16 %0, v[172:175], a[56:59], %0" : "+v"(a2[0][0]) :  : ); SB();
  asm volatile("ds_read_b64_tr_b16 v[194:195], %0 offset:768" :  : "v"(aW[1][0][2]) : "memory"); SB();
  asm volatile("v_mfma_f32_16x16x32_bf16 %0, v[172:175], a[60:63], %0" : "+v"(a2[1][0]) :  : ); SB();
  asm volatile("ds_read_b128 a[96:99], %0 offset:32768" :  : "v"(aX[0][0]) : "memory"); SB();
  asm volatile("v_mfma_f32_16x16x32_bf16 %0, v[176:179], a[56:59], %0" : "+v"(a2[0][1]) :  : ); SB();
  asm volatile("ds_read_b128 a[100:103], %0 offset:32768" :  : "v"(aX[0][1]) : "memory"); SB();
  asm volatile("v_mfma_f32_16x16x32_bf16 %0, v[176:179], a[60:63], %0" : "+v"(a2[1][1]) :  : ); SB();
  asm volatile("ds_read_b128 a[104:107], %0 offset:32768" :  : "v"(aX[1][0]) : "memory"); SB();
  asm volatile("v_mfma_f32_16x16x32_bf16 %0, v[180:183], a[56:59], %0" : "+v"(a2[0][2]) :  : ); SB();
  asm volatile("ds_read_b128 a[108:111], %0 offset:32768" :  : "v"(aX[1][1]) : "memory"); SB();
  asm volatile("v_mfma_f32_16x16x32_bf16 %0, v[180:183], a[60:63], %0" : "+v"(a2[1][2]) :  : ); SB();
  asm volatile("ds_read_b128 a[112:115], %0 offset:40960" :  : "v"(aX[0][0]) : "memory"); SB();
  asm volatile("v_mfma_f32_16x16x32_bf16 %0, %1, a[80:83], %0" : "+v"(rs) : "v"(ones) : ); SB();
  asm volatile("buffer_load_dwordx4 %0, %1, %2 offen offset:1024 lds" :  : "v"(voB[1]), "s"(rW), "s"(kw_[7]) : "memory"); SB();
  asm volatile("v_mfma_f32_16x16x32_bf16 %0, a[80:83], a[80:83], %0" : "+v"(gr) :  : ); SB();
  asm volatile("ds_read_b128 a[116:119], %0 offset:40960" :  : "v"(aX[0][1]) : "memory"); SB();
  asm volatile("v_mfma_f32_16x16x32_bf16 %0, %1, a[88:91], %0" : "+v"(rs) : "v"(ones) : ); SB();
  asm volatile("ds_read_b64_tr_b16 v[160:161], %0 offset:0" :  : "v"(aW[1][1][0]) : "memory"); SB();
  asm volatile("v_mfma_f32_16x16x32_bf16 %0, a[88:91], a[88:91], %0" : "+v"(gr) :  : ); SB();
  asm volatile("ds_read_b64_tr_b16 v[162:163], %0 offset:768" :  : "v"(aW[1][1][0]) : "memory"); SB();
  asm volatile("buffer_load_dwordx4 %0, %1, %2 offen offset:2048 lds" :  : "v"(voB[2]), "s"(rW), "s"(kw_[7]) : "memory"); SB();
  asm volatile("s_waitcnt lgkmcnt(8)\n\tv_mfma_f32_16x16x32_bf16 %0, v[184:187], a[64:67], %0" : "+v"(a2[0][0]) :  : ); SB();
  asm volatile("ds_read_b64_tr_b16 v[164:165], %0 offset:0" :  : "v"(aW[1][1][1]) : "memory"); SB();
  asm volatile("s_add_u32 m0, %0, 0" :  : "s"(mW) : "memory", "scc"); SB();
  asm volatile("v_mfma_f32_16x16x32_bf16 %0, v[184:187], a[68:71], %0" : "+v"(a2[1][0]) :  : ); SB();
  asm volatile("ds_read_b64_tr_b16 v[166:167], %0 offset:768" :  : "v"(aW[1][1][1]) : "memory"); SB();
  asm volatile("v_mfma_f32_16x16x32_bf16 %0, v[184:187], %1, %0" : "+v"(c2[0]) : "v"(ones) : ); SB();
  asm volatile("ds_read_b64_tr_b16 v[168:169], %0 offset:0" :  : "v"(aW[1][1][2]) : "memory"); SB();
  asm volatile("v_mfma_f32_16x16x32_bf16 %0, v[188:191], a[64:67], %0" : "+v"(a2[0][1]) :  : ); SB();
  asm volatile("ds_read_b64_tr_b16 v[170:171], %0 offset:768" :  : "v"(aW[1][1][2]) : "memory"); SB();
  asm volatile("v_mfma_f32_16x16x32_bf16 %0, v[188:191], a[68:71], %0" : "+v"(a2[1][1]) :  : ); SB();
  asm volatile("buffer_load_dwordx4 %0, %1, %2 offen lds" :  : "v"(voB[0]), "s"(rW), "s"(kw_[8]) : "memory"); SB();
  asm volatile("v_mfma_f32_16x16x32_bf16 %0, v[188:191], %1, %0" : "+v"(c2[1]) : "v"(ones) : ); SB();
  asm volatile("ds_read_b128 a[120:123], %0 offset:40960" :  : "v"(aX[1][0]) : "memory"); SB();
  asm volatile("v_mfma_f32_16x16x32_bf16 %0, v[192:195], a[64:67], %0" : "+v"(a2[0][2]) :  : ); SB();
  asm volatile("ds_read_b128 a[124:127], %0 offset:40960" :  : "v"(aX[1][1]) : "memory"); SB();
  asm volatile("v_mfma_f32_16x16x32_bf16 %0, v[192:195], a[68:71], %0" : "+v"(a2[1][2]) :  : ); SB();
  asm volatile("buffer_load_dwordx4 %0, %1, %2 offen offset:1024 lds" :  : "v"(voB[1]), "s"(rW), "s"(kw_[8]) : "memory"); SB();
  asm volatile("v_mfma_f32_16x16x32_bf16 %0, v[192:195], %1, %0" : "+v"(c2[2]) : "v"(ones) : ); SB();
  asm volatile("buffer_load_dwordx4 %0, %1, %2 offen offset:2048 lds" :  : "v"(voB[2]), "s"(rW), "s"(kw_[8]) : "memory"); SB();
  asm volatile("s_add_u32 m0, %0, 12288" :  : "s"(mW) : "memory", "scc"); SB();
  asm volatile("s_nop 0\n\tbuffer_load_dwordx4 %0, %1, %2 offen lds" :  : "v"(voB[0]), "s"(rW), "s"(kw_[9]) : "memory"); SB();
  asm volatile("s_waitcnt vmcnt(10)\n\ts_barrier" :  :  : "memory"); SB();
  asm volatile("ds_read_b64_tr_b16 v[172:173], %0 offset:12288" :  : "v"(aW[1][0][0]) : "memory"); SB();
  asm volatile("s_waitcnt lgkmcnt(1)\n\tv_mfma_f32_16x16x32_bf16 %0, %1, a[96:99], %0" : "+v"(rs) : "v"(ones) : ); SB();
  asm volatile("ds_read_b64_tr_b16 v[174:175], %0 offset:13056" :  : "v"(aW[1][0][0]) : "memory"); SB();
  asm volatile("v_mfma_f32_16x16x32_bf16 %0, a[96:99], a[96:99], %0" : "+v"(gr) :  : ); SB();
  asm volatile("ds_read_b64_tr_b16 v[176:177], %0 offset:12288" :  : "v"(aW[1][0][1]) : "memory"); SB();
  asm volatile("v_mfma_f32_16x16x32_bf16 %0, %1, a[104:107], %0" : "+v"(rs) : "v"(ones) : ); SB();
  asm volatile("ds_read_b64_tr_b16 v[178:179], %0 offset:13056" :  : "v"(aW[1][0][1]) : "memory"); SB();
  asm volatile("v_mfma_f32_16x16x32_bf16 %0, a[104:107], a[104:107], %0" : "+v"(gr) :  : ); SB();
  asm volatile("ds_read_b64_tr_b16 v[180:181], %0 offset:12288" :  : "v"(aW[1][0][2]) : "memory"); SB();
  asm volatile("v_mfma_f32_16x16x32_bf16 %0, v[160:163], a[72:75], %0" : "+v"(a2[0][0]) :  : ); SB();
  asm volatile("ds_read_b64_tr_b16 v[182:183], %0 offset:13056" :  : "v"(aW[1][0][2]) : "memory"); SB();
  asm volatile("v_mfma_f32_16x16x32_bf16 %0, v[160:163], a[76:79], %0" : "+v"(a2[1][0]) :  : ); SB();
  asm volatile("ds_read_b128 a[128:131], %0 offset:49152" :  : "v"(aX[0][0]) : "memory"); SB();
  asm volatile("v_mfma_f32_16x16x32_bf16 %0, v[164:167], a[72:75], %0" : "+v"(a2[0][1]) :  : ); SB();
  asm volatile("ds_read_b128 a[132:135], %0 offset:49152" :  : "v"(aX[0][1]) : "memory"); SB();
  asm volatile("v_mfma_f32_16x16x32_bf16 %0, v[164:167], a[76:79], %0" : "+v"(a2[1][1]) :  : ); SB();
  asm volatile("ds_read_b128 a[136:139], %0 offset:49152" :  : "v"(aX[1][0]) : "memory"); SB();
  asm volatile("v_mfma_f32_16x16x32_bf16 %0, v[168:171], a[72:75], %0" : "+v"(a2[0][2]) :  : ); SB();
  asm volatile("ds_read_b128 a[140:143], %0 offset:49152" :  : "v"(aX[1][1]) : "memory"); SB();
  asm volatile("v_mfma_f32_16x16x32_bf16 %0, v[168:171], a[76:79], %0" : "+v"(a2[1][2]) :  : ); SB();
  asm volatile("ds_read_b128 a[144:147], %0 offset:57344" :  : "v"(aX[0][0]) : "memory"); SB();
  asm volatile("v_mfma_f32_16x16x32_bf16 %0, %1, a[112:115], %0" : "+v"(rs) : "v"(ones) : ); SB();
  asm volatile("ds_read_b128 a[148:151], %0 offset:57344" :  : "v"(aX[0][1]) : "memory"); SB();
  asm volatile("v_mfma_f32_16x16x32_bf16 %0, a[112:115], a[112:115], %0" : "+v"(gr) :  : ); SB();
  asm volatile("ds_read_b128 a[152:155], %0 offset:57344" :  : "v"(aX[1][0]) : "memory"); SB();
  asm volatile("v_mfma_f32_16x16x32_bf16 %0, %1, a[120:123], %0" : "+v"(rs) : "v"(ones) : ); SB();
  asm volatile("ds_read_b64_tr_b16 v[184:185], %0 offset:12288" :  : "v"(aW[1][1][0]) : "memory"); SB();
  asm volatile("v_mfma_f32_16x16x32_bf16 %0, a[120:123], a[120:123], %0" : "+v"(gr) :  : ); SB();
  asm volatile("ds_read_b64_tr_b16 v[186:187], %0 offset:13056" :  : "v"(aW[1][1][0]) : "memory"); SB();
  asm volatile("s_waitcnt lgkmcnt(9)\n\tv_mfma_f32_16x16x32_bf16 %0, v[172:175], a[80:83], %0" : "+v"(a2[0][0]) :  : ); SB();
  asm volatile("ds_read_b64_tr_b16 v[188:189], %0 offset:12288" :  : "v"(aW[1][1][1]) : "memory"); SB();
  asm volatile("v_mfma_f32_16x16x32_bf16 %0, v[172:175], a[84:87], %0" : "+v"(a2[1][0]) :  : ); SB();
  asm volatile("ds_read_b64_tr_b16 v[190:191], %0 offset:13056" :  : "v"(aW[1][1][1]) : "memory"); SB();
  asm volatile("v_mfma_f32_16x16x32_bf16 %0, v[172:175], %1, %0" : "+v"(c2[0]) : "v"(ones) : ); SB();
  asm volatile("ds_read_b64_tr_b16 v[192:193], %0 offset:12288" :  : "v"(aW[1][1][2]) : "memory"); SB();
  asm volatile("buffer_load_dwordx4 %0, %1, %2 offen offset:1024 lds" :  : "v"(voB[1]), "s"(rW), "s"(kw_[9]) : "memory"); SB();
  asm volatile("v_mfma_f32_16x16x32_bf16 %0, v[176:179], a[80:83], %0" : "+v"(a2[0][1]) :  : ); SB();
  asm volatile("ds_read_b64_tr_b16 v[194:195], %0 offset:13056" :  : "v"(aW[1][1][2]) : "memory"); SB();
  asm volatile("v_mfma_f32_16x16x32_bf16 %0, v[176:179], a[84:87], %0" : "+v"(a2[1][1]) :  : ); SB();
  asm volatile("ds_read_b128 a[156:159], %0 offset:57344" :  : "v"(aX[1][1]) : "memory"); SB();
  asm volatile("v_mfma_f32_16x16x32_bf16 %0, v[176:179], %1, %0" : "+v"(c2[1]) : "v"(ones) : ); SB();
  asm volatile("v_cmp_le_u32 vcc, %2, %1\n\ts_mov_b64 exec, vcc\n\tds_read_b64 %0, %1\n\ts_mov_b64 exec, -1" : "+v"(xv[0]) : "v"(aV[0]), "s"(lb + 131072) : "memory", "vcc"); SB();
  asm volatile("v_mfma_f32_16x16x32_bf16 %0, v[180:183], a[80:83], %0" : "+v"(a2[0][2]) :  : ); SB();
  asm volatile("buffer_load_dwordx4 %0, %1, %2 offen offset:2048 lds" :  : "v"(voB[2]), "s"(rW), "s"(kw_[9]) : "memory"); SB();
  asm volatile("s_add_u32 m0, %0, 98304" :  : "s"(mA) : "memory", "scc"); SB();
  asm volatile("v_mfma_f32_16x16x32_bf16 %0, v[180:183], a[84:87], %0" : "+v"(a2[1][2]) :  : ); SB();
  asm volatile("v_cmp_le_u32 vcc, %2, %1\n\ts_mov_b64 exec, vcc\n\tds_read_b64 %0, %1\n\ts_mov_b64 exec, -1" : "+v"(xv[1]) : "v"(aV[1]), "s"(lb + 131072) : "memory", "vcc"); SB();
  asm volatile("v_mfma_f32_16x16x32_bf16 %0, v[180:183], %1, %0" : "+v"(c2[2]) : "v"(ones) : ); SB();
  asm volatile("s_waitcnt vmcnt(9)\n\ts_barrier" :  :  : "memory"); SB();
  asm volatile("ds_read_b64_tr_b16 v[160:161], %0 offset:24576" :  : "v"(aW[1][0][0]) : "memory"); SB();
  asm volatile("buffer_load_dwordx4 %0, %1, %2 offen lds" :  : "v"(voK[0]), "s"(rK), "s"(0u) : "memory"); SB();
  asm volatile("s_waitcnt lgkmcnt(4)\n\tv_mfma_f32_16x16x32_bf16 %0, %1, a[128:131], %0" : "+v"(rs) : "v"(ones) : ); SB();
  asm volatile("ds_read_b64_tr_b16 v[162:163], %0 offset:25344" :  : "v"(aW[1][0][0]) : "memory"); SB();
  asm volatile("v_mfma_f32_16x16x32_bf16 %0, a[128:131], a[128:131], %0" : "+v"(gr) :  : ); SB();
  asm volatile("ds_read_b64_tr_b16 v[164:165], %0 offset:24576" :  : "v"(aW[1][0][1]) : "memory"); SB();
  asm volatile("v_mfma_f32_16x16x32_bf16 %0, %1, a[136:139], %0" : "+v"(rs) : "v"(ones) : ); SB();
  asm volatile("ds_read_b64_tr_b16 v[166:167], %0 offset:25344" :  : "v"(aW[1][0][1]) : "memory"); SB();
  asm volatile("v_mfma_f32_16x16x32_bf16 %0, a[136:139], a[136:139], %0" : "+v"(gr) :  : ); SB();
  asm volatile("ds_read_b64_tr_b16 v[168:169], %0 offset:24576" :  : "v"(aW[1][0][2]) : "memory"); SB();
  asm volatile("v_mfma_f32_16x16x32_bf16 %0, v[184:187], a[88:91], %0" : "+v"(a2[0][0]) :  : ); SB();
  asm volatile("ds_read_b64_tr_b16 v[170:171], %0 offset:25344" :  : "v"(aW[1][0][2]) : "memory"); SB();
  asm volatile("v_mfma_f32_16x16x32_bf16 %0, v[184:187], a[92:95], %0" : "+v"(a2[1][0]) :  : ); SB();
  asm volatile("v_cmp_le_u32 vcc, %2, %1\n\ts_mov_b64 exec, vcc\n\tds_read_b64 %0, %1\n\ts_mov_b64 exec, -1" : "+v"(xv[2]) : "v"(aV[2]), "s"(lb + 131072) : "memory", "vcc"); SB();
  asm volatile("v_mfma_f32_16x16x32_bf16 %0, v[188:191], a[88:91], %0" : "+v"(a2[0][1]) :  : ); SB();
  asm volatile("v_cmp_le_u32 vcc, %2, %1\n\ts_mov_b64 exec, vcc\n\tds_read_b64 %0, %1\n\ts_mov_b64 exec, -1" : "+v"(xv[3]) : "v"(aV[3]), "s"(lb + 131072) : "memory", "vcc"); SB();
  asm volatile("v_mfma_f32_16x16x32_bf16 %0, v[188:191], a[92:95], %0" : "+v"(a2[1][1]) :  : ); SB();
  asm volatile("v_cmp_le_u32 vcc, %2, %1\n\ts_mov_b64 exec, vcc\n\tds_read_b64 %0, %1\n\ts_mov_b64 exec, -1" : "+v"(xv[4]) : "v"(aV[4]), "s"(lb + 131072) : "memory", "vcc"); SB();
  asm volatile("v_mfma_f32_16x16x32_bf16 %0, v[192:195], a[88:91], %0" : "+v"(a2[0][2]) :  : ); SB();
  asm volatile("ds_read_b64_tr_b16 v[172:173], %0 offset:24576" :  : "v"(aW[1][1][0]) : "memory"); SB();
  asm volatile("v_mfma_f32_16x16x32_bf16 %0, v[192:195], a[92:95], %0" : "+v"(a2[1][2]) :  : ); SB();
  asm volatile("ds_read_b64_tr_b16 v[174:175], %0 offset:25344" :  : "v"(aW[1][1][0]) : "memory"); SB();
  asm volatile("v_mfma_f32_16x16x32_bf16 %0, %1, a[144:147], %0" : "+v"(rs) : "v"(ones) : ); SB();
  asm volatile("ds_read_b64_tr_b16 v[176:177], %0 offset:24576" :  : "v"(aW[1][1][1]) : "memory"); SB();
  asm volatile("v_mfma_f32_16x16x32_bf16 %0, a[144:147], a[144:147], %0" : "+v"(gr) :  : ); SB();
  asm volatile("ds_read_b64_tr_b16 v[178:179], %0 offset:25344" :  : "v"(aW[1][1][1]) : "memory"); SB();
  asm volatile("s_waitcnt lgkmcnt(8)\n\tv_mfma_f32_16x16x32_bf16 %0, %1, a[152:155], %0" : "+v"(rs) : "v"(ones) : ); SB();
  asm volatile("ds_read_b64_tr_b16 v[180:181], %0 offset:24576" :  : "v"(aW[1][1][2]) : "memory"); SB();
  asm volatile("v_mfma_f32_16x16x32_bf16 %0, a[152:155], a[152:155], %0" : "+v"(gr) :  : ); SB();
  asm volatile("ds_read_b64_tr_b16 v[182:183], %0 offset:25344" :  : "v"(aW[1][1][2]) : "memory"); SB();
  asm volatile("v_mfma_f32_16x16x32_bf16 %0, v[160:163], a[96:99], %0" : "+v"(a2[0][0]) :  : ); SB();
  asm volatile("buffer_load_dwordx4 %0, %1, %2 offen offset:1024 lds" :  : "v"(voK[1]), "s"(rK), "s"(0u) : "memory"); SB();
  asm volatile("buffer_load_dwordx4 %0, %1, %2 offen offset:2048 lds" :  : "v"(voK[2]), "s"(rK), "s"(0u) : "memory"); SB();
  asm volatile("s_mov_b32 exec_lo, 0xff\n\ts_mov_b32 exec_hi, 0\n\tbuffer_load_dwordx4 %0, %1, %2 offen offset:3072 lds\n\ts_mov_b64 exec, -1" :  : "v"(voK[3]), "s"(rK), "s"(0u) : "memory"); SB();
  asm volatile("s_waitcnt vmcnt(10)\n\ts_barrier" :  :  : "memory"); SB();
  asm volatile("ds_read_b64_tr_b16 v[184:185], %0 offset:36864" :  : "v"(aW[1][0][0]) : "memory"); SB();
  asm volatile("v_mfma_f32_16x16x32_bf16 %0, v[160:163], a[100:103], %0" : "+v"(a2[1][0]) :  : ); SB();
  asm volatile("ds_read_b64_tr_b16 v[186:187], %0 offset:37632" :  : "v"(aW[1][0][0]) : "memory"); SB();
  asm volatile("v_mfma_f32_16x16x32_bf16 %0, v[160:163], %1, %0" : "+v"(c2[0]) : "v"(ones) : ); SB();
  asm volatile("ds_read_b64_tr_b16 v[188:189], %0 offset:36864" :  : "v"(aW[1][0][1]) : "memory"); SB();
  asm volatile("v_mfma_f32_16x16x32_bf16 %0, v[164:167], a[96:99], %0" : "+v"(a2[0][1]) :  : ); SB();
  asm volatile("ds_read_b64_tr_b16 v[190:191], %0 offset:37632" :  : "v"(aW[1][0][1]) : "memory"); SB();
  asm volatile("v_mfma_f32_16x16x32_bf16 %0, v[164:167], a[100:103], %0" : "+v"(a2[1][1]) :  : ); SB();
  asm volatile("ds_read_b64_tr_b16 v[192:193], %0 offset:36864" :  : "v"(aW[1][0][2]) : "memory"); SB();
  asm volatile("v_mfma_f32_16x16x32_bf16 %0, v[164:167], %1, %0" : "+v"(c2[1]) : "v"(ones) : ); SB();
  asm volatile("ds_read_b64_tr_b16 v[194:195], %0 offset:37632" :  : "v"(aW[1][0][2]) : "memory"); SB();
  asm volatile("s_waitcnt lgkmcnt(6)\n\tv_mfma_f32_16x16x32_bf16 %0, v[168:171], a[96:99], %0" : "+v"(a2[0][2]) :  : ); SB();
  asm volatile("s_mov_b32 exec_lo, -1\n\ts_mov_b32 exec_hi, 0xff\n\tbuffer_load_dwordx4 %0, %1, %2 offen offset:3328 lds\n\ts_mov_b64 exec, -1" :  : "v"(voH), "s"(rS), "s"(0u) : "memory"); SB();
  asm volatile("s_add_u32 m0, %0, 102400" :  : "s"(mA) : "memory", "scc"); SB();
  asm volatile("v_mfma_f32_16x16x32_bf16 %0, v[168:171], a[100:103], %0" : "+v"(a2[1][2]) :  : ); SB();
  asm volatile("buffer_load_dwordx4 %0, %1, %2 offen lds" :  : "v"(voV[0]), "s"(rV), "s"(0u) : "memory"); SB();
  asm volatile("v_mfma_f32_16x16x32_bf16 %0, v[168:171], %1, %0" : "+v"(c2[2]) : "v"(ones) : ); SB();
  asm volatile("v_mfma_f32_16x16x32_bf16 %0, v[172:175], a[104:107], %0" : "+v"(a2[0][0]) :  : ); SB();
  asm volatile("ds_read_b64_tr_b16 v[160:161], %0 offset:36864" :  : "v"(aW[1][1][0]) : "memory"); SB();
  asm volatile("v_cndmask_b32 %0, %1, %2, %3" : "=v"(xt[0]) : "v"(gr[0]), "v"(gr[1]), "s"(0xAAAAAAAAAAAAAAAAull) : ); SB();
  asm volatile("v_mfma_f32_16x16x32_bf16 %0, v[172:175], a[108:111], %0" : "+v"(a2[1][0]) :  : ); SB();
  asm volatile("s_mov_b32 exec_lo, 0x10001\n\ts_mov_b32 exec_hi, 0x10001\n\tds_write_b128 %0, %1 offset:0\n\ts_mov_b64 exec, -1" :  : "v"(ca), "v"(c2[0]) : "memory"); SB();
  asm volatile("ds_read_b64_tr_b16 v[162:163], %0 offset:37632" :  : "v"(aW[1][1][0]) : "memory"); SB();
  asm volatile("v_cndmask_b32 %0, %1, %2, %3" : "=v"(xt[1]) : "v"(gr[2]), "v"(gr[3]), "s"(0xAAAAAAAAAAAAAAAAull) : ); SB();
  asm volatile("v_mfma_f32_16x16x32_bf16 %0, v[176:179], a[104:107], %0" : "+v"(a2[0][1]) :  : ); SB();
  asm volatile("ds_read_b64_tr_b16 v[164:165], %0 offset:36864" :  : "v"(aW[1][1][1]) : "memory"); SB();
  asm volatile("v_mul_f32 %0, 0x3acccccd, %1" : "=v"(xs[0][0]) : "v"(rs[0]) : ); SB();
  asm volatile("v_mfma_f32_16x16x32_bf16 %0, v[176:179], a[108:111], %0" : "+v"(a2[1][1]) :  : ); SB();
  asm volatile("s_mov_b32 exec_lo, 0x10001\n\ts_mov_b32 exec_hi, 0x10001\n\tds_write_b128 %0, %1 offset:64\n\ts_mov_b64 exec, -1" :  : "v"(ca), "v"(c2[1]) : "memory"); SB();
  asm volatile("ds_read_b64_tr_b16 v[166:167], %0 offset:37632" :  : "v"(aW[1][1][1]) : "memory"); SB();
  asm volatile("buffer_load_dwordx4 %0, %1, %2 offen offset:1024 lds" :  : "v"(voV[1]), "s"(rV), "s"(0u) : "memory"); SB();
  asm volatile("v_mfma_f32_16x16x32_bf16 %0, v[180:183], a[104:107], %0" : "+v"(a2[0][2]) :  : ); SB();
  asm volatile("ds_read_b64_tr_b16 v[168:169], %0 offset:36864" :  : "v"(aW[1][1][2]) : "memory"); SB();
  asm volatile("v_cndmask_b32 %0, %0, %1, %2" : "+v"(xt[0]) : "v"(xt[1]), "s"(0xCCCCCCCCCCCCCCCCull) : ); SB();
  asm volatile("v_mfma_f32_16x16x32_bf16 %0, v[180:183], a[108:111], %0" : "+v"(a2[1][2]) :  : ); SB();
  asm volatile("s_mov_b32 exec_lo, 0x10001\n\ts_mov_b32 exec_hi, 0x10001\n\tds_write_b128 %0, %1 offset:128\n\ts_mov_b64 exec, -1" :  : "v"(ca), "v"(c2[2]) : "memory"); SB();
  asm volatile("ds_read_b64_tr_b16 v[170:171], %0 offset:37632" :  : "v"(aW[1][1][2]) : "memory"); SB();
  asm volatile("s_waitcnt lgkmcnt(6)\n\tv_mfma_f32_16x16x32_bf16 %0, v[184:187], a[112:115], %0" : "+v"(a2[0][0]) :  : ); SB();
  asm volatile("s_mov_b32 exec_lo, -1\n\ts_mov_b32 exec_hi, 0\n\tbuffer_load_dwordx4 %0, %1, %2 offen offset:2048 lds\n\ts_mov_b64 exec, -1" :  : "v"(voV[2]), "s"(rV), "s"(0u) : "memory"); SB();
  asm volatile("v_mul_f32 %0, 0x3a83126f, %1" : "=v"(xt[1]) : "v"(rs[0]) : ); SB();
  asm volatile("v_mfma_f32_16x16x32_bf16 %0, v[184:187], a[116:119], %0" : "+v"(a2[1][0]) :  : ); SB();
  asm volatile("buffer_load_dwordx4 %0, %1, %2 offen offset:3072 lds" :  : "v"(voO), "s"(rO), "s"(0u) : "memory"); SB();
  asm volatile("v_fma_f32 %0, -%1, %2, %3" : "=v"(xs[0][1]) : "v"(rs[0]), "v"(xs[0][0]), "v"(xt[0]) : ); SB();
  asm volatile("v_mfma_f32_16x16x32_bf16 %0, v[184:187], %1, %0" : "+v"(c2[0]) : "v"(ones) : ); SB();
  asm volatile("v_mul_f32 %0, %0, %1" : "+v"(xt[1]) : "v"(xs[0][0]) : ); SB();
  asm volatile("v_mfma_f32_16x16x32_bf16 %0, v[188:191], a[112:115], %0" : "+v"(a2[0][1]) :  : ); SB();
  asm volatile("s_waitcnt vmcnt(12)\n\ts_barrier" :  :  : "memory"); SB();
  asm volatile("ds_read_b64_tr_b16 v[172:173], %0 offset:0" :  : "v"(aW[0][0][0]) : "memory"); SB();
  asm volatile("v_mfma_f32_16x16x32_bf16 %0, v[188:191], a[116:119], %0" : "+v"(a2[1][1]) :  : ); SB();
  asm volatile("ds_read_b64_tr_b16 v[174:175], %0 offset:768" :  : "v"(aW[0][0][0]) : "memory"); SB();
  asm volatile("v_mfma_f32_16x16x32_bf16 %0, v[188:191], %1, %0" : "+v"(c2[1]) : "v"(ones) : ); SB();
  asm volatile("ds_read_b64_tr_b16 v[176:177], %0 offset:0" :  : "v"(aW[0][0][1]) : "memory"); SB();
  asm volatile("v_mfma_f32_16x16x32_bf16 %0, v[192:195], a[112:115], %0" : "+v"(a2[0][2]) :  : ); SB();
  asm volatile("ds_read_b64_tr_b16 v[178:179], %0 offset:768" :  : "v"(aW[0][0][1]) : "memory"); SB();
  asm volatile("v_mfma_f32_16x16x32_bf16 %0, v[192:195], a[116:119], %0" : "+v"(a2[1][2]) :  : ); SB();
  asm volatile("ds_read_b64_tr_b16 v[180:181], %0 offset:0" :  : "v"(aW[0][0][2]) : "memory"); SB();
  asm volatile("v_mfma_f32_16x16x32_bf16 %0, v[192:195], %1, %0" : "+v"(c2[2]) : "v"(ones) : ); SB();
  asm volatile("ds_read_b64_tr_b16 v[182:183], %0 offset:768" :  : "v"(aW[0][0][2]) : "memory"); SB();
  asm volatile("v_mfma_f32_16x16x32_bf16 %0, v[160:163], a[120:123], %0" : "+v"(a2[0][0]) :  : ); SB();
  if (__builtin_expect((__builtin_amdgcn_ballot_w64(!(xs[0][1] >= xt[1])) & 0xF0000F0000F0000Full) != 0, 0)) {
    const f32x2 mm = {xs[0][0], xs[0][0]};
    f32x2 q2 = {0.f, 0.f};
    const u32x4* xp = (const u32x4*)(x + (size_t)(rb * 64 + w * 16 + l15) * 1280 + g4 * 320);
#pragma unroll 1
    for (int c = 0; c < 20; ++c) {
      u32x4 xq;
      asm volatile("global_load_dwordx4 %0, %1, off\n\ts_waitcnt vmcnt(0)" : "=&v"(xq) : "v"(xp + c) : "memory");
#pragma unroll
      for (int e = 0; e < 4; ++e) { const f32x2 d = unpk(xq[e]) - mm; q2 = __builtin_elementwise_fma(d, d, q2); }
    }
    xs[0][1] = red32(red16(q2[0] + q2[1]));
  }
  SB();
  asm volatile("v_mfma_f32_16x16x32_bf16 %0, v[160:163], a[124:127], %0" : "+v"(a2[1][0]) :  : ); SB();
  asm volatile("buffer_load_dwordx4 v[228:231], %0, %1, %2 offen" :  : "v"(vF), "s"(rF), "s"(0u) : "memory"); SB();
  asm volatile("v_max_f32 %0, 0, %0" : "+v"(xs[0][1]) :  : ); SB();
  asm volatile("v_mfma_f32_16x16x32_bf16 %0, v[160:163], %1, %0" : "+v"(c2[0]) : "v"(ones) : ); SB();
  asm volatile("buffer_load_dwordx4 v[232:235], %0, %1, %2 offen" :  : "v"(vF), "s"(rF), "s"(4096u) : "memory"); SB();
  asm volatile("s_waitcnt lgkmcnt(5)\n\tv_mfma_f32_16x16x32_bf16 %0, v[164:167], a[120:123], %0" : "+v"(a2[0][1]) :  : ); SB();
  asm volatile("ds_read_b64_tr_b16 v[184:185], %0 offset:0" :  : "v"(aW[0][1][0]) : "memory"); SB();
  asm volatile("v_mul_f32 %0, 0x3acccccd, %0" : "+v"(xs[0][1]) :  : ); SB();
  asm volatile("v_mfma_f32_16x16x32_bf16 %0, v[164:167], a[124:127], %0" : "+v"(a2[1][1]) :  : ); SB();
  asm volatile("ds_read_b64_tr_b16 v[186:187], %0 offset:768" :  : "v"(aW[0][1][0]) : "memory"); SB();
  asm volatile("v_cvt_pk_bf16_f32 %0, %0, %0" : "+v"(xs[0][1]) :  : ); SB();
  asm volatile("v_mfma_f32_16x16x32_bf16 %0, v[164:167], %1, %0" : "+v"(c2[1]) : "v"(ones) : ); SB();
  asm volatile("ds_read_b64_tr_b16 v[188:189], %0 offset:0" :  : "v"(aW[0][1][1]) : "memory"); SB();
  asm volatile("v_lshlrev_b32 %0, 16, %0" : "+v"(xs[0][1]) :  : ); SB();
  asm volatile("v_mfma_f32_16x16x32_bf16 %0, v[168:171], a[120:123], %0" : "+v"(a2[0][2]) :  : ); SB();
  asm volatile("ds_read_b64_tr_b16 v[190:191], %0 offset:768" :  : "v"(aW[0][1][1]) : "memory"); SB();
  asm volatile("v_add_f32 %0, 0x3727c5ac, %0" : "+v"(xs[0][1]) :  : ); SB();
  asm volatile("v_mfma_f32_16x16x32_bf16 %0, v[168:171], a[124:127], %0" : "+v"(a2[1][2]) :  : ); SB();
  asm volatile("ds_read_b64_tr_b16 v[192:193], %0 offset:0" :  : "v"(aW[0][1][2]) : "memory"); SB();
  asm volatile("v_cvt_pk_bf16_f32 %0, %0, %0" : "+v"(xs[0][1]) :  : ); SB();
  asm volatile("v_mfma_f32_16x16x32_bf16 %0, v[168:171], %1, %0" : "+v"(c2[2]) : "v"(ones) : ); SB();
  asm volatile("ds_read_b64_tr_b16 v[194:195], %0 offset:768" :  : "v"(aW[0][1][2]) : "memory"); SB();
  asm volatile("s_waitcnt lgkmcnt(6)\n\tv_mfma_f32_16x16x32_bf16 %0, v[172:175], a[128:131], %0" : "+v"(a2[0][0]) :  : ); SB();
  asm volatile("buffer_load_dwordx4 v[236:239], %0, %1, %2 offen" :  : "v"(vF), "s"(rF), "s"(c2s_) : "memory"); SB();
  asm volatile("v_lshlrev_b32 %0, 16, %0" : "+v"(xs[0][1]) :  : ); SB();
  asm volatile("v_mfma_f32_16x16x32_bf16 %0, v[172:175], a[132:135], %0" : "+v"(a2[1][0]) :  : ); SB();
  asm volatile("v_sqrt_f32 %0, %0" : "+v"(xs[0][1]) :  : ); SB();
  asm volatile("v_mfma_f32_16x16x32_bf16 %0, v[172:175], %1, %0" : "+v"(c2[0]) : "v"(ones) : ); SB();
  asm volatile("v_cvt_pk_bf16_f32 %0, %0, %0" : "+v"(xs[0][0]) :  : ); SB();
  asm volatile("v_mfma_f32_16x16x32_bf16 %0, v[176:179], a[128:131], %0" : "+v"(a2[0][1]) :  : ); SB();
  asm volatile("v_lshlrev_b32 %0, 16, %0" : "+v"(xs[0][0]) :  : ); SB();
  asm volatile("v_cvt_pk_bf16_f32 %0, %0, %0" : "+v"(xs[0][1]) :  : ); SB();
  asm volatile("v_lshlrev_b32 %0, 16, %0" : "+v"(xs[0][1]) :  : ); SB();
  asm volatile("v_rcp_f32 %0, %1" : "=v"(xt[0]) : "v"(xs[0][1]) : ); SB();
  asm volatile("s_nop 0\n\tv_mul_f32 %0, 0xc0135761, %1" : "=v"(xs[0][2]) : "v"(xt[0]) : ); SB();
  asm volatile("v_mul_f32 %0, %1, %1" : "=v"(xt[1]) : "v"(xt[0]) : ); SB();
  asm volatile("v_mul_f32 %0, 0xbdd2d3e8, %0" : "+v"(xt[0]) :  : ); SB();
  asm volatile("v_mul_f32 %0, %1, %2" : "=v"(xs[0][3]) : "v"(xt[0]), "v"(xt[1]) : ); SB();
  asm volatile("s_mov_b32 exec_lo, 0xf0000f\n\ts_mov_b32 exec_hi, 0xf0000f00\n\tds_write_b128 %0, %1\n\ts_mov_b64 exec, -1" :  : "v"(xw), "v"(xs[0]) : "memory"); SB();
  asm volatile("" : "+v"(xv[0]), "+v"(xv[1]), "+v"(xv[2]), "+v"(xv[3]), "+v"(xv[4]) :  : "memory"); SB();
  asm volatile("s_waitcnt vmcnt(12) lgkmcnt(0)\n\ts_barrier" :  :  : "memory"); SB();
  asm volatile("ds_read_b64_tr_b16 v[160:161], %0 offset:12288" :  : "v"(aW[0][0][0]) : "memory"); SB();
  asm volatile("s_add_u32 m0, %0, 24576" :  : "s"(mW) : "memory", "scc"); SB();
  asm volatile("v_mfma_f32_16x16x32_bf16 %0, v[176:179], a[132:135], %0" : "+v"(a2[1][1]) :  : ); SB();
  asm volatile("ds_read_b64_tr_b16 v[162:163], %0 offset:13056" :  : "v"(aW[0][0][0]) : "memory"); SB();
  asm volatile("v_mfma_f32_16x16x32_bf16 %0, v[176:179], %1, %0" : "+v"(c2[1]) : "v"(ones) : ); SB();
  asm volatile("ds_read_b64_tr_b16 v[164:165], %0 offset:12288" :  : "v"(aW[0][0][1]) : "memory"); SB();
  asm volatile("v_mfma_f32_16x16x32_bf16 %0, v[180:183], a[128:131], %0" : "+v"(a2[0][2]) :  : ); SB();
  asm volatile("ds_read_b64_tr_b16 v[166:167], %0 offset:13056" :  : "v"(aW[0][0][1]) : "memory"); SB();
  asm volatile("v_mfma_f32_16x16x32_bf16 %0, v[180:183], a[132:135], %0" : "+v"(a2[1][2]) :  : ); SB();
  asm volatile("ds_read_b64_tr_b16 v[168:169], %0 offset:12288" :  : "v"(aW[0][0][2]) : "memory"); SB();
  asm volatile("v_mfma_f32_16x16x32_bf16 %0, v[180:183], %1, %0" : "+v"(c2[2]) : "v"(ones) : ); SB();
  asm volatile("ds_read_b64_tr_b16 v[170:171], %0 offset:13056" :  : "v"(aW[0][0][2]) : "memory"); SB();
  asm volatile("v_mfma_f32_16x16x32_bf16 %0, v[184:187], a[136:139], %0" : "+v"(a2[0][0]) :  : ); SB();
  asm volatile("ds_read_b128 %0, %1 offset:0" : "=&v"(xs[0]) : "v"(xw) : "memory"); SB();
  asm volatile("v_mfma_f32_16x16x32_bf16 %0, v[184:187], a[140:143], %0" : "+v"(a2[1][0]) :  : ); SB();
  asm volatile("ds_read_b128 %0, %1 offset:0" : "=&v"(xs[1]) : "v"(xr_) : "memory"); SB();
  asm volatile("v_mfma_f32_16x16x32_bf16 %0, v[184:187], %1, %0" : "+v"(c2[0]) : "v"(ones) : ); SB();
  asm volatile("ds_read_b128 %0, %1 offset:0" : "=&v"(co[0]) : "v"(cr) : "memory"); SB();
  asm volatile("v_mfma_f32_16x16x32_bf16 %0, v[188:191], a[136:139], %0" : "+v"(a2[0][1]) :  : ); SB();
  asm volatile("ds_read_b64_tr_b16 v[172:173], %0 offset:12288" :  : "v"(aW[0][1][0]) : "memory"); SB();
  asm volatile("v_mfma_f32_16x16x32_bf16 %0, v[188:191], a[140:143], %0" : "+v"(a2[1][1]) :  : ); SB();
  asm volatile("ds_read_b64_tr_b16 v[174:175], %0 offset:13056" :  : "v"(aW[0][1][0]) : "memory"); SB();
  asm volatile("v_mfma_f32_16x16x32_bf16 %0, v[188:191], %1, %0" : "+v"(c2[1]) : "v"(ones) : ); SB();
  asm volatile("ds_read_b64_tr_b16 v[176:177], %0 offset:12288" :  : "v"(aW[0][1][1]) : "memory"); SB();
  asm volatile("v_mfma_f32_16x16x32_bf16 %0, v[192:195], a[136:139], %0" : "+v"(a2[0][2]) :  : ); SB();
  asm volatile("ds_read_b64_tr_b16 v[178:179], %0 offset:13056" :  : "v"(aW[0][1][1]) : "memory"); SB();
  asm volatile("v_mfma_f32_16x16x32_bf16 %0, v[192:195], a[140:143], %0" : "+v"(a2[1][2]) :  : ); SB();
  asm volatile("ds_read_b64_tr_b16 v[180:181], %0 offset:12288" :  : "v"(aW[0][1][2]) : "memory"); SB();
  asm volatile("buffer_load_dwordx4 %0, %1, %2 offen lds" :  : "v"(voB[0]), "s"(rW1), "s"(kw_[0]) : "memory"); SB();
  asm volatile("v_mfma_f32_16x16x32_bf16 %0, v[192:195], %1, %0" : "+v"(c2[2]) : "v"(ones) : ); SB();
  asm volatile("ds_read_b64_tr_b16 v[182:183], %0 offset:13056" :  : "v"(aW[0][1][2]) : "memory"); SB();
  asm volatile("s_waitcnt lgkmcnt(9)\n\tv_mfma_f32_16x16x32_bf16 %0, v[160:163], a[144:147], %0" : "+v"(a2[0][0]) :  : ); SB();
  asm volatile("ds_read_b128 %0, %1 offset:64" : "=&v"(co[1]) : "v"(cr) : "memory"); SB();
  asm volatile("v_mfma_f32_16x16x32_bf16 %0, v[160:163], a[148:151], %0" : "+v"(a2[1][0]) :  : ); SB();
  asm volatile("ds_read_b128 %0, %1 offset:128" : "=&v"(co[2]) : "v"(cr) : "memory"); SB();
  asm volatile("v_mfma_f32_16x16x32_bf16 %0, v[160:163], %1, %0" : "+v"(c2[0]) : "v"(ones) : ); SB();
  asm volatile("buffer_load_dwordx4 %0, %1, %2 offen offset:1024 lds" :  : "v"(voB[1]), "s"(rW1), "s"(kw_[0]) : "memory"); SB();
  asm volatile("v_mfma_f32_16x16x32_bf16 %0, v[164:167], a[144:147], %0" : "+v"(a2[0][1]) :  : ); SB();
  asm volatile("buffer_load_dwordx4 %0, %1, %2 offen offset:2048 lds" :  : "v"(voB[2]), "s"(rW1), "s"(kw_[0]) : "memory"); SB();
  asm volatile("v_mfma_f32_16x16x32_bf16 %0, v[164:167], a[148:151], %0" : "+v"(a2[1][1]) :  : ); SB();
  asm volatile("v_mfma_f32_16x16x32_bf16 %0, v[164:167], %1, %0" : "+v"(c2[1]) : "v"(ones) : ); SB();
  asm volatile("v_mfma_f32_16x16x32_bf16 %0, v[168:171], a[144:147], %0" : "+v"(a2[0][2]) :  : ); SB();
  asm volatile("s_add_u32 m0, %0, 36864" :  : "s"(mW) : "memory", "scc"); SB();
  asm volatile("v_mfma_f32_16x16x32_bf16 %0, v[168:171], a[148:151], %0" : "+v"(a2[1][2]) :  : ); SB();
  asm volatile("buffer_load_dwordx4 %0, %1, %2 offen lds" :  : "v"(voB[0]), "s"(rW1), "s"(kw_[1]) : "memory"); SB();
  asm volatile("v_mfma_f32_16x16x32_bf16 %0, v[168:171], %1, %0" : "+v"(c2[2]) : "v"(ones) : ); SB();
  asm volatile("s_waitcnt lgkmcnt(3)\n\tv_mfma_f32_16x16x32_bf16 %0, v[172:175], a[152:155], %0" : "+v"(a2[0][0]) :  : ); SB();
  asm volatile("v_mfma_f32_16x16x32_bf16 %0, v[172:175], a[156:159], %0" : "+v"(a2[1][0]) :  : ); SB();
  asm volatile("buffer_load_dwordx4 %0, %1, %2 offen offset:1024 lds" :  : "v"(voB[1]), "s"(rW1), "s"(kw_[1]) : "memory"); SB();
  asm volatile("v_mfma_f32_16x16x32_bf16 %0, v[172:175], %1, %0" : "+v"(c2[0]) : "v"(ones) : ); SB();
  asm volatile("v_mfma_f32_16x16x32_bf16 %0, v[176:179], a[152:155], %0" : "+v"(a2[0][1]) :  : ); SB();
  asm volatile("v_mfma_f32_16x16x32_bf16 %0, v[176:179], a[156:159], %0" : "+v"(a2[1][1]) :  : ); SB();
  asm volatile("v_mfma_f32_16x16x32_bf16 %0, v[176:179], %1, %0" : "+v"(c2[1]) : "v"(ones) : ); SB();
  asm volatile("s_waitcnt lgkmcnt(0)\n\tv_mfma_f32_16x16x32_bf16 %0, v[180:183], a[152:155], %0" : "+v"(a2[0][2]) :  : ); SB();
  asm volatile("buffer_load_dwordx4 %0, %1, %2 offen offset:2048 lds" :  : "v"(voB[2]), "s"(rW1), "s"(kw_[1]) : "memory"); SB();
  asm volatile("v_mfma_f32_16x16x32_bf16 %0, v[180:183], a[156:159], %0" : "+v"(a2[1][2]) :  : ); SB();
  asm volatile("v_mfma_f32_16x16x32_bf16 %0, v[180:183], %1, %0" : "+v"(c2[2]) : "v"(ones) : ); SB();
  ST(3);
  ST(5);
  const unsigned long long tg = ((unsigned long long)tg1 << 32) | tg0;
  asm volatile("" : "+v"(xs[0]), "+v"(xs[1]), "+v"(co[0]), "+v"(co[1]), "+v"(co[2]) :  : "memory"); SB();
  asm volatile("s_nop 3" : "+v"(a2[0][0]), "+v"(a2[0][1]), "+v"(a2[0][2]), "+v"(a2[1][0]), "+v"(a2[1][1]), "+v"(a2[1][2]), "+v"(c2[0]), "+v"(c2[1]), "+v"(c2[2]) :  : ); SB();
  for (int i = 0; i < 2; ++i) { xm[i] = xs[i][0]; xd[i] = xs[i][1]; k1[i] = xs[i][2]; k2[i] = xs[i][3]; }
  c2[0] += co[0]; c2[1] += co[1]; c2[2] += co[2];
  f32x2 ge2[2];
  auto act = [&](f32x2& t, f32x2& e, const f32x4& a, const f32x4& cs, int i, int h) {
    t = __builtin_elementwise_fma((f32x2){-xm[i], -xm[i]}, (f32x2){cs[2 * h], cs[2 * h + 1]}, (f32x2){a[2 * h], a[2 * h + 1]});
    const f32x2 u = t * __builtin_elementwise_fma(t * t, (f32x2){k2[i], k2[i]}, (f32x2){k1[i], k1[i]});
    e = (f32x2){__builtin_amdgcn_exp2f(u[0]), __builtin_amdgcn_exp2f(u[1])};
  };
  auto act2 = [&](f32x2& t, f32x2 e, int i) {
    e = __builtin_elementwise_fma(e, (f32x2){xd[i], xd[i]}, (f32x2){xd[i], xd[i]});
    t *= (f32x2){__builtin_amdgcn_rcpf(e[0]), __builtin_amdgcn_rcpf(e[1])};
  };
  f32x2 gv[2][3][2];
  float sv[2][2], pv[2];
  act(gv[0][0][0], ge2[0], a2[0][0], c2[0], 0, 0); act(gv[0][0][1], ge2[1], a2[0][0], c2[0], 0, 1);
  asm volatile("" : "+v"(gv[0][0][0]), "+v"(gv[0][0][1]), "+v"(ge2[0]), "+v"(ge2[1]));
  SB();
  asm volatile("s_add_u32 m0, %0, 49152" :  : "s"(mW) : "memory", "scc"); SB();
  asm volatile("s_nop 0\n\tbuffer_load_dwordx4 %0, %1, %2 offen lds" :  : "v"(voB[0]), "s"(rW1), "s"(kw_[2]) : "memory"); SB();
  asm volatile("buffer_load_dwordx4 %0, %1, %2 offen offset:1024 lds" :  : "v"(voB[1]), "s"(rW1), "s"(kw_[2]) : "memory"); SB();
  act2(gv[0][0][0], ge2[0], 0); act2(gv[0][0][1], ge2[1], 0);
  asm volatile("" : "+v"(gv[0][0][0]), "+v"(gv[0][0][1]));
  SB();
  asm volatile("buffer_load_dwordx4 %0, %1, %2 offen offset:2048 lds" :  : "v"(voB[2]), "s"(rW1), "s"(kw_[2]) : "memory"); SB();
  act(gv[0][1][0], ge2[0], a2[0][1], c2[1], 0, 0); act(gv[0][1][1], ge2[1], a2[0][1], c2[1], 0, 1);
  asm volatile("" : "+v"(gv[0][1][0]), "+v"(gv[0][1][1]), "+v"(ge2[0]), "+v"(ge2[1]));
  SB();
  asm volatile("s_add_u32 m0, %0, 61440" :  : "s"(mW) : "memory", "scc"); SB();
  asm volatile("s_nop 0\n\tbuffer_load_dwordx4 %0, %1, %2 offen lds" :  : "v"(voB[0]), "s"(rW1), "s"(kw_[3]) : "memory"); SB();
  asm volatile("buffer_load_dwordx4 %0, %1, %2 offen offset:1024 lds" :  : "v"(voB[1]), "s"(rW1), "s"(kw_[3]) : "memory"); SB();
  act2(gv[0][1][0], ge2[0], 0); act2(gv[0][1][1], ge2[1], 0);
  asm volatile("" : "+v"(gv[0][1][0]), "+v"(gv[0][1][1]));
  SB();
  asm volatile("buffer_load_dwordx4 %0, %1, %2 offen offset:2048 lds" :  : "v"(voB[2]), "s"(rW1), "s"(kw_[3]) : "memory"); SB();
  act(gv[0][2][0], ge2[0], a2[0][2], c2[2], 0, 0); act(gv[0][2][1], ge2[1], a2[0][2], c2[2], 0, 1);
  asm volatile("" : "+v"(gv[0][2][0]), "+v"(gv[0][2][1]), "+v"(ge2[0]), "+v"(ge2[1]));
  SB();
  asm volatile("s_add_u32 m0, %0, 73728" :  : "s"(mW) : "memory", "scc"); SB();
  asm volatile("s_nop 0\n\tbuffer_load_dwordx4 %0, %1, %2 offen lds" :  : "v"(voB[0]), "s"(rW1), "s"(kw_[4]) : "memory"); SB();
  asm volatile("buffer_load_dwordx4 %0, %1, %2 offen offset:1024 lds" :  : "v"(voB[1]), "s"(rW1), "s"(kw_[4]) : "memory"); SB();
  act2(gv[0][2][0], ge2[0], 0); act2(gv[0][2][1], ge2[1], 0);
  asm volatile("" : "+v"(gv[0][2][0]), "+v"(gv[0][2][1]));
  SB();
  asm volatile("buffer_load_dwordx4 %0, %1, %2 offen offset:2048 lds" :  : "v"(voB[2]), "s"(rW1), "s"(kw_[4]) : "memory"); SB();
  act(gv[1][0][0], ge2[0], a2[1][0], c2[0], 1, 0); act(gv[1][0][1], ge2[1], a2[1][0], c2[0], 1, 1);
  asm volatile("" : "+v"(gv[1][0][0]), "+v"(gv[1][0][1]), "+v"(ge2[0]), "+v"(ge2[1]));
  SB();
  act2(gv[1][0][0], ge2[0], 1); act2(gv[1][0][1], ge2[1], 1);
  asm volatile("" : "+v"(gv[1][0][0]), "+v"(gv[1][0][1]));
  SB();
  act(gv[1][1][0], ge2[0], a2[1][1], c2[1], 1, 0); act(gv[1][1][1], ge2[1], a2[1][1], c2[1], 1, 1);
  asm volatile("" : "+v"(gv[1][1][0]), "+v"(gv[1][1][1]), "+v"(ge2[0]), "+v"(ge2[1]));
  SB();
  act2(gv[1][1][0], ge2[0], 1); act2(gv[1][1][1], ge2[1], 1);
  asm volatile("" : "+v"(gv[1][1][0]), "+v"(gv[1][1][1]));
  SB();
  act(gv[1][2][0], ge2[0], a2[1][2], c2[2], 1, 0); act(gv[1][2][1], ge2[1], a2[1][2], c2[2], 1, 1);
  asm volatile("" : "+v"(gv[1][2][0]), "+v"(gv[1][2][1]), "+v"(ge2[0]), "+v"(ge2[1]));
  SB();
  asm volatile("s_add_u32 m0, %0, 86016" :  : "s"(mW) : "memory", "scc"); SB();
  asm volatile("s_nop 0\n\tbuffer_load_dwordx4 %0, %1, %2 offen lds" :  : "v"(voB[0]), "s"(rW1), "s"(kw_[5]) : "memory"); SB();
  act2(gv[1][2][0], ge2[0], 1); act2(gv[1][2][1], ge2[1], 1);
  asm volatile("" : "+v"(gv[1][2][0]), "+v"(gv[1][2][1]));
  SB();
  asm volatile("buffer_load_dwordx4 %0, %1, %2 offen offset:1024 lds" :  : "v"(voB[1]), "s"(rW1), "s"(kw_[5]) : "memory"); SB();
#pragma unroll
  for (int i = 0; i < 2; ++i) {
    const unsigned p0 = __float_as_uint(gv[i][0][0][0]);
    const auto pa = __builtin_amdgcn_permlane16_swap(p0, p0, false, false);
    const auto pb = __builtin_amdgcn_permlane32_swap(pa[0], pa[0], false, false);
    pv[i] = __uint_as_float(pb[0]);
  }
  asm volatile("" : "+v"(pv[0]), "+v"(pv[1]));
  f32x2 s1[2], s2[2];
  SB();
  asm volatile("buffer_load_dwordx4 %0, %1, %2 offen offset:2048 lds" :  : "v"(voB[2]), "s"(rW1), "s"(kw_[5]) : "memory"); SB();
#pragma unroll
  for (int m = 0; m < 4; ++m) {
    const int i = m & 1, n = 0 + (m >> 1);
    const f32x2 d = gv[i][n >> 1][n & 1] - (f32x2){pv[i], pv[i]};
    s1[i] = n ? s1[i] + d : d; s2[i] = n ? __builtin_elementwise_fma(d, d, s2[i]) : d * d;
  }
  asm volatile("" : "+v"(s1[0]), "+v"(s1[1]), "+v"(s2[0]), "+v"(s2[1]));
  SB();
#pragma unroll
  for (int m = 0; m < 4; ++m) {
    const int i = m & 1, n = 2 + (m >> 1);
    const f32x2 d = gv[i][n >> 1][n & 1] - (f32x2){pv[i], pv[i]};
    s1[i] = n ? s1[i] + d : d; s2[i] = n ? __builtin_elementwise_fma(d, d, s2[i]) : d * d;
  }
  asm volatile("" : "+v"(s1[0]), "+v"(s1[1]), "+v"(s2[0]), "+v"(s2[1]));
  SB();
  asm volatile("s_add_u32 m0, %0, 0" :  : "s"(mW) : "memory", "scc"); SB();
  asm volatile("s_nop 0\n\tbuffer_load_dwordx4 %0, %1, %2 offen lds" :  : "v"(voB[0]), "s"(rW1), "s"(kw_[6]) : "memory"); SB();
#pragma unroll
  for (int m = 0; m < 4; ++m) {
    const int i = m & 1, n = 4 + (m >> 1);
    const f32x2 d = gv[i][n >> 1][n & 1] - (f32x2){pv[i], pv[i]};
    s1[i] = n ? s1[i] + d : d; s2[i] = n ? __builtin_elementwise_fma(d, d, s2[i]) : d * d;
  }
  asm volatile("" : "+v"(s1[0]), "+v"(s1[1]), "+v"(s2[0]), "+v"(s2[1]));
  SB();
  asm volatile("buffer_load_dwordx4 %0, %1, %2 offen offset:1024 lds" :  : "v"(voB[1]), "s"(rW1), "s"(kw_[6]) : "memory"); SB();
  for (int i = 0; i < 2; ++i) { sv[i][0] = s1[i][0] + s1[i][1]; sv[i][1] = s2[i][0] + s2[i][1]; }
  {
    const float t1 = red16(sel32(sv[0][0], sv[1][0])), t2 = red16(sel32(sv[0][1], sv[1][1]));
    const float dl = t1 * (1.0f / 48.0f);
    const u32x4 so = {__float_as_uint((lane < 32 ? pv[0] : pv[1]) + dl), __float_as_uint(fmaxf(t2 - t1 * dl, 0.f)), tg0, tg1};
    SB();
  asm volatile("s_mov_b32 exec_lo, 0xffff\n\ts_mov_b32 exec_hi, 0xffff\n\tbuffer_store_dwordx4 %1, %0, %2, %3 offen offset:0 sc1\n\ts_mov_b64 exec, -1\n\ts_nop 0" :  : "v"(vo), "v"(so), "s"(rWS), "s"(0u) : "memory"); SB();
  }
  ST(6);
  unsigned zp[2][3][2];
#pragma unroll
  for (int n = 0; n < 12; ++n) { zp[n / 6][(n >> 1) % 3][n & 1] = pk2(gv[n / 6][(n >> 1) % 3][n & 1]); asm volatile("" : "+v"(zp[n / 6][(n >> 1) % 3][n & 1])); }
  SB();
  asm volatile("s_waitcnt vmcnt(24)\n\ts_barrier" :  :  : "memory"); SB();
  ST(2);
  asm volatile("ds_read_b64_tr_b16 %0, %1 offset:0" : "=&v"(wt[0]) : "v"(oa) : "memory"); SB();
  asm volatile("ds_read_b64_tr_b16 %0, %1 offset:512" : "=&v"(wt[1]) : "v"(oa) : "memory"); SB();
  asm volatile("ds_read_b64_tr_b16 %0, %1 offset:0" : "=&v"(kp[0]) : "v"(ka) : "memory"); SB();
  asm volatile("ds_read_b64_tr_b16 %0, %1 offset:640" : "=&v"(kc[0]) : "v"(ka) : "memory"); SB();
  asm volatile("ds_read_b64_tr_b16 %0, %1 offset:4096" : "=&v"(vv[0]) : "v"(ka) : "memory"); SB();
  asm volatile("ds_read_b64 %0, %1 offset:0" : "=&v"(sw[0]) : "v"(sa) : "memory"); SB();
  asm volatile("buffer_load_dwordx4 %0, %1, %2 offen offset:2048 lds" :  : "v"(voB[2]), "s"(rW1), "s"(kw_[6]) : "memory"); SB();
  asm volatile("s_add_u32 m0, %0, 131072" :  : "s"(mA) : "memory", "scc"); SB();
  asm volatile("s_nop 0\n\tbuffer_load_dwordx4 %0, %1, %2 offen lds" :  : "v"(voS), "s"(rA), "s"(ao[0]) : "memory"); SB();
  asm volatile("ds_read_b64_tr_b16 %0, %1 offset:32" : "=&v"(kp[1]) : "v"(ka) : "memory"); SB();
  asm volatile("ds_read_b64_tr_b16 %0, %1 offset:672" : "=&v"(kc[1]) : "v"(ka) : "memory"); SB();
  asm volatile("ds_read_b64_tr_b16 %0, %1 offset:4128" : "=&v"(vv[1]) : "v"(ka) : "memory"); SB();
  asm volatile("ds_read_b64 %0, %1 offset:128" : "=&v"(sw[1]) : "v"(sa) : "memory"); SB();
  asm volatile("buffer_load_dwordx4 %0, %1, %2 offen offset:1024 lds" :  : "v"(voS), "s"(rA), "s"(ao[1]) : "memory"); SB();
  asm volatile("ds_read_b64_tr_b16 %0, %1 offset:64" : "=&v"(kp[2]) : "v"(ka) : "memory"); SB();
  asm volatile("ds_read_b64_tr_b16 %0, %1 offset:704" : "=&v"(kc[2]) : "v"(ka) : "memory"); SB();
  asm volatile("ds_read_b64_tr_b16 %0, %1 offset:4160" : "=&v"(vv[2]) : "v"(ka) : "memory"); SB();
  asm volatile("ds_read_b64 %0, %1 offset:256" : "=&v"(sw[2]) : "v"(sa) : "memory"); SB();
  asm volatile("ds_read_b64_tr_b16 %0, %1 offset:96" : "=&v"(kp[3]) : "v"(ka) : "memory"); SB();
  asm volatile("ds_read_b64_tr_b16 %0, %1 offset:736" : "=&v"(kc[3]) : "v"(ka) : "memory"); SB();
  asm volatile("ds_read_b64_tr_b16 %0, %1 offset:4192" : "=&v"(vv[3]) : "v"(ka) : "memory"); SB();
  asm volatile("ds_read_b64 %0, %1 offset:384" : "=&v"(sw[3]) : "v"(sa) : "memory"); SB();
  asm volatile("buffer_load_dwordx4 %0, %1, %2 offen offset:2048 lds" :  : "v"(voS), "s"(rA), "s"(ao[2]) : "memory"); SB();
  asm volatile("ds_read_b64_tr_b16 %0, %1 offset:128" : "=&v"(kp[4]) : "v"(ka) : "memory"); SB();
  asm volatile("ds_read_b64_tr_b16 %0, %1 offset:768" : "=&v"(kc[4]) : "v"(ka) : "memory"); SB();
  asm volatile("ds_read_b64_tr_b16 %0, %1 offset:4224" : "=&v"(vv[4]) : "v"(ka) : "memory"); SB();
  asm volatile("ds_read_b64 %0, %1 offset:512" : "=&v"(sw[4]) : "v"(sa) : "memory"); SB();
  asm volatile("buffer_load_dwordx4 %0, %1, %2 offen offset:3072 lds" :  : "v"(voS), "s"(rA), "s"(ao[3]) : "memory"); SB();
  asm volatile("s_waitcnt lgkmcnt(8)" : "+v"(kp[0]), "+v"(kc[0]), "+v"(vv[0]), "+v"(sw[0]), "+v"(wt[0]), "+v"(wt[1]) :  : "memory"); SB();
  asm volatile("v_alignbit_b32 %4, %14, %14, 16\n\tv_alignbit_b32 %5, %10, %9, 16\n\tv_alignbit_b32 %6, %11, %10, 16\n\tv_alignbit_b32 %7, %13, %13, 16\n\tv_alignbit_b32 %8, %12, %11, 16\n\tv_dot2_f32_bf16 %0, %5, %4, 0\n\tv_dot2_f32_bf16 %1, %10, %4, 0\n\tv_dot2_f32_bf16 %2, %6, %4, 0\n\tv_dot2_f32_bf16 %3, %11, %4, 0\n\tv_dot2_f32_bf16 %0, %6, %7, %0\n\tv_dot2_f32_bf16 %1, %11, %7, %1\n\tv_dot2_f32_bf16 %2, %8, %7, %2\n\tv_dot2_f32_bf16 %3, %12, %7, %3" : "=&v"(kt[0][0]), "=&v"(kt[0][1]), "=&v"(kt[1][0]), "=&v"(kt[1][1]), "=&v"(ku[0]), "=&v"(ku[1]), "=&v"(ku[2]), "=&v"(ku[3]), "=&v"(ku[4]) : "v"(kp[0][0]), "v"(kp[0][1]), "v"(kc[0][0]), "v"(kc[0][1]), "v"(sw[0][0]), "v"(sw[0][1]) : ); SB();
  asm volatile("s_add_u32 m0, %0, 135168" :  : "s"(mA) : "memory", "scc"); SB();
  asm volatile("s_nop 0\n\tbuffer_load_dwordx4 %0, %1, %2 offen lds" :  : "v"(voS), "s"(rA), "s"(ao[4]) : "memory"); SB();
  asm volatile("v_lshlrev_b32 %0, 16, %4\n\tv_and_b32 %1, 0xffff0000, %4\n\tv_lshlrev_b32 %2, 16, %5\n\tv_and_b32 %3, 0xffff0000, %5" : "=&v"(kf[0][0]), "=&v"(kf[0][1]), "=&v"(kf[1][0]), "=&v"(kf[1][1]) : "v"(vv[0][0]), "v"(vv[0][1]) : ); SB();
  kt[0] *= kf[0]; kt[1] *= kf[1]; SB();
  ko[0] = (u32x2){pk2(kt[0]), pk2(kt[1])};
  asm volatile("" : "+v"(ko[0]));
  SB();
  asm volatile("buffer_load_dwordx4 %0, %1, %2 offen offset:1024 lds" :  : "v"(voS), "s"(rA), "s"(ao[5]) : "memory"); SB();
  asm volatile("ds_write_b64 %0, %1 offset:0" :  : "v"(kw), "v"(ko[0]) : "memory"); SB();
  asm volatile("" : "+v"(kp[1]), "+v"(kc[1]), "+v"(vv[1]), "+v"(sw[1]) :  : "memory"); SB();
  asm volatile("v_alignbit_b32 %4, %14, %14, 16\n\tv_alignbit_b32 %5, %10, %9, 16\n\tv_alignbit_b32 %6, %11, %10, 16\n\tv_alignbit_b32 %7, %13, %13, 16\n\tv_alignbit_b32 %8, %12, %11, 16\n\tv_dot2_f32_bf16 %0, %5, %4, 0\n\tv_dot2_f32_bf16 %1, %10, %4, 0\n\tv_dot2_f32_bf16 %2, %6, %4, 0\n\tv_dot2_f32_bf16 %3, %11, %4, 0\n\tv_dot2_f32_bf16 %0, %6, %7, %0\n\tv_dot2_f32_bf16 %1, %11, %7, %1\n\tv_dot2_f32_bf16 %2, %8, %7, %2\n\tv_dot2_f32_bf16 %3, %12, %7, %3" : "=&v"(kt[0][0]), "=&v"(kt[0][1]), "=&v"(kt[1][0]), "=&v"(kt[1][1]), "=&v"(ku[0]), "=&v"(ku[1]), "=&v"(ku[2]), "=&v"(ku[3]), "=&v"(ku[4]) : "v"(kp[1][0]), "v"(kp[1][1]), "v"(kc[1][0]), "v"(kc[1][1]), "v"(sw[1][0]), "v"(sw[1][1]) : ); SB();
  asm volatile("buffer_load_dwordx4 %0, %1, %2 offen offset:2048 lds" :  : "v"(voS), "s"(rA), "s"(ao[6]) : "memory"); SB();
  asm volatile("v_lshlrev_b32 %0, 16, %4\n\tv_and_b32 %1, 0xffff0000, %4\n\tv_lshlrev_b32 %2, 16, %5\n\tv_and_b32 %3, 0xffff0000, %5" : "=&v"(kf[0][0]), "=&v"(kf[0][1]), "=&v"(kf[1][0]), "=&v"(kf[1][1]) : "v"(vv[1][0]), "v"(vv[1][1]) : ); SB();
  kt[0] *= kf[0]; kt[1] *= kf[1]; SB();
  ko[1] = (u32x2){pk2(kt[0]), pk2(kt[1])};
  asm volatile("" : "+v"(ko[1]));
  SB();
  asm volatile("ds_write_b64 %0, %1 offset:2304" :  : "v"(kw), "v"(ko[1]) : "memory"); SB();
  asm volatile("" : "+v"(kp[2]), "+v"(kc[2]), "+v"(vv[2]), "+v"(sw[2]) :  : "memory"); SB();
  asm volatile("v_alignbit_b32 %4, %14, %14, 16\n\tv_alignbit_b32 %5, %10, %9, 16\n\tv_alignbit_b32 %6, %11, %10, 16\n\tv_alignbit_b32 %7, %13, %13, 16\n\tv_alignbit_b32 %8, %12, %11, 16\n\tv_dot2_f32_bf16 %0, %5, %4, 0\n\tv_dot2_f32_bf16 %1, %10, %4, 0\n\tv_dot2_f32_bf16 %2, %6, %4, 0\n\tv_dot2_f32_bf16 %3, %11, %4, 0\n\tv_dot2_f32_bf16 %0, %6, %7, %0\n\tv_dot2_f32_bf16 %1, %11, %7, %1\n\tv_dot2_f32_bf16 %2, %8, %7, %2\n\tv_dot2_f32_bf16 %3, %12, %7, %3" : "=&v"(kt[0][0]), "=&v"(kt[0][1]), "=&v"(kt[1][0]), "=&v"(kt[1][1]), "=&v"(ku[0]), "=&v"(ku[1]), "=&v"(ku[2]), "=&v"(ku[3]), "=&v"(ku[4]) : "v"(kp[2][0]), "v"(kp[2][1]), "v"(kc[2][0]), "v"(kc[2][1]), "v"(sw[2][0]), "v"(sw[2][1]) : ); SB();
  asm volatile("buffer_load_dwordx4 %0, %1, %2 offen offset:3072 lds" :  : "v"(voS), "s"(rA), "s"(ao[7]) : "memory"); SB();
  asm volatile("v_xor_b32 %0, 128, %0" : "+v"(voS) :  : "memory"); SB();
  asm volatile("v_lshlrev_b32 %0, 16, %4\n\tv_and_b32 %1, 0xffff0000, %4\n\tv_lshlrev_b32 %2, 16, %5\n\tv_and_b32 %3, 0xffff0000, %5" : "=&v"(kf[0][0]), "=&v"(kf[0][1]), "=&v"(kf[1][0]), "=&v"(kf[1][1]) : "v"(vv[2][0]), "v"(vv[2][1]) : ); SB();
  kt[0] *= kf[0]; kt[1] *= kf[1]; SB();
  ko[2] = (u32x2){pk2(kt[0]), pk2(kt[1])};
  asm volatile("" : "+v"(ko[2]));
  SB();
  asm volatile("ds_write_b64 %0, %1 offset:4608" :  : "v"(kw), "v"(ko[2]) : "memory"); SB();
  asm volatile("s_waitcnt lgkmcnt(2)" : "+v"(kp[3]), "+v"(kc[3]), "+v"(vv[3]), "+v"(sw[3]) :  : "memory"); SB();
  asm volatile("v_alignbit_b32 %4, %14, %14, 16\n\tv_alignbit_b32 %5, %10, %9, 16\n\tv_alignbit_b32 %6, %11, %10, 16\n\tv_alignbit_b32 %7, %13, %13, 16\n\tv_alignbit_b32 %8, %12, %11, 16\n\tv_dot2_f32_bf16 %0, %5, %4, 0\n\tv_dot2_f32_bf16 %1, %10, %4, 0\n\tv_dot2_f32_bf16 %2, %6, %4, 0\n\tv_dot2_f32_bf16 %3, %11, %4, 0\n\tv_dot2_f32_bf16 %0, %6, %7, %0\n\tv_dot2_f32_bf16 %1, %11, %7, %1\n\tv_dot2_f32_bf16 %2, %8, %7, %2\n\tv_dot2_f32_bf16 %3, %12, %7, %3" : "=&v"(kt[0][0]), "=&v"(kt[0][1]), "=&v"(kt[1][0]), "=&v"(kt[1][1]), "=&v"(ku[0]), "=&v"(ku[1]), "=&v"(ku[2]), "=&v"(ku[3]), "=&v"(ku[4]) : "v"(kp[3][0]), "v"(kp[3][1]), "v"(kc[3][0]), "v"(kc[3][1]), "v"(sw[3][0]), "v"(sw[3][1]) : ); SB();
  asm volatile("s_add_u32 m0, %0, 98304" :  : "s"(mA) : "memory", "scc"); SB();
  asm volatile("s_nop 0\n\tbuffer_load_dwordx4 %0, %1, %2 offen lds" :  : "v"(voS), "s"(rA), "s"(ao[0]) : "memory"); SB();
  asm volatile("buffer_load_dwordx4 %0, %1, %2 offen offset:1024 lds" :  : "v"(voS), "s"(rA), "s"(ao[1]) : "memory"); SB();
  asm volatile("v_lshlrev_b32 %0, 16, %4\n\tv_and_b32 %1, 0xffff0000, %4\n\tv_lshlrev_b32 %2, 16, %5\n\tv_and_b32 %3, 0xffff0000, %5" : "=&v"(kf[0][0]), "=&v"(kf[0][1]), "=&v"(kf[1][0]), "=&v"(kf[1][1]) : "v"(vv[3][0]), "v"(vv[3][1]) : ); SB();
  kt[0] *= kf[0]; kt[1] *= kf[1]; SB();
  ko[3] = (u32x2){pk2(kt[0]), pk2(kt[1])};
  asm volatile("" : "+v"(ko[3]));
  SB();
  asm volatile("buffer_load_dwordx4 %0, %1, %2 offen offset:2048 lds" :  : "v"(voS), "s"(rA), "s"(ao[2]) : "memory"); SB();
  asm volatile("ds_write_b64 %0, %1 offset:6912" :  : "v"(kw), "v"(ko[3]) : "memory"); SB();
  asm volatile("" : "+v"(kp[4]), "+v"(kc[4]), "+v"(vv[4]), "+v"(sw[4]) :  : "memory"); SB();
  asm volatile("v_alignbit_b32 %4, %14, %14, 16\n\tv_alignbit_b32 %5, %10, %9, 16\n\tv_alignbit_b32 %6, %11, %10, 16\n\tv_alignbit_b32 %7, %13, %13, 16\n\tv_alignbit_b32 %8, %12, %11, 16\n\tv_dot2_f32_bf16 %0, %5, %4, 0\n\tv_dot2_f32_bf16 %1, %10, %4, 0\n\tv_dot2_f32_bf16 %2, %6, %4, 0\n\tv_dot2_f32_bf16 %3, %11, %4, 0\n\tv_dot2_f32_bf16 %0, %6, %7, %0\n\tv_dot2_f32_bf16 %1, %11, %7, %1\n\tv_dot2_f32_bf16 %2, %8, %7, %2\n\tv_dot2_f32_bf16 %3, %12, %7, %3" : "=&v"(kt[0][0]), "=&v"(kt[0][1]), "=&v"(kt[1][0]), "=&v"(kt[1][1]), "=&v"(ku[0]), "=&v"(ku[1]), "=&v"(ku[2]), "=&v"(ku[3]), "=&v"(ku[4]) : "v"(kp[4][0]), "v"(kp[4][1]), "v"(kc[4][0]), "v"(kc[4][1]), "v"(sw[4][0]), "v"(sw[4][1]) : ); SB();
  asm volatile("v_lshlrev_b32 %0, 16, %4\n\tv_and_b32 %1, 0xffff0000, %4\n\tv_lshlrev_b32 %2, 16, %5\n\tv_and_b32 %3, 0xffff0000, %5" : "=&v"(kf[0][0]), "=&v"(kf[0][1]), "=&v"(kf[1][0]), "=&v"(kf[1][1]) : "v"(vv[4][0]), "v"(vv[4][1]) : ); SB();
  kt[0] *= kf[0]; kt[1] *= kf[1]; SB();
  ko[4] = (u32x2){pk2(kt[0]), pk2(kt[1])};
  asm volatile("" : "+v"(ko[4]));
  SB();
  asm volatile("ds_write_b64 %0, %1 offset:9216" :  : "v"(kw), "v"(ko[4]) : "memory"); SB();
  ST(16);
  asm volatile("s_waitcnt vmcnt(27) lgkmcnt(0)\n\ts_barrier" :  :  : "memory"); SB();
  asm volatile("ds_read_b128 %0, %1 offset:0" : "=&v"(kq[0]) : "v"(kr) : "memory"); SB();
  asm volatile("ds_read_b128 %0, %1 offset:1152" : "=&v"(kq[1]) : "v"(kr) : "memory"); SB();
  asm volatile("ds_read_b128 %0, %1 offset:2304" : "=&v"(kq[2]) : "v"(kr) : "memory"); SB();
  asm volatile("ds_read_b64_tr_b16 v[184:185], %0 offset:24576" :  : "v"(aW[0][0][0]) : "memory"); SB();
  asm volatile("ds_read_b64_tr_b16 v[186:187], %0 offset:25344" :  : "v"(aW[0][0][0]) : "memory"); SB();
  asm volatile("ds_read_b64_tr_b16 v[188:189], %0 offset:24576" :  : "v"(aW[0][0][1]) : "memory"); SB();
  asm volatile("ds_read_b64_tr_b16 v[190:191], %0 offset:25344" :  : "v"(aW[0][0][1]) : "memory"); SB();
  asm volatile("ds_read_b64_tr_b16 v[192:193], %0 offset:24576" :  : "v"(aW[0][0][2]) : "memory"); SB();
  asm volatile("ds_read_b64_tr_b16 v[194:195], %0 offset:25344" :  : "v"(aW[0][0][2]) : "memory"); SB();
  ST(7);
  asm volatile("s_waitcnt lgkmcnt(6)" : "+v"(kq[0]), "+v"(kq[1]), "+v"(kq[2]) :  : "memory"); SB();
  unsigned kz[3] = {kq[0][3], kq[1][3] | (w + 1) >> 2, kq[2][3] | (w + 1) >> 2};
  asm volatile("" : "+v"(kz[0]), "+v"(kz[1]), "+v"(kz[2])); SB();
  asm volatile("buffer_load_dwordx4 v[196:199], %0, %1, %2 offen offset:0 sc1" :  : "v"(vS), "s"(rWS), "s"(0u) : "memory"); SB();
  asm volatile("buffer_load_dwordx4 v[200:203], %0, %1, %2 offen offset:1024 sc1" :  : "v"(vS), "s"(rWS), "s"(0u) : "memory"); SB();
  asm volatile("s_waitcnt vmcnt(11)" :  :  : "memory"); SB();
  asm volatile("ds_read_b128 a[160:163], %0 offset:32768" :  : "v"(aA[0]) : "memory"); SB();
  asm volatile("s_waitcnt lgkmcnt(2)\n\tv_mfma_f32_16x16x32_bf16 %0, v[184:187], a[0:3], 0" : "=&v"(a1[0][0]) :  : ); SB();
  asm volatile("ds_read_b64_tr_b16 v[160:161], %0 offset:24576" :  : "v"(aW[0][1][0]) : "memory"); SB();
  asm volatile("v_mfma_f32_16x16x32_bf16 %0, v[184:187], a[4:7], 0" : "=&v"(a1[1][0]) :  : ); SB();
  asm volatile("ds_read_b64_tr_b16 v[162:163], %0 offset:25344" :  : "v"(aW[0][1][0]) : "memory"); SB();
  asm volatile("v_mfma_f32_16x16x32_bf16 %0, v[184:187], %1, 0" : "=&v"(c1[0]) : "v"(ones) : ); SB();
  asm volatile("ds_read_b64_tr_b16 v[164:165], %0 offset:24576" :  : "v"(aW[0][1][1]) : "memory"); SB();
  asm volatile("v_mfma_f32_16x16x32_bf16 %0, v[188:191], a[0:3], 0" : "=&v"(a1[0][1]) :  : ); SB();
  asm volatile("ds_read_b64_tr_b16 v[166:167], %0 offset:25344" :  : "v"(aW[0][1][1]) : "memory"); SB();
  asm volatile("v_mfma_f32_16x16x32_bf16 %0, v[188:191], a[4:7], 0" : "=&v"(a1[1][1]) :  : ); SB();
  asm volatile("ds_read_b64_tr_b16 v[168:169], %0 offset:24576" :  : "v"(aW[0][1][2]) : "memory"); SB();
  asm volatile("v_mfma_f32_16x16x32_bf16 %0, v[188:191], %1, 0" : "=&v"(c1[1]) : "v"(ones) : ); SB();
  asm volatile("ds_read_b64_tr_b16 v[170:171], %0 offset:25344" :  : "v"(aW[0][1][2]) : "memory"); SB();
  asm volatile("s_waitcnt lgkmcnt(7)\n\tv_mfma_f32_16x16x32_bf16 %0, v[192:195], a[0:3], 0" : "=&v"(a1[0][2]) :  : ); SB();
  asm volatile("ds_read_b64_tr_b16 v[172:173], %0 offset:36864" :  : "v"(aW[0][0][0]) : "memory"); SB();
  asm volatile("buffer_load_dwordx4 v[204:207], %0, %1, %2 offen offset:2048 sc1" :  : "v"(vS), "s"(rWS), "s"(0u) : "memory"); SB();
  asm volatile("v_mfma_f32_16x16x32_bf16 %0, v[192:195], a[4:7], 0" : "=&v"(a1[1][2]) :  : ); SB();
  asm volatile("ds_read_b64_tr_b16 v[174:175], %0 offset:37632" :  : "v"(aW[0][0][0]) : "memory"); SB();
  asm volatile("v_mfma_f32_16x16x32_bf16 %0, v[192:195], %1, 0" : "=&v"(c1[2]) : "v"(ones) : ); SB();
  asm volatile("ds_read_b64_tr_b16 v[176:177], %0 offset:36864" :  : "v"(aW[0][0][1]) : "memory"); SB();
  asm volatile("s_waitcnt lgkmcnt(7)\n\tv_mfma_f32_16x16x32_bf16 %0, v[160:163], a[8:11], %0" : "+v"(a1[0][0]) :  : ); SB();
  asm volatile("ds_read_b64_tr_b16 v[178:179], %0 offset:37632" :  : "v"(aW[0][0][1]) : "memory"); SB();
  asm volatile("buffer_load_dwordx4 v[208:211], %0, %1, %2 offen offset:3072 sc1" :  : "v"(vS), "s"(rWS), "s"(0u) : "memory"); SB();
  asm volatile("v_mfma_f32_16x16x32_bf16 %0, v[160:163], a[12:15], %0" : "+v"(a1[1][0]) :  : ); SB();
  asm volatile("ds_read_b64_tr_b16 v[180:181], %0 offset:36864" :  : "v"(aW[0][0][2]) : "memory"); SB();
  asm volatile("s_waitcnt lgkmcnt(7)\n\tv_mfma_f32_16x16x32_bf16 %0, v[164:167], a[8:11], %0" : "+v"(a1[0][1]) :  : ); SB();
  asm volatile("ds_read_b64_tr_b16 v[182:183], %0 offset:37632" :  : "v"(aW[0][0][2]) : "memory"); SB();
  asm volatile("v_mfma_f32_16x16x32_bf16 %0, v[164:167], a[12:15], %0" : "+v"(a1[1][1]) :  : ); SB();
  asm volatile("ds_read_b64_tr_b16 v[184:185], %0 offset:36864" :  : "v"(aW[0][1][0]) : "memory"); SB();
  asm volatile("s_waitcnt lgkmcnt(7)\n\tv_mfma_f32_16x16x32_bf16 %0, v[168:171], a[8:11], %0" : "+v"(a1[0][2]) :  : ); SB();
  asm volatile("ds_read_b64_tr_b16 v[186:187], %0 offset:37632" :  : "v"(aW[0][1][0]) : "memory"); SB();
  asm volatile("v_mfma_f32_16x16x32_bf16 %0, v[168:171], a[12:15], %0" : "+v"(a1[1][2]) :  : ); SB();
  asm volatile("ds_read_b64_tr_b16 v[188:189], %0 offset:36864" :  : "v"(aW[0][1][1]) : "memory"); SB();
  asm volatile("buffer_load_dwordx4 v[212:215], %0, %1, %2 offen offset:0 sc1" :  : "v"(vS), "s"(rWS), "s"(4096u) : "memory"); SB();
  asm volatile("s_waitcnt lgkmcnt(7)\n\tv_mfma_f32_16x16x32_bf16 %0, v[172:175], a[16:19], %0" : "+v"(a1[0][0]) :  : ); SB();
  asm volatile("ds_read_b64_tr_b16 v[190:191], %0 offset:37632" :  : "v"(aW[0][1][1]) : "memory"); SB();
  asm volatile("v_mfma_f32_16x16x32_bf16 %0, v[172:175], a[20:23], %0" : "+v"(a1[1][0]) :  : ); SB();
  asm volatile("ds_read_b64_tr_b16 v[192:193], %0 offset:36864" :  : "v"(aW[0][1][2]) : "memory"); SB();
  asm volatile("v_mfma_f32_16x16x32_bf16 %0, v[172:175], %1, %0" : "+v"(c1[0]) : "v"(ones) : ); SB();
  asm volatile("ds_read_b64_tr_b16 v[194:195], %0 offset:37632" :  : "v"(aW[0][1][2]) : "memory"); SB();
  asm volatile("s_waitcnt lgkmcnt(8)\n\tv_mfma_f32_16x16x32_bf16 %0, v[176:179], a[16:19], %0" : "+v"(a1[0][1]) :  : ); SB();
  asm volatile("s_barrier" :  :  : "memory"); SB();
  asm volatile("ds_read_b64_tr_b16 v[160:161], %0 offset:0" :  : "v"(aW[1][0][0]) : "memory"); SB();
  asm volatile("v_mfma_f32_16x16x32_bf16 %0, v[176:179], a[20:23], %0" : "+v"(a1[1][1]) :  : ); SB();
  asm volatile("ds_read_b64_tr_b16 v[162:163], %0 offset:768" :  : "v"(aW[1][0][0]) : "memory"); SB();
  asm volatile("buffer_load_dwordx4 v[216:219], %0, %1, %2 offen offset:1024 sc1" :  : "v"(vS), "s"(rWS), "s"(4096u) : "memory"); SB();
  asm volatile("v_mfma_f32_16x16x32_bf16 %0, v[176:179], %1, %0" : "+v"(c1[1]) : "v"(ones) : ); SB();
  asm volatile("ds_read_b64_tr_b16 v[164:165], %0 offset:0" :  : "v"(aW[1][0][1]) : "memory"); SB();
  asm volatile("s_waitcnt lgkmcnt(7)\n\tv_mfma_f32_16x16x32_bf16 %0, v[180:183], a[16:19], %0" : "+v"(a1[0][2]) :  : ); SB();
  asm volatile("ds_read_b64_tr_b16 v[166:167], %0 offset:768" :  : "v"(aW[1][0][1]) : "memory"); SB();
  asm volatile("v_mfma_f32_16x16x32_bf16 %0, v[180:183], a[20:23], %0" : "+v"(a1[1][2]) :  : ); SB();
  asm volatile("ds_read_b64_tr_b16 v[168:169], %0 offset:0" :  : "v"(aW[1][0][2]) : "memory"); SB();
  asm volatile("v_mfma_f32_16x16x32_bf16 %0, v[180:183], %1, %0" : "+v"(c1[2]) : "v"(ones) : ); SB();
  asm volatile("ds_read_b64_tr_b16 v[170:171], %0 offset:768" :  : "v"(aW[1][0][2]) : "memory"); SB();
  asm volatile("buffer_load_dwordx4 v[220:223], %0, %1, %2 offen offset:2048 sc1" :  : "v"(vS), "s"(rWS), "s"(4096u) : "memory"); SB();
  asm volatile("v_mfma_f32_16x16x32_bf16 %0, v[184:187], a[24:27], %0" : "+v"(a1[0][0]) :  : ); SB();
  asm volatile("ds_read_b64_tr_b16 v[172:173], %0 offset:0" :  : "v"(aW[1][1][0]) : "memory"); SB();
  asm volatile("v_mfma_f32_16x16x32_bf16 %0, v[184:187], a[28:31], %0" : "+v"(a1[1][0]) :  : ); SB();
  asm volatile("ds_read_b64_tr_b16 v[174:175], %0 offset:768" :  : "v"(aW[1][1][0]) : "memory"); SB();
  asm volatile("s_waitcnt lgkmcnt(8)\n\tv_mfma_f32_16x16x32_bf16 %0, v[188:191], a[24:27], %0" : "+v"(a1[0][1]) :  : ); SB();
  asm volatile("ds_read_b64_tr_b16 v[176:177], %0 offset:0" :  : "v"(aW[1][1][1]) : "memory"); SB();
  asm volatile("buffer_load_dwordx4 v[224:227], %0, %1, %2 offen offset:3072 sc1" :  : "v"(vS), "s"(rWS), "s"(4096u) : "memory"); SB();
  asm volatile("v_mfma_f32_16x16x32_bf16 %0, v[188:191], a[28:31], %0" : "+v"(a1[1][1]) :  : ); SB();
  asm volatile("ds_read_b64_tr_b16 v[178:179], %0 offset:768" :  : "v"(aW[1][1][1]) : "memory"); SB();
  asm volatile("v_mfma_f32_16x16x32_bf16 %0, v[192:195], a[24:27], %0" : "+v"(a1[0][2]) :  : ); SB();
  asm volatile("ds_read_b64_tr_b16 v[180:181], %0 offset:0" :  : "v"(aW[1][1][2]) : "memory"); SB();
  asm volatile("v_mfma_f32_16x16x32_bf16 %0, v[192:195], a[28:31], %0" : "+v"(a1[1][2]) :  : ); SB();
  asm volatile("ds_read_b64_tr_b16 v[182:183], %0 offset:768" :  : "v"(aW[1][1][2]) : "memory"); SB();
  asm volatile("buffer_store_dwordx4 %1, %0, %2, %3 offen offset:0 sc1\n\ts_nop 1" :  : "v"(vKV), "v"(kq[0]), "s"(rWS), "s"(0u) : "memory"); SB();
  ST(8);
  { unsigned lane = lane0, w = w0, rb = rb0, cb = cb0;
  asm volatile("" : "+v"(lane)); asm volatile("" : "+s"(w), "+s"(rb), "+s"(cb)); SB();

  unsigned zwa[2] = {lb + 98304 + 4096 + w * 8192 + (wn * 16 + l15) * 104 + g4 * 8, lb + 98304 + 4096 + w * 8192 + ((wn ^ 1) * 16 + l15) * 104 + g4 * 8};
  unsigned zra = lb + 98304 + 4096 + w * 8192 + (8 * g4 + q4) * 104 + 8 * p4, vZ_ = vZ;
  asm volatile("" : "+v"(zwa[0]), "+v"(zwa[1]), "+v"(zra), "+v"(vZ_));
  SB();
  asm volatile("buffer_store_dwordx4 %1, %0, %2, %3 offen offset:0 sc1\n\ts_nop 1" :  : "v"(vKV), "v"(kq[1]), "s"(rWS), "s"(w3k[0]) : "memory"); SB();
  asm volatile("buffer_store_dwordx4 %1, %0, %2, %3 offen offset:0 sc1\n\ts_nop 1" :  : "v"(vKV), "v"(kq[2]), "s"(rWS), "s"(w3k[1]) : "memory"); SB();
  asm volatile("ds_read_b128 a[176:179], %0 offset:32768" :  : "v"(aA[1]) : "memory"); SB();
  asm volatile("s_waitcnt vmcnt(18)" :  :  : "memory"); SB();
  asm volatile("ds_read_b128 a[164:167], %0 offset:34816" :  : "v"(aA[0]) : "memory"); SB();
  asm volatile("ds_read_b128 a[180:183], %0 offset:34816" :  : "v"(aA[1]) : "memory"); SB();
  asm volatile("s_waitcnt vmcnt(16)" :  :  : "memory"); SB();
  asm volatile("buffer_store_dwordx4 %1, %0, %2, %3 offen offset:0 sc1\n\ts_nop 1" :  : "v"(vWO), "v"(((u32x4){wt[0][0], wt[0][1], wt[1][0], wt[1][1]})), "s"(rWS), "s"(0u) : "memory"); SB();
  asm volatile("ds_read_b128 a[168:171], %0 offset:36864" :  : "v"(aA[0]) : "memory"); SB();
  asm volatile("ds_read_b128 a[184:187], %0 offset:36864" :  : "v"(aA[1]) : "memory"); SB();
  asm volatile("s_waitcnt vmcnt(15)" :  :  : "memory"); SB();
  asm volatile("ds_read_b128 a[172:175], %0 offset:38912" :  : "v"(aA[0]) : "memory"); SB();
  asm volatile("ds_write_b64 %0, %1 offset:0" :  : "v"(zwa[0]), "v"(((u32x2){zp[0][0][0], zp[0][0][1]})) : "memory"); SB();
  asm volatile("ds_write_b64 %0, %1 offset:32" :  : "v"(zwa[0]), "v"(((u32x2){zp[0][1][0], zp[0][1][1]})) : "memory"); SB();
  asm volatile("ds_write_b64 %0, %1 offset:64" :  : "v"(zwa[0]), "v"(((u32x2){zp[0][2][0], zp[0][2][1]})) : "memory"); SB();
  asm volatile("ds_read_b128 a[188:191], %0 offset:38912" :  : "v"(aA[1]) : "memory"); SB();
  asm volatile("s_waitcnt vmcnt(13)" :  :  : "memory"); SB();
  asm volatile("ds_read_b128 a[192:195], %0 offset:0" :  : "v"(aA[0]) : "memory"); SB();
  asm volatile("ds_read_b128 a[208:211], %0 offset:0" :  : "v"(aA[1]) : "memory"); SB();
  asm volatile("ds_write_b64 %0, %1 offset:0" :  : "v"(zwa[1]), "v"(((u32x2){zp[1][0][0], zp[1][0][1]})) : "memory"); SB();
  asm volatile("ds_write_b64 %0, %1 offset:32" :  : "v"(zwa[1]), "v"(((u32x2){zp[1][1][0], zp[1][1][1]})) : "memory"); SB();
  asm volatile("ds_write_b64 %0, %1 offset:64" :  : "v"(zwa[1]), "v"(((u32x2){zp[1][2][0], zp[1][2][1]})) : "memory"); SB();
  asm volatile("ds_read_b64_tr_b16 %0, %1 offset:0" : "=&v"(zt[0][0]) : "v"(zra) : "memory"); SB();
  asm volatile("ds_read_b64_tr_b16 %0, %1 offset:416" : "=&v"(zt[0][1]) : "v"(zra) : "memory"); SB();
  asm volatile("ds_read_b64_tr_b16 %0, %1 offset:32" : "=&v"(zt[1][0]) : "v"(zra) : "memory"); SB();
  asm volatile("ds_read_b64_tr_b16 %0, %1 offset:448" : "=&v"(zt[1][1]) : "v"(zra) : "memory"); SB();
  asm volatile("ds_read_b64_tr_b16 %0, %1 offset:64" : "=&v"(zt[2][0]) : "v"(zra) : "memory"); SB();
  asm volatile("ds_read_b64_tr_b16 %0, %1 offset:480" : "=&v"(zt[2][1]) : "v"(zra) : "memory"); SB();
  asm volatile("s_waitcnt lgkmcnt(0)" : "+v"(zt[0][0]), "+v"(zt[0][1]), "+v"(zt[1][0]), "+v"(zt[1][1]), "+v"(zt[2][0]), "+v"(zt[2][1]) :  : "memory"); SB();
  asm volatile("s_add_u32 m0, %0, 12288" :  : "s"(mW) : "memory", "scc"); SB();
  asm volatile("s_nop 0\n\tbuffer_load_dwordx4 %0, %1, %2 offen lds" :  : "v"(voB[0]), "s"(rW1), "s"(kw_[7]) : "memory"); SB();
  asm volatile("buffer_load_dwordx4 %0, %1, %2 offen offset:1024 lds" :  : "v"(voB[1]), "s"(rW1), "s"(kw_[7]) : "memory"); SB();
  asm volatile("buffer_load_dwordx4 %0, %1, %2 offen offset:2048 lds" :  : "v"(voB[2]), "s"(rW1), "s"(kw_[7]) : "memory"); SB();
  asm volatile("s_add_u32 m0, %0, 24576" :  : "s"(mW) : "memory", "scc"); SB();
  asm volatile("s_nop 0\n\tbuffer_load_dwordx4 %0, %1, %2 offen lds" :  : "v"(voB[0]), "s"(rW1), "s"(kw_[8]) : "memory"); SB();
  asm volatile("buffer_load_dwordx4 %0, %1, %2 offen offset:1024 lds" :  : "v"(voB[1]), "s"(rW1), "s"(kw_[8]) : "memory"); SB();
  asm volatile("buffer_load_dwordx4 %0, %1, %2 offen offset:2048 lds" :  : "v"(voB[2]), "s"(rW1), "s"(kw_[8]) : "memory"); SB();
  asm volatile("s_waitcnt vmcnt(10)\n\t.Lc_%=:\n\tv_cmpx_eq_u64 vcc, %1, v[198:199]\n\tv_cmpx_eq_u64 vcc, %1, v[202:203]\n\tv_cmpx_eq_u64 vcc, %1, v[206:207]\n\tv_cmpx_eq_u64 vcc, %1, v[210:211]\n\tv_cmpx_eq_u64 vcc, %1, v[214:215]\n\tv_cmpx_eq_u64 vcc, %1, v[218:219]\n\tv_cmpx_eq_u64 vcc, %1, v[222:223]\n\tv_cmpx_eq_u64 vcc, %1, v[226:227]\n\ts_cmp_eq_u64 vcc, -1\n\ts_mov_b64 exec, -1\n\ts_cbranch_scc0 .Lr_%=\n\t.Ld_%=:\n\t.subsection 1\n\t.Lr_%=:\n\tv_add_u32 %0, -1, %0\n\ts_nop 1\n\tv_readfirstlane_b32 vcc_hi, %0\n\ts_nop 3\n\ts_cmp_lt_i32 vcc_hi, 1\n\ts_cbranch_scc1 .Ld_%=\n\ts_sleep 2\n\ts_movk_i32 vcc_lo, 0x1000\n\tbuffer_load_dwordx4 v[196:199], %2, %3, 0 offen offset:0 sc1\n\tbuffer_load_dwordx4 v[200:203], %2, %3, 0 offen offset:1024 sc1\n\tbuffer_load_dwordx4 v[204:207], %2, %3, 0 offen offset:2048 sc1\n\tbuffer_load_dwordx4 v[208:211], %2, %3, 0 offen offset:3072 sc1\n\tbuffer_load_dwordx4 v[212:215], %2, %3, vcc_lo offen offset:0 sc1\n\tbuffer_load_dwordx4 v[216:219], %2, %3, vcc_lo offen offset:1024 sc1\n\tbuffer_load_dwordx4 v[220:223], %2, %3, vcc_lo offen offset:2048 sc1\n\tbuffer_load_dwordx4 v[224:227], %2, %3, vcc_lo offen offset:3072 sc1\n\ts_waitcnt vmcnt(0)\n\ts_branch .Lc_%=\n\t.subsection 0" : "+v"(tmo) : "s"(tg), "v"(vS), "s"(rWS) : "memory", "vcc", "scc"); SB();
  ST(9);
  {
    f32x2 s4[4], dm[4];
  asm volatile("v_pk_add_f32 %0, v[196:197], v[200:201]\n\tv_pk_add_f32 %1, v[204:205], v[208:209]\n\tv_pk_add_f32 %2, v[212:213], v[216:217]\n\tv_pk_add_f32 %3, v[220:221], v[224:225]" : "=&v"(s4[0]), "=&v"(s4[1]), "=&v"(s4[2]), "=&v"(s4[3]) :  : ); SB();
    const f32x2 sm = (s4[0] + s4[1]) + (s4[2] + s4[3]);
    const float mean = (sm[0] + dppf<0x128>(sm[0])) * (1.0f / 16.0f);
  asm volatile("v_sub_f32 %0, v196, %8\n\tv_sub_f32 %1, v200, %8\n\tv_sub_f32 %2, v204, %8\n\tv_sub_f32 %3, v208, %8\n\tv_sub_f32 %4, v212, %8\n\tv_sub_f32 %5, v216, %8\n\tv_sub_f32 %6, v220, %8\n\tv_sub_f32 %7, v224, %8" : "=&v"(dm[0][0]), "=&v"(dm[0][1]), "=&v"(dm[1][0]), "=&v"(dm[1][1]), "=&v"(dm[2][0]), "=&v"(dm[2][1]), "=&v"(dm[3][0]), "=&v"(dm[3][1]) : "v"(mean) : ); SB();
    const f32x2 dq = __builtin_elementwise_fma(dm[0], dm[0], dm[1] * dm[1]) + __builtin_elementwise_fma(dm[2], dm[2], dm[3] * dm[3]);
    float qs = __builtin_fmaf(dq[0] + dq[1], 48.0f, sm[1]);
    qs += dppf<0x128>(qs);
    float r = __builtin_amdgcn_rcpf(rbf(__builtin_amdgcn_sqrtf(rbf(rbf(qs * (1.0f / 768.0f)) + 1e-5f))));
    float mr = -rbf(mean) * r;
    asm volatile("" : "+v"(r), "+v"(mr)); SB();
  asm volatile("s_add_u32 m0, %0, 98304" :  : "s"(mA) : "memory", "scc"); SB();
  asm volatile("s_nop 0\n\tbuffer_load_dwordx4 %0, %1, %2 offen offset:3072 lds" :  : "v"(voS), "s"(rA), "s"(ao[3]) : "memory"); SB();
  asm volatile("s_add_u32 m0, %0, 102400" :  : "s"(mA) : "memory", "scc"); SB();
  asm volatile("s_nop 0\n\tbuffer_load_dwordx4 %0, %1, %2 offen lds" :  : "v"(voS), "s"(rA), "s"(ao[4]) : "memory"); SB();
    const f32x2 re[4] = {{dppf<0x150>(r), dppf<0x151>(r)}, {dppf<0x152>(r), dppf<0x153>(r)}, {dppf<0x154>(r), dppf<0x155>(r)}, {dppf<0x156>(r), dppf<0x157>(r)}};
    const f32x2 me[4] = {{dppf<0x150>(mr), dppf<0x151>(mr)}, {dppf<0x152>(mr), dppf<0x153>(mr)}, {dppf<0x154>(mr), dppf<0x155>(mr)}, {dppf<0x156>(mr), dppf<0x157>(mr)}};
    u32x4 zo[3];
#pragma unroll
    for (int e = 0; e < 4; ++e) zo[0][e] = pk2(__builtin_elementwise_fma(unpk(zt[0][e >> 1][e & 1]), re[e], me[e]));
    SB();
  asm volatile("buffer_store_dwordx4 %1, %0, %2, %3 offen offset:0 sc1\n\ts_nop 1" :  : "v"(vZ_), "v"(zo[0]), "s"(rWS), "s"(0u) : "memory"); SB();
#pragma unroll
    for (int e = 0; e < 4; ++e) zo[1][e] = pk2(__builtin_elementwise_fma(unpk(zt[1][e >> 1][e & 1]), re[e], me[e]));
    SB();
  asm volatile("buffer_store_dwordx4 %1, %0, %2, %3 offen offset:1024 sc1\n\ts_nop 1" :  : "v"(vZ_), "v"(zo[1]), "s"(rWS), "s"(0u) : "memory"); SB();
#pragma unroll
    for (int e = 0; e < 4; ++e) zo[2][e] = pk2(__builtin_elementwise_fma(unpk(zt[2][e >> 1][e & 1]), re[e], me[e]));
    SB();
  asm volatile("buffer_store_dwordx4 %1, %0, %2, %3 offen offset:2048 sc1\n\ts_nop 1" :  : "v"(vZ_), "v"(zo[2]), "s"(rWS), "s"(0u) : "memory"); SB();
  }
  ST(10);
  asm volatile("v_mfma_f32_16x16x32_bf16 %0, v[160:163], a[32:35], %0" : "+v"(a1[0][0]) :  : ); SB();
  asm volatile("ds_read_b64_tr_b16 v[184:185], %0 offset:12288" :  : "v"(aW[1][0][0]) : "memory"); SB();
  asm volatile("v_mfma_f32_16x16x32_bf16 %0, v[160:163], a[36:39], %0" : "+v"(a1[1][0]) :  : ); SB();
  asm volatile("ds_read_b64_tr_b16 v[186:187], %0 offset:13056" :  : "v"(aW[1][0][0]) : "memory"); SB();
  asm volatile("v_mfma_f32_16x16x32_bf16 %0, v[160:163], %1, %0" : "+v"(c1[0]) : "v"(ones) : ); SB();
  asm volatile("ds_read_b64_tr_b16 v[188:189], %0 offset:12288" :  : "v"(aW[1][0][1]) : "memory"); SB();
  asm volatile("v_mfma_f32_16x16x32_bf16 %0, v[164:167], a[32:35], %0" : "+v"(a1[0][1]) :  : ); SB();
  asm volatile("ds_read_b64_tr_b16 v[190:191], %0 offset:13056" :  : "v"(aW[1][0][1]) : "memory"); SB();
  asm volatile("v_mfma_f32_16x16x32_bf16 %0, v[164:167], a[36:39], %0" : "+v"(a1[1][1]) :  : ); SB();
  asm volatile("ds_read_b64_tr_b16 v[192:193], %0 offset:12288" :  : "v"(aW[1][0][2]) : "memory"); SB();
  asm volatile("v_mfma_f32_16x16x32_bf16 %0, v[164:167], %1, %0" : "+v"(c1[1]) : "v"(ones) : ); SB();
  asm volatile("ds_read_b64_tr_b16 v[194:195], %0 offset:13056" :  : "v"(aW[1][0][2]) : "memory"); SB();
  asm volatile("v_mfma_f32_16x16x32_bf16 %0, v[168:171], a[32:35], %0" : "+v"(a1[0][2]) :  : ); SB();
  ca = lb + 57344 + w * 192 + g4 * 16; asm volatile("" : "+v"(ca)); SB();
  asm volatile("v_mfma_f32_16x16x32_bf16 %0, v[168:171], a[36:39], %0" : "+v"(a1[1][2]) :  : ); SB();
  cr = lb + 57344 + (w ^ 2) * 192 + g4 * 16; asm volatile("" : "+v"(cr)); SB();
  asm volatile("v_mfma_f32_16x16x32_bf16 %0, v[168:171], %1, %0" : "+v"(c1[2]) : "v"(ones) : ); SB();
  asm volatile("buffer_load_dwordx4 %0, %1, %2 offen offset:1024 lds" :  : "v"(voS), "s"(rA), "s"(ao[5]) : "memory"); SB();
  asm volatile("v_mfma_f32_16x16x32_bf16 %0, v[172:175], a[40:43], %0" : "+v"(a1[0][0]) :  : ); SB();
  asm volatile("ds_read_b64_tr_b16 v[160:161], %0 offset:12288" :  : "v"(aW[1][1][0]) : "memory"); SB();
  asm volatile("s_waitcnt vmcnt(12)\n\ts_mov_b64 exec, 1\n\tbuffer_store_dwordx2 %0, off, %1, %2 sc1\n\ts_mov_b64 exec, -1" :  : "v"(((u32x2){tg0, tg1})), "s"(rWS), "s"(sFK) : "memory"); SB();
  asm volatile("v_mfma_f32_16x16x32_bf16 %0, v[172:175], a[44:47], %0" : "+v"(a1[1][0]) :  : ); SB();
  asm volatile("ds_read_b64_tr_b16 v[162:163], %0 offset:13056" :  : "v"(aW[1][1][0]) : "memory"); SB();
  asm volatile("v_lshlrev_b32 %0, 16, v245" : "=v"(dD[0]) :  : ); SB();
  asm volatile("v_mfma_f32_16x16x32_bf16 %0, v[176:179], a[40:43], %0" : "+v"(a1[0][1]) :  : ); SB();
  asm volatile("ds_read_b64_tr_b16 v[164:165], %0 offset:12288" :  : "v"(aW[1][1][1]) : "memory"); SB();
  asm volatile("v_lshlrev_b32 %0, 16, v246" : "=v"(dD[1]) :  : ); SB();
  asm volatile("v_mfma_f32_16x16x32_bf16 %0, v[176:179], a[44:47], %0" : "+v"(a1[1][1]) :  : ); SB();
  asm volatile("ds_read_b64_tr_b16 v[166:167], %0 offset:13056" :  : "v"(aW[1][1][1]) : "memory"); SB();
  asm volatile("v_lshlrev_b32 %0, 16, v247" : "=v"(dD[2]) :  : ); SB();
  asm volatile("v_mfma_f32_16x16x32_bf16 %0, v[180:183], a[40:43], %0" : "+v"(a1[0][2]) :  : ); SB();
  asm volatile("ds_read_b64_tr_b16 v[168:169], %0 offset:12288" :  : "v"(aW[1][1][2]) : "memory"); SB();
  asm volatile("v_lshlrev_b32 %0, 16, v248" : "=v"(bs) :  : ); SB();
  asm volatile("v_mfma_f32_16x16x32_bf16 %0, v[180:183], a[44:47], %0" : "+v"(a1[1][2]) :  : ); SB();
  asm volatile("ds_read_b64_tr_b16 v[170:171], %0 offset:13056" :  : "v"(aW[1][1][2]) : "memory"); SB();
  asm volatile("s_waitcnt lgkmcnt(6)\n\tv_mfma_f32_16x16x32_bf16 %0, v[184:187], a[48:51], %0" : "+v"(a1[0][0]) :  : ); SB();
  asm volatile("s_barrier" :  :  : "memory"); SB();
  asm volatile("ds_read_b64_tr_b16 v[172:173], %0 offset:24576" :  : "v"(aW[1][0][0]) : "memory"); SB();
  asm volatile("s_add_u32 m0, %0, 36864" :  : "s"(mW) : "memory", "scc"); SB();
  asm volatile("v_mfma_f32_16x16x32_bf16 %0, v[184:187], a[52:55], %0" : "+v"(a1[1][0]) :  : ); SB();
  asm volatile("ds_read_b64_tr_b16 v[174:175], %0 offset:25344" :  : "v"(aW[1][0][0]) : "memory"); SB();
  asm volatile("buffer_load_dwordx4 %0, %1, %2 offen lds" :  : "v"(voB[0]), "s"(rW1), "s"(kw_[9]) : "memory"); SB();
  asm volatile("v_mfma_f32_16x16x32_bf16 %0, v[184:187], %1, %0" : "+v"(c1[0]) : "v"(ones) : ); SB();
  asm volatile("ds_read_b64_tr_b16 v[176:177], %0 offset:24576" :  : "v"(aW[1][0][1]) : "memory"); SB();
  asm volatile("v_mov_b32_dpp v242, v231 wave_shr:1 row_mask:0xf bank_mask:0xf" :  :  : ); SB();
  asm volatile("v_mfma_f32_16x16x32_bf16 %0, v[188:191], a[48:51], %0" : "+v"(a1[0][1]) :  : ); SB();
  asm volatile("ds_read_b64_tr_b16 v[178:179], %0 offset:25344" :  : "v"(aW[1][0][1]) : "memory"); SB();
  asm volatile("v_alignbit_b32 %0, v231, v231, 16" : "=v"(im[0][0]) :  : ); SB();
  asm volatile("v_mfma_f32_16x16x32_bf16 %0, v[188:191], a[52:55], %0" : "+v"(a1[1][1]) :  : ); SB();
  asm volatile("ds_read_b64_tr_b16 v[180:181], %0 offset:24576" :  : "v"(aW[1][0][2]) : "memory"); SB();
  asm volatile("buffer_load_dwordx4 %0, %1, %2 offen offset:1024 lds" :  : "v"(voB[1]), "s"(rW1), "s"(kw_[9]) : "memory"); SB();
  asm volatile("v_mfma_f32_16x16x32_bf16 %0, v[188:191], %1, %0" : "+v"(c1[1]) : "v"(ones) : ); SB();
  asm volatile("ds_read_b64_tr_b16 v[182:183], %0 offset:25344" :  : "v"(aW[1][0][2]) : "memory"); SB();
  asm volatile("v_alignbit_b32 %0, v230, v230, 16" : "=v"(im[0][1]) :  : ); SB();
  asm volatile("v_mfma_f32_16x16x32_bf16 %0, v[192:195], a[48:51], %0" : "+v"(a1[0][2]) :  : ); SB();
  asm volatile("buffer_load_dwordx4 %0, %1, %2 offen offset:2048 lds" :  : "v"(voB[2]), "s"(rW1), "s"(kw_[9]) : "memory"); SB();
  asm volatile("v_alignbit_b32 %0, v229, v229, 16" : "=v"(im[0][2]) :  : ); SB();
  asm volatile("v_mfma_f32_16x16x32_bf16 %0, v[192:195], a[52:55], %0" : "+v"(a1[1][2]) :  : ); SB();
  asm volatile("v_alignbit_b32 %0, v228, v228, 16" : "=v"(im[0][3]) :  : ); SB();
  asm volatile("v_mfma_f32_16x16x32_bf16 %0, v[192:195], %1, %0" : "+v"(c1[2]) : "v"(ones) : ); SB();
  asm volatile("v_bfi_b32 %0, %1, v231, v230" : "=v"(im[1][0]) : "s"(0xFFFFu) : ); SB();
  asm volatile("s_waitcnt lgkmcnt(6)\n\tv_mfma_f32_16x16x32_bf16 %0, v[160:163], a[56:59], %0" : "+v"(a1[0][0]) :  : ); SB();
  asm volatile("ds_read_b64_tr_b16 v[184:185], %0 offset:24576" :  : "v"(aW[1][1][0]) : "memory"); SB();
  asm volatile("s_add_u32 m0, %0, 102400" :  : "s"(mA) : "memory", "scc"); SB();
  asm volatile("v_mfma_f32_16x16x32_bf16 %0, v[160:163], a[60:63], %0" : "+v"(a1[1][0]) :  : ); SB();
  asm volatile("ds_read_b64_tr_b16 v[186:187], %0 offset:25344" :  : "v"(aW[1][1][0]) : "memory"); SB();
  asm volatile("v_bfi_b32 %0, %1, v230, v229" : "=v"(im[1][1]) : "s"(0xFFFFu) : ); SB();
  asm volatile("v_mfma_f32_16x16x32_bf16 %0, v[164:167], a[56:59], %0" : "+v"(a1[0][1]) :  : ); SB();
  asm volatile("ds_read_b64_tr_b16 v[188:189], %0 offset:24576" :  : "v"(aW[1][1][1]) : "memory"); SB();
  asm volatile("v_bfi_b32 %0, %1, v229, v228" : "=v"(im[1][2]) : "s"(0xFFFFu) : ); SB();
  asm volatile("v_mfma_f32_16x16x32_bf16 %0, v[164:167], a[60:63], %0" : "+v"(a1[1][1]) :  : ); SB();
  asm volatile("ds_read_b64_tr_b16 v[190:191], %0 offset:25344" :  : "v"(aW[1][1][1]) : "memory"); SB();
  asm volatile("v_bfi_b32 %0, %1, v228, v242" : "=v"(im[1][3]) : "s"(0xFFFFu) : ); SB();
  asm volatile("v_mfma_f32_16x16x32_bf16 %0, v[168:171], a[56:59], %0" : "+v"(a1[0][2]) :  : ); SB();
  asm volatile("ds_read_b64_tr_b16 v[192:193], %0 offset:24576" :  : "v"(aW[1][1][2]) : "memory"); SB();
  asm volatile("buffer_load_dwordx4 %0, %1, %2 offen offset:2048 lds" :  : "v"(voS), "s"(rA), "s"(ao[6]) : "memory"); SB();
  asm volatile("v_mfma_f32_16x16x32_bf16 %0, v[168:171], a[60:63], %0" : "+v"(a1[1][2]) :  : ); SB();
  asm volatile("ds_read_b64_tr_b16 v[194:195], %0 offset:25344" :  : "v"(aW[1][1][2]) : "memory"); SB();
  asm volatile("s_waitcnt lgkmcnt(6)\n\tv_mfma_f32_16x16x32_bf16 %0, v[172:175], a[64:67], %0" : "+v"(a1[0][0]) :  : ); SB();
  asm volatile("ds_read_b64_tr_b16 v[160:161], %0 offset:36864" :  : "v"(aW[1][0][0]) : "memory"); SB();
  asm volatile("v_mov_b32_dpp v243, v235 wave_shr:1 row_mask:0xf bank_mask:0xf" :  :  : ); SB();
  asm volatile("v_mfma_f32_16x16x32_bf16 %0, v[172:175], a[68:71], %0" : "+v"(a1[1][0]) :  : ); SB();
  asm volatile("ds_read_b64_tr_b16 v[162:163], %0 offset:37632" :  : "v"(aW[1][0][0]) : "memory"); SB();
  asm volatile("v_alignbit_b32 %0, v235, v235, 16" : "=v"(im[2][0]) :  : ); SB();
  asm volatile("v_mfma_f32_16x16x32_bf16 %0, v[172:175], %1, %0" : "+v"(c1[0]) : "v"(ones) : ); SB();
  asm volatile("ds_read_b64_tr_b16 v[164:165], %0 offset:36864" :  : "v"(aW[1][0][1]) : "memory"); SB();
  asm volatile("v_alignbit_b32 %0, v234, v234, 16" : "=v"(im[2][1]) :  : ); SB();
  asm volatile("v_mfma_f32_16x16x32_bf16 %0, v[176:179], a[64:67], %0" : "+v"(a1[0][1]) :  : ); SB();
  asm volatile("ds_read_b64_tr_b16 v[166:167], %0 offset:37632" :  : "v"(aW[1][0][1]) : "memory"); SB();
  asm volatile("buffer_load_dwordx4 %0, %1, %2 offen offset:3072 lds" :  : "v"(voS), "s"(rA), "s"(ao[7]) : "memory"); SB();
  asm volatile("v_mfma_f32_16x16x32_bf16 %0, v[176:179], a[68:71], %0" : "+v"(a1[1][1]) :  : ); SB();
  asm volatile("ds_read_b64_tr_b16 v[168:169], %0 offset:36864" :  : "v"(aW[1][0][2]) : "memory"); SB();
  asm volatile("v_alignbit_b32 %0, v233, v233, 16" : "=v"(im[2][2]) :  : ); SB();
  asm volatile("v_mfma_f32_16x16x32_bf16 %0, v[176:179], %1, %0" : "+v"(c1[1]) : "v"(ones) : ); SB();
  asm volatile("ds_read_b64_tr_b16 v[170:171], %0 offset:37632" :  : "v"(aW[1][0][2]) : "memory"); SB();
  asm volatile("v_alignbit_b32 %0, v232, v232, 16" : "=v"(im[2][3]) :  : ); SB();
  asm volatile("v_mfma_f32_16x16x32_bf16 %0, v[180:183], a[64:67], %0" : "+v"(a1[0][2]) :  : ); SB();
  asm volatile("s_waitcnt vmcnt(11)" :  :  : "memory"); SB();
  asm volatile("ds_read_b128 a[196:199], %0 offset:2048" :  : "v"(aA[0]) : "memory"); SB();
  asm volatile("v_mfma_f32_16x16x32_bf16 %0, v[180:183], a[68:71], %0" : "+v"(a1[1][2]) :  : ); SB();
  asm volatile("ds_read_b128 a[212:215], %0 offset:2048" :  : "v"(aA[1]) : "memory"); SB();
  asm volatile("v_bfi_b32 %0, %1, v235, v234" : "=v"(im[3][0]) : "s"(0xFFFFu) : ); SB();
  asm volatile("v_mfma_f32_16x16x32_bf16 %0, v[180:183], %1, %0" : "+v"(c1[2]) : "v"(ones) : ); SB();
  asm volatile("v_xor_b32 %0, 384, %0" : "+v"(voS) :  : "memory"); SB();
  asm volatile("s_waitcnt lgkmcnt(8)\n\tv_mfma_f32_16x16x32_bf16 %0, v[184:187], a[72:75], %0" : "+v"(a1[0][0]) :  : ); SB();
  asm volatile("ds_read_b64_tr_b16 v[172:173], %0 offset:36864" :  : "v"(aW[1][1][0]) : "memory"); SB();
  asm volatile("s_add_u32 m0, %0, 131072" :  : "s"(mA) : "memory", "scc"); SB();
  asm volatile("v_mfma_f32_16x16x32_bf16 %0, v[184:187], a[76:79], %0" : "+v"(a1[1][0]) :  : ); SB();
  asm volatile("ds_read_b64_tr_b16 v[174:175], %0 offset:37632" :  : "v"(aW[1][1][0]) : "memory"); SB();
  asm volatile("buffer_load_dwordx4 %0, %1, %2 offen lds" :  : "v"(voS), "s"(rA), "s"(ao[0]) : "memory"); SB();
  asm volatile("v_mfma_f32_16x16x32_bf16 %0, v[188:191], a[72:75], %0" : "+v"(a1[0][1]) :  : ); SB();
  asm volatile("ds_read_b64_tr_b16 v[176:177], %0 offset:36864" :  : "v"(aW[1][1][1]) : "memory"); SB();
  asm volatile("buffer_load_dwordx4 %0, %1, %2 offen offset:1024 lds" :  : "v"(voS), "s"(rA), "s"(ao[1]) : "memory"); SB();
  asm volatile("v_mfma_f32_16x16x32_bf16 %0, v[188:191], a[76:79], %0" : "+v"(a1[1][1]) :  : ); SB();
  asm volatile("ds_read_b64_tr_b16 v[178:179], %0 offset:37632" :  : "v"(aW[1][1][1]) : "memory"); SB();
  asm volatile("v_bfi_b32 %0, %1, v234, v233" : "=v"(im[3][1]) : "s"(0xFFFFu) : ); SB();
  asm volatile("v_mfma_f32_16x16x32_bf16 %0, v[192:195], a[72:75], %0" : "+v"(a1[0][2]) :  : ); SB();
  asm volatile("ds_read_b64_tr_b16 v[180:181], %0 offset:36864" :  : "v"(aW[1][1][2]) : "memory"); SB();
  asm volatile("v_bfi_b32 %0, %1, v233, v232" : "=v"(im[3][2]) : "s"(0xFFFFu) : ); SB();
  asm volatile("v_mfma_f32_16x16x32_bf16 %0, v[192:195], a[76:79], %0" : "+v"(a1[1][2]) :  : ); SB();
  asm volatile("ds_read_b64_tr_b16 v[182:183], %0 offset:37632" :  : "v"(aW[1][1][2]) : "memory"); SB();
  asm volatile("s_waitcnt lgkmcnt(8)\n\tv_mfma_f32_16x16x32_bf16 %0, v[160:163], a[80:83], %0" : "+v"(a1[0][0]) :  : ); SB();
  unsigned vFZ = W_FZ + cb * 1024 + w * 256 + (lane & 31) * 8;
  unsigned vB = W_Z2 + (w * 1024 + (cb & 1) * 128 + cb * 6) * 1024 + l16;
  unsigned ha = lb + 59520 + 4608 + (2040 - 8 * (w * 64 + lane)) * 2;
  unsigned za = lb + 59520 + min(w, 2u) * 12928 + (lane < 32 ? lane * 16 : 4608 + ((lane >> 1) & 1) * 4160 + 4096 + (lane & 1) * 16);
  unsigned ew[2] = {lb + 32768 + w * 6144 + wn * 3072 + l16, lb + 32768 + (w ^ 1) * 6144 + wn * 3072 + l16};
  unsigned aa[4], bb;
  {
    const unsigned c = (l15 + 1) & 1;
    const unsigned o = lb + 59520 + 2 * 12928 - w * 128 - 192;
#pragma unroll
    for (int k = 0; k < 4; ++k) aa[k] = o + 4608 + c * 4160 + (2031 - l15 + 8 * g4 - c) * 2 - 512 - 1024 * k;
    bb = o + 512 + 32 * l15 - 32 + 16 * g4;
  }
  unsigned dd = lb + 0 + w * 8192 + g4 * 256 + ((l15 ^ ((g4 >> 1) << 3)) << 4);
  asm volatile("" : "+v"(aa[0]), "+v"(aa[1]), "+v"(aa[2]), "+v"(aa[3]), "+v"(bb), "+v"(dd), "+v"(vFZ), "+v"(vB), "+v"(ha), "+v"(za), "+v"(ew[0]), "+v"(ew[1]));
  SB();
  asm volatile("buffer_load_dwordx4 %0, %1, %2 offen offset:2048 lds" :  : "v"(voS), "s"(rA), "s"(ao[2]) : "memory"); SB();
  asm volatile("buffer_load_dwordx4 %0, %1, %2 offen offset:3072 lds" :  : "v"(voS), "s"(rA), "s"(ao[3]) : "memory"); SB();
  asm volatile("s_barrier" :  :  : "memory"); SB();
  asm volatile("ds_read_b64_tr_b16 v[184:185], %0 offset:0" :  : "v"(aW[0][0][0]) : "memory"); SB();
  asm volatile("s_add_u32 m0, %0, 135168" :  : "s"(mA) : "memory", "scc"); SB();
  asm volatile("v_mfma_f32_16x16x32_bf16 %0, v[160:163], a[84:87], %0" : "+v"(a1[1][0]) :  : ); SB();
  asm volatile("ds_read_b64_tr_b16 v[186:187], %0 offset:768" :  : "v"(aW[0][0][0]) : "memory"); SB();
  asm volatile("v_bfi_b32 %0, %1, v232, v243" : "=v"(im[3][3]) : "s"(0xFFFFu) : ); SB();
  asm volatile("v_mfma_f32_16x16x32_bf16 %0, v[160:163], %1, %0" : "+v"(c1[0]) : "v"(ones) : ); SB();
  asm volatile("ds_read_b64_tr_b16 v[188:189], %0 offset:0" :  : "v"(aW[0][0][1]) : "memory"); SB();
  asm volatile("v_mov_b32_dpp v244, v239 wave_shr:1 row_mask:0xf bank_mask:0xf" :  :  : ); SB();
  asm volatile("v_mfma_f32_16x16x32_bf16 %0, v[164:167], a[80:83], %0" : "+v"(a1[0][1]) :  : ); SB();
  asm volatile("ds_read_b64_tr_b16 v[190:191], %0 offset:768" :  : "v"(aW[0][0][1]) : "memory"); SB();
  asm volatile("v_alignbit_b32 %0, v239, v239, 16" : "=v"(im[4][0]) :  : ); SB();
  asm volatile("v_mfma_f32_16x16x32_bf16 %0, v[164:167], a[84:87], %0" : "+v"(a1[1][1]) :  : ); SB();
  asm volatile("ds_read_b64_tr_b16 v[192:193], %0 offset:0" :  : "v"(aW[0][0][2]) : "memory"); SB();
  asm volatile("v_alignbit_b32 %0, v238, v238, 16" : "=v"(im[4][1]) :  : ); SB();
  asm volatile("v_mfma_f32_16x16x32_bf16 %0, v[164:167], %1, %0" : "+v"(c1[1]) : "v"(ones) : ); SB();
  asm volatile("ds_read_b64_tr_b16 v[194:195], %0 offset:768" :  : "v"(aW[0][0][2]) : "memory"); SB();
  asm volatile("v_alignbit_b32 %0, v237, v237, 16" : "=v"(im[4][2]) :  : ); SB();
  asm volatile("v_mfma_f32_16x16x32_bf16 %0, v[168:171], a[80:83], %0" : "+v"(a1[0][2]) :  : ); SB();
  asm volatile("buffer_load_dwordx4 %0, %1, %2 offen lds" :  : "v"(voS), "s"(rA), "s"(ao[4]) : "memory"); SB();
  asm volatile("v_alignbit_b32 %0, v236, v236, 16" : "=v"(im[4][3]) :  : ); SB();
  asm volatile("v_mfma_f32_16x16x32_bf16 %0, v[168:171], a[84:87], %0" : "+v"(a1[1][2]) :  : ); SB();
  asm volatile("buffer_load_dwordx4 %0, %1, %2 offen offset:1024 lds" :  : "v"(voS), "s"(rA), "s"(ao[5]) : "memory"); SB();
  asm volatile("v_bfi_b32 %0, %1, v239, v238" : "=v"(im[5][0]) : "s"(0xFFFFu) : ); SB();
  asm volatile("v_mfma_f32_16x16x32_bf16 %0, v[168:171], %1, %0" : "+v"(c1[2]) : "v"(ones) : ); SB();
  asm volatile("v_bfi_b32 %0, %1, v238, v237" : "=v"(im[5][1]) : "s"(0xFFFFu) : ); SB();
  asm volatile("s_waitcnt lgkmcnt(5)\n\tv_mfma_f32_16x16x32_bf16 %0, v[172:175], a[88:91], %0" : "+v"(a1[0][0]) :  : ); SB();
  asm volatile("ds_read_b64_tr_b16 v[160:161], %0 offset:0" :  : "v"(aW[0][1][0]) : "memory"); SB();
  asm volatile("v_bfi_b32 %0, %1, v237, v236" : "=v"(im[5][2]) : "s"(0xFFFFu) : ); SB();
  asm volatile("v_mfma_f32_16x16x32_bf16 %0, v[172:175], a[92:95], %0" : "+v"(a1[1][0]) :  : ); SB();
  asm volatile("ds_read_b64_tr_b16 v[162:163], %0 offset:768" :  : "v"(aW[0][1][0]) : "memory"); SB();
  asm volatile("v_bfi_b32 %0, %1, v236, v244" : "=v"(im[5][3]) : "s"(0xFFFFu) : ); SB();
  asm volatile("v_mfma_f32_16x16x32_bf16 %0, v[176:179], a[88:91], %0" : "+v"(a1[0][1]) :  : ); SB();
  asm volatile("ds_read_b64_tr_b16 v[164:165], %0 offset:0" :  : "v"(aW[0][1][1]) : "memory"); SB();
  asm volatile("s_waitcnt vmcnt(13)\n\ts_mov_b64 exec, 1\n\tbuffer_store_dwordx2 %0, off, %1, %2 sc1\n\ts_mov_b64 exec, -1" :  : "v"(((u32x2){tg0, tg1})), "s"(rWS), "s"(sFZ) : "memory"); SB();
  asm volatile("v_mfma_f32_16x16x32_bf16 %0, v[176:179], a[92:95], %0" : "+v"(a1[1][1]) :  : ); SB();
  asm volatile("ds_read_b64_tr_b16 v[166:167], %0 offset:768" :  : "v"(aW[0][1][1]) : "memory"); SB();
  asm volatile("v_mov_b32 %0, 0" : "=v"(zero4[0]) :  : ); SB();
  asm volatile("v_mfma_f32_16x16x32_bf16 %0, v[180:183], a[88:91], %0" : "+v"(a1[0][2]) :  : ); SB();
  asm volatile("ds_read_b64_tr_b16 v[168:169], %0 offset:0" :  : "v"(aW[0][1][2]) : "memory"); SB();
  asm volatile("s_waitcnt vmcnt(13)" :  :  : "memory"); SB();
  asm volatile("v_mfma_f32_16x16x32_bf16 %0, v[180:183], a[92:95], %0" : "+v"(a1[1][2]) :  : ); SB();
  asm volatile("ds_read_b64_tr_b16 v[170:171], %0 offset:768" :  : "v"(aW[0][1][2]) : "memory"); SB();
  asm volatile("s_waitcnt lgkmcnt(6)\n\tv_mfma_f32_16x16x32_bf16 %0, v[184:187], a[96:99], %0" : "+v"(a1[0][0]) :  : ); SB();
  asm volatile("ds_read_b64_tr_b16 v[172:173], %0 offset:12288" :  : "v"(aW[0][0][0]) : "memory"); SB();
  asm volatile("v_mov_b32 %0, 0" : "=v"(zero4[1]) :  : ); SB();
  asm volatile("v_mfma_f32_16x16x32_bf16 %0, v[184:187], a[100:103], %0" : "+v"(a1[1][0]) :  : ); SB();
  asm volatile("ds_read_b64_tr_b16 v[174:175], %0 offset:13056" :  : "v"(aW[0][0][0]) : "memory"); SB();
  asm volatile("v_mov_b32 %0, 0" : "=v"(zero4[2]) :  : ); SB();
  asm volatile("v_mfma_f32_16x16x32_bf16 %0, v[184:187], %1, %0" : "+v"(c1[0]) : "v"(ones) : ); SB();
  asm volatile("ds_read_b64_tr_b16 v[176:177], %0 offset:12288" :  : "v"(aW[0][0][1]) : "memory"); SB();
  asm volatile("v_mov_b32 %0, 0" : "=v"(zero4[3]) :  : ); SB();
  asm volatile("v_mfma_f32_16x16x32_bf16 %0, v[188:191], a[96:99], %0" : "+v"(a1[0][1]) :  : ); SB();
  asm volatile("ds_read_b64_tr_b16 v[178:179], %0 offset:13056" :  : "v"(aW[0][0][1]) : "memory"); SB();
  asm volatile("v_mfma_f32_16x16x32_bf16 %0, v[188:191], a[100:103], %0" : "+v"(a1[1][1]) :  : ); SB();
  asm volatile("ds_read_b64_tr_b16 v[180:181], %0 offset:12288" :  : "v"(aW[0][0][2]) : "memory"); SB();
  asm volatile("v_mfma_f32_16x16x32_bf16 %0, v[188:191], %1, %0" : "+v"(c1[1]) : "v"(ones) : ); SB();
  asm volatile("ds_read_b64_tr_b16 v[182:183], %0 offset:13056" :  : "v"(aW[0][0][2]) : "memory"); SB();
  asm volatile("v_mfma_f32_16x16x32_bf16 %0, v[192:195], a[96:99], %0" : "+v"(a1[0][2]) :  : ); SB();
  asm volatile("ds_read_b128 a[200:203], %0 offset:4096" :  : "v"(aA[0]) : "memory"); SB();
  asm volatile("v_mfma_f32_16x16x32_bf16 %0, v[192:195], a[100:103], %0" : "+v"(a1[1][2]) :  : ); SB();
  asm volatile("ds_read_b128 a[216:219], %0 offset:4096" :  : "v"(aA[1]) : "memory"); SB();
  asm volatile("v_mfma_f32_16x16x32_bf16 %0, v[192:195], %1, %0" : "+v"(c1[2]) : "v"(ones) : ); SB();
  asm volatile("buffer_load_dwordx4 %0, %1, %2 offen offset:2048 lds" :  : "v"(voS), "s"(rA), "s"(ao[6]) : "memory"); SB();
  asm volatile("s_waitcnt lgkmcnt(8)\n\tv_mfma_f32_16x16x32_bf16 %0, v[160:163], a[104:107], %0" : "+v"(a1[0][0]) :  : ); SB();
  asm volatile("ds_read_b64_tr_b16 v[184:185], %0 offset:12288" :  : "v"(aW[0][1][0]) : "memory"); SB();
  asm volatile("v_mfma_f32_16x16x32_bf16 %0, v[160:163], a[108:111], %0" : "+v"(a1[1][0]) :  : ); SB();
  asm volatile("s_mov_b32 exec_lo, 0x10001\n\ts_mov_b32 exec_hi, 0x10001\n\tds_write_b128 %0, %1 offset:0\n\ts_mov_b64 exec, -1" :  : "v"(ca), "v"(c1[0]) : "memory"); SB();
  asm volatile("ds_read_b64_tr_b16 v[186:187], %0 offset:13056" :  : "v"(aW[0][1][0]) : "memory"); SB();
  asm volatile("v_mfma_f32_16x16x32_bf16 %0, v[164:167], a[104:107], %0" : "+v"(a1[0][1]) :  : ); SB();
  asm volatile("ds_read_b64_tr_b16 v[188:189], %0 offset:12288" :  : "v"(aW[0][1][1]) : "memory"); SB();
  asm volatile("buffer_load_dwordx4 %0, %1, %2 offen offset:3072 lds" :  : "v"(voS), "s"(rA), "s"(ao[7]) : "memory"); SB();
  asm volatile("v_mfma_f32_16x16x32_bf16 %0, v[164:167], a[108:111], %0" : "+v"(a1[1][1]) :  : ); SB();
  asm volatile("s_mov_b32 exec_lo, 0x10001\n\ts_mov_b32 exec_hi, 0x10001\n\tds_write_b128 %0, %1 offset:64\n\ts_mov_b64 exec, -1" :  : "v"(ca), "v"(c1[1]) : "memory"); SB();
  asm volatile("ds_read_b64_tr_b16 v[190:191], %0 offset:13056" :  : "v"(aW[0][1][1]) : "memory"); SB();
  asm volatile("v_mfma_f32_16x16x32_bf16 %0, v[168:171], a[104:107], %0" : "+v"(a1[0][2]) :  : ); SB();
  asm volatile("ds_read_b64_tr_b16 v[192:193], %0 offset:12288" :  : "v"(aW[0][1][2]) : "memory"); SB();
  asm volatile("v_mfma_f32_16x16x32_bf16 %0, v[168:171], a[108:111], %0" : "+v"(a1[1][2]) :  : ); SB();
  asm volatile("s_mov_b32 exec_lo, 0x10001\n\ts_mov_b32 exec_hi, 0x10001\n\tds_write_b128 %0, %1 offset:128\n\ts_mov_b64 exec, -1" :  : "v"(ca), "v"(c1[2]) : "memory"); SB();
  asm volatile("ds_read_b64_tr_b16 v[194:195], %0 offset:13056" :  : "v"(aW[0][1][2]) : "memory"); SB();
  asm volatile("v_xor_b32 %0, 128, %0" : "+v"(voS) :  : "memory"); SB();
  asm volatile("s_waitcnt lgkmcnt(6)\n\tv_mfma_f32_16x16x32_bf16 %0, v[172:175], a[112:115], %0" : "+v"(a1[0][0]) :  : ); SB();
  asm volatile("s_add_u32 m0, %0, 98304" :  : "s"(mA) : "memory", "scc"); SB();
  asm volatile("v_mfma_f32_16x16x32_bf16 %0, v[172:175], a[116:119], %0" : "+v"(a1[1][0]) :  : ); SB();
  asm volatile("buffer_load_dwordx4 %0, %1, %2 offen lds" :  : "v"(voS), "s"(rA), "s"(ao[0]) : "memory"); SB();
  asm volatile("v_mfma_f32_16x16x32_bf16 %0, v[172:175], %1, %0" : "+v"(c1[0]) : "v"(ones) : ); SB();
  asm volatile("buffer_load_dwordx4 %0, %1, %2 offen offset:1024 lds" :  : "v"(voS), "s"(rA), "s"(ao[1]) : "memory"); SB();
  asm volatile("s_waitcnt vmcnt(11)" :  :  : "memory"); SB();
  asm volatile("v_mfma_f32_16x16x32_bf16 %0, v[176:179], a[112:115], %0" : "+v"(a1[0][1]) :  : ); SB();
  asm volatile("s_waitcnt lgkmcnt(1)\n\ts_barrier" :  :  : "memory"); SB();
  asm volatile("ds_read_b64_tr_b16 v[160:161], %0 offset:24576" :  : "v"(aW[0][0][0]) : "memory"); SB();
  asm volatile("ds_read_b128 a[204:207], %0 offset:6144" :  : "v"(aA[0]) : "memory"); SB();
  asm volatile("v_mfma_f32_16x16x32_bf16 %0, v[176:179], a[116:119], %0" : "+v"(a1[1][1]) :  : ); SB();
  asm volatile("ds_read_b64_tr_b16 v[162:163], %0 offset:25344" :  : "v"(aW[0][0][0]) : "memory"); SB();
  asm volatile("ds_read_b128 a[220:223], %0 offset:6144" :  : "v"(aA[1]) : "memory"); SB();
  asm volatile("v_mfma_f32_16x16x32_bf16 %0, v[176:179], %1, %0" : "+v"(c1[1]) : "v"(ones) : ); SB();
  asm volatile("ds_read_b64_tr_b16 v[164:165], %0 offset:24576" :  : "v"(aW[0][0][1]) : "memory"); SB();
  asm volatile("s_waitcnt vmcnt(9)" :  :  : "memory"); SB();
  asm volatile("v_mfma_f32_16x16x32_bf16 %0, v[180:183], a[112:115], %0" : "+v"(a1[0][2]) :  : ); SB();
  asm volatile("ds_read_b64_tr_b16 v[166:167], %0 offset:25344" :  : "v"(aW[0][0][1]) : "memory"); SB();
  asm volatile("ds_read_b128 a[224:227], %0 offset:32768" :  : "v"(aA[0]) : "memory"); SB();
  asm volatile("v_mfma_f32_16x16x32_bf16 %0, v[180:183], a[116:119], %0" : "+v"(a1[1][2]) :  : ); SB();
  asm volatile("ds_read_b64_tr_b16 v[168:169], %0 offset:24576" :  : "v"(aW[0][0][2]) : "memory"); SB();
  asm volatile("ds_read_b128 a[240:243], %0 offset:32768" :  : "v"(aA[1]) : "memory"); SB();
  asm volatile("v_mfma_f32_16x16x32_bf16 %0, v[180:183], %1, %0" : "+v"(c1[2]) : "v"(ones) : ); SB();
  asm volatile("ds_read_b64_tr_b16 v[170:171], %0 offset:25344" :  : "v"(aW[0][0][2]) : "memory"); SB();
  asm volatile("buffer_load_dwordx4 %0, %1, %2 offen offset:2048 lds" :  : "v"(voS), "s"(rA), "s"(ao[2]) : "memory"); SB();
  asm volatile("v_mfma_f32_16x16x32_bf16 %0, v[184:187], a[120:123], %0" : "+v"(a1[0][0]) :  : ); SB();
  asm volatile("buffer_load_dwordx4 %0, %1, %2 offen offset:3072 lds" :  : "v"(voS), "s"(rA), "s"(ao[3]) : "memory"); SB();
  asm volatile("v_mfma_f32_16x16x32_bf16 %0, v[184:187], a[124:127], %0" : "+v"(a1[1][0]) :  : ); SB();
  asm volatile("ds_read_b128 %0, %1 offset:0" : "=&v"(co[0]) : "v"(cr) : "memory"); SB();
  asm volatile("v_mfma_f32_16x16x32_bf16 %0, v[184:187], %1, %0" : "+v"(c1[0]) : "v"(ones) : ); SB();
  asm volatile("s_waitcnt vmcnt(9)" :  :  : "memory"); SB();
  asm volatile("ds_read_b128 a[228:231], %0 offset:34816" :  : "v"(aA[0]) : "memory"); SB();
  asm volatile("v_mfma_f32_16x16x32_bf16 %0, v[188:191], a[120:123], %0" : "+v"(a1[0][1]) :  : ); SB();
  asm volatile("ds_read_b64_tr_b16 v[172:173], %0 offset:24576" :  : "v"(aW[0][1][0]) : "memory"); SB();
  asm volatile("ds_read_b128 a[244:247], %0 offset:34816" :  : "v"(aA[1]) : "memory"); SB();
  asm volatile("v_mfma_f32_16x16x32_bf16 %0, v[188:191], a[124:127], %0" : "+v"(a1[1][1]) :  : ); SB();
  asm volatile("ds_read_b64_tr_b16 v[174:175], %0 offset:25344" :  : "v"(aW[0][1][0]) : "memory"); SB();
  asm volatile("s_add_u32 m0, %0, 102400" :  : "s"(mA) : "memory", "scc"); SB();
  asm volatile("v_mfma_f32_16x16x32_bf16 %0, v[188:191], %1, %0" : "+v"(c1[1]) : "v"(ones) : ); SB();
  asm volatile("ds_read_b64_tr_b16 v[176:177], %0 offset:24576" :  : "v"(aW[0][1][1]) : "memory"); SB();
  asm volatile("buffer_load_dwordx4 %0, %1, %2 offen lds" :  : "v"(voS), "s"(rA), "s"(ao[4]) : "memory"); SB();
  asm volatile("s_waitcnt lgkmcnt(8)\n\tv_mfma_f32_16x16x32_bf16 %0, v[192:195], a[120:123], %0" : "+v"(a1[0][2]) :  : ); SB();
  asm volatile("ds_read_b64_tr_b16 v[178:179], %0 offset:25344" :  : "v"(aW[0][1][1]) : "memory"); SB();
  asm volatile("buffer_load_dwordx4 %0, %1, %2 offen offset:1024 lds" :  : "v"(voS), "s"(rA), "s"(ao[5]) : "memory"); SB();
  asm volatile("v_mfma_f32_16x16x32_bf16 %0, v[192:195], a[124:127], %0" : "+v"(a1[1][2]) :  : ); SB();
  asm volatile("ds_read_b64_tr_b16 v[180:181], %0 offset:24576" :  : "v"(aW[0][1][2]) : "memory"); SB();
  asm volatile("v_mfma_f32_16x16x32_bf16 %0, v[192:195], %1, %0" : "+v"(c1[2]) : "v"(ones) : ); SB();
  asm volatile("ds_read_b64_tr_b16 v[182:183], %0 offset:25344" :  : "v"(aW[0][1][2]) : "memory"); SB();
  asm volatile("s_waitcnt vmcnt(9)" :  :  : "memory"); SB();
  asm volatile("v_mfma_f32_16x16x32_bf16 %0, v[160:163], a[128:131], %0" : "+v"(a1[0][0]) :  : ); SB();
  asm volatile("ds_read_b128 a[232:235], %0 offset:36864" :  : "v"(aA[0]) : "memory"); SB();
  asm volatile("v_mfma_f32_16x16x32_bf16 %0, v[160:163], a[132:135], %0" : "+v"(a1[1][0]) :  : ); SB();
  asm volatile("ds_read_b128 a[248:251], %0 offset:36864" :  : "v"(aA[1]) : "memory"); SB();
  asm volatile("v_mfma_f32_16x16x32_bf16 %0, v[160:163], %1, %0" : "+v"(c1[0]) : "v"(ones) : ); SB();
  asm volatile("buffer_load_dwordx4 %0, %1, %2 offen offset:2048 lds" :  : "v"(voS), "s"(rA), "s"(ao[6]) : "memory"); SB();
  asm volatile("v_mfma_f32_16x16x32_bf16 %0, v[164:167], a[128:131], %0" : "+v"(a1[0][1]) :  : ); SB();
  asm volatile("ds_read_b64_tr_b16 v[184:185], %0 offset:36864" :  : "v"(aW[0][0][0]) : "memory"); SB();
  asm volatile("buffer_load_dwordx2 %0, %1, %2, %3 offen sc1" : "=&v"(fk) : "v"(vFZ), "s"(rWS), "s"(8192u) : "memory"); SB();
  asm volatile("v_mfma_f32_16x16x32_bf16 %0, v[164:167], a[132:135], %0" : "+v"(a1[1][1]) :  : ); SB();
  asm volatile("ds_read_b64_tr_b16 v[186:187], %0 offset:37632" :  : "v"(aW[0][0][0]) : "memory"); SB();
  asm volatile("v_mfma_f32_16x16x32_bf16 %0, v[164:167], %1, %0" : "+v"(c1[1]) : "v"(ones) : ); SB();
  asm volatile("ds_read_b64_tr_b16 v[188:189], %0 offset:36864" :  : "v"(aW[0][0][1]) : "memory"); SB();
  asm volatile("s_waitcnt lgkmcnt(8)\n\tv_mfma_f32_16x16x32_bf16 %0, v[168:171], a[128:131], %0" : "+v"(a1[0][2]) :  : ); SB();
  asm volatile("ds_read_b64_tr_b16 v[190:191], %0 offset:37632" :  : "v"(aW[0][0][1]) : "memory"); SB();
  asm volatile("buffer_load_dwordx4 %0, %1, %2 offen offset:3072 lds" :  : "v"(voS), "s"(rA), "s"(ao[7]) : "memory"); SB();
  asm volatile("v_mfma_f32_16x16x32_bf16 %0, v[168:171], a[132:135], %0" : "+v"(a1[1][2]) :  : ); SB();
  asm volatile("ds_read_b64_tr_b16 v[192:193], %0 offset:36864" :  : "v"(aW[0][0][2]) : "memory"); SB();
  asm volatile("v_xor_b32 %0, 896, %0" : "+v"(voS) :  : "memory"); SB();
  asm volatile("v_mfma_f32_16x16x32_bf16 %0, v[168:171], %1, %0" : "+v"(c1[2]) : "v"(ones) : ); SB();
  asm volatile("ds_read_b64_tr_b16 v[194:195], %0 offset:37632" :  : "v"(aW[0][0][2]) : "memory"); SB();
  asm volatile("v_mfma_f32_16x16x32_bf16 %0, v[172:175], a[136:139], %0" : "+v"(a1[0][0]) :  : ); SB();
  asm volatile("ds_read_b128 %0, %1 offset:64" : "=&v"(co[1]) : "v"(cr) : "memory"); SB();
  asm volatile("v_mfma_f32_16x16x32_bf16 %0, v[172:175], a[140:143], %0" : "+v"(a1[1][0]) :  : ); SB();
  asm volatile("s_add_u32 m0, %0, 131072" :  : "s"(mA) : "memory", "scc"); SB();
  asm volatile("ds_read_b128 %0, %1 offset:128" : "=&v"(co[2]) : "v"(cr) : "memory"); SB();
  asm volatile("v_mfma_f32_16x16x32_bf16 %0, v[172:175], %1, %0" : "+v"(c1[0]) : "v"(ones) : ); SB();
  asm volatile("ds_write_b128 %0, %1 offset:0" :  : "v"(ha), "v"(im[0]) : "memory"); SB();
  asm volatile("ds_write_b128 %0, %1 offset:4160" :  : "v"(ha), "v"(im[1]) : "memory"); SB();
  asm volatile("buffer_load_dwordx4 %0, %1, %2 offen lds" :  : "v"(voS), "s"(rA), "s"(ao[0]) : "memory"); SB();
  asm volatile("s_waitcnt lgkmcnt(6)\n\tv_mfma_f32_16x16x32_bf16 %0, v[176:179], a[136:139], %0" : "+v"(a1[0][1]) :  : ); SB();
  asm volatile("ds_write_b128 %0, %1 offset:12928" :  : "v"(ha), "v"(im[2]) : "memory"); SB();
  asm volatile("ds_write_b128 %0, %1 offset:17088" :  : "v"(ha), "v"(im[3]) : "memory"); SB();
  asm volatile("ds_read_b64_tr_b16 v[160:161], %0 offset:36864" :  : "v"(aW[0][1][0]) : "memory"); SB();
  asm volatile("s_waitcnt vmcnt(10)" :  :  : "memory"); SB();
  asm volatile("v_mfma_f32_16x16x32_bf16 %0, v[176:179], a[140:143], %0" : "+v"(a1[1][1]) :  : ); SB();
  asm volatile("ds_read_b64_tr_b16 v[162:163], %0 offset:37632" :  : "v"(aW[0][1][0]) : "memory"); SB();
  asm volatile("ds_read_b128 a[236:239], %0 offset:38912" :  : "v"(aA[0]) : "memory"); SB();
  asm volatile("v_mfma_f32_16x16x32_bf16 %0, v[176:179], %1, %0" : "+v"(c1[1]) : "v"(ones) : ); SB();
  asm volatile("ds_write_b128 %0, %1 offset:25856" :  : "v"(ha), "v"(im[4]) : "memory"); SB();
  asm volatile("ds_read_b64_tr_b16 v[164:165], %0 offset:36864" :  : "v"(aW[0][1][1]) : "memory"); SB();
  asm volatile("ds_read_b128 a[252:255], %0 offset:38912" :  : "v"(aA[1]) : "memory"); SB();
  asm volatile("v_mfma_f32_16x16x32_bf16 %0, v[180:183], a[136:139], %0" : "+v"(a1[0][2]) :  : ); SB();
  asm volatile("ds_read_b64_tr_b16 v[166:167], %0 offset:37632" :  : "v"(aW[0][1][1]) : "memory"); SB();
  asm volatile("buffer_load_dwordx4 %0, %1, %2 offen offset:1024 lds" :  : "v"(voS), "s"(rA), "s"(ao[1]) : "memory"); SB();
  asm volatile("v_mfma_f32_16x16x32_bf16 %0, v[180:183], a[140:143], %0" : "+v"(a1[1][2]) :  : ); SB();
  asm volatile("ds_read_b64_tr_b16 v[168:169], %0 offset:36864" :  : "v"(aW[0][1][2]) : "memory"); SB();
  asm volatile("buffer_load_dwordx4 %0, %1, %2 offen offset:2048 lds" :  : "v"(voS), "s"(rA), "s"(ao[2]) : "memory"); SB();
  asm volatile("v_mfma_f32_16x16x32_bf16 %0, v[180:183], %1, %0" : "+v"(c1[2]) : "v"(ones) : ); SB();
  asm volatile("ds_read_b64_tr_b16 v[170:171], %0 offset:37632" :  : "v"(aW[0][1][2]) : "memory"); SB();
  asm volatile("v_mfma_f32_16x16x32_bf16 %0, v[184:187], a[144:147], %0" : "+v"(a1[0][0]) :  : ); SB();
  asm volatile("v_mfma_f32_16x16x32_bf16 %0, v[184:187], a[148:151], %0" : "+v"(a1[1][0]) :  : ); SB();
  asm volatile("ds_write_b128 %0, %1 offset:30016" :  : "v"(ha), "v"(im[5]) : "memory"); SB();
  asm volatile("buffer_load_dwordx4 %0, %1, %2 offen offset:3072 lds" :  : "v"(voS), "s"(rA), "s"(ao[3]) : "memory"); SB();
  asm volatile("v_mfma_f32_16x16x32_bf16 %0, v[184:187], %1, %0" : "+v"(c1[0]) : "v"(ones) : ); SB();
  asm volatile("v_mfma_f32_16x16x32_bf16 %0, v[188:191], a[144:147], %0" : "+v"(a1[0][1]) :  : ); SB();
  asm volatile("s_add_u32 m0, %0, 135168" :  : "s"(mA) : "memory", "scc"); SB();
  asm volatile("v_mfma_f32_16x16x32_bf16 %0, v[188:191], a[148:151], %0" : "+v"(a1[1][1]) :  : ); SB();
  asm volatile("buffer_load_dwordx4 %0, %1, %2 offen lds" :  : "v"(voS), "s"(rA), "s"(ao[4]) : "memory"); SB();
  asm volatile("s_waitcnt vmcnt(12)" :  :  : "memory"); SB();
  asm volatile("v_mfma_f32_16x16x32_bf16 %0, v[188:191], %1, %0" : "+v"(c1[1]) : "v"(ones) : ); SB();
  asm volatile("ds_read_b128 a[0:3], %0 offset:0" :  : "v"(aA[0]) : "memory"); SB();
  asm volatile("s_waitcnt lgkmcnt(3)\n\tv_mfma_f32_16x16x32_bf16 %0, v[192:195], a[144:147], %0" : "+v"(a1[0][2]) :  : ); SB();
  asm volatile("ds_write_b128 %0, %1 offset:0" :  : "v"(za), "v"(zero4) : "memory"); SB();
  asm volatile("ds_read_b128 a[16:19], %0 offset:0" :  : "v"(aA[1]) : "memory"); SB();
  asm volatile("v_mfma_f32_16x16x32_bf16 %0, v[192:195], a[148:151], %0" : "+v"(a1[1][2]) :  : ); SB();
  asm volatile("buffer_load_dwordx4 %0, %1, %2 offen offset:1024 lds" :  : "v"(voS), "s"(rA), "s"(ao[5]) : "memory"); SB();
  asm volatile("v_mfma_f32_16x16x32_bf16 %0, v[192:195], %1, %0" : "+v"(c1[2]) : "v"(ones) : ); SB();
  asm volatile("v_mfma_f32_16x16x32_bf16 %0, v[160:163], a[152:155], %0" : "+v"(a1[0][0]) :  : ); SB();
  asm volatile("buffer_load_dwordx4 %0, %1, %2 offen offset:2048 lds" :  : "v"(voS), "s"(rA), "s"(ao[6]) : "memory"); SB();
  asm volatile("v_mfma_f32_16x16x32_bf16 %0, v[160:163], a[156:159], %0" : "+v"(a1[1][0]) :  : ); SB();
  asm volatile("v_mfma_f32_16x16x32_bf16 %0, v[160:163], %1, %0" : "+v"(c1[0]) : "v"(ones) : ); SB();
  asm volatile("v_mfma_f32_16x16x32_bf16 %0, v[164:167], a[152:155], %0" : "+v"(a1[0][1]) :  : ); SB();
  asm volatile("buffer_load_dwordx4 %0, %1, %2 offen offset:3072 lds" :  : "v"(voS), "s"(rA), "s"(ao[7]) : "memory"); SB();
  asm volatile("v_mfma_f32_16x16x32_bf16 %0, v[164:167], a[156:159], %0" : "+v"(a1[1][1]) :  : ); SB();
  asm volatile("v_mfma_f32_16x16x32_bf16 %0, v[164:167], %1, %0" : "+v"(c1[1]) : "v"(ones) : ); SB();
  asm volatile("buffer_load_dwordx2 %0, %1, %2, %3 offen sc1" : "=&v"(fz) : "v"(vFZ), "s"(rWS), "s"(0u) : "memory"); SB();
  asm volatile("s_waitcnt lgkmcnt(2)\n\tv_mfma_f32_16x16x32_bf16 %0, v[168:171], a[152:155], %0" : "+v"(a1[0][2]) :  : ); SB();
  asm volatile("s_waitcnt lgkmcnt(0)" :  :  : "memory"); SB();
  asm volatile("s_waitcnt vmcnt(14)" :  :  : "memory"); SB();
  asm volatile("v_mfma_f32_16x16x32_bf16 %0, v[168:171], a[156:159], %0" : "+v"(a1[1][2]) :  : ); SB();
  asm volatile("ds_read_b128 a[4:7], %0 offset:2048" :  : "v"(aA[0]) : "memory"); SB();
  asm volatile("v_mfma_f32_16x16x32_bf16 %0, v[168:171], %1, %0" : "+v"(c1[2]) : "v"(ones) : ); SB();
  asm volatile("ds_read_b128 a[20:23], %0 offset:2048" :  : "v"(aA[1]) : "memory"); SB();
  ST(11);
  f32x4 ac[8];
  u32x2 cA[2][8][2];
  asm volatile("v_xor_b32 %0, 128, %0" : "+v"(voS) :  : "memory"); SB();
  asm volatile("s_add_u32 m0, %0, 98304" :  : "s"(mA) : "memory", "scc"); SB();
  asm volatile("s_nop 0\n\tbuffer_load_dwordx4 %0, %1, %2 offen lds" :  : "v"(voS), "s"(rA), "s"(ao[0]) : "memory"); SB();
  asm volatile("buffer_load_dwordx4 %0, %1, %2 offen offset:1024 lds" :  : "v"(voS), "s"(rA), "s"(ao[1]) : "memory"); SB();
  asm volatile("s_waitcnt vmcnt(14)" :  :  : "memory"); SB();
  asm volatile("ds_read_b128 a[8:11], %0 offset:4096" :  : "v"(aA[0]) : "memory"); SB();
  asm volatile("ds_read_b128 a[24:27], %0 offset:4096" :  : "v"(aA[1]) : "memory"); SB();
  asm volatile("s_waitcnt lgkmcnt(2)" :  :  : "memory"); SB();
  asm volatile("buffer_load_dwordx4 %0, %1, %2 offen offset:2048 lds" :  : "v"(voS), "s"(rA), "s"(ao[2]) : "memory"); SB();
  asm volatile("s_waitcnt vmcnt(13)\n\t.Lc_%=:\n\tv_cmp_ne_u64 vcc, %2, %1\n\ts_cbranch_vccnz .Lr_%=\n\t.Ld_%=:\n\t.subsection 1\n\t.Lr_%=:\n\tv_add_u32 %0, -1, %0\n\ts_nop 1\n\tv_readfirstlane_b32 vcc_hi, %0\n\ts_nop 3\n\ts_cmp_lt_i32 vcc_hi, 1\n\ts_cbranch_scc1 .Ld_%=\n\ts_sleep 2\n\ts_mov_b32 vcc_lo, 8192\n\tbuffer_load_dwordx2 %1, %3, %4, vcc_lo offen sc1\n\ts_waitcnt vmcnt(0)\n\ts_branch .Lc_%=\n\t.subsection 0" : "+v"(tmo), "+v"(fk) : "s"(tg), "v"(vFZ), "s"(rWS) : "memory", "vcc", "scc"); SB();
  asm volatile("s_add_u32 m0, %0, 85888" :  : "s"(mU) : "memory", "scc"); SB();
  asm volatile("s_nop 0\n\tbuffer_load_dwordx4 %0, %1, %2 offen sc1 lds" :  : "v"(vF), "s"(rWS), "s"(uso) : "memory"); SB();
  asm volatile("s_add_u32 m0, %0, 98304" :  : "s"(mA) : "memory", "scc"); SB();
  asm volatile("s_nop 0\n\tbuffer_load_dwordx4 %0, %1, %2 offen offset:3072 lds" :  : "v"(voS), "s"(rA), "s"(ao[3]) : "memory"); SB();
  asm volatile("s_waitcnt vmcnt(14)" :  :  : "memory"); SB();
  asm volatile("ds_read_b128 a[12:15], %0 offset:6144" :  : "v"(aA[0]) : "memory"); SB();
  asm volatile("ds_read_b128 a[28:31], %0 offset:6144" :  : "v"(aA[1]) : "memory"); SB();
  asm volatile("s_waitcnt lgkmcnt(2)" :  :  : "memory"); SB();
  asm volatile("s_add_u32 m0, %0, 102400" :  : "s"(mA) : "memory", "scc"); SB();
  asm volatile("s_nop 0\n\tbuffer_load_dwordx4 %0, %1, %2 offen lds" :  : "v"(voS), "s"(rA), "s"(ao[4]) : "memory"); SB();
  asm volatile("buffer_load_dwordx4 %0, %1, %2 offen offset:1024 lds" :  : "v"(voS), "s"(rA), "s"(ao[5]) : "memory"); SB();
  asm volatile("s_waitcnt lgkmcnt(0)" :  :  : "memory"); SB();
  asm volatile("s_waitcnt vmcnt(14)" :  :  : "memory"); SB();
  asm volatile("ds_read_b128 a[32:35], %0 offset:32768" :  : "v"(aA[0]) : "memory"); SB();
  asm volatile("ds_read_b128 a[48:51], %0 offset:32768" :  : "v"(aA[1]) : "memory"); SB();
  asm volatile("buffer_load_dwordx4 %0, %1, %2 offen offset:2048 lds" :  : "v"(voS), "s"(rA), "s"(ao[6]) : "memory"); SB();
  asm volatile("buffer_load_dwordx4 %0, %1, %2 offen offset:3072 lds" :  : "v"(voS), "s"(rA), "s"(ao[7]) : "memory"); SB();
  asm volatile("s_waitcnt vmcnt(14)" :  :  : "memory"); SB();
  asm volatile("ds_read_b128 a[36:39], %0 offset:34816" :  : "v"(aA[0]) : "memory"); SB();
  asm volatile("ds_read_b128 a[52:55], %0 offset:34816" :  : "v"(aA[1]) : "memory"); SB();
  asm volatile("s_waitcnt lgkmcnt(2)" :  :  : "memory"); SB();
  asm volatile("v_xor_b32 %0, 384, %0" : "+v"(voS) :  : "memory"); SB();
  asm volatile("s_add_u32 m0, %0, 131072" :  : "s"(mA) : "memory", "scc"); SB();
  asm volatile("s_nop 0\n\tbuffer_load_dwordx4 %0, %1, %2 offen lds" :  : "v"(voS), "s"(rA), "s"(ao[0]) : "memory"); SB();
  asm volatile("buffer_load_dwordx4 %0, %1, %2 offen offset:1024 lds" :  : "v"(voS), "s"(rA), "s"(ao[1]) : "memory"); SB();
  asm volatile("s_waitcnt vmcnt(14)" :  :  : "memory"); SB();
  asm volatile("ds_read_b128 a[40:43], %0 offset:36864" :  : "v"(aA[0]) : "memory"); SB();
  asm volatile("ds_read_b128 a[56:59], %0 offset:36864" :  : "v"(aA[1]) : "memory"); SB();
  asm volatile("s_waitcnt lgkmcnt(2)" :  :  : "memory"); SB();
  asm volatile("buffer_load_dwordx4 %0, %1, %2 offen offset:2048 lds" :  : "v"(voS), "s"(rA), "s"(ao[2]) : "memory"); SB();
  asm volatile("buffer_load_dwordx4 %0, %1, %2 offen offset:3072 lds" :  : "v"(voS), "s"(rA), "s"(ao[3]) : "memory"); SB();
  asm volatile("s_waitcnt vmcnt(14)" :  :  : "memory"); SB();
  asm volatile("ds_read_b128 a[44:47], %0 offset:38912" :  : "v"(aA[0]) : "memory"); SB();
  asm volatile("ds_read_b128 a[60:63], %0 offset:38912" :  : "v"(aA[1]) : "memory"); SB();
  asm volatile("s_waitcnt vmcnt(13)\n\t.Lc_%=:\n\tv_cmp_ne_u64 vcc, %2, %1\n\ts_cbranch_vccnz .Lr_%=\n\t.Ld_%=:\n\t.subsection 1\n\t.Lr_%=:\n\tv_add_u32 %0, -1, %0\n\ts_nop 1\n\tv_readfirstlane_b32 vcc_hi, %0\n\ts_nop 3\n\ts_cmp_lt_i32 vcc_hi, 1\n\ts_cbranch_scc1 .Ld_%=\n\ts_sleep 2\n\ts_mov_b32 vcc_lo, 0\n\tbuffer_load_dwordx2 %1, %3, %4, vcc_lo offen sc1\n\ts_waitcnt vmcnt(0)\n\ts_branch .Lc_%=\n\t.subsection 0" : "+v"(tmo), "+v"(fz) : "s"(tg), "v"(vFZ), "s"(rWS) : "memory", "vcc", "scc"); SB();
  asm volatile("buffer_load_dwordx4 v[160:163], %0, %1, %2 offen offset:0 sc1" :  : "v"(vB), "s"(rWS), "s"(0u) : "memory"); SB();
  asm volatile("s_waitcnt lgkmcnt(2)" :  :  : "memory"); SB();
  asm volatile("buffer_load_dwordx4 v[164:167], %0, %1, %2 offen offset:1024 sc1" :  : "v"(vB), "s"(rWS), "s"(0u) : "memory"); SB();
  asm volatile("s_waitcnt vmcnt(13)" :  :  : "memory"); SB();
  asm volatile("ds_read_b128 a[64:67], %0 offset:0" :  : "v"(aA[0]) : "memory"); SB();
  asm volatile("ds_read_b128 a[80:83], %0 offset:0" :  : "v"(aA[1]) : "memory"); SB();
  asm volatile("buffer_load_dwordx4 v[168:171], %0, %1, %2 offen offset:0 sc1" :  : "v"(vB), "s"(rWS), "s"(65536u) : "memory"); SB();
  asm volatile("s_waitcnt vmcnt(12) lgkmcnt(0)\n\ts_barrier" :  :  : "memory"); SB();
  ST(14);
  asm volatile("ds_read2_b32 %0, %1 offset0:176 offset1:177" : "=&v"(cA[0][0][0]) : "v"(aa[0]) : "memory"); SB();
  asm volatile("ds_read2_b32 %0, %1 offset0:178 offset1:179" : "=&v"(cA[0][0][1]) : "v"(aa[0]) : "memory"); SB();
  asm volatile("ds_read_b128 a[124:127], %0 offset:3776" :  : "v"(bb) : "memory"); SB();
  asm volatile("ds_read_b128 a[120:123], %0 offset:3264" :  : "v"(bb) : "memory"); SB();
  asm volatile("ds_read_b128 a[116:119], %0 offset:2752" :  : "v"(bb) : "memory"); SB();
  asm volatile("ds_read_b128 a[112:115], %0 offset:2240" :  : "v"(bb) : "memory"); SB();
  asm volatile("ds_read2_b32 %0, %1 offset0:48 offset1:49" : "=&v"(cA[0][1][0]) : "v"(aa[0]) : "memory"); SB();
  asm volatile("ds_read2_b32 %0, %1 offset0:50 offset1:51" : "=&v"(cA[0][1][1]) : "v"(aa[0]) : "memory"); SB();
  asm volatile("ds_read_b128 a[108:111], %0 offset:1728" :  : "v"(bb) : "memory"); SB();
  asm volatile("ds_read2_b32 %0, %1 offset0:176 offset1:177" : "=&v"(cA[0][2][0]) : "v"(aa[1]) : "memory"); SB();
  asm volatile("" : "+v"(a1[0][0]), "+v"(a1[0][1]), "+v"(a1[0][2]), "+v"(a1[1][0]), "+v"(a1[1][1]), "+v"(a1[1][2]), "+v"(c1[0]), "+v"(c1[1]), "+v"(c1[2]), "+v"(co[0]), "+v"(co[1]), "+v"(co[2]) :  : ); SB();
  c1[0] += co[0]; c1[1] += co[1]; c1[2] += co[2];
#pragma unroll
  for (int n = 0; n < 6; ++n) { a1[n / 3][n % 3] = __builtin_elementwise_fma((f32x4){-xm[n / 3], -xm[n / 3], -xm[n / 3], -xm[n / 3]}, c1[n % 3], a1[n / 3][n % 3]); asm volatile("" : "+v"(a1[n / 3][n % 3])); }
  SB();
  asm volatile("s_waitcnt vmcnt(11)" :  :  : "memory"); SB();
  asm volatile("ds_read_b128 a[68:71], %0 offset:2048" :  : "v"(aA[0]) : "memory"); SB();
  asm volatile("ds_read_b128 a[84:87], %0 offset:2048" :  : "v"(aA[1]) : "memory"); SB();
  asm volatile("s_waitcnt vmcnt(9)" :  :  : "memory"); SB();
  asm volatile("ds_read_b128 a[72:75], %0 offset:4096" :  : "v"(aA[0]) : "memory"); SB();
  asm volatile("ds_read_b128 a[88:91], %0 offset:4096" :  : "v"(aA[1]) : "memory"); SB();
  asm volatile("buffer_load_dwordx4 v[172:175], %0, %1, %2 offen offset:1024 sc1" :  : "v"(vB), "s"(rWS), "s"(65536u) : "memory"); SB();
  asm volatile("v_xor_b32 %0, 131072, %0" : "+v"(vB) :  : "memory"); SB();
  asm volatile("buffer_load_dwordx4 v[176:179], %0, %1, %2 offen offset:0 sc1" :  : "v"(vB), "s"(rWS), "s"(0u) : "memory"); SB();
  if (nch == 3) {
  asm volatile("s_waitcnt lgkmcnt(5)\n\tv_mfma_f32_16x16x32_bf16 %0, %1, a[124:127], 0" : "=&v"(ac[7]) : "v"(((u32x4){cA[0][0][0][0], cA[0][0][0][1], cA[0][0][1][0], cA[0][0][1][1]})) : ); SB();
  asm volatile("ds_read2_b32 %0, %1 offset0:178 offset1:179" : "=&v"(cA[0][2][1]) : "v"(aa[1]) : "memory"); SB();
  asm volatile("v_mfma_f32_16x16x32_bf16 %0, %1, a[120:123], 0" : "=&v"(ac[6]) : "v"(((u32x4){cA[0][0][0][0], cA[0][0][0][1], cA[0][0][1][0], cA[0][0][1][1]})) : ); SB();
  asm volatile("ds_read2_b32 %0, %1 offset0:48 offset1:49" : "=&v"(cA[0][3][0]) : "v"(aa[1]) : "memory"); SB();
  asm volatile("v_mfma_f32_16x16x32_bf16 %0, %1, a[116:119], 0" : "=&v"(ac[5]) : "v"(((u32x4){cA[0][0][0][0], cA[0][0][0][1], cA[0][0][1][0], cA[0][0][1][1]})) : ); SB();
  asm volatile("ds_read2_b32 %0, %1 offset0:50 offset1:51" : "=&v"(cA[0][3][1]) : "v"(aa[1]) : "memory"); SB();
  asm volatile("buffer_load_dwordx4 v[180:183], %0, %1, %2 offen offset:1024 sc1" :  : "v"(vB), "s"(rWS), "s"(0u) : "memory"); SB();
  asm volatile("v_mfma_f32_16x16x32_bf16 %0, %1, a[112:115], 0" : "=&v"(ac[4]) : "v"(((u32x4){cA[0][0][0][0], cA[0][0][0][1], cA[0][0][1][0], cA[0][0][1][1]})) : ); SB();
  asm volatile("ds_read_b128 a[104:107], %0 offset:1216" :  : "v"(bb) : "memory"); SB();
  asm volatile("v_mfma_f32_16x16x32_bf16 %0, %1, a[120:123], %0" : "+v"(ac[7]) : "v"(((u32x4){cA[0][1][0][0], cA[0][1][0][1], cA[0][1][1][0], cA[0][1][1][1]})) : ); SB();
  asm volatile("ds_read2_b32 %0, %1 offset0:176 offset1:177" : "=&v"(cA[0][4][0]) : "v"(aa[2]) : "memory"); SB();
  asm volatile("v_mfma_f32_16x16x32_bf16 %0, %1, a[116:119], %0" : "+v"(ac[6]) : "v"(((u32x4){cA[0][1][0][0], cA[0][1][0][1], cA[0][1][1][0], cA[0][1][1][1]})) : ); SB();
  asm volatile("ds_read2_b32 %0, %1 offset0:178 offset1:179" : "=&v"(cA[0][4][1]) : "v"(aa[2]) : "memory"); SB();
  asm volatile("v_mfma_f32_16x16x32_bf16 %0, %1, a[112:115], %0" : "+v"(ac[5]) : "v"(((u32x4){cA[0][1][0][0], cA[0][1][0][1], cA[0][1][1][0], cA[0][1][1][1]})) : ); SB();
  asm volatile("ds_read2_b32 %0, %1 offset0:48 offset1:49" : "=&v"(cA[0][5][0]) : "v"(aa[2]) : "memory"); SB();
  asm volatile("s_waitcnt vmcnt(10)" :  :  : "memory"); SB();
  asm volatile("v_mfma_f32_16x16x32_bf16 %0, %1, a[108:111], %0" : "+v"(ac[4]) : "v"(((u32x4){cA[0][1][0][0], cA[0][1][0][1], cA[0][1][1][0], cA[0][1][1][1]})) : ); SB();
  asm volatile("ds_read2_b32 %0, %1 offset0:50 offset1:51" : "=&v"(cA[0][5][1]) : "v"(aa[2]) : "memory"); SB();
  asm volatile("ds_read_b128 a[76:79], %0 offset:6144" :  : "v"(aA[0]) : "memory"); SB();
  asm volatile("s_waitcnt lgkmcnt(8)\n\tv_mfma_f32_16x16x32_bf16 %0, %1, a[116:119], %0" : "+v"(ac[7]) : "v"(((u32x4){cA[0][2][0][0], cA[0][2][0][1], cA[0][2][1][0], cA[0][2][1][1]})) : ); SB();
  asm volatile("ds_read_b128 a[100:103], %0 offset:704" :  : "v"(bb) : "memory"); SB();
  asm volatile("ds_read_b128 a[92:95], %0 offset:6144" :  : "v"(aA[1]) : "memory"); SB();
  asm volatile("v_mfma_f32_16x16x32_bf16 %0, %1, a[112:115], %0" : "+v"(ac[6]) : "v"(((u32x4){cA[0][2][0][0], cA[0][2][0][1], cA[0][2][1][0], cA[0][2][1][1]})) : ); SB();
  asm volatile("ds_read2_b32 %0, %1 offset0:176 offset1:177" : "=&v"(cA[0][6][0]) : "v"(aa[3]) : "memory"); SB();
  asm volatile("buffer_load_dwordx4 v[184:187], %0, %1, %2 offen offset:0 sc1" :  : "v"(vB), "s"(rWS), "s"(65536u) : "memory"); SB();
  asm volatile("v_mfma_f32_16x16x32_bf16 %0, %1, a[108:111], %0" : "+v"(ac[5]) : "v"(((u32x4){cA[0][2][0][0], cA[0][2][0][1], cA[0][2][1][0], cA[0][2][1][1]})) : ); SB();
  asm volatile("ds_read2_b32 %0, %1 offset0:178 offset1:179" : "=&v"(cA[0][6][1]) : "v"(aa[3]) : "memory"); SB();
  asm volatile("buffer_load_dwordx4 v[188:191], %0, %1, %2 offen offset:1024 sc1" :  : "v"(vB), "s"(rWS), "s"(65536u) : "memory"); SB();
  asm volatile("v_mfma_f32_16x16x32_bf16 %0, %1, a[108:111], 0" : "=&v"(ac[3]) : "v"(((u32x4){cA[0][0][0][0], cA[0][0][0][1], cA[0][0][1][0], cA[0][0][1][1]})) : ); SB();
  asm volatile("ds_read2_b32 %0, %1 offset0:48 offset1:49" : "=&v"(cA[0][7][0]) : "v"(aa[3]) : "memory"); SB();
  asm volatile("s_waitcnt lgkmcnt(10)\n\tv_mfma_f32_16x16x32_bf16 %0, %1, a[112:115], %0" : "+v"(ac[7]) : "v"(((u32x4){cA[0][3][0][0], cA[0][3][0][1], cA[0][3][1][0], cA[0][3][1][1]})) : ); SB();
  asm volatile("ds_read2_b32 %0, %1 offset0:50 offset1:51" : "=&v"(cA[0][7][1]) : "v"(aa[3]) : "memory"); SB();
  asm volatile("v_mfma_f32_16x16x32_bf16 %0, %1, a[108:111], %0" : "+v"(ac[6]) : "v"(((u32x4){cA[0][3][0][0], cA[0][3][0][1], cA[0][3][1][0], cA[0][3][1][1]})) : ); SB();
  asm volatile("ds_read_b128 a[96:99], %0 offset:192" :  : "v"(bb) : "memory"); SB();
  asm volatile("v_xor_b32 %0, 393216, %0" : "+v"(vB) :  : "memory"); SB();
  asm volatile("v_mfma_f32_16x16x32_bf16 %0, %1, a[104:107], %0" : "+v"(ac[5]) : "v"(((u32x4){cA[0][3][0][0], cA[0][3][0][1], cA[0][3][1][0], cA[0][3][1][1]})) : ); SB();
  asm volatile("ds_read2_b32 %0, %1 offset0:160 offset1:161" : "=&v"(cA[1][0][0]) : "v"(aa[0]) : "memory"); SB();
  asm volatile("buffer_load_dwordx4 v[192:195], %0, %1, %2 offen offset:0 sc1" :  : "v"(vB), "s"(rWS), "s"(0u) : "memory"); SB();
  asm volatile("v_mfma_f32_16x16x32_bf16 %0, %1, a[104:107], %0" : "+v"(ac[4]) : "v"(((u32x4){cA[0][2][0][0], cA[0][2][0][1], cA[0][2][1][0], cA[0][2][1][1]})) : ); SB();
  asm volatile("s_waitcnt lgkmcnt(8)\n\tv_mfma_f32_16x16x32_bf16 %0, %1, a[108:111], %0" : "+v"(ac[7]) : "v"(((u32x4){cA[0][4][0][0], cA[0][4][0][1], cA[0][4][1][0], cA[0][4][1][1]})) : ); SB();
  asm volatile("ds_read2_b32 %0, %1 offset0:162 offset1:163" : "=&v"(cA[1][0][1]) : "v"(aa[0]) : "memory"); SB();
  asm volatile("buffer_load_dwordx4 v[196:199], %0, %1, %2 offen offset:1024 sc1" :  : "v"(vB), "s"(rWS), "s"(0u) : "memory"); SB();
  asm volatile("v_mfma_f32_16x16x32_bf16 %0, %1, a[104:107], %0" : "+v"(ac[6]) : "v"(((u32x4){cA[0][4][0][0], cA[0][4][0][1], cA[0][4][1][0], cA[0][4][1][1]})) : ); SB();
  asm volatile("ds_read_b128 a[156:159], %0 offset:3712" :  : "v"(bb) : "memory"); SB();
  asm volatile("v_mfma_f32_16x16x32_bf16 %0, %1, a[104:107], %0" : "+v"(ac[3]) : "v"(((u32x4){cA[0][1][0][0], cA[0][1][0][1], cA[0][1][1][0], cA[0][1][1][1]})) : ); SB();
  asm volatile("ds_read_b128 a[152:155], %0 offset:3200" :  : "v"(bb) : "memory"); SB();
  asm volatile("v_mfma_f32_16x16x32_bf16 %0, %1, a[104:107], 0" : "=&v"(ac[2]) : "v"(((u32x4){cA[0][0][0][0], cA[0][0][0][1], cA[0][0][1][0], cA[0][0][1][1]})) : ); SB();
  asm volatile("ds_write_b128 %0, %1 offset:0" :  : "v"(ew[0]), "v"(a1[0][0]) : "memory"); SB();
  asm volatile("v_mfma_f32_16x16x32_bf16 %0, %1, a[104:107], %0" : "+v"(ac[7]) : "v"(((u32x4){cA[0][5][0][0], cA[0][5][0][1], cA[0][5][1][0], cA[0][5][1][1]})) : ); SB();
  asm volatile("ds_read_b128 a[128:131], %0 offset:128" :  : "v"(bb) : "memory"); SB();
  asm volatile("buffer_load_dwordx4 v[200:203], %0, %1, %2 offen offset:0 sc1" :  : "v"(vB), "s"(rWS), "s"(65536u) : "memory"); SB();
  asm volatile("s_waitcnt lgkmcnt(6)\n\tv_mfma_f32_16x16x32_bf16 %0, %1, a[100:103], %0" : "+v"(ac[6]) : "v"(((u32x4){cA[0][5][0][0], cA[0][5][0][1], cA[0][5][1][0], cA[0][5][1][1]})) : ); SB();
  asm volatile("ds_read_b128 a[132:135], %0 offset:640" :  : "v"(bb) : "memory"); SB();
  asm volatile("buffer_load_dwordx4 v[204:207], %0, %1, %2 offen offset:1024 sc1" :  : "v"(vB), "s"(rWS), "s"(65536u) : "memory"); SB();
  asm volatile("v_mfma_f32_16x16x32_bf16 %0, %1, a[100:103], %0" : "+v"(ac[5]) : "v"(((u32x4){cA[0][4][0][0], cA[0][4][0][1], cA[0][4][1][0], cA[0][4][1][1]})) : ); SB();
  asm volatile("ds_read2_b32 %0, %1 offset0:32 offset1:33" : "=&v"(cA[1][1][0]) : "v"(aa[0]) : "memory"); SB();
  asm volatile("v_mfma_f32_16x16x32_bf16 %0, %1, a[100:103], %0" : "+v"(ac[4]) : "v"(((u32x4){cA[0][3][0][0], cA[0][3][0][1], cA[0][3][1][0], cA[0][3][1][1]})) : ); SB();
  asm volatile("v_xor_b32 %0, 262144, %0" : "+v"(vB) :  : "memory"); SB();
  asm volatile("v_mfma_f32_16x16x32_bf16 %0, %1, a[100:103], %0" : "+v"(ac[7]) : "v"(((u32x4){cA[0][6][0][0], cA[0][6][0][1], cA[0][6][1][0], cA[0][6][1][1]})) : ); SB();
  asm volatile("ds_read2_b32 %0, %1 offset0:34 offset1:35" : "=&v"(cA[1][1][1]) : "v"(aa[0]) : "memory"); SB();
  asm volatile("v_mfma_f32_16x16x32_bf16 %0, %1, a[100:103], %0" : "+v"(ac[3]) : "v"(((u32x4){cA[0][2][0][0], cA[0][2][0][1], cA[0][2][1][0], cA[0][2][1][1]})) : ); SB();
  asm volatile("ds_read_b128 a[148:151], %0 offset:2688" :  : "v"(bb) : "memory"); SB();
  asm volatile("buffer_load_dwordx4 v[208:211], %0, %1, %2 offen offset:0 sc1" :  : "v"(vB), "s"(rWS), "s"(2048u) : "memory"); SB();
  asm volatile("v_mfma_f32_16x16x32_bf16 %0, %1, a[100:103], %0" : "+v"(ac[2]) : "v"(((u32x4){cA[0][1][0][0], cA[0][1][0][1], cA[0][1][1][0], cA[0][1][1][1]})) : ); SB();
  asm volatile("ds_read_b128 a[136:139], %0 offset:1152" :  : "v"(bb) : "memory"); SB();
  asm volatile("v_mfma_f32_16x16x32_bf16 %0, %1, a[100:103], 0" : "=&v"(ac[1]) : "v"(((u32x4){cA[0][0][0][0], cA[0][0][0][1], cA[0][0][1][0], cA[0][0][1][1]})) : ); SB();
  asm volatile("ds_write_b128 %0, %1 offset:1024" :  : "v"(ew[0]), "v"(a1[0][1]) : "memory"); SB();
  asm volatile("v_mfma_f32_16x16x32_bf16 %0, %1, a[96:99], %0" : "+v"(ac[7]) : "v"(((u32x4){cA[0][7][0][0], cA[0][7][0][1], cA[0][7][1][0], cA[0][7][1][1]})) : ); SB();
  asm volatile("ds_read2_b32 %0, %1 offset0:160 offset1:161" : "=&v"(cA[1][2][0]) : "v"(aa[1]) : "memory"); SB();
  asm volatile("buffer_load_dwordx4 v[212:215], %0, %1, %2 offen offset:1024 sc1" :  : "v"(vB), "s"(rWS), "s"(2048u) : "memory"); SB();
  asm volatile("v_mfma_f32_16x16x32_bf16 %0, %1, a[96:99], %0" : "+v"(ac[6]) : "v"(((u32x4){cA[0][6][0][0], cA[0][6][0][1], cA[0][6][1][0], cA[0][6][1][1]})) : ); SB();
  asm volatile("ds_read2_b32 %0, %1 offset0:162 offset1:163" : "=&v"(cA[1][2][1]) : "v"(aa[1]) : "memory"); SB();
  asm volatile("v_mfma_f32_16x16x32_bf16 %0, %1, a[96:99], %0" : "+v"(ac[5]) : "v"(((u32x4){cA[0][5][0][0], cA[0][5][0][1], cA[0][5][1][0], cA[0][5][1][1]})) : ); SB();
  asm volatile("ds_read_b128 a[140:143], %0 offset:1664" :  : "v"(bb) : "memory"); SB();
  asm volatile("v_mfma_f32_16x16x32_bf16 %0, %1, a[96:99], %0" : "+v"(ac[4]) : "v"(((u32x4){cA[0][4][0][0], cA[0][4][0][1], cA[0][4][1][0], cA[0][4][1][1]})) : ); SB();
  asm volatile("buffer_load_dwordx4 v[216:219], %0, %1, %2 offen offset:0 sc1" :  : "v"(vB), "s"(rWS), "s"(67584u) : "memory"); SB();
  asm volatile("v_mfma_f32_16x16x32_bf16 %0, %1, a[96:99], %0" : "+v"(ac[3]) : "v"(((u32x4){cA[0][3][0][0], cA[0][3][0][1], cA[0][3][1][0], cA[0][3][1][1]})) : ); SB();
  asm volatile("ds_read_b128 a[144:147], %0 offset:2176" :  : "v"(bb) : "memory"); SB();
  asm volatile("v_mfma_f32_16x16x32_bf16 %0, %1, a[96:99], %0" : "+v"(ac[2]) : "v"(((u32x4){cA[0][2][0][0], cA[0][2][0][1], cA[0][2][1][0], cA[0][2][1][1]})) : ); SB();
  asm volatile("ds_read2_b32 %0, %1 offset0:32 offset1:33" : "=&v"(cA[1][3][0]) : "v"(aa[1]) : "memory"); SB();
  asm volatile("v_mfma_f32_16x16x32_bf16 %0, %1, a[96:99], %0" : "+v"(ac[1]) : "v"(((u32x4){cA[0][1][0][0], cA[0][1][0][1], cA[0][1][1][0], cA[0][1][1][1]})) : ); SB();
  asm volatile("ds_read2_b32 %0, %1 offset0:34 offset1:35" : "=&v"(cA[1][3][1]) : "v"(aa[1]) : "memory"); SB();
  asm volatile("v_mfma_f32_16x16x32_bf16 %0, %1, a[96:99], 0" : "=&v"(ac[0]) : "v"(((u32x4){cA[0][0][0][0], cA[0][0][0][1], cA[0][0][1][0], cA[0][0][1][1]})) : ); SB();
  asm volatile("buffer_load_dwordx4 v[220:223], %0, %1, %2 offen offset:1024 sc1" :  : "v"(vB), "s"(rWS), "s"(67584u) : "memory"); SB();
  asm volatile("s_waitcnt lgkmcnt(7)\n\tv_mfma_f32_16x16x32_bf16 %0, %1, a[156:159], %0" : "+v"(ac[7]) : "v"(((u32x4){cA[1][0][0][0], cA[1][0][0][1], cA[1][0][1][0], cA[1][0][1][1]})) : ); SB();
  asm volatile("ds_read2_b32 %0, %1 offset0:160 offset1:161" : "=&v"(cA[1][4][0]) : "v"(aa[2]) : "memory"); SB();
  asm volatile("v_mfma_f32_16x16x32_bf16 %0, %1, a[152:155], %0" : "+v"(ac[6]) : "v"(((u32x4){cA[1][0][0][0], cA[1][0][0][1], cA[1][0][1][0], cA[1][0][1][1]})) : ); SB();
  asm volatile("ds_read2_b32 %0, %1 offset0:162 offset1:163" : "=&v"(cA[1][4][1]) : "v"(aa[2]) : "memory"); SB();
  asm volatile("v_mfma_f32_16x16x32_bf16 %0, %1, a[128:131], %0" : "+v"(ac[0]) : "v"(((u32x4){cA[1][0][0][0], cA[1][0][0][1], cA[1][0][1][0], cA[1][0][1][1]})) : ); SB();
  asm volatile("ds_read2_b32 %0, %1 offset0:32 offset1:33" : "=&v"(cA[1][5][0]) : "v"(aa[2]) : "memory"); SB();
  asm volatile("v_mfma_f32_16x16x32_bf16 %0, %1, a[132:135], %0" : "+v"(ac[1]) : "v"(((u32x4){cA[1][0][0][0], cA[1][0][0][1], cA[1][0][1][0], cA[1][0][1][1]})) : ); SB();
  asm volatile("v_xor_b32 %0, 131072, %0" : "+v"(vB) :  : "memory"); SB();
  asm volatile("v_mfma_f32_16x16x32_bf16 %0, %1, a[152:155], %0" : "+v"(ac[7]) : "v"(((u32x4){cA[1][1][0][0], cA[1][1][0][1], cA[1][1][1][0], cA[1][1][1][1]})) : ); SB();
  asm volatile("ds_read2_b32 %0, %1 offset0:34 offset1:35" : "=&v"(cA[1][5][1]) : "v"(aa[2]) : "memory"); SB();
  asm volatile("v_mfma_f32_16x16x32_bf16 %0, %1, a[148:151], %0" : "+v"(ac[6]) : "v"(((u32x4){cA[1][1][0][0], cA[1][1][0][1], cA[1][1][1][0], cA[1][1][1][1]})) : ); SB();
  asm volatile("ds_read2_b32 %0, %1 offset0:160 offset1:161" : "=&v"(cA[1][6][0]) : "v"(aa[3]) : "memory"); SB();
  asm volatile("v_mfma_f32_16x16x32_bf16 %0, %1, a[136:139], %0" : "+v"(ac[2]) : "v"(((u32x4){cA[1][0][0][0], cA[1][0][0][1], cA[1][0][1][0], cA[1][0][1][1]})) : ); SB();
  asm volatile("ds_read2_b32 %0, %1 offset0:162 offset1:163" : "=&v"(cA[1][6][1]) : "v"(aa[3]) : "memory"); SB();
  asm volatile("v_mfma_f32_16x16x32_bf16 %0, %1, a[128:131], %0" : "+v"(ac[1]) : "v"(((u32x4){cA[1][1][0][0], cA[1][1][0][1], cA[1][1][1][0], cA[1][1][1][1]})) : ); SB();
  asm volatile("ds_write_b128 %0, %1 offset:0" :  : "v"(dd), "v"(ac[0]) : "memory"); SB();
  asm volatile("s_waitcnt lgkmcnt(7)\n\tv_mfma_f32_16x16x32_bf16 %0, %1, a[148:151], %0" : "+v"(ac[7]) : "v"(((u32x4){cA[1][2][0][0], cA[1][2][0][1], cA[1][2][1][0], cA[1][2][1][1]})) : ); SB();
  asm volatile("ds_read2_b32 %0, %1 offset0:32 offset1:33" : "=&v"(cA[1][7][0]) : "v"(aa[3]) : "memory"); SB();
  asm volatile("v_mfma_f32_16x16x32_bf16 %0, %1, a[140:143], %0" : "+v"(ac[3]) : "v"(((u32x4){cA[1][0][0][0], cA[1][0][0][1], cA[1][0][1][0], cA[1][0][1][1]})) : ); SB();
  asm volatile("ds_read2_b32 %0, %1 offset0:34 offset1:35" : "=&v"(cA[1][7][1]) : "v"(aa[3]) : "memory"); SB();
  asm volatile("v_mfma_f32_16x16x32_bf16 %0, %1, a[132:135], %0" : "+v"(ac[2]) : "v"(((u32x4){cA[1][1][0][0], cA[1][1][0][1], cA[1][1][1][0], cA[1][1][1][1]})) : ); SB();
  asm volatile("buffer_load_dwordx4 v[224:227], %0, %1, %2 offen offset:0 sc1" :  : "v"(vB), "s"(rWS), "s"(2048u) : "memory"); SB();
  asm volatile("v_mfma_f32_16x16x32_bf16 %0, %1, a[144:147], %0" : "+v"(ac[4]) : "v"(((u32x4){cA[1][0][0][0], cA[1][0][0][1], cA[1][0][1][0], cA[1][0][1][1]})) : ); SB();
  asm volatile("ds_write_b128 %0, %1 offset:1024" :  : "v"(dd), "v"(ac[1]) : "memory"); SB();
  asm volatile("v_mfma_f32_16x16x32_bf16 %0, %1, a[144:147], %0" : "+v"(ac[7]) : "v"(((u32x4){cA[1][3][0][0], cA[1][3][0][1], cA[1][3][1][0], cA[1][3][1][1]})) : ); SB();
  asm volatile("buffer_load_dwordx4 v[228:231], %0, %1, %2 offen offset:1024 sc1" :  : "v"(vB), "s"(rWS), "s"(2048u) : "memory"); SB();
  asm volatile("v_mfma_f32_16x16x32_bf16 %0, %1, a[136:139], %0" : "+v"(ac[3]) : "v"(((u32x4){cA[1][1][0][0], cA[1][1][0][1], cA[1][1][1][0], cA[1][1][1][1]})) : ); SB();
  asm volatile("buffer_load_dwordx4 v[232:235], %0, %1, %2 offen offset:0 sc1" :  : "v"(vB), "s"(rWS), "s"(67584u) : "memory"); SB();
  asm volatile("v_mfma_f32_16x16x32_bf16 %0, %1, a[128:131], %0" : "+v"(ac[2]) : "v"(((u32x4){cA[1][2][0][0], cA[1][2][0][1], cA[1][2][1][0], cA[1][2][1][1]})) : ); SB();
  asm volatile("ds_write_b128 %0, %1 offset:2048" :  : "v"(ew[0]), "v"(a1[0][2]) : "memory"); SB();
  asm volatile("v_mfma_f32_16x16x32_bf16 %0, %1, a[140:143], %0" : "+v"(ac[4]) : "v"(((u32x4){cA[1][1][0][0], cA[1][1][0][1], cA[1][1][1][0], cA[1][1][1][1]})) : ); SB();
  asm volatile("buffer_load_dwordx4 v[236:239], %0, %1, %2 offen offset:1024 sc1" :  : "v"(vB), "s"(rWS), "s"(67584u) : "memory"); SB();
  asm volatile("v_mfma_f32_16x16x32_bf16 %0, %1, a[148:151], %0" : "+v"(ac[5]) : "v"(((u32x4){cA[1][0][0][0], cA[1][0][0][1], cA[1][0][1][0], cA[1][0][1][1]})) : ); SB();
  asm volatile("v_xor_b32 %0, 393216, %0" : "+v"(vB) :  : "memory"); SB();
  asm volatile("v_mfma_f32_16x16x32_bf16 %0, %1, a[132:135], %0" : "+v"(ac[3]) : "v"(((u32x4){cA[1][2][0][0], cA[1][2][0][1], cA[1][2][1][0], cA[1][2][1][1]})) : ); SB();
  asm volatile("ds_write_b128 %0, %1 offset:0" :  : "v"(ew[1]), "v"(a1[1][0]) : "memory"); SB();
  asm volatile("v_mfma_f32_16x16x32_bf16 %0, %1, a[144:147], %0" : "+v"(ac[6]) : "v"(((u32x4){cA[1][2][0][0], cA[1][2][0][1], cA[1][2][1][0], cA[1][2][1][1]})) : ); SB();
  asm volatile("ds_write_b128 %0, %1 offset:2048" :  : "v"(dd), "v"(ac[2]) : "memory"); SB();
  asm volatile("v_mfma_f32_16x16x32_bf16 %0, %1, a[136:139], %0" : "+v"(ac[4]) : "v"(((u32x4){cA[1][2][0][0], cA[1][2][0][1], cA[1][2][1][0], cA[1][2][1][1]})) : ); SB();
  asm volatile("buffer_load_dwordx4 v[240:243], %0, %1, %2 offen offset:0 sc1" :  : "v"(vB), "s"(rWS), "s"(2048u) : "memory"); SB();
  asm volatile("v_mfma_f32_16x16x32_bf16 %0, %1, a[144:147], %0" : "+v"(ac[5]) : "v"(((u32x4){cA[1][1][0][0], cA[1][1][0][1], cA[1][1][1][0], cA[1][1][1][1]})) : ); SB();
  asm volatile("ds_write_b128 %0, %1 offset:1024" :  : "v"(ew[1]), "v"(a1[1][1]) : "memory"); SB();
  asm volatile("v_mfma_f32_16x16x32_bf16 %0, %1, a[128:131], %0" : "+v"(ac[3]) : "v"(((u32x4){cA[1][3][0][0], cA[1][3][0][1], cA[1][3][1][0], cA[1][3][1][1]})) : ); SB();
  asm volatile("buffer_load_dwordx4 v[244:247], %0, %1, %2 offen offset:1024 sc1" :  : "v"(vB), "s"(rWS), "s"(2048u) : "memory"); SB();
  asm volatile("v_mfma_f32_16x16x32_bf16 %0, %1, a[140:143], %0" : "+v"(ac[6]) : "v"(((u32x4){cA[1][3][0][0], cA[1][3][0][1], cA[1][3][1][0], cA[1][3][1][1]})) : ); SB();
  asm volatile("ds_write_b128 %0, %1 offset:2048" :  : "v"(ew[1]), "v"(a1[1][2]) : "memory"); SB();
  asm volatile("v_mfma_f32_16x16x32_bf16 %0, %1, a[132:135], %0" : "+v"(ac[4]) : "v"(((u32x4){cA[1][3][0][0], cA[1][3][0][1], cA[1][3][1][0], cA[1][3][1][1]})) : ); SB();
  asm volatile("buffer_load_dwordx4 v[248:251], %0, %1, %2 offen offset:0 sc1" :  : "v"(vB), "s"(rWS), "s"(67584u) : "memory"); SB();
  asm volatile("v_mfma_f32_16x16x32_bf16 %0, %1, a[140:143], %0" : "+v"(ac[5]) : "v"(((u32x4){cA[1][2][0][0], cA[1][2][0][1], cA[1][2][1][0], cA[1][2][1][1]})) : ); SB();
  asm volatile("s_waitcnt lgkmcnt(3)\n\tv_mfma_f32_16x16x32_bf16 %0, %1, a[140:143], %0" : "+v"(ac[7]) : "v"(((u32x4){cA[1][4][0][0], cA[1][4][0][1], cA[1][4][1][0], cA[1][4][1][1]})) : ); SB();
  asm volatile("ds_write_b128 %0, %1 offset:3072" :  : "v"(dd), "v"(ac[3]) : "memory"); SB();
  asm volatile("v_mfma_f32_16x16x32_bf16 %0, %1, a[136:139], %0" : "+v"(ac[6]) : "v"(((u32x4){cA[1][4][0][0], cA[1][4][0][1], cA[1][4][1][0], cA[1][4][1][1]})) : ); SB();
  asm volatile("buffer_load_dwordx4 v[252:255], %0, %1, %2 offen offset:1024 sc1" :  : "v"(vB), "s"(rWS), "s"(67584u) : "memory"); SB();
  asm volatile("v_mfma_f32_16x16x32_bf16 %0, %1, a[128:131], %0" : "+v"(ac[4]) : "v"(((u32x4){cA[1][4][0][0], cA[1][4][0][1], cA[1][4][1][0], cA[1][4][1][1]})) : ); SB();
  asm volatile("v_xor_b32 %0, 131072, %0" : "+v"(vB) :  : "memory"); SB();
  asm volatile("v_mfma_f32_16x16x32_bf16 %0, %1, a[136:139], %0" : "+v"(ac[5]) : "v"(((u32x4){cA[1][3][0][0], cA[1][3][0][1], cA[1][3][1][0], cA[1][3][1][1]})) : ); SB();
  asm volatile("v_mfma_f32_16x16x32_bf16 %0, %1, a[136:139], %0" : "+v"(ac[7]) : "v"(((u32x4){cA[1][5][0][0], cA[1][5][0][1], cA[1][5][1][0], cA[1][5][1][1]})) : ); SB();
  asm volatile("v_mfma_f32_16x16x32_bf16 %0, %1, a[132:135], %0" : "+v"(ac[6]) : "v"(((u32x4){cA[1][5][0][0], cA[1][5][0][1], cA[1][5][1][0], cA[1][5][1][1]})) : ); SB();
  asm volatile("s_add_u32 m0, %0, 135168" :  : "s"(mA) : "memory", "scc"); SB();
  asm volatile("v_mfma_f32_16x16x32_bf16 %0, %1, a[132:135], %0" : "+v"(ac[5]) : "v"(((u32x4){cA[1][4][0][0], cA[1][4][0][1], cA[1][4][1][0], cA[1][4][1][1]})) : ); SB();
  asm volatile("ds_write_b128 %0, %1 offset:4096" :  : "v"(dd), "v"(ac[4]) : "memory"); SB();
  asm volatile("v_mfma_f32_16x16x32_bf16 %0, %1, a[132:135], %0" : "+v"(ac[7]) : "v"(((u32x4){cA[1][6][0][0], cA[1][6][0][1], cA[1][6][1][0], cA[1][6][1][1]})) : ); SB();
  asm volatile("buffer_load_dwordx4 %0, %1, %2 offen lds" :  : "v"(voS), "s"(rA), "s"(ao[4]) : "memory"); SB();
  asm volatile("v_mfma_f32_16x16x32_bf16 %0, %1, a[128:131], %0" : "+v"(ac[6]) : "v"(((u32x4){cA[1][6][0][0], cA[1][6][0][1], cA[1][6][1][0], cA[1][6][1][1]})) : ); SB();
  asm volatile("buffer_load_dwordx4 %0, %1, %2 offen offset:1024 lds" :  : "v"(voS), "s"(rA), "s"(ao[5]) : "memory"); SB();
  asm volatile("v_mfma_f32_16x16x32_bf16 %0, %1, a[128:131], %0" : "+v"(ac[5]) : "v"(((u32x4){cA[1][5][0][0], cA[1][5][0][1], cA[1][5][1][0], cA[1][5][1][1]})) : ); SB();
  asm volatile("v_mfma_f32_16x16x32_bf16 %0, %1, a[128:131], %0" : "+v"(ac[7]) : "v"(((u32x4){cA[1][7][0][0], cA[1][7][0][1], cA[1][7][1][0], cA[1][7][1][1]})) : ); SB();
  asm volatile("s_nop 5" : "+v"(ac[5]), "+v"(ac[6]), "+v"(ac[7]) :  : ); SB();
  asm volatile("ds_write_b128 %0, %1 offset:6144" :  : "v"(dd), "v"(ac[6]) : "memory"); SB();
  asm volatile("ds_write_b128 %0, %1 offset:5120" :  : "v"(dd), "v"(ac[5]) : "memory"); SB();
  asm volatile("ds_write_b128 %0, %1 offset:7168" :  : "v"(dd), "v"(ac[7]) : "memory"); SB();
  } else {
  asm volatile("; ELSE_BEGIN"); SB();
  asm volatile("s_waitcnt lgkmcnt(0)" ::: "memory"); SB();
  asm volatile("buffer_load_dwordx4 v[180:183], %0, %1, %2 offen offset:1024 sc1" :  : "v"(vB), "s"(rWS), "s"(0u) : "memory"); SB();
  asm volatile("s_waitcnt lgkmcnt(0)" ::: "memory"); SB();
  asm volatile("s_waitcnt vmcnt(10)" :  :  : "memory"); SB();
  asm volatile("s_waitcnt lgkmcnt(0)" ::: "memory"); SB();
  asm volatile("ds_read_b128 a[76:79], %0 offset:6144" :  : "v"(aA[0]) : "memory"); SB();
  asm volatile("s_waitcnt lgkmcnt(0)" ::: "memory"); SB();
  asm volatile("ds_read_b128 a[92:95], %0 offset:6144" :  : "v"(aA[1]) : "memory"); SB();
  asm volatile("s_waitcnt lgkmcnt(0)" ::: "memory"); SB();
  asm volatile("buffer_load_dwordx4 v[184:187], %0, %1, %2 offen offset:0 sc1" :  : "v"(vB), "s"(rWS), "s"(65536u) : "memory"); SB();
  asm volatile("s_waitcnt lgkmcnt(0)" ::: "memory"); SB();
  asm volatile("buffer_load_dwordx4 v[188:191], %0, %1, %2 offen offset:1024 sc1" :  : "v"(vB), "s"(rWS), "s"(65536u) : "memory"); SB();
  asm volatile("s_waitcnt lgkmcnt(0)" ::: "memory"); SB();
  asm volatile("v_xor_b32 %0, 393216, %0" : "+v"(vB) :  : "memory"); SB();
  asm volatile("s_waitcnt lgkmcnt(0)" ::: "memory"); SB();
  asm volatile("buffer_load_dwordx4 v[192:195], %0, %1, %2 offen offset:0 sc1" :  : "v"(vB), "s"(rWS), "s"(0u) : "memory"); SB();
  asm volatile("s_waitcnt lgkmcnt(0)" ::: "memory"); SB();
  asm volatile("buffer_load_dwordx4 v[196:199], %0, %1, %2 offen offset:1024 sc1" :  : "v"(vB), "s"(rWS), "s"(0u) : "memory"); SB();
  asm volatile("s_waitcnt lgkmcnt(0)" ::: "memory"); SB();
  asm volatile("ds_write_b128 %0, %1 offset:0" :  : "v"(ew[0]), "v"(a1[0][0]) : "memory"); SB();
  asm volatile("s_waitcnt lgkmcnt(0)" ::: "memory"); SB();
  asm volatile("buffer_load_dwordx4 v[200:203], %0, %1, %2 offen offset:0 sc1" :  : "v"(vB), "s"(rWS), "s"(65536u) : "memory"); SB();
  asm volatile("s_waitcnt lgkmcnt(0)" ::: "memory"); SB();
  asm volatile("buffer_load_dwordx4 v[204:207], %0, %1, %2 offen offset:1024 sc1" :  : "v"(vB), "s"(rWS), "s"(65536u) : "memory"); SB();
  asm volatile("s_waitcnt lgkmcnt(0)" ::: "memory"); SB();
  asm volatile("v_xor_b32 %0, 262144, %0" : "+v"(vB) :  : "memory"); SB();
  asm volatile("s_waitcnt lgkmcnt(0)" ::: "memory"); SB();
  asm volatile("buffer_load_dwordx4 v[208:211], %0, %1, %2 offen offset:0 sc1" :  : "v"(vB), "s"(rWS), "s"(2048u) : "memory"); SB();
  asm volatile("s_waitcnt lgkmcnt(0)" ::: "memory"); SB();
  asm volatile("ds_write_b128 %0, %1 offset:1024" :  : "v"(ew[0]), "v"(a1[0][1]) : "memory"); SB();
  asm volatile("s_waitcnt lgkmcnt(0)" ::: "memory"); SB();
  asm volatile("buffer_load_dwordx4 v[212:215], %0, %1, %2 offen offset:1024 sc1" :  : "v"(vB), "s"(rWS), "s"(2048u) : "memory"); SB();
  asm volatile("s_waitcnt lgkmcnt(0)" ::: "memory"); SB();
  asm volatile("buffer_load_dwordx4 v[216:219], %0, %1, %2 offen offset:0 sc1" :  : "v"(vB), "s"(rWS), "s"(67584u) : "memory"); SB();
  asm volatile("s_waitcnt lgkmcnt(0)" ::: "memory"); SB();
  asm volatile("buffer_load_dwordx4 v[220:223], %0, %1, %2 offen offset:1024 sc1" :  : "v"(vB), "s"(rWS), "s"(67584u) : "memory"); SB();
  asm volatile("s_waitcnt lgkmcnt(0)" ::: "memory"); SB();
  asm volatile("v_xor_b32 %0, 131072, %0" : "+v"(vB) :  : "memory"); SB();
  asm volatile("s_waitcnt lgkmcnt(0)" ::: "memory"); SB();
  asm volatile("buffer_load_dwordx4 v[224:227], %0, %1, %2 offen offset:0 sc1" :  : "v"(vB), "s"(rWS), "s"(2048u) : "memory"); SB();
  asm volatile("s_waitcnt lgkmcnt(0)" ::: "memory"); SB();
  asm volatile("buffer_load_dwordx4 v[228:231], %0, %1, %2 offen offset:1024 sc1" :  : "v"(vB), "s"(rWS), "s"(2048u) : "memory"); SB();
  asm volatile("s_waitcnt lgkmcnt(0)" ::: "memory"); SB();
  asm volatile("buffer_load_dwordx4 v[232:235], %0, %1, %2 offen offset:0 sc1" :  : "v"(vB), "s"(rWS), "s"(67584u) : "memory"); SB();
  asm volatile("s_waitcnt lgkmcnt(0)" ::: "memory"); SB();
  asm volatile("ds_write_b128 %0, %1 offset:2048" :  : "v"(ew[0]), "v"(a1[0][2]) : "memory"); SB();
  asm volatile("s_waitcnt lgkmcnt(0)" ::: "memory"); SB();
  asm volatile("buffer_load_dwordx4 v[236:239], %0, %1, %2 offen offset:1024 sc1" :  : "v"(vB), "s"(rWS), "s"(67584u) : "memory"); SB();
  asm volatile("s_waitcnt lgkmcnt(0)" ::: "memory"); SB();
  asm volatile("v_xor_b32 %0, 393216, %0" : "+v"(vB) :  : "memory"); SB();
  asm volatile("s_waitcnt lgkmcnt(0)" ::: "memory"); SB();
  asm volatile("ds_write_b128 %0, %1 offset:0" :  : "v"(ew[1]), "v"(a1[1][0]) : "memory"); SB();
  asm volatile("s_waitcnt lgkmcnt(0)" ::: "memory"); SB();
  asm volatile("buffer_load_dwordx4 v[240:243], %0, %1, %2 offen offset:0 sc1" :  : "v"(vB), "s"(rWS), "s"(2048u) : "memory"); SB();
  asm volatile("s_waitcnt lgkmcnt(0)" ::: "memory"); SB();
  asm volatile("ds_write_b128 %0, %1 offset:1024" :  : "v"(ew[1]), "v"(a1[1][1]) : "memory"); SB();
  asm volatile("s_waitcnt lgkmcnt(0)" ::: "memory"); SB();
  asm volatile("buffer_load_dwordx4 v[244:247], %0, %1, %2 offen offset:1024 sc1" :  : "v"(vB), "s"(rWS), "s"(2048u) : "memory"); SB();
  asm volatile("s_waitcnt lgkmcnt(0)" ::: "memory"); SB();
  asm volatile("ds_write_b128 %0, %1 offset:2048" :  : "v"(ew[1]), "v"(a1[1][2]) : "memory"); SB();
  asm volatile("s_waitcnt lgkmcnt(0)" ::: "memory"); SB();
  asm volatile("buffer_load_dwordx4 v[248:251], %0, %1, %2 offen offset:0 sc1" :  : "v"(vB), "s"(rWS), "s"(67584u) : "memory"); SB();
  asm volatile("s_waitcnt lgkmcnt(0)" ::: "memory"); SB();
  asm volatile("buffer_load_dwordx4 v[252:255], %0, %1, %2 offen offset:1024 sc1" :  : "v"(vB), "s"(rWS), "s"(67584u) : "memory"); SB();
  asm volatile("s_waitcnt lgkmcnt(0)" ::: "memory"); SB();
  asm volatile("v_xor_b32 %0, 131072, %0" : "+v"(vB) :  : "memory"); SB();
  asm volatile("s_waitcnt lgkmcnt(0)" ::: "memory"); SB();
  asm volatile("s_add_u32 m0, %0, 135168" :  : "s"(mA) : "memory", "scc"); SB();
  asm volatile("s_waitcnt lgkmcnt(0)" ::: "memory"); SB();
  asm volatile("buffer_load_dwordx4 %0, %1, %2 offen lds" :  : "v"(voS), "s"(rA), "s"(ao[4]) : "memory"); SB();
  asm volatile("s_waitcnt lgkmcnt(0)" ::: "memory"); SB();
  asm volatile("s_waitcnt lgkmcnt(0)" ::: "memory"); SB();
  asm volatile("buffer_load_dwordx4 %0, %1, %2 offen offset:1024 lds" :  : "v"(voS), "s"(rA), "s"(ao[5]) : "memory"); SB();
  asm volatile("s_waitcnt lgkmcnt(0)" ::: "memory"); SB();
  asm volatile("s_waitcnt lgkmcnt(0) ; ELSE_END" ::: "memory"); SB();
  }
  ST(15);
  f32x4 g[2][4][2], pr[6], yq[8], bs4 = {bs, bs, bs, bs};
  u32x4 ho[3], ykv, y2;
  float gt[4], ge[4];
  unsigned vH_, pw2, pe[3], vX, vO, vO2, vP, vFH, vFY, vHr, hso[8], ys2, ydr, ykr, er, sFH, sFY;
  int dl;
  asm volatile("" : "+v"(bs4)); SB();
  asm volatile("s_waitcnt vmcnt(24)" :  :  : "memory"); SB();
  asm volatile("v_mfma_f32_16x16x32_bf16 %0, v[160:163], a[160:163], %1" : "=&v"(g[0][0][0]) : "v"(bs4) : ); SB();
  { unsigned lane = lane0, w = w0, rb = rb0, cb = cb0;
  asm volatile("" : "+v"(lane)); asm volatile("" : "+s"(w), "+s"(rb), "+s"(cb)); SB();

  asm volatile("v_mfma_f32_16x16x32_bf16 %0, v[160:163], a[164:167], 0" : "=&v"(g[0][1][0]) :  : ); SB();
  er = lb + 32768 + w * 6144 + l16; asm volatile("" : "+v"(er)); SB();
  asm volatile("v_mfma_f32_16x16x32_bf16 %0, v[160:163], a[168:171], 0" : "=&v"(g[0][2][0]) :  : ); SB();
  asm volatile("s_waitcnt lgkmcnt(0)\n\ts_barrier" :  :  : "memory"); SB();
  asm volatile("ds_read_b128 %0, %1 offset:0" : "=&v"(a1[0][0]) : "v"(er) : "memory"); SB();
  asm volatile("v_mfma_f32_16x16x32_bf16 %0, v[160:163], a[172:175], 0" : "=&v"(g[0][3][0]) :  : ); SB();
  asm volatile("ds_read_b128 a[96:99], %0 offset:32768" :  : "v"(aA[0]) : "memory"); SB();
  asm volatile("v_mfma_f32_16x16x32_bf16 %0, v[164:167], a[160:163], %1" : "=&v"(g[0][0][1]) : "v"(bs4) : ); SB();
  asm volatile("ds_read_b128 a[112:115], %0 offset:32768" :  : "v"(aA[1]) : "memory"); SB();
  asm volatile("v_mfma_f32_16x16x32_bf16 %0, v[164:167], a[164:167], 0" : "=&v"(g[0][1][1]) :  : ); SB();
  asm volatile("ds_read_b128 a[100:103], %0 offset:34816" :  : "v"(aA[0]) : "memory"); SB();
  asm volatile("v_mfma_f32_16x16x32_bf16 %0, v[164:167], a[168:171], 0" : "=&v"(g[0][2][1]) :  : ); SB();
  asm volatile("ds_read_b128 a[116:119], %0 offset:34816" :  : "v"(aA[1]) : "memory"); SB();
  asm volatile("v_mfma_f32_16x16x32_bf16 %0, v[164:167], a[172:175], 0" : "=&v"(g[0][3][1]) :  : ); SB();
  asm volatile("buffer_load_dwordx4 %0, %1, %2 offen offset:2048 lds" :  : "v"(voS), "s"(rA), "s"(ao[6]) : "memory"); SB();
  ST(25);
  asm volatile("s_waitcnt vmcnt(23)" :  :  : "memory"); SB();
  asm volatile("v_mfma_f32_16x16x32_bf16 %0, v[168:171], a[176:179], %0" : "+v"(g[0][0][0]) :  : ); SB();
  asm volatile("buffer_load_dwordx4 v[160:163], %0, %1, %2 offen offset:0 sc1" :  : "v"(vB), "s"(rWS), "s"(0u) : "memory"); SB();
  asm volatile("v_mfma_f32_16x16x32_bf16 %0, v[168:171], a[180:183], %0" : "+v"(g[0][1][0]) :  : ); SB();
  asm volatile("ds_read_b128 %0, %1 offset:1024" : "=&v"(a1[0][1]) : "v"(er) : "memory"); SB();
  asm volatile("v_mfma_f32_16x16x32_bf16 %0, v[168:171], a[184:187], %0" : "+v"(g[0][2][0]) :  : ); SB();
  asm volatile("ds_read_b128 %0, %1 offset:2048" : "=&v"(a1[0][2]) : "v"(er) : "memory"); SB();
  asm volatile("v_mfma_f32_16x16x32_bf16 %0, v[168:171], a[188:191], %0" : "+v"(g[0][3][0]) :  : ); SB();
  asm volatile("buffer_load_dwordx4 v[164:167], %0, %1, %2 offen offset:1024 sc1" :  : "v"(vB), "s"(rWS), "s"(0u) : "memory"); SB();
  asm volatile("v_mfma_f32_16x16x32_bf16 %0, v[172:175], a[176:179], %0" : "+v"(g[0][0][1]) :  : ); SB();
  asm volatile("ds_read_b128 %0, %1 offset:3072" : "=&v"(a1[1][0]) : "v"(er) : "memory"); SB();
  asm volatile("v_mfma_f32_16x16x32_bf16 %0, v[172:175], a[180:183], %0" : "+v"(g[0][1][1]) :  : ); SB();
  asm volatile("ds_read_b128 %0, %1 offset:4096" : "=&v"(a1[1][1]) : "v"(er) : "memory"); SB();
  asm volatile("v_mfma_f32_16x16x32_bf16 %0, v[172:175], a[184:187], %0" : "+v"(g[0][2][1]) :  : ); SB();
  asm volatile("ds_read_b128 %0, %1 offset:5120" : "=&v"(a1[1][2]) : "v"(er) : "memory"); SB();
  asm volatile("v_mfma_f32_16x16x32_bf16 %0, v[172:175], a[188:191], %0" : "+v"(g[0][3][1]) :  : ); SB();
  asm volatile("buffer_load_dwordx4 %0, %1, %2 offen offset:3072 lds" :  : "v"(voS), "s"(rA), "s"(ao[7]) : "memory"); SB();
  asm volatile("s_waitcnt vmcnt(24)" :  :  : "memory"); SB();
  asm volatile("v_mfma_f32_16x16x32_bf16 %0, v[176:179], a[192:195], %0" : "+v"(g[0][0][0]) :  : ); SB();
  ydr = lb + 0 + (w * 2 + (lane >> 5)) * 1024 + (lane & 1) * 512; asm volatile("" : "+v"(ydr)); SB();
  asm volatile("v_mfma_f32_16x16x32_bf16 %0, v[176:179], a[196:199], %0" : "+v"(g[0][1][0]) :  : ); SB();
  ydr = ydr + ((((lane >> 1) & 15) ^ ((lane & 1) << 3)) << 4); asm volatile("" : "+v"(ydr)); SB();
  asm volatile("v_mfma_f32_16x16x32_bf16 %0, v[176:179], a[200:203], %0" : "+v"(g[0][2][0]) :  : ); SB();
  ykr = lb + 59520 + 512 + w * 1024 + l16; asm volatile("" : "+v"(ykr)); SB();
  asm volatile("v_mfma_f32_16x16x32_bf16 %0, v[176:179], a[204:207], %0" : "+v"(g[0][3][0]) :  : ); SB();
  asm volatile("buffer_load_dwordx4 v[168:171], %0, %1, %2 offen offset:0 sc1" :  : "v"(vB), "s"(rWS), "s"(65536u) : "memory"); SB();
  pw2 = er + (122880 - 32768); asm volatile("" : "+v"(pw2)); SB();
  asm volatile("v_mfma_f32_16x16x32_bf16 %0, v[180:183], a[192:195], %0" : "+v"(g[0][0][1]) :  : ); SB();
  pe[0] = lb + 122880 + (w ^ 1) * 6144 + l16; asm volatile("" : "+v"(pe[0])); SB();
  asm volatile("v_mfma_f32_16x16x32_bf16 %0, v[180:183], a[196:199], %0" : "+v"(g[0][1][1]) :  : ); SB();
  pe[1] = lb + 122880 + (w ^ 2) * 6144 + l16; asm volatile("" : "+v"(pe[1])); SB();
  asm volatile("v_mfma_f32_16x16x32_bf16 %0, v[180:183], a[200:203], %0" : "+v"(g[0][2][1]) :  : ); SB();
  asm volatile("buffer_load_dwordx4 v[172:175], %0, %1, %2 offen offset:1024 sc1" :  : "v"(vB), "s"(rWS), "s"(65536u) : "memory"); SB();
  asm volatile("v_mfma_f32_16x16x32_bf16 %0, v[180:183], a[204:207], %0" : "+v"(g[0][3][1]) :  : ); SB();
  pe[2] = lb + 122880 + (w ^ 3) * 6144 + l16; asm volatile("" : "+v"(pe[2])); SB();
  asm volatile("s_waitcnt vmcnt(24)" :  :  : "memory"); SB();
  asm volatile("v_mfma_f32_16x16x32_bf16 %0, v[184:187], a[208:211], %0" : "+v"(g[0][0][0]) :  : ); SB();
  asm volatile("s_waitcnt lgkmcnt(0)" : "+v"(a1[0][0]), "+v"(a1[0][1]), "+v"(a1[0][2]), "+v"(a1[1][0]), "+v"(a1[1][1]), "+v"(a1[1][2]) :  : "memory"); SB();
  asm volatile("v_xor_b32 %0, 917504, %0" : "+v"(vB) :  : "memory"); SB();
  asm volatile("v_mfma_f32_16x16x32_bf16 %0, v[184:187], a[212:215], %0" : "+v"(g[0][1][0]) :  : ); SB();
  dl = (int)(wm * 12928 - wn * 256 + w * 128) - 2 * 12928; asm volatile("" : "+s"(dl)); SB();
  asm volatile("v_mfma_f32_16x16x32_bf16 %0, v[184:187], a[216:219], %0" : "+v"(g[0][2][0]) :  : ); SB();
  asm volatile("buffer_load_dwordx4 v[176:179], %0, %1, %2 offen offset:0 sc1" :  : "v"(vB), "s"(rWS), "s"(0u) : "memory"); SB();
  asm volatile("v_add_u32 %0, %1, %0" : "+v"(aa[0]) : "s"(dl) : ); SB();
  asm volatile("v_mfma_f32_16x16x32_bf16 %0, v[184:187], a[220:223], %0" : "+v"(g[0][3][0]) :  : ); SB();
  asm volatile("v_xor_b32 %0, 128, %0" : "+v"(voS) :  : "memory"); SB();
  asm volatile("v_add_u32 %0, %1, %0" : "+v"(aa[1]) : "s"(dl) : ); SB();
  asm volatile("v_mfma_f32_16x16x32_bf16 %0, v[188:191], a[208:211], %0" : "+v"(g[0][0][1]) :  : ); SB();
  asm volatile("v_add_u32 %0, %1, %0" : "+v"(aa[2]) : "s"(dl) : ); SB();
  asm volatile("v_mfma_f32_16x16x32_bf16 %0, v[188:191], a[212:215], %0" : "+v"(g[0][1][1]) :  : ); SB();
  asm volatile("buffer_load_dwordx4 v[180:183], %0, %1, %2 offen offset:1024 sc1" :  : "v"(vB), "s"(rWS), "s"(0u) : "memory"); SB();
  asm volatile("v_add_u32 %0, %1, %0" : "+v"(aa[3]) : "s"(dl) : ); SB();
  asm volatile("v_mfma_f32_16x16x32_bf16 %0, v[188:191], a[216:219], %0" : "+v"(g[0][2][1]) :  : ); SB();
  asm volatile("v_add_u32 %0, %1, %0" : "+v"(bb) : "s"(dl) : ); SB();
  asm volatile("v_mfma_f32_16x16x32_bf16 %0, v[188:191], a[220:223], %0" : "+v"(g[0][3][1]) :  : ); SB();
  asm volatile("v_mul_f32 %0, %1, %1" : "=v"(gt[0]) : "v"(a1[0][0][0]) : ); SB();
  asm volatile("s_waitcnt vmcnt(24)" :  :  : "memory"); SB();
  asm volatile("v_mfma_f32_16x16x32_bf16 %0, v[192:195], a[224:227], %0" : "+v"(g[0][0][0]) :  : ); SB();
  asm volatile("buffer_load_dwordx4 v[184:187], %0, %1, %2 offen offset:0 sc1" :  : "v"(vB), "s"(rWS), "s"(65536u) : "memory"); SB();
  asm volatile("v_mul_f32 %0, %1, %1" : "=v"(gt[1]) : "v"(a1[0][0][1]) : ); SB();
  asm volatile("v_mfma_f32_16x16x32_bf16 %0, v[192:195], a[228:231], %0" : "+v"(g[0][1][0]) :  : ); SB();
  asm volatile("v_mul_f32 %0, %1, %1" : "=v"(gt[2]) : "v"(a1[0][0][2]) : ); SB();
  asm volatile("v_mfma_f32_16x16x32_bf16 %0, v[192:195], a[232:235], %0" : "+v"(g[0][2][0]) :  : ); SB();
  asm volatile("v_mul_f32 %0, %1, %1" : "=v"(gt[3]) : "v"(a1[0][0][3]) : ); SB();
  asm volatile("v_mfma_f32_16x16x32_bf16 %0, v[192:195], a[236:239], %0" : "+v"(g[0][3][0]) :  : ); SB();
  asm volatile("v_fma_f32 %0, %0, %1, %2" : "+v"(gt[0]) : "v"(k2[0]), "v"(k1[0]) : ); SB();
  asm volatile("v_mfma_f32_16x16x32_bf16 %0, v[196:199], a[224:227], %0" : "+v"(g[0][0][1]) :  : ); SB();
  asm volatile("buffer_load_dwordx4 v[188:191], %0, %1, %2 offen offset:1024 sc1" :  : "v"(vB), "s"(rWS), "s"(65536u) : "memory"); SB();
  asm volatile("v_fma_f32 %0, %0, %1, %2" : "+v"(gt[1]) : "v"(k2[0]), "v"(k1[0]) : ); SB();
  asm volatile("v_mfma_f32_16x16x32_bf16 %0, v[196:199], a[228:231], %0" : "+v"(g[0][1][1]) :  : ); SB();
  asm volatile("v_xor_b32 %0, 131072, %0" : "+v"(vB) :  : "memory"); SB();
  asm volatile("v_fma_f32 %0, %0, %1, %2" : "+v"(gt[2]) : "v"(k2[0]), "v"(k1[0]) : ); SB();
  asm volatile("v_mfma_f32_16x16x32_bf16 %0, v[196:199], a[232:235], %0" : "+v"(g[0][2][1]) :  : ); SB();
  asm volatile("v_fma_f32 %0, %0, %1, %2" : "+v"(gt[3]) : "v"(k2[0]), "v"(k1[0]) : ); SB();
  asm volatile("v_mfma_f32_16x16x32_bf16 %0, v[196:199], a[236:239], %0" : "+v"(g[0][3][1]) :  : ); SB();
  asm volatile("s_add_u32 m0, %0, 98304" :  : "s"(mA) : "memory", "scc"); SB();
  asm volatile("v_mul_f32 %0, %0, %1" : "+v"(gt[0]) : "v"(a1[0][0][0]) : ); SB();
  asm volatile("s_waitcnt vmcnt(24)" :  :  : "memory"); SB();
  asm volatile("v_mfma_f32_16x16x32_bf16 %0, v[200:203], a[240:243], %0" : "+v"(g[0][0][0]) :  : ); SB();
  asm volatile("buffer_load_dwordx4 %0, %1, %2 offen lds" :  : "v"(voS), "s"(rA), "s"(ao[0]) : "memory"); SB();
  asm volatile("v_mul_f32 %0, %0, %1" : "+v"(gt[1]) : "v"(a1[0][0][1]) : ); SB();
  asm volatile("v_mfma_f32_16x16x32_bf16 %0, v[200:203], a[244:247], %0" : "+v"(g[0][1][0]) :  : ); SB();
  asm volatile("v_mul_f32 %0, %0, %1" : "+v"(gt[2]) : "v"(a1[0][0][2]) : ); SB();
  asm volatile("v_mfma_f32_16x16x32_bf16 %0, v[200:203], a[248:251], %0" : "+v"(g[0][2][0]) :  : ); SB();
  asm volatile("s_waitcnt vmcnt(11)" :  :  : "memory"); SB();
  asm volatile("ds_read_b128 a[104:107], %0 offset:36864" :  : "v"(aA[0]) : "memory"); SB();
  asm volatile("v_mfma_f32_16x16x32_bf16 %0, v[200:203], a[252:255], %0" : "+v"(g[0][3][0]) :  : ); SB();
  asm volatile("ds_read_b128 a[120:123], %0 offset:36864" :  : "v"(aA[1]) : "memory"); SB();
  asm volatile("v_mul_f32 %0, %0, %1" : "+v"(gt[3]) : "v"(a1[0][0][3]) : ); SB();
  asm volatile("v_mfma_f32_16x16x32_bf16 %0, v[204:207], a[240:243], %0" : "+v"(g[0][0][1]) :  : ); SB();
  asm volatile("buffer_load_dwordx4 v[192:195], %0, %1, %2 offen offset:0 sc1" :  : "v"(vB), "s"(rWS), "s"(0u) : "memory"); SB();
  asm volatile("v_exp_f32 %0, %1" : "=v"(ge[0]) : "v"(gt[0]) : ); SB();
  asm volatile("v_mfma_f32_16x16x32_bf16 %0, v[204:207], a[244:247], %0" : "+v"(g[0][1][1]) :  : ); SB();
  asm volatile("v_exp_f32 %0, %1" : "=v"(ge[1]) : "v"(gt[1]) : ); SB();
  asm volatile("v_mfma_f32_16x16x32_bf16 %0, v[204:207], a[248:251], %0" : "+v"(g[0][2][1]) :  : ); SB();
  asm volatile("buffer_load_dwordx4 v[196:199], %0, %1, %2 offen offset:1024 sc1" :  : "v"(vB), "s"(rWS), "s"(0u) : "memory"); SB();
  asm volatile("v_exp_f32 %0, %1" : "=v"(ge[2]) : "v"(gt[2]) : ); SB();
  asm volatile("v_mfma_f32_16x16x32_bf16 %0, v[204:207], a[252:255], %0" : "+v"(g[0][3][1]) :  : ); SB();
  asm volatile("v_exp_f32 %0, %1" : "=v"(ge[3]) : "v"(gt[3]) : ); SB();
  asm volatile("v_mfma_f32_16x16x32_bf16 %0, v[208:211], a[160:163], %1" : "=&v"(g[1][0][0]) : "v"(bs4) : ); SB();
  asm volatile("v_fma_f32 %0, %0, %1, %1" : "+v"(ge[0]) : "v"(xd[0]) : ); SB();
  asm volatile("v_mfma_f32_16x16x32_bf16 %0, v[208:211], a[164:167], 0" : "=&v"(g[1][1][0]) :  : ); SB();
  asm volatile("buffer_load_dwordx4 v[200:203], %0, %1, %2 offen offset:0 sc1" :  : "v"(vB), "s"(rWS), "s"(65536u) : "memory"); SB();
  asm volatile("v_fma_f32 %0, %0, %1, %1" : "+v"(ge[1]) : "v"(xd[0]) : ); SB();
  asm volatile("v_mfma_f32_16x16x32_bf16 %0, v[208:211], a[168:171], 0" : "=&v"(g[1][2][0]) :  : ); SB();
  asm volatile("v_fma_f32 %0, %0, %1, %1" : "+v"(ge[2]) : "v"(xd[0]) : ); SB();
  asm volatile("v_mfma_f32_16x16x32_bf16 %0, v[208:211], a[172:175], 0" : "=&v"(g[1][3][0]) :  : ); SB();
  asm volatile("v_fma_f32 %0, %0, %1, %1" : "+v"(ge[3]) : "v"(xd[0]) : ); SB();
  asm volatile("v_mfma_f32_16x16x32_bf16 %0, v[212:215], a[160:163], %1" : "=&v"(g[1][0][1]) : "v"(bs4) : ); SB();
  asm volatile("v_rcp_f32 %0, %1" : "=v"(gt[0]) : "v"(ge[0]) : ); SB();
  asm volatile("v_mfma_f32_16x16x32_bf16 %0, v[212:215], a[164:167], 0" : "=&v"(g[1][1][1]) :  : ); SB();
  asm volatile("v_rcp_f32 %0, %1" : "=v"(gt[1]) : "v"(ge[1]) : ); SB();
  asm volatile("v_mfma_f32_16x16x32_bf16 %0, v[212:215], a[168:171], 0" : "=&v"(g[1][2][1]) :  : ); SB();
  asm volatile("v_rcp_f32 %0, %1" : "=v"(gt[2]) : "v"(ge[2]) : ); SB();
  asm volatile("v_mfma_f32_16x16x32_bf16 %0, v[212:215], a[172:175], 0" : "=&v"(g[1][3][1]) :  : ); SB();
  asm volatile("v_rcp_f32 %0, %1" : "=v"(gt[3]) : "v"(ge[3]) : ); SB();
  asm volatile("v_mfma_f32_16x16x32_bf16 %0, v[216:219], a[176:179], %0" : "+v"(g[1][0][0]) :  : ); SB();
  asm volatile("buffer_load_dwordx4 v[204:207], %0, %1, %2 offen offset:1024 sc1" :  : "v"(vB), "s"(rWS), "s"(65536u) : "memory"); SB();
  asm volatile("v_mul_f32 %0, %0, %1" : "+v"(a1[0][0][0]) : "v"(gt[0]) : ); SB();
  asm volatile("v_mfma_f32_16x16x32_bf16 %0, v[216:219], a[180:183], %0" : "+v"(g[1][1][0]) :  : ); SB();
  asm volatile("v_xor_b32 %0, 393216, %0" : "+v"(vB) :  : "memory"); SB();
  asm volatile("v_mul_f32 %0, %0, %1" : "+v"(a1[0][0][1]) : "v"(gt[1]) : ); SB();
  asm volatile("v_mfma_f32_16x16x32_bf16 %0, v[216:219], a[184:187], %0" : "+v"(g[1][2][0]) :  : ); SB();
  asm volatile("v_mul_f32 %0, %0, %1" : "+v"(a1[0][0][2]) : "v"(gt[2]) : ); SB();
  asm volatile("v_mfma_f32_16x16x32_bf16 %0, v[216:219], a[188:191], %0" : "+v"(g[1][3][0]) :  : ); SB();
  asm volatile("v_mul_f32 %0, %0, %1" : "+v"(a1[0][0][3]) : "v"(gt[3]) : ); SB();
  asm volatile("v_mfma_f32_16x16x32_bf16 %0, v[220:223], a[176:179], %0" : "+v"(g[1][0][1]) :  : ); SB();
  asm volatile("buffer_load_dwordx4 v[208:211], %0, %1, %2 offen offset:0 sc1" :  : "v"(vB), "s"(rWS), "s"(0u) : "memory"); SB();
  asm volatile("v_mul_f32 %0, %1, %1" : "=v"(gt[0]) : "v"(a1[0][1][0]) : ); SB();
  asm volatile("v_mfma_f32_16x16x32_bf16 %0, v[220:223], a[180:183], %0" : "+v"(g[1][1][1]) :  : ); SB();
  asm volatile("v_mul_f32 %0, %1, %1" : "=v"(gt[1]) : "v"(a1[0][1][1]) : ); SB();
  asm volatile("v_mfma_f32_16x16x32_bf16 %0, v[220:223], a[184:187], %0" : "+v"(g[1][2][1]) :  : ); SB();
  asm volatile("v_mul_f32 %0, %1, %1" : "=v"(gt[2]) : "v"(a1[0][1][2]) : ); SB();
  asm volatile("v_mfma_f32_16x16x32_bf16 %0, v[220:223], a[188:191], %0" : "+v"(g[1][3][1]) :  : ); SB();
  asm volatile("buffer_load_dwordx4 v[212:215], %0, %1, %2 offen offset:1024 sc1" :  : "v"(vB), "s"(rWS), "s"(0u) : "memory"); SB();
  ST(26);
  asm volatile("v_mfma_f32_16x16x32_bf16 %0, v[224:227], a[192:195], %0" : "+v"(g[1][0][0]) :  : ); SB();
  vH_ = vH; asm volatile("" : "+v"(vH_)); SB();
  asm volatile("v_mfma_f32_16x16x32_bf16 %0, v[224:227], a[196:199], %0" : "+v"(g[1][1][0]) :  : ); SB();
  vO = W_WO + (cb * 120 + w * 30 + 0u * 15) * 1024 + l16; asm volatile("" : "+v"(vO)); SB();
  asm volatile("v_mfma_f32_16x16x32_bf16 %0, v[224:227], a[200:203], %0" : "+v"(g[1][2][0]) :  : ); SB();
  vO2 = vO + 15360 - 0u * 30720; asm volatile("" : "+v"(vO2)); SB();
  asm volatile("v_mfma_f32_16x16x32_bf16 %0, v[224:227], a[204:207], %0" : "+v"(g[1][3][0]) :  : ); SB();
  vFH = W_FH + rb * 256 + w * 64 + (lane & 7) * 8; asm volatile("" : "+v"(vFH)); SB();
  asm volatile("v_mfma_f32_16x16x32_bf16 %0, v[228:231], a[192:195], %0" : "+v"(g[1][0][1]) :  : ); SB();
  vHr = W_HH + (rb * 96 + w * 6) * 1024 + l16; asm volatile("" : "+v"(vHr)); SB();
  asm volatile("v_mfma_f32_16x16x32_bf16 %0, v[228:231], a[196:199], %0" : "+v"(g[1][1][1]) :  : ); SB();
  vX = (rb * 64 + w * 16 + l15) * 1280; asm volatile("" : "+v"(vX)); SB();
  asm volatile("v_mfma_f32_16x16x32_bf16 %0, v[228:231], a[200:203], %0" : "+v"(g[1][2][1]) :  : ); SB();
  vX = vX + cb * 160 + 8 * g4; asm volatile("" : "+v"(vX)); SB();
  asm volatile("v_mfma_f32_16x16x32_bf16 %0, v[228:231], a[204:207], %0" : "+v"(g[1][3][1]) :  : ); SB();
  sFH = W_FH + ((rb * 8 + cb) * 4 + w) * 8; asm volatile("" : "+s"(sFH)); SB();
  asm volatile("v_mfma_f32_16x16x32_bf16 %0, v[232:235], a[208:211], %0" : "+v"(g[1][0][0]) :  : ); SB();
  asm volatile("s_waitcnt vmcnt(13)" :  :  : "memory"); SB();
  asm volatile("ds_read_b128 a[108:111], %0 offset:38912" :  : "v"(aA[0]) : "memory"); SB();
  asm volatile("v_mfma_f32_16x16x32_bf16 %0, v[232:235], a[212:215], %0" : "+v"(g[1][1][0]) :  : ); SB();
  asm volatile("ds_read_b128 a[124:127], %0 offset:38912" :  : "v"(aA[1]) : "memory"); SB();
  asm volatile("v_mov_b32 %0, -1" : "=v"(ho[2][0]) :  : ); SB();
  asm volatile("v_mfma_f32_16x16x32_bf16 %0, v[232:235], a[216:219], %0" : "+v"(g[1][2][0]) :  : ); SB();
  asm volatile("buffer_load_dwordx4 %0, %1, %2 offen offset:1024 lds" :  : "v"(voS), "s"(rA), "s"(ao[1]) : "memory"); SB();
  asm volatile("v_mov_b32 %0, -1" : "=v"(ho[2][1]) :  : ); SB();
  asm volatile("v_mfma_f32_16x16x32_bf16 %0, v[232:235], a[220:223], %0" : "+v"(g[1][3][0]) :  : ); SB();
  asm volatile("buffer_load_dwordx4 %0, %1, %2 offen offset:2048 lds" :  : "v"(voS), "s"(rA), "s"(ao[2]) : "memory"); SB();
  asm volatile("v_mov_b32 %0, -1" : "=v"(ho[2][2]) :  : ); SB();
  asm volatile("v_mfma_f32_16x16x32_bf16 %0, v[236:239], a[208:211], %0" : "+v"(g[1][0][1]) :  : ); SB();
  asm volatile("v_mov_b32 %0, -1" : "=v"(ho[2][3]) :  : ); SB();
  asm volatile("v_mfma_f32_16x16x32_bf16 %0, v[236:239], a[212:215], %0" : "+v"(g[1][1][1]) :  : ); SB();
  hso[0] = (w ^ 0) * 24576 + 0u * 3072; asm volatile("" : "+s"(hso[0])); SB();
  hso[1] = hso[0] ^ 3072; asm volatile("" : "+s"(hso[1])); SB();
  asm volatile("v_mfma_f32_16x16x32_bf16 %0, v[236:239], a[216:219], %0" : "+v"(g[1][2][1]) :  : ); SB();
  hso[2] = (w ^ 1) * 24576 + 0u * 3072; asm volatile("" : "+s"(hso[2])); SB();
  hso[3] = hso[2] ^ 3072; asm volatile("" : "+s"(hso[3])); SB();
  asm volatile("v_mfma_f32_16x16x32_bf16 %0, v[236:239], a[220:223], %0" : "+v"(g[1][3][1]) :  : ); SB();
  hso[4] = (w ^ 2) * 24576 + 0u * 3072; asm volatile("" : "+s"(hso[4])); SB();
  asm volatile("v_mfma_f32_16x16x32_bf16 %0, v[240:243], a[224:227], %0" : "+v"(g[1][0][0]) :  : ); SB();
  hso[5] = hso[4] ^ 3072; asm volatile("" : "+s"(hso[5])); SB();
  hso[6] = (w ^ 3) * 24576 + 0u * 3072; asm volatile("" : "+s"(hso[6])); SB();
  asm volatile("v_mfma_f32_16x16x32_bf16 %0, v[240:243], a[228:231], %0" : "+v"(g[1][1][0]) :  : ); SB();
  hso[7] = hso[6] ^ 3072; asm volatile("" : "+s"(hso[7])); SB();
  asm volatile("buffer_load_dwordx4 %0, %1, %2 offen offset:3072 lds" :  : "v"(voS), "s"(rA), "s"(ao[3]) : "memory"); SB();
  asm volatile("v_mfma_f32_16x16x32_bf16 %0, v[240:243], a[232:235], %0" : "+v"(g[1][2][0]) :  : ); SB();
  asm volatile("s_add_u32 m0, %0, 102400" :  : "s"(mA) : "memory", "scc"); SB();
  asm volatile("v_mul_f32 %0, %1, %1" : "=v"(gt[3]) : "v"(a1[0][1][3]) : ); SB();
  asm volatile("v_mfma_f32_16x16x32_bf16 %0, v[240:243], a[236:239], %0" : "+v"(g[1][3][0]) :  : ); SB();
  asm volatile("buffer_load_dwordx4 %0, %1, %2 offen lds" :  : "v"(voS), "s"(rA), "s"(ao[4]) : "memory"); SB();
  asm volatile("v_fma_f32 %0, %0, %1, %2" : "+v"(gt[0]) : "v"(k2[0]), "v"(k1[0]) : ); SB();
  asm volatile("v_mfma_f32_16x16x32_bf16 %0, v[244:247], a[224:227], %0" : "+v"(g[1][0][1]) :  : ); SB();
  asm volatile("v_fma_f32 %0, %0, %1, %2" : "+v"(gt[1]) : "v"(k2[0]), "v"(k1[0]) : ); SB();
  asm volatile("v_mfma_f32_16x16x32_bf16 %0, v[244:247], a[228:231], %0" : "+v"(g[1][1][1]) :  : ); SB();
  asm volatile("v_fma_f32 %0, %0, %1, %2" : "+v"(gt[2]) : "v"(k2[0]), "v"(k1[0]) : ); SB();
  asm volatile("v_mfma_f32_16x16x32_bf16 %0, v[244:247], a[232:235], %0" : "+v"(g[1][2][1]) :  : ); SB();
  asm volatile("buffer_load_dwordx4 %0, %1, %2 offen offset:1024 lds" :  : "v"(voS), "s"(rA), "s"(ao[5]) : "memory"); SB();
  asm volatile("v_fma_f32 %0, %0, %1, %2" : "+v"(gt[3]) : "v"(k2[0]), "v"(k1[0]) : ); SB();
  asm volatile("v_mfma_f32_16x16x32_bf16 %0, v[244:247], a[236:239], %0" : "+v"(g[1][3][1]) :  : ); SB();
  asm volatile("v_mul_f32 %0, %0, %1" : "+v"(gt[0]) : "v"(a1[0][1][0]) : ); SB();
  asm volatile("v_mfma_f32_16x16x32_bf16 %0, v[248:251], a[240:243], %0" : "+v"(g[1][0][0]) :  : ); SB();
  asm volatile("v_mul_f32 %0, %0, %1" : "+v"(gt[1]) : "v"(a1[0][1][1]) : ); SB();
  asm volatile("v_mfma_f32_16x16x32_bf16 %0, v[248:251], a[244:247], %0" : "+v"(g[1][1][0]) :  : ); SB();
  asm volatile("buffer_load_dwordx4 %0, %1, %2 offen offset:2048 lds" :  : "v"(voS), "s"(rA), "s"(ao[6]) : "memory"); SB();
  asm volatile("v_mul_f32 %0, %0, %1" : "+v"(gt[2]) : "v"(a1[0][1][2]) : ); SB();
  asm volatile("v_mfma_f32_16x16x32_bf16 %0, v[248:251], a[248:251], %0" : "+v"(g[1][2][0]) :  : ); SB();
  asm volatile("v_mul_f32 %0, %0, %1" : "+v"(gt[3]) : "v"(a1[0][1][3]) : ); SB();
  asm volatile("v_mfma_f32_16x16x32_bf16 %0, v[248:251], a[252:255], %0" : "+v"(g[1][3][0]) :  : ); SB();
  asm volatile("v_exp_f32 %0, %1" : "=v"(ge[0]) : "v"(gt[0]) : ); SB();
  asm volatile("v_mfma_f32_16x16x32_bf16 %0, v[252:255], a[240:243], %0" : "+v"(g[1][0][1]) :  : ); SB();
  asm volatile("v_exp_f32 %0, %1" : "=v"(ge[1]) : "v"(gt[1]) : ); SB();
  asm volatile("v_mfma_f32_16x16x32_bf16 %0, v[252:255], a[244:247], %0" : "+v"(g[1][1][1]) :  : ); SB();
  asm volatile("buffer_load_dwordx4 %0, %1, %2 offen offset:3072 lds" :  : "v"(voS), "s"(rA), "s"(ao[7]) : "memory"); SB();
  asm volatile("v_exp_f32 %0, %1" : "=v"(ge[2]) : "v"(gt[2]) : ); SB();
  asm volatile("v_mfma_f32_16x16x32_bf16 %0, v[252:255], a[248:251], %0" : "+v"(g[1][2][1]) :  : ); SB();
  asm volatile("v_exp_f32 %0, %1" : "=v"(ge[3]) : "v"(gt[3]) : ); SB();
  asm volatile("v_mfma_f32_16x16x32_bf16 %0, v[252:255], a[252:255], %0" : "+v"(g[1][3][1]) :  : ); SB();
  asm volatile("v_fma_f32 %0, %0, %1, %1" : "+v"(ge[0]) : "v"(xd[0]) : ); SB();
  asm volatile("v_mfma_f32_16x16x32_bf16 %0, v[160:163], a[0:3], %0" : "+v"(g[0][0][0]) :  : ); SB();
  asm volatile("buffer_load_dwordx4 v[216:219], %0, %1, %2 offen offset:0 sc1" :  : "v"(vB), "s"(rWS), "s"(65536u) : "memory"); SB();
  asm volatile("v_fma_f32 %0, %0, %1, %1" : "+v"(ge[1]) : "v"(xd[0]) : ); SB();
  asm volatile("v_mfma_f32_16x16x32_bf16 %0, v[160:163], a[4:7], %0" : "+v"(g[0][1][0]) :  : ); SB();
  asm volatile("v_fma_f32 %0, %0, %1, %1" : "+v"(ge[2]) : "v"(xd[0]) : ); SB();
  asm volatile("v_mfma_f32_16x16x32_bf16 %0, v[160:163], a[8:11], %0" : "+v"(g[0][2][0]) :  : ); SB();
  asm volatile("v_fma_f32 %0, %0, %1, %1" : "+v"(ge[3]) : "v"(xd[0]) : ); SB();
  asm volatile("v_mfma_f32_16x16x32_bf16 %0, v[160:163], a[12:15], %0" : "+v"(g[0][3][0]) :  : ); SB();
  asm volatile("buffer_load_dwordx4 v[220:223], %0, %1, %2 offen offset:1024 sc1" :  : "v"(vB), "s"(rWS), "s"(65536u) : "memory"); SB();
  asm volatile("v_rcp_f32 %0, %1" : "=v"(gt[0]) : "v"(ge[0]) : ); SB();
  asm volatile("v_mfma_f32_16x16x32_bf16 %0, v[164:167], a[0:3], %0" : "+v"(g[0][0][1]) :  : ); SB();
  asm volatile("v_xor_b32 %0, 131072, %0" : "+v"(vB) :  : "memory"); SB();
  asm volatile("v_rcp_f32 %0, %1" : "=v"(gt[1]) : "v"(ge[1]) : ); SB();
  asm volatile("v_mfma_f32_16x16x32_bf16 %0, v[164:167], a[4:7], %0" : "+v"(g[0][1][1]) :  : ); SB();
  asm volatile("v_rcp_f32 %0, %1" : "=v"(gt[2]) : "v"(ge[2]) : ); SB();
  asm volatile("v_mfma_f32_16x16x32_bf16 %0, v[164:167], a[8:11], %0" : "+v"(g[0][2][1]) :  : ); SB();
  asm volatile("v_rcp_f32 %0, %1" : "=v"(gt[3]) : "v"(ge[3]) : ); SB();
  asm volatile("v_mfma_f32_16x16x32_bf16 %0, v[164:167], a[12:15], %0" : "+v"(g[0][3][1]) :  : ); SB();
  asm volatile("buffer_load_dwordx4 v[224:227], %0, %1, %2 offen offset:0 sc1" :  : "v"(vB), "s"(rWS), "s"(0u) : "memory"); SB();
  asm volatile("s_waitcnt vmcnt(21)" :  :  : "memory"); SB();
  asm volatile("v_mfma_f32_16x16x32_bf16 %0, v[168:171], a[16:19], %0" : "+v"(g[0][0][0]) :  : ); SB();
  asm volatile("v_mul_f32 %0, %0, %1" : "+v"(a1[0][1][0]) : "v"(gt[0]) : ); SB();
  asm volatile("v_mfma_f32_16x16x32_bf16 %0, v[168:171], a[20:23], %0" : "+v"(g[0][1][0]) :  : ); SB();
  asm volatile("v_mul_f32 %0, %0, %1" : "+v"(a1[0][1][1]) : "v"(gt[1]) : ); SB();
  asm volatile("v_mfma_f32_16x16x32_bf16 %0, v[168:171], a[24:27], %0" : "+v"(g[0][2][0]) :  : ); SB();
  asm volatile("buffer_load_dwordx4 v[228:231], %0, %1, %2 offen offset:1024 sc1" :  : "v"(vB), "s"(rWS), "s"(0u) : "memory"); SB();
  asm volatile("v_mul_f32 %0, %0, %1" : "+v"(a1[0][1][2]) : "v"(gt[2]) : ); SB();
  asm volatile("v_mfma_f32_16x16x32_bf16 %0, v[168:171], a[28:31], %0" : "+v"(g[0][3][0]) :  : ); SB();
  asm volatile("v_mul_f32 %0, %0, %1" : "+v"(a1[0][1][3]) : "v"(gt[3]) : ); SB();
  asm volatile("v_mfma_f32_16x16x32_bf16 %0, v[172:175], a[16:19], %0" : "+v"(g[0][0][1]) :  : ); SB();
  asm volatile("v_mul_f32 %0, %1, %1" : "=v"(gt[0]) : "v"(a1[0][2][0]) : ); SB();
  asm volatile("v_mfma_f32_16x16x32_bf16 %0, v[172:175], a[20:23], %0" : "+v"(g[0][1][1]) :  : ); SB();
  asm volatile("v_mul_f32 %0, %1, %1" : "=v"(gt[1]) : "v"(a1[0][2][1]) : ); SB();
  asm volatile("v_mfma_f32_16x16x32_bf16 %0, v[172:175], a[24:27], %0" : "+v"(g[0][2][1]) :  : ); SB();
  asm volatile("buffer_load_dwordx4 v[232:235], %0, %1, %2 offen offset:0 sc1" :  : "v"(vB), "s"(rWS), "s"(65536u) : "memory"); SB();
  asm volatile("v_mul_f32 %0, %1, %1" : "=v"(gt[2]) : "v"(a1[0][2][2]) : ); SB();
  asm volatile("v_mfma_f32_16x16x32_bf16 %0, v[172:175], a[28:31], %0" : "+v"(g[0][3][1]) :  : ); SB();
  asm volatile("v_mul_f32 %0, %1, %1" : "=v"(gt[3]) : "v"(a1[0][2][3]) : ); SB();
  asm volatile("s_waitcnt vmcnt(21)" :  :  : "memory"); SB();
  asm volatile("v_mfma_f32_16x16x32_bf16 %0, v[176:179], a[32:35], %0" : "+v"(g[0][0][0]) :  : ); SB();
  asm volatile("v_fma_f32 %0, %0, %1, %2" : "+v"(gt[0]) : "v"(k2[0]), "v"(k1[0]) : ); SB();
  asm volatile("v_mfma_f32_16x16x32_bf16 %0, v[176:179], a[36:39], %0" : "+v"(g[0][1][0]) :  : ); SB();
  asm volatile("buffer_load_dwordx4 v[236:239], %0, %1, %2 offen offset:1024 sc1" :  : "v"(vB), "s"(rWS), "s"(65536u) : "memory"); SB();
  asm volatile("v_fma_f32 %0, %0, %1, %2" : "+v"(gt[1]) : "v"(k2[0]), "v"(k1[0]) : ); SB();
  asm volatile("v_mfma_f32_16x16x32_bf16 %0, v[176:179], a[40:43], %0" : "+v"(g[0][2][0]) :  : ); SB();
  asm volatile("v_xor_b32 %0, 524288, %0" : "+v"(vB) :  : "memory"); SB();
  asm volatile("v_fma_f32 %0, %0, %1, %2" : "+v"(gt[2]) : "v"(k2[0]), "v"(k1[0]) : ); SB();
  asm volatile("v_mfma_f32_16x16x32_bf16 %0, v[176:179], a[44:47], %0" : "+v"(g[0][3][0]) :  : ); SB();
  asm volatile("v_fma_f32 %0, %0, %1, %2" : "+v"(gt[3]) : "v"(k2[0]), "v"(k1[0]) : ); SB();
  asm volatile("v_mfma_f32_16x16x32_bf16 %0, v[180:183], a[32:35], %0" : "+v"(g[0][0][1]) :  : ); SB();
  asm volatile("v_mul_f32 %0, %0, %1" : "+v"(gt[0]) : "v"(a1[0][2][0]) : ); SB();
  asm volatile("v_mfma_f32_16x16x32_bf16 %0, v[180:183], a[36:39], %0" : "+v"(g[0][1][1]) :  : ); SB();
  asm volatile("buffer_load_dwordx4 v[240:243], %0, %1, %2 offen offset:0 sc1" :  : "v"(vB), "s"(rWS), "s"(2048u) : "memory"); SB();
  asm volatile("v_mul_f32 %0, %0, %1" : "+v"(gt[1]) : "v"(a1[0][2][1]) : ); SB();
  asm volatile("v_mfma_f32_16x16x32_bf16 %0, v[180:183], a[40:43], %0" : "+v"(g[0][2][1]) :  : ); SB();
  asm volatile("v_mul_f32 %0, %0, %1" : "+v"(gt[2]) : "v"(a1[0][2][2]) : ); SB();
  asm volatile("v_mfma_f32_16x16x32_bf16 %0, v[180:183], a[44:47], %0" : "+v"(g[0][3][1]) :  : ); SB();
  asm volatile("v_mul_f32 %0, %0, %1" : "+v"(gt[3]) : "v"(a1[0][2][3]) : ); SB();
  asm volatile("s_waitcnt vmcnt(21)" :  :  : "memory"); SB();
  asm volatile("v_mfma_f32_16x16x32_bf16 %0, v[184:187], a[48:51], %0" : "+v"(g[0][0][0]) :  : ); SB();
  asm volatile("v_exp_f32 %0, %1" : "=v"(ge[0]) : "v"(gt[0]) : ); SB();
  asm volatile("v_mfma_f32_16x16x32_bf16 %0, v[184:187], a[52:55], %0" : "+v"(g[0][1][0]) :  : ); SB();
  asm volatile("v_exp_f32 %0, %1" : "=v"(ge[1]) : "v"(gt[1]) : ); SB();
  asm volatile("v_mfma_f32_16x16x32_bf16 %0, v[184:187], a[56:59], %0" : "+v"(g[0][2][0]) :  : ); SB();
  asm volatile("v_exp_f32 %0, %1" : "=v"(ge[2]) : "v"(gt[2]) : ); SB();
  asm volatile("v_mfma_f32_16x16x32_bf16 %0, v[184:187], a[60:63], %0" : "+v"(g[0][3][0]) :  : ); SB();
  asm volatile("s_waitcnt vmcnt(13)" :  :  : "memory"); SB();
  asm volatile("ds_read_b128 a[128:131], %0 offset:0" :  : "v"(aA[0]) : "memory"); SB();
  asm volatile("v_mfma_f32_16x16x32_bf16 %0, v[188:191], a[48:51], %0" : "+v"(g[0][0][1]) :  : ); SB();
  asm volatile("ds_read_b128 a[144:147], %0 offset:0" :  : "v"(aA[1]) : "memory"); SB();
  asm volatile("v_exp_f32 %0, %1" : "=v"(ge[3]) : "v"(gt[3]) : ); SB();
  asm volatile("v_mfma_f32_16x16x32_bf16 %0, v[188:191], a[52:55], %0" : "+v"(g[0][1][1]) :  : ); SB();
  asm volatile("buffer_load_dwordx4 v[244:247], %0, %1, %2 offen offset:1024 sc1" :  : "v"(vB), "s"(rWS), "s"(2048u) : "memory"); SB();
  asm volatile("v_fma_f32 %0, %0, %1, %1" : "+v"(ge[0]) : "v"(xd[0]) : ); SB();
  asm volatile("v_mfma_f32_16x16x32_bf16 %0, v[188:191], a[56:59], %0" : "+v"(g[0][2][1]) :  : ); SB();
  asm volatile("buffer_load_dwordx4 v[248:251], %0, %1, %2 offen offset:0 sc1" :  : "v"(vB), "s"(rWS), "s"(67584u) : "memory"); SB();
  asm volatile("v_fma_f32 %0, %0, %1, %1" : "+v"(ge[1]) : "v"(xd[0]) : ); SB();
  asm volatile("v_mfma_f32_16x16x32_bf16 %0, v[188:191], a[60:63], %0" : "+v"(g[0][3][1]) :  : ); SB();
  asm volatile("v_fma_f32 %0, %0, %1, %1" : "+v"(ge[2]) : "v"(xd[0]) : ); SB();
  ST(27);
  asm volatile("v_mfma_f32_16x16x32_bf16 %0, v[192:195], a[64:67], %0" : "+v"(g[0][0][0]) :  : ); SB();
  asm volatile("v_fma_f32 %0, %0, %1, %1" : "+v"(ge[3]) : "v"(xd[0]) : ); SB();
  asm volatile("v_mfma_f32_16x16x32_bf16 %0, v[192:195], a[68:71], %0" : "+v"(g[0][1][0]) :  : ); SB();
  asm volatile("v_rcp_f32 %0, %1" : "=v"(gt[0]) : "v"(ge[0]) : ); SB();
  asm volatile("v_mfma_f32_16x16x32_bf16 %0, v[192:195], a[72:75], %0" : "+v"(g[0][2][0]) :  : ); SB();
  asm volatile("s_waitcnt vmcnt(13)" :  :  : "memory"); SB();
  asm volatile("ds_read_b128 a[132:135], %0 offset:2048" :  : "v"(aA[0]) : "memory"); SB();
  asm volatile("v_mfma_f32_16x16x32_bf16 %0, v[192:195], a[76:79], %0" : "+v"(g[0][3][0]) :  : ); SB();
  asm volatile("ds_read_b128 a[148:151], %0 offset:2048" :  : "v"(aA[1]) : "memory"); SB();
  asm volatile("v_rcp_f32 %0, %1" : "=v"(gt[1]) : "v"(ge[1]) : ); SB();
  asm volatile("v_mfma_f32_16x16x32_bf16 %0, v[196:199], a[64:67], %0" : "+v"(g[0][0][1]) :  : ); SB();
  asm volatile("buffer_load_dwordx4 v[252:255], %0, %1, %2 offen offset:1024 sc1" :  : "v"(vB), "s"(rWS), "s"(67584u) : "memory"); SB();
  asm volatile("v_rcp_f32 %0, %1" : "=v"(gt[2]) : "v"(ge[2]) : ); SB();
  asm volatile("v_mfma_f32_16x16x32_bf16 %0, v[196:199], a[68:71], %0" : "+v"(g[0][1][1]) :  : ); SB();
  asm volatile("v_xor_b32 %0, 917504, %0" : "+v"(vB) :  : "memory"); SB();
  asm volatile("v_rcp_f32 %0, %1" : "=v"(gt[3]) : "v"(ge[3]) : ); SB();
  asm volatile("v_mfma_f32_16x16x32_bf16 %0, v[196:199], a[72:75], %0" : "+v"(g[0][2][1]) :  : ); SB();
  asm volatile("buffer_load_dwordx4 v[160:163], %0, %1, %2 offen offset:0 sc1" :  : "v"(vB), "s"(rWS), "s"(2048u) : "memory"); SB();
  asm volatile("v_mul_f32 %0, %0, %1" : "+v"(a1[0][2][0]) : "v"(gt[0]) : ); SB();
  asm volatile("v_mfma_f32_16x16x32_bf16 %0, v[196:199], a[76:79], %0" : "+v"(g[0][3][1]) :  : ); SB();
  asm volatile("v_mul_f32 %0, %0, %1" : "+v"(a1[0][2][1]) : "v"(gt[1]) : ); SB();
  asm volatile("v_mfma_f32_16x16x32_bf16 %0, v[200:203], a[80:83], %0" : "+v"(g[0][0][0]) :  : ); SB();
  asm volatile("v_mul_f32 %0, %0, %1" : "+v"(a1[0][2][2]) : "v"(gt[2]) : ); SB();
  asm volatile("v_mfma_f32_16x16x32_bf16 %0, v[200:203], a[84:87], %0" : "+v"(g[0][1][0]) :  : ); SB();
  asm volatile("s_waitcnt vmcnt(13)" :  :  : "memory"); SB();
  asm volatile("ds_read_b128 a[136:139], %0 offset:4096" :  : "v"(aA[0]) : "memory"); SB();
  asm volatile("v_mfma_f32_16x16x32_bf16 %0, v[200:203], a[88:91], %0" : "+v"(g[0][2][0]) :  : ); SB();
  asm volatile("ds_read_b128 a[152:155], %0 offset:4096" :  : "v"(aA[1]) : "memory"); SB();
  asm volatile("v_mul_f32 %0, %0, %1" : "+v"(a1[0][2][3]) : "v"(gt[3]) : ); SB();
  asm volatile("v_mfma_f32_16x16x32_bf16 %0, v[200:203], a[92:95], %0" : "+v"(g[0][3][0]) :  : ); SB();
  asm volatile("buffer_load_dwordx4 v[164:167], %0, %1, %2 offen offset:1024 sc1" :  : "v"(vB), "s"(rWS), "s"(2048u) : "memory"); SB();
  asm volatile("v_mul_f32 %0, %1, %1" : "=v"(gt[0]) : "v"(a1[1][0][0]) : ); SB();
  asm volatile("v_mfma_f32_16x16x32_bf16 %0, v[204:207], a[80:83], %0" : "+v"(g[0][0][1]) :  : ); SB();
  asm volatile("buffer_load_dwordx4 v[168:171], %0, %1, %2 offen offset:0 sc1" :  : "v"(vB), "s"(rWS), "s"(67584u) : "memory"); SB();
  asm volatile("v_mul_f32 %0, %1, %1" : "=v"(gt[1]) : "v"(a1[1][0][1]) : ); SB();
  asm volatile("v_mfma_f32_16x16x32_bf16 %0, v[204:207], a[84:87], %0" : "+v"(g[0][1][1]) :  : ); SB();
  asm volatile("v_mul_f32 %0, %1, %1" : "=v"(gt[2]) : "v"(a1[1][0][2]) : ); SB();
  asm volatile("v_mfma_f32_16x16x32_bf16 %0, v[204:207], a[88:91], %0" : "+v"(g[0][2][1]) :  : ); SB();
  asm volatile("v_mul_f32 %0, %1, %1" : "=v"(gt[3]) : "v"(a1[1][0][3]) : ); SB();
  asm volatile("v_mfma_f32_16x16x32_bf16 %0, v[204:207], a[92:95], %0" : "+v"(g[0][3][1]) :  : ); SB();
  asm volatile("s_waitcnt vmcnt(13)" :  :  : "memory"); SB();
  asm volatile("v_fma_f32 %0, %0, %1, %2" : "+v"(gt[0]) : "v"(k2[0]), "v"(k1[0]) : ); SB();
  asm volatile("v_mfma_f32_16x16x32_bf16 %0, v[208:211], a[96:99], %0" : "+v"(g[0][0][0]) :  : ); SB();
  asm volatile("ds_read_b128 a[140:143], %0 offset:6144" :  : "v"(aA[0]) : "memory"); SB();
  asm volatile("v_fma_f32 %0, %0, %1, %2" : "+v"(gt[1]) : "v"(k2[0]), "v"(k1[0]) : ); SB();
  asm volatile("v_mfma_f32_16x16x32_bf16 %0, v[208:211], a[100:103], %0" : "+v"(g[0][1][0]) :  : ); SB();
  asm volatile("ds_read_b128 a[156:159], %0 offset:6144" :  : "v"(aA[1]) : "memory"); SB();
  asm volatile("s_waitcnt lgkmcnt(4)\n\tv_mfma_f32_16x16x32_bf16 %0, v[208:211], a[104:107], %0" : "+v"(g[0][2][0]) :  : ); SB();
  asm volatile("buffer_load_dwordx4 v[172:175], %0, %1, %2 offen offset:1024 sc1" :  : "v"(vB), "s"(rWS), "s"(67584u) : "memory"); SB();
  asm volatile("v_fma_f32 %0, %0, %1, %2" : "+v"(gt[2]) : "v"(k2[0]), "v"(k1[0]) : ); SB();
  asm volatile("v_mfma_f32_16x16x32_bf16 %0, v[208:211], a[108:111], %0" : "+v"(g[0][3][0]) :  : ); SB();
  asm volatile("v_xor_b32 %0, 131072, %0" : "+v"(vB) :  : "memory"); SB();
  asm volatile("v_fma_f32 %0, %0, %1, %2" : "+v"(gt[3]) : "v"(k2[0]), "v"(k1[0]) : ); SB();
  asm volatile("v_mfma_f32_16x16x32_bf16 %0, v[212:215], a[96:99], %0" : "+v"(g[0][0][1]) :  : ); SB();
  asm volatile("buffer_load_dwordx4 v[176:179], %0, %1, %2 offen offset:0 sc1" :  : "v"(vB), "s"(rWS), "s"(2048u) : "memory"); SB();
  asm volatile("v_mul_f32 %0, %0, %1" : "+v"(gt[0]) : "v"(a1[1][0][0]) : ); SB();
  asm volatile("v_mfma_f32_16x16x32_bf16 %0, v[212:215], a[100:103], %0" : "+v"(g[0][1][1]) :  : ); SB();
  asm volatile("v_mul_f32 %0, %0, %1" : "+v"(gt[1]) : "v"(a1[1][0][1]) : ); SB();
  asm volatile("v_mfma_f32_16x16x32_bf16 %0, v[212:215], a[104:107], %0" : "+v"(g[0][2][1]) :  : ); SB();
  asm volatile("buffer_load_dwordx4 v[180:183], %0, %1, %2 offen offset:1024 sc1" :  : "v"(vB), "s"(rWS), "s"(2048u) : "memory"); SB();
  asm volatile("v_mul_f32 %0, %0, %1" : "+v"(gt[2]) : "v"(a1[1][0][2]) : ); SB();
  asm volatile("v_mfma_f32_16x16x32_bf16 %0, v[212:215], a[108:111], %0" : "+v"(g[0][3][1]) :  : ); SB();
  asm volatile("v_mul_f32 %0, %0, %1" : "+v"(gt[3]) : "v"(a1[1][0][3]) : ); SB();
  asm volatile("s_waitcnt vmcnt(14)" :  :  : "memory"); SB();
  asm volatile("v_mfma_f32_16x16x32_bf16 %0, v[216:219], a[112:115], %0" : "+v"(g[0][0][0]) :  : ); SB();
  asm volatile("v_exp_f32 %0, %1" : "=v"(ge[0]) : "v"(gt[0]) : ); SB();
  asm volatile("v_mfma_f32_16x16x32_bf16 %0, v[216:219], a[116:119], %0" : "+v"(g[0][1][0]) :  : ); SB();
  asm volatile("v_exp_f32 %0, %1" : "=v"(ge[1]) : "v"(gt[1]) : ); SB();
  asm volatile("v_mfma_f32_16x16x32_bf16 %0, v[216:219], a[120:123], %0" : "+v"(g[0][2][0]) :  : ); SB();
  asm volatile("buffer_load_dwordx4 v[184:187], %0, %1, %2 offen offset:0 sc1" :  : "v"(vB), "s"(rWS), "s"(67584u) : "memory"); SB();
  asm volatile("v_exp_f32 %0, %1" : "=v"(ge[2]) : "v"(gt[2]) : ); SB();
  asm volatile("v_mfma_f32_16x16x32_bf16 %0, v[216:219], a[124:127], %0" : "+v"(g[0][3][0]) :  : ); SB();
  asm volatile("v_exp_f32 %0, %1" : "=v"(ge[3]) : "v"(gt[3]) : ); SB();
  asm volatile("v_mfma_f32_16x16x32_bf16 %0, v[220:223], a[112:115], %0" : "+v"(g[0][0][1]) :  : ); SB();
  asm volatile("v_fma_f32 %0, %0, %1, %1" : "+v"(ge[0]) : "v"(xd[0]) : ); SB();
  asm volatile("v_mfma_f32_16x16x32_bf16 %0, v[220:223], a[116:119], %0" : "+v"(g[0][1][1]) :  : ); SB();
  asm volatile("buffer_load_dwordx4 v[188:191], %0, %1, %2 offen offset:1024 sc1" :  : "v"(vB), "s"(rWS), "s"(67584u) : "memory"); SB();
  asm volatile("v_fma_f32 %0, %0, %1, %1" : "+v"(ge[1]) : "v"(xd[0]) : ); SB();
  asm volatile("v_mfma_f32_16x16x32_bf16 %0, v[220:223], a[120:123], %0" : "+v"(g[0][2][1]) :  : ); SB();
  asm volatile("v_xor_b32 %0, 393216, %0" : "+v"(vB) :  : "memory"); SB();
  asm volatile("v_fma_f32 %0, %0, %1, %1" : "+v"(ge[2]) : "v"(xd[0]) : ); SB();
  asm volatile("v_mfma_f32_16x16x32_bf16 %0, v[220:223], a[124:127], %0" : "+v"(g[0][3][1]) :  : ); SB();
  asm volatile("v_fma_f32 %0, %0, %1, %1" : "+v"(ge[3]) : "v"(xd[0]) : ); SB();
  asm volatile("s_waitcnt vmcnt(14)" :  :  : "memory"); SB();
  asm volatile("v_mfma_f32_16x16x32_bf16 %0, v[224:227], a[128:131], %0" : "+v"(g[0][0][0]) :  : ); SB();
  asm volatile("v_rcp_f32 %0, %1" : "=v"(gt[0]) : "v"(ge[0]) : ); SB();
  asm volatile("v_mfma_f32_16x16x32_bf16 %0, v[224:227], a[132:135], %0" : "+v"(g[0][1][0]) :  : ); SB();
  asm volatile("buffer_load_dwordx4 v[192:195], %0, %1, %2 offen offset:0 sc1" :  : "v"(vB), "s"(rWS), "s"(2048u) : "memory"); SB();
  asm volatile("s_waitcnt lgkmcnt(0)\n\tv_mfma_f32_16x16x32_bf16 %0, v[224:227], a[136:139], %0" : "+v"(g[0][2][0]) :  : ); SB();
  asm volatile("v_rcp_f32 %0, %1" : "=v"(gt[1]) : "v"(ge[1]) : ); SB();
  asm volatile("v_mfma_f32_16x16x32_bf16 %0, v[224:227], a[140:143], %0" : "+v"(g[0][3][0]) :  : ); SB();
  asm volatile("v_rcp_f32 %0, %1" : "=v"(gt[2]) : "v"(ge[2]) : ); SB();
  asm volatile("v_mfma_f32_16x16x32_bf16 %0, v[228:231], a[128:131], %0" : "+v"(g[0][0][1]) :  : ); SB();
  asm volatile("buffer_load_dwordx4 v[196:199], %0, %1, %2 offen offset:1024 sc1" :  : "v"(vB), "s"(rWS), "s"(2048u) : "memory"); SB();
  asm volatile("v_rcp_f32 %0, %1" : "=v"(gt[3]) : "v"(ge[3]) : ); SB();
  asm volatile("v_mfma_f32_16x16x32_bf16 %0, v[228:231], a[132:135], %0" : "+v"(g[0][1][1]) :  : ); SB();
  asm volatile("v_mul_f32 %0, %0, %1" : "+v"(a1[1][0][0]) : "v"(gt[0]) : ); SB();
  asm volatile("v_mfma_f32_16x16x32_bf16 %0, v[228:231], a[136:139], %0" : "+v"(g[0][2][1]) :  : ); SB();
  asm volatile("v_mul_f32 %0, %0, %1" : "+v"(a1[1][0][1]) : "v"(gt[1]) : ); SB();
  asm volatile("v_mfma_f32_16x16x32_bf16 %0, v[228:231], a[140:143], %0" : "+v"(g[0][3][1]) :  : ); SB();
  asm volatile("v_mul_f32 %0, %0, %1" : "+v"(a1[1][0][2]) : "v"(gt[2]) : ); SB();
  asm volatile("s_waitcnt vmcnt(14)" :  :  : "memory"); SB();
  asm volatile("v_mfma_f32_16x16x32_bf16 %0, v[232:235], a[144:147], %0" : "+v"(g[0][0][0]) :  : ); SB();
  asm volatile("buffer_load_dwordx4 v[200:203], %0, %1, %2 offen offset:0 sc1" :  : "v"(vB), "s"(rWS), "s"(67584u) : "memory"); SB();
  asm volatile("v_mul_f32 %0, %0, %1" : "+v"(a1[1][0][3]) : "v"(gt[3]) : ); SB();
  asm volatile("v_mfma_f32_16x16x32_bf16 %0, v[232:235], a[148:151], %0" : "+v"(g[0][1][0]) :  : ); SB();
  asm volatile("v_mul_f32 %0, %1, %1" : "=v"(gt[0]) : "v"(a1[1][1][0]) : ); SB();
  asm volatile("v_mfma_f32_16x16x32_bf16 %0, v[232:235], a[152:155], %0" : "+v"(g[0][2][0]) :  : ); SB();
  asm volatile("v_mul_f32 %0, %1, %1" : "=v"(gt[1]) : "v"(a1[1][1][1]) : ); SB();
  asm volatile("v_mfma_f32_16x16x32_bf16 %0, v[232:235], a[156:159], %0" : "+v"(g[0][3][0]) :  : ); SB();
  asm volatile("v_mul_f32 %0, %1, %1" : "=v"(gt[2]) : "v"(a1[1][1][2]) : ); SB();
  asm volatile("v_mfma_f32_16x16x32_bf16 %0, v[236:239], a[144:147], %0" : "+v"(g[0][0][1]) :  : ); SB();
  asm volatile("v_mul_f32 %0, %1, %1" : "=v"(gt[3]) : "v"(a1[1][1][3]) : ); SB();
  asm volatile("v_mfma_f32_16x16x32_bf16 %0, v[236:239], a[148:151], %0" : "+v"(g[0][1][1]) :  : ); SB();
  asm volatile("buffer_load_dwordx4 v[204:207], %0, %1, %2 offen offset:1024 sc1" :  : "v"(vB), "s"(rWS), "s"(67584u) : "memory"); SB();
  asm volatile("v_fma_f32 %0, %0, %1, %2" : "+v"(gt[0]) : "v"(k2[0]), "v"(k1[0]) : ); SB();
  asm volatile("v_mfma_f32_16x16x32_bf16 %0, v[236:239], a[152:155], %0" : "+v"(g[0][2][1]) :  : ); SB();
  asm volatile("v_xor_b32 %0, 131072, %0" : "+v"(vB) :  : "memory"); SB();
  asm volatile("v_fma_f32 %0, %0, %1, %2" : "+v"(gt[1]) : "v"(k2[0]), "v"(k1[0]) : ); SB();
  asm volatile("v_mfma_f32_16x16x32_bf16 %0, v[236:239], a[156:159], %0" : "+v"(g[0][3][1]) :  : ); SB();
  asm volatile("v_fma_f32 %0, %0, %1, %2" : "+v"(gt[2]) : "v"(k2[0]), "v"(k1[0]) : ); SB();
  asm volatile("s_waitcnt vmcnt(14)" :  :  : "memory"); SB();
  asm volatile("v_mfma_f32_16x16x32_bf16 %0, v[240:243], a[0:3], %0" : "+v"(g[1][0][0]) :  : ); SB();
  asm volatile("s_barrier" :  :  : "memory"); SB();
  asm volatile("buffer_load_dwordx4 v[208:211], %0, %1, %2 offen offset:0 sc1" :  : "v"(vB), "s"(rWS), "s"(2048u) : "memory"); SB();
  asm volatile("v_mfma_f32_16x16x32_bf16 %0, v[240:243], a[4:7], %0" : "+v"(g[1][1][0]) :  : ); SB();
  asm volatile("v_fma_f32 %0, %0, %1, %2" : "+v"(gt[3]) : "v"(k2[0]), "v"(k1[0]) : ); SB();
  asm volatile("v_mfma_f32_16x16x32_bf16 %0, v[240:243], a[8:11], %0" : "+v"(g[1][2][0]) :  : ); SB();
  asm volatile("ds_write_b128 %0, %1 offset:0" :  : "v"(pw2), "v"(g[0][1][0]) : "memory"); SB();
  asm volatile("v_mfma_f32_16x16x32_bf16 %0, v[240:243], a[12:15], %0" : "+v"(g[1][3][0]) :  : ); SB();
  asm volatile("buffer_load_dwordx4 v[212:215], %0, %1, %2 offen offset:1024 sc1" :  : "v"(vB), "s"(rWS), "s"(2048u) : "memory"); SB();
  asm volatile("v_mul_f32 %0, %0, %1" : "+v"(gt[0]) : "v"(a1[1][1][0]) : ); SB();
  asm volatile("v_mfma_f32_16x16x32_bf16 %0, v[244:247], a[0:3], %0" : "+v"(g[1][0][1]) :  : ); SB();
  asm volatile("v_mul_f32 %0, %0, %1" : "+v"(gt[1]) : "v"(a1[1][1][1]) : ); SB();
  asm volatile("v_mfma_f32_16x16x32_bf16 %0, v[244:247], a[4:7], %0" : "+v"(g[1][1][1]) :  : ); SB();
  asm volatile("ds_write_b128 %0, %1 offset:1024" :  : "v"(pw2), "v"(g[0][1][1]) : "memory"); SB();
  asm volatile("v_mfma_f32_16x16x32_bf16 %0, v[244:247], a[8:11], %0" : "+v"(g[1][2][1]) :  : ); SB();
  asm volatile("buffer_load_dwordx4 v[216:219], %0, %1, %2 offen offset:0 sc1" :  : "v"(vB), "s"(rWS), "s"(67584u) : "memory"); SB();
  asm volatile("v_mul_f32 %0, %0, %1" : "+v"(gt[2]) : "v"(a1[1][1][2]) : ); SB();
  asm volatile("v_mfma_f32_16x16x32_bf16 %0, v[244:247], a[12:15], %0" : "+v"(g[1][3][1]) :  : ); SB();
  asm volatile("v_mul_f32 %0, %0, %1" : "+v"(gt[3]) : "v"(a1[1][1][3]) : ); SB();
  asm volatile("s_waitcnt vmcnt(15)" :  :  : "memory"); SB();
  asm volatile("v_mfma_f32_16x16x32_bf16 %0, v[248:251], a[16:19], %0" : "+v"(g[1][0][0]) :  : ); SB();
  asm volatile("ds_write_b128 %0, %1 offset:2048" :  : "v"(pw2), "v"(g[0][2][0]) : "memory"); SB();
  asm volatile("v_mfma_f32_16x16x32_bf16 %0, v[248:251], a[20:23], %0" : "+v"(g[1][1][0]) :  : ); SB();
  asm volatile("buffer_store_dwordx4 %1, %0, %2, %3 offen offset:2048 sc1\n\ts_nop 1" :  : "v"(vH_), "v"(ho[2]), "s"(rWS), "s"(0u) : "memory"); SB();
  asm volatile("v_exp_f32 %0, %1" : "=v"(ge[0]) : "v"(gt[0]) : ); SB();
  asm volatile("v_mfma_f32_16x16x32_bf16 %0, v[248:251], a[24:27], %0" : "+v"(g[1][2][0]) :  : ); SB();
  asm volatile("buffer_load_dwordx4 v[220:223], %0, %1, %2 offen offset:1024 sc1" :  : "v"(vB), "s"(rWS), "s"(67584u) : "memory"); SB();
  asm volatile("v_exp_f32 %0, %1" : "=v"(ge[1]) : "v"(gt[1]) : ); SB();
  asm volatile("v_mfma_f32_16x16x32_bf16 %0, v[248:251], a[28:31], %0" : "+v"(g[1][3][0]) :  : ); SB();
  asm volatile("ds_write_b128 %0, %1 offset:3072" :  : "v"(pw2), "v"(g[0][2][1]) : "memory"); SB();
  asm volatile("v_mfma_f32_16x16x32_bf16 %0, v[252:255], a[16:19], %0" : "+v"(g[1][0][1]) :  : ); SB();
  asm volatile("v_xor_b32 %0, 917504, %0" : "+v"(vB) :  : "memory"); SB();
  asm volatile("v_exp_f32 %0, %1" : "=v"(ge[2]) : "v"(gt[2]) : ); SB();
  asm volatile("v_mfma_f32_16x16x32_bf16 %0, v[252:255], a[20:23], %0" : "+v"(g[1][1][1]) :  : ); SB();
  asm volatile("buffer_load_dwordx4 v[224:227], %0, %1, %2 offen offset:0 sc1" :  : "v"(vB), "s"(rWS), "s"(4096u) : "memory"); SB();
  asm volatile("v_exp_f32 %0, %1" : "=v"(ge[3]) : "v"(gt[3]) : ); SB();
  asm volatile("v_mfma_f32_16x16x32_bf16 %0, v[252:255], a[24:27], %0" : "+v"(g[1][2][1]) :  : ); SB();
  asm volatile("ds_write_b128 %0, %1 offset:4096" :  : "v"(pw2), "v"(g[0][3][0]) : "memory"); SB();
  asm volatile("v_mfma_f32_16x16x32_bf16 %0, v[252:255], a[28:31], %0" : "+v"(g[1][3][1]) :  : ); SB();
  asm volatile("buffer_load_dwordx4 v[228:231], %0, %1, %2 offen offset:1024 sc1" :  : "v"(vB), "s"(rWS), "s"(4096u) : "memory"); SB();
  ST(28);
  asm volatile("s_waitcnt vmcnt(17)" :  :  : "memory"); SB();
  asm volatile("v_mfma_f32_16x16x32_bf16 %0, v[160:163], a[32:35], %0" : "+v"(g[1][0][0]) :  : ); SB();
  asm volatile("v_fma_f32 %0, %0, %1, %1" : "+v"(ge[0]) : "v"(xd[0]) : ); SB();
  asm volatile("v_mfma_f32_16x16x32_bf16 %0, v[160:163], a[36:39], %0" : "+v"(g[1][1][0]) :  : ); SB();
  asm volatile("ds_write_b128 %0, %1 offset:5120" :  : "v"(pw2), "v"(g[0][3][1]) : "memory"); SB();
  asm volatile("v_mfma_f32_16x16x32_bf16 %0, v[160:163], a[40:43], %0" : "+v"(g[1][2][0]) :  : ); SB();
  asm volatile("buffer_load_dwordx4 v[232:235], %0, %1, %2 offen offset:0 sc1" :  : "v"(vB), "s"(rWS), "s"(69632u) : "memory"); SB();
  asm volatile("v_fma_f32 %0, %0, %1, %1" : "+v"(ge[1]) : "v"(xd[0]) : ); SB();
  asm volatile("v_mfma_f32_16x16x32_bf16 %0, v[160:163], a[44:47], %0" : "+v"(g[1][3][0]) :  : ); SB();
  asm volatile("v_fma_f32 %0, %0, %1, %1" : "+v"(ge[2]) : "v"(xd[0]) : ); SB();
  asm volatile("v_mfma_f32_16x16x32_bf16 %0, v[164:167], a[32:35], %0" : "+v"(g[1][0][1]) :  : ); SB();
  asm volatile("s_waitcnt lgkmcnt(0)\n\ts_barrier" :  :  : "memory"); SB();
  asm volatile("ds_read_b128 %0, %1 offset:0" : "=&v"(pr[0]) : "v"(pe[0]) : "memory"); SB();
  asm volatile("v_mfma_f32_16x16x32_bf16 %0, v[164:167], a[36:39], %0" : "+v"(g[1][1][1]) :  : ); SB();
  asm volatile("ds_read_b128 %0, %1 offset:1024" : "=&v"(pr[1]) : "v"(pe[0]) : "memory"); SB();
  asm volatile("v_fma_f32 %0, %0, %1, %1" : "+v"(ge[3]) : "v"(xd[0]) : ); SB();
  asm volatile("v_mfma_f32_16x16x32_bf16 %0, v[164:167], a[40:43], %0" : "+v"(g[1][2][1]) :  : ); SB();
  asm volatile("ds_read_b128 %0, %1 offset:2048" : "=&v"(pr[2]) : "v"(pe[1]) : "memory"); SB();
  asm volatile("v_rcp_f32 %0, %1" : "=v"(gt[0]) : "v"(ge[0]) : ); SB();
  asm volatile("v_mfma_f32_16x16x32_bf16 %0, v[164:167], a[44:47], %0" : "+v"(g[1][3][1]) :  : ); SB();
  asm volatile("ds_read_b128 %0, %1 offset:3072" : "=&v"(pr[3]) : "v"(pe[1]) : "memory"); SB();
  asm volatile("s_waitcnt vmcnt(16)" :  :  : "memory"); SB();
  asm volatile("v_mfma_f32_16x16x32_bf16 %0, v[168:171], a[48:51], %0" : "+v"(g[1][0][0]) :  : ); SB();
  asm volatile("buffer_load_dwordx4 v[236:239], %0, %1, %2 offen offset:1024 sc1" :  : "v"(vB), "s"(rWS), "s"(69632u) : "memory"); SB();
  asm volatile("v_rcp_f32 %0, %1" : "=v"(gt[1]) : "v"(ge[1]) : ); SB();
  asm volatile("v_mfma_f32_16x16x32_bf16 %0, v[168:171], a[52:55], %0" : "+v"(g[1][1][0]) :  : ); SB();
  asm volatile("ds_read_b128 %0, %1 offset:4096" : "=&v"(pr[4]) : "v"(pe[2]) : "memory"); SB();
  asm volatile("v_rcp_f32 %0, %1" : "=v"(gt[2]) : "v"(ge[2]) : ); SB();
  asm volatile("v_mfma_f32_16x16x32_bf16 %0, v[168:171], a[56:59], %0" : "+v"(g[1][2][0]) :  : ); SB();
  asm volatile("ds_read_b128 %0, %1 offset:5120" : "=&v"(pr[5]) : "v"(pe[2]) : "memory"); SB();
  asm volatile("v_rcp_f32 %0, %1" : "=v"(gt[3]) : "v"(ge[3]) : ); SB();
  asm volatile("v_mfma_f32_16x16x32_bf16 %0, v[168:171], a[60:63], %0" : "+v"(g[1][3][0]) :  : ); SB();
  asm volatile("v_xor_b32 %0, 131072, %0" : "+v"(vB) :  : "memory"); SB();
  asm volatile("v_mul_f32 %0, %0, %1" : "+v"(a1[1][1][0]) : "v"(gt[0]) : ); SB();
  asm volatile("v_mfma_f32_16x16x32_bf16 %0, v[172:175], a[48:51], %0" : "+v"(g[1][0][1]) :  : ); SB();
  asm volatile("buffer_load_dwordx4 v[240:243], %0, %1, %2 offen offset:0 sc1" :  : "v"(vB), "s"(rWS), "s"(4096u) : "memory"); SB();
  asm volatile("v_mul_f32 %0, %0, %1" : "+v"(a1[1][1][1]) : "v"(gt[1]) : ); SB();
  asm volatile("v_mfma_f32_16x16x32_bf16 %0, v[172:175], a[52:55], %0" : "+v"(g[1][1][1]) :  : ); SB();
  asm volatile("buffer_load_dwordx4 v[244:247], %0, %1, %2 offen offset:1024 sc1" :  : "v"(vB), "s"(rWS), "s"(4096u) : "memory"); SB();
  asm volatile("v_mul_f32 %0, %0, %1" : "+v"(a1[1][1][2]) : "v"(gt[2]) : ); SB();
  asm volatile("v_mfma_f32_16x16x32_bf16 %0, v[172:175], a[56:59], %0" : "+v"(g[1][2][1]) :  : ); SB();
  asm volatile("v_mul_f32 %0, %0, %1" : "+v"(a1[1][1][3]) : "v"(gt[3]) : ); SB();
  asm volatile("v_mfma_f32_16x16x32_bf16 %0, v[172:175], a[60:63], %0" : "+v"(g[1][3][1]) :  : ); SB();
  asm volatile("v_mul_f32 %0, %1, %1" : "=v"(gt[0]) : "v"(a1[1][2][0]) : ); SB();
  asm volatile("s_waitcnt vmcnt(17)" :  :  : "memory"); SB();
  asm volatile("v_mfma_f32_16x16x32_bf16 %0, v[176:179], a[64:67], %0" : "+v"(g[1][0][0]) :  : ); SB();
  asm volatile("s_waitcnt lgkmcnt(0)" : "+v"(pr[0]), "+v"(pr[1]), "+v"(pr[2]), "+v"(pr[3]), "+v"(pr[4]), "+v"(pr[5]) :  : "memory"); SB();
  asm volatile("buffer_load_dwordx4 v[248:251], %0, %1, %2 offen offset:0 sc1" :  : "v"(vB), "s"(rWS), "s"(69632u) : "memory"); SB();
  asm volatile("v_mfma_f32_16x16x32_bf16 %0, v[176:179], a[68:71], %0" : "+v"(g[1][1][0]) :  : ); SB();
  asm volatile("v_add_f32 %0, %0, %1" : "+v"(pr[0][0]) : "v"(g[0][0][0][0]) : ); SB();
  asm volatile("v_mfma_f32_16x16x32_bf16 %0, v[176:179], a[72:75], %0" : "+v"(g[1][2][0]) :  : ); SB();
  asm volatile("v_add_f32 %0, %0, %1" : "+v"(pr[0][1]) : "v"(g[0][0][0][1]) : ); SB();
  asm volatile("v_mfma_f32_16x16x32_bf16 %0, v[176:179], a[76:79], %0" : "+v"(g[1][3][0]) :  : ); SB();
  asm volatile("buffer_load_dwordx4 v[252:255], %0, %1, %2 offen offset:1024 sc1" :  : "v"(vB), "s"(rWS), "s"(69632u) : "memory"); SB();
  asm volatile("v_add_f32 %0, %0, %1" : "+v"(pr[0][2]) : "v"(g[0][0][0][2]) : ); SB();
  asm volatile("v_mfma_f32_16x16x32_bf16 %0, v[180:183], a[64:67], %0" : "+v"(g[1][0][1]) :  : ); SB();
  asm volatile("v_xor_b32 %0, 393216, %0" : "+v"(vB) :  : "memory"); SB();
  asm volatile("v_add_f32 %0, %0, %1" : "+v"(pr[0][3]) : "v"(g[0][0][0][3]) : ); SB();
  asm volatile("v_mfma_f32_16x16x32_bf16 %0, v[180:183], a[68:71], %0" : "+v"(g[1][1][1]) :  : ); SB();
  asm volatile("v_add_f32 %0, %0, %1" : "+v"(pr[1][0]) : "v"(g[0][0][1][0]) : ); SB();
  asm volatile("v_mfma_f32_16x16x32_bf16 %0, v[180:183], a[72:75], %0" : "+v"(g[1][2][1]) :  : ); SB();
  asm volatile("buffer_load_dwordx4 v[160:163], %0, %1, %2 offen offset:0 sc1" :  : "v"(vB), "s"(rWS), "s"(4096u) : "memory"); SB();
  asm volatile("v_add_f32 %0, %0, %1" : "+v"(pr[1][1]) : "v"(g[0][0][1][1]) : ); SB();
  asm volatile("v_mfma_f32_16x16x32_bf16 %0, v[180:183], a[76:79], %0" : "+v"(g[1][3][1]) :  : ); SB();
  asm volatile("v_add_f32 %0, %0, %1" : "+v"(pr[1][2]) : "v"(g[0][0][1][2]) : ); SB();
  asm volatile("s_waitcnt vmcnt(18)" :  :  : "memory"); SB();
  asm volatile("v_mfma_f32_16x16x32_bf16 %0, v[184:187], a[80:83], %0" : "+v"(g[1][0][0]) :  : ); SB();
  asm volatile("v_add_f32 %0, %0, %1" : "+v"(pr[1][3]) : "v"(g[0][0][1][3]) : ); SB();
  asm volatile("v_mfma_f32_16x16x32_bf16 %0, v[184:187], a[84:87], %0" : "+v"(g[1][1][0]) :  : ); SB();
  asm volatile("v_add_f32 %0, %0, %1" : "+v"(pr[2][0]) : "v"(pr[4][0]) : ); SB();
  asm volatile("v_mfma_f32_16x16x32_bf16 %0, v[184:187], a[88:91], %0" : "+v"(g[1][2][0]) :  : ); SB();
  asm volatile("buffer_load_dwordx4 v[164:167], %0, %1, %2 offen offset:1024 sc1" :  : "v"(vB), "s"(rWS), "s"(4096u) : "memory"); SB();
  asm volatile("v_add_f32 %0, %0, %1" : "+v"(pr[2][1]) : "v"(pr[4][1]) : ); SB();
  asm volatile("v_mfma_f32_16x16x32_bf16 %0, v[184:187], a[92:95], %0" : "+v"(g[1][3][0]) :  : ); SB();
  asm volatile("v_add_f32 %0, %0, %1" : "+v"(pr[2][2]) : "v"(pr[4][2]) : ); SB();
  asm volatile("v_mfma_f32_16x16x32_bf16 %0, v[188:191], a[80:83], %0" : "+v"(g[1][0][1]) :  : ); SB();
  asm volatile("v_add_f32 %0, %0, %1" : "+v"(pr[2][3]) : "v"(pr[4][3]) : ); SB();
  asm volatile("v_mfma_f32_16x16x32_bf16 %0, v[188:191], a[84:87], %0" : "+v"(g[1][1][1]) :  : ); SB();
  asm volatile("buffer_load_dwordx4 v[168:171], %0, %1, %2 offen offset:0 sc1" :  : "v"(vB), "s"(rWS), "s"(69632u) : "memory"); SB();
  asm volatile("v_add_f32 %0, %0, %1" : "+v"(pr[3][0]) : "v"(pr[5][0]) : ); SB();
  asm volatile("v_mfma_f32_16x16x32_bf16 %0, v[188:191], a[88:91], %0" : "+v"(g[1][2][1]) :  : ); SB();
  asm volatile("v_add_f32 %0, %0, %1" : "+v"(pr[3][1]) : "v"(pr[5][1]) : ); SB();
  asm volatile("v_mfma_f32_16x16x32_bf16 %0, v[188:191], a[92:95], %0" : "+v"(g[1][3][1]) :  : ); SB();
  asm volatile("v_add_f32 %0, %0, %1" : "+v"(pr[3][2]) : "v"(pr[5][2]) : ); SB();
  asm volatile("s_waitcnt vmcnt(18)" :  :  : "memory"); SB();
  asm volatile("v_mfma_f32_16x16x32_bf16 %0, v[192:195], a[96:99], %0" : "+v"(g[1][0][0]) :  : ); SB();
  asm volatile("v_add_f32 %0, %0, %1" : "+v"(pr[3][3]) : "v"(pr[5][3]) : ); SB();
  asm volatile("v_mfma_f32_16x16x32_bf16 %0, v[192:195], a[100:103], %0" : "+v"(g[1][1][0]) :  : ); SB();
  asm volatile("buffer_load_dwordx4 v[172:175], %0, %1, %2 offen offset:1024 sc1" :  : "v"(vB), "s"(rWS), "s"(69632u) : "memory"); SB();
  asm volatile("v_add_f32 %0, %0, %1" : "+v"(pr[0][0]) : "v"(pr[2][0]) : ); SB();
  asm volatile("v_mfma_f32_16x16x32_bf16 %0, v[192:195], a[104:107], %0" : "+v"(g[1][2][0]) :  : ); SB();
  asm volatile("v_xor_b32 %0, 131072, %0" : "+v"(vB) :  : "memory"); SB();
  asm volatile("v_add_f32 %0, %0, %1" : "+v"(pr[0][1]) : "v"(pr[2][1]) : ); SB();
  asm volatile("v_mfma_f32_16x16x32_bf16 %0, v[192:195], a[108:111], %0" : "+v"(g[1][3][0]) :  : ); SB();
  asm volatile("v_add_f32 %0, %0, %1" : "+v"(pr[0][2]) : "v"(pr[2][2]) : ); SB();
  asm volatile("v_mfma_f32_16x16x32_bf16 %0, v[196:199], a[96:99], %0" : "+v"(g[1][0][1]) :  : ); SB();
  asm volatile("v_add_f32 %0, %0, %1" : "+v"(pr[0][3]) : "v"(pr[2][3]) : ); SB();
  asm volatile("v_mfma_f32_16x16x32_bf16 %0, v[196:199], a[100:103], %0" : "+v"(g[1][1][1]) :  : ); SB();
  asm volatile("v_add_f32 %0, %0, %1" : "+v"(pr[1][0]) : "v"(pr[3][0]) : ); SB();
  asm volatile("v_mfma_f32_16x16x32_bf16 %0, v[196:199], a[104:107], %0" : "+v"(g[1][2][1]) :  : ); SB();
  asm volatile("v_add_f32 %0, %0, %1" : "+v"(pr[1][1]) : "v"(pr[3][1]) : ); SB();
  asm volatile("v_mfma_f32_16x16x32_bf16 %0, v[196:199], a[108:111], %0" : "+v"(g[1][3][1]) :  : ); SB();
  asm volatile("v_add_f32 %0, %0, %1" : "+v"(pr[1][2]) : "v"(pr[3][2]) : ); SB();
  asm volatile("s_waitcnt vmcnt(17)" :  :  : "memory"); SB();
  asm volatile("v_mfma_f32_16x16x32_bf16 %0, v[200:203], a[112:115], %0" : "+v"(g[1][0][0]) :  : ); SB();
  asm volatile("v_add_f32 %0, %0, %1" : "+v"(pr[1][3]) : "v"(pr[3][3]) : ); SB();
  asm volatile("v_mfma_f32_16x16x32_bf16 %0, v[200:203], a[116:119], %0" : "+v"(g[1][1][0]) :  : ); SB();
  asm volatile("v_mul_f32 %0, %0, %1" : "+v"(pr[0][0]) : "v"(a1[0][0][0]) : ); SB();
  asm volatile("v_mfma_f32_16x16x32_bf16 %0, v[200:203], a[120:123], %0" : "+v"(g[1][2][0]) :  : ); SB();
  asm volatile("v_mul_f32 %0, %0, %1" : "+v"(pr[0][1]) : "v"(a1[0][0][1]) : ); SB();
  asm volatile("v_mfma_f32_16x16x32_bf16 %0, v[200:203], a[124:127], %0" : "+v"(g[1][3][0]) :  : ); SB();
  asm volatile("v_mul_f32 %0, %0, %1" : "+v"(pr[0][2]) : "v"(a1[0][0][2]) : ); SB();
  asm volatile("v_mfma_f32_16x16x32_bf16 %0, v[204:207], a[112:115], %0" : "+v"(g[1][0][1]) :  : ); SB();
  asm volatile("v_mul_f32 %0, %0, %1" : "+v"(pr[0][3]) : "v"(a1[0][0][3]) : ); SB();
  asm volatile("v_mfma_f32_16x16x32_bf16 %0, v[204:207], a[116:119], %0" : "+v"(g[1][1][1]) :  : ); SB();
  asm volatile("v_mul_f32 %0, %0, %1" : "+v"(pr[1][0]) : "v"(a1[0][1][0]) : ); SB();
  asm volatile("v_mfma_f32_16x16x32_bf16 %0, v[204:207], a[120:123], %0" : "+v"(g[1][2][1]) :  : ); SB();
  asm volatile("buffer_load_dwordx4 v[176:179], %0, %1, %2 offen offset:0 sc1" :  : "v"(vB), "s"(rWS), "s"(4096u) : "memory"); SB();
  asm volatile("v_mul_f32 %0, %0, %1" : "+v"(pr[1][1]) : "v"(a1[0][1][1]) : ); SB();
  asm volatile("v_mfma_f32_16x16x32_bf16 %0, v[204:207], a[124:127], %0" : "+v"(g[1][3][1]) :  : ); SB();
  asm volatile("v_mul_f32 %0, %0, %1" : "+v"(pr[1][2]) : "v"(a1[0][1][2]) : ); SB();
  asm volatile("s_waitcnt vmcnt(16)" :  :  : "memory"); SB();
  asm volatile("v_mfma_f32_16x16x32_bf16 %0, v[208:211], a[128:131], %0" : "+v"(g[1][0][0]) :  : ); SB();
  asm volatile("v_mul_f32 %0, %0, %1" : "+v"(pr[1][3]) : "v"(a1[0][1][3]) : ); SB();
  asm volatile("v_mfma_f32_16x16x32_bf16 %0, v[208:211], a[132:135], %0" : "+v"(g[1][1][0]) :  : ); SB();
  asm volatile("buffer_load_dwordx4 v[180:183], %0, %1, %2 offen offset:1024 sc1" :  : "v"(vB), "s"(rWS), "s"(4096u) : "memory"); SB();
  asm volatile("v_cvt_pk_bf16_f32 %0, %1, %2" : "=v"(ho[0][0]) : "v"(pr[0][0]), "v"(pr[0][1]) : ); SB();
  asm volatile("v_mfma_f32_16x16x32_bf16 %0, v[208:211], a[136:139], %0" : "+v"(g[1][2][0]) :  : ); SB();
  asm volatile("v_cvt_pk_bf16_f32 %0, %1, %2" : "=v"(ho[0][1]) : "v"(pr[0][2]), "v"(pr[0][3]) : ); SB();
  asm volatile("v_mfma_f32_16x16x32_bf16 %0, v[208:211], a[140:143], %0" : "+v"(g[1][3][0]) :  : ); SB();
  asm volatile("v_cvt_pk_bf16_f32 %0, %1, %2" : "=v"(ho[0][2]) : "v"(pr[1][0]), "v"(pr[1][1]) : ); SB();
  asm volatile("v_mfma_f32_16x16x32_bf16 %0, v[212:215], a[128:131], %0" : "+v"(g[1][0][1]) :  : ); SB();
  asm volatile("v_cvt_pk_bf16_f32 %0, %1, %2" : "=v"(ho[0][3]) : "v"(pr[1][2]), "v"(pr[1][3]) : ); SB();
  asm volatile("v_mfma_f32_16x16x32_bf16 %0, v[212:215], a[132:135], %0" : "+v"(g[1][1][1]) :  : ); SB();
  asm volatile("buffer_store_dwordx4 %1, %0, %2, %3 offen offset:0 sc1\n\ts_nop 1" :  : "v"(vH_), "v"(ho[0]), "s"(rWS), "s"(0u) : "memory"); SB();
  asm volatile("v_mul_f32 %0, %1, %1" : "=v"(gt[1]) : "v"(a1[1][2][1]) : ); SB();
  asm volatile("v_mfma_f32_16x16x32_bf16 %0, v[212:215], a[136:139], %0" : "+v"(g[1][2][1]) :  : ); SB();
  asm volatile("v_mul_f32 %0, %1, %1" : "=v"(gt[2]) : "v"(a1[1][2][2]) : ); SB();
  asm volatile("v_mfma_f32_16x16x32_bf16 %0, v[212:215], a[140:143], %0" : "+v"(g[1][3][1]) :  : ); SB();
  asm volatile("v_mul_f32 %0, %1, %1" : "=v"(gt[3]) : "v"(a1[1][2][3]) : ); SB();
  asm volatile("s_waitcnt vmcnt(15)" :  :  : "memory"); SB();
  asm volatile("v_mfma_f32_16x16x32_bf16 %0, v[216:219], a[144:147], %0" : "+v"(g[1][0][0]) :  : ); SB();
  asm volatile("buffer_load_dwordx4 v[184:187], %0, %1, %2 offen offset:0 sc1" :  : "v"(vB), "s"(rWS), "s"(69632u) : "memory"); SB();
  asm volatile("v_fma_f32 %0, %0, %1, %2" : "+v"(gt[0]) : "v"(k2[0]), "v"(k1[0]) : ); SB();
  asm volatile("v_mfma_f32_16x16x32_bf16 %0, v[216:219], a[148:151], %0" : "+v"(g[1][1][0]) :  : ); SB();
  asm volatile("v_fma_f32 %0, %0, %1, %2" : "+v"(gt[1]) : "v"(k2[0]), "v"(k1[0]) : ); SB();
  asm volatile("v_mfma_f32_16x16x32_bf16 %0, v[216:219], a[152:155], %0" : "+v"(g[1][2][0]) :  : ); SB();
  asm volatile("v_fma_f32 %0, %0, %1, %2" : "+v"(gt[2]) : "v"(k2[0]), "v"(k1[0]) : ); SB();
  asm volatile("v_mfma_f32_16x16x32_bf16 %0, v[216:219], a[156:159], %0" : "+v"(g[1][3][0]) :  : ); SB();
  asm volatile("v_fma_f32 %0, %0, %1, %2" : "+v"(gt[3]) : "v"(k2[0]), "v"(k1[0]) : ); SB();
  asm volatile("v_mfma_f32_16x16x32_bf16 %0, v[220:223], a[144:147], %0" : "+v"(g[1][0][1]) :  : ); SB();
  asm volatile("buffer_load_dwordx4 v[188:191], %0, %1, %2 offen offset:1024 sc1" :  : "v"(vB), "s"(rWS), "s"(69632u) : "memory"); SB();
  asm volatile("v_mul_f32 %0, %0, %1" : "+v"(gt[0]) : "v"(a1[1][2][0]) : ); SB();
  asm volatile("v_mfma_f32_16x16x32_bf16 %0, v[220:223], a[148:151], %0" : "+v"(g[1][1][1]) :  : ); SB();
  asm volatile("v_xor_b32 %0, 917504, %0" : "+v"(vB) :  : "memory"); SB();
  asm volatile("v_mul_f32 %0, %0, %1" : "+v"(gt[1]) : "v"(a1[1][2][1]) : ); SB();
  asm volatile("v_mfma_f32_16x16x32_bf16 %0, v[220:223], a[152:155], %0" : "+v"(g[1][2][1]) :  : ); SB();
  asm volatile("v_mul_f32 %0, %0, %1" : "+v"(gt[2]) : "v"(a1[1][2][2]) : ); SB();
  asm volatile("v_mfma_f32_16x16x32_bf16 %0, v[220:223], a[156:159], %0" : "+v"(g[1][3][1]) :  : ); SB();
  asm volatile("buffer_load_dwordx4 v[192:195], %0, %1, %2 offen offset:0 sc1" :  : "v"(vB), "s"(rWS), "s"(4096u) : "memory"); SB();
  ST(29);
  asm volatile("s_waitcnt vmcnt(16)" :  :  : "memory"); SB();
  asm volatile("v_mfma_f32_16x16x32_bf16 %0, v[224:227], a[160:163], %1" : "=&v"(g[0][0][0]) : "v"(bs4) : ); SB();
  asm volatile("s_barrier" :  :  : "memory"); SB();
  asm volatile("v_mul_f32 %0, %0, %1" : "+v"(gt[3]) : "v"(a1[1][2][3]) : ); SB();
  asm volatile("v_mfma_f32_16x16x32_bf16 %0, v[224:227], a[164:167], 0" : "=&v"(g[0][1][0]) :  : ); SB();
  asm volatile("v_exp_f32 %0, %1" : "=v"(ge[0]) : "v"(gt[0]) : ); SB();
  asm volatile("v_mfma_f32_16x16x32_bf16 %0, v[224:227], a[168:171], 0" : "=&v"(g[0][2][0]) :  : ); SB();
  asm volatile("ds_write_b128 %0, %1 offset:0" :  : "v"(pw2), "v"(g[1][1][0]) : "memory"); SB();
  asm volatile("v_mfma_f32_16x16x32_bf16 %0, v[224:227], a[172:175], 0" : "=&v"(g[0][3][0]) :  : ); SB();
  asm volatile("buffer_load_dwordx4 v[196:199], %0, %1, %2 offen offset:1024 sc1" :  : "v"(vB), "s"(rWS), "s"(4096u) : "memory"); SB();
  asm volatile("v_exp_f32 %0, %1" : "=v"(ge[1]) : "v"(gt[1]) : ); SB();
  asm volatile("v_mfma_f32_16x16x32_bf16 %0, v[228:231], a[160:163], %1" : "=&v"(g[0][0][1]) : "v"(bs4) : ); SB();
  asm volatile("buffer_load_dwordx4 v[200:203], %0, %1, %2 offen offset:0 sc1" :  : "v"(vB), "s"(rWS), "s"(69632u) : "memory"); SB();
  asm volatile("v_exp_f32 %0, %1" : "=v"(ge[2]) : "v"(gt[2]) : ); SB();
  asm volatile("v_mfma_f32_16x16x32_bf16 %0, v[228:231], a[164:167], 0" : "=&v"(g[0][1][1]) :  : ); SB();
  asm volatile("ds_write_b128 %0, %1 offset:1024" :  : "v"(pw2), "v"(g[1][1][1]) : "memory"); SB();
  asm volatile("v_mfma_f32_16x16x32_bf16 %0, v[228:231], a[168:171], 0" : "=&v"(g[0][2][1]) :  : ); SB();
  asm volatile("buffer_load_dwordx4 v[204:207], %0, %1, %2 offen offset:1024 sc1" :  : "v"(vB), "s"(rWS), "s"(69632u) : "memory"); SB();
  asm volatile("v_exp_f32 %0, %1" : "=v"(ge[3]) : "v"(gt[3]) : ); SB();
  asm volatile("v_mfma_f32_16x16x32_bf16 %0, v[228:231], a[172:175], 0" : "=&v"(g[0][3][1]) :  : ); SB();
  asm volatile("v_xor_b32 %0, 131072, %0" : "+v"(vB) :  : "memory"); SB();
  asm volatile("s_waitcnt vmcnt(17)" :  :  : "memory"); SB();
  asm volatile("v_mfma_f32_16x16x32_bf16 %0, v[232:235], a[176:179], %0" : "+v"(g[0][0][0]) :  : ); SB();
  asm volatile("ds_write_b128 %0, %1 offset:2048" :  : "v"(pw2), "v"(g[1][2][0]) : "memory"); SB();
  asm volatile("v_mfma_f32_16x16x32_bf16 %0, v[232:235], a[180:183], %0" : "+v"(g[0][1][0]) :  : ); SB();
  asm volatile("buffer_load_dwordx4 v[208:211], %0, %1, %2 offen offset:0 sc1" :  : "v"(vB), "s"(rWS), "s"(4096u) : "memory"); SB();
  asm volatile("v_fma_f32 %0, %0, %1, %1" : "+v"(ge[0]) : "v"(xd[0]) : ); SB();
  asm volatile("v_mfma_f32_16x16x32_bf16 %0, v[232:235], a[184:187], %0" : "+v"(g[0][2][0]) :  : ); SB();
  asm volatile("v_fma_f32 %0, %0, %1, %1" : "+v"(ge[1]) : "v"(xd[0]) : ); SB();
  asm volatile("v_mfma_f32_16x16x32_bf16 %0, v[232:235], a[188:191], %0" : "+v"(g[0][3][0]) :  : ); SB();
  asm volatile("ds_write_b128 %0, %1 offset:3072" :  : "v"(pw2), "v"(g[1][2][1]) : "memory"); SB();
  asm volatile("v_mfma_f32_16x16x32_bf16 %0, v[236:239], a[176:179], %0" : "+v"(g[0][0][1]) :  : ); SB();
  asm volatile("buffer_load_dwordx4 v[212:215], %0, %1, %2 offen offset:1024 sc1" :  : "v"(vB), "s"(rWS), "s"(4096u) : "memory"); SB();
  asm volatile("v_fma_f32 %0, %0, %1, %1" : "+v"(ge[2]) : "v"(xd[0]) : ); SB();
  asm volatile("v_mfma_f32_16x16x32_bf16 %0, v[236:239], a[180:183], %0" : "+v"(g[0][1][1]) :  : ); SB();
  asm volatile("v_fma_f32 %0, %0, %1, %1" : "+v"(ge[3]) : "v"(xd[0]) : ); SB();
  asm volatile("v_mfma_f32_16x16x32_bf16 %0, v[236:239], a[184:187], %0" : "+v"(g[0][2][1]) :  : ); SB();
  asm volatile("ds_write_b128 %0, %1 offset:4096" :  : "v"(pw2), "v"(g[1][3][0]) : "memory"); SB();
  asm volatile("v_mfma_f32_16x16x32_bf16 %0, v[236:239], a[188:191], %0" : "+v"(g[0][3][1]) :  : ); SB();
  asm volatile("buffer_load_dwordx4 v[216:219], %0, %1, %2 offen offset:0 sc1" :  : "v"(vB), "s"(rWS), "s"(69632u) : "memory"); SB();
  asm volatile("s_waitcnt vmcnt(18)" :  :  : "memory"); SB();
  asm volatile("v_mfma_f32_16x16x32_bf16 %0, v[240:243], a[192:195], %0" : "+v"(g[0][0][0]) :  : ); SB();
  asm volatile("buffer_load_dwordx4 v[220:223], %0, %1, %2 offen offset:1024 sc1" :  : "v"(vB), "s"(rWS), "s"(69632u) : "memory"); SB();
  asm volatile("v_rcp_f32 %0, %1" : "=v"(gt[0]) : "v"(ge[0]) : ); SB();
  asm volatile("v_mfma_f32_16x16x32_bf16 %0, v[240:243], a[196:199], %0" : "+v"(g[0][1][0]) :  : ); SB();
  asm volatile("ds_write_b128 %0, %1 offset:5120" :  : "v"(pw2), "v"(g[1][3][1]) : "memory"); SB();
  asm volatile("v_mfma_f32_16x16x32_bf16 %0, v[240:243], a[200:203], %0" : "+v"(g[0][2][0]) :  : ); SB();
  asm volatile("v_xor_b32 %0, 393216, %0" : "+v"(vB) :  : "memory"); SB();
  asm volatile("v_rcp_f32 %0, %1" : "=v"(gt[1]) : "v"(ge[1]) : ); SB();
  asm volatile("v_mfma_f32_16x16x32_bf16 %0, v[240:243], a[204:207], %0" : "+v"(g[0][3][0]) :  : ); SB();
  asm volatile("buffer_load_dwordx4 v[224:227], %0, %1, %2 offen offset:0 sc1" :  : "v"(vB), "s"(rWS), "s"(4096u) : "memory"); SB();
  asm volatile("v_rcp_f32 %0, %1" : "=v"(gt[2]) : "v"(ge[2]) : ); SB();
  asm volatile("v_mfma_f32_16x16x32_bf16 %0, v[244:247], a[192:195], %0" : "+v"(g[0][0][1]) :  : ); SB();
  asm volatile("s_waitcnt lgkmcnt(0)\n\ts_barrier" :  :  : "memory"); SB();
  asm volatile("ds_read_b128 %0, %1 offset:0" : "=&v"(pr[0]) : "v"(pe[0]) : "memory"); SB();
  asm volatile("v_mfma_f32_16x16x32_bf16 %0, v[244:247], a[196:199], %0" : "+v"(g[0][1][1]) :  : ); SB();
  asm volatile("ds_read_b128 %0, %1 offset:1024" : "=&v"(pr[1]) : "v"(pe[0]) : "memory"); SB();
  asm volatile("v_rcp_f32 %0, %1" : "=v"(gt[3]) : "v"(ge[3]) : ); SB();
  asm volatile("v_mfma_f32_16x16x32_bf16 %0, v[244:247], a[200:203], %0" : "+v"(g[0][2][1]) :  : ); SB();
  asm volatile("ds_read_b128 %0, %1 offset:2048" : "=&v"(pr[2]) : "v"(pe[1]) : "memory"); SB();
  asm volatile("v_mul_f32 %0, %0, %1" : "+v"(a1[1][2][0]) : "v"(gt[0]) : ); SB();
  asm volatile("v_mfma_f32_16x16x32_bf16 %0, v[244:247], a[204:207], %0" : "+v"(g[0][3][1]) :  : ); SB();
  asm volatile("ds_read_b128 %0, %1 offset:3072" : "=&v"(pr[3]) : "v"(pe[1]) : "memory"); SB();
  asm volatile("s_waitcnt vmcnt(18)" :  :  : "memory"); SB();
  asm volatile("v_mfma_f32_16x16x32_bf16 %0, v[248:251], a[208:211], %0" : "+v"(g[0][0][0]) :  : ); SB();
  asm volatile("ds_read_b128 %0, %1 offset:4096" : "=&v"(pr[4]) : "v"(pe[2]) : "memory"); SB();
  asm volatile("v_mul_f32 %0, %0, %1" : "+v"(a1[1][2][1]) : "v"(gt[1]) : ); SB();
  asm volatile("v_mfma_f32_16x16x32_bf16 %0, v[248:251], a[212:215], %0" : "+v"(g[0][1][0]) :  : ); SB();
  asm volatile("ds_read_b128 %0, %1 offset:5120" : "=&v"(pr[5]) : "v"(pe[2]) : "memory"); SB();
  asm volatile("v_mul_f32 %0, %0, %1" : "+v"(a1[1][2][2]) : "v"(gt[2]) : ); SB();
  asm volatile("v_mfma_f32_16x16x32_bf16 %0, v[248:251], a[216:219], %0" : "+v"(g[0][2][0]) :  : ); SB();
  asm volatile("buffer_load_dwordx4 v[228:231], %0, %1, %2 offen offset:1024 sc1" :  : "v"(vB), "s"(rWS), "s"(4096u) : "memory"); SB();
  asm volatile("v_mul_f32 %0, %0, %1" : "+v"(a1[1][2][3]) : "v"(gt[3]) : ); SB();
  asm volatile("v_mfma_f32_16x16x32_bf16 %0, v[248:251], a[220:223], %0" : "+v"(g[0][3][0]) :  : ); SB();
  asm volatile("buffer_load_dwordx4 v[232:235], %0, %1, %2 offen offset:0 sc1" :  : "v"(vB), "s"(rWS), "s"(69632u) : "memory"); SB();
  asm volatile("v_mfma_f32_16x16x32_bf16 %0, v[252:255], a[208:211], %0" : "+v"(g[0][0][1]) :  : ); SB();
  asm volatile("v_mfma_f32_16x16x32_bf16 %0, v[252:255], a[212:215], %0" : "+v"(g[0][1][1]) :  : ); SB();
  asm volatile("v_mfma_f32_16x16x32_bf16 %0, v[252:255], a[216:219], %0" : "+v"(g[0][2][1]) :  : ); SB();
  asm volatile("v_mfma_f32_16x16x32_bf16 %0, v[252:255], a[220:223], %0" : "+v"(g[0][3][1]) :  : ); SB();
  asm volatile("s_waitcnt lgkmcnt(0)" : "+v"(pr[0]), "+v"(pr[1]), "+v"(pr[2]), "+v"(pr[3]), "+v"(pr[4]), "+v"(pr[5]) :  : "memory"); SB();
  asm volatile("v_add_f32 %0, %0, %1" : "+v"(pr[0][0]) : "v"(g[1][0][0][0]) : ); SB();
  asm volatile("s_waitcnt vmcnt(18)" :  :  : "memory"); SB();
  asm volatile("v_mfma_f32_16x16x32_bf16 %0, v[160:163], a[224:227], %0" : "+v"(g[0][0][0]) :  : ); SB();
  asm volatile("v_add_f32 %0, %0, %1" : "+v"(pr[0][1]) : "v"(g[1][0][0][1]) : ); SB();
  asm volatile("v_mfma_f32_16x16x32_bf16 %0, v[160:163], a[228:231], %0" : "+v"(g[0][1][0]) :  : ); SB();
  asm volatile("v_add_f32 %0, %0, %1" : "+v"(pr[0][2]) : "v"(g[1][0][0][2]) : ); SB();
  asm volatile("v_mfma_f32_16x16x32_bf16 %0, v[160:163], a[232:235], %0" : "+v"(g[0][2][0]) :  : ); SB();
  asm volatile("v_add_f32 %0, %0, %1" : "+v"(pr[0][3]) : "v"(g[1][0][0][3]) : ); SB();
  asm volatile("v_mfma_f32_16x16x32_bf16 %0, v[160:163], a[236:239], %0" : "+v"(g[0][3][0]) :  : ); SB();
  asm volatile("v_add_f32 %0, %0, %1" : "+v"(pr[1][0]) : "v"(g[1][0][1][0]) : ); SB();
  asm volatile("v_mfma_f32_16x16x32_bf16 %0, v[164:167], a[224:227], %0" : "+v"(g[0][0][1]) :  : ); SB();
  asm volatile("v_add_f32 %0, %0, %1" : "+v"(pr[1][1]) : "v"(g[1][0][1][1]) : ); SB();
  asm volatile("v_mfma_f32_16x16x32_bf16 %0, v[164:167], a[228:231], %0" : "+v"(g[0][1][1]) :  : ); SB();
  asm volatile("buffer_load_dwordx4 v[236:239], %0, %1, %2 offen offset:1024 sc1" :  : "v"(vB), "s"(rWS), "s"(69632u) : "memory"); SB();
  asm volatile("v_add_f32 %0, %0, %1" : "+v"(pr[1][2]) : "v"(g[1][0][1][2]) : ); SB();
  asm volatile("v_mfma_f32_16x16x32_bf16 %0, v[164:167], a[232:235], %0" : "+v"(g[0][2][1]) :  : ); SB();
  asm volatile("v_xor_b32 %0, 131072, %0" : "+v"(vB) :  : "memory"); SB();
  asm volatile("v_add_f32 %0, %0, %1" : "+v"(pr[1][3]) : "v"(g[1][0][1][3]) : ); SB();
  asm volatile("v_mfma_f32_16x16x32_bf16 %0, v[164:167], a[236:239], %0" : "+v"(g[0][3][1]) :  : ); SB();
  asm volatile("v_add_f32 %0, %0, %1" : "+v"(pr[2][0]) : "v"(pr[4][0]) : ); SB();
  asm volatile("s_waitcnt vmcnt(17)" :  :  : "memory"); SB();
  asm volatile("v_mfma_f32_16x16x32_bf16 %0, v[168:171], a[240:243], %0" : "+v"(g[0][0][0]) :  : ); SB();
  asm volatile("buffer_load_dwordx4 v[240:243], %0, %1, %2 offen offset:0 sc1" :  : "v"(vB), "s"(rWS), "s"(4096u) : "memory"); SB();
  asm volatile("v_add_f32 %0, %0, %1" : "+v"(pr[2][1]) : "v"(pr[4][1]) : ); SB();
  asm volatile("v_mfma_f32_16x16x32_bf16 %0, v[168:171], a[244:247], %0" : "+v"(g[0][1][0]) :  : ); SB();
  asm volatile("v_add_f32 %0, %0, %1" : "+v"(pr[2][2]) : "v"(pr[4][2]) : ); SB();
  asm volatile("v_mfma_f32_16x16x32_bf16 %0, v[168:171], a[248:251], %0" : "+v"(g[0][2][0]) :  : ); SB();
  asm volatile("v_add_f32 %0, %0, %1" : "+v"(pr[2][3]) : "v"(pr[4][3]) : ); SB();
  asm volatile("v_mfma_f32_16x16x32_bf16 %0, v[168:171], a[252:255], %0" : "+v"(g[0][3][0]) :  : ); SB();
  asm volatile("v_add_f32 %0, %0, %1" : "+v"(pr[3][0]) : "v"(pr[5][0]) : ); SB();
  asm volatile("v_mfma_f32_16x16x32_bf16 %0, v[172:175], a[240:243], %0" : "+v"(g[0][0][1]) :  : ); SB();
  asm volatile("buffer_load_dwordx4 v[244:247], %0, %1, %2 offen offset:1024 sc1" :  : "v"(vB), "s"(rWS), "s"(4096u) : "memory"); SB();
  asm volatile("v_add_f32 %0, %0, %1" : "+v"(pr[3][1]) : "v"(pr[5][1]) : ); SB();
  asm volatile("v_mfma_f32_16x16x32_bf16 %0, v[172:175], a[244:247], %0" : "+v"(g[0][1][1]) :  : ); SB();
  asm volatile("v_add_f32 %0, %0, %1" : "+v"(pr[3][2]) : "v"(pr[5][2]) : ); SB();
  asm volatile("v_mfma_f32_16x16x32_bf16 %0, v[172:175], a[248:251], %0" : "+v"(g[0][2][1]) :  : ); SB();
  asm volatile("v_add_f32 %0, %0, %1" : "+v"(pr[3][3]) : "v"(pr[5][3]) : ); SB();
  asm volatile("v_mfma_f32_16x16x32_bf16 %0, v[172:175], a[252:255], %0" : "+v"(g[0][3][1]) :  : ); SB();
  asm volatile("s_add_u32 m0, %0, 60032" :  : "s"(mU) : "memory", "scc"); SB();
  asm volatile("v_add_f32 %0, %0, %1" : "+v"(pr[0][0]) : "v"(pr[2][0]) : ); SB();
  asm volatile("s_waitcnt vmcnt(17)" :  :  : "memory"); SB();
  asm volatile("v_mfma_f32_16x16x32_bf16 %0, v[176:179], a[0:3], %0" : "+v"(g[0][0][0]) :  : ); SB();
  asm volatile("buffer_load_dwordx4 %0, %1, %2 offen sc1 lds" :  : "v"(vF), "s"(rWS), "s"(W_KV) : "memory"); SB();
  asm volatile("v_add_f32 %0, %0, %1" : "+v"(pr[0][1]) : "v"(pr[2][1]) : ); SB();
  asm volatile("v_mfma_f32_16x16x32_bf16 %0, v[176:179], a[4:7], %0" : "+v"(g[0][1][0]) :  : ); SB();
  asm volatile("v_add_f32 %0, %0, %1" : "+v"(pr[0][2]) : "v"(pr[2][2]) : ); SB();
  asm volatile("v_mfma_f32_16x16x32_bf16 %0, v[176:179], a[8:11], %0" : "+v"(g[0][2][0]) :  : ); SB();
  asm volatile("v_add_f32 %0, %0, %1" : "+v"(pr[0][3]) : "v"(pr[2][3]) : ); SB();
  asm volatile("v_mfma_f32_16x16x32_bf16 %0, v[176:179], a[12:15], %0" : "+v"(g[0][3][0]) :  : ); SB();
  asm volatile("s_add_u32 m0, %0, 72960" :  : "s"(mU) : "memory", "scc"); SB();
  asm volatile("v_add_f32 %0, %0, %1" : "+v"(pr[1][0]) : "v"(pr[3][0]) : ); SB();
  asm volatile("v_mfma_f32_16x16x32_bf16 %0, v[180:183], a[0:3], %0" : "+v"(g[0][0][1]) :  : ); SB();
  asm volatile("buffer_load_dwordx4 %0, %1, %2 offen sc1 lds" :  : "v"(vF), "s"(rWS), "s"(W_KV + 4096) : "memory"); SB();
  asm volatile("v_add_f32 %0, %0, %1" : "+v"(pr[1][1]) : "v"(pr[3][1]) : ); SB();
  asm volatile("v_mfma_f32_16x16x32_bf16 %0, v[180:183], a[4:7], %0" : "+v"(g[0][1][1]) :  : ); SB();
  asm volatile("v_add_f32 %0, %0, %1" : "+v"(pr[1][2]) : "v"(pr[3][2]) : ); SB();
  asm volatile("v_mfma_f32_16x16x32_bf16 %0, v[180:183], a[8:11], %0" : "+v"(g[0][2][1]) :  : ); SB();
  asm volatile("buffer_load_dwordx4 v[248:251], %0, %1, %2 offen offset:0 sc1" :  : "v"(vB), "s"(rWS), "s"(69632u) : "memory"); SB();
  asm volatile("v_add_f32 %0, %0, %1" : "+v"(pr[1][3]) : "v"(pr[3][3]) : ); SB();
  asm volatile("v_mfma_f32_16x16x32_bf16 %0, v[180:183], a[12:15], %0" : "+v"(g[0][3][1]) :  : ); SB();
  asm volatile("v_mul_f32 %0, %0, %1" : "+v"(pr[0][0]) : "v"(a1[0][2][0]) : ); SB();
  asm volatile("s_waitcnt vmcnt(17)" :  :  : "memory"); SB();
  asm volatile("v_mfma_f32_16x16x32_bf16 %0, v[184:187], a[16:19], %0" : "+v"(g[0][0][0]) :  : ); SB();
  asm volatile("v_mul_f32 %0, %0, %1" : "+v"(pr[0][1]) : "v"(a1[0][2][1]) : ); SB();
  asm volatile("v_mfma_f32_16x16x32_bf16 %0, v[184:187], a[20:23], %0" : "+v"(g[0][1][0]) :  : ); SB();
  asm volatile("v_mul_f32 %0, %0, %1" : "+v"(pr[0][2]) : "v"(a1[0][2][2]) : ); SB();
  asm volatile("v_mfma_f32_16x16x32_bf16 %0, v[184:187], a[24:27], %0" : "+v"(g[0][2][0]) :  : ); SB();
  asm volatile("buffer_load_dwordx4 v[252:255], %0, %1, %2 offen offset:1024 sc1" :  : "v"(vB), "s"(rWS), "s"(69632u) : "memory"); SB();
  asm volatile("v_mul_f32 %0, %0, %1" : "+v"(pr[0][3]) : "v"(a1[0][2][3]) : ); SB();
  asm volatile("v_mfma_f32_16x16x32_bf16 %0, v[184:187], a[28:31], %0" : "+v"(g[0][3][0]) :  : ); SB();
  asm volatile("v_mul_f32 %0, %0, %1" : "+v"(pr[1][0]) : "v"(a1[1][0][0]) : ); SB();
  asm volatile("v_mfma_f32_16x16x32_bf16 %0, v[188:191], a[16:19], %0" : "+v"(g[0][0][1]) :  : ); SB();
  asm volatile("v_mul_f32 %0, %0, %1" : "+v"(pr[1][1]) : "v"(a1[1][0][1]) : ); SB();
  asm volatile("v_mfma_f32_16x16x32_bf16 %0, v[188:191], a[20:23], %0" : "+v"(g[0][1][1]) :  : ); SB();
  asm volatile("v_mul_f32 %0, %0, %1" : "+v"(pr[1][2]) : "v"(a1[1][0][2]) : ); SB();
  asm volatile("v_mfma_f32_16x16x32_bf16 %0, v[188:191], a[24:27], %0" : "+v"(g[0][2][1]) :  : ); SB();
  asm volatile("buffer_load_dwordx4 a[160:163], %0, %1, %2 offen offset:0 sc1" :  : "v"(vO), "s"(rWS), "s"(0u) : "memory"); SB();
  asm volatile("v_mul_f32 %0, %0, %1" : "+v"(pr[1][3]) : "v"(a1[1][0][3]) : ); SB();
  asm volatile("v_mfma_f32_16x16x32_bf16 %0, v[188:191], a[28:31], %0" : "+v"(g[0][3][1]) :  : ); SB();
  asm volatile("v_cvt_pk_bf16_f32 %0, %1, %2" : "=v"(ho[1][0]) : "v"(pr[0][0]), "v"(pr[0][1]) : ); SB();
  asm volatile("s_waitcnt vmcnt(17)" :  :  : "memory"); SB();
  asm volatile("v_mfma_f32_16x16x32_bf16 %0, v[192:195], a[32:35], %0" : "+v"(g[0][0][0]) :  : ); SB();
  asm volatile("v_cvt_pk_bf16_f32 %0, %1, %2" : "=v"(ho[1][1]) : "v"(pr[0][2]), "v"(pr[0][3]) : ); SB();
  asm volatile("v_mfma_f32_16x16x32_bf16 %0, v[192:195], a[36:39], %0" : "+v"(g[0][1][0]) :  : ); SB();
  asm volatile("buffer_load_dwordx4 a[164:167], %0, %1, %2 offen offset:1024 sc1" :  : "v"(vO), "s"(rWS), "s"(0u) : "memory"); SB();
  asm volatile("v_cvt_pk_bf16_f32 %0, %1, %2" : "=v"(ho[1][2]) : "v"(pr[1][0]), "v"(pr[1][1]) : ); SB();
  asm volatile("v_mfma_f32_16x16x32_bf16 %0, v[192:195], a[40:43], %0" : "+v"(g[0][2][0]) :  : ); SB();
  asm volatile("v_cvt_pk_bf16_f32 %0, %1, %2" : "=v"(ho[1][3]) : "v"(pr[1][2]), "v"(pr[1][3]) : ); SB();
  asm volatile("v_mfma_f32_16x16x32_bf16 %0, v[192:195], a[44:47], %0" : "+v"(g[0][3][0]) :  : ); SB();
  asm volatile("v_mfma_f32_16x16x32_bf16 %0, v[196:199], a[32:35], %0" : "+v"(g[0][0][1]) :  : ); SB();
  asm volatile("v_mfma_f32_16x16x32_bf16 %0, v[196:199], a[36:39], %0" : "+v"(g[0][1][1]) :  : ); SB();
  asm volatile("buffer_store_dwordx4 %1, %0, %2, %3 offen offset:1024 sc1\n\ts_nop 1" :  : "v"(vH_), "v"(ho[1]), "s"(rWS), "s"(0u) : "memory"); SB();
  asm volatile("v_mfma_f32_16x16x32_bf16 %0, v[196:199], a[40:43], %0" : "+v"(g[0][2][1]) :  : ); SB();
  asm volatile("v_mfma_f32_16x16x32_bf16 %0, v[196:199], a[44:47], %0" : "+v"(g[0][3][1]) :  : ); SB();
  asm volatile("s_waitcnt vmcnt(17)" :  :  : "memory"); SB();
  asm volatile("v_mfma_f32_16x16x32_bf16 %0, v[200:203], a[48:51], %0" : "+v"(g[0][0][0]) :  : ); SB();
  asm volatile("buffer_load_dwordx4 a[168:171], %0, %1, %2 offen offset:2048 sc1" :  : "v"(vO), "s"(rWS), "s"(0u) : "memory"); SB();
  asm volatile("v_mfma_f32_16x16x32_bf16 %0, v[200:203], a[52:55], %0" : "+v"(g[0][1][0]) :  : ); SB();
  asm volatile("v_mfma_f32_16x16x32_bf16 %0, v[200:203], a[56:59], %0" : "+v"(g[0][2][0]) :  : ); SB();
  asm volatile("v_mfma_f32_16x16x32_bf16 %0, v[200:203], a[60:63], %0" : "+v"(g[0][3][0]) :  : ); SB();
  asm volatile("v_mfma_f32_16x16x32_bf16 %0, v[204:207], a[48:51], %0" : "+v"(g[0][0][1]) :  : ); SB();
  asm volatile("buffer_load_dwordx4 a[172:175], %0, %1, %2 offen offset:3072 sc1" :  : "v"(vO), "s"(rWS), "s"(0u) : "memory"); SB();
  asm volatile("v_mfma_f32_16x16x32_bf16 %0, v[204:207], a[52:55], %0" : "+v"(g[0][1][1]) :  : ); SB();
  asm volatile("v_mfma_f32_16x16x32_bf16 %0, v[204:207], a[56:59], %0" : "+v"(g[0][2][1]) :  : ); SB();
  asm volatile("v_mfma_f32_16x16x32_bf16 %0, v[204:207], a[60:63], %0" : "+v"(g[0][3][1]) :  : ); SB();
  asm volatile("s_waitcnt vmcnt(17)" :  :  : "memory"); SB();
  asm volatile("v_mfma_f32_16x16x32_bf16 %0, v[208:211], a[64:67], %0" : "+v"(g[0][0][0]) :  : ); SB();
  asm volatile("ds_read_b128 %0, %1 offset:0" : "=&v"(yq[0]) : "v"(ydr) : "memory"); SB();
  asm volatile("v_mfma_f32_16x16x32_bf16 %0, v[208:211], a[68:71], %0" : "+v"(g[0][1][0]) :  : ); SB();
  asm volatile("ds_read_b128 %0, %1 offset:256" : "=&v"(yq[1]) : "v"(ydr) : "memory"); SB();
  asm volatile("v_mfma_f32_16x16x32_bf16 %0, v[208:211], a[72:75], %0" : "+v"(g[0][2][0]) :  : ); SB();
  asm volatile("ds_read_b128 %0, %1 offset:8192" : "=&v"(yq[2]) : "v"(ydr) : "memory"); SB();
  asm volatile("v_mfma_f32_16x16x32_bf16 %0, v[208:211], a[76:79], %0" : "+v"(g[0][3][0]) :  : ); SB();
  asm volatile("ds_read_b128 %0, %1 offset:8448" : "=&v"(yq[3]) : "v"(ydr) : "memory"); SB();
  asm volatile("v_mfma_f32_16x16x32_bf16 %0, v[212:215], a[64:67], %0" : "+v"(g[0][0][1]) :  : ); SB();
  asm volatile("ds_read_b128 %0, %1 offset:16384" : "=&v"(yq[4]) : "v"(ydr) : "memory"); SB();
  asm volatile("v_mfma_f32_16x16x32_bf16 %0, v[212:215], a[68:71], %0" : "+v"(g[0][1][1]) :  : ); SB();
  asm volatile("ds_read_b128 %0, %1 offset:16640" : "=&v"(yq[5]) : "v"(ydr) : "memory"); SB();
  asm volatile("v_mfma_f32_16x16x32_bf16 %0, v[212:215], a[72:75], %0" : "+v"(g[0][2][1]) :  : ); SB();
  asm volatile("ds_read_b128 %0, %1 offset:24576" : "=&v"(yq[6]) : "v"(ydr) : "memory"); SB();
  asm volatile("v_mfma_f32_16x16x32_bf16 %0, v[212:215], a[76:79], %0" : "+v"(g[0][3][1]) :  : ); SB();
  asm volatile("ds_read_b128 %0, %1 offset:24832" : "=&v"(yq[7]) : "v"(ydr) : "memory"); SB();
  asm volatile("s_waitcnt vmcnt(15)" :  :  : "memory"); SB();
  asm volatile("v_mfma_f32_16x16x32_bf16 %0, v[216:219], a[80:83], %0" : "+v"(g[0][0][0]) :  : ); SB();
  asm volatile("ds_read_b128 %0, %1 offset:25856" : "=&v"(ykv) : "v"(ykr) : "memory"); SB();
  asm volatile("v_mfma_f32_16x16x32_bf16 %0, v[216:219], a[84:87], %0" : "+v"(g[0][1][0]) :  : ); SB();
  asm volatile("buffer_load_dwordx4 a[176:179], %0, %1, %2 offen offset:0 sc1" :  : "v"(vO), "s"(rWS), "s"(4096u) : "memory"); SB();
  asm volatile("v_mfma_f32_16x16x32_bf16 %0, v[216:219], a[88:91], %0" : "+v"(g[0][2][0]) :  : ); SB();
  asm volatile("buffer_load_dwordx4 a[180:183], %0, %1, %2 offen offset:1024 sc1" :  : "v"(vO), "s"(rWS), "s"(4096u) : "memory"); SB();
  asm volatile("v_mfma_f32_16x16x32_bf16 %0, v[216:219], a[92:95], %0" : "+v"(g[0][3][0]) :  : ); SB();
  asm volatile("v_mfma_f32_16x16x32_bf16 %0, v[220:223], a[80:83], %0" : "+v"(g[0][0][1]) :  : ); SB();
  asm volatile("v_mfma_f32_16x16x32_bf16 %0, v[220:223], a[84:87], %0" : "+v"(g[0][1][1]) :  : ); SB();
  asm volatile("buffer_load_dwordx4 a[184:187], %0, %1, %2 offen offset:2048 sc1" :  : "v"(vO), "s"(rWS), "s"(4096u) : "memory"); SB();
  asm volatile("v_mfma_f32_16x16x32_bf16 %0, v[220:223], a[88:91], %0" : "+v"(g[0][2][1]) :  : ); SB();
  asm volatile("v_mfma_f32_16x16x32_bf16 %0, v[220:223], a[92:95], %0" : "+v"(g[0][3][1]) :  : ); SB();
  asm volatile("buffer_load_dwordx4 a[188:191], %0, %1, %2 offen offset:3072 sc1" :  : "v"(vO), "s"(rWS), "s"(4096u) : "memory"); SB();
  asm volatile("buffer_load_dwordx4 a[192:195], %0, %1, %2 offen offset:0 sc1" :  : "v"(vO), "s"(rWS), "s"(8192u) : "memory"); SB();
  asm volatile("s_waitcnt vmcnt(12)\n\ts_barrier" :  :  : "memory"); SB();
  asm volatile("s_waitcnt vmcnt(10)" :  :  : "memory"); SB();
  asm volatile("v_mfma_f32_16x16x32_bf16 %0, v[224:227], a[100:103], %0" : "+v"(g[0][1][0]) :  : ); SB();
  asm volatile("buffer_load_dwordx4 a[196:199], %0, %1, %2 offen offset:1024 sc1" :  : "v"(vO), "s"(rWS), "s"(8192u) : "memory"); SB();
  asm volatile("v_mfma_f32_16x16x32_bf16 %0, v[228:231], a[100:103], %0" : "+v"(g[0][1][1]) :  : ); SB();
  asm volatile("ds_read2_b32 a[64:65], %0 offset0:176 offset1:177" :  : "v"(aa[0]) : "memory"); SB();
  asm volatile("v_mfma_f32_16x16x32_bf16 %0, v[232:235], a[116:119], %0" : "+v"(g[0][1][0]) :  : ); SB();
  asm volatile("ds_read2_b32 a[66:67], %0 offset0:178 offset1:179" :  : "v"(aa[0]) : "memory"); SB();
  asm volatile("v_mfma_f32_16x16x32_bf16 %0, v[236:239], a[116:119], %0" : "+v"(g[0][1][1]) :  : ); SB();
  asm volatile("ds_read_b128 a[60:63], %0 offset:3776" :  : "v"(bb) : "memory"); SB();
  asm volatile("v_mfma_f32_16x16x32_bf16 %0, v[240:243], a[132:135], %0" : "+v"(g[0][1][0]) :  : ); SB();
  asm volatile("buffer_load_dwordx4 a[200:203], %0, %1, %2 offen offset:0 sc1" :  : "v"(vO2), "s"(rWS), "s"(0u) : "memory"); SB();
  asm volatile("v_mfma_f32_16x16x32_bf16 %0, v[244:247], a[132:135], %0" : "+v"(g[0][1][1]) :  : ); SB();
  asm volatile("ds_read_b128 a[56:59], %0 offset:3264" :  : "v"(bb) : "memory"); SB();
  asm volatile("v_mfma_f32_16x16x32_bf16 %0, v[248:251], a[148:151], %0" : "+v"(g[0][1][0]) :  : ); SB();
  asm volatile("ds_read_b128 a[52:55], %0 offset:2752" :  : "v"(bb) : "memory"); SB();
  asm volatile("v_mfma_f32_16x16x32_bf16 %0, v[252:255], a[148:151], %0" : "+v"(g[0][1][1]) :  : ); SB();
  asm volatile("buffer_load_dwordx4 a[204:207], %0, %1, %2 offen offset:1024 sc1" :  : "v"(vO2), "s"(rWS), "s"(0u) : "memory"); SB();
  asm volatile("v_mfma_f32_16x16x32_bf16 %0, v[224:227], a[104:107], %0" : "+v"(g[0][2][0]) :  : ); SB();
  asm volatile("ds_read_b128 a[48:51], %0 offset:2240" :  : "v"(bb) : "memory"); SB();
  asm volatile("v_mfma_f32_16x16x32_bf16 %0, v[228:231], a[104:107], %0" : "+v"(g[0][2][1]) :  : ); SB();
  asm volatile("ds_read2_b32 a[68:69], %0 offset0:48 offset1:49" :  : "v"(aa[0]) : "memory"); SB();
  asm volatile("v_mfma_f32_16x16x32_bf16 %0, v[232:235], a[120:123], %0" : "+v"(g[0][2][0]) :  : ); SB();
  asm volatile("ds_write_b128 %0, %1 offset:0" :  : "v"(pw2), "v"(g[0][1][0]) : "memory"); SB();
  asm volatile("v_mfma_f32_16x16x32_bf16 %0, v[236:239], a[120:123], %0" : "+v"(g[0][2][1]) :  : ); SB();
  asm volatile("ds_read2_b32 a[70:71], %0 offset0:50 offset1:51" :  : "v"(aa[0]) : "memory"); SB();
  asm volatile("v_mfma_f32_16x16x32_bf16 %0, v[240:243], a[136:139], %0" : "+v"(g[0][2][0]) :  : ); SB();
  asm volatile("ds_read_b128 a[44:47], %0 offset:1728" :  : "v"(bb) : "memory"); SB();
  asm volatile("v_mfma_f32_16x16x32_bf16 %0, v[244:247], a[136:139], %0" : "+v"(g[0][2][1]) :  : ); SB();
  asm volatile("ds_write_b128 %0, %1 offset:1024" :  : "v"(pw2), "v"(g[0][1][1]) : "memory"); SB();
  asm volatile("v_mfma_f32_16x16x32_bf16 %0, v[248:251], a[152:155], %0" : "+v"(g[0][2][0]) :  : ); SB();
  asm volatile("ds_read2_b32 a[72:73], %0 offset0:176 offset1:177" :  : "v"(aa[1]) : "memory"); SB();
  asm volatile("v_mfma_f32_16x16x32_bf16 %0, v[252:255], a[152:155], %0" : "+v"(g[0][2][1]) :  : ); SB();
  asm volatile("ds_read2_b32 a[74:75], %0 offset0:178 offset1:179" :  : "v"(aa[1]) : "memory"); SB();
  asm volatile("v_mfma_f32_16x16x32_bf16 %0, v[224:227], a[108:111], %0" : "+v"(g[0][3][0]) :  : ); SB();
  asm volatile("ds_read2_b32 a[76:77], %0 offset0:48 offset1:49" :  : "v"(aa[1]) : "memory"); SB();
  asm volatile("v_mfma_f32_16x16x32_bf16 %0, v[228:231], a[108:111], %0" : "+v"(g[0][3][1]) :  : ); SB();
  asm volatile("ds_read2_b32 a[78:79], %0 offset0:50 offset1:51" :  : "v"(aa[1]) : "memory"); SB();
  asm volatile("v_mfma_f32_16x16x32_bf16 %0, v[232:235], a[124:127], %0" : "+v"(g[0][3][0]) :  : ); SB();
  asm volatile("ds_write_b128 %0, %1 offset:2048" :  : "v"(pw2), "v"(g[0][2][0]) : "memory"); SB();
  asm volatile("v_mfma_f32_16x16x32_bf16 %0, v[236:239], a[124:127], %0" : "+v"(g[0][3][1]) :  : ); SB();
  asm volatile("ds_read_b128 a[40:43], %0 offset:1216" :  : "v"(bb) : "memory"); SB();
  asm volatile("v_mfma_f32_16x16x32_bf16 %0, v[240:243], a[140:143], %0" : "+v"(g[0][3][0]) :  : ); SB();
  asm volatile("ds_read2_b32 a[80:81], %0 offset0:176 offset1:177" :  : "v"(aa[2]) : "memory"); SB();
  asm volatile("v_mfma_f32_16x16x32_bf16 %0, v[244:247], a[140:143], %0" : "+v"(g[0][3][1]) :  : ); SB();
  asm volatile("ds_write_b128 %0, %1 offset:3072" :  : "v"(pw2), "v"(g[0][2][1]) : "memory"); SB();
  asm volatile("v_mfma_f32_16x16x32_bf16 %0, v[248:251], a[156:159], %0" : "+v"(g[0][3][0]) :  : ); SB();
  asm volatile("s_waitcnt vmcnt(10)\n\ts_mov_b64 exec, 1\n\tbuffer_store_dwordx2 %0, off, %1, %2 sc1\n\ts_mov_b64 exec, -1" :  : "v"(((u32x2){tg0, tg1})), "s"(rWS), "s"(sFH) : "memory"); SB();
  asm volatile("buffer_load_dwordx4 a[208:211], %0, %1, %2 offen offset:2048 sc1" :  : "v"(vO2), "s"(rWS), "s"(0u) : "memory"); SB();
  asm volatile("v_mfma_f32_16x16x32_bf16 %0, v[252:255], a[156:159], %0" : "+v"(g[0][3][1]) :  : ); SB();
  asm volatile("ds_read2_b32 a[82:83], %0 offset0:178 offset1:179" :  : "v"(aa[2]) : "memory"); SB();
  asm volatile("v_mfma_f32_16x16x32_bf16 %0, v[224:227], a[96:99], %0" : "+v"(g[0][0][0]) :  : ); SB();
  asm volatile("ds_read2_b32 a[84:85], %0 offset0:48 offset1:49" :  : "v"(aa[2]) : "memory"); SB();
  asm volatile("v_mfma_f32_16x16x32_bf16 %0, v[228:231], a[96:99], %0" : "+v"(g[0][0][1]) :  : ); SB();
  asm volatile("buffer_load_dwordx4 a[212:215], %0, %1, %2 offen offset:3072 sc1" :  : "v"(vO2), "s"(rWS), "s"(0u) : "memory"); SB();
  asm volatile("v_mfma_f32_16x16x32_bf16 %0, v[232:235], a[112:115], %0" : "+v"(g[0][0][0]) :  : ); SB();
  asm volatile("ds_write_b128 %0, %1 offset:4096" :  : "v"(pw2), "v"(g[0][3][0]) : "memory"); SB();
  asm volatile("v_mfma_f32_16x16x32_bf16 %0, v[236:239], a[112:115], %0" : "+v"(g[0][0][1]) :  : ); SB();
  asm volatile("ds_read2_b32 a[86:87], %0 offset0:50 offset1:51" :  : "v"(aa[2]) : "memory"); SB();
  asm volatile("v_mfma_f32_16x16x32_bf16 %0, v[240:243], a[128:131], %0" : "+v"(g[0][0][0]) :  : ); SB();
  asm volatile("buffer_load_dwordx4 a[216:219], %0, %1, %2 offen offset:0 sc1" :  : "v"(vO2), "s"(rWS), "s"(4096u) : "memory"); SB();
  asm volatile("v_mfma_f32_16x16x32_bf16 %0, v[244:247], a[128:131], %0" : "+v"(g[0][0][1]) :  : ); SB();
  asm volatile("ds_write_b128 %0, %1 offset:5120" :  : "v"(pw2), "v"(g[0][3][1]) : "memory"); SB();
  asm volatile("v_mfma_f32_16x16x32_bf16 %0, v[248:251], a[144:147], %0" : "+v"(g[0][0][0]) :  : ); SB();
  asm volatile("buffer_load_dwordx4 a[220:223], %0, %1, %2 offen offset:1024 sc1" :  : "v"(vO2), "s"(rWS), "s"(4096u) : "memory"); SB();
  asm volatile("v_mfma_f32_16x16x32_bf16 %0, v[252:255], a[144:147], %0" : "+v"(g[0][0][1]) :  : ); SB();
  asm volatile("ds_read_b128 a[36:39], %0 offset:704" :  : "v"(bb) : "memory"); SB();
  if ((rb | cb | w) == 0) asm volatile("buffer_store_dwordx2 %0, off, %1, 0 sc1" :: "v"(((u32x2){tg0, tg1})), "s"(rWS) : "memory");
  SB();
  ST(17);
  unsigned pw3, p3[3], yw, ys[2], vYr, ya, po;
  asm volatile("s_waitcnt lgkmcnt(4)\n\tv_mfma_f32_16x16x32_bf16 %0, a[64:67], a[60:63], 0" : "=&v"(ac[7]) :  : ); SB();
  asm volatile("ds_read2_b32 a[88:89], %0 offset0:176 offset1:177" :  : "v"(aa[3]) : "memory"); SB();
  asm volatile("" : "+v"(yq[0]), "+v"(yq[1]), "+v"(yq[2]), "+v"(yq[3]), "+v"(yq[4]), "+v"(yq[5]), "+v"(yq[6]), "+v"(yq[7]), "+v"(ykv) :  : "memory"); SB();
  asm volatile("v_mfma_f32_16x16x32_bf16 %0, a[64:67], a[56:59], 0" : "=&v"(ac[6]) :  : ); SB();
  asm volatile("ds_read2_b32 a[90:91], %0 offset0:178 offset1:179" :  : "v"(aa[3]) : "memory"); SB();
  asm volatile("buffer_load_dwordx4 a[224:227], %0, %1, %2 offen offset:2048 sc1" :  : "v"(vO2), "s"(rWS), "s"(4096u) : "memory"); SB();
  asm volatile("v_mfma_f32_16x16x32_bf16 %0, a[64:67], a[52:55], 0" : "=&v"(ac[5]) :  : ); SB();
  asm volatile("ds_read2_b32 a[92:93], %0 offset0:48 offset1:49" :  : "v"(aa[3]) : "memory"); SB();
  asm volatile("v_add_f32 %0, %0, %1" : "+v"(yq[0][0]) : "v"(yq[2][0]) : ); SB();
  asm volatile("v_mfma_f32_16x16x32_bf16 %0, a[64:67], a[48:51], 0" : "=&v"(ac[4]) :  : ); SB();
  asm volatile("ds_read2_b32 a[94:95], %0 offset0:50 offset1:51" :  : "v"(aa[3]) : "memory"); SB();
  asm volatile("buffer_load_dwordx4 a[228:231], %0, %1, %2 offen offset:3072 sc1" :  : "v"(vO2), "s"(rWS), "s"(4096u) : "memory"); SB();
  asm volatile("v_mfma_f32_16x16x32_bf16 %0, a[68:71], a[56:59], %0" : "+v"(ac[7]) :  : ); SB();
  asm volatile("ds_read_b128 a[32:35], %0 offset:192" :  : "v"(bb) : "memory"); SB();
  asm volatile("s_waitcnt lgkmcnt(6)\n\ts_barrier" :  :  : "memory"); SB();
  asm volatile("v_mfma_f32_16x16x32_bf16 %0, a[68:71], a[52:55], %0" : "+v"(ac[6]) :  : ); SB();
  asm volatile("ds_read2_b32 a[128:129], %0 offset0:160 offset1:161" :  : "v"(aa[0]) : "memory"); SB();
  asm volatile("buffer_load_dwordx4 a[232:235], %0, %1, %2 offen offset:0 sc1" :  : "v"(vO2), "s"(rWS), "s"(8192u) : "memory"); SB();
  asm volatile("v_mfma_f32_16x16x32_bf16 %0, a[68:71], a[48:51], %0" : "+v"(ac[5]) :  : ); SB();
  asm volatile("ds_read2_b32 a[130:131], %0 offset0:162 offset1:163" :  : "v"(aa[0]) : "memory"); SB();
  asm volatile("v_add_f32 %0, %0, %1" : "+v"(yq[0][1]) : "v"(yq[2][1]) : ); SB();
  asm volatile("v_mfma_f32_16x16x32_bf16 %0, a[68:71], a[44:47], %0" : "+v"(ac[4]) :  : ); SB();
  asm volatile("ds_read_b128 %0, %1 offset:0" : "=&v"(pr[0]) : "v"(pe[0]) : "memory"); SB();
  asm volatile("v_add_f32 %0, %0, %1" : "+v"(yq[0][2]) : "v"(yq[2][2]) : ); SB();
  asm volatile("v_mfma_f32_16x16x32_bf16 %0, a[72:75], a[52:55], %0" : "+v"(ac[7]) :  : ); SB();
  asm volatile("ds_read_b128 a[124:127], %0 offset:3712" :  : "v"(bb) : "memory"); SB();
  asm volatile("buffer_load_dwordx4 a[236:239], %0, %1, %2 offen offset:1024 sc1" :  : "v"(vO2), "s"(rWS), "s"(8192u) : "memory"); SB();
  asm volatile("v_mfma_f32_16x16x32_bf16 %0, a[72:75], a[48:51], %0" : "+v"(ac[6]) :  : ); SB();
  asm volatile("ds_read_b128 a[120:123], %0 offset:3200" :  : "v"(bb) : "memory"); SB();
  asm volatile("v_add_f32 %0, %0, %1" : "+v"(yq[0][3]) : "v"(yq[2][3]) : ); SB();
  asm volatile("v_mfma_f32_16x16x32_bf16 %0, a[72:75], a[44:47], %0" : "+v"(ac[5]) :  : ); SB();
  asm volatile("ds_read_b128 a[116:119], %0 offset:2688" :  : "v"(bb) : "memory"); SB();
  asm volatile("v_add_f32 %0, %0, %1" : "+v"(yq[1][0]) : "v"(yq[3][0]) : ); SB();
  asm volatile("v_mfma_f32_16x16x32_bf16 %0, a[64:67], a[44:47], 0" : "=&v"(ac[3]) :  : ); SB();
  asm volatile("ds_read_b128 %0, %1 offset:1024" : "=&v"(pr[1]) : "v"(pe[0]) : "memory"); SB();
  asm volatile("v_add_f32 %0, %0, %1" : "+v"(yq[1][1]) : "v"(yq[3][1]) : ); SB();
  asm volatile("v_mfma_f32_16x16x32_bf16 %0, a[76:79], a[48:51], %0" : "+v"(ac[7]) :  : ); SB();
  asm volatile("ds_read_b128 a[112:115], %0 offset:2176" :  : "v"(bb) : "memory"); SB();
  asm volatile("buffer_load_dwordx4 a[240:243], %0, %1, %2 offen offset:2048 sc1" :  : "v"(vO), "s"(rWS), "s"(8192u) : "memory"); SB();
  asm volatile("v_mfma_f32_16x16x32_bf16 %0, a[76:79], a[44:47], %0" : "+v"(ac[6]) :  : ); SB();
  asm volatile("ds_read2_b32 a[132:133], %0 offset0:32 offset1:33" :  : "v"(aa[0]) : "memory"); SB();
  asm volatile("v_add_f32 %0, %0, %1" : "+v"(yq[1][2]) : "v"(yq[3][2]) : ); SB();
  asm volatile("v_mfma_f32_16x16x32_bf16 %0, a[76:79], a[40:43], %0" : "+v"(ac[5]) :  : ); SB();
  asm volatile("ds_read2_b32 a[134:135], %0 offset0:34 offset1:35" :  : "v"(aa[0]) : "memory"); SB();
  asm volatile("buffer_load_dwordx4 a[244:247], %0, %1, %2 offen offset:3072 sc1" :  : "v"(vO), "s"(rWS), "s"(8192u) : "memory"); SB();
  asm volatile("v_mfma_f32_16x16x32_bf16 %0, a[72:75], a[40:43], %0" : "+v"(ac[4]) :  : ); SB();
  asm volatile("ds_read_b128 %0, %1 offset:2048" : "=&v"(pr[2]) : "v"(pe[1]) : "memory"); SB();
  asm volatile("v_add_f32 %0, %0, %1" : "+v"(yq[1][3]) : "v"(yq[3][3]) : ); SB();
  asm volatile("v_mfma_f32_16x16x32_bf16 %0, a[80:83], a[44:47], %0" : "+v"(ac[7]) :  : ); SB();
  asm volatile("ds_read_b128 a[108:111], %0 offset:1664" :  : "v"(bb) : "memory"); SB();
  asm volatile("v_add_f32 %0, %0, %1" : "+v"(yq[4][0]) : "v"(yq[6][0]) : ); SB();
  asm volatile("v_mfma_f32_16x16x32_bf16 %0, a[80:83], a[40:43], %0" : "+v"(ac[6]) :  : ); SB();
  asm volatile("ds_read2_b32 a[136:137], %0 offset0:160 offset1:161" :  : "v"(aa[1]) : "memory"); SB();
  asm volatile("v_add_f32 %0, %0, %1" : "+v"(yq[4][1]) : "v"(yq[6][1]) : ); SB();
  asm volatile("v_mfma_f32_16x16x32_bf16 %0, a[68:71], a[40:43], %0" : "+v"(ac[3]) :  : ); SB();
  asm volatile("ds_read2_b32 a[138:139], %0 offset0:162 offset1:163" :  : "v"(aa[1]) : "memory"); SB();
  asm volatile("buffer_load_dwordx4 a[248:251], %0, %1, %2 offen offset:0 sc1" :  : "v"(vO), "s"(rWS), "s"(12288u) : "memory"); SB();
  asm volatile("v_mfma_f32_16x16x32_bf16 %0, a[64:67], a[40:43], 0" : "=&v"(ac[2]) :  : ); SB();
  asm volatile("ds_read_b128 %0, %1 offset:3072" : "=&v"(pr[3]) : "v"(pe[1]) : "memory"); SB();
  asm volatile("v_add_f32 %0, %0, %1" : "+v"(yq[4][2]) : "v"(yq[6][2]) : ); SB();
  asm volatile("v_mfma_f32_16x16x32_bf16 %0, a[84:87], a[40:43], %0" : "+v"(ac[7]) :  : ); SB();
  asm volatile("ds_read2_b32 a[140:141], %0 offset0:32 offset1:33" :  : "v"(aa[1]) : "memory"); SB();
  asm volatile("s_waitcnt lgkmcnt(8)\n\tv_mfma_f32_16x16x32_bf16 %0, a[84:87], a[36:39], %0" : "+v"(ac[6]) :  : ); SB();
  asm volatile("ds_read2_b32 a[142:143], %0 offset0:34 offset1:35" :  : "v"(aa[1]) : "memory"); SB();
  asm volatile("v_add_f32 %0, %0, %1" : "+v"(yq[4][3]) : "v"(yq[6][3]) : ); SB();
  asm volatile("v_mfma_f32_16x16x32_bf16 %0, a[80:83], a[36:39], %0" : "+v"(ac[5]) :  : ); SB();
  asm volatile("ds_read_b128 a[104:107], %0 offset:1152" :  : "v"(bb) : "memory"); SB();
  asm volatile("v_add_f32 %0, %0, %1" : "+v"(yq[5][0]) : "v"(yq[7][0]) : ); SB();
  asm volatile("v_mfma_f32_16x16x32_bf16 %0, a[76:79], a[36:39], %0" : "+v"(ac[4]) :  : ); SB();
  asm volatile("ds_read_b128 %0, %1 offset:4096" : "=&v"(pr[4]) : "v"(pe[2]) : "memory"); SB();
  asm volatile("v_add_f32 %0, %0, %1" : "+v"(yq[5][1]) : "v"(yq[7][1]) : ); SB();
  asm volatile("v_mfma_f32_16x16x32_bf16 %0, a[88:91], a[36:39], %0" : "+v"(ac[7]) :  : ); SB();
  asm volatile("ds_read2_b32 a[144:145], %0 offset0:160 offset1:161" :  : "v"(aa[2]) : "memory"); SB();
  asm volatile("v_add_f32 %0, %0, %1" : "+v"(yq[5][2]) : "v"(yq[7][2]) : ); SB();
  asm volatile("v_mfma_f32_16x16x32_bf16 %0, a[72:75], a[36:39], %0" : "+v"(ac[3]) :  : ); SB();
  asm volatile("ds_read2_b32 a[146:147], %0 offset0:162 offset1:163" :  : "v"(aa[2]) : "memory"); SB();
  asm volatile("v_add_f32 %0, %0, %1" : "+v"(yq[5][3]) : "v"(yq[7][3]) : ); SB();
  asm volatile("v_mfma_f32_16x16x32_bf16 %0, a[68:71], a[36:39], %0" : "+v"(ac[2]) :  : ); SB();
  asm volatile("ds_read2_b32 a[148:149], %0 offset0:32 offset1:33" :  : "v"(aa[2]) : "memory"); SB();
  asm volatile("v_add_f32 %0, %0, %1" : "+v"(yq[0][0]) : "v"(yq[4][0]) : ); SB();
  asm volatile("v_mfma_f32_16x16x32_bf16 %0, a[64:67], a[36:39], 0" : "=&v"(ac[1]) :  : ); SB();
  asm volatile("ds_read_b128 %0, %1 offset:5120" : "=&v"(pr[5]) : "v"(pe[2]) : "memory"); SB();
  asm volatile("v_add_f32 %0, %0, %1" : "+v"(yq[0][1]) : "v"(yq[4][1]) : ); SB();
  asm volatile("v_mfma_f32_16x16x32_bf16 %0, a[92:95], a[32:35], %0" : "+v"(ac[7]) :  : ); SB();
  asm volatile("ds_read2_b32 a[150:151], %0 offset0:34 offset1:35" :  : "v"(aa[2]) : "memory"); SB();
  asm volatile("v_add_f32 %0, %0, %1" : "+v"(yq[0][2]) : "v"(yq[4][2]) : ); SB();
  asm volatile("v_mfma_f32_16x16x32_bf16 %0, a[88:91], a[32:35], %0" : "+v"(ac[6]) :  : ); SB();
  asm volatile("ds_read_b128 a[100:103], %0 offset:640" :  : "v"(bb) : "memory"); SB();
  asm volatile("v_add_f32 %0, %0, %1" : "+v"(yq[0][3]) : "v"(yq[4][3]) : ); SB();
  asm volatile("v_mfma_f32_16x16x32_bf16 %0, a[84:87], a[32:35], %0" : "+v"(ac[5]) :  : ); SB();
  asm volatile("ds_read2_b32 a[152:153], %0 offset0:160 offset1:161" :  : "v"(aa[3]) : "memory"); SB();
  asm volatile("v_add_f32 %0, %0, %1" : "+v"(yq[1][0]) : "v"(yq[5][0]) : ); SB();
  asm volatile("v_mfma_f32_16x16x32_bf16 %0, a[80:83], a[32:35], %0" : "+v"(ac[4]) :  : ); SB();
  pw3 = lb + 98304 + w * 15360 + l16; asm volatile("" : "+v"(pw3)); SB();
  asm volatile("v_mfma_f32_16x16x32_bf16 %0, a[76:79], a[32:35], %0" : "+v"(ac[3]) :  : ); SB();
  asm volatile("ds_read2_b32 a[154:155], %0 offset0:162 offset1:163" :  : "v"(aa[3]) : "memory"); SB();
  asm volatile("v_add_f32 %0, %0, %1" : "+v"(yq[1][1]) : "v"(yq[5][1]) : ); SB();
  asm volatile("v_mfma_f32_16x16x32_bf16 %0, a[72:75], a[32:35], %0" : "+v"(ac[2]) :  : ); SB();
  asm volatile("ds_read2_b32 a[156:157], %0 offset0:32 offset1:33" :  : "v"(aa[3]) : "memory"); SB();
  asm volatile("s_waitcnt lgkmcnt(5)" : "+v"(pr[0]), "+v"(pr[1]), "+v"(pr[2]), "+v"(pr[3]), "+v"(pr[4]), "+v"(pr[5]) :  : "memory"); SB();
  asm volatile("v_mfma_f32_16x16x32_bf16 %0, a[68:71], a[32:35], %0" : "+v"(ac[1]) :  : ); SB();
  asm volatile("ds_read2_b32 a[158:159], %0 offset0:34 offset1:35" :  : "v"(aa[3]) : "memory"); SB();
  asm volatile("buffer_load_dwordx4 a[252:255], %0, %1, %2 offen offset:1024 sc1" :  : "v"(vO), "s"(rWS), "s"(12288u) : "memory"); SB();
  asm volatile("v_mfma_f32_16x16x32_bf16 %0, a[64:67], a[32:35], 0" : "=&v"(ac[0]) :  : ); SB();
  {
    const f32x4 h0 = ((g[0][0][0] + pr[0]) + (pr[2] + pr[4])) * a1[1][1], h1 = ((g[0][0][1] + pr[1]) + (pr[3] + pr[5])) * a1[1][2];
    ho[2] = (u32x4){pack2(h0[0], h0[1]), pack2(h0[2], h0[3]), pack2(h1[0], h1[1]), nohole(pack2(h1[2], h1[3]))};
    SB();
  }
  asm volatile("v_mfma_f32_16x16x32_bf16 %0, a[128:131], a[124:127], %0" : "+v"(ac[7]) :  : ); SB();
  asm volatile("ds_read_b128 a[96:99], %0 offset:128" :  : "v"(bb) : "memory"); SB();
  asm volatile("buffer_load_dwordx4 a[0:3], %0, %1, %2 offen offset:2048 sc1" :  : "v"(vO), "s"(rWS), "s"(12288u) : "memory"); SB();
  asm volatile("v_mfma_f32_16x16x32_bf16 %0, a[128:131], a[120:123], %0" : "+v"(ac[6]) :  : ); SB();
  asm volatile("ds_read2_b32 a[64:65], %0 offset0:144 offset1:145" :  : "v"(aa[0]) : "memory"); SB();
  asm volatile("buffer_load_dwordx4 a[4:7], %0, %1, %2 offen offset:2048 sc1" :  : "v"(vO2), "s"(rWS), "s"(8192u) : "memory"); SB();
  asm volatile("v_mfma_f32_16x16x32_bf16 %0, a[128:131], a[116:119], %0" : "+v"(ac[5]) :  : ); SB();
  asm volatile("ds_read2_b32 a[66:67], %0 offset0:146 offset1:147" :  : "v"(aa[0]) : "memory"); SB();
  asm volatile("buffer_load_dwordx4 a[8:11], %0, %1, %2 offen offset:3072 sc1" :  : "v"(vO2), "s"(rWS), "s"(8192u) : "memory"); SB();
  asm volatile("v_mfma_f32_16x16x32_bf16 %0, a[128:131], a[112:115], %0" : "+v"(ac[4]) :  : ); SB();
  asm volatile("buffer_store_dwordx4 %1, %0, %2, %3 offen offset:2048 sc1\n\ts_nop 1" :  : "v"(vH_), "v"(ho[2]), "s"(rWS), "s"(0u) : "memory"); SB();
  asm volatile("v_mfma_f32_16x16x32_bf16 %0, a[132:135], a[120:123], %0" : "+v"(ac[7]) :  : ); SB();
  asm volatile("ds_read_b128 a[60:63], %0 offset:3648" :  : "v"(bb) : "memory"); SB();
  asm volatile("v_add_f32 %0, %0, %1" : "+v"(yq[1][2]) : "v"(yq[5][2]) : ); SB();
  asm volatile("v_mfma_f32_16x16x32_bf16 %0, a[132:135], a[116:119], %0" : "+v"(ac[6]) :  : ); SB();
  asm volatile("ds_read_b128 a[56:59], %0 offset:3136" :  : "v"(bb) : "memory"); SB();
  asm volatile("v_add_f32 %0, %0, %1" : "+v"(yq[1][3]) : "v"(yq[5][3]) : ); SB();
  asm volatile("v_mfma_f32_16x16x32_bf16 %0, a[132:135], a[112:115], %0" : "+v"(ac[5]) :  : ); SB();
  asm volatile("ds_read_b128 a[52:55], %0 offset:2624" :  : "v"(bb) : "memory"); SB();
  asm volatile("v_lshlrev_b32 %0, 16, %1" : "=v"(yq[2][0]) : "v"(ykv[0]) : ); SB();
  asm volatile("v_mfma_f32_16x16x32_bf16 %0, a[132:135], a[108:111], %0" : "+v"(ac[4]) :  : ); SB();
  p3[0] = lb + 98304 + (w ^ 1) * 15360 + l16; asm volatile("" : "+v"(p3[0])); SB();
  asm volatile("v_mfma_f32_16x16x32_bf16 %0, a[136:139], a[116:119], %0" : "+v"(ac[7]) :  : ); SB();
  asm volatile("ds_read_b128 a[48:51], %0 offset:2112" :  : "v"(bb) : "memory"); SB();
  asm volatile("buffer_load_dwordx4 a[12:15], %0, %1, %2 offen offset:0 sc1" :  : "v"(vO2), "s"(rWS), "s"(12288u) : "memory"); SB();
  asm volatile("v_mfma_f32_16x16x32_bf16 %0, a[136:139], a[112:115], %0" : "+v"(ac[6]) :  : ); SB();
  asm volatile("ds_read2_b32 a[68:69], %0 offset0:16 offset1:17" :  : "v"(aa[0]) : "memory"); SB();
  asm volatile("v_and_b32 %0, 0xffff0000, %1" : "=v"(yq[2][1]) : "v"(ykv[0]) : ); SB();
  asm volatile("v_mfma_f32_16x16x32_bf16 %0, a[136:139], a[108:111], %0" : "+v"(ac[5]) :  : ); SB();
  asm volatile("ds_read2_b32 a[70:71], %0 offset0:18 offset1:19" :  : "v"(aa[0]) : "memory"); SB();
  asm volatile("v_lshlrev_b32 %0, 16, %1" : "=v"(yq[2][2]) : "v"(ykv[1]) : ); SB();
  asm volatile("v_mfma_f32_16x16x32_bf16 %0, a[128:131], a[108:111], %0" : "+v"(ac[3]) :  : ); SB();
  p3[1] = lb + 98304 + (w ^ 2) * 15360 + l16; asm volatile("" : "+v"(p3[1])); SB();
  asm volatile("v_mfma_f32_16x16x32_bf16 %0, a[140:143], a[112:115], %0" : "+v"(ac[7]) :  : ); SB();
  asm volatile("ds_read_b128 a[44:47], %0 offset:1600" :  : "v"(bb) : "memory"); SB();
  asm volatile("buffer_load_dwordx4 a[16:19], %0, %1, %2 offen offset:1024 sc1" :  : "v"(vO2), "s"(rWS), "s"(12288u) : "memory"); SB();
  asm volatile("v_mfma_f32_16x16x32_bf16 %0, a[140:143], a[108:111], %0" : "+v"(ac[6]) :  : ); SB();
  asm volatile("ds_read2_b32 a[72:73], %0 offset0:144 offset1:145" :  : "v"(aa[1]) : "memory"); SB();
  asm volatile("v_and_b32 %0, 0xffff0000, %1" : "=v"(yq[2][3]) : "v"(ykv[1]) : ); SB();
  asm volatile("v_mfma_f32_16x16x32_bf16 %0, a[140:143], a[104:107], %0" : "+v"(ac[5]) :  : ); SB();
  asm volatile("ds_read2_b32 a[74:75], %0 offset0:146 offset1:147" :  : "v"(aa[1]) : "memory"); SB();
  asm volatile("buffer_load_dwordx4 a[20:23], %0, %1, %2 offen offset:2048 sc1" :  : "v"(vO2), "s"(rWS), "s"(12288u) : "memory"); SB();
  asm volatile("v_mfma_f32_16x16x32_bf16 %0, a[136:139], a[104:107], %0" : "+v"(ac[4]) :  : ); SB();
  p3[2] = lb + 98304 + (w ^ 3) * 15360 + l16; asm volatile("" : "+v"(p3[2])); SB();
  asm volatile("v_mfma_f32_16x16x32_bf16 %0, a[144:147], a[108:111], %0" : "+v"(ac[7]) :  : ); SB();
  asm volatile("ds_read2_b32 a[76:77], %0 offset0:16 offset1:17" :  : "v"(aa[1]) : "memory"); SB();
  asm volatile("v_lshlrev_b32 %0, 16, %1" : "=v"(yq[3][0]) : "v"(ykv[2]) : ); SB();
  asm volatile("v_mfma_f32_16x16x32_bf16 %0, a[144:147], a[104:107], %0" : "+v"(ac[6]) :  : ); SB();
  asm volatile("ds_read2_b32 a[78:79], %0 offset0:18 offset1:19" :  : "v"(aa[1]) : "memory"); SB();
  asm volatile("v_and_b32 %0, 0xffff0000, %1" : "=v"(yq[3][1]) : "v"(ykv[2]) : ); SB();
  asm volatile("v_mfma_f32_16x16x32_bf16 %0, a[132:135], a[104:107], %0" : "+v"(ac[3]) :  : ); SB();
  asm volatile("ds_read_b128 a[40:43], %0 offset:1088" :  : "v"(bb) : "memory"); SB();
  asm volatile("buffer_load_dwordx2 %0, %1, %2, %3 offen sc1" : "=&v"(fh) : "v"(vFH), "s"(rWS), "s"(0u) : "memory"); SB();
  asm volatile("v_mfma_f32_16x16x32_bf16 %0, a[128:131], a[104:107], %0" : "+v"(ac[2]) :  : ); SB();
  yw = (lane ^ ((lane >> 3) & 6)) * 16; asm volatile("" : "+v"(yw)); SB();
  asm volatile("s_waitcnt lgkmcnt(6)\n\tv_mfma_f32_16x16x32_bf16 %0, a[148:151], a[104:107], %0" : "+v"(ac[7]) :  : ); SB();
  asm volatile("ds_read2_b32 a[80:81], %0 offset0:144 offset1:145" :  : "v"(aa[2]) : "memory"); SB();
  asm volatile("v_lshlrev_b32 %0, 16, %1" : "=v"(yq[3][2]) : "v"(ykv[3]) : ); SB();
  asm volatile("v_mfma_f32_16x16x32_bf16 %0, a[148:151], a[100:103], %0" : "+v"(ac[6]) :  : ); SB();
  asm volatile("ds_read2_b32 a[82:83], %0 offset0:146 offset1:147" :  : "v"(aa[2]) : "memory"); SB();
  asm volatile("v_and_b32 %0, 0xffff0000, %1" : "=v"(yq[3][3]) : "v"(ykv[3]) : ); SB();
  asm volatile("v_mfma_f32_16x16x32_bf16 %0, a[144:147], a[100:103], %0" : "+v"(ac[5]) :  : ); SB();
  asm volatile("ds_read2_b32 a[84:85], %0 offset0:16 offset1:17" :  : "v"(aa[2]) : "memory"); SB();
  asm volatile("v_fmac_f32 %0, %1, %2" : "+v"(yq[0][0]) : "v"(yq[2][0]), "v"(dD[2]) : ); SB();
  asm volatile("v_mfma_f32_16x16x32_bf16 %0, a[140:143], a[100:103], %0" : "+v"(ac[4]) :  : ); SB();
  yw = yw + lb + 32768 + w * 3072; asm volatile("" : "+v"(yw)); SB();
  asm volatile("v_mfma_f32_16x16x32_bf16 %0, a[152:155], a[100:103], %0" : "+v"(ac[7]) :  : ); SB();
  asm volatile("ds_read2_b32 a[86:87], %0 offset0:18 offset1:19" :  : "v"(aa[2]) : "memory"); SB();
  asm volatile("v_fmac_f32 %0, %1, %2" : "+v"(yq[0][1]) : "v"(yq[2][1]), "v"(dD[2]) : ); SB();
  asm volatile("v_mfma_f32_16x16x32_bf16 %0, a[136:139], a[100:103], %0" : "+v"(ac[3]) :  : ); SB();
  asm volatile("ds_read_b128 a[36:39], %0 offset:576" :  : "v"(bb) : "memory"); SB();
  asm volatile("v_fmac_f32 %0, %1, %2" : "+v"(yq[0][2]) : "v"(yq[2][2]), "v"(dD[2]) : ); SB();
  asm volatile("v_mfma_f32_16x16x32_bf16 %0, a[132:135], a[100:103], %0" : "+v"(ac[2]) :  : ); SB();
  asm volatile("ds_read2_b32 a[88:89], %0 offset0:144 offset1:145" :  : "v"(aa[3]) : "memory"); SB();
  asm volatile("v_fmac_f32 %0, %1, %2" : "+v"(yq[0][3]) : "v"(yq[2][3]), "v"(dD[2]) : ); SB();
  asm volatile("v_mfma_f32_16x16x32_bf16 %0, a[128:131], a[100:103], %0" : "+v"(ac[1]) :  : ); SB();
  ya = lb + 32768 + (4 * g4 + q4) * 128 + 8 * p4; asm volatile("" : "+v"(ya)); SB();
  asm volatile("v_mfma_f32_16x16x32_bf16 %0, a[156:159], a[96:99], %0" : "+v"(ac[7]) :  : ); SB();
  asm volatile("ds_read2_b32 a[90:91], %0 offset0:146 offset1:147" :  : "v"(aa[3]) : "memory"); SB();
  asm volatile("v_fmac_f32 %0, %1, %2" : "+v"(yq[1][0]) : "v"(yq[3][0]), "v"(dD[2]) : ); SB();
  asm volatile("v_mfma_f32_16x16x32_bf16 %0, a[152:155], a[96:99], %0" : "+v"(ac[6]) :  : ); SB();
  asm volatile("ds_read2_b32 a[92:93], %0 offset0:16 offset1:17" :  : "v"(aa[3]) : "memory"); SB();
  asm volatile("v_fmac_f32 %0, %1, %2" : "+v"(yq[1][1]) : "v"(yq[3][1]), "v"(dD[2]) : ); SB();
  asm volatile("v_mfma_f32_16x16x32_bf16 %0, a[148:151], a[96:99], %0" : "+v"(ac[5]) :  : ); SB();
  asm volatile("ds_read2_b32 a[94:95], %0 offset0:18 offset1:19" :  : "v"(aa[3]) : "memory"); SB();
  asm volatile("v_fmac_f32 %0, %1, %2" : "+v"(yq[1][2]) : "v"(yq[3][2]), "v"(dD[2]) : ); SB();
  asm volatile("v_mfma_f32_16x16x32_bf16 %0, a[144:147], a[96:99], %0" : "+v"(ac[4]) :  : ); SB();
  po = (2 * g4 + (q4 >> 1)) & 3; asm volatile("" : "+v"(po)); SB();
  asm volatile("v_mfma_f32_16x16x32_bf16 %0, a[140:143], a[96:99], %0" : "+v"(ac[3]) :  : ); SB();
  asm volatile("ds_read_b128 a[32:35], %0 offset:64" :  : "v"(bb) : "memory"); SB();
  asm volatile("v_fmac_f32 %0, %1, %2" : "+v"(yq[1][3]) : "v"(yq[3][3]), "v"(dD[2]) : ); SB();
  asm volatile("v_mfma_f32_16x16x32_bf16 %0, a[136:139], a[96:99], %0" : "+v"(ac[2]) :  : ); SB();
  ya = ya + ((w ^ po) << 5); asm volatile("" : "+v"(ya)); SB();
  asm volatile("v_mfma_f32_16x16x32_bf16 %0, a[132:135], a[96:99], %0" : "+v"(ac[1]) :  : ); SB();
  po = vX + 24 * (g4 & 1); asm volatile("" : "+v"(po)); SB();
  asm volatile("v_mfma_f32_16x16x32_bf16 %0, a[128:131], a[96:99], %0" : "+v"(ac[0]) :  : ); SB();
  asm volatile("v_cvt_pk_bf16_f32 %0, %1, %2" : "=v"(y2[0]) : "v"(yq[0][0]), "v"(yq[0][1]) : ); SB();
  asm volatile("v_mfma_f32_16x16x32_bf16 %0, a[64:67], a[60:63], %0" : "+v"(ac[7]) :  : ); SB();
  asm volatile("ds_read2_b32 a[128:129], %0 offset0:128 offset1:129" :  : "v"(aa[0]) : "memory"); SB();
  asm volatile("v_cvt_pk_bf16_f32 %0, %1, %2" : "=v"(y2[1]) : "v"(yq[0][2]), "v"(yq[0][3]) : ); SB();
  asm volatile("v_mfma_f32_16x16x32_bf16 %0, a[64:67], a[56:59], %0" : "+v"(ac[6]) :  : ); SB();
  asm volatile("ds_read2_b32 a[130:131], %0 offset0:130 offset1:131" :  : "v"(aa[0]) : "memory"); SB();
  asm volatile("v_cvt_pk_bf16_f32 %0, %1, %2" : "=v"(y2[2]) : "v"(yq[1][0]), "v"(yq[1][1]) : ); SB();
  asm volatile("v_mfma_f32_16x16x32_bf16 %0, a[64:67], a[52:55], %0" : "+v"(ac[5]) :  : ); SB();
  asm volatile("ds_read_b128 a[124:127], %0 offset:3584" :  : "v"(bb) : "memory"); SB();
  asm volatile("v_cvt_pk_bf16_f32 %0, %1, %2" : "=v"(y2[3]) : "v"(yq[1][2]), "v"(yq[1][3]) : ); SB();
  asm volatile("v_mfma_f32_16x16x32_bf16 %0, a[64:67], a[48:51], %0" : "+v"(ac[4]) :  : ); SB();
  asm volatile("v_min_u32 %0, -2, %0" : "+v"(y2[3]) :  : ); SB();
  asm volatile("v_mfma_f32_16x16x32_bf16 %0, a[68:71], a[56:59], %0" : "+v"(ac[7]) :  : ); SB();
  asm volatile("ds_read_b128 a[120:123], %0 offset:3072" :  : "v"(bb) : "memory"); SB();
  asm volatile("v_xnor_b32 %0, %0, %1" : "+v"(y2[3]) : "v"(ykv[3]) : ); SB();
  asm volatile("v_mfma_f32_16x16x32_bf16 %0, a[68:71], a[52:55], %0" : "+v"(ac[6]) :  : ); SB();
  asm volatile("ds_read_b128 a[96:99], %0 offset:0" :  : "v"(bb) : "memory"); SB();
  asm volatile("v_mfma_f32_16x16x32_bf16 %0, a[68:71], a[48:51], %0" : "+v"(ac[5]) :  : ); SB();
  asm volatile("ds_read_b128 a[100:103], %0 offset:512" :  : "v"(bb) : "memory"); SB();
  asm volatile("buffer_store_dwordx4 %1, %0, %2, %3 offen offset:0 sc1\n\ts_nop 1" :  : "v"(vF), "v"(y2), "s"(rWS), "s"(uso) : "memory"); SB();
  asm volatile("s_waitcnt lgkmcnt(6)\n\tv_mfma_f32_16x16x32_bf16 %0, a[68:71], a[44:47], %0" : "+v"(ac[4]) :  : ); SB();
  asm volatile("v_mfma_f32_16x16x32_bf16 %0, a[72:75], a[52:55], %0" : "+v"(ac[7]) :  : ); SB();
  asm volatile("ds_read2_b32 a[132:133], %0 offset0:0 offset1:1" :  : "v"(aa[0]) : "memory"); SB();
  asm volatile("v_mfma_f32_16x16x32_bf16 %0, a[72:75], a[48:51], %0" : "+v"(ac[6]) :  : ); SB();
  asm volatile("ds_read2_b32 a[134:135], %0 offset0:2 offset1:3" :  : "v"(aa[0]) : "memory"); SB();
  asm volatile("v_mfma_f32_16x16x32_bf16 %0, a[72:75], a[44:47], %0" : "+v"(ac[5]) :  : ); SB();
  asm volatile("ds_read_b128 a[116:119], %0 offset:2560" :  : "v"(bb) : "memory"); SB();
  asm volatile("v_mfma_f32_16x16x32_bf16 %0, a[64:67], a[44:47], %0" : "+v"(ac[3]) :  : ); SB();
  asm volatile("v_mfma_f32_16x16x32_bf16 %0, a[76:79], a[48:51], %0" : "+v"(ac[7]) :  : ); SB();
  asm volatile("ds_read_b128 a[104:107], %0 offset:1024" :  : "v"(bb) : "memory"); SB();
  asm volatile("v_mfma_f32_16x16x32_bf16 %0, a[76:79], a[44:47], %0" : "+v"(ac[6]) :  : ); SB();
  asm volatile("ds_read2_b32 a[136:137], %0 offset0:128 offset1:129" :  : "v"(aa[1]) : "memory"); SB();
  asm volatile("v_mfma_f32_16x16x32_bf16 %0, a[76:79], a[40:43], %0" : "+v"(ac[5]) :  : ); SB();
  asm volatile("ds_read2_b32 a[138:139], %0 offset0:130 offset1:131" :  : "v"(aa[1]) : "memory"); SB();
  asm volatile("v_mfma_f32_16x16x32_bf16 %0, a[72:75], a[40:43], %0" : "+v"(ac[4]) :  : ); SB();
  asm volatile("v_mfma_f32_16x16x32_bf16 %0, a[80:83], a[44:47], %0" : "+v"(ac[7]) :  : ); SB();
  asm volatile("ds_read_b128 a[108:111], %0 offset:1536" :  : "v"(bb) : "memory"); SB();
  asm volatile("v_mfma_f32_16x16x32_bf16 %0, a[80:83], a[40:43], %0" : "+v"(ac[6]) :  : ); SB();
  asm volatile("ds_read_b128 a[112:115], %0 offset:2048" :  : "v"(bb) : "memory"); SB();
  asm volatile("v_mfma_f32_16x16x32_bf16 %0, a[68:71], a[40:43], %0" : "+v"(ac[3]) :  : ); SB();
  asm volatile("ds_read2_b32 a[140:141], %0 offset0:0 offset1:1" :  : "v"(aa[1]) : "memory"); SB();
  asm volatile("v_mfma_f32_16x16x32_bf16 %0, a[64:67], a[40:43], %0" : "+v"(ac[2]) :  : ); SB();
  asm volatile("v_mfma_f32_16x16x32_bf16 %0, a[84:87], a[40:43], %0" : "+v"(ac[7]) :  : ); SB();
  asm volatile("ds_read2_b32 a[142:143], %0 offset0:2 offset1:3" :  : "v"(aa[1]) : "memory"); SB();
  asm volatile("v_mfma_f32_16x16x32_bf16 %0, a[84:87], a[36:39], %0" : "+v"(ac[6]) :  : ); SB();
  asm volatile("ds_read2_b32 a[144:145], %0 offset0:128 offset1:129" :  : "v"(aa[2]) : "memory"); SB();
  asm volatile("v_mfma_f32_16x16x32_bf16 %0, a[80:83], a[36:39], %0" : "+v"(ac[5]) :  : ); SB();
  asm volatile("ds_read2_b32 a[146:147], %0 offset0:130 offset1:131" :  : "v"(aa[2]) : "memory"); SB();
  asm volatile("v_mfma_f32_16x16x32_bf16 %0, a[76:79], a[36:39], %0" : "+v"(ac[4]) :  : ); SB();
  asm volatile("v_mfma_f32_16x16x32_bf16 %0, a[88:91], a[36:39], %0" : "+v"(ac[7]) :  : ); SB();
  asm volatile("ds_read2_b32 a[148:149], %0 offset0:0 offset1:1" :  : "v"(aa[2]) : "memory"); SB();
  asm volatile("v_mfma_f32_16x16x32_bf16 %0, a[72:75], a[36:39], %0" : "+v"(ac[3]) :  : ); SB();
  asm volatile("ds_read2_b32 a[150:151], %0 offset0:2 offset1:3" :  : "v"(aa[2]) : "memory"); SB();
  asm volatile("v_mfma_f32_16x16x32_bf16 %0, a[68:71], a[36:39], %0" : "+v"(ac[2]) :  : ); SB();
  asm volatile("ds_read2_b32 a[152:153], %0 offset0:128 offset1:129" :  : "v"(aa[3]) : "memory"); SB();
  asm volatile("v_mfma_f32_16x16x32_bf16 %0, a[64:67], a[36:39], %0" : "+v"(ac[1]) :  : ); SB();
  asm volatile("v_mfma_f32_16x16x32_bf16 %0, a[92:95], a[32:35], %0" : "+v"(ac[7]) :  : ); SB();
  asm volatile("ds_read2_b32 a[154:155], %0 offset0:130 offset1:131" :  : "v"(aa[3]) : "memory"); SB();
  asm volatile("v_mfma_f32_16x16x32_bf16 %0, a[88:91], a[32:35], %0" : "+v"(ac[6]) :  : ); SB();
  asm volatile("ds_read2_b32 a[156:157], %0 offset0:0 offset1:1" :  : "v"(aa[3]) : "memory"); SB();
  asm volatile("v_mfma_f32_16x16x32_bf16 %0, a[84:87], a[32:35], %0" : "+v"(ac[5]) :  : ); SB();
  asm volatile("ds_read2_b32 a[158:159], %0 offset0:2 offset1:3" :  : "v"(aa[3]) : "memory"); SB();
  asm volatile("v_mfma_f32_16x16x32_bf16 %0, a[80:83], a[32:35], %0" : "+v"(ac[4]) :  : ); SB();
  asm volatile("v_mfma_f32_16x16x32_bf16 %0, a[76:79], a[32:35], %0" : "+v"(ac[3]) :  : ); SB();
  asm volatile("v_mfma_f32_16x16x32_bf16 %0, a[72:75], a[32:35], %0" : "+v"(ac[2]) :  : ); SB();
  asm volatile("v_mfma_f32_16x16x32_bf16 %0, a[68:71], a[32:35], %0" : "+v"(ac[1]) :  : ); SB();
  asm volatile("v_mfma_f32_16x16x32_bf16 %0, a[64:67], a[32:35], %0" : "+v"(ac[0]) :  : ); SB();
  asm volatile("s_waitcnt lgkmcnt(4)\n\tv_mfma_f32_16x16x32_bf16 %0, a[128:131], a[124:127], %0" : "+v"(ac[7]) :  : ); SB();
  asm volatile("v_mfma_f32_16x16x32_bf16 %0, a[128:131], a[120:123], %0" : "+v"(ac[6]) :  : ); SB();
  asm volatile("v_mfma_f32_16x16x32_bf16 %0, a[128:131], a[96:99], %0" : "+v"(ac[0]) :  : ); SB();
  asm volatile("v_mfma_f32_16x16x32_bf16 %0, a[128:131], a[100:103], %0" : "+v"(ac[1]) :  : ); SB();
  asm volatile("v_mfma_f32_16x16x32_bf16 %0, a[132:135], a[120:123], %0" : "+v"(ac[7]) :  : ); SB();
  asm volatile("v_mfma_f32_16x16x32_bf16 %0, a[132:135], a[116:119], %0" : "+v"(ac[6]) :  : ); SB();
  asm volatile("v_mfma_f32_16x16x32_bf16 %0, a[128:131], a[104:107], %0" : "+v"(ac[2]) :  : ); SB();
  asm volatile("ds_write_b128 %0, %1 offset:0" :  : "v"(dd), "v"(ac[0]) : "memory"); SB();
  asm volatile("v_mfma_f32_16x16x32_bf16 %0, a[132:135], a[96:99], %0" : "+v"(ac[1]) :  : ); SB();
  asm volatile("s_waitcnt vmcnt(1)\n\t.Lc_%=:\n\tv_cmp_ne_u64 vcc, %2, %1\n\ts_cbranch_vccnz .Lr_%=\n\t.Ld_%=:\n\t.subsection 1\n\t.Lr_%=:\n\tv_add_u32 %0, -1, %0\n\ts_nop 1\n\tv_readfirstlane_b32 vcc_hi, %0\n\ts_nop 3\n\ts_cmp_lt_i32 vcc_hi, 1\n\ts_cbranch_scc1 .Ld_%=\n\ts_sleep 2\n\ts_mov_b32 vcc_lo, 0\n\tbuffer_load_dwordx2 %1, %3, %4, vcc_lo offen sc1\n\ts_waitcnt vmcnt(0)\n\ts_branch .Lc_%=\n\t.subsection 0" : "+v"(tmo), "+v"(fh) : "s"(tg), "v"(vFH), "s"(rWS) : "memory", "vcc", "scc"); SB();
  asm volatile("buffer_load_dwordx4 v[164:167], %0, %1, %2 offen offset:0 sc1" :  : "v"(vHr), "s"(rWS), "s"(hso[2]) : "memory"); SB();
  asm volatile("v_mfma_f32_16x16x32_bf16 %0, a[136:139], a[116:119], %0" : "+v"(ac[7]) :  : ); SB();
  asm volatile("buffer_load_dwordx4 v[168:171], %0, %1, %2 offen offset:0 sc1" :  : "v"(vHr), "s"(rWS), "s"(hso[4]) : "memory"); SB();
  asm volatile("v_mfma_f32_16x16x32_bf16 %0, a[128:131], a[108:111], %0" : "+v"(ac[3]) :  : ); SB();
  asm volatile("buffer_load_dwordx4 v[172:175], %0, %1, %2 offen offset:0 sc1" :  : "v"(vHr), "s"(rWS), "s"(hso[6]) : "memory"); SB();
  asm volatile("v_mfma_f32_16x16x32_bf16 %0, a[132:135], a[100:103], %0" : "+v"(ac[2]) :  : ); SB();
  asm volatile("v_mfma_f32_16x16x32_bf16 %0, a[128:131], a[112:115], %0" : "+v"(ac[4]) :  : ); SB();
  asm volatile("buffer_load_dwordx4 v[160:163], %0, %1, %2 offen offset:0 sc1" :  : "v"(vHr), "s"(rWS), "s"(hso[0]) : "memory"); SB();
  asm volatile("v_mfma_f32_16x16x32_bf16 %0, a[140:143], a[112:115], %0" : "+v"(ac[7]) :  : ); SB();
  asm volatile("ds_write_b128 %0, %1 offset:1024" :  : "v"(dd), "v"(ac[1]) : "memory"); SB();
  asm volatile("v_mfma_f32_16x16x32_bf16 %0, a[132:135], a[104:107], %0" : "+v"(ac[3]) :  : ); SB();
  asm volatile("buffer_load_dwordx4 v[180:183], %0, %1, %2 offen offset:1024 sc1" :  : "v"(vHr), "s"(rWS), "s"(hso[2]) : "memory"); SB();
  asm volatile("v_mfma_f32_16x16x32_bf16 %0, a[136:139], a[96:99], %0" : "+v"(ac[2]) :  : ); SB();
  asm volatile("v_mfma_f32_16x16x32_bf16 %0, a[132:135], a[108:111], %0" : "+v"(ac[4]) :  : ); SB();
  asm volatile("v_mfma_f32_16x16x32_bf16 %0, a[128:131], a[116:119], %0" : "+v"(ac[5]) :  : ); SB();
  asm volatile("v_mfma_f32_16x16x32_bf16 %0, a[136:139], a[100:103], %0" : "+v"(ac[3]) :  : ); SB();
  asm volatile("buffer_load_dwordx4 v[184:187], %0, %1, %2 offen offset:1024 sc1" :  : "v"(vHr), "s"(rWS), "s"(hso[4]) : "memory"); SB();
  asm volatile("v_mfma_f32_16x16x32_bf16 %0, a[136:139], a[112:115], %0" : "+v"(ac[6]) :  : ); SB();
  asm volatile("ds_write_b128 %0, %1 offset:2048" :  : "v"(dd), "v"(ac[2]) : "memory"); SB();
  asm volatile("v_mfma_f32_16x16x32_bf16 %0, a[136:139], a[104:107], %0" : "+v"(ac[4]) :  : ); SB();
  asm volatile("buffer_load_dwordx4 v[188:191], %0, %1, %2 offen offset:1024 sc1" :  : "v"(vHr), "s"(rWS), "s"(hso[6]) : "memory"); SB();
  asm volatile("v_mfma_f32_16x16x32_bf16 %0, a[132:135], a[112:115], %0" : "+v"(ac[5]) :  : ); SB();
  asm volatile("v_mfma_f32_16x16x32_bf16 %0, a[140:143], a[96:99], %0" : "+v"(ac[3]) :  : ); SB();
  asm volatile("v_mfma_f32_16x16x32_bf16 %0, a[140:143], a[108:111], %0" : "+v"(ac[6]) :  : ); SB();
  asm volatile("v_mfma_f32_16x16x32_bf16 %0, a[140:143], a[100:103], %0" : "+v"(ac[4]) :  : ); SB();
  asm volatile("buffer_load_dwordx4 v[176:179], %0, %1, %2 offen offset:1024 sc1" :  : "v"(vHr), "s"(rWS), "s"(hso[0]) : "memory"); SB();
  asm volatile("v_mfma_f32_16x16x32_bf16 %0, a[136:139], a[108:111], %0" : "+v"(ac[5]) :  : ); SB();
  asm volatile("v_mfma_f32_16x16x32_bf16 %0, a[144:147], a[108:111], %0" : "+v"(ac[7]) :  : ); SB();
  asm volatile("ds_write_b128 %0, %1 offset:3072" :  : "v"(dd), "v"(ac[3]) : "memory"); SB();
  asm volatile("v_mfma_f32_16x16x32_bf16 %0, a[144:147], a[104:107], %0" : "+v"(ac[6]) :  : ); SB();
  asm volatile("buffer_load_dwordx4 v[196:199], %0, %1, %2 offen offset:0 sc1" :  : "v"(vHr), "s"(rWS), "s"(hso[3]) : "memory"); SB();
  asm volatile("v_mfma_f32_16x16x32_bf16 %0, a[144:147], a[96:99], %0" : "+v"(ac[4]) :  : ); SB();
  asm volatile("v_mfma_f32_16x16x32_bf16 %0, a[140:143], a[104:107], %0" : "+v"(ac[5]) :  : ); SB();
  asm volatile("v_mfma_f32_16x16x32_bf16 %0, a[148:151], a[104:107], %0" : "+v"(ac[7]) :  : ); SB();
  asm volatile("buffer_load_dwordx4 v[200:203], %0, %1, %2 offen offset:0 sc1" :  : "v"(vHr), "s"(rWS), "s"(hso[5]) : "memory"); SB();
  asm volatile("v_mfma_f32_16x16x32_bf16 %0, a[148:151], a[100:103], %0" : "+v"(ac[6]) :  : ); SB();
  asm volatile("v_mfma_f32_16x16x32_bf16 %0, a[144:147], a[100:103], %0" : "+v"(ac[5]) :  : ); SB();
  asm volatile("ds_write_b128 %0, %1 offset:4096" :  : "v"(dd), "v"(ac[4]) : "memory"); SB();
  asm volatile("s_waitcnt lgkmcnt(2)\n\tv_mfma_f32_16x16x32_bf16 %0, a[152:155], a[100:103], %0" : "+v"(ac[7]) :  : ); SB();
  asm volatile("buffer_load_dwordx4 v[204:207], %0, %1, %2 offen offset:0 sc1" :  : "v"(vHr), "s"(rWS), "s"(hso[7]) : "memory"); SB();
  asm volatile("v_mfma_f32_16x16x32_bf16 %0, a[152:155], a[96:99], %0" : "+v"(ac[6]) :  : ); SB();
  asm volatile("v_mfma_f32_16x16x32_bf16 %0, a[148:151], a[96:99], %0" : "+v"(ac[5]) :  : ); SB();
  asm volatile("buffer_load_dwordx4 v[192:195], %0, %1, %2 offen offset:0 sc1" :  : "v"(vHr), "s"(rWS), "s"(hso[1]) : "memory"); SB();
  asm volatile("v_mfma_f32_16x16x32_bf16 %0, a[156:159], a[96:99], %0" : "+v"(ac[7]) :  : ); SB();
  asm volatile("s_nop 5" : "+v"(ac[5]), "+v"(ac[6]), "+v"(ac[7]) :  : ); SB();
  asm volatile("ds_write_b128 %0, %1 offset:6144" :  : "v"(dd), "v"(ac[6]) : "memory"); SB();
  asm volatile("ds_write_b128 %0, %1 offset:5120" :  : "v"(dd), "v"(ac[5]) : "memory"); SB();
  asm volatile("ds_write_b128 %0, %1 offset:7168" :  : "v"(dd), "v"(ac[7]) : "memory"); SB();
  ST(18);
  f32x4 yp[2][4], o3[4][5];
  u32x4 yk[2], yo[2];
  unsigned vtmp;
  asm volatile("ds_read_b128 %0, %1 offset:0" : "=&v"(yk[0]) : "v"(ykr) : "memory"); SB();
  asm volatile("ds_read_b128 %0, %1 offset:12928" : "=&v"(yk[1]) : "v"(ykr) : "memory"); SB();
  asm volatile("s_waitcnt lgkmcnt(0)\n\ts_barrier" :  :  : "memory"); SB();
  asm volatile("ds_read_b128 %0, %1 offset:0" : "=&v"(yp[0][0]) : "v"(ydr) : "memory"); SB();
  asm volatile("ds_read_b128 %0, %1 offset:256" : "=&v"(yp[0][1]) : "v"(ydr) : "memory"); SB();
  asm volatile("ds_read_b128 %0, %1 offset:8192" : "=&v"(yp[0][2]) : "v"(ydr) : "memory"); SB();
  asm volatile("ds_read_b128 %0, %1 offset:8448" : "=&v"(yp[0][3]) : "v"(ydr) : "memory"); SB();
  asm volatile("ds_read_b128 %0, %1 offset:16384" : "=&v"(yp[1][0]) : "v"(ydr) : "memory"); SB();
  asm volatile("ds_read_b128 %0, %1 offset:16640" : "=&v"(yp[1][1]) : "v"(ydr) : "memory"); SB();
  asm volatile("ds_read_b128 %0, %1 offset:24576" : "=&v"(yp[1][2]) : "v"(ydr) : "memory"); SB();
  asm volatile("ds_read_b128 %0, %1 offset:24832" : "=&v"(yp[1][3]) : "v"(ydr) : "memory"); SB();
  asm volatile("buffer_load_dwordx4 v[212:215], %0, %1, %2 offen offset:1024 sc1" :  : "v"(vHr), "s"(rWS), "s"(hso[3]) : "memory"); SB();
  asm volatile("buffer_load_dwordx4 v[216:219], %0, %1, %2 offen offset:1024 sc1" :  : "v"(vHr), "s"(rWS), "s"(hso[5]) : "memory"); SB();
  asm volatile("buffer_load_dwordx4 v[220:223], %0, %1, %2 offen offset:1024 sc1" :  : "v"(vHr), "s"(rWS), "s"(hso[7]) : "memory"); SB();
  asm volatile("" : "+v"(yk[0]), "+v"(yk[1]) :  : ); SB();
  f32x2 yd[2][4];
#pragma unroll
  for (int n = 0; n < 8; ++n) { const float Dv = dD[n >> 2]; yd[n >> 2][n & 3] = unpk(yk[n >> 2][n & 3]) * (f32x2){Dv, Dv}; asm volatile("" : "+v"(yd[n >> 2][n & 3])); }
  SB();
  asm volatile("s_waitcnt lgkmcnt(0)" : "+v"(yp[0][0]), "+v"(yp[0][1]), "+v"(yp[0][2]), "+v"(yp[0][3]) :  : "memory"); SB();
  {
    const f32x4 s0 = yp[0][0] + yp[0][2], s1 = yp[0][1] + yp[0][3];
    yo[0] = (u32x4){pk2((f32x2){s0[0], s0[1]} + yd[0][0]), pk2((f32x2){s0[2], s0[3]} + yd[0][1]), pk2((f32x2){s1[0], s1[1]} + yd[0][2]), ~(nohole(pk2((f32x2){s1[2], s1[3]} + yd[0][3])) ^ yk[0][3])};
    SB();
  }
  asm volatile("buffer_store_dwordx4 %1, %0, %2, %3 offen offset:0 sc1\n\ts_nop 1" :  : "v"(vF), "v"(yo[0]), "s"(rWS), "s"(W_KV + 0) : "memory"); SB();
  asm volatile("" : "+v"(yp[1][0]), "+v"(yp[1][1]), "+v"(yp[1][2]), "+v"(yp[1][3]) :  : "memory"); SB();
  {
    const f32x4 s0 = yp[1][0] + yp[1][2], s1 = yp[1][1] + yp[1][3];
    yo[1] = (u32x4){pk2((f32x2){s0[0], s0[1]} + yd[1][0]), pk2((f32x2){s0[2], s0[3]} + yd[1][1]), pk2((f32x2){s1[0], s1[1]} + yd[1][2]), ~(nohole(pk2((f32x2){s1[2], s1[3]} + yd[1][3])) ^ yk[1][3])};
    SB();
  }
  asm volatile("buffer_store_dwordx4 %1, %0, %2, %3 offen offset:0 sc1\n\ts_nop 1" :  : "v"(vF), "v"(yo[1]), "s"(rWS), "s"(W_KV + 4096) : "memory"); SB();
  ST(19);
  ST(20);
  f32x2 xf[5][2], sv[5][2];
  float rd, mr;
  asm volatile("s_waitcnt vmcnt(13)" :  :  : "memory"); SB();
  asm volatile("v_mfma_f32_16x16x32_bf16 %0, a[160:163], v[164:167], 0" : "=&v"(o3[1][0]) :  : ); SB();
  asm volatile("buffer_load_dwordx4 v[208:211], %0, %1, %2 offen offset:1024 sc1" :  : "v"(vHr), "s"(rWS), "s"(hso[1]) : "memory"); SB();
  asm volatile("v_mfma_f32_16x16x32_bf16 %0, a[164:167], v[164:167], 0" : "=&v"(o3[1][1]) :  : ); SB();
  asm volatile("buffer_load_dwordx4 v[228:231], %0, %1, %2 offen offset:2048 sc1" :  : "v"(vHr), "s"(rWS), "s"(hso[2]) : "memory"); SB();
  asm volatile("v_mfma_f32_16x16x32_bf16 %0, a[168:171], v[164:167], 0" : "=&v"(o3[1][2]) :  : ); SB();
  asm volatile("v_mfma_f32_16x16x32_bf16 %0, a[172:175], v[164:167], 0" : "=&v"(o3[1][3]) :  : ); SB();
  asm volatile("buffer_load_dwordx4 v[232:235], %0, %1, %2 offen offset:2048 sc1" :  : "v"(vHr), "s"(rWS), "s"(hso[4]) : "memory"); SB();
  asm volatile("v_mfma_f32_16x16x32_bf16 %0, a[176:179], v[164:167], 0" : "=&v"(o3[1][4]) :  : ); SB();
  asm volatile("v_mfma_f32_16x16x32_bf16 %0, a[160:163], v[168:171], 0" : "=&v"(o3[2][0]) :  : ); SB();
  asm volatile("v_mfma_f32_16x16x32_bf16 %0, a[164:167], v[168:171], 0" : "=&v"(o3[2][1]) :  : ); SB();
  asm volatile("buffer_load_dwordx4 v[236:239], %0, %1, %2 offen offset:2048 sc1" :  : "v"(vHr), "s"(rWS), "s"(hso[6]) : "memory"); SB();
  asm volatile("v_mfma_f32_16x16x32_bf16 %0, a[168:171], v[168:171], 0" : "=&v"(o3[2][2]) :  : ); SB();
  asm volatile("v_mfma_f32_16x16x32_bf16 %0, a[172:175], v[168:171], 0" : "=&v"(o3[2][3]) :  : ); SB();
  asm volatile("v_mfma_f32_16x16x32_bf16 %0, a[176:179], v[168:171], 0" : "=&v"(o3[2][4]) :  : ); SB();
  asm volatile("v_mfma_f32_16x16x32_bf16 %0, a[160:163], v[172:175], 0" : "=&v"(o3[3][0]) :  : ); SB();
  asm volatile("buffer_load_dwordx4 v[224:227], %0, %1, %2 offen offset:2048 sc1" :  : "v"(vHr), "s"(rWS), "s"(hso[0]) : "memory"); SB();
  asm volatile("v_mfma_f32_16x16x32_bf16 %0, a[164:167], v[172:175], 0" : "=&v"(o3[3][1]) :  : ); SB();
  asm volatile("v_mfma_f32_16x16x32_bf16 %0, a[168:171], v[172:175], 0" : "=&v"(o3[3][2]) :  : ); SB();
  asm volatile("v_mfma_f32_16x16x32_bf16 %0, a[172:175], v[172:175], 0" : "=&v"(o3[3][3]) :  : ); SB();
  asm volatile("buffer_load_dwordx4 v[244:247], %0, %1, %2 offen offset:2048 sc1" :  : "v"(vHr), "s"(rWS), "s"(hso[3]) : "memory"); SB();
  asm volatile("v_mfma_f32_16x16x32_bf16 %0, a[176:179], v[172:175], 0" : "=&v"(o3[3][4]) :  : ); SB();
  asm volatile("s_waitcnt vmcnt(15)" :  :  : "memory"); SB();
  asm volatile("v_mfma_f32_16x16x32_bf16 %0, a[180:183], v[180:183], %0" : "+v"(o3[1][0]) :  : ); SB();
  asm volatile("v_rcp_f32 %0, %1" : "=v"(rd) : "v"(xd[0]) : ); SB();
  asm volatile("v_mfma_f32_16x16x32_bf16 %0, a[184:187], v[180:183], %0" : "+v"(o3[1][1]) :  : ); SB();
  asm volatile("v_mul_f32 %0, -%1, %2" : "=v"(mr) : "v"(xm[0]), "v"(rd) : ); SB();
  asm volatile("v_mfma_f32_16x16x32_bf16 %0, a[188:191], v[180:183], %0" : "+v"(o3[1][2]) :  : ); SB();
  asm volatile("buffer_load_dwordx4 v[248:251], %0, %1, %2 offen offset:2048 sc1" :  : "v"(vHr), "s"(rWS), "s"(hso[5]) : "memory"); SB();
  asm volatile("v_lshlrev_b32 %0, 16, %1" : "=v"(xf[0][0][0]) : "v"(xv[0][0]) : ); SB();
  asm volatile("v_mfma_f32_16x16x32_bf16 %0, a[192:195], v[180:183], %0" : "+v"(o3[1][3]) :  : ); SB();
  asm volatile("v_and_b32 %0, 0xffff0000, %1" : "=v"(xf[0][0][1]) : "v"(xv[0][0]) : ); SB();
  asm volatile("v_mfma_f32_16x16x32_bf16 %0, a[196:199], v[180:183], %0" : "+v"(o3[1][4]) :  : ); SB();
  asm volatile("v_lshlrev_b32 %0, 16, %1" : "=v"(xf[0][1][0]) : "v"(xv[0][1]) : ); SB();
  asm volatile("v_mfma_f32_16x16x32_bf16 %0, a[180:183], v[184:187], %0" : "+v"(o3[2][0]) :  : ); SB();
  asm volatile("buffer_load_dwordx4 v[252:255], %0, %1, %2 offen offset:2048 sc1" :  : "v"(vHr), "s"(rWS), "s"(hso[7]) : "memory"); SB();
  asm volatile("v_and_b32 %0, 0xffff0000, %1" : "=v"(xf[0][1][1]) : "v"(xv[0][1]) : ); SB();
  asm volatile("v_mfma_f32_16x16x32_bf16 %0, a[184:187], v[184:187], %0" : "+v"(o3[2][1]) :  : ); SB();
  asm volatile("v_lshlrev_b32 %0, 16, %1" : "=v"(xf[1][0][0]) : "v"(xv[1][0]) : ); SB();
  asm volatile("v_mfma_f32_16x16x32_bf16 %0, a[188:191], v[184:187], %0" : "+v"(o3[2][2]) :  : ); SB();
  asm volatile("v_and_b32 %0, 0xffff0000, %1" : "=v"(xf[1][0][1]) : "v"(xv[1][0]) : ); SB();
  asm volatile("v_mfma_f32_16x16x32_bf16 %0, a[192:195], v[184:187], %0" : "+v"(o3[2][3]) :  : ); SB();
  asm volatile("v_lshlrev_b32 %0, 16, %1" : "=v"(xf[1][1][0]) : "v"(xv[1][1]) : ); SB();
  asm volatile("v_mfma_f32_16x16x32_bf16 %0, a[196:199], v[184:187], %0" : "+v"(o3[2][4]) :  : ); SB();
  asm volatile("buffer_load_dwordx4 v[240:243], %0, %1, %2 offen offset:2048 sc1" :  : "v"(vHr), "s"(rWS), "s"(hso[1]) : "memory"); SB();
  asm volatile("v_and_b32 %0, 0xffff0000, %1" : "=v"(xf[1][1][1]) : "v"(xv[1][1]) : ); SB();
  asm volatile("v_mfma_f32_16x16x32_bf16 %0, a[180:183], v[188:191], %0" : "+v"(o3[3][0]) :  : ); SB();
  asm volatile("v_lshlrev_b32 %0, 16, %1" : "=v"(xf[2][0][0]) : "v"(xv[2][0]) : ); SB();
  asm volatile("v_mfma_f32_16x16x32_bf16 %0, a[184:187], v[188:191], %0" : "+v"(o3[3][1]) :  : ); SB();
  asm volatile("v_and_b32 %0, 0xffff0000, %1" : "=v"(xf[2][0][1]) : "v"(xv[2][0]) : ); SB();
  asm volatile("v_mfma_f32_16x16x32_bf16 %0, a[188:191], v[188:191], %0" : "+v"(o3[3][2]) :  : ); SB();
  asm volatile("v_lshlrev_b32 %0, 16, %1" : "=v"(xf[2][1][0]) : "v"(xv[2][1]) : ); SB();
  asm volatile("v_mfma_f32_16x16x32_bf16 %0, a[192:195], v[188:191], %0" : "+v"(o3[3][3]) :  : ); SB();
  asm volatile("v_and_b32 %0, 0xffff0000, %1" : "=v"(xf[2][1][1]) : "v"(xv[2][1]) : ); SB();
  asm volatile("v_mfma_f32_16x16x32_bf16 %0, a[196:199], v[188:191], %0" : "+v"(o3[3][4]) :  : ); SB();
  asm volatile("v_lshlrev_b32 %0, 16, %1" : "=v"(xf[3][0][0]) : "v"(xv[3][0]) : ); SB();
  asm volatile("s_waitcnt vmcnt(14)" :  :  : "memory"); SB();
  asm volatile("v_mfma_f32_16x16x32_bf16 %0, a[200:203], v[196:199], %0" : "+v"(o3[1][0]) :  : ); SB();
  asm volatile("v_and_b32 %0, 0xffff0000, %1" : "=v"(xf[3][0][1]) : "v"(xv[3][0]) : ); SB();
  asm volatile("v_mfma_f32_16x16x32_bf16 %0, a[204:207], v[196:199], %0" : "+v"(o3[1][1]) :  : ); SB();
  asm volatile("v_lshlrev_b32 %0, 16, %1" : "=v"(xf[3][1][0]) : "v"(xv[3][1]) : ); SB();
  asm volatile("v_mfma_f32_16x16x32_bf16 %0, a[208:211], v[196:199], %0" : "+v"(o3[1][2]) :  : ); SB();
  asm volatile("v_and_b32 %0, 0xffff0000, %1" : "=v"(xf[3][1][1]) : "v"(xv[3][1]) : ); SB();
  asm volatile("v_mfma_f32_16x16x32_bf16 %0, a[212:215], v[196:199], %0" : "+v"(o3[1][3]) :  : ); SB();
  asm volatile("v_lshlrev_b32 %0, 16, %1" : "=v"(xf[4][0][0]) : "v"(xv[4][0]) : ); SB();
  asm volatile("v_mfma_f32_16x16x32_bf16 %0, a[216:219], v[196:199], %0" : "+v"(o3[1][4]) :  : ); SB();
  asm volatile("v_and_b32 %0, 0xffff0000, %1" : "=v"(xf[4][0][1]) : "v"(xv[4][0]) : ); SB();
  asm volatile("v_mfma_f32_16x16x32_bf16 %0, a[200:203], v[200:203], %0" : "+v"(o3[2][0]) :  : ); SB();
  asm volatile("v_lshlrev_b32 %0, 16, %1" : "=v"(xf[4][1][0]) : "v"(xv[4][1]) : ); SB();
  asm volatile("v_mfma_f32_16x16x32_bf16 %0, a[204:207], v[200:203], %0" : "+v"(o3[2][1]) :  : ); SB();
  asm volatile("v_and_b32 %0, 0xffff0000, %1" : "=v"(xf[4][1][1]) : "v"(xv[4][1]) : ); SB();
  asm volatile("v_mfma_f32_16x16x32_bf16 %0, a[208:211], v[200:203], %0" : "+v"(o3[2][2]) :  : ); SB();
  asm volatile("v_fma_f32 %0, %1, %2, %3" : "=v"(sv[0][0][0]) : "v"(xf[0][0][0]), "v"(rd), "v"(mr) : ); SB();
  asm volatile("v_mfma_f32_16x16x32_bf16 %0, a[212:215], v[200:203], %0" : "+v"(o3[2][3]) :  : ); SB();
  asm volatile("v_fma_f32 %0, %1, %2, %3" : "=v"(sv[0][0][1]) : "v"(xf[0][0][1]), "v"(rd), "v"(mr) : ); SB();
  asm volatile("v_mfma_f32_16x16x32_bf16 %0, a[216:219], v[200:203], %0" : "+v"(o3[2][4]) :  : ); SB();
  asm volatile("v_fma_f32 %0, %1, %2, %3" : "=v"(sv[0][1][0]) : "v"(xf[0][1][0]), "v"(rd), "v"(mr) : ); SB();
  asm volatile("v_mfma_f32_16x16x32_bf16 %0, a[200:203], v[204:207], %0" : "+v"(o3[3][0]) :  : ); SB();
  asm volatile("v_fma_f32 %0, %1, %2, %3" : "=v"(sv[0][1][1]) : "v"(xf[0][1][1]), "v"(rd), "v"(mr) : ); SB();
  asm volatile("v_mfma_f32_16x16x32_bf16 %0, a[204:207], v[204:207], %0" : "+v"(o3[3][1]) :  : ); SB();
  asm volatile("v_fma_f32 %0, %1, %2, %3" : "=v"(sv[1][0][0]) : "v"(xf[1][0][0]), "v"(rd), "v"(mr) : ); SB();
  asm volatile("v_mfma_f32_16x16x32_bf16 %0, a[208:211], v[204:207], %0" : "+v"(o3[3][2]) :  : ); SB();
  asm volatile("v_fma_f32 %0, %1, %2, %3" : "=v"(sv[1][0][1]) : "v"(xf[1][0][1]), "v"(rd), "v"(mr) : ); SB();
  asm volatile("v_mfma_f32_16x16x32_bf16 %0, a[212:215], v[204:207], %0" : "+v"(o3[3][3]) :  : ); SB();
  asm volatile("v_fma_f32 %0, %1, %2, %3" : "=v"(sv[1][1][0]) : "v"(xf[1][1][0]), "v"(rd), "v"(mr) : ); SB();
  asm volatile("v_mfma_f32_16x16x32_bf16 %0, a[216:219], v[204:207], %0" : "+v"(o3[3][4]) :  : ); SB();
  asm volatile("v_fma_f32 %0, %1, %2, %3" : "=v"(sv[1][1][1]) : "v"(xf[1][1][1]), "v"(rd), "v"(mr) : ); SB();
  asm volatile("s_waitcnt vmcnt(8)" :  :  : "memory"); SB();
  asm volatile("v_mfma_f32_16x16x32_bf16 %0, a[220:223], v[212:215], %0" : "+v"(o3[1][0]) :  : ); SB();
  asm volatile("v_fma_f32 %0, %1, %2, %3" : "=v"(sv[2][0][0]) : "v"(xf[2][0][0]), "v"(rd), "v"(mr) : ); SB();
  asm volatile("v_mfma_f32_16x16x32_bf16 %0, a[224:227], v[212:215], %0" : "+v"(o3[1][1]) :  : ); SB();
  asm volatile("v_fma_f32 %0, %1, %2, %3" : "=v"(sv[2][0][1]) : "v"(xf[2][0][1]), "v"(rd), "v"(mr) : ); SB();
  asm volatile("v_mfma_f32_16x16x32_bf16 %0, a[228:231], v[212:215], %0" : "+v"(o3[1][2]) :  : ); SB();
  asm volatile("v_fma_f32 %0, %1, %2, %3" : "=v"(sv[2][1][0]) : "v"(xf[2][1][0]), "v"(rd), "v"(mr) : ); SB();
  asm volatile("v_mfma_f32_16x16x32_bf16 %0, a[232:235], v[212:215], %0" : "+v"(o3[1][3]) :  : ); SB();
  asm volatile("v_fma_f32 %0, %1, %2, %3" : "=v"(sv[2][1][1]) : "v"(xf[2][1][1]), "v"(rd), "v"(mr) : ); SB();
  asm volatile("v_mfma_f32_16x16x32_bf16 %0, a[236:239], v[212:215], %0" : "+v"(o3[1][4]) :  : ); SB();
  asm volatile("v_fma_f32 %0, %1, %2, %3" : "=v"(sv[3][0][0]) : "v"(xf[3][0][0]), "v"(rd), "v"(mr) : ); SB();
  asm volatile("v_mfma_f32_16x16x32_bf16 %0, a[220:223], v[216:219], %0" : "+v"(o3[2][0]) :  : ); SB();
  asm volatile("v_fma_f32 %0, %1, %2, %3" : "=v"(sv[3][0][1]) : "v"(xf[3][0][1]), "v"(rd), "v"(mr) : ); SB();
  asm volatile("v_mfma_f32_16x16x32_bf16 %0, a[224:227], v[216:219], %0" : "+v"(o3[2][1]) :  : ); SB();
  asm volatile("v_fma_f32 %0, %1, %2, %3" : "=v"(sv[3][1][0]) : "v"(xf[3][1][0]), "v"(rd), "v"(mr) : ); SB();
  asm volatile("v_mfma_f32_16x16x32_bf16 %0, a[228:231], v[216:219], %0" : "+v"(o3[2][2]) :  : ); SB();
  asm volatile("v_fma_f32 %0, %1, %2, %3" : "=v"(sv[3][1][1]) : "v"(xf[3][1][1]), "v"(rd), "v"(mr) : ); SB();
  asm volatile("v_mfma_f32_16x16x32_bf16 %0, a[232:235], v[216:219], %0" : "+v"(o3[2][3]) :  : ); SB();
  asm volatile("v_fma_f32 %0, %1, %2, %3" : "=v"(sv[4][0][0]) : "v"(xf[4][0][0]), "v"(rd), "v"(mr) : ); SB();
  asm volatile("v_mfma_f32_16x16x32_bf16 %0, a[236:239], v[216:219], %0" : "+v"(o3[2][4]) :  : ); SB();
  asm volatile("v_fma_f32 %0, %1, %2, %3" : "=v"(sv[4][0][1]) : "v"(xf[4][0][1]), "v"(rd), "v"(mr) : ); SB();
  asm volatile("v_mfma_f32_16x16x32_bf16 %0, a[220:223], v[220:223], %0" : "+v"(o3[3][0]) :  : ); SB();
  asm volatile("v_fma_f32 %0, %1, %2, %3" : "=v"(sv[4][1][0]) : "v"(xf[4][1][0]), "v"(rd), "v"(mr) : ); SB();
  asm volatile("v_mfma_f32_16x16x32_bf16 %0, a[224:227], v[220:223], %0" : "+v"(o3[3][1]) :  : ); SB();
  asm volatile("v_fma_f32 %0, %1, %2, %3" : "=v"(sv[4][1][1]) : "v"(xf[4][1][1]), "v"(rd), "v"(mr) : ); SB();
  asm volatile("v_mfma_f32_16x16x32_bf16 %0, a[228:231], v[220:223], %0" : "+v"(o3[3][2]) :  : ); SB();
  asm volatile("v_mfma_f32_16x16x32_bf16 %0, a[232:235], v[220:223], %0" : "+v"(o3[3][3]) :  : ); SB();
  asm volatile("v_mfma_f32_16x16x32_bf16 %0, a[236:239], v[220:223], %0" : "+v"(o3[3][4]) :  : ); SB();
  ST(30);
  asm volatile("s_waitcnt vmcnt(0)\n\t.Lc_%=:\n\tv_max3_u32 %1, v231, v235, v239\n\tv_max3_u32 %1, %1, v227, v247\n\tv_max3_u32 %1, %1, v251, v255\n\tv_max3_u32 %1, %1, v243, v243\n\tv_cmp_eq_u32 vcc, -1, %1\n\ts_cbranch_vccnz .Lr_%=\n\t.Ld_%=:\n\t.subsection 1\n\t.Lr_%=:\n\tv_add_u32 %0, -1, %0\n\ts_nop 1\n\tv_readfirstlane_b32 vcc_hi, %0\n\ts_nop 3\n\ts_cmp_lt_i32 vcc_hi, 1\n\ts_cbranch_scc1 .Ld_%=\n\ts_sleep 2\n\tbuffer_load_dwordx4 v[228:231], %2, %3, %6 offen offset:2048 sc1\n\tbuffer_load_dwordx4 v[232:235], %2, %3, %8 offen offset:2048 sc1\n\tbuffer_load_dwordx4 v[236:239], %2, %3, %10 offen offset:2048 sc1\n\tbuffer_load_dwordx4 v[224:227], %2, %3, %4 offen offset:2048 sc1\n\tbuffer_load_dwordx4 v[244:247], %2, %3, %7 offen offset:2048 sc1\n\tbuffer_load_dwordx4 v[248:251], %2, %3, %9 offen offset:2048 sc1\n\tbuffer_load_dwordx4 v[252:255], %2, %3, %11 offen offset:2048 sc1\n\tbuffer_load_dwordx4 v[240:243], %2, %3, %5 offen offset:2048 sc1\n\ts_waitcnt vmcnt(0)\n\ts_branch .Lc_%=\n\t.subsection 0" : "+v"(tmo), "=&v"(vtmp) : "v"(vHr), "s"(rWS), "s"(hso[0]), "s"(hso[1]), "s"(hso[2]), "s"(hso[3]), "s"(hso[4]), "s"(hso[5]), "s"(hso[6]), "s"(hso[7]) : "memory", "vcc", "scc"); SB();
  asm volatile("v_mfma_f32_16x16x32_bf16 %0, a[240:243], v[228:231], %0" : "+v"(o3[1][0]) :  : ); SB();
  asm volatile("v_mfma_f32_16x16x32_bf16 %0, a[244:247], v[228:231], %0" : "+v"(o3[1][1]) :  : ); SB();
  asm volatile("v_mfma_f32_16x16x32_bf16 %0, a[248:251], v[228:231], %0" : "+v"(o3[1][2]) :  : ); SB();
  asm volatile("v_mfma_f32_16x16x32_bf16 %0, a[252:255], v[228:231], %0" : "+v"(o3[1][3]) :  : ); SB();
  asm volatile("v_mfma_f32_16x16x32_bf16 %0, a[0:3], v[228:231], %0" : "+v"(o3[1][4]) :  : ); SB();
  asm volatile("v_mfma_f32_16x16x32_bf16 %0, a[4:7], v[244:247], %0" : "+v"(o3[1][0]) :  : ); SB();
  asm volatile("v_mfma_f32_16x16x32_bf16 %0, a[8:11], v[244:247], %0" : "+v"(o3[1][1]) :  : ); SB();
  asm volatile("v_mfma_f32_16x16x32_bf16 %0, a[12:15], v[244:247], %0" : "+v"(o3[1][2]) :  : ); SB();
  asm volatile("v_mfma_f32_16x16x32_bf16 %0, a[16:19], v[244:247], %0" : "+v"(o3[1][3]) :  : ); SB();
  asm volatile("v_mfma_f32_16x16x32_bf16 %0, a[20:23], v[244:247], %0" : "+v"(o3[1][4]) :  : ); SB();
  asm volatile("v_mfma_f32_16x16x32_bf16 %0, a[240:243], v[232:235], %0" : "+v"(o3[2][0]) :  : ); SB();
  asm volatile("ds_write_b128 %0, %1 offset:0" :  : "v"(pw3), "v"(o3[1][0]) : "memory"); SB();
  asm volatile("v_mfma_f32_16x16x32_bf16 %0, a[244:247], v[232:235], %0" : "+v"(o3[2][1]) :  : ); SB();
  asm volatile("v_mfma_f32_16x16x32_bf16 %0, a[248:251], v[232:235], %0" : "+v"(o3[2][2]) :  : ); SB();
  asm volatile("v_mfma_f32_16x16x32_bf16 %0, a[252:255], v[232:235], %0" : "+v"(o3[2][3]) :  : ); SB();
  asm volatile("ds_write_b128 %0, %1 offset:1024" :  : "v"(pw3), "v"(o3[1][1]) : "memory"); SB();
  asm volatile("v_mfma_f32_16x16x32_bf16 %0, a[0:3], v[232:235], %0" : "+v"(o3[2][4]) :  : ); SB();
  asm volatile("v_mfma_f32_16x16x32_bf16 %0, a[4:7], v[248:251], %0" : "+v"(o3[2][0]) :  : ); SB();
  asm volatile("buffer_load_dwordx4 v[164:167], %0, %1, %2 offen offset:0 sc1" :  : "v"(vKV), "s"(rWS), "s"(0u) : "memory"); SB();
  asm volatile("v_mfma_f32_16x16x32_bf16 %0, a[8:11], v[248:251], %0" : "+v"(o3[2][1]) :  : ); SB();
  asm volatile("buffer_load_dwordx4 v[168:171], %0, %1, %2 offen offset:0 sc1" :  : "v"(vKV), "s"(rWS), "s"(w3k[0]) : "memory"); SB();
  asm volatile("v_mfma_f32_16x16x32_bf16 %0, a[12:15], v[248:251], %0" : "+v"(o3[2][2]) :  : ); SB();
  asm volatile("buffer_load_dwordx4 v[172:175], %0, %1, %2 offen offset:0 sc1" :  : "v"(vKV), "s"(rWS), "s"(w3k[1]) : "memory"); SB();
  asm volatile("v_mfma_f32_16x16x32_bf16 %0, a[16:19], v[248:251], %0" : "+v"(o3[2][3]) :  : ); SB();
  asm volatile("ds_write_b128 %0, %1 offset:2048" :  : "v"(pw3), "v"(o3[1][2]) : "memory"); SB();
  asm volatile("v_mfma_f32_16x16x32_bf16 %0, a[20:23], v[248:251], %0" : "+v"(o3[2][4]) :  : ); SB();
  asm volatile("v_mfma_f32_16x16x32_bf16 %0, a[240:243], v[236:239], %0" : "+v"(o3[3][0]) :  : ); SB();
  asm volatile("v_mfma_f32_16x16x32_bf16 %0, a[244:247], v[236:239], %0" : "+v"(o3[3][1]) :  : ); SB();
  asm volatile("ds_write_b128 %0, %1 offset:3072" :  : "v"(pw3), "v"(o3[1][3]) : "memory"); SB();
  asm volatile("v_mfma_f32_16x16x32_bf16 %0, a[248:251], v[236:239], %0" : "+v"(o3[3][2]) :  : ); SB();
  asm volatile("v_mfma_f32_16x16x32_bf16 %0, a[252:255], v[236:239], %0" : "+v"(o3[3][3]) :  : ); SB();
  asm volatile("v_mfma_f32_16x16x32_bf16 %0, a[0:3], v[236:239], %0" : "+v"(o3[3][4]) :  : ); SB();
  asm volatile("ds_write_b128 %0, %1 offset:4096" :  : "v"(pw3), "v"(o3[1][4]) : "memory"); SB();
  asm volatile("v_mfma_f32_16x16x32_bf16 %0, a[4:7], v[252:255], %0" : "+v"(o3[3][0]) :  : ); SB();
  asm volatile("v_mfma_f32_16x16x32_bf16 %0, a[8:11], v[252:255], %0" : "+v"(o3[3][1]) :  : ); SB();
  asm volatile("v_mfma_f32_16x16x32_bf16 %0, a[12:15], v[252:255], %0" : "+v"(o3[3][2]) :  : ); SB();
  asm volatile("ds_write_b128 %0, %1 offset:5120" :  : "v"(pw3), "v"(o3[2][0]) : "memory"); SB();
  asm volatile("v_mfma_f32_16x16x32_bf16 %0, a[16:19], v[252:255], %0" : "+v"(o3[3][3]) :  : ); SB();
  asm volatile("v_mfma_f32_16x16x32_bf16 %0, a[20:23], v[252:255], %0" : "+v"(o3[3][4]) :  : ); SB();
  asm volatile("v_mfma_f32_16x16x32_bf16 %0, a[160:163], v[160:163], 0" : "=&v"(o3[0][0]) :  : ); SB();
  asm volatile("ds_write_b128 %0, %1 offset:6144" :  : "v"(pw3), "v"(o3[2][1]) : "memory"); SB();
  asm volatile("v_mfma_f32_16x16x32_bf16 %0, a[164:167], v[160:163], 0" : "=&v"(o3[0][1]) :  : ); SB();
  asm volatile("v_mfma_f32_16x16x32_bf16 %0, a[168:171], v[160:163], 0" : "=&v"(o3[0][2]) :  : ); SB();
  asm volatile("v_mfma_f32_16x16x32_bf16 %0, a[172:175], v[160:163], 0" : "=&v"(o3[0][3]) :  : ); SB();
  asm volatile("ds_write_b128 %0, %1 offset:7168" :  : "v"(pw3), "v"(o3[2][2]) : "memory"); SB();
  asm volatile("v_mfma_f32_16x16x32_bf16 %0, a[176:179], v[160:163], 0" : "=&v"(o3[0][4]) :  : ); SB();
  asm volatile("v_mfma_f32_16x16x32_bf16 %0, a[180:183], v[176:179], %0" : "+v"(o3[0][0]) :  : ); SB();
  asm volatile("v_mfma_f32_16x16x32_bf16 %0, a[184:187], v[176:179], %0" : "+v"(o3[0][1]) :  : ); SB();
  asm volatile("ds_write_b128 %0, %1 offset:8192" :  : "v"(pw3), "v"(o3[2][3]) : "memory"); SB();
  asm volatile("v_mfma_f32_16x16x32_bf16 %0, a[188:191], v[176:179], %0" : "+v"(o3[0][2]) :  : ); SB();
  asm volatile("v_mfma_f32_16x16x32_bf16 %0, a[192:195], v[176:179], %0" : "+v"(o3[0][3]) :  : ); SB();
  asm volatile("v_mfma_f32_16x16x32_bf16 %0, a[196:199], v[176:179], %0" : "+v"(o3[0][4]) :  : ); SB();
  asm volatile("ds_write_b128 %0, %1 offset:9216" :  : "v"(pw3), "v"(o3[2][4]) : "memory"); SB();
  asm volatile("v_mfma_f32_16x16x32_bf16 %0, a[200:203], v[192:195], %0" : "+v"(o3[0][0]) :  : ); SB();
  asm volatile("v_mfma_f32_16x16x32_bf16 %0, a[204:207], v[192:195], %0" : "+v"(o3[0][1]) :  : ); SB();
  asm volatile("v_mfma_f32_16x16x32_bf16 %0, a[208:211], v[192:195], %0" : "+v"(o3[0][2]) :  : ); SB();
  asm volatile("ds_write_b128 %0, %1 offset:10240" :  : "v"(pw3), "v"(o3[3][0]) : "memory"); SB();
  asm volatile("v_mfma_f32_16x16x32_bf16 %0, a[212:215], v[192:195], %0" : "+v"(o3[0][3]) :  : ); SB();
  asm volatile("v_mfma_f32_16x16x32_bf16 %0, a[216:219], v[192:195], %0" : "+v"(o3[0][4]) :  : ); SB();
  asm volatile("v_mfma_f32_16x16x32_bf16 %0, a[220:223], v[208:211], %0" : "+v"(o3[0][0]) :  : ); SB();
  asm volatile("ds_write_b128 %0, %1 offset:11264" :  : "v"(pw3), "v"(o3[3][1]) : "memory"); SB();
  asm volatile("v_mfma_f32_16x16x32_bf16 %0, a[224:227], v[208:211], %0" : "+v"(o3[0][1]) :  : ); SB();
  asm volatile("v_mfma_f32_16x16x32_bf16 %0, a[228:231], v[208:211], %0" : "+v"(o3[0][2]) :  : ); SB();
  asm volatile("v_mfma_f32_16x16x32_bf16 %0, a[232:235], v[208:211], %0" : "+v"(o3[0][3]) :  : ); SB();
  asm volatile("ds_write_b128 %0, %1 offset:12288" :  : "v"(pw3), "v"(o3[3][2]) : "memory"); SB();
  asm volatile("v_mfma_f32_16x16x32_bf16 %0, a[236:239], v[208:211], %0" : "+v"(o3[0][4]) :  : ); SB();
  asm volatile("v_mfma_f32_16x16x32_bf16 %0, a[240:243], v[224:227], %0" : "+v"(o3[0][0]) :  : ); SB();
  asm volatile("v_mfma_f32_16x16x32_bf16 %0, a[244:247], v[224:227], %0" : "+v"(o3[0][1]) :  : ); SB();
  asm volatile("ds_write_b128 %0, %1 offset:13312" :  : "v"(pw3), "v"(o3[3][3]) : "memory"); SB();
  asm volatile("v_mfma_f32_16x16x32_bf16 %0, a[248:251], v[224:227], %0" : "+v"(o3[0][2]) :  : ); SB();
  asm volatile("v_mfma_f32_16x16x32_bf16 %0, a[252:255], v[224:227], %0" : "+v"(o3[0][3]) :  : ); SB();
  asm volatile("v_mfma_f32_16x16x32_bf16 %0, a[0:3], v[224:227], %0" : "+v"(o3[0][4]) :  : ); SB();
  asm volatile("ds_write_b128 %0, %1 offset:14336" :  : "v"(pw3), "v"(o3[3][4]) : "memory"); SB();
  asm volatile("v_mfma_f32_16x16x32_bf16 %0, a[4:7], v[240:243], %0" : "+v"(o3[0][0]) :  : ); SB();
  asm volatile("v_mfma_f32_16x16x32_bf16 %0, a[8:11], v[240:243], %0" : "+v"(o3[0][1]) :  : ); SB();
  asm volatile("v_mfma_f32_16x16x32_bf16 %0, a[12:15], v[240:243], %0" : "+v"(o3[0][2]) :  : ); SB();
  asm volatile("v_mfma_f32_16x16x32_bf16 %0, a[16:19], v[240:243], %0" : "+v"(o3[0][3]) :  : ); SB();
  asm volatile("v_mfma_f32_16x16x32_bf16 %0, a[20:23], v[240:243], %0" : "+v"(o3[0][4]) :  : ); SB();
  asm volatile("s_waitcnt lgkmcnt(0)\n\ts_barrier" :  :  : "memory"); SB();
  asm volatile("ds_read_b128 %0, %1 offset:0" : "=&v"(o3[1][0]) : "v"(p3[0]) : "memory"); SB();
  asm volatile("ds_read_b128 %0, %1 offset:5120" : "=&v"(o3[2][0]) : "v"(p3[1]) : "memory"); SB();
  asm volatile("ds_read_b128 %0, %1 offset:10240" : "=&v"(o3[3][0]) : "v"(p3[2]) : "memory"); SB();
  asm volatile("ds_read_b128 %0, %1 offset:1024" : "=&v"(o3[1][1]) : "v"(p3[0]) : "memory"); SB();
  asm volatile("ds_read_b128 %0, %1 offset:6144" : "=&v"(o3[2][1]) : "v"(p3[1]) : "memory"); SB();
  asm volatile("ds_read_b128 %0, %1 offset:11264" : "=&v"(o3[3][1]) : "v"(p3[2]) : "memory"); SB();
  asm volatile("ds_read_b128 %0, %1 offset:2048" : "=&v"(o3[1][2]) : "v"(p3[0]) : "memory"); SB();
  asm volatile("ds_read_b128 %0, %1 offset:7168" : "=&v"(o3[2][2]) : "v"(p3[1]) : "memory"); SB();
  asm volatile("ds_read_b128 %0, %1 offset:12288" : "=&v"(o3[3][2]) : "v"(p3[2]) : "memory"); SB();
  asm volatile("ds_read_b128 %0, %1 offset:3072" : "=&v"(o3[1][3]) : "v"(p3[0]) : "memory"); SB();
  asm volatile("ds_read_b128 %0, %1 offset:8192" : "=&v"(o3[2][3]) : "v"(p3[1]) : "memory"); SB();
  asm volatile("ds_read_b128 %0, %1 offset:13312" : "=&v"(o3[3][3]) : "v"(p3[2]) : "memory"); SB();
  asm volatile("ds_read_b128 %0, %1 offset:4096" : "=&v"(o3[1][4]) : "v"(p3[0]) : "memory"); SB();
  asm volatile("ds_read_b128 %0, %1 offset:9216" : "=&v"(o3[2][4]) : "v"(p3[1]) : "memory"); SB();
  asm volatile("ds_read_b128 %0, %1 offset:14336" : "=&v"(o3[3][4]) : "v"(p3[2]) : "memory"); SB();
  ST(21);
  u32x2 yr[5];
  asm volatile("s_waitcnt lgkmcnt(9)" : "+v"(o3[0][0]), "+v"(o3[0][1]), "+v"(o3[1][0]), "+v"(o3[1][1]), "+v"(o3[2][0]), "+v"(o3[2][1]), "+v"(o3[3][0]), "+v"(o3[3][1]) :  : "memory"); SB();
#pragma unroll
  for (int j = 0; j <= 1; ++j) {
    const f32x4 o4 = (o3[0][j] + o3[1][j]) + (o3[2][j] + o3[3][j]);
#pragma unroll
    for (int h = 0; h < 2; ++h) {
      const f32x2 o = {o4[2 * h], o4[2 * h + 1]};
      const f32x2 a = o * (f32x2){-1.4426950f, -1.4426950f};
      const f32x2 e = (f32x2){__builtin_amdgcn_exp2f(a[0]), __builtin_amdgcn_exp2f(a[1])} + (f32x2){1.0f, 1.0f};
      const f32x2 r = {__builtin_amdgcn_rcpf(e[0]), __builtin_amdgcn_rcpf(e[1])};
      sv[j][h] = (sv[j][h] * o) * r;
    }
  }
  asm volatile("" : "+v"(sv[0][0]), "+v"(sv[0][1]), "+v"(sv[1][0]), "+v"(sv[1][1]));
  SB();
  asm volatile("s_waitcnt lgkmcnt(0)" : "+v"(o3[0][2]), "+v"(o3[0][3]), "+v"(o3[1][2]), "+v"(o3[1][3]), "+v"(o3[2][2]), "+v"(o3[2][3]), "+v"(o3[3][2]), "+v"(o3[3][3]) :  : "memory"); SB();
#pragma unroll
  for (int j = 2; j <= 3; ++j) {
    const f32x4 o4 = (o3[0][j] + o3[1][j]) + (o3[2][j] + o3[3][j]);
#pragma unroll
    for (int h = 0; h < 2; ++h) {
      const f32x2 o = {o4[2 * h], o4[2 * h + 1]};
      const f32x2 a = o * (f32x2){-1.4426950f, -1.4426950f};
      const f32x2 e = (f32x2){__builtin_amdgcn_exp2f(a[0]), __builtin_amdgcn_exp2f(a[1])} + (f32x2){1.0f, 1.0f};
      const f32x2 r = {__builtin_amdgcn_rcpf(e[0]), __builtin_amdgcn_rcpf(e[1])};
      sv[j][h] = (sv[j][h] * o) * r;
    }
  }
  asm volatile("" : "+v"(sv[2][0]), "+v"(sv[2][1]), "+v"(sv[3][0]), "+v"(sv[3][1]));
  SB();
  ST(22);
  asm volatile("s_waitcnt vmcnt(0)\n\t.Lc_%=:\n\tv_xnor_b32 v167, v167, %6\n\tv_xnor_b32 v171, v171, %7\n\tv_xnor_b32 v175, v175, %8\n\tv_max3_u32 %1, v167, v171, v175\n\tv_cmp_eq_u32 vcc, -1, %1\n\ts_cbranch_vccnz .Lr_%=\n\t.Ld_%=:\n\t.subsection 1\n\t.Lr_%=:\n\tv_add_u32 %0, -1, %0\n\ts_nop 1\n\tv_readfirstlane_b32 vcc_hi, %0\n\ts_nop 3\n\ts_cmp_lt_i32 vcc_hi, 1\n\ts_cbranch_scc1 .Ld_%=\n\ts_sleep 2\n\tbuffer_load_dwordx4 v[164:167], %2, %3, 0 offen sc1\n\tbuffer_load_dwordx4 v[168:171], %2, %3, %4 offen sc1\n\tbuffer_load_dwordx4 v[172:175], %2, %3, %5 offen sc1\n\ts_waitcnt vmcnt(0)\n\ts_branch .Lc_%=\n\t.subsection 0" : "+v"(tmo), "=&v"(vtmp) : "v"(vKV), "s"(rWS), "s"(w3k[0]), "s"(w3k[1]), "v"(kz[0]), "v"(kz[1]), "v"(kz[2]) : "memory", "vcc", "scc"); SB();
  ST(31);
  asm volatile("ds_write_b128 %0, v[164:167] offset:0" :  : "v"(yw) : "memory"); SB();
  asm volatile("ds_write_b128 %0, v[168:171] offset:1024" :  : "v"(yw) : "memory"); SB();
  asm volatile("ds_write_b128 %0, v[172:175] offset:2048" :  : "v"(yw) : "memory"); SB();
  asm volatile("s_waitcnt lgkmcnt(0)\n\ts_barrier" :  :  : "memory"); SB();
  asm volatile("ds_read_b64_tr_b16 %0, %1 offset:0" : "=&v"(yr[0]) : "v"(ya) : "memory"); SB();
  asm volatile("ds_read_b64_tr_b16 %0, %1 offset:2048" : "=&v"(yr[1]) : "v"(ya) : "memory"); SB();
  asm volatile("ds_read_b64_tr_b16 %0, %1 offset:4096" : "=&v"(yr[2]) : "v"(ya) : "memory"); SB();
  asm volatile("ds_read_b64_tr_b16 %0, %1 offset:6144" : "=&v"(yr[3]) : "v"(ya) : "memory"); SB();
  asm volatile("ds_read_b64_tr_b16 %0, %1 offset:8192" : "=&v"(yr[4]) : "v"(ya) : "memory"); SB();
  asm volatile("" : "+v"(o3[0][4]), "+v"(o3[1][4]), "+v"(o3[2][4]), "+v"(o3[3][4]) :  : "memory"); SB();
#pragma unroll
  for (int j = 4; j <= 4; ++j) {
    const f32x4 o4 = (o3[0][j] + o3[1][j]) + (o3[2][j] + o3[3][j]);
#pragma unroll
    for (int h = 0; h < 2; ++h) {
      const f32x2 o = {o4[2 * h], o4[2 * h + 1]};
      const f32x2 a = o * (f32x2){-1.4426950f, -1.4426950f};
      const f32x2 e = (f32x2){__builtin_amdgcn_exp2f(a[0]), __builtin_amdgcn_exp2f(a[1])} + (f32x2){1.0f, 1.0f};
      const f32x2 r = {__builtin_amdgcn_rcpf(e[0]), __builtin_amdgcn_rcpf(e[1])};
      sv[j][h] = (sv[j][h] * o) * r;
    }
  }
  asm volatile("" : "+v"(sv[4][0]), "+v"(sv[4][1]));
  SB();
  asm volatile("s_waitcnt lgkmcnt(0)" : "+v"(yr[0]), "+v"(yr[1]), "+v"(yr[2]), "+v"(yr[3]), "+v"(yr[4]) :  : "memory"); SB();
  {
    unsigned pk[5][2];
#pragma unroll
    for (int n = 0; n < 10; ++n) pk[n >> 1][n & 1] = pk2(__builtin_elementwise_fma(sv[n >> 1][n & 1], unpk(yr[n >> 1][n & 1]), xf[n >> 1][n & 1]));
#pragma unroll
    for (int q = 0; q < 2; ++q) {
      const auto s0 = __builtin_amdgcn_permlane16_swap(pk[2 * q][0], pk[2 * q + 1][0], false, false);
      const auto s1 = __builtin_amdgcn_permlane16_swap(pk[2 * q][1], pk[2 * q + 1][1], false, false);
      asm volatile("s_nop 1\n\tglobal_store_dwordx4 %0, %1, %2 offset:%3 " :: "v"(po), "v"((u32x4){s0[0], s1[0], s0[1], s1[1]}), "s"(out), "i"(64 * q) : "memory");
    }
    asm volatile("global_store_dwordx2 %0, %1, %2 offset:128 " :: "v"(vX), "v"((u32x2){pk[4][0], pk[4][1]}), "s"(out) : "memory");
  }
  ST(23);
  ST_OUT();
  }}}
}

extern "C" __global__ void k_end() { asm volatile(".subsection 2\n.Lcode_end:\n\ts_endpgm\n\t.subsection 0"); }

extern "C" void kernel_launch(void* const* d_in, const int* in_sizes, int n_in,
                              void* d_out, int out_size, void* d_ws, size_t ws_size,
                              hipStream_t stream) {
  (void)hipFuncSetAttribute((const void*)k_all, hipFuncAttributeMaxDynamicSharedMemorySize, 163840);
  k_all<<<256, 256, 163840, stream>>>((const char*)d_in[0], (const char*)d_in[1], (const char*)d_in[2], (const char*)d_in[3], (const char*)d_in[4],
                                      (const char*)d_in[5], (const char*)d_in[7], (const char*)d_in[8], (const char*)d_in[9], (const char*)d_in[10],
                                      (char*)d_out, (char*)d_ws);
}
```

```cpp
#include <hip/hip_runtime.h>
#include <stdint.h>

typedef unsigned short u16;
typedef __attribute__((ext_vector_type(4))) float f32x4;
typedef __attribute__((ext_vector_type(2))) float f32x2;
typedef __attribute__((ext_vector_type(4))) unsigned int u32x4;
typedef __attribute__((ext_vector_type(2))) unsigned int u32x2;
typedef __attribute__((ext_vector_type(2))) __bf16 bf16x2n;
typedef __attribute__((ext_vector_type(8))) unsigned long long u64x8;
typedef __attribute__((ext_vector_type(4))) unsigned long long u64x4;
#define LDS_AS __attribute__((address_space(3)))
#define GAS __attribute__((address_space(1)))
#define SB() __builtin_amdgcn_sched_barrier(0)
#define DI __device__ __forceinline__

#define SGA
#define ST_DECL do {} while (0)
#define ST(i) do {} while (0)
#define ST_OUT() do {} while (0)

constexpr int S_ = 2048, D_ = 640;
constexpr unsigned HOLE = 0xFFFFFFFFu;
constexpr int W_ST = 4096, W_FZ = 768 << 10, W_FK = W_FZ + 8192, W_FH = W_FK + 8192, W_KV = 1 << 20, W_Z2 = 4 << 20, W_HH = 11 << 20, W_WO = 14 << 20;

#define wm (w >> 1)
#define wn (w & 1)
#define g4 (lane >> 4)
#define l15 (lane & 15)
#define q4 ((lane >> 2) & 3)
#define p4 (lane & 3)
#define l16 (lane * 16)
#define nch (3 - (rb >> 4))
#define ch0 (cb * 80 + rb * 2 + min(rb, 16u))
#define c2o ((nch - 2) * 2)
#define c2s (8192u + ((rb >> 4) << 30))
#define w3s ((w + 1) >> 2 << 30)
#define vZ (W_Z2 + ((rb * 2 + wm) * 64 + cb * 6 + wn * 3) * 1024 + l16)
#define vH (W_HH + ((rb * 4 + w) * 24 + cb * 3) * 1024 + l16)

DI unsigned pack2(float lo, float hi) {
  f32x2 v = {lo, hi};
  return __builtin_bit_cast(unsigned, __builtin_convertvector(v, bf16x2n));
}
DI float lo16(unsigned w) { return __uint_as_float(w << 16); }
DI float hi16(unsigned w) { return __uint_as_float(w & 0xFFFF0000u); }
DI float rbf(float f) { return lo16(pack2(f, f)); }
DI f32x2 unpk(unsigned w) { f32x2 r = {lo16(w), hi16(w)}; return r; }
DI unsigned pk2(f32x2 v) { return pack2(v[0], v[1]); }
DI unsigned nohole(unsigned a) { return a < HOLE - 1 ? a : HOLE - 1; }
template <int CTRL> DI float dppf(float v) {
  return __builtin_bit_cast(float, __builtin_amdgcn_update_dpp(0, __builtin_bit_cast(int, v), CTRL, 0xF, 0xF, true));
}
DI float red16(float v) {
  const auto a = __builtin_amdgcn_permlane16_swap(__float_as_uint(v), __float_as_uint(v), false, false);
  return __uint_as_float(a[0]) + __uint_as_float(a[1]);
}
DI float red32(float v) {
  const auto c = __builtin_amdgcn_permlane32_swap(__float_as_uint(v), __float_as_uint(v), false, false);
  return __uint_as_float(c[0]) + __uint_as_float(c[1]);
}
DI float sel32(float a, float b) {
  const auto c = __builtin_amdgcn_permlane32_swap(__float_as_uint(a), __float_as_uint(b), false, false);
  return __uint_as_float(c[0]) + __uint_as_float(c[1]);
}
DI unsigned lds_addr(const void* p) { return (unsigned)(uintptr_t)(LDS_AS const char*)p; }

__global__ __launch_bounds__(256, 1) __attribute__((amdgpu_num_vgpr(160))) SGA void k_all(
    const char*, const char*, const char*, const char*, const char*, const char*, const char*, const char*, const char*, const char*, char*, char*) {
  extern __shared__ __attribute__((aligned(16))) char smem[];
  ST_DECL;
  ST(0);
  u64x8 ka0; u64x4 ka1;
  asm volatile("s_load_dwordx16 %0, %2, 0x0\n\ts_load_dwordx8 %1, %2, 0x40" : "=&s"(ka0), "=&s"(ka1) : "s"(__builtin_amdgcn_kernarg_segment_ptr()) : "memory"); SB();
  const unsigned tid = threadIdx.x, lane0 = tid & 63, w0 = __builtin_amdgcn_readfirstlane(tid >> 6);
  const unsigned b = blockIdx.x, xcd = b & 7, idx = b >> 3;
  const unsigned rb0 = 8 * (xcd >> 1) + (idx >> 2), cb0 = 4 * (xcd & 1) + (idx & 3);
  const unsigned lb = lds_addr(smem);
  const unsigned lane = lane0, w = w0, rb = rb0, cb = cb0;
  {
    unsigned long long pc; unsigned clen;
    asm volatile("s_getpc_b64 %0\n.Lpc_here_%=:\n\ts_mov_b32 %1, .Lcode_end-.Lpc_here_%=" : "=s"(pc), "=s"(clen));
    const unsigned o = min((((lane & 3) * 4 + w) * 32 + idx) * 128, clen - 4u) & ~3u;
    asm volatile("s_mov_b64 exec, 15\n\tglobal_load_dword v254, %0, %1\n\ts_mov_b64 exec, -1" :: "v"(o), "s"(pc) : "memory");
    SB();
  }
  unsigned voA = (rb * 64 + w * 16) * 1280 + __umul24(lane >> 3, 1280) + (((lane & 7) ^ (lane >> 3)) << 4), voB[3];
#pragma unroll
  for (int i = 0; i < 3; ++i) { const unsigned r = __umul24(i * 64 + lane, 171) >> 11; voB[i] = w * 49152 + cb * 192 + __umul24(r, 2880) + ((lane ^ ((r >> 3) << 1)) << 4); }
  unsigned mX = lb + w * 2048, mW = lb + w * 3072, mA;
  const unsigned rot = ((rb ^ cb) & 3) | (cb < 3 ? 4 : cb == 3 ? (~rb & 2) << 1 : 0);
  unsigned kx[10], kw_[10], voA2 = voA + 9216;
  auto kofs = [&](int s) {
    asm volatile("s_xor_b32 %0, %2, %3\n\ts_mul_i32 %1, %0, 0x30000\n\ts_lshl_b32 %0, %0, 7" : "=&s"(kx[s]), "=&s"(kw_[s]) : "s"(s < 8 ? rot : rot & 1), "i"(s) : "scc");
  };
  kofs(0); kofs(1);
  asm volatile("" : "+v"(voA), "+v"(voA2), "+v"(voB[0]), "+v"(voB[1]), "+v"(voB[2]));
  asm volatile("" : "+s"(mX), "+s"(mW)); SB();
  asm volatile("s_waitcnt lgkmcnt(0)" : "+s"(ka0), "+s"(ka1) :: "memory"); SB();
  const char *x = (const char*)ka0[0], *kin = (const char*)ka0[1], *vin = (const char*)ka0[2], *shift_k = (const char*)ka0[3], *ssm_k = (const char*)ka0[4];
  const char *ssm_D = (const char*)ka0[5], *w_in = (const char*)ka0[6], *w_sp = (const char*)ka0[7], *b_sp = (const char*)ka1[0], *w_out = (const char*)ka1[1];
  char *out = (char*)ka1[2], *ws = (char*)ka1[3];
  const auto rX = __builtin_amdgcn_make_buffer_rsrc((void*)x, 0, S_ * D_ * 2, 0x00020000);
  const auto rW = __builtin_amdgcn_make_buffer_rsrc((void*)(w_in + 1536), 0, 640 * 3072 - 1536, 0x00020000);
  asm volatile("; named registers" :  :  : "v255", "a255"); SB();
  asm volatile("s_add_u32 m0, %0, 81920" :  : "s"(mX) : "memory", "scc"); SB();
  asm volatile("s_nop 0\n\tbuffer_load_dwordx4 %0, %1, %2 offen lds" :  : "v"(voA), "s"(rX), "s"(kx[0]) : "memory"); SB();
  asm volatile("buffer_load_dwordx4 %0, %1, %2 offen offset:1024 lds" :  : "v"(voA2), "s"(rX), "s"(kx[0]) : "memory"); SB();
  asm volatile("s_add_u32 m0, %0, 0" :  : "s"(mW) : "memory", "scc"); SB();
  asm volatile("s_nop 0\n\tbuffer_load_dwordx4 %0, %1, %2 offen lds" :  : "v"(voB[0]), "s"(rW), "s"(kw_[0]) : "memory"); SB();
  asm volatile("buffer_load_dwordx4 %0, %1, %2 offen offset:1024 lds" :  : "v"(voB[1]), "s"(rW), "s"(kw_[0]) : "memory"); SB();
  asm volatile("buffer_load_dwordx4 %0, %1, %2 offen offset:2048 lds" :  : "v"(voB[2]), "s"(rW), "s"(kw_[0]) : "memory"); SB();
  asm volatile("s_add_u32 m0, %0, 90112" :  : "s"(mX) : "memory", "scc"); SB();
  asm volatile("s_nop 0\n\tbuffer_load_dwordx4 %0, %1, %2 offen lds" :  : "v"(voA), "s"(rX), "s"(kx[1]) : "memory"); SB();
  asm volatile("buffer_load_dwordx4 %0, %1, %2 offen offset:1024 lds" :  : "v"(voA2), "s"(rX), "s"(kx[1]) : "memory"); SB();
  kofs(2); kofs(3); kofs(4); kofs(5); kofs(6); kofs(7); kofs(8); kofs(9);
  const auto rWS = __builtin_amdgcn_make_buffer_rsrc((void*)ws, 0, 32 << 20, 0x00020000);
  const auto rW1 = __builtin_amdgcn_make_buffer_rsrc((void*)w_in, 0, 640 * 3072, 0x00020000);
  mA = lb + w * 8192; asm volatile("" : "+s"(mA));
  { unsigned lane = lane0, w = w0, rb = rb0, cb = cb0;
  asm volatile("" : "+v"(lane)); asm volatile("" : "+s"(w), "+s"(rb), "+s"(cb)); SB();

  unsigned ao[8];
  u32x2 kp[5], kc[5], vv[5], sw[5], wt[2], ko[5];
  u32x4 kq[3];
  f32x2 kt[2], kf[2];
  unsigned ku[5];
  unsigned aX[2][2], aW[2][2][3];
#pragma unroll
  for (int n = 0; n < 4; ++n) aX[n >> 1][n & 1] = lb + 98304 + (wm * 32 + ((n & 1) ^ wn) * 16 + l15) * 128 + (((((n >> 1) ^ wm) * 4 + g4) ^ (l15 & 7)) << 4);
#pragma unroll
  for (int n = 0; n < 6; ++n) {
    aW[0][n / 3][n % 3] = lb + ((n / 3 ^ wm) * 32 + 8 * g4 + q4) * 192 + (((wn * 3 + n % 3) ^ (g4 & 1)) << 5) + 8 * p4;
    aW[1][n / 3][n % 3] = aW[0][n / 3][n % 3] + 49152;
    asm volatile("" : "+v"(aW[0][n / 3][n % 3]), "+v"(aW[1][n / 3][n % 3]));
  }
  f32x4 co[3];
  u32x4 ones = {0x3F803F80u, 0x3F803F80u, 0x3F803F80u, 0x3F803F80u};
  f32x4 a2[2][3], a1[2][3], rs, gr, c2[3], c1[3];
  u32x2 zt[3][2];
  asm volatile("" : "+v"(ones), "+v"(aX[0][0]), "+v"(aX[0][1]), "+v"(aX[1][0]), "+v"(aX[1][1]));
  int tmo = (1 << 18);
  u32x2 fk, fz, fh, fi;
  unsigned tg0, tg1;
  float xm[2], xd[2], k1[2], k2[2];
  f32x4 xs[2];
  float xt[4];
  u32x2 xv[5];
  unsigned mU, uso, w3k[2], sFK, sFZ;
  float dD[3], bs;
  u32x4 im[6], zero4;
  SB();
  asm volatile("s_add_u32 m0, %0, 12288" :  : "s"(mW) : "memory", "scc"); SB();
  asm volatile("s_nop 0\n\tbuffer_load_dwordx4 %0, %1, %2 offen lds" :  : "v"(voB[0]), "s"(rW), "s"(kw_[1]) : "memory"); SB();
  asm volatile("buffer_load_dwordx4 %0, %1, %2 offen offset:1024 lds" :  : "v"(voB[1]), "s"(rW), "s"(kw_[1]) : "memory"); SB();
  asm volatile("buffer_load_dwordx4 %0, %1, %2 offen offset:2048 lds" :  : "v"(voB[2]), "s"(rW), "s"(kw_[1]) : "memory"); SB();
  const auto rK = __builtin_amdgcn_make_buffer_rsrc((void*)kin, 0, S_ * D_ * 2, 0x00020000);
  const auto rV = __builtin_amdgcn_make_buffer_rsrc((void*)vin, 0, S_ * D_ * 2, 0x00020000);
  const auto rS = __builtin_amdgcn_make_buffer_rsrc((void*)(shift_k - 3328), 0, D_ * 8 + 3328, 0x00020000);
  const auto rO = __builtin_amdgcn_make_buffer_rsrc((void*)(w_out - 3072), 0, 768 * 1280 + 3072, 0x00020000);
  unsigned voK[4], voV[3], voH = cb * 640 + l16;
#pragma unroll
  for (int n = 0; n < 4; ++n) { const unsigned o = n * 64 + lane, r = (o * 205) >> 11; voK[n] = (rb * 64 + w * 16 - 4) * 1280 + cb * 160 + r * 1120 + lane * 16; }
#pragma unroll
  for (int n = 0; n < 3; ++n) voV[n] = voK[n] + 5120;
  const unsigned wof = (rb >> 3) * 30 + min((rb & 7) * 4 + w, 29u), wok = (wof * 205) >> 10;
  unsigned voO = (lane >> 1) * 1280 + (lane & 1) * 16 + wok * 40960 + (wof - 5 * wok) * 32 + cb * 160, vWO = W_WO + (cb * 120 + wof) * 1024 + l16;
  asm volatile("" : "+v"(vWO), "+v"(voK[0]), "+v"(voK[1]), "+v"(voK[2]), "+v"(voK[3]), "+v"(voV[0]), "+v"(voV[1]), "+v"(voV[2]), "+v"(voH), "+v"(voO));
  SB();
  const auto rF = __builtin_amdgcn_make_buffer_rsrc((void*)ssm_k, 0, S_ * D_ * 2, 0x00020000);
  unsigned vF = ch0 * 4096 + w * 1024 + l16, c2s_ = c2s;
  unsigned hpo[3], vD = ch0 * 2, vD2 = vD + c2o * 2 - 4, vBs = (rb * 64 + w * 16 + l15) * 2;
  hpo[0] = w ? ch0 * 4096 + w * 1024 - 4 : 0x40000000u; hpo[1] = hpo[0] + 4096; hpo[2] = hpo[0] + c2s;
  asm volatile("" : "+s"(hpo[0]), "+s"(hpo[1]), "+s"(hpo[2]), "+v"(vD), "+v"(vD2), "+v"(vBs), "+v"(vF), "+s"(c2s_));
  SB();
  unsigned ka = lb + 98304 + w * 8192 + (4 * p4 + q4) * 160 + 8 * g4, oa = lb + 98304 + 7168 + w * 8192 + (4 * g4 + q4) * 32 + 8 * p4;
  unsigned sa = lb + 98304 + 3328 + w * 8192 + (4 * g4 + p4) * 8;
  unsigned kw = lb + 12288 + (4 * g4 + p4) * 144 + w * 32 + q4 * 8, kr = lb + 12288 + (w * 24 + (lane >> 3)) * 144 + (lane & 7) * 16;
  unsigned vKV = W_KV + (cb * 80 + w * 24 + (lane >> 3)) * 4096 + rb * 128 + (lane & 7) * 16;
  unsigned vS = W_ST + (rb * 2 + wm) * 8192 + wn * 512 + (lane >> 5) * 512 + l16;
  asm volatile("" : "+v"(ka), "+v"(oa), "+v"(sa), "+v"(kw), "+v"(kr), "+v"(vKV), "+v"(vS));
  SB();
  ST(1);
  SB();
  asm volatile("s_add_u32 m0, %0, 98304" :  : "s"(mX) : "memory", "scc"); SB();
  asm volatile("s_nop 0\n\tbuffer_load_dwordx4 %0, %1, %2 offen lds" :  : "v"(voA), "s"(rX), "s"(kx[2]) : "memory"); SB();
  asm volatile("buffer_load_dwordx4 %0, %1, %2 offen offset:1024 lds" :  : "v"(voA2), "s"(rX), "s"(kx[2]) : "memory"); SB();
  asm volatile("buffer_load_dwordx2 v[252:253], off, %0, 0 sc1" :  : "s"(rWS) : "memory"); SB();
  asm volatile("s_add_u32 m0, %0, 106496" :  : "s"(mX) : "memory", "scc"); SB();
  asm volatile("s_nop 0\n\tbuffer_load_dwordx4 %0, %1, %2 offen lds" :  : "v"(voA), "s"(rX), "s"(kx[3]) : "memory"); SB();
  asm volatile("buffer_load_dwordx4 %0, %1, %2 offen offset:1024 lds" :  : "v"(voA2), "s"(rX), "s"(kx[3]) : "memory"); SB();
  asm volatile("s_add_u32 m0, %0, 24576" :  : "s"(mW) : "memory", "scc"); SB();
  asm volatile("s_nop 0\n\tbuffer_load_dwordx4 %0, %1, %2 offen lds" :  : "v"(voB[0]), "s"(rW), "s"(kw_[2]) : "memory"); SB();
  asm volatile("s_waitcnt vmcnt(11) lgkmcnt(0)\n\ts_barrier" :  :  : "memory"); SB();
  ST(4);
  asm volatile("ds_read_b128 a[0:3], %0 offset:0" :  : "v"((aX[0][0] - 16384)) : "memory"); SB();
  asm volatile("ds_read_b128 a[4:7], %0 offset:0" :  : "v"((aX[0][1] - 16384)) : "memory"); SB();
  asm volatile("ds_read_b64_tr_b16 v[160:161], %0 offset:0" :  : "v"(aW[0][0][0]) : "memory"); SB();
  asm volatile("ds_read_b64_tr_b16 v[162:163], %0 offset:768" :  : "v"(aW[0][0][0]) : "memory"); SB();
  asm volatile("ds_read_b64_tr_b16 v[164:165], %0 offset:0" :  : "v"(aW[0][0][1]) : "memory"); SB();
  asm volatile("ds_read_b64_tr_b16 v[166:167], %0 offset:768" :  : "v"(aW[0][0][1]) : "memory"); SB();
  asm volatile("ds_read_b64_tr_b16 v[168:169], %0 offset:0" :  : "v"(aW[0][0][2]) : "memory"); SB();
  asm volatile("ds_read_b64_tr_b16 v[170:171], %0 offset:768" :  : "v"(aW[0][0][2]) : "memory"); SB();
  asm volatile("ds_read_b128 a[8:11], %0 offset:0" :  : "v"((aX[1][0] - 16384)) : "memory"); SB();
  asm volatile("ds_read_b128 a[12:15], %0 offset:0" :  : "v"((aX[1][1] - 16384)) : "memory"); SB();
  asm volatile("s_waitcnt lgkmcnt(6)\n\tv_mfma_f32_16x16x32_bf16 %0, v[160:163], a[0:3], 0" : "=&v"(a2[0][0]) :  : ); SB();
  asm volatile("ds_read_b64_tr_b16 v[172:173], %0 offset:0" :  : "v"(aW[0][1][0]) : "memory"); SB();
  asm volatile("buffer_load_dwordx4 %0, %1, %2 offen offset:1024 lds" :  : "v"(voB[1]), "s"(rW), "s"(kw_[2]) : "memory"); SB();
  asm volatile("v_mfma_f32_16x16x32_bf16 %0, v[160:163], a[4:7], 0" : "=&v"(a2[1][0]) :  : ); SB();
  asm volatile("ds_read_b64_tr_b16 v[174:175], %0 offset:768" :  : "v"(aW[0][1][0]) : "memory"); SB();
  asm volatile("buffer_load_dwordx4 %0, %1, %2 offen offset:2048 lds" :  : "v"(voB[2]), "s"(rW), "s"(kw_[2]) : "memory"); SB();
  asm volatile("v_mfma_f32_16x16x32_bf16 %0, v[160:163], %1, 0" : "=&v"(c2[0]) : "v"(ones) : ); SB();
  asm volatile("ds_read_b64_tr_b16 v[176:177], %0 offset:0" :  : "v"(aW[0][1][1]) : "memory"); SB();
  asm volatile("s_waitcnt lgkmcnt(5)\n\tv_mfma_f32_16x16x32_bf16 %0, v[164:167], a[0:3], 0" : "=&v"(a2[0][1]) :  : ); SB();
  asm volatile("ds_read_b64_tr_b16 v[178:179], %0 offset:768" :  : "v"(aW[0][1][1]) : "memory"); SB();
  asm volatile("v_mfma_f32_16x16x32_bf16 %0, v[164:167], a[4:7], 0" : "=&v"(a2[1][1]) :  : ); SB();
  asm volatile("ds_read_b64_tr_b16 v[180:181], %0 offset:0" :  : "v"(aW[0][1][2]) : "memory"); SB();
  asm volatile("s_add_u32 m0, %0, 114688" :  : "s"(mX) : "memory", "scc"); SB();
  asm volatile("v_mfma_f32_16x16x32_bf16 %0, v[164:167], %1, 0" : "=&v"(c2[1]) : "v"(ones) : ); SB();
  asm volatile("ds_read_b64_tr_b16 v[182:183], %0 offset:768" :  : "v"(aW[0][1][2]) : "memory"); SB();
  asm volatile("buffer_load_dwordx4 %0, %1, %2 offen lds" :  : "v"(voA), "s"(rX), "s"(kx[4]) : "memory"); SB();
  asm volatile("v_mfma_f32_16x16x32_bf16 %0, v[168:171], a[0:3], 0" : "=&v"(a2[0][2]) :  : ); SB();
  unsigned aV[5];
#pragma unroll
  for (int j = 0; j < 5; ++j) {
    const unsigned c = cb * 10 + 2 * j, t = (c >> 3) ^ (c < 64 ? rot : rot & 1);
    aV[j] = lb + 81920 + t * 8192 + w * 2048 + l15 * 128 + ((((c & 7) + (g4 >> 1)) ^ (l15 & 7)) << 4) + (g4 & 1) * 8;
    asm volatile("" : "+v"(aV[j]));
  }
  SB();
  asm volatile("buffer_load_dwordx4 %0, %1, %2 offen offset:1024 lds" :  : "v"(voA2), "s"(rX), "s"(kx[4]) : "memory"); SB();
  asm volatile("s_add_u32 m0, %0, 122880" :  : "s"(mX) : "memory", "scc"); SB();
  asm volatile("s_nop 0\n\tbuffer_load_dwordx4 %0, %1, %2 offen lds" :  : "v"(voA), "s"(rX), "s"(kx[5]) : "memory"); SB();
  asm volatile("s_waitcnt vmcnt(11)\n\ts_barrier" :  :  : "memory"); SB();
  asm volatile("ds_read_b64_tr_b16 v[184:185], %0 offset:12288" :  : "v"(aW[0][0][0]) : "memory"); SB();
  asm volatile("v_mfma_f32_16x16x32_bf16 %0, v[168:171], a[4:7], 0" : "=&v"(a2[1][2]) :  : ); SB();
  asm volatile("ds_read_b64_tr_b16 v[186:187], %0 offset:13056" :  : "v"(aW[0][0][0]) : "memory"); SB();
  asm volatile("v_mfma_f32_16x16x32_bf16 %0, v[168:171], %1, 0" : "=&v"(c2[2]) : "v"(ones) : ); SB();
  asm volatile("ds_read_b64_tr_b16 v[188:189], %0 offset:12288" :  : "v"(aW[0][0][1]) : "memory"); SB();
  asm volatile("v_mfma_f32_16x16x32_bf16 %0, %1, a[0:3], 0" : "=&v"(rs) : "v"(ones) : ); SB();
  asm volatile("ds_read_b128 a[16:19], %0 offset:8192" :  : "v"((aX[0][0] - 16384)) : "memory"); SB();
  asm volatile("ds_read_b128 a[20:23], %0 offset:8192" :  : "v"((aX[0][1] - 16384)) : "memory"); SB();
  asm volatile("ds_read_b64_tr_b16 v[190:191], %0 offset:13056" :  : "v"(aW[0][0][1]) : "memory"); SB();
  asm volatile("buffer_load_dwordx4 %0, %1, %2 offen offset:1024 lds" :  : "v"(voA2), "s"(rX), "s"(kx[5]) : "memory"); SB();
  asm volatile("v_mfma_f32_16x16x32_bf16 %0, a[0:3], a[0:3], 0" : "=&v"(gr) :  : ); SB();
  asm volatile("ds_read_b64_tr_b16 v[192:193], %0 offset:12288" :  : "v"(aW[0][0][2]) : "memory"); SB();
  asm volatile("s_waitcnt lgkmcnt(7)\n\tv_mfma_f32_16x16x32_bf16 %0, v[172:175], a[8:11], %0" : "+v"(a2[0][0]) :  : ); SB();
  asm volatile("ds_read_b64_tr_b16 v[194:195], %0 offset:13056" :  : "v"(aW[0][0][2]) : "memory"); SB();
  asm volatile("s_add_u32 m0, %0, 36864" :  : "s"(mW) : "memory", "scc"); SB();
  asm volatile("v_mfma_f32_16x16x32_bf16 %0, v[172:175], a[12:15], %0" : "+v"(a2[1][0]) :  : ); SB();
  asm volatile("ds_read_b128 a[24:27], %0 offset:8192" :  : "v"((aX[1][0] - 16384)) : "memory"); SB();
  asm volatile("v_mfma_f32_16x16x32_bf16 %0, v[176:179], a[8:11], %0" : "+v"(a2[0][1]) :  : ); SB();
  asm volatile("ds_read_b128 a[28:31], %0 offset:8192" :  : "v"((aX[1][1] - 16384)) : "memory"); SB();
  asm volatile("v_mfma_f32_16x16x32_bf16 %0, v[176:179], a[12:15], %0" : "+v"(a2[1][1]) :  : ); SB();
  asm volatile("buffer_load_dwordx4 %0, %1, %2 offen lds" :  : "v"(voB[0]), "s"(rW), "s"(kw_[3]) : "memory"); SB();
  asm volatile("v_mfma_f32_16x16x32_bf16 %0, v[180:183], a[8:11], %0" : "+v"(a2[0][2]) :  : ); SB();
  asm volatile("ds_read_b64_tr_b16 v[160:161], %0 offset:12288" :  : "v"(aW[0][1][0]) : "memory"); SB();
  asm volatile("v_mfma_f32_16x16x32_bf16 %0, v[180:183], a[12:15], %0" : "+v"(a2[1][2]) :  : ); SB();
  asm volatile("ds_read_b64_tr_b16 v[162:163], %0 offset:13056" :  : "v"(aW[0][1][0]) : "memory"); SB();
  asm volatile("v_mfma_f32_16x16x32_bf16 %0, %1, a[8:11], %0" : "+v"(rs) : "v"(ones) : ); SB();
  asm volatile("ds_read_b64_tr_b16 v[164:165], %0 offset:12288" :  : "v"(aW[0][1][1]) : "memory"); SB();
  asm volatile("v_mfma_f32_16x16x32_bf16 %0, a[8:11], a[8:11], %0" : "+v"(gr) :  : ); SB();
  asm volatile("ds_read_b64_tr_b16 v[166:167], %0 offset:13056" :  : "v"(aW[0][1][1]) : "memory"); SB();
  asm volatile("s_waitcnt lgkmcnt(8)\n\tv_mfma_f32_16x16x32_bf16 %0, v[184:187], a[16:19], %0" : "+v"(a2[0][0]) :  : ); SB();
  asm volatile("ds_read_b64_tr_b16 v[168:169], %0 offset:12288" :  : "v"(aW[0][1][2]) : "memory"); SB();
  asm volatile("v_mfma_f32_16x16x32_bf16 %0, v[184:187], a[20:23], %0" : "+v"(a2[1][0]) :  : ); SB();
  asm volatile("ds_read_b64_tr_b16 v[170:171], %0 offset:13056" :  : "v"(aW[0][1][2]) : "memory"); SB();
  asm volatile("v_mfma_f32_16x16x32_bf16 %0, v[184:187], %1, %0" : "+v"(c2[0]) : "v"(ones) : ); SB();
  asm volatile("buffer_load_dwordx4 %0, %1, %2 offen offset:1024 lds" :  : "v"(voB[1]), "s"(rW), "s"(kw_[3]) : "memory"); SB();
  asm volatile("v_mfma_f32_16x16x32_bf16 %0, v[188:191], a[16:19], %0" : "+v"(a2[0][1]) :  : ); SB();
  asm volatile("buffer_load_dwordx4 %0, %1, %2 offen offset:2048 lds" :  : "v"(voB[2]), "s"(rW), "s"(kw_[3]) : "memory"); SB();
  asm volatile("v_mfma_f32_16x16x32_bf16 %0, v[188:191], a[20:23], %0" : "+v"(a2[1][1]) :  : ); SB();
  asm volatile("s_add_u32 m0, %0, 131072" :  : "s"(mX) : "memory", "scc"); SB();
  asm volatile("s_nop 0\n\tbuffer_load_dwordx4 %0, %1, %2 offen lds" :  : "v"(voA), "s"(rX), "s"(kx[6]) : "memory"); SB();
  asm volatile("buffer_load_dwordx4 %0, %1, %2 offen offset:1024 lds" :  : "v"(voA2), "s"(rX), "s"(kx[6]) : "memory"); SB();
  asm volatile("s_add_u32 m0, %0, 139264" :  : "s"(mX) : "memory", "scc"); SB();
  asm volatile("s_nop 0\n\tbuffer_load_dwordx4 %0, %1, %2 offen lds" :  : "v"(voA), "s"(rX), "s"(kx[7]) : "memory"); SB();
  asm volatile("s_waitcnt vmcnt(10)\n\ts_barrier" :  :  : "memory"); SB();
  asm volatile("ds_read_b64_tr_b16 v[172:173], %0 offset:24576" :  : "v"(aW[0][0][0]) : "memory"); SB();
  asm volatile("v_mfma_f32_16x16x32_bf16 %0, v[188:191], %1, %0" : "+v"(c2[1]) : "v"(ones) : ); SB();
  asm volatile("ds_read_b64_tr_b16 v[174:175], %0 offset:25344" :  : "v"(aW[0][0][0]) : "memory"); SB();
  asm volatile("s_waitcnt lgkmcnt(2)\n\tv_mfma_f32_16x16x32_bf16 %0, v[192:195], a[16:19], %0" : "+v"(a2[0][2]) :  : ); SB();
  asm volatile("ds_read_b64_tr_b16 v[176:177], %0 offset:24576" :  : "v"(aW[0][0][1]) : "memory"); SB();
  asm volatile("v_mfma_f32_16x16x32_bf16 %0, v[192:195], a[20:23], %0" : "+v"(a2[1][2]) :  : ); SB();
  asm volatile("ds_read_b64_tr_b16 v[178:179], %0 offset:25344" :  : "v"(aW[0][0][1]) : "memory"); SB();
  asm volatile("v_mfma_f32_16x16x32_bf16 %0, v[192:195], %1, %0" : "+v"(c2[2]) : "v"(ones) : ); SB();
  asm volatile("ds_read_b64_tr_b16 v[180:181], %0 offset:24576" :  : "v"(aW[0][0][2]) : "memory"); SB();
  asm volatile("v_mfma_f32_16x16x32_bf16 %0, %1, a[16:19], %0" : "+v"(rs) : "v"(ones) : ); SB();
  asm volatile("ds_read_b128 a[32:35], %0 offset:0" :  : "v"(aX[0][0]) : "memory"); SB();
  asm volatile("ds_read_b128 a[36:39], %0 offset:0" :  : "v"(aX[0][1]) : "memory"); SB();
  asm volatile("ds_read_b64_tr_b16 v[182:183], %0 offset:25344" :  : "v"(aW[0][0][2]) : "memory"); SB();
  asm volatile("v_mfma_f32_16x16x32_bf16 %0, a[16:19], a[16:19], %0" : "+v"(gr) :  : ); SB();
  asm volatile("ds_read_b128 a[40:43], %0 offset:0" :  : "v"(aX[1][0]) : "memory"); SB();
  asm volatile("v_mfma_f32_16x16x32_bf16 %0, v[160:163], a[24:27], %0" : "+v"(a2[0][0]) :  : ); SB();
  asm volatile("ds_read_b128 a[44:47], %0 offset:0" :  : "v"(aX[1][1]) : "memory"); SB();
  asm volatile("v_mfma_f32_16x16x32_bf16 %0, v[160:163], a[28:31], %0" : "+v"(a2[1][0]) :  : ); SB();
  asm volatile("ds_read_b128 a[48:51], %0 offset:8192" :  : "v"(aX[0][0]) : "memory"); SB();
  asm volatile("v_mfma_f32_16x16x32_bf16 %0, v[164:167], a[24:27], %0" : "+v"(a2[0][1]) :  : ); SB();
  asm volatile("ds_read_b128 a[52:55], %0 offset:8192" :  : "v"(aX[0][1]) : "memory"); SB();
  asm volatile("v_mfma_f32_16x16x32_bf16 %0, v[164:167], a[28:31], %0" : "+v"(a2[1][1]) :  : ); SB();
  asm volatile("ds_read_b128 a[56:59], %0 offset:8192" :  : "v"(aX[1][0]) : "memory"); SB();
  asm volatile("v_mfma_f32_16x16x32_bf16 %0, v[168:171], a[24:27], %0" : "+v"(a2[0][2]) :  : ); SB();
  asm volatile("ds_read_b128 a[60:63], %0 offset:8192" :  : "v"(aX[1][1]) : "memory"); SB();
  asm volatile("v_mfma_f32_16x16x32_bf16 %0, v[168:171], a[28:31], %0" : "+v"(a2[1][2]) :  : ); SB();
  asm volatile("buffer_load_dwordx4 %0, %1, %2 offen offset:1024 lds" :  : "v"(voA2), "s"(rX), "s"(kx[7]) : "memory"); SB();
  asm volatile("s_add_u32 m0, %0, 49152" :  : "s"(mW) : "memory", "scc"); SB();
  asm volatile("v_mfma_f32_16x16x32_bf16 %0, %1, a[24:27], %0" : "+v"(rs) : "v"(ones) : ); SB();
  asm volatile("ds_read_b64_tr_b16 v[184:185], %0 offset:24576" :  : "v"(aW[0][1][0]) : "memory"); SB();
  asm volatile("v_mfma_f32_16x16x32_bf16 %0, a[24:27], a[24:27], %0" : "+v"(gr) :  : ); SB();
  asm volatile("ds_read_b64_tr_b16 v[186:187], %0 offset:25344" :  : "v"(aW[0][1][0]) : "memory"); SB();
  asm volatile("s_waitcnt lgkmcnt(9)\n\tv_mfma_f32_16x16x32_bf16 %0, v[172:175], a[32:35], %0" : "+v"(a2[0][0]) :  : ); SB();
  asm volatile("ds_read_b64_tr_b16 v[188:189], %0 offset:24576" :  : "v"(aW[0][1][1]) : "memory"); SB();
  asm volatile("v_mfma_f32_16x16x32_bf16 %0, v[172:175], a[36:39], %0" : "+v"(a2[1][0]) :  : ); SB();
  asm volatile("ds_read_b64_tr_b16 v[190:191], %0 offset:25344" :  : "v"(aW[0][1][1]) : "memory"); SB();
  asm volatile("v_mfma_f32_16x16x32_bf16 %0, v[172:175], %1, %0" : "+v"(c2[0]) : "v"(ones) : ); SB();
  asm volatile("ds_read_b64_tr_b16 v[192:193], %0 offset:24576" :  : "v"(aW[0][1][2]) : "memory"); SB();
  asm volatile("buffer_load_dwordx4 %0, %1, %2 offen lds" :  : "v"(voB[0]), "s"(rW), "s"(kw_[4]) : "memory"); SB();
  asm volatile("v_mfma_f32_16x16x32_bf16 %0, v[176:179], a[32:35], %0" : "+v"(a2[0][1]) :  : ); SB();
  asm volatile("ds_read_b64_tr_b16 v[194:195], %0 offset:25344" :  : "v"(aW[0][1][2]) : "memory"); SB();
  asm volatile("v_mfma_f32_16x16x32_bf16 %0, v[176:179], a[36:39], %0" : "+v"(a2[1][1]) :  : ); SB();
  asm volatile("buffer_load_dwordx4 %0, %1, %2 offen offset:1024 lds" :  : "v"(voB[1]), "s"(rW), "s"(kw_[4]) : "memory"); SB();
  asm volatile("v_mfma_f32_16x16x32_bf16 %0, v[176:179], %1, %0" : "+v"(c2[1]) : "v"(ones) : ); SB();
  asm volatile("s_waitcnt lgkmcnt(7)\n\tv_mfma_f32_16x16x32_bf16 %0, v[180:183], a[32:35], %0" : "+v"(a2[0][2]) :  : ); SB();
  asm volatile("v_mfma_f32_16x16x32_bf16 %0, v[180:183], a[36:39], %0" : "+v"(a2[1][2]) :  : ); SB();
  asm volatile("buffer_load_dwordx4 %0, %1, %2 offen offset:2048 lds" :  : "v"(voB[2]), "s"(rW), "s"(kw_[4]) : "memory"); SB();
  asm volatile("v_mfma_f32_16x16x32_bf16 %0, v[180:183], %1, %0" : "+v"(c2[2]) : "v"(ones) : ); SB();
  const auto rA = __builtin_amdgcn_make_buffer_rsrc((void*)(w_sp - 3072), 0, S_ * S_ * 2 + 3072, 0x00020000);
  unsigned ca = lb + 98304 + 6912 + w * 8192 + g4 * 16, cr = lb + 98304 + 6912 + (w ^ 2) * 8192 + g4 * 16;
  unsigned voS = (rb * 64 + (lane >> 3)) * 4096 + w * 1024 + (cb & 1) * 128 + (((lane & 7) ^ (lane >> 3)) << 4), aA[2];
#pragma unroll
  for (int k = 0; k < 2; ++k) aA[k] = lb + 98304 + w * 8192 + l15 * 128 + (((k * 4 + g4) ^ (l15 & 7)) << 4);
  unsigned xw = lb + 98304 + 6656 + w * 8192 + l15 * 16, xr_ = lb + 98304 + 6656 + (w ^ 1) * 8192 + l15 * 16;
  unsigned vo = W_ST + ((rb * 2 + wm) * 8 + cb) * 1024 + wn * 128 + ((lane >> 5) ^ wn) * 512 + (l15 >> 3) * 256 + (l15 & 7) * 16;
  asm volatile("" : "+v"(ca), "+v"(cr), "+v"(voS), "+v"(aA[0]), "+v"(aA[1]), "+v"(xw), "+v"(xr_), "+v"(vo));
  SB();
  asm volatile("s_add_u32 m0, %0, 147456" :  : "s"(mX) : "memory", "scc"); SB();
  asm volatile("s_nop 0\n\tbuffer_load_dwordx4 %0, %1, %2 offen lds" :  : "v"(voA), "s"(rX), "s"(kx[8]) : "memory"); SB();
  asm volatile("buffer_load_dwordx4 %0, %1, %2 offen offset:1024 lds" :  : "v"(voA2), "s"(rX), "s"(kx[8]) : "memory"); SB();
  asm volatile("s_waitcnt vmcnt(9)\n\ts_barrier" :  :  : "memory"); SB();
  asm volatile("ds_read_b64 %0, %1 offset:0" : "=&v"(xv[0]) : "v"(aV[0]) : "memory"); SB();
  asm volatile("ds_read_b64 %0, %1 offset:0" : "=&v"(xv[1]) : "v"(aV[1]) : "memory"); SB();
  asm volatile("ds_read_b64_tr_b16 v[160:161], %0 offset:36864" :  : "v"(aW[0][0][0]) : "memory"); SB();
  asm volatile("s_add_u32 m0, %0, 155648" :  : "s"(mX) : "memory", "scc"); SB();
  asm volatile("v_mfma_f32_16x16x32_bf16 %0, %1, a[32:35], %0" : "+v"(rs) : "v"(ones) : ); SB();
  asm volatile("ds_read_b64 %0, %1 offset:0" : "=&v"(xv[2]) : "v"(aV[2]) : "memory"); SB();
  asm volatile("ds_read_b64 %0, %1 offset:0" : "=&v"(xv[3]) : "v"(aV[3]) : "memory"); SB();
  asm volatile("ds_read_b64_tr_b16 v[162:163], %0 offset:37632" :  : "v"(aW[0][0][0]) : "memory"); SB();
  asm volatile("buffer_load_dwordx4 %0, %1, %2 offen lds" :  : "v"(voA), "s"(rX), "s"(kx[9]) : "memory"); SB();
  asm volatile("v_mfma_f32_16x16x32_bf16 %0, a[32:35], a[32:35], %0" : "+v"(gr) :  : ); SB();
  asm volatile("ds_read_b64 %0, %1 offset:0" : "=&v"(xv[4]) : "v"(aV[4]) : "memory"); SB();
  asm volatile("ds_read_b64_tr_b16 v[164:165], %0 offset:36864" :  : "v"(aW[0][0][1]) : "memory"); SB();
  asm volatile("buffer_load_dwordx4 %0, %1, %2 offen offset:1024 lds" :  : "v"(voA2), "s"(rX), "s"(kx[9]) : "memory"); SB();
  asm volatile("v_mfma_f32_16x16x32_bf16 %0, %1, a[40:43], %0" : "+v"(rs) : "v"(ones) : ); SB();
  asm volatile("ds_read_b64_tr_b16 v[166:167], %0 offset:37632" :  : "v"(aW[0][0][1]) : "memory"); SB();
  asm volatile("v_mfma_f32_16x16x32_bf16 %0, a[40:43], a[40:43], %0" : "+v"(gr) :  : ); SB();
  asm volatile("ds_read_b64_tr_b16 v[168:169], %0 offset:36864" :  : "v"(aW[0][0][2]) : "memory"); SB();
  asm volatile("s_add_u32 m0, %0, 61440" :  : "s"(mW) : "memory", "scc"); SB();
  asm volatile("s_waitcnt lgkmcnt(10)\n\tv_mfma_f32_16x16x32_bf16 %0, v[184:187], a[40:43], %0" : "+v"(a2[0][0]) :  : ); SB();
  asm volatile("ds_read_b64_tr_b16 v[170:171], %0 offset:37632" :  : "v"(aW[0][0][2]) : "memory"); SB();
  asm volatile("buffer_load_dwordx4 %0, %1, %2 offen lds" :  : "v"(voB[0]), "s"(rW), "s"(kw_[5]) : "memory"); SB();
  asm volatile("v_mfma_f32_16x16x32_bf16 %0, v[184:187], a[44:47], %0" : "+v"(a2[1][0]) :  : ); SB();
  asm volatile("ds_read_b128 a[64:67], %0 offset:16384" :  : "v"(aX[0][0]) : "memory"); SB();
  asm volatile("v_mfma_f32_16x16x32_bf16 %0, v[188:191], a[40:43], %0" : "+v"(a2[0][1]) :  : ); SB();
  asm volatile("ds_read_b128 a[68:71], %0 offset:16384" :  : "v"(aX[0][1]) : "memory"); SB();
  asm volatile("v_mfma_f32_16x16x32_bf16 %0, v[188:191], a[44:47], %0" : "+v"(a2[1][1]) :  : ); SB();
  asm volatile("ds_read_b128 a[72:75], %0 offset:16384" :  : "v"(aX[1][0]) : "memory"); SB();
  asm volatile("v_mfma_f32_16x16x32_bf16 %0, v[192:195], a[40:43], %0" : "+v"(a2[0][2]) :  : ); SB();
  asm volatile("ds_read_b128 a[76:79], %0 offset:16384" :  : "v"(aX[1][1]) : "memory"); SB();
  asm volatile("v_mfma_f32_16x16x32_bf16 %0, v[192:195], a[44:47], %0" : "+v"(a2[1][2]) :  : ); SB();
  asm volatile("ds_read_b128 a[80:83], %0 offset:24576" :  : "v"(aX[0][0]) : "memory"); SB();
  asm volatile("v_mfma_f32_16x16x32_bf16 %0, %1, a[48:51], %0" : "+v"(rs) : "v"(ones) : ); SB();
  asm volatile("ds_read_b128 a[84:87], %0 offset:24576" :  : "v"(aX[0][1]) : "memory"); SB();
  asm volatile("v_mfma_f32_16x16x32_bf16 %0, a[48:51], a[48:51], %0" : "+v"(gr) :  : ); SB();
  asm volatile("ds_read_b128 a[88:91], %0 offset:24576" :  : "v"(aX[1][0]) : "memory"); SB();
  asm volatile("v_mfma_f32_16x16x32_bf16 %0, %1, a[56:59], %0" : "+v"(rs) : "v"(ones) : ); SB();
  asm volatile("ds_read_b64_tr_b16 v[172:173], %0 offset:36864" :  : "v"(aW[0][1][0]) : "memory"); SB();
  asm volatile("v_mfma_f32_16x16x32_bf16 %0, a[56:59], a[56:59], %0" : "+v"(gr) :  : ); SB();
  asm volatile("ds_read_b64_tr_b16 v[174:175], %0 offset:37632" :  : "v"(aW[0][1][0]) : "memory"); SB();
  asm volatile("s_waitcnt lgkmcnt(9)\n\tv_mfma_f32_16x16x32_bf16 %0, v[160:163], a[48:51], %0" : "+v"(a2[0][0]) :  : ); SB();
  asm volatile("ds_read_b64_tr_b16 v[176:177], %0 offset:36864" :  : "v"(aW[0][1][1]) : "memory"); SB();
  asm volatile("v_mfma_f32_16x16x32_bf16 %0, v[160:163], a[52:55], %0" : "+v"(a2[1][0]) :  : ); SB();
  asm volatile("ds_read_b64_tr_b16 v[178:179], %0 offset:37632" :  : "v"(aW[0][1][1]) : "memory"); SB();
  asm volatile("v_mfma_f32_16x16x32_bf16 %0, v[160:163], %1, %0" : "+v"(c2[0]) : "v"(ones) : ); SB();
  asm volatile("ds_read_b64_tr_b16 v[180:181], %0 offset:36864" :  : "v"(aW[0][1][2]) : "memory"); SB();
  asm volatile("v_mfma_f32_16x16x32_bf16 %0, v[164:167], a[48:51], %0" : "+v"(a2[0][1]) :  : ); SB();
  asm volatile("ds_read_b64_tr_b16 v[182:183], %0 offset:37632" :  : "v"(aW[0][1][2]) : "memory"); SB();
  asm volatile("v_mfma_f32_16x16x32_bf16 %0, v[164:167], a[52:55], %0" : "+v"(a2[1][1]) :  : ); SB();
  asm volatile("ds_read_b128 a[92:95], %0 offset:24576" :  : "v"(aX[1][1]) : "memory"); SB();
  asm volatile("v_mfma_f32_16x16x32_bf16 %0, v[164:167], %1, %0" : "+v"(c2[1]) : "v"(ones) : ); SB();
  asm volatile("buffer_load_dwordx4 %0, %1, %2 offen offset:1024 lds" :  : "v"(voB[1]), "s"(rW), "s"(kw_[5]) : "memory"); SB();
  asm volatile("v_mfma_f32_16x16x32_bf16 %0, v[168:171], a[48:51], %0" : "+v"(a2[0][2]) :  : ); SB();
  asm volatile("buffer_load_dwordx4 %0, %1, %2 offen offset:2048 lds" :  : "v"(voB[2]), "s"(rW), "s"(kw_[5]) : "memory"); SB();
  asm volatile("v_mfma_f32_16x16x32_bf16 %0, v[168:171], a[52:55], %0" : "+v"(a2[1][2]) :  : ); SB();
  asm volatile("v_mfma_f32_16x16x32_bf16 %0, v[168:171], %1, %0" : "+v"(c2[2]) : "v"(ones) : ); SB();
#pragma unroll
  for (int n = 0; n < 8; ++n) { ao[n] = (n ^ (2 * w)) * 32768 + (3 - (n & 3)) * 1024; asm volatile("" : "+s"(ao[n])); }
  w3k[0] = w3s + 32768; w3k[1] = w3s + 65536; sFZ = W_FZ + ((cb * 32 + rb) * 4 + w) * 8; sFK = sFZ + 8192; mU = lb + w * 1024; uso = W_KV + c2s;
  asm volatile("" : "+s"(w3k[0]), "+s"(w3k[1]), "+s"(sFZ), "+s"(sFK), "+s"(mU), "+s"(uso));
  SB();
  asm volatile("s_add_u32 m0, %0, 73728" :  : "s"(mW) : "memory", "scc"); SB();
  asm volatile("s_nop 0\n\tbuffer_load_dwordx4 %0, %1, %2 offen lds" :  : "v"(voB[0]), "s"(rW), "s"(kw_[6]) : "memory"); SB();
  asm volatile("v_readfirstlane_b32 %0, v252\n\tv_readfirstlane_b32 %1, v253\n\ts_nop 0\n\ts_add_u32 %0, %0, 1\n\ts_addc_u32 %1, %1, 0" : "=&s"(tg0), "=&s"(tg1) :  : "memory", "scc"); SB();
  asm volatile("buffer_load_dwordx4 %0, %1, %2 offen offset:1024 lds" :  : "v"(voB[1]), "s"(rW), "s"(kw_[6]) : "memory"); SB();
  asm volatile("buffer_load_dwordx4 %0, %1, %2 offen offset:2048 lds" :  : "v"(voB[2]), "s"(rW), "s"(kw_[6]) : "memory"); SB();
  asm volatile("s_waitcnt vmcnt(10)\n\ts_barrier" :  :  : "memory"); SB();
  asm volatile("ds_read_b64_tr_b16 v[184:185], %0 offset:0" :  : "v"(aW[1][0][0]) : "memory"); SB();
  asm volatile("s_waitcnt lgkmcnt(1)\n\tv_mfma_f32_16x16x32_bf16 %0, %1, a[64:67], %0" : "+v"(rs) : "v"(ones) : ); SB();
  asm volatile("ds_read_b64_tr_b16 v[186:187], %0 offset:768" :  : "v"(aW[1][0][0]) : "memory"); SB();
  asm volatile("s_add_u32 m0, %0, 86016" :  : "s"(mW) : "memory", "scc"); SB();
  asm volatile("v_mfma_f32_16x16x32_bf16 %0, a[64:67], a[64:67], %0" : "+v"(gr) :  : ); SB();
  asm volatile("ds_read_b64_tr_b16 v[188:189], %0 offset:0" :  : "v"(aW[1][0][1]) : "memory"); SB();
  asm volatile("v_mfma_f32_16x16x32_bf16 %0, %1, a[72:75], %0" : "+v"(rs) : "v"(ones) : ); SB();
  asm volatile("ds_read_b64_tr_b16 v[190:191], %0 offset:768" :  : "v"(aW[1][0][1]) : "memory"); SB();
  asm volatile("v_mfma_f32_16x16x32_bf16 %0, a[72:75], a[72:75], %0" : "+v"(gr) :  : ); SB();
  asm volatile("ds_read_b64_tr_b16 v[192:193], %0 offset:0" :  : "v"(aW[1][0][2]) : "memory"); SB();
  asm volatile("v_mfma_f32_16x16x32_bf16 %0, v[172:175], a[56:59], %0" : "+v"(a2[0][0]) :  : ); SB();
  asm volatile("ds_read_b64_tr_b16 v[194:195], %0 offset:768" :  : "v"(aW[1][0][2]) : "memory"); SB();
  asm volatile("v_mfma_f32_16x16x32_bf16 %0, v[172:175], a[60:63], %0" : "+v"(a2[1][0]) :  : ); SB();
  asm volatile("ds_read_b128 a[96:99], %0 offset:32768" :  : "v"(aX[0][0]) : "memory"); SB();
  asm volatile("v_mfma_f32_16x16x32_bf16 %0, v[176:179], a[56:59], %0" : "+v"(a2[0][1]) :  : ); SB();
  asm volatile("ds_read_b128 a[100:103], %0 offset:32768" :  : "v"(aX[0][1]) : "memory"); SB();
  asm volatile("v_mfma_f32_16x16x32_bf16 %0, v[176:179], a[60:63], %0" : "+v"(a2[1][1]) :  : ); SB();
  asm volatile("ds_read_b128 a[104:107], %0 offset:32768" :  : "v"(aX[1][0]) : "memory"); SB();
  asm volatile("v_mfma_f32_16x16x32_bf16 %0, v[180:183], a[56:59], %0" : "+v"(a2[0][2]) :  : ); SB();
  asm volatile("ds_read_b128 a[108:111], %0 offset:32768" :  : "v"(aX[1][1]) : "memory"); SB();
  asm volatile("v_mfma_f32_16x16x32_bf16 %0, v[180:183], a[60:63], %0" : "+v"(a2[1][2]) :  : ); SB();
  asm volatile("ds_read_b128 a[112:115], %0 offset:40960" :  : "v"(aX[0][0]) : "memory"); SB();
  asm volatile("v_mfma_f32_16x16x32_bf16 %0, %1, a[80:83], %0" : "+v"(rs) : "v"(ones) : ); SB();
  asm volatile("ds_read_b128 a[116:119], %0 offset:40960" :  : "v"(aX[0][1]) : "memory"); SB();
  asm volatile("v_mfma_f32_16x16x32_bf16 %0, a[80:83], a[80:83], %0" : "+v"(gr) :  : ); SB();
  asm volatile("ds_read_b128 a[120:123], %0 offset:40960" :  : "v"(aX[1][0]) : "memory"); SB();
  asm volatile("v_mfma_f32_16x16x32_bf16 %0, %1, a[88:91], %0" : "+v"(rs) : "v"(ones) : ); SB();
  asm volatile("ds_read_b64_tr_b16 v[160:161], %0 offset:0" :  : "v"(aW[1][1][0]) : "memory"); SB();
  asm volatile("buffer_load_dwordx4 %0, %1, %2 offen lds" :  : "v"(voB[0]), "s"(rW), "s"(kw_[7]) : "memory"); SB();
  asm volatile("v_mfma_f32_16x16x32_bf16 %0, a[88:91], a[88:91], %0" : "+v"(gr) :  : ); SB();
  asm volatile("ds_read_b64_tr_b16 v[162:163], %0 offset:768" :  : "v"(aW[1][1][0]) : "memory"); SB();
  asm volatile("s_waitcnt lgkmcnt(9)\n\tv_mfma_f32_16x16x32_bf16 %0, v[184:187], a[64:67], %0" : "+v"(a2[0][0]) :  : ); SB();
  asm volatile("ds_read_b64_tr_b16 v[164:165], %0 offset:0" :  : "v"(aW[1][1][1]) : "memory"); SB();
  asm volatile("v_mfma_f32_16x16x32_bf16 %0, v[184:187], a[68:71], %0" : "+v"(a2[1][0]) :  : ); SB();
  asm volatile("ds_read_b64_tr_b16 v[166:167], %0 offset:768" :  : "v"(aW[1][1][1]) : "memory"); SB();
  asm volatile("buffer_load_dwordx4 %0, %1, %2 offen offset:1024 lds" :  : "v"(voB[1]), "s"(rW), "s"(kw_[7]) : "memory"); SB();
  asm volatile("v_mfma_f32_16x16x32_bf16 %0, v[184:187], %1, %0" : "+v"(c2[0]) : "v"(ones) : ); SB();
  asm volatile("ds_read_b64_tr_b16 v[168:169], %0 offset:0" :  : "v"(aW[1][1][2]) : "memory"); SB();
  asm volatile("v_mfma_f32_16x16x32_bf16 %0, v[188:191], a[64:67], %0" : "+v"(a2[0][1]) :  : ); SB();
  asm volatile("ds_read_b64_tr_b16 v[170:171], %0 offset:768" :  : "v"(aW[1][1][2]) : "memory"); SB();
  asm volatile("v_mfma_f32_16x16x32_bf16 %0, v[188:191], a[68:71], %0" : "+v"(a2[1][1]) :  : ); SB();
  asm volatile("ds_read_b128 a[124:127], %0 offset:40960" :  : "v"(aX[1][1]) : "memory"); SB();
  asm volatile("v_mfma_f32_16x16x32_bf16 %0, v[188:191], %1, %0" : "+v"(c2[1]) : "v"(ones) : ); SB();
  asm volatile("buffer_load_dwordx4 %0, %1, %2 offen offset:2048 lds" :  : "v"(voB[2]), "s"(rW), "s"(kw_[7]) : "memory"); SB();
  asm volatile("v_mfma_f32_16x16x32_bf16 %0, v[192:195], a[64:67], %0" : "+v"(a2[0][2]) :  : ); SB();
  asm volatile("s_add_u32 m0, %0, 0" :  : "s"(mW) : "memory", "scc"); SB();
  asm volatile("v_mfma_f32_16x16x32_bf16 %0, v[192:195], a[68:71], %0" : "+v"(a2[1][2]) :  : ); SB();
  asm volatile("buffer_load_dwordx4 %0, %1, %2 offen lds" :  : "v"(voB[0]), "s"(rW), "s"(kw_[8]) : "memory"); SB();
  asm volatile("v_mfma_f32_16x16x32_bf16 %0, v[192:195], %1, %0" : "+v"(c2[2]) : "v"(ones) : ); SB();
  asm volatile("buffer_load_dwordx4 %0, %1, %2 offen offset:1024 lds" :  : "v"(voB[1]), "s"(rW), "s"(kw_[8]) : "memory"); SB();
  asm volatile("buffer_load_dwordx4 %0, %1, %2 offen offset:2048 lds" :  : "v"(voB[2]), "s"(rW), "s"(kw_[8]) : "memory"); SB();
  asm volatile("s_add_u32 m0, %0, 12288" :  : "s"(mW) : "memory", "scc"); SB();
  asm volatile("s_nop 0\n\tbuffer_load_dwordx4 %0, %1, %2 offen lds" :  : "v"(voB[0]), "s"(rW), "s"(kw_[9]) : "memory"); SB();
  asm volatile("s_waitcnt vmcnt(10)\n\ts_barrier" :  :  : "memory"); SB();
  asm volatile("ds_read_b64_tr_b16 v[172:173], %0 offset:12288" :  : "v"(aW[1][0][0]) : "memory"); SB();
  asm volatile("s_waitcnt lgkmcnt(1)\n\tv_mfma_f32_16x16x32_bf16 %0, %1, a[96:99], %0" : "+v"(rs) : "v"(ones) : ); SB();
  asm volatile("ds_read_b64_tr_b16 v[174:175], %0 offset:13056" :  : "v"(aW[1][0][0]) : "memory"); SB();
  asm volatile("v_mfma_f32_16x16x32_bf16 %0, a[96:99], a[96:99], %0" : "+v"(gr) :  : ); SB();
  asm volatile("ds_read_b64_tr_b16 v[176:177], %0 offset:12288" :  : "v"(aW[1][0][1]) : "memory"); SB();
  asm volatile("v_mfma_f32_16x16x32_bf16 %0, %1, a[104:107], %0" : "+v"(rs) : "v"(ones) : ); SB();
  asm volatile("ds_read_b64_tr_b16 v[178:179], %0 offset:13056" :  : "v"(aW[1][0][1]) : "memory"); SB();
  asm volatile("v_mfma_f32_16x16x32_bf16 %0, a[104:107], a[104:107], %0" : "+v"(gr) :  : ); SB();
  asm volatile("ds_read_b64_tr_b16 v[180:181], %0 offset:12288" :  : "v"(aW[1][0][2]) : "memory"); SB();
  asm volatile("v_mfma_f32_16x16x32_bf16 %0, v[160:163], a[72:75], %0" : "+v"(a2[0][0]) :  : ); SB();
  asm volatile("ds_read_b64_tr_b16 v[182:183], %0 offset:13056" :  : "v"(aW[1][0][2]) : "memory"); SB();
  asm volatile("v_mfma_f32_16x16x32_bf16 %0, v[160:163], a[76:79], %0" : "+v"(a2[1][0]) :  : ); SB();
  asm volatile("ds_read_b128 a[128:131], %0 offset:49152" :  : "v"(aX[0][0]) : "memory"); SB();
  asm volatile("v_mfma_f32_16x16x32_bf16 %0, v[164:167], a[72:75], %0" : "+v"(a2[0][1]) :  : ); SB();
  asm volatile("ds_read_b128 a[132:135], %0 offset:49152" :  : "v"(aX[0][1]) : "memory"); SB();
  asm volatile("v_mfma_f32_16x16x32_bf16 %0, v[164:167], a[76:79], %0" : "+v"(a2[1][1]) :  : ); SB();
  asm volatile("ds_read_b128 a[136:139], %0 offset:49152" :  : "v"(aX[1][0]) : "memory"); SB();
  asm volatile("v_mfma_f32_16x16x32_bf16 %0, v[168:171], a[72:75], %0" : "+v"(a2[0][2]) :  : ); SB();
  asm volatile("ds_read_b128 a[140:143], %0 offset:49152" :  : "v"(aX[1][1]) : "memory"); SB();
  asm volatile("v_mfma_f32_16x16x32_bf16 %0, v[168:171], a[76:79], %0" : "+v"(a2[1][2]) :  : ); SB();
  asm volatile("ds_read_b128 a[144:147], %0 offset:57344" :  : "v"(aX[0][0]) : "memory"); SB();
  asm volatile("v_mfma_f32_16x16x32_bf16 %0, %1, a[112:115], %0" : "+v"(rs) : "v"(ones) : ); SB();
  asm volatile("buffer_load_dwordx4 %0, %1, %2 offen offset:1024 lds" :  : "v"(voB[1]), "s"(rW), "s"(kw_[9]) : "memory"); SB();
  asm volatile("v_mfma_f32_16x16x32_bf16 %0, a[112:115], a[112:115], %0" : "+v"(gr) :  : ); SB();
  asm volatile("ds_read_b128 a[148:151], %0 offset:57344" :  : "v"(aX[0][1]) : "memory"); SB();
  asm volatile("v_mfma_f32_16x16x32_bf16 %0, %1, a[120:123], %0" : "+v"(rs) : "v"(ones) : ); SB();
  asm volatile("ds_read_b64_tr_b16 v[184:185], %0 offset:12288" :  : "v"(aW[1][1][0]) : "memory"); SB();
  asm volatile("v_mfma_f32_16x16x32_bf16 %0, a[120:123], a[120:123], %0" : "+v"(gr) :  : ); SB();
  asm volatile("ds_read_b64_tr_b16 v[186:187], %0 offset:13056" :  : "v"(aW[1][1][0]) : "memory"); SB();
  asm volatile("s_waitcnt lgkmcnt(8)\n\tv_mfma_f32_16x16x32_bf16 %0, v[172:175], a[80:83], %0" : "+v"(a2[0][0]) :  : ); SB();
  asm volatile("ds_read_b64_tr_b16 v[188:189], %0 offset:12288" :  : "v"(aW[1][1][1]) : "memory"); SB();
  asm volatile("buffer_load_dwordx4 %0, %1, %2 offen offset:2048 lds" :  : "v"(voB[2]), "s"(rW), "s"(kw_[9]) : "memory"); SB();
  asm volatile("v_mfma_f32_16x16x32_bf16 %0, v[172:175], a[84:87], %0" : "+v"(a2[1][0]) :  : ); SB();
  asm volatile("ds_read_b64_tr_b16 v[190:191], %0 offset:13056" :  : "v"(aW[1][1][1]) : "memory"); SB();
  asm volatile("v_mfma_f32_16x16x32_bf16 %0, v[172:175], %1, %0" : "+v"(c2[0]) : "v"(ones) : ); SB();
  asm volatile("ds_read_b64_tr_b16 v[192:193], %0 offset:12288" :  : "v"(aW[1][1][2]) : "memory"); SB();
  asm volatile("v_mfma_f32_16x16x32_bf16 %0, v[176:179], a[80:83], %0" : "+v"(a2[0][1]) :  : ); SB();
  asm volatile("ds_read_b64_tr_b16 v[194:195], %0 offset:13056" :  : "v"(aW[1][1][2]) : "memory"); SB();
  asm volatile("v_mfma_f32_16x16x32_bf16 %0, v[176:179], a[84:87], %0" : "+v"(a2[1][1]) :  : ); SB();
  asm volatile("ds_read_b128 a[152:155], %0 offset:57344" :  : "v"(aX[1][0]) : "memory"); SB();
  asm volatile("v_mfma_f32_16x16x32_bf16 %0, v[176:179], %1, %0" : "+v"(c2[1]) : "v"(ones) : ); SB();
  asm volatile("ds_read_b128 a[156:159], %0 offset:57344" :  : "v"(aX[1][1]) : "memory"); SB();
  asm volatile("v_mfma_f32_16x16x32_bf16 %0, v[180:183], a[80:83], %0" : "+v"(a2[0][2]) :  : ); SB();
  asm volatile("v_cmp_le_u32 vcc, %2, %1\n\ts_mov_b64 exec, vcc\n\tds_read_b64 %0, %1\n\ts_mov_b64 exec, -1" : "+v"(xv[0]) : "v"(aV[0]), "s"(lb + 131072) : "memory", "vcc"); SB();
  asm volatile("v_mfma_f32_16x16x32_bf16 %0, v[180:183], a[84:87], %0" : "+v"(a2[1][2]) :  : ); SB();
  asm volatile("v_cmp_le_u32 vcc, %2, %1\n\ts_mov_b64 exec, vcc\n\tds_read_b64 %0, %1\n\ts_mov_b64 exec, -1" : "+v"(xv[1]) : "v"(aV[1]), "s"(lb + 131072) : "memory", "vcc"); SB();
  asm volatile("v_mfma_f32_16x16x32_bf16 %0, v[180:183], %1, %0" : "+v"(c2[2]) : "v"(ones) : ); SB();
  asm volatile("s_waitcnt vmcnt(9)\n\ts_barrier" :  :  : "memory"); SB();
  asm volatile("ds_read_b64_tr_b16 v[160:161], %0 offset:24576" :  : "v"(aW[1][0][0]) : "memory"); SB();
  asm volatile("s_waitcnt lgkmcnt(6)\n\tv_mfma_f32_16x16x32_bf16 %0, %1, a[128:131], %0" : "+v"(rs) : "v"(ones) : ); SB();
  asm volatile("ds_read_b64_tr_b16 v[162:163], %0 offset:25344" :  : "v"(aW[1][0][0]) : "memory"); SB();
  asm volatile("v_mfma_f32_16x16x32_bf16 %0, a[128:131], a[128:131], %0" : "+v"(gr) :  : ); SB();
  asm volatile("ds_read_b64_tr_b16 v[164:165], %0 offset:24576" :  : "v"(aW[1][0][1]) : "memory"); SB();
  asm volatile("v_mfma_f32_16x16x32_bf16 %0, %1, a[136:139], %0" : "+v"(rs) : "v"(ones) : ); SB();
  asm volatile("ds_read_b64_tr_b16 v[166:167], %0 offset:25344" :  : "v"(aW[1][0][1]) : "memory"); SB();
  asm volatile("v_mfma_f32_16x16x32_bf16 %0, a[136:139], a[136:139], %0" : "+v"(gr) :  : ); SB();
  asm volatile("ds_read_b64_tr_b16 v[168:169], %0 offset:24576" :  : "v"(aW[1][0][2]) : "memory"); SB();
  asm volatile("v_mfma_f32_16x16x32_bf16 %0, v[184:187], a[88:91], %0" : "+v"(a2[0][0]) :  : ); SB();
  asm volatile("ds_read_b64_tr_b16 v[170:171], %0 offset:25344" :  : "v"(aW[1][0][2]) : "memory"); SB();
  asm volatile("v_mfma_f32_16x16x32_bf16 %0, v[184:187], a[92:95], %0" : "+v"(a2[1][0]) :  : ); SB();
  asm volatile("v_cmp_le_u32 vcc, %2, %1\n\ts_mov_b64 exec, vcc\n\tds_read_b64 %0, %1\n\ts_mov_b64 exec, -1" : "+v"(xv[2]) : "v"(aV[2]), "s"(lb + 131072) : "memory", "vcc"); SB();
  asm volatile("v_mfma_f32_16x16x32_bf16 %0, v[188:191], a[88:91], %0" : "+v"(a2[0][1]) :  : ); SB();
  asm volatile("v_cmp_le_u32 vcc, %2, %1\n\ts_mov_b64 exec, vcc\n\tds_read_b64 %0, %1\n\ts_mov_b64 exec, -1" : "+v"(xv[3]) : "v"(aV[3]), "s"(lb + 131072) : "memory", "vcc"); SB();
  asm volatile("v_mfma_f32_16x16x32_bf16 %0, v[188:191], a[92:95], %0" : "+v"(a2[1][1]) :  : ); SB();
  asm volatile("v_cmp_le_u32 vcc, %2, %1\n\ts_mov_b64 exec, vcc\n\tds_read_b64 %0, %1\n\ts_mov_b64 exec, -1" : "+v"(xv[4]) : "v"(aV[4]), "s"(lb + 131072) : "memory", "vcc"); SB();
  asm volatile("s_waitcnt lgkmcnt(7)\n\tv_mfma_f32_16x16x32_bf16 %0, v[192:195], a[88:91], %0" : "+v"(a2[0][2]) :  : ); SB();
  asm volatile("ds_read_b64_tr_b16 v[172:173], %0 offset:24576" :  : "v"(aW[1][1][0]) : "memory"); SB();
  asm volatile("v_mfma_f32_16x16x32_bf16 %0, v[192:195], a[92:95], %0" : "+v"(a2[1][2]) :  : ); SB();
  asm volatile("ds_read_b64_tr_b16 v[174:175], %0 offset:25344" :  : "v"(aW[1][1][0]) : "memory"); SB();
  asm volatile("v_mfma_f32_16x16x32_bf16 %0, %1, a[144:147], %0" : "+v"(rs) : "v"(ones) : ); SB();
  asm volatile("ds_read_b64_tr_b16 v[176:177], %0 offset:24576" :  : "v"(aW[1][1][1]) : "memory"); SB();
  asm volatile("buffer_load_dword v242, off, %0, %1" :  : "s"(rF), "s"(hpo[0]) : "memory"); SB();
  asm volatile("v_mfma_f32_16x16x32_bf16 %0, a[144:147], a[144:147], %0" : "+v"(gr) :  : ); SB();
  asm volatile("ds_read_b64_tr_b16 v[178:179], %0 offset:25344" :  : "v"(aW[1][1][1]) : "memory"); SB();
  asm volatile("s_add_u32 m0, %0, 98304" :  : "s"(mA) : "memory", "scc"); SB();
  asm volatile("v_mfma_f32_16x16x32_bf16 %0, %1, a[152:155], %0" : "+v"(rs) : "v"(ones) : ); SB();
  asm volatile("ds_read_b64_tr_b16 v[180:181], %0 offset:24576" :  : "v"(aW[1][1][2]) : "memory"); SB();
  asm volatile("buffer_load_dwordx4 %0, %1, %2 offen lds" :  : "v"(voK[0]), "s"(rK), "s"(0u) : "memory"); SB();
  asm volatile("v_mfma_f32_16x16x32_bf16 %0, a[152:155], a[152:155], %0" : "+v"(gr) :  : ); SB();
  asm volatile("ds_read_b64_tr_b16 v[182:183], %0 offset:25344" :  : "v"(aW[1][1][2]) : "memory"); SB();
  asm volatile("v_mfma_f32_16x16x32_bf16 %0, v[160:163], a[96:99], %0" : "+v"(a2[0][0]) :  : ); SB();
  asm volatile("buffer_load_dwordx4 %0, %1, %2 offen offset:1024 lds" :  : "v"(voK[1]), "s"(rK), "s"(0u) : "memory"); SB();
  asm volatile("buffer_load_dwordx4 %0, %1, %2 offen offset:2048 lds" :  : "v"(voK[2]), "s"(rK), "s"(0u) : "memory"); SB();
  asm volatile("s_waitcnt vmcnt(10)\n\ts_barrier" :  :  : "memory"); SB();
  asm volatile("ds_read_b64_tr_b16 v[184:185], %0 offset:36864" :  : "v"(aW[1][0][0]) : "memory"); SB();
  asm volatile("v_mfma_f32_16x16x32_bf16 %0, v[160:163], a[100:103], %0" : "+v"(a2[1][0]) :  : ); SB();
  asm volatile("ds_read_b64_tr_b16 v[186:187], %0 offset:37632" :  : "v"(aW[1][0][0]) : "memory"); SB();
  asm volatile("v_mfma_f32_16x16x32_bf16 %0, v[160:163], %1, %0" : "+v"(c2[0]) : "v"(ones) : ); SB();
  asm volatile("ds_read_b64_tr_b16 v[188:189], %0 offset:36864" :  : "v"(aW[1][0][1]) : "memory"); SB();
  asm volatile("s_waitcnt lgkmcnt(3)\n\tv_mfma_f32_16x16x32_bf16 %0, v[164:167], a[96:99], %0" : "+v"(a2[0][1]) :  : ); SB();
  asm volatile("ds_read_b64_tr_b16 v[190:191], %0 offset:37632" :  : "v"(aW[1][0][1]) : "memory"); SB();
  asm volatile("global_load_ushort v245, %0, %1 offset:0" :  : "v"(vD), "s"(ssm_D) : "memory"); SB();
  asm volatile("v_mfma_f32_16x16x32_bf16 %0, v[164:167], a[100:103], %0" : "+v"(a2[1][1]) :  : ); SB();
  asm volatile("ds_read_b64_tr_b16 v[192:193], %0 offset:36864" :  : "v"(aW[1][0][2]) : "memory"); SB();
  asm volatile("s_mov_b32 exec_lo, 0xff\n\ts_mov_b32 exec_hi, 0\n\tbuffer_load_dwordx4 %0, %1, %2 offen offset:3072 lds\n\ts_mov_b64 exec, -1" :  : "v"(voK[3]), "s"(rK), "s"(0u) : "memory"); SB();
  asm volatile("v_mfma_f32_16x16x32_bf16 %0, v[164:167], %1, %0" : "+v"(c2[1]) : "v"(ones) : ); SB();
  asm volatile("ds_read_b64_tr_b16 v[194:195], %0 offset:37632" :  : "v"(aW[1][0][2]) : "memory"); SB();
  asm volatile("v_mfma_f32_16x16x32_bf16 %0, v[168:171], a[96:99], %0" : "+v"(a2[0][2]) :  : ); SB();
  asm volatile("v_mfma_f32_16x16x32_bf16 %0, v[168:171], a[100:103], %0" : "+v"(a2[1][2]) :  : ); SB();
  asm volatile("s_mov_b32 exec_lo, -1\n\ts_mov_b32 exec_hi, 0xff\n\tbuffer_load_dwordx4 %0, %1, %2 offen offset:3328 lds\n\ts_mov_b64 exec, -1" :  : "v"(voH), "s"(rS), "s"(0u) : "memory"); SB();
  asm volatile("v_mfma_f32_16x16x32_bf16 %0, v[168:171], %1, %0" : "+v"(c2[2]) : "v"(ones) : ); SB();
  asm volatile("v_mfma_f32_16x16x32_bf16 %0, v[172:175], a[104:107], %0" : "+v"(a2[0][0]) :  : ); SB();
  asm volatile("ds_read_b64_tr_b16 v[160:161], %0 offset:36864" :  : "v"(aW[1][1][0]) : "memory"); SB();
  asm volatile("s_add_u32 m0, %0, 102400" :  : "s"(mA) : "memory", "scc"); SB();
  asm volatile("v_mfma_f32_16x16x32_bf16 %0, v[172:175], a[108:111], %0" : "+v"(a2[1][0]) :  : ); SB();
  asm volatile("s_mov_b32 exec_lo, 0x10001\n\ts_mov_b32 exec_hi, 0x10001\n\tds_write_b128 %0, %1 offset:0\n\ts_mov_b64 exec, -1" :  : "v"(ca), "v"(c2[0]) : "memory"); SB();
  asm volatile("ds_read_b64_tr_b16 v[162:163], %0 offset:37632" :  : "v"(aW[1][1][0]) : "memory"); SB();
  asm volatile("buffer_load_dwordx4 %0, %1, %2 offen lds" :  : "v"(voV[0]), "s"(rV), "s"(0u) : "memory"); SB();
  asm volatile("v_mfma_f32_16x16x32_bf16 %0, v[176:179], a[104:107], %0" : "+v"(a2[0][1]) :  : ); SB();
  asm volatile("ds_read_b64_tr_b16 v[164:165], %0 offset:36864" :  : "v"(aW[1][1][1]) : "memory"); SB();
  asm volatile("v_cndmask_b32 %0, %1, %2, %3" : "=v"(xt[0]) : "v"(gr[0]), "v"(gr[1]), "s"(0xAAAAAAAAAAAAAAAAull) : ); SB();
  asm volatile("v_mfma_f32_16x16x32_bf16 %0, v[176:179], a[108:111], %0" : "+v"(a2[1][1]) :  : ); SB();
  asm volatile("s_mov_b32 exec_lo, 0x10001\n\ts_mov_b32 exec_hi, 0x10001\n\tds_write_b128 %0, %1 offset:64\n\ts_mov_b64 exec, -1" :  : "v"(ca), "v"(c2[1]) : "memory"); SB();
  asm volatile("ds_read_b64_tr_b16 v[166:167], %0 offset:37632" :  : "v"(aW[1][1][1]) : "memory"); SB();
  asm volatile("buffer_load_dword v243, off, %0, %1" :  : "s"(rF), "s"(hpo[1]) : "memory"); SB();
  asm volatile("v_mfma_f32_16x16x32_bf16 %0, v[180:183], a[104:107], %0" : "+v"(a2[0][2]) :  : ); SB();
  asm volatile("ds_read_b64_tr_b16 v[168:169], %0 offset:36864" :  : "v"(aW[1][1][2]) : "memory"); SB();
  asm volatile("buffer_load_dwordx4 %0, %1, %2 offen offset:1024 lds" :  : "v"(voV[1]), "s"(rV), "s"(0u) : "memory"); SB();
  asm volatile("v_mfma_f32_16x16x32_bf16 %0, v[180:183], a[108:111], %0" : "+v"(a2[1][2]) :  : ); SB();
  asm volatile("s_mov_b32 exec_lo, 0x10001\n\ts_mov_b32 exec_hi, 0x10001\n\tds_write_b128 %0, %1 offset:128\n\ts_mov_b64 exec, -1" :  : "v"(ca), "v"(c2[2]) : "memory"); SB();
  asm volatile("ds_read_b64_tr_b16 v[170:171], %0 offset:37632" :  : "v"(aW[1][1][2]) : "memory"); SB();
  asm volatile("s_waitcnt lgkmcnt(5)\n\tv_mfma_f32_16x16x32_bf16 %0, v[184:187], a[112:115], %0" : "+v"(a2[0][0]) :  : ); SB();
  asm volatile("s_mov_b32 exec_lo, -1\n\ts_mov_b32 exec_hi, 0\n\tbuffer_load_dwordx4 %0, %1, %2 offen offset:2048 lds\n\ts_mov_b64 exec, -1" :  : "v"(voV[2]), "s"(rV), "s"(0u) : "memory"); SB();
  asm volatile("v_cndmask_b32 %0, %1, %2, %3" : "=v"(xt[1]) : "v"(gr[2]), "v"(gr[3]), "s"(0xAAAAAAAAAAAAAAAAull) : ); SB();
  asm volatile("v_mfma_f32_16x16x32_bf16 %0, v[184:187], a[116:119], %0" : "+v"(a2[1][0]) :  : ); SB();
  asm volatile("buffer_load_dwordx4 %0, %1, %2 offen offset:3072 lds" :  : "v"(voO), "s"(rO), "s"(0u) : "memory"); SB();
  asm volatile("v_mul_f32 %0, 0x3acccccd, %1" : "=v"(xs[0][0]) : "v"(rs[0]) : ); SB();
  asm volatile("v_mfma_f32_16x16x32_bf16 %0, v[184:187], %1, %0" : "+v"(c2[0]) : "v"(ones) : ); SB();
  asm volatile("global_load_ushort v246, %0, %1 offset:2" :  : "v"(vD), "s"(ssm_D) : "memory"); SB();
  asm volatile("v_cndmask_b32 %0, %0, %1, %2" : "+v"(xt[0]) : "v"(xt[1]), "s"(0xCCCCCCCCCCCCCCCCull) : ); SB();
  asm volatile("v_mfma_f32_16x16x32_bf16 %0, v[188:191], a[112:115], %0" : "+v"(a2[0][1]) :  : ); SB();
  asm volatile("s_waitcnt vmcnt(16)\n\ts_barrier" :  :  : "memory"); SB();
  asm volatile("ds_read_b64_tr_b16 v[172:173], %0 offset:0" :  : "v"(aW[0][0][0]) : "memory"); SB();
  asm volatile("v_mul_f32 %0, 0x3a83126f, %1" : "=v"(xt[1]) : "v"(rs[0]) : ); SB();
  asm volatile("v_mfma_f32_16x16x32_bf16 %0, v[188:191], a[116:119], %0" : "+v"(a2[1][1]) :  : ); SB();
  asm volatile("ds_read_b64_tr_b16 v[174:175], %0 offset:768" :  : "v"(aW[0][0][0]) : "memory"); SB();
  asm volatile("v_fma_f32 %0, -%1, %2, %3" : "=v"(xs[0][1]) : "v"(rs[0]), "v"(xs[0][0]), "v"(xt[0]) : ); SB();
  asm volatile("v_mfma_f32_16x16x32_bf16 %0, v[188:191], %1, %0" : "+v"(c2[1]) : "v"(ones) : ); SB();
  asm volatile("ds_read_b64_tr_b16 v[176:177], %0 offset:0" :  : "v"(aW[0][0][1]) : "memory"); SB();
  asm volatile("v_mul_f32 %0, %0, %1" : "+v"(xt[1]) : "v"(xs[0][0]) : ); SB();
  asm volatile("v_mfma_f32_16x16x32_bf16 %0, v[192:195], a[112:115], %0" : "+v"(a2[0][2]) :  : ); SB();
  asm volatile("ds_read_b64_tr_b16 v[178:179], %0 offset:768" :  : "v"(aW[0][0][1]) : "memory"); SB();
  asm volatile("v_mfma_f32_16x16x32_bf16 %0, v[192:195], a[116:119], %0" : "+v"(a2[1][2]) :  : ); SB();
  asm volatile("ds_read_b64_tr_b16 v[180:181], %0 offset:0" :  : "v"(aW[0][0][2]) : "memory"); SB();
  asm volatile("v_mfma_f32_16x16x32_bf16 %0, v[192:195], %1, %0" : "+v"(c2[2]) : "v"(ones) : ); SB();
  asm volatile("ds_read_b64_tr_b16 v[182:183], %0 offset:768" :  : "v"(aW[0][0][2]) : "memory"); SB();
  asm volatile("v_mfma_f32_16x16x32_bf16 %0, v[160:163], a[120:123], %0" : "+v"(a2[0][0]) :  : ); SB();
  if (__builtin_expect((__builtin_amdgcn_ballot_w64(!(xs[0][1] >= xt[1])) & 0xF0000F0000F0000Full) != 0, 0)) {
    const f32x2 mm = {xs[0][0], xs[0][0]};
    f32x2 q2 = {0.f, 0.f};
    const u32x4* xp = (const u32x4*)(x + (size_t)(rb * 64 + w * 16 + l15) * 1280 + g4 * 320);
#pragma unroll 1
    for (int c = 0; c < 20; ++c) {
      u32x4 xq;
      asm volatile("global_load_dwordx4 %0, %1, off\n\ts_waitcnt vmcnt(0)" : "=&v"(xq) : "v"(xp + c) : "memory");
#pragma unroll
      for (int e = 0; e < 4; ++e) { const f32x2 d = unpk(xq[e]) - mm; q2 = __builtin_elementwise_fma(d, d, q2); }
    }
    xs[0][1] = red32(red16(q2[0] + q2[1]));
  }
  SB();
  asm volatile("v_mfma_f32_16x16x32_bf16 %0, v[160:163], a[124:127], %0" : "+v"(a2[1][0]) :  : ); SB();
  asm volatile("buffer_load_dword v244, off, %0, %1" :  : "s"(rF), "s"(hpo[2]) : "memory"); SB();
  asm volatile("v_max_f32 %0, 0, %0" : "+v"(xs[0][1]) :  : ); SB();
  asm volatile("v_mfma_f32_16x16x32_bf16 %0, v[160:163], %1, %0" : "+v"(c2[0]) : "v"(ones) : ); SB();
  asm volatile("buffer_load_dwordx4 v[228:231], %0, %1, %2 offen" :  : "v"(vF), "s"(rF), "s"(0u) : "memory"); SB();
  asm volatile("s_waitcnt lgkmcnt(5)\n\tv_mfma_f32_16x16x32_bf16 %0, v[164:167], a[120:123], %0" : "+v"(a2[0][1]) :  : ); SB();
  asm volatile("ds_read_b64_tr_b16 v[184:185], %0 offset:0" :  : "v"(aW[0][1][0]) : "memory"); SB();
  asm volatile("v_mul_f32 %0, 0x3acccccd, %0" : "+v"(xs[0][1]) :  : ); SB();
  asm volatile("v_mfma_f32_16x16x32_bf16 %0, v[164:167], a[124:127], %0" : "+v"(a2[1][1]) :  : ); SB();
  asm volatile("ds_read_b64_tr_b16 v[186:187], %0 offset:768" :  : "v"(aW[0][1][0]) : "memory"); SB();
  asm volatile("v_cvt_pk_bf16_f32 %0, %0, %0" : "+v"(xs[0][1]) :  : ); SB();
  asm volatile("v_mfma_f32_16x16x32_bf16 %0, v[164:167], %1, %0" : "+v"(c2[1]) : "v"(ones) : ); SB();
  asm volatile("ds_read_b64_tr_b16 v[188:189], %0 offset:0" :  : "v"(aW[0][1][1]) : "memory"); SB();
  asm volatile("v_lshlrev_b32 %0, 16, %0" : "+v"(xs[0][1]) :  : ); SB();
  asm volatile("v_mfma_f32_16x16x32_bf16 %0, v[168:171], a[120:123], %0" : "+v"(a2[0][2]) :  : ); SB();
  asm volatile("ds_read_b64_tr_b16 v[190:191], %0 offset:768" :  : "v"(aW[0][1][1]) : "memory"); SB();
  asm volatile("v_add_f32 %0, 0x3727c5ac, %0" : "+v"(xs[0][1]) :  : ); SB();
  asm volatile("v_mfma_f32_16x16x32_bf16 %0, v[168:171], a[124:127], %0" : "+v"(a2[1][2]) :  : ); SB();
  asm volatile("ds_read_b64_tr_b16 v[192:193], %0 offset:0" :  : "v"(aW[0][1][2]) : "memory"); SB();
  asm volatile("v_cvt_pk_bf16_f32 %0, %0, %0" : "+v"(xs[0][1]) :  : ); SB();
  asm volatile("v_mfma_f32_16x16x32_bf16 %0, v[168:171], %1, %0" : "+v"(c2[2]) : "v"(ones) : ); SB();
  asm volatile("ds_read_b64_tr_b16 v[194:195], %0 offset:768" :  : "v"(aW[0][1][2]) : "memory"); SB();
  asm volatile("s_waitcnt lgkmcnt(6)\n\tv_mfma_f32_16x16x32_bf16 %0, v[172:175], a[128:131], %0" : "+v"(a2[0][0]) :  : ); SB();
  asm volatile("global_load_ushort v247, %0, %1 offset:4" :  : "v"(vD2), "s"(ssm_D) : "memory"); SB();
  asm volatile("v_lshlrev_b32 %0, 16, %0" : "+v"(xs[0][1]) :  : ); SB();
  asm volatile("v_mfma_f32_16x16x32_bf16 %0, v[172:175], a[132:135], %0" : "+v"(a2[1][0]) :  : ); SB();
  asm volatile("buffer_load_dwordx4 v[232:235], %0, %1, %2 offen" :  : "v"(vF), "s"(rF), "s"(4096u) : "memory"); SB();
  asm volatile("v_mfma_f32_16x16x32_bf16 %0, v[172:175], %1, %0" : "+v"(c2[0]) : "v"(ones) : ); SB();
  asm volatile("v_sqrt_f32 %0, %0" : "+v"(xs[0][1]) :  : ); SB();
  asm volatile("v_mfma_f32_16x16x32_bf16 %0, v[176:179], a[128:131], %0" : "+v"(a2[0][1]) :  : ); SB();
  asm volatile("v_cvt_pk_bf16_f32 %0, %0, %0" : "+v"(xs[0][0]) :  : ); SB();
  asm volatile("v_lshlrev_b32 %0, 16, %0" : "+v"(xs[0][0]) :  : ); SB();
  asm volatile("v_cvt_pk_bf16_f32 %0, %0, %0" : "+v"(xs[0][1]) :  : ); SB();
  asm volatile("v_lshlrev_b32 %0, 16, %0" : "+v"(xs[0][1]) :  : ); SB();
  asm volatile("v_rcp_f32 %0, %1" : "=v"(xt[0]) : "v"(xs[0][1]) : ); SB();
  asm volatile("s_nop 0\n\tv_mul_f32 %0, 0xc0135761, %1" : "=v"(xs[0][2]) : "v"(xt[0]) : ); SB();
  asm volatile("v_mul_f32 %0, %1, %1" : "=v"(xt[1]) : "v"(xt[0]) : ); SB();
  asm volatile("v_mul_f32 %0, 0xbdd2d3e8, %0" : "+v"(xt[0]) :  : ); SB();
  asm volatile("v_mul_f32 %0, %1, %2" : "=v"(xs[0][3]) : "v"(xt[0]), "v"(xt[1]) : ); SB();
  asm volatile("s_mov_b32 exec_lo, 0xf0000f\n\ts_mov_b32 exec_hi, 0xf0000f00\n\tds_write_b128 %0, %1\n\ts_mov_b64 exec, -1" :  : "v"(xw), "v"(xs[0]) : "memory"); SB();
  asm volatile("" : "+v"(xv[0]), "+v"(xv[1]), "+v"(xv[2]), "+v"(xv[3]), "+v"(xv[4]) :  : "memory"); SB();
  asm volatile("s_waitcnt vmcnt(17) lgkmcnt(0)\n\ts_barrier" :  :  : "memory"); SB();
  asm volatile("ds_read_b64_tr_b16 v[160:161], %0 offset:12288" :  : "v"(aW[0][0][0]) : "memory"); SB();
  asm volatile("v_mfma_f32_16x16x32_bf16 %0, v[176:179], a[132:135], %0" : "+v"(a2[1][1]) :  : ); SB();
  asm volatile("ds_read_b64_tr_b16 v[162:163], %0 offset:13056" :  : "v"(aW[0][0][0]) : "memory"); SB();
  asm volatile("v_mfma_f32_16x16x32_bf16 %0, v[176:179], %1, %0" : "+v"(c2[1]) : "v"(ones) : ); SB();
  asm volatile("ds_read_b64_tr_b16 v[164:165], %0 offset:12288" :  : "v"(aW[0][0][1]) : "memory"); SB();
  asm volatile("v_mfma_f32_16x16x32_bf16 %0, v[180:183], a[128:131], %0" : "+v"(a2[0][2]) :  : ); SB();
  asm volatile("ds_read_b64_tr_b16 v[166:167], %0 offset:13056" :  : "v"(aW[0][0][1]) : "memory"); SB();
  asm volatile("v_mfma_f32_16x16x32_bf16 %0, v[180:183], a[132:135], %0" : "+v"(a2[1][2]) :  : ); SB();
  asm volatile("ds_read_b64_tr_b16 v[168:169], %0 offset:12288" :  : "v"(aW[0][0][2]) : "memory"); SB();
  asm volatile("v_mfma_f32_16x16x32_bf16 %0, v[180:183], %1, %0" : "+v"(c2[2]) : "v"(ones) : ); SB();
  asm volatile("ds_read_b64_tr_b16 v[170:171], %0 offset:13056" :  : "v"(aW[0][0][2]) : "memory"); SB();
  asm volatile("v_mfma_f32_16x16x32_bf16 %0, v[184:187], a[136:139], %0" : "+v"(a2[0][0]) :  : ); SB();
  asm volatile("ds_read_b128 %0, %1 offset:0" : "=&v"(xs[0]) : "v"(xw) : "memory"); SB();
  asm volatile("v_mfma_f32_16x16x32_bf16 %0, v[184:187], a[140:143], %0" : "+v"(a2[1][0]) :  : ); SB();
  asm volatile("ds_read_b128 %0, %1 offset:0" : "=&v"(xs[1]) : "v"(xr_) : "memory"); SB();
  asm volatile("v_mfma_f32_16x16x32_bf16 %0, v[184:187], %1, %0" : "+v"(c2[0]) : "v"(ones) : ); SB();
  asm volatile("ds_read_b128 %0, %1 offset:0" : "=&v"(co[0]) : "v"(cr) : "memory"); SB();
  asm volatile("v_mfma_f32_16x16x32_bf16 %0, v[188:191], a[136:139], %0" : "+v"(a2[0][1]) :  : ); SB();
  asm volatile("ds_read_b64_tr_b16 v[172:173], %0 offset:12288" :  : "v"(aW[0][1][0]) : "memory"); SB();
  asm volatile("v_mfma_f32_16x16x32_bf16 %0, v[188:191], a[140:143], %0" : "+v"(a2[1][1]) :  : ); SB();
  asm volatile("ds_read_b64_tr_b16 v[174:175], %0 offset:13056" :  : "v"(aW[0][1][0]) : "memory"); SB();
  asm volatile("v_mfma_f32_16x16x32_bf16 %0, v[188:191], %1, %0" : "+v"(c2[1]) : "v"(ones) : ); SB();
  asm volatile("ds_read_b64_tr_b16 v[176:177], %0 offset:12288" :  : "v"(aW[0][1][1]) : "memory"); SB();
  asm volatile("v_mfma_f32_16x16x32_bf16 %0, v[192:195], a[136:139], %0" : "+v"(a2[0][2]) :  : ); SB();
  asm volatile("ds_read_b64_tr_b16 v[178:179], %0 offset:13056" :  : "v"(aW[0][1][1]) : "memory"); SB();
  asm volatile("v_mfma_f32_16x16x32_bf16 %0, v[192:195], a[140:143], %0" : "+v"(a2[1][2]) :  : ); SB();
  asm volatile("ds_read_b64_tr_b16 v[180:181], %0 offset:12288" :  : "v"(aW[0][1][2]) : "memory"); SB();
  asm volatile("v_mfma_f32_16x16x32_bf16 %0, v[192:195], %1, %0" : "+v"(c2[2]) : "v"(ones) : ); SB();
  asm volatile("ds_read_b64_tr_b16 v[182:183], %0 offset:13056" :  : "v"(aW[0][1][2]) : "memory"); SB();
  asm volatile("s_waitcnt lgkmcnt(9)\n\tv_mfma_f32_16x16x32_bf16 %0, v[160:163], a[144:147], %0" : "+v"(a2[0][0]) :  : ); SB();
  asm volatile("ds_read_b128 %0, %1 offset:64" : "=&v"(co[1]) : "v"(cr) : "memory"); SB();
  asm volatile("v_mfma_f32_16x16x32_bf16 %0, v[160:163], a[148:151], %0" : "+v"(a2[1][0]) :  : ); SB();
  asm volatile("ds_read_b128 %0, %1 offset:128" : "=&v"(co[2]) : "v"(cr) : "memory"); SB();
  asm volatile("v_mfma_f32_16x16x32_bf16 %0, v[160:163], %1, %0" : "+v"(c2[0]) : "v"(ones) : ); SB();
  asm volatile("global_load_ushort v248, %0, %1" :  : "v"(vBs), "s"(b_sp) : "memory"); SB();
  asm volatile("v_mfma_f32_16x16x32_bf16 %0, v[164:167], a[144:147], %0" : "+v"(a2[0][1]) :  : ); SB();
  asm volatile("buffer_load_dwordx4 v[236:239], %0, %1, %2 offen" :  : "v"(vF), "s"(rF), "s"(c2s_) : "memory"); SB();
  asm volatile("v_mfma_f32_16x16x32_bf16 %0, v[164:167], a[148:151], %0" : "+v"(a2[1][1]) :  : ); SB();
  asm volatile("v_mfma_f32_16x16x32_bf16 %0, v[164:167], %1, %0" : "+v"(c2[1]) : "v"(ones) : ); SB();
  asm volatile("v_mfma_f32_16x16x32_bf16 %0, v[168:171], a[144:147], %0" : "+v"(a2[0][2]) :  : ); SB();
  asm volatile("s_add_u32 m0, %0, 24576" :  : "s"(mW) : "memory", "scc"); SB();
  asm volatile("v_mfma_f32_16x16x32_bf16 %0, v[168:171], a[148:151], %0" : "+v"(a2[1][2]) :  : ); SB();
  asm volatile("buffer_load_dwordx4 %0, %1, %2 offen lds" :  : "v"(voB[0]), "s"(rW1), "s"(kw_[0]) : "memory"); SB();
  asm volatile("v_mfma_f32_16x16x32_bf16 %0, v[168:171], %1, %0" : "+v"(c2[2]) : "v"(ones) : ); SB();
  asm volatile("s_waitcnt lgkmcnt(3)\n\tv_mfma_f32_16x16x32_bf16 %0, v[172:175], a[152:155], %0" : "+v"(a2[0][0]) :  : ); SB();
  asm volatile("v_mfma_f32_16x16x32_bf16 %0, v[172:175], a[156:159], %0" : "+v"(a2[1][0]) :  : ); SB();
  asm volatile("buffer_load_dwordx4 %0, %1, %2 offen offset:1024 lds" :  : "v"(voB[1]), "s"(rW1), "s"(kw_[0]) : "memory"); SB();
  asm volatile("v_mfma_f32_16x16x32_bf16 %0, v[172:175], %1, %0" : "+v"(c2[0]) : "v"(ones) : ); SB();
  asm volatile("v_mfma_f32_16x16x32_bf16 %0, v[176:179], a[152:155], %0" : "+v"(a2[0][1]) :  : ); SB();
  asm volatile("v_mfma_f32_16x16x32_bf16 %0, v[176:179], a[156:159], %0" : "+v"(a2[1][1]) :  : ); SB();
  asm volatile("buffer_load_dwordx4 %0, %1, %2 offen offset:2048 lds" :  : "v"(voB[2]), "s"(rW1), "s"(kw_[0]) : "memory"); SB();
  asm volatile("v_mfma_f32_16x16x32_bf16 %0, v[176:179], %1, %0" : "+v"(c2[1]) : "v"(ones) : ); SB();
  asm volatile("s_waitcnt lgkmcnt(0)\n\tv_mfma_f32_16x16x32_bf16 %0, v[180:183], a[152:155], %0" : "+v"(a2[0][2]) :  : ); SB();
  asm volatile("v_mfma_f32_16x16x32_bf16 %0, v[180:183], a[156:159], %0" : "+v"(a2[1][2]) :  : ); SB();
  asm volatile("v_mfma_f32_16x16x32_bf16 %0, v[180:183], %1, %0" : "+v"(c2[2]) : "v"(ones) : ); SB();
  asm volatile("s_add_u32 m0, %0, 36864" :  : "s"(mW) : "memory", "scc"); SB();
  asm volatile("s_nop 0\n\tbuffer_load_dwordx4 %0, %1, %2 offen lds" :  : "v"(voB[0]), "s"(rW1), "s"(kw_[1]) : "memory"); SB();
  ST(3);
  ST(5);
  const unsigned long long tg = ((unsigned long long)tg1 << 32) | tg0;
  asm volatile("" : "+v"(xs[0]), "+v"(xs[1]), "+v"(co[0]), "+v"(co[1]), "+v"(co[2]) :  : "memory"); SB();
  asm volatile("s_nop 3" : "+v"(a2[0][0]), "+v"(a2[0][1]), "+v"(a2[0][2]), "+v"(a2[1][0]), "+v"(a2[1][1]), "+v"(a2[1][2]), "+v"(c2[0]), "+v"(c2[1]), "+v"(c2[2]) :  : ); SB();
  for (int i = 0; i < 2; ++i) { xm[i] = xs[i][0]; xd[i] = xs[i][1]; k1[i] = xs[i][2]; k2[i] = xs[i][3]; }
  c2[0] += co[0]; c2[1] += co[1]; c2[2] += co[2];
  f32x2 ge2[2];
  auto act = [&](f32x2& t, f32x2& e, const f32x4& a, const f32x4& cs, int i, int h) {
    t = __builtin_elementwise_fma((f32x2){-xm[i], -xm[i]}, (f32x2){cs[2 * h], cs[2 * h + 1]}, (f32x2){a[2 * h], a[2 * h + 1]});
    const f32x2 u = t * __builtin_elementwise_fma(t * t, (f32x2){k2[i], k2[i]}, (f32x2){k1[i], k1[i]});
    e = (f32x2){__builtin_amdgcn_exp2f(u[0]), __builtin_amdgcn_exp2f(u[1])};
  };
  auto act2 = [&](f32x2& t, f32x2 e, int i) {
    e = __builtin_elementwise_fma(e, (f32x2){xd[i], xd[i]}, (f32x2){xd[i], xd[i]});
    t *= (f32x2){__builtin_amdgcn_rcpf(e[0]), __builtin_amdgcn_rcpf(e[1])};
  };
  f32x2 gv[2][3][2];
  float sv[2][2], pv[2];
  act(gv[0][0][0], ge2[0], a2[0][0], c2[0], 0, 0); act(gv[0][0][1], ge2[1], a2[0][0], c2[0], 0, 1);
  asm volatile("" : "+v"(gv[0][0][0]), "+v"(gv[0][0][1]), "+v"(ge2[0]), "+v"(ge2[1]));
  SB();
  asm volatile("buffer_load_dwordx4 %0, %1, %2 offen offset:1024 lds" :  : "v"(voB[1]), "s"(rW1), "s"(kw_[1]) : "memory"); SB();
  asm volatile("buffer_load_dwordx4 %0, %1, %2 offen offset:2048 lds" :  : "v"(voB[2]), "s"(rW1), "s"(kw_[1]) : "memory"); SB();
  act2(gv[0][0][0], ge2[0], 0); act2(gv[0][0][1], ge2[1], 0);
  asm volatile("" : "+v"(gv[0][0][0]), "+v"(gv[0][0][1]));
  SB();
  asm volatile("s_add_u32 m0, %0, 49152" :  : "s"(mW) : "memory", "scc"); SB();
  asm volatile("s_nop 0\n\tbuffer_load_dwordx4 %0, %1, %2 offen lds" :  : "v"(voB[0]), "s"(rW1), "s"(kw_[2]) : "memory"); SB();
  act(gv[0][1][0], ge2[0], a2[0][1], c2[1], 0, 0); act(gv[0][1][1], ge2[1], a2[0][1], c2[1], 0, 1);
  asm volatile("" : "+v"(gv[0][1][0]), "+v"(gv[0][1][1]), "+v"(ge2[0]), "+v"(ge2[1]));
  SB();
  asm volatile("buffer_load_dwordx4 %0, %1, %2 offen offset:1024 lds" :  : "v"(voB[1]), "s"(rW1), "s"(kw_[2]) : "memory"); SB();
  asm volatile("buffer_load_dwordx4 %0, %1, %2 offen offset:2048 lds" :  : "v"(voB[2]), "s"(rW1), "s"(kw_[2]) : "memory"); SB();
  act2(gv[0][1][0], ge2[0], 0); act2(gv[0][1][1], ge2[1], 0);
  asm volatile("" : "+v"(gv[0][1][0]), "+v"(gv[0][1][1]));
  SB();
  asm volatile("s_add_u32 m0, %0, 61440" :  : "s"(mW) : "memory", "scc"); SB();
  asm volatile("s_nop 0\n\tbuffer_load_dwordx4 %0, %1, %2 offen lds" :  : "v"(voB[0]), "s"(rW1), "s"(kw_[3]) : "memory"); SB();
  act(gv[0][2][0], ge2[0], a2[0][2], c2[2], 0, 0); act(gv[0][2][1], ge2[1], a2[0][2], c2[2], 0, 1);
  asm volatile("" : "+v"(gv[0][2][0]), "+v"(gv[0][2][1]), "+v"(ge2[0]), "+v"(ge2[1]));
  SB();
  asm volatile("buffer_load_dwordx4 %0, %1, %2 offen offset:1024 lds" :  : "v"(voB[1]), "s"(rW1), "s"(kw_[3]) : "memory"); SB();
  asm volatile("buffer_load_dwordx4 %0, %1, %2 offen offset:2048 lds" :  : "v"(voB[2]), "s"(rW1), "s"(kw_[3]) : "memory"); SB();
  act2(gv[0][2][0], ge2[0], 0); act2(gv[0][2][1], ge2[1], 0);
  asm volatile("" : "+v"(gv[0][2][0]), "+v"(gv[0][2][1]));
  SB();
  asm volatile("s_add_u32 m0, %0, 73728" :  : "s"(mW) : "memory", "scc"); SB();
  asm volatile("s_nop 0\n\tbuffer_load_dwordx4 %0, %1, %2 offen lds" :  : "v"(voB[0]), "s"(rW1), "s"(kw_[4]) : "memory"); SB();
  act(gv[1][0][0], ge2[0], a2[1][0], c2[0], 1, 0); act(gv[1][0][1], ge2[1], a2[1][0], c2[0], 1, 1);
  asm volatile("" : "+v"(gv[1][0][0]), "+v"(gv[1][0][1]), "+v"(ge2[0]), "+v"(ge2[1]));
  SB();
  asm volatile("buffer_load_dwordx4 %0, %1, %2 offen offset:1024 lds" :  : "v"(voB[1]), "s"(rW1), "s"(kw_[4]) : "memory"); SB();
  act2(gv[1][0][0], ge2[0], 1); act2(gv[1][0][1], ge2[1], 1);
  asm volatile("" : "+v"(gv[1][0][0]), "+v"(gv[1][0][1]));
  SB();
  act(gv[1][1][0], ge2[0], a2[1][1], c2[1], 1, 0); act(gv[1][1][1], ge2[1], a2[1][1], c2[1], 1, 1);
  asm volatile("" : "+v"(gv[1][1][0]), "+v"(gv[1][1][1]), "+v"(ge2[0]), "+v"(ge2[1]));
  SB();
  act2(gv[1][1][0], ge2[0], 1); act2(gv[1][1][1], ge2[1], 1);
  asm volatile("" : "+v"(gv[1][1][0]), "+v"(gv[1][1][1]));
  SB();
  act(gv[1][2][0], ge2[0], a2[1][2], c2[2], 1, 0); act(gv[1][2][1], ge2[1], a2[1][2], c2[2], 1, 1);
  asm volatile("" : "+v"(gv[1][2][0]), "+v"(gv[1][2][1]), "+v"(ge2[0]), "+v"(ge2[1]));
  SB();
  act2(gv[1][2][0], ge2[0], 1); act2(gv[1][2][1], ge2[1], 1);
  asm volatile("" : "+v"(gv[1][2][0]), "+v"(gv[1][2][1]));
  SB();
#pragma unroll
  for (int i = 0; i < 2; ++i) {
    const unsigned p0 = __float_as_uint(gv[i][0][0][0]);
    const auto pa = __builtin_amdgcn_permlane16_swap(p0, p0, false, false);
    const auto pb = __builtin_amdgcn_permlane32_swap(pa[0], pa[0], false, false);
    pv[i] = __uint_as_float(pb[0]);
  }
  asm volatile("" : "+v"(pv[0]), "+v"(pv[1]));
  f32x2 s1[2], s2[2];
  SB();
#pragma unroll
  for (int m = 0; m < 4; ++m) {
    const int i = m & 1, n = 0 + (m >> 1);
    const f32x2 d = gv[i][n >> 1][n & 1] - (f32x2){pv[i], pv[i]};
    s1[i] = n ? s1[i] + d : d; s2[i] = n ? __builtin_elementwise_fma(d, d, s2[i]) : d * d;
  }
  asm volatile("" : "+v"(s1[0]), "+v"(s1[1]), "+v"(s2[0]), "+v"(s2[1]));
  SB();
  asm volatile("buffer_load_dwordx4 %0, %1, %2 offen offset:2048 lds" :  : "v"(voB[2]), "s"(rW1), "s"(kw_[4]) : "memory"); SB();
#pragma unroll
  for (int m = 0; m < 4; ++m) {
    const int i = m & 1, n = 2 + (m >> 1);
    const f32x2 d = gv[i][n >> 1][n & 1] - (f32x2){pv[i], pv[i]};
    s1[i] = n ? s1[i] + d : d; s2[i] = n ? __builtin_elementwise_fma(d, d, s2[i]) : d * d;
  }
  asm volatile("" : "+v"(s1[0]), "+v"(s1[1]), "+v"(s2[0]), "+v"(s2[1]));
  SB();
  asm volatile("s_add_u32 m0, %0, 86016" :  : "s"(mW) : "memory", "scc"); SB();
  asm volatile("s_nop 0\n\tbuffer_load_dwordx4 %0, %1, %2 offen lds" :  : "v"(voB[0]), "s"(rW1), "s"(kw_[5]) : "memory"); SB();
#pragma unroll
  for (int m = 0; m < 4; ++m) {
    const int i = m & 1, n = 4 + (m >> 1);
    const f32x2 d = gv[i][n >> 1][n & 1] - (f32x2){pv[i], pv[i]};
    s1[i] = n ? s1[i] + d : d; s2[i] = n ? __builtin_elementwise_fma(d, d, s2[i]) : d * d;
  }
  asm volatile("" : "+v"(s1[0]), "+v"(s1[1]), "+v"(s2[0]), "+v"(s2[1]));
  SB();
  asm volatile("buffer_load_dwordx4 %0, %1, %2 offen offset:1024 lds" :  : "v"(voB[1]), "s"(rW1), "s"(kw_[5]) : "memory"); SB();
  for (int i = 0; i < 2; ++i) { sv[i][0] = s1[i][0] + s1[i][1]; sv[i][1] = s2[i][0] + s2[i][1]; }
  {
    const float t1 = red16(sel32(sv[0][0], sv[1][0])), t2 = red16(sel32(sv[0][1], sv[1][1]));
    const float dl = t1 * (1.0f / 48.0f);
    const u32x4 so = {__float_as_uint((lane < 32 ? pv[0] : pv[1]) + dl), __float_as_uint(fmaxf(t2 - t1 * dl, 0.f)), tg0, tg1};
    SB();
  asm volatile("s_mov_b32 exec_lo, 0xffff\n\ts_mov_b32 exec_hi, 0xffff\n\tbuffer_store_dwordx4 %1, %0, %2, %3 offen offset:0 sc1\n\ts_mov_b64 exec, -1\n\ts_nop 0" :  : "v"(vo), "v"(so), "s"(rWS), "s"(0u) : "memory"); SB();
  }
  ST(6);
  unsigned zp[2][3][2];
#pragma unroll
  for (int n = 0; n < 12; ++n) { zp[n / 6][(n >> 1) % 3][n & 1] = pk2(gv[n / 6][(n >> 1) % 3][n & 1]); asm volatile("" : "+v"(zp[n / 6][(n >> 1) % 3][n & 1])); }
  SB();
  asm volatile("s_waitcnt vmcnt(25)\n\ts_barrier" :  :  : "memory"); SB();
  ST(2);
  asm volatile("ds_read_b64_tr_b16 %0, %1 offset:0" : "=&v"(wt[0]) : "v"(oa) : "memory"); SB();
  asm volatile("ds_read_b64_tr_b16 %0, %1 offset:512" : "=&v"(wt[1]) : "v"(oa) : "memory"); SB();
  asm volatile("ds_read_b64_tr_b16 %0, %1 offset:0" : "=&v"(kp[0]) : "v"(ka) : "memory"); SB();
  asm volatile("ds_read_b64_tr_b16 %0, %1 offset:640" : "=&v"(kc[0]) : "v"(ka) : "memory"); SB();
  asm volatile("ds_read_b64_tr_b16 %0, %1 offset:4096" : "=&v"(vv[0]) : "v"(ka) : "memory"); SB();
  asm volatile("ds_read_b64 %0, %1 offset:0" : "=&v"(sw[0]) : "v"(sa) : "memory"); SB();
  asm volatile("buffer_load_dwordx4 %0, %1, %2 offen offset:2048 lds" :  : "v"(voB[2]), "s"(rW1), "s"(kw_[5]) : "memory"); SB();
  asm volatile("ds_read_b64_tr_b16 %0, %1 offset:32" : "=&v"(kp[1]) : "v"(ka) : "memory"); SB();
  asm volatile("ds_read_b64_tr_b16 %0, %1 offset:672" : "=&v"(kc[1]) : "v"(ka) : "memory"); SB();
  asm volatile("ds_read_b64_tr_b16 %0, %1 offset:4128" : "=&v"(vv[1]) : "v"(ka) : "memory"); SB();
  asm volatile("ds_read_b64 %0, %1 offset:128" : "=&v"(sw[1]) : "v"(sa) : "memory"); SB();
  asm volatile("s_add_u32 m0, %0, 0" :  : "s"(mW) : "memory", "scc"); SB();
  asm volatile("s_nop 0\n\tbuffer_load_dwordx4 %0, %1, %2 offen lds" :  : "v"(voB[0]), "s"(rW1), "s"(kw_[6]) : "memory"); SB();
  asm volatile("ds_read_b64_tr_b16 %0, %1 offset:64" : "=&v"(kp[2]) : "v"(ka) : "memory"); SB();
  asm volatile("ds_read_b64_tr_b16 %0, %1 offset:704" : "=&v"(kc[2]) : "v"(ka) : "memory"); SB();
  asm volatile("ds_read_b64_tr_b16 %0, %1 offset:4160" : "=&v"(vv[2]) : "v"(ka) : "memory"); SB();
  asm volatile("ds_read_b64 %0, %1 offset:256" : "=&v"(sw[2]) : "v"(sa) : "memory"); SB();
  asm volatile("buffer_load_dwordx4 %0, %1, %2 offen offset:1024 lds" :  : "v"(voB[1]), "s"(rW1), "s"(kw_[6]) : "memory"); SB();
  asm volatile("ds_read_b64_tr_b16 %0, %1 offset:96" : "=&v"(kp[3]) : "v"(ka) : "memory"); SB();
  asm volatile("ds_read_b64_tr_b16 %0, %1 offset:736" : "=&v"(kc[3]) : "v"(ka) : "memory"); SB();
  asm volatile("ds_read_b64_tr_b16 %0, %1 offset:4192" : "=&v"(vv[3]) : "v"(ka) : "memory"); SB();
  asm volatile("ds_read_b64 %0, %1 offset:384" : "=&v"(sw[3]) : "v"(sa) : "memory"); SB();
  asm volatile("buffer_load_dwordx4 %0, %1, %2 offen offset:2048 lds" :  : "v"(voB[2]), "s"(rW1), "s"(kw_[6]) : "memory"); SB();
  asm volatile("ds_read_b64_tr_b16 %0, %1 offset:128" : "=&v"(kp[4]) : "v"(ka) : "memory"); SB();
  asm volatile("ds_read_b64_tr_b16 %0, %1 offset:768" : "=&v"(kc[4]) : "v"(ka) : "memory"); SB();
  asm volatile("ds_read_b64_tr_b16 %0, %1 offset:4224" : "=&v"(vv[4]) : "v"(ka) : "memory"); SB();
  asm volatile("ds_read_b64 %0, %1 offset:512" : "=&v"(sw[4]) : "v"(sa) : "memory"); SB();
  asm volatile("s_waitcnt lgkmcnt(8)" : "+v"(kp[0]), "+v"(kc[0]), "+v"(vv[0]), "+v"(sw[0]), "+v"(wt[0]), "+v"(wt[1]) :  : "memory"); SB();
  asm volatile("v_alignbit_b32 %4, %14, %14, 16\n\tv_alignbit_b32 %5, %10, %9, 16\n\tv_alignbit_b32 %6, %11, %10, 16\n\tv_alignbit_b32 %7, %13, %13, 16\n\tv_alignbit_b32 %8, %12, %11, 16\n\tv_dot2_f32_bf16 %0, %5, %4, 0\n\tv_dot2_f32_bf16 %1, %10, %4, 0\n\tv_dot2_f32_bf16 %2, %6, %4, 0\n\tv_dot2_f32_bf16 %3, %11, %4, 0\n\tv_dot2_f32_bf16 %0, %6, %7, %0\n\tv_dot2_f32_bf16 %1, %11, %7, %1\n\tv_dot2_f32_bf16 %2, %8, %7, %2\n\tv_dot2_f32_bf16 %3, %12, %7, %3" : "=&v"(kt[0][0]), "=&v"(kt[0][1]), "=&v"(kt[1][0]), "=&v"(kt[1][1]), "=&v"(ku[0]), "=&v"(ku[1]), "=&v"(ku[2]), "=&v"(ku[3]), "=&v"(ku[4]) : "v"(kp[0][0]), "v"(kp[0][1]), "v"(kc[0][0]), "v"(kc[0][1]), "v"(sw[0][0]), "v"(sw[0][1]) : ); SB();
  asm volatile("s_add_u32 m0, %0, 131072" :  : "s"(mA) : "memory", "scc"); SB();
  asm volatile("s_nop 0\n\tbuffer_load_dwordx4 %0, %1, %2 offen lds" :  : "v"(voS), "s"(rA), "s"(ao[0]) : "memory"); SB();
  asm volatile("buffer_load_dwordx4 %0, %1, %2 offen offset:1024 lds" :  : "v"(voS), "s"(rA), "s"(ao[1]) : "memory"); SB();
  asm volatile("v_lshlrev_b32 %0, 16, %4\n\tv_and_b32 %1, 0xffff0000, %4\n\tv_lshlrev_b32 %2, 16, %5\n\tv_and_b32 %3, 0xffff0000, %5" : "=&v"(kf[0][0]), "=&v"(kf[0][1]), "=&v"(kf[1][0]), "=&v"(kf[1][1]) : "v"(vv[0][0]), "v"(vv[0][1]) : ); SB();
  kt[0] *= kf[0]; kt[1] *= kf[1]; SB();
  ko[0] = (u32x2){pk2(kt[0]), pk2(kt[1])};
  asm volatile("" : "+v"(ko[0]));
  SB();
  asm volatile("ds_write_b64 %0, %1 offset:0" :  : "v"(kw), "v"(ko[0]) : "memory"); SB();
  asm volatile("" : "+v"(kp[1]), "+v"(kc[1]), "+v"(vv[1]), "+v"(sw[1]) :  : "memory"); SB();
  asm volatile("v_alignbit_b32 %4, %14, %14, 16\n\tv_alignbit_b32 %5, %10, %9, 16\n\tv_alignbit_b32 %6, %11, %10, 16\n\tv_alignbit_b32 %7, %13, %13, 16\n\tv_alignbit_b32 %8, %12, %11, 16\n\tv_dot2_f32_bf16 %0, %5, %4, 0\n\tv_dot2_f32_bf16 %1, %10, %4, 0\n\tv_dot2_f32_bf16 %2, %6, %4, 0\n\tv_dot2_f32_bf16 %3, %11, %4, 0\n\tv_dot2_f32_bf16 %0, %6, %7, %0\n\tv_dot2_f32_bf16 %1, %11, %7, %1\n\tv_dot2_f32_bf16 %2, %8, %7, %2\n\tv_dot2_f32_bf16 %3, %12, %7, %3" : "=&v"(kt[0][0]), "=&v"(kt[0][1]), "=&v"(kt[1][0]), "=&v"(kt[1][1]), "=&v"(ku[0]), "=&v"(ku[1]), "=&v"(ku[2]), "=&v"(ku[3]), "=&v"(ku[4]) : "v"(kp[1][0]), "v"(kp[1][1]), "v"(kc[1][0]), "v"(kc[1][1]), "v"(sw[1][0]), "v"(sw[1][1]) : ); SB();
  asm volatile("buffer_load_dwordx4 %0, %1, %2 offen offset:2048 lds" :  : "v"(voS), "s"(rA), "s"(ao[2]) : "memory"); SB();
  asm volatile("buffer_load_dwordx4 %0, %1, %2 offen offset:3072 lds" :  : "v"(voS), "s"(rA), "s"(ao[3]) : "memory"); SB();
  asm volatile("v_lshlrev_b32 %0, 16, %4\n\tv_and_b32 %1, 0xffff0000, %4\n\tv_lshlrev_b32 %2, 16, %5\n\tv_and_b32 %3, 0xffff0000, %5" : "=&v"(kf[0][0]), "=&v"(kf[0][1]), "=&v"(kf[1][0]), "=&v"(kf[1][1]) : "v"(vv[1][0]), "v"(vv[1][1]) : ); SB();
  kt[0] *= kf[0]; kt[1] *= kf[1]; SB();
  ko[1] = (u32x2){pk2(kt[0]), pk2(kt[1])};
  asm volatile("" : "+v"(ko[1]));
  SB();
  asm volatile("ds_write_b64 %0, %1 offset:2304" :  : "v"(kw), "v"(ko[1]) : "memory"); SB();
  asm volatile("" : "+v"(kp[2]), "+v"(kc[2]), "+v"(vv[2]), "+v"(sw[2]) :  : "memory"); SB();
  asm volatile("v_alignbit_b32 %4, %14, %14, 16\n\tv_alignbit_b32 %5, %10, %9, 16\n\tv_alignbit_b32 %6, %11, %10, 16\n\tv_alignbit_b32 %7, %13, %13, 16\n\tv_alignbit_b32 %8, %12, %11, 16\n\tv_dot2_f32_bf16 %0, %5, %4, 0\n\tv_dot2_f32_bf16 %1, %10, %4, 0\n\tv_dot2_f32_bf16 %2, %6, %4, 0\n\tv_dot2_f32_bf16 %3, %11, %4, 0\n\tv_dot2_f32_bf16 %0, %6, %7, %0\n\tv_dot2_f32_bf16 %1, %11, %7, %1\n\tv_dot2_f32_bf16 %2, %8, %7, %2\n\tv_dot2_f32_bf16 %3, %12, %7, %3" : "=&v"(kt[0][0]), "=&v"(kt[0][1]), "=&v"(kt[1][0]), "=&v"(kt[1][1]), "=&v"(ku[0]), "=&v"(ku[1]), "=&v"(ku[2]), "=&v"(ku[3]), "=&v"(ku[4]) : "v"(kp[2][0]), "v"(kp[2][1]), "v"(kc[2][0]), "v"(kc[2][1]), "v"(sw[2][0]), "v"(sw[2][1]) : ); SB();
  asm volatile("s_add_u32 m0, %0, 135168" :  : "s"(mA) : "memory", "scc"); SB();
  asm volatile("s_nop 0\n\tbuffer_load_dwordx4 %0, %1, %2 offen lds" :  : "v"(voS), "s"(rA), "s"(ao[4]) : "memory"); SB();
  asm volatile("buffer_load_dwordx4 %0, %1, %2 offen offset:1024 lds" :  : "v"(voS), "s"(rA), "s"(ao[5]) : "memory"); SB();
  asm volatile("v_lshlrev_b32 %0, 16, %4\n\tv_and_b32 %1, 0xffff0000, %4\n\tv_lshlrev_b32 %2, 16, %5\n\tv_and_b32 %3, 0xffff0000, %5" : "=&v"(kf[0][0]), "=&v"(kf[0][1]), "=&v"(kf[1][0]), "=&v"(kf[1][1]) : "v"(vv[2][0]), "v"(vv[2][1]) : ); SB();
  kt[0] *= kf[0]; kt[1] *= kf[1]; SB();
  ko[2] = (u32x2){pk2(kt[0]), pk2(kt[1])};
  asm volatile("" : "+v"(ko[2]));
  SB();
  asm volatile("ds_write_b64 %0, %1 offset:4608" :  : "v"(kw), "v"(ko[2]) : "memory"); SB();
  asm volatile("s_waitcnt lgkmcnt(1)" : "+v"(kp[3]), "+v"(kc[3]), "+v"(vv[3]), "+v"(sw[3]) :  : "memory"); SB();
  asm volatile("v_alignbit_b32 %4, %14, %14, 16\n\tv_alignbit_b32 %5, %10, %9, 16\n\tv_alignbit_b32 %6, %11, %10, 16\n\tv_alignbit_b32 %7, %13, %13, 16\n\tv_alignbit_b32 %8, %12, %11, 16\n\tv_dot2_f32_bf16 %0, %5, %4, 0\n\tv_dot2_f32_bf16 %1, %10, %4, 0\n\tv_dot2_f32_bf16 %2, %6, %4, 0\n\tv_dot2_f32_bf16 %3, %11, %4, 0\n\tv_dot2_f32_bf16 %0, %6, %7, %0\n\tv_dot2_f32_bf16 %1, %11, %7, %1\n\tv_dot2_f32_bf16 %2, %8, %7, %2\n\tv_dot2_f32_bf16 %3, %12, %7, %3" : "=&v"(kt[0][0]), "=&v"(kt[0][1]), "=&v"(kt[1][0]), "=&v"(kt[1][1]), "=&v"(ku[0]), "=&v"(ku[1]), "=&v"(ku[2]), "=&v"(ku[3]), "=&v"(ku[4]) : "v"(kp[3][0]), "v"(kp[3][1]), "v"(kc[3][0]), "v"(kc[3][1]), "v"(sw[3][0]), "v"(sw[3][1]) : ); SB();
  asm volatile("buffer_load_dwordx4 %0, %1, %2 offen offset:2048 lds" :  : "v"(voS), "s"(rA), "s"(ao[6]) : "memory"); SB();
  asm volatile("buffer_load_dwordx4 %0, %1, %2 offen offset:3072 lds" :  : "v"(voS), "s"(rA), "s"(ao[7]) : "memory"); SB();
  asm volatile("v_xor_b32 %0, 128, %0" : "+v"(voS) :  : "memory"); SB();
  asm volatile("v_lshlrev_b32 %0, 16, %4\n\tv_and_b32 %1, 0xffff0000, %4\n\tv_lshlrev_b32 %2, 16, %5\n\tv_and_b32 %3, 0xffff0000, %5" : "=&v"(kf[0][0]), "=&v"(kf[0][1]), "=&v"(kf[1][0]), "=&v"(kf[1][1]) : "v"(vv[3][0]), "v"(vv[3][1]) : ); SB();
  kt[0] *= kf[0]; kt[1] *= kf[1]; SB();
  ko[3] = (u32x2){pk2(kt[0]), pk2(kt[1])};
  asm volatile("" : "+v"(ko[3]));
  SB();
  asm volatile("ds_write_b64 %0, %1 offset:6912" :  : "v"(kw), "v"(ko[3]) : "memory"); SB();
  asm volatile("" : "+v"(kp[4]), "+v"(kc[4]), "+v"(vv[4]), "+v"(sw[4]) :  : "memory"); SB();
  asm volatile("v_alignbit_b32 %4, %14, %14, 16\n\tv_alignbit_b32 %5, %10, %9, 16\n\tv_alignbit_b32 %6, %11, %10, 16\n\tv_alignbit_b32 %7, %13, %13, 16\n\tv_alignbit_b32 %8, %12, %11, 16\n\tv_dot2_f32_bf16 %0, %5, %4, 0\n\tv_dot2_f32_bf16 %1, %10, %4, 0\n\tv_dot2_f32_bf16 %2, %6, %4, 0\n\tv_dot2_f32_bf16 %3, %11, %4, 0\n\tv_dot2_f32_bf16 %0, %6, %7, %0\n\tv_dot2_f32_bf16 %1, %11, %7, %1\n\tv_dot2_f32_bf16 %2, %8, %7, %2\n\tv_dot2_f32_bf16 %3, %12, %7, %3" : "=&v"(kt[0][0]), "=&v"(kt[0][1]), "=&v"(kt[1][0]), "=&v"(kt[1][1]), "=&v"(ku[0]), "=&v"(ku[1]), "=&v"(ku[2]), "=&v"(ku[3]), "=&v"(ku[4]) : "v"(kp[4][0]), "v"(kp[4][1]), "v"(kc[4][0]), "v"(kc[4][1]), "v"(sw[4][0]), "v"(sw[4][1]) : ); SB();
  asm volatile("v_lshlrev_b32 %0, 16, %4\n\tv_and_b32 %1, 0xffff0000, %4\n\tv_lshlrev_b32 %2, 16, %5\n\tv_and_b32 %3, 0xffff0000, %5" : "=&v"(kf[0][0]), "=&v"(kf[0][1]), "=&v"(kf[1][0]), "=&v"(kf[1][1]) : "v"(vv[4][0]), "v"(vv[4][1]) : ); SB();
  kt[0] *= kf[0]; kt[1] *= kf[1]; SB();
  ko[4] = (u32x2){pk2(kt[0]), pk2(kt[1])};
  asm volatile("" : "+v"(ko[4]));
  SB();
  asm volatile("ds_write_b64 %0, %1 offset:9216" :  : "v"(kw), "v"(ko[4]) : "memory"); SB();
  ST(16);
  asm volatile("s_waitcnt vmcnt(24) lgkmcnt(0)\n\ts_barrier" :  :  : "memory"); SB();
  asm volatile("ds_read_b128 %0, %1 offset:0" : "=&v"(kq[0]) : "v"(kr) : "memory"); SB();
  asm volatile("ds_read_b128 %0, %1 offset:1152" : "=&v"(kq[1]) : "v"(kr) : "memory"); SB();
  asm volatile("ds_read_b128 %0, %1 offset:2304" : "=&v"(kq[2]) : "v"(kr) : "memory"); SB();
  asm volatile("ds_read_b64_tr_b16 v[184:185], %0 offset:24576" :  : "v"(aW[0][0][0]) : "memory"); SB();
  asm volatile("ds_read_b64_tr_b16 v[186:187], %0 offset:25344" :  : "v"(aW[0][0][0]) : "memory"); SB();
  asm volatile("ds_read_b64_tr_b16 v[188:189], %0 offset:24576" :  : "v"(aW[0][0][1]) : "memory"); SB();
  asm volatile("ds_read_b64_tr_b16 v[190:191], %0 offset:25344" :  : "v"(aW[0][0][1]) : "memory"); SB();
  asm volatile("ds_read_b64_tr_b16 v[192:193], %0 offset:24576" :  : "v"(aW[0][0][2]) : "memory"); SB();
  asm volatile("ds_read_b64_tr_b16 v[194:195], %0 offset:25344" :  : "v"(aW[0][0][2]) : "memory"); SB();
  ST(7);
  asm volatile("s_waitcnt lgkmcnt(6)" : "+v"(kq[0]), "+v"(kq[1]), "+v"(kq[2]) :  : "memory"); SB();
  unsigned kz[3] = {kq[0][3], kq[1][3] | (w + 1) >> 2, kq[2][3] | (w + 1) >> 2};
  asm volatile("" : "+v"(kz[0]), "+v"(kz[1]), "+v"(kz[2])); SB();
  asm volatile("buffer_load_dwordx4 v[196:199], %0, %1, %2 offen offset:0 sc1" :  : "v"(vS), "s"(rWS), "s"(0u) : "memory"); SB();
  asm volatile("buffer_load_dwordx4 v[200:203], %0, %1, %2 offen offset:2048 sc1" :  : "v"(vS), "s"(rWS), "s"(0u) : "memory"); SB();
  asm volatile("s_waitcnt lgkmcnt(3)\n\tv_mfma_f32_16x16x32_bf16 %0, v[184:187], a[0:3], 0" : "=&v"(a1[0][0]) :  : ); SB();
  asm volatile("ds_read_b64_tr_b16 v[160:161], %0 offset:24576" :  : "v"(aW[0][1][0]) : "memory"); SB();
  asm volatile("v_mfma_f32_16x16x32_bf16 %0, v[184:187], a[4:7], 0" : "=&v"(a1[1][0]) :  : ); SB();
  asm volatile("ds_read_b64_tr_b16 v[162:163], %0 offset:25344" :  : "v"(aW[0][1][0]) : "memory"); SB();
  asm volatile("v_mfma_f32_16x16x32_bf16 %0, v[184:187], %1, 0" : "=&v"(c1[0]) : "v"(ones) : ); SB();
  asm volatile("ds_read_b64_tr_b16 v[164:165], %0 offset:24576" :  : "v"(aW[0][1][1]) : "memory"); SB();
  asm volatile("s_waitcnt lgkmcnt(3)\n\tv_mfma_f32_16x16x32_bf16 %0, v[188:191], a[0:3], 0" : "=&v"(a1[0][1]) :  : ); SB();
  asm volatile("ds_read_b64_tr_b16 v[166:167], %0 offset:25344" :  : "v"(aW[0][1][1]) : "memory"); SB();
  asm volatile("v_mfma_f32_16x16x32_bf16 %0, v[188:191], a[4:7], 0" : "=&v"(a1[1][1]) :  : ); SB();
  asm volatile("ds_read_b64_tr_b16 v[168:169], %0 offset:24576" :  : "v"(aW[0][1][2]) : "memory"); SB();
  asm volatile("v_mfma_f32_16x16x32_bf16 %0, v[188:191], %1, 0" : "=&v"(c1[1]) : "v"(ones) : ); SB();
  asm volatile("ds_read_b64_tr_b16 v[170:171], %0 offset:25344" :  : "v"(aW[0][1][2]) : "memory"); SB();
  asm volatile("v_mfma_f32_16x16x32_bf16 %0, v[192:195], a[0:3], 0" : "=&v"(a1[0][2]) :  : ); SB();
  asm volatile("ds_read_b64_tr_b16 v[172:173], %0 offset:36864" :  : "v"(aW[0][0][0]) : "memory"); SB();
  asm volatile("v_mfma_f32_16x16x32_bf16 %0, v[192:195], a[4:7], 0" : "=&v"(a1[1][2]) :  : ); SB();
  asm volatile("ds_read_b64_tr_b16 v[174:175], %0 offset:37632" :  : "v"(aW[0][0][0]) : "memory"); SB();
  asm volatile("buffer_load_dwordx4 v[204:207], %0, %1, %2 offen offset:0 sc1" :  : "v"(vS), "s"(rWS), "s"(4096u) : "memory"); SB();
  asm volatile("v_mfma_f32_16x16x32_bf16 %0, v[192:195], %1, 0" : "=&v"(c1[2]) : "v"(ones) : ); SB();
  asm volatile("ds_read_b64_tr_b16 v[176:177], %0 offset:36864" :  : "v"(aW[0][0][1]) : "memory"); SB();
  asm volatile("s_waitcnt lgkmcnt(7)\n\tv_mfma_f32_16x16x32_bf16 %0, v[160:163], a[8:11], %0" : "+v"(a1[0][0]) :  : ); SB();
  asm volatile("ds_read_b64_tr_b16 v[178:179], %0 offset:37632" :  : "v"(aW[0][0][1]) : "memory"); SB();
  asm volatile("v_mfma_f32_16x16x32_bf16 %0, v[160:163], a[12:15], %0" : "+v"(a1[1][0]) :  : ); SB();
  asm volatile("ds_read_b64_tr_b16 v[180:181], %0 offset:36864" :  : "v"(aW[0][0][2]) : "memory"); SB();
  asm volatile("s_waitcnt lgkmcnt(7)\n\tv_mfma_f32_16x16x32_bf16 %0, v[164:167], a[8:11], %0" : "+v"(a1[0][1]) :  : ); SB();
  asm volatile("ds_read_b64_tr_b16 v[182:183], %0 offset:37632" :  : "v"(aW[0][0][2]) : "memory"); SB();
  asm volatile("v_mfma_f32_16x16x32_bf16 %0, v[164:167], a[12:15], %0" : "+v"(a1[1][1]) :  : ); SB();
  asm volatile("ds_read_b64_tr_b16 v[184:185], %0 offset:36864" :  : "v"(aW[0][1][0]) : "memory"); SB();
  asm volatile("buffer_load_dwordx4 v[208:211], %0, %1, %2 offen offset:2048 sc1" :  : "v"(vS), "s"(rWS), "s"(4096u) : "memory"); SB();
  asm volatile("s_waitcnt lgkmcnt(7)\n\tv_mfma_f32_16x16x32_bf16 %0, v[168:171], a[8:11], %0" : "+v"(a1[0][2]) :  : ); SB();
  asm volatile("ds_read_b64_tr_b16 v[186:187], %0 offset:37632" :  : "v"(aW[0][1][0]) : "memory"); SB();
  asm volatile("v_mfma_f32_16x16x32_bf16 %0, v[168:171], a[12:15], %0" : "+v"(a1[1][2]) :  : ); SB();
  asm volatile("ds_read_b64_tr_b16 v[188:189], %0 offset:36864" :  : "v"(aW[0][1][1]) : "memory"); SB();
  asm volatile("s_waitcnt lgkmcnt(7)\n\tv_mfma_f32_16x16x32_bf16 %0, v[172:175], a[16:19], %0" : "+v"(a1[0][0]) :  : ); SB();
  asm volatile("ds_read_b64_tr_b16 v[190:191], %0 offset:37632" :  : "v"(aW[0][1][1]) : "memory"); SB();
  asm volatile("buffer_store_dwordx4 %1, %0, %2, %3 offen offset:0 sc1\n\ts_nop 1" :  : "v"(vKV), "v"(kq[0]), "s"(rWS), "s"(0u) : "memory"); SB();
  asm volatile("v_mfma_f32_16x16x32_bf16 %0, v[172:175], a[20:23], %0" : "+v"(a1[1][0]) :  : ); SB();
  asm volatile("ds_read_b64_tr_b16 v[192:193], %0 offset:36864" :  : "v"(aW[0][1][2]) : "memory"); SB();
  asm volatile("v_mfma_f32_16x16x32_bf16 %0, v[172:175], %1, %0" : "+v"(c1[0]) : "v"(ones) : ); SB();
  asm volatile("ds_read_b64_tr_b16 v[194:195], %0 offset:37632" :  : "v"(aW[0][1][2]) : "memory"); SB();
  asm volatile("s_waitcnt lgkmcnt(8)\n\tv_mfma_f32_16x16x32_bf16 %0, v[176:179], a[16:19], %0" : "+v"(a1[0][1]) :  : ); SB();
  asm volatile("s_waitcnt vmcnt(23)\n\ts_barrier" :  :  : "memory"); SB();
  asm volatile("ds_read_b64_tr_b16 v[160:161], %0 offset:0" :  : "v"(aW[1][0][0]) : "memory"); SB();
  asm volatile("v_mfma_f32_16x16x32_bf16 %0, v[176:179], a[20:23], %0" : "+v"(a1[1][1]) :  : ); SB();
  asm volatile("ds_read_b64_tr_b16 v[162:163], %0 offset:768" :  : "v"(aW[1][0][0]) : "memory"); SB();
  asm volatile("buffer_store_dwordx4 %1, %0, %2, %3 offen offset:0 sc1\n\ts_nop 1" :  : "v"(vKV), "v"(kq[1]), "s"(rWS), "s"(w3k[0]) : "memory"); SB();
  asm volatile("v_mfma_f32_16x16x32_bf16 %0, v[176:179], %1, %0" : "+v"(c1[1]) : "v"(ones) : ); SB();
  asm volatile("ds_read_b64_tr_b16 v[164:165], %0 offset:0" :  : "v"(aW[1][0][1]) : "memory"); SB();
  asm volatile("s_waitcnt lgkmcnt(6)\n\tv_mfma_f32_16x16x32_bf16 %0, v[180:183], a[16:19], %0" : "+v"(a1[0][2]) :  : ); SB();
  asm volatile("ds_read_b64_tr_b16 v[166:167], %0 offset:768" :  : "v"(aW[1][0][1]) : "memory"); SB();
  asm volatile("v_mfma_f32_16x16x32_bf16 %0, v[180:183], a[20:23], %0" : "+v"(a1[1][2]) :  : ); SB();
  asm volatile("ds_read_b64_tr_b16 v[168:169], %0 offset:0" :  : "v"(aW[1][0][2]) : "memory"); SB();
  asm volatile("buffer_store_dwordx4 %1, %0, %2, %3 offen offset:0 sc1\n\ts_nop 1" :  : "v"(vKV), "v"(kq[2]), "s"(rWS), "s"(w3k[1]) : "memory"); SB();
  asm volatile("v_mfma_f32_16x16x32_bf16 %0, v[180:183], %1, %0" : "+v"(c1[2]) : "v"(ones) : ); SB();
  asm volatile("ds_read_b64_tr_b16 v[170:171], %0 offset:768" :  : "v"(aW[1][0][2]) : "memory"); SB();
  asm volatile("v_mfma_f32_16x16x32_bf16 %0, v[184:187], a[24:27], %0" : "+v"(a1[0][0]) :  : ); SB();
  asm volatile("ds_read_b64_tr_b16 v[172:173], %0 offset:0" :  : "v"(aW[1][1][0]) : "memory"); SB();
  asm volatile("v_mfma_f32_16x16x32_bf16 %0, v[184:187], a[28:31], %0" : "+v"(a1[1][0]) :  : ); SB();
  asm volatile("ds_read_b64_tr_b16 v[174:175], %0 offset:768" :  : "v"(aW[1][1][0]) : "memory"); SB();
  asm volatile("s_waitcnt lgkmcnt(8)\n\tv_mfma_f32_16x16x32_bf16 %0, v[188:191], a[24:27], %0" : "+v"(a1[0][1]) :  : ); SB();
  asm volatile("ds_read_b64_tr_b16 v[176:177], %0 offset:0" :  : "v"(aW[1][1][1]) : "memory"); SB();
  asm volatile("v_mfma_f32_16x16x32_bf16 %0, v[188:191], a[28:31], %0" : "+v"(a1[1][1]) :  : ); SB();
  asm volatile("ds_read_b64_tr_b16 v[178:179], %0 offset:768" :  : "v"(aW[1][1][1]) : "memory"); SB();
  asm volatile("buffer_store_dwordx4 %1, %0, %2, %3 offen offset:0 sc1\n\ts_nop 1" :  : "v"(vWO), "v"(((u32x4){wt[0][0], wt[0][1], wt[1][0], wt[1][1]})), "s"(rWS), "s"(0u) : "memory"); SB();
  asm volatile("v_mfma_f32_16x16x32_bf16 %0, v[192:195], a[24:27], %0" : "+v"(a1[0][2]) :  : ); SB();
  asm volatile("ds_read_b64_tr_b16 v[180:181], %0 offset:0" :  : "v"(aW[1][1][2]) : "memory"); SB();
  asm volatile("s_waitcnt vmcnt(14)" :  :  : "memory"); SB();
  asm volatile("v_mfma_f32_16x16x32_bf16 %0, v[192:195], a[28:31], %0" : "+v"(a1[1][2]) :  : ); SB();
  asm volatile("ds_read_b64_tr_b16 v[182:183], %0 offset:768" :  : "v"(aW[1][1][2]) : "memory"); SB();
  asm volatile("s_waitcnt lgkmcnt(8)\n\tv_mfma_f32_16x16x32_bf16 %0, v[160:163], a[32:35], %0" : "+v"(a1[0][0]) :  : ); SB();
  asm volatile("ds_read_b64_tr_b16 v[184:185], %0 offset:12288" :  : "v"(aW[1][0][0]) : "memory"); SB();
  asm volatile("ds_read_b128 a[160:163], %0 offset:32768" :  : "v"(aA[0]) : "memory"); SB();
  asm volatile("v_mfma_f32_16x16x32_bf16 %0, v[160:163], a[36:39], %0" : "+v"(a1[1][0]) :  : ); SB();
  asm volatile("ds_read_b64_tr_b16 v[186:187], %0 offset:13056" :  : "v"(aW[1][0][0]) : "memory"); SB();
  asm volatile("ds_read_b128 a[176:179], %0 offset:32768" :  : "v"(aA[1]) : "memory"); SB();
  asm volatile("v_mfma_f32_16x16x32_bf16 %0, v[160:163], %1, %0" : "+v"(c1[0]) : "v"(ones) : ); SB();
  asm volatile("ds_read_b64_tr_b16 v[188:189], %0 offset:12288" :  : "v"(aW[1][0][1]) : "memory"); SB();
  asm volatile("s_waitcnt vmcnt(12)" :  :  : "memory"); SB();
  asm volatile("v_mfma_f32_16x16x32_bf16 %0, v[164:167], a[32:35], %0" : "+v"(a1[0][1]) :  : ); SB();
  asm volatile("ds_read_b64_tr_b16 v[190:191], %0 offset:13056" :  : "v"(aW[1][0][1]) : "memory"); SB();
  asm volatile("ds_read_b128 a[164:167], %0 offset:34816" :  : "v"(aA[0]) : "memory"); SB();
  asm volatile("v_mfma_f32_16x16x32_bf16 %0, v[164:167], a[36:39], %0" : "+v"(a1[1][1]) :  : ); SB();
  asm volatile("ds_read_b64_tr_b16 v[192:193], %0 offset:12288" :  : "v"(aW[1][0][2]) : "memory"); SB();
  asm volatile("ds_read_b128 a[180:183], %0 offset:34816" :  : "v"(aA[1]) : "memory"); SB();
  asm volatile("v_mfma_f32_16x16x32_bf16 %0, v[164:167], %1, %0" : "+v"(c1[1]) : "v"(ones) : ); SB();
  asm volatile("ds_read_b64_tr_b16 v[194:195], %0 offset:13056" :  : "v"(aW[1][0][2]) : "memory"); SB();
  asm volatile("s_waitcnt vmcnt(10)" :  :  : "memory"); SB();
  asm volatile("s_waitcnt lgkmcnt(12)\n\tv_mfma_f32_16x16x32_bf16 %0, v[168:171], a[32:35], %0" : "+v"(a1[0][2]) :  : ); SB();
  asm volatile("ds_read_b128 a[168:171], %0 offset:36864" :  : "v"(aA[0]) : "memory"); SB();
  asm volatile("v_mfma_f32_16x16x32_bf16 %0, v[168:171], a[36:39], %0" : "+v"(a1[1][2]) :  : ); SB();
  asm volatile("ds_read_b128 a[184:187], %0 offset:36864" :  : "v"(aA[1]) : "memory"); SB();
  asm volatile("s_waitcnt vmcnt(8)" :  :  : "memory"); SB();
  asm volatile("v_mfma_f32_16x16x32_bf16 %0, v[168:171], %1, %0" : "+v"(c1[2]) : "v"(ones) : ); SB();
  asm volatile("ds_read_b128 a[172:175], %0 offset:38912" :  : "v"(aA[0]) : "memory"); SB();
  asm volatile("v_mfma_f32_16x16x32_bf16 %0, v[172:175], a[40:43], %0" : "+v"(a1[0][0]) :  : ); SB();
  asm volatile("ds_read_b64_tr_b16 v[160:161], %0 offset:12288" :  : "v"(aW[1][1][0]) : "memory"); SB();
  asm volatile("ds_read_b128 a[188:191], %0 offset:38912" :  : "v"(aA[1]) : "memory"); SB();
  asm volatile("v_mfma_f32_16x16x32_bf16 %0, v[172:175], a[44:47], %0" : "+v"(a1[1][0]) :  : ); SB();
  asm volatile("ds_read_b64_tr_b16 v[162:163], %0 offset:13056" :  : "v"(aW[1][1][0]) : "memory"); SB();
  asm volatile("s_add_u32 m0, %0, 12288" :  : "s"(mW) : "memory", "scc"); SB();
  asm volatile("v_mfma_f32_16x16x32_bf16 %0, v[176:179], a[40:43], %0" : "+v"(a1[0][1]) :  : ); SB();
  asm volatile("ds_read_b64_tr_b16 v[164:165], %0 offset:12288" :  : "v"(aW[1][1][1]) : "memory"); SB();
  asm volatile("buffer_load_dwordx4 %0, %1, %2 offen lds" :  : "v"(voB[0]), "s"(rW1), "s"(kw_[7]) : "memory"); SB();
  asm volatile("v_mfma_f32_16x16x32_bf16 %0, v[176:179], a[44:47], %0" : "+v"(a1[1][1]) :  : ); SB();
  asm volatile("ds_read_b64_tr_b16 v[166:167], %0 offset:13056" :  : "v"(aW[1][1][1]) : "memory"); SB();
  asm volatile("s_waitcnt lgkmcnt(10)\n\tv_mfma_f32_16x16x32_bf16 %0, v[180:183], a[40:43], %0" : "+v"(a1[0][2]) :  : ); SB();
  asm volatile("ds_read_b64_tr_b16 v[168:169], %0 offset:12288" :  : "v"(aW[1][1][2]) : "memory"); SB();
  asm volatile("v_mfma_f32_16x16x32_bf16 %0, v[180:183], a[44:47], %0" : "+v"(a1[1][2]) :  : ); SB();
  asm volatile("ds_read_b64_tr_b16 v[170:171], %0 offset:13056" :  : "v"(aW[1][1][2]) : "memory"); SB();
  ST(8);
  { unsigned lane = lane0, w = w0, rb = rb0, cb = cb0;
  asm volatile("" : "+v"(lane)); asm volatile("" : "+s"(w), "+s"(rb), "+s"(cb)); SB();

  unsigned zwa[2] = {lb + 98304 + 4096 + w * 8192 + (wn * 16 + l15) * 104 + g4 * 8, lb + 98304 + 4096 + w * 8192 + ((wn ^ 1) * 16 + l15) * 104 + g4 * 8};
  unsigned zra = lb + 98304 + 4096 + w * 8192 + (8 * g4 + q4) * 104 + 8 * p4, vZ_ = vZ;
  unsigned nw = lb + 98304 + 7424 + wm * 16384 + wn * 64 + (g4 & 1) * 32 + (lane & 7) * 4, nr = lb + 98304 + 7424 + wm * 16384 + g4 * 32;
  asm volatile("" : "+v"(zwa[0]), "+v"(zwa[1]), "+v"(zra), "+v"(vZ_), "+v"(nw), "+v"(nr));
  SB();
  asm volatile("buffer_load_dwordx4 %0, %1, %2 offen offset:1024 lds" :  : "v"(voB[1]), "s"(rW1), "s"(kw_[7]) : "memory"); SB();
  asm volatile("buffer_load_dwordx4 %0, %1, %2 offen offset:2048 lds" :  : "v"(voB[2]), "s"(rW1), "s"(kw_[7]) : "memory"); SB();
  asm volatile("s_add_u32 m0, %0, 24576" :  : "s"(mW) : "memory", "scc"); SB();
  asm volatile("s_nop 0\n\tbuffer_load_dwordx4 %0, %1, %2 offen lds" :  : "v"(voB[0]), "s"(rW1), "s"(kw_[8]) : "memory"); SB();
  asm volatile("buffer_load_dwordx4 %0, %1, %2 offen offset:1024 lds" :  : "v"(voB[1]), "s"(rW1), "s"(kw_[8]) : "memory"); SB();
  asm volatile("buffer_load_dwordx4 %0, %1, %2 offen offset:2048 lds" :  : "v"(voB[2]), "s"(rW1), "s"(kw_[8]) : "memory"); SB();
  asm volatile("s_waitcnt vmcnt(10)\n\t.Lc_%=:\n\tv_cmpx_eq_u64 vcc, %1, v[198:199]\n\tv_cmpx_eq_u64 vcc, %1, v[202:203]\n\tv_cmpx_eq_u64 vcc, %1, v[206:207]\n\tv_cmpx_eq_u64 vcc, %1, v[210:211]\n\ts_cmp_eq_u64 vcc, -1\n\ts_mov_b64 exec, -1\n\ts_cbranch_scc0 .Lr_%=\n\t.Ld_%=:\n\t.subsection 1\n\t.Lr_%=:\n\tv_add_u32 %0, -1, %0\n\ts_nop 1\n\tv_readfirstlane_b32 vcc_hi, %0\n\ts_nop 3\n\ts_cmp_lt_i32 vcc_hi, 1\n\ts_cbranch_scc1 .Ld_%=\n\ts_sleep 2\n\ts_movk_i32 vcc_lo, 0x1000\n\tbuffer_load_dwordx4 v[196:199], %2, %3, 0 offen offset:0 sc1\n\tbuffer_load_dwordx4 v[200:203], %2, %3, 0 offen offset:2048 sc1\n\tbuffer_load_dwordx4 v[204:207], %2, %3, vcc_lo offen offset:0 sc1\n\tbuffer_load_dwordx4 v[208:211], %2, %3, vcc_lo offen offset:2048 sc1\n\ts_waitcnt vmcnt(0)\n\ts_branch .Lc_%=\n\t.subsection 0" : "+v"(tmo) : "s"(tg), "v"(vS), "s"(rWS) : "memory", "vcc", "scc"); SB();
  ST(9);
  {
    f32x2 s4[2], dm[2];
  asm volatile("v_pk_add_f32 %0, v[196:197], v[200:201]\n\tv_pk_add_f32 %1, v[204:205], v[208:209]" : "=&v"(s4[0]), "=&v"(s4[1]) :  : ); SB();
    const f32x2 sm = s4[0] + s4[1];
    const float mean = red32(sm[0] + dppf<0x128>(sm[0])) * (1.0f / 16.0f);
  asm volatile("v_sub_f32 %0, v196, %4\n\tv_sub_f32 %1, v200, %4\n\tv_sub_f32 %2, v204, %4\n\tv_sub_f32 %3, v208, %4" : "=&v"(dm[0][0]), "=&v"(dm[0][1]), "=&v"(dm[1][0]), "=&v"(dm[1][1]) : "v"(mean) : ); SB();
    const f32x2 dq = __builtin_elementwise_fma(dm[0], dm[0], dm[1] * dm[1]);
    float qs = __builtin_fmaf(dq[0] + dq[1], 48.0f, sm[1]);
    qs = red32(qs + dppf<0x128>(qs));
    float r = __builtin_amdgcn_rcpf(rbf(__builtin_amdgcn_sqrtf(rbf(rbf(qs * (1.0f / 768.0f)) + 1e-5f))));
    float mr = -rbf(mean) * r;
    f32x4 rm[4];
    asm volatile("" : "+v"(r), "+v"(mr)); SB();
  asm volatile("s_mov_b64 exec, 0xff00ff\n\tds_write2_b32 %0, %1, %2 offset1:32\n\ts_mov_b64 exec, -1" :  : "v"(nw), "v"(r), "v"(mr) : "memory"); SB();
  asm volatile("ds_write_b64 %0, %1 offset:0" :  : "v"(zwa[0]), "v"(((u32x2){zp[0][0][0], zp[0][0][1]})) : "memory"); SB();
  asm volatile("ds_write_b64 %0, %1 offset:32" :  : "v"(zwa[0]), "v"(((u32x2){zp[0][1][0], zp[0][1][1]})) : "memory"); SB();
  asm volatile("ds_write_b64 %0, %1 offset:64" :  : "v"(zwa[0]), "v"(((u32x2){zp[0][2][0], zp[0][2][1]})) : "memory"); SB();
  asm volatile("s_add_u32 m0, %0, 98304" :  : "s"(mA) : "memory", "scc"); SB();
  asm volatile("s_nop 0\n\tbuffer_load_dwordx4 %0, %1, %2 offen lds" :  : "v"(voS), "s"(rA), "s"(ao[0]) : "memory"); SB();
  asm volatile("buffer_load_dwordx4 %0, %1, %2 offen offset:1024 lds" :  : "v"(voS), "s"(rA), "s"(ao[1]) : "memory"); SB();
  asm volatile("ds_write_b64 %0, %1 offset:0" :  : "v"(zwa[1]), "v"(((u32x2){zp[1][0][0], zp[1][0][1]})) : "memory"); SB();
  asm volatile("ds_write_b64 %0, %1 offset:32" :  : "v"(zwa[1]), "v"(((u32x2){zp[1][1][0], zp[1][1][1]})) : "memory"); SB();
  asm volatile("ds_write_b64 %0, %1 offset:64" :  : "v"(zwa[1]), "v"(((u32x2){zp[1][2][0], zp[1][2][1]})) : "memory"); SB();
  asm volatile("buffer_load_dwordx4 %0, %1, %2 offen offset:2048 lds" :  : "v"(voS), "s"(rA), "s"(ao[2]) : "memory"); SB();
  asm volatile("ds_read_b64_tr_b16 %0, %1 offset:0" : "=&v"(zt[0][0]) : "v"(zra) : "memory"); SB();
  asm volatile("ds_read_b64_tr_b16 %0, %1 offset:416" : "=&v"(zt[0][1]) : "v"(zra) : "memory"); SB();
  asm volatile("ds_read_b64_tr_b16 %0, %1 offset:32" : "=&v"(zt[1][0]) : "v"(zra) : "memory"); SB();
  asm volatile("ds_read_b64_tr_b16 %0, %1 offset:448" : "=&v"(zt[1][1]) : "v"(zra) : "memory"); SB();
  asm volatile("ds_read_b64_tr_b16 %0, %1 offset:64" : "=&v"(zt[2][0]) : "v"(zra) : "memory"); SB();
  asm volatile("ds_read_b64_tr_b16 %0, %1 offset:480" : "=&v"(zt[2][1]) : "v"(zra) : "memory"); SB();
  asm volatile("s_waitcnt lgkmcnt(6)\n\ts_barrier" :  :  : "memory"); SB();
  asm volatile("ds_read_b128 %0, %1 offset:0" : "=&v"(rm[0]) : "v"(nr) : "memory"); SB();
  asm volatile("ds_read_b128 %0, %1 offset:16" : "=&v"(rm[1]) : "v"(nr) : "memory"); SB();
  asm volatile("ds_read_b128 %0, %1 offset:128" : "=&v"(rm[2]) : "v"(nr) : "memory"); SB();
  asm volatile("ds_read_b128 %0, %1 offset:144" : "=&v"(rm[3]) : "v"(nr) : "memory"); SB();
  asm volatile("s_add_u32 m0, %0, 36864" :  : "s"(mW) : "memory", "scc"); SB();
  asm volatile("s_nop 0\n\tbuffer_load_dwordx4 %0, %1, %2 offen lds" :  : "v"(voB[0]), "s"(rW1), "s"(kw_[9]) : "memory"); SB();
  asm volatile("buffer_load_dwordx4 %0, %1, %2 offen offset:1024 lds" :  : "v"(voB[1]), "s"(rW1), "s"(kw_[9]) : "memory"); SB();
  asm volatile("s_waitcnt lgkmcnt(4)" : "+v"(zt[0][0]), "+v"(zt[0][1]), "+v"(zt[1][0]), "+v"(zt[1][1]), "+v"(zt[2][0]), "+v"(zt[2][1]) :  : "memory"); SB();
  asm volatile("s_waitcnt lgkmcnt(0)" : "+v"(rm[0]), "+v"(rm[1]), "+v"(rm[2]), "+v"(rm[3]) :  : "memory"); SB();
    const f32x2 re[4] = {{rm[0][0], rm[0][1]}, {rm[0][2], rm[0][3]}, {rm[1][0], rm[1][1]}, {rm[1][2], rm[1][3]}};
    const f32x2 me[4] = {{rm[2][0], rm[2][1]}, {rm[2][2], rm[2][3]}, {rm[3][0], rm[3][1]}, {rm[3][2], rm[3][3]}};
    u32x4 zo[3];
#pragma unroll
    for (int e = 0; e < 4; ++e) zo[0][e] = pk2(__builtin_elementwise_fma(unpk(zt[0][e >> 1][e & 1]), re[e], me[e]));
    SB();
  asm volatile("buffer_store_dwordx4 %1, %0, %2, %3 offen offset:0 sc1\n\ts_nop 1" :  : "v"(vZ_), "v"(zo[0]), "s"(rWS), "s"(0u) : "memory"); SB();
#pragma unroll
    for (int e = 0; e < 4; ++e) zo[1][e] = pk2(__builtin_elementwise_fma(unpk(zt[1][e >> 1][e & 1]), re[e], me[e]));
    SB();
  asm volatile("buffer_store_dwordx4 %1, %0, %2, %3 offen offset:1024 sc1\n\ts_nop 1" :  : "v"(vZ_), "v"(zo[1]), "s"(rWS), "s"(0u) : "memory"); SB();
#pragma unroll
    for (int e = 0; e < 4; ++e) zo[2][e] = pk2(__builtin_elementwise_fma(unpk(zt[2][e >> 1][e & 1]), re[e], me[e]));
    SB();
  asm volatile("buffer_store_dwordx4 %1, %0, %2, %3 offen offset:2048 sc1\n\ts_nop 1" :  : "v"(vZ_), "v"(zo[2]), "s"(rWS), "s"(0u) : "memory"); SB();
  }
  ST(10);
  asm volatile("v_mfma_f32_16x16x32_bf16 %0, v[184:187], a[48:51], %0" : "+v"(a1[0][0]) :  : ); SB();
  asm volatile("s_barrier" :  :  : "memory"); SB();
  asm volatile("ds_read_b64_tr_b16 v[172:173], %0 offset:24576" :  : "v"(aW[1][0][0]) : "memory"); SB();
  asm volatile("s_waitcnt vmcnt(14)\n\ts_mov_b64 exec, 1\n\tbuffer_store_dwordx2 %0, off, %1, %2 sc1\n\ts_mov_b64 exec, -1" :  : "v"(((u32x2){tg0, tg1})), "s"(rWS), "s"(sFK) : "memory"); SB();
  asm volatile("v_mfma_f32_16x16x32_bf16 %0, v[184:187], a[52:55], %0" : "+v"(a1[1][0]) :  : ); SB();
  asm volatile("ds_read_b64_tr_b16 v[174:175], %0 offset:25344" :  : "v"(aW[1][0][0]) : "memory"); SB();
  asm volatile("v_mfma_f32_16x16x32_bf16 %0, v[184:187], %1, %0" : "+v"(c1[0]) : "v"(ones) : ); SB();
  asm volatile("ds_read_b64_tr_b16 v[176:177], %0 offset:24576" :  : "v"(aW[1][0][1]) : "memory"); SB();
  asm volatile("v_mfma_f32_16x16x32_bf16 %0, v[188:191], a[48:51], %0" : "+v"(a1[0][1]) :  : ); SB();
  asm volatile("ds_read_b64_tr_b16 v[178:179], %0 offset:25344" :  : "v"(aW[1][0][1]) : "memory"); SB();
  asm volatile("v_mfma_f32_16x16x32_bf16 %0, v[188:191], a[52:55], %0" : "+v"(a1[1][1]) :  : ); SB();
  asm volatile("ds_read_b64_tr_b16 v[180:181], %0 offset:24576" :  : "v"(aW[1][0][2]) : "memory"); SB();
  asm volatile("v_mfma_f32_16x16x32_bf16 %0, v[188:191], %1, %0" : "+v"(c1[1]) : "v"(ones) : ); SB();
  asm volatile("ds_read_b64_tr_b16 v[182:183], %0 offset:25344" :  : "v"(aW[1][0][2]) : "memory"); SB();
  asm volatile("v_mfma_f32_16x16x32_bf16 %0, v[192:195], a[48:51], %0" : "+v"(a1[0][2]) :  : ); SB();
  ca = lb + 57344 + w * 192 + g4 * 16; asm volatile("" : "+v"(ca)); SB();
  asm volatile("v_mfma_f32_16x16x32_bf16 %0, v[192:195], a[52:55], %0" : "+v"(a1[1][2]) :  : ); SB();
  cr = lb + 57344 + (w ^ 2) * 192 + g4 * 16; asm volatile("" : "+v"(cr)); SB();
  asm volatile("v_mfma_f32_16x16x32_bf16 %0, v[192:195], %1, %0" : "+v"(c1[2]) : "v"(ones) : ); SB();
  asm volatile("buffer_load_dwordx4 %0, %1, %2 offen offset:2048 lds" :  : "v"(voB[2]), "s"(rW1), "s"(kw_[9]) : "memory"); SB();
  asm volatile("v_mfma_f32_16x16x32_bf16 %0, v[160:163], a[56:59], %0" : "+v"(a1[0][0]) :  : ); SB();
  asm volatile("ds_read_b64_tr_b16 v[184:185], %0 offset:24576" :  : "v"(aW[1][1][0]) : "memory"); SB();
  asm volatile("s_add_u32 m0, %0, 98304" :  : "s"(mA) : "memory", "scc"); SB();
  asm volatile("v_mfma_f32_16x16x32_bf16 %0, v[160:163], a[60:63], %0" : "+v"(a1[1][0]) :  : ); SB();
  asm volatile("ds_read_b64_tr_b16 v[186:187], %0 offset:25344" :  : "v"(aW[1][1][0]) : "memory"); SB();
  asm volatile("v_lshlrev_b32 %0, 16, v245" : "=v"(dD[0]) :  : ); SB();
  asm volatile("v_mfma_f32_16x16x32_bf16 %0, v[164:167], a[56:59], %0" : "+v"(a1[0][1]) :  : ); SB();
  asm volatile("ds_read_b64_tr_b16 v[188:189], %0 offset:24576" :  : "v"(aW[1][1][1]) : "memory"); SB();
  asm volatile("v_lshlrev_b32 %0, 16, v246" : "=v"(dD[1]) :  : ); SB();
  asm volatile("v_mfma_f32_16x16x32_bf16 %0, v[164:167], a[60:63], %0" : "+v"(a1[1][1]) :  : ); SB();
  asm volatile("ds_read_b64_tr_b16 v[190:191], %0 offset:25344" :  : "v"(aW[1][1][1]) : "memory"); SB();
  asm volatile("v_lshlrev_b32 %0, 16, v247" : "=v"(dD[2]) :  : ); SB();
  asm volatile("v_mfma_f32_16x16x32_bf16 %0, v[168:171], a[56:59], %0" : "+v"(a1[0][2]) :  : ); SB();
  asm volatile("ds_read_b64_tr_b16 v[192:193], %0 offset:24576" :  : "v"(aW[1][1][2]) : "memory"); SB();
  asm volatile("v_lshlrev_b32 %0, 16, v248" : "=v"(bs) :  : ); SB();
  asm volatile("v_mfma_f32_16x16x32_bf16 %0, v[168:171], a[60:63], %0" : "+v"(a1[1][2]) :  : ); SB();
  asm volatile("ds_read_b64_tr_b16 v[194:195], %0 offset:25344" :  : "v"(aW[1][1][2]) : "memory"); SB();
  asm volatile("s_waitcnt lgkmcnt(6)\n\tv_mfma_f32_16x16x32_bf16 %0, v[172:175], a[64:67], %0" : "+v"(a1[0][0]) :  : ); SB();
  asm volatile("ds_read_b64_tr_b16 v[160:161], %0 offset:36864" :  : "v"(aW[1][0][0]) : "memory"); SB();
  asm volatile("v_mov_b32_dpp v242, v231 wave_shr:1 row_mask:0xf bank_mask:0xf" :  :  : ); SB();
  asm volatile("v_mfma_f32_16x16x32_bf16 %0, v[172:175], a[68:71], %0" : "+v"(a1[1][0]) :  : ); SB();
  asm volatile("ds_read_b64_tr_b16 v[162:163], %0 offset:37632" :  : "v"(aW[1][0][0]) : "memory"); SB();
  asm volatile("buffer_load_dwordx4 %0, %1, %2 offen offset:3072 lds" :  : "v"(voS), "s"(rA), "s"(ao[3]) : "memory"); SB();
  asm volatile("v_mfma_f32_16x16x32_bf16 %0, v[172:175], %1, %0" : "+v"(c1[0]) : "v"(ones) : ); SB();
  asm volatile("ds_read_b64_tr_b16 v[164:165], %0 offset:36864" :  : "v"(aW[1][0][1]) : "memory"); SB();
  asm volatile("s_add_u32 m0, %0, 102400" :  : "s"(mA) : "memory", "scc"); SB();
  asm volatile("v_mfma_f32_16x16x32_bf16 %0, v[176:179], a[64:67], %0" : "+v"(a1[0][1]) :  : ); SB();
  asm volatile("ds_read_b64_tr_b16 v[166:167], %0 offset:37632" :  : "v"(aW[1][0][1]) : "memory"); SB();
  asm volatile("v_alignbit_b32 %0, v231, v231, 16" : "=v"(im[0][0]) :  : ); SB();
  asm volatile("v_mfma_f32_16x16x32_bf16 %0, v[176:179], a[68:71], %0" : "+v"(a1[1][1]) :  : ); SB();
  asm volatile("ds_read_b64_tr_b16 v[168:169], %0 offset:36864" :  : "v"(aW[1][0][2]) : "memory"); SB();
  asm volatile("v_alignbit_b32 %0, v230, v230, 16" : "=v"(im[0][1]) :  : ); SB();
  asm volatile("v_mfma_f32_16x16x32_bf16 %0, v[176:179], %1, %0" : "+v"(c1[1]) : "v"(ones) : ); SB();
  asm volatile("ds_read_b64_tr_b16 v[170:171], %0 offset:37632" :  : "v"(aW[1][0][2]) : "memory"); SB();
  asm volatile("buffer_load_dwordx4 %0, %1, %2 offen lds" :  : "v"(voS), "s"(rA), "s"(ao[4]) : "memory"); SB();
  asm volatile("v_mfma_f32_16x16x32_bf16 %0, v[180:183], a[64:67], %0" : "+v"(a1[0][2]) :  : ); SB();
  asm volatile("s_waitcnt vmcnt(10)" :  :  : "memory"); SB();
  asm volatile("ds_read_b128 a[192:195], %0 offset:0" :  : "v"(aA[0]) : "memory"); SB();
  asm volatile("v_mfma_f32_16x16x32_bf16 %0, v[180:183], a[68:71], %0" : "+v"(a1[1][2]) :  : ); SB();
  asm volatile("ds_read_b128 a[208:211], %0 offset:0" :  : "v"(aA[1]) : "memory"); SB();
  asm volatile("v_alignbit_b32 %0, v229, v229, 16" : "=v"(im[0][2]) :  : ); SB();
  asm volatile("v_mfma_f32_16x16x32_bf16 %0, v[180:183], %1, %0" : "+v"(c1[2]) : "v"(ones) : ); SB();
  asm volatile("buffer_load_dwordx4 %0, %1, %2 offen offset:1024 lds" :  : "v"(voS), "s"(rA), "s"(ao[5]) : "memory"); SB();
  asm volatile("s_waitcnt lgkmcnt(7)\n\tv_mfma_f32_16x16x32_bf16 %0, v[184:187], a[72:75], %0" : "+v"(a1[0][0]) :  : ); SB();
  asm volatile("ds_read_b64_tr_b16 v[172:173], %0 offset:36864" :  : "v"(aW[1][1][0]) : "memory"); SB();
  asm volatile("v_alignbit_b32 %0, v228, v228, 16" : "=v"(im[0][3]) :  : ); SB();
  asm volatile("v_mfma_f32_16x16x32_bf16 %0, v[184:187], a[76:79], %0" : "+v"(a1[1][0]) :  : ); SB();
  asm volatile("ds_read_b64_tr_b16 v[174:175], %0 offset:37632" :  : "v"(aW[1][1][0]) : "memory"); SB();
  asm volatile("v_bfi_b32 %0, %1, v231, v230" : "=v"(im[1][0]) : "s"(0xFFFFu) : ); SB();
  asm volatile("v_mfma_f32_16x16x32_bf16 %0, v[188:191], a[72:75], %0" : "+v"(a1[0][1]) :  : ); SB();
  asm volatile("ds_read_b64_tr_b16 v[176:177], %0 offset:36864" :  : "v"(aW[1][1][1]) : "memory"); SB();
  asm volatile("v_bfi_b32 %0, %1, v230, v229" : "=v"(im[1][1]) : "s"(0xFFFFu) : ); SB();
  asm volatile("v_mfma_f32_16x16x32_bf16 %0, v[188:191], a[76:79], %0" : "+v"(a1[1][1]) :  : ); SB();
  asm volatile("ds_read_b64_tr_b16 v[178:179], %0 offset:37632" :  : "v"(aW[1][1][1]) : "memory"); SB();
  asm volatile("v_bfi_b32 %0, %1, v229, v228" : "=v"(im[1][2]) : "s"(0xFFFFu) : ); SB();
  asm volatile("v_mfma_f32_16x16x32_bf16 %0, v[192:195], a[72:75], %0" : "+v"(a1[0][2]) :  : ); SB();
  asm volatile("ds_read_b64_tr_b16 v[180:181], %0 offset:36864" :  : "v"(aW[1][1][2]) : "memory"); SB();
  asm volatile("buffer_load_dwordx4 %0, %1, %2 offen offset:2048 lds" :  : "v"(voS), "s"(rA), "s"(ao[6]) : "memory"); SB();
  asm volatile("v_mfma_f32_16x16x32_bf16 %0, v[192:195], a[76:79], %0" : "+v"(a1[1][2]) :  : ); SB();
  asm volatile("ds_read_b64_tr_b16 v[182:183], %0 offset:37632" :  : "v"(aW[1][1][2]) : "memory"); SB();
  asm volatile("s_waitcnt lgkmcnt(8)\n\tv_mfma_f32_16x16x32_bf16 %0, v[160:163], a[80:83], %0" : "+v"(a1[0][0]) :  : ); SB();
  unsigned vFZ = W_FZ + cb * 1024 + w * 256 + (lane & 31) * 8;
  unsigned vB = W_Z2 + (w * 1024 + (cb & 1) * 128 + cb * 6) * 1024 + l16;
  unsigned ha = lb + 59520 + 4608 + (2040 - 8 * (w * 64 + lane)) * 2;
  unsigned za = lb + 59520 + min(w, 2u) * 12928 + (lane < 32 ? lane * 16 : 4608 + ((lane >> 1) & 1) * 4160 + 4096 + (lane & 1) * 16);
  unsigned ew[2] = {lb + 32768 + w * 6144 + wn * 3072 + l16, lb + 32768 + (w ^ 1) * 6144 + wn * 3072 + l16};
  unsigned aa[4], bb;
  {
    const unsigned c = (l15 + 1) & 1;
    const unsigned o = lb + 59520 + 2 * 12928 - w * 128 - 192;
#pragma unroll
    for (int k = 0; k < 4; ++k) aa[k] = o + 4608 + c * 4160 + (2031 - l15 + 8 * g4 - c) * 2 - 512 - 1024 * k;
    bb = o + 512 + 32 * l15 - 32 + 16 * g4;
  }
  unsigned dd = lb + 0 + w * 8192 + g4 * 256 + ((l15 ^ ((g4 >> 1) << 3)) << 4);
  asm volatile("" : "+v"(aa[0]), "+v"(aa[1]), "+v"(aa[2]), "+v"(aa[3]), "+v"(bb), "+v"(dd), "+v"(vFZ), "+v"(vB), "+v"(ha), "+v"(za), "+v"(ew[0]), "+v"(ew[1]));
  SB();
  asm volatile("buffer_load_dwordx4 %0, %1, %2 offen offset:3072 lds" :  : "v"(voS), "s"(rA), "s"(ao[7]) : "memory"); SB();
  asm volatile("v_xor_b32 %0, 384, %0" : "+v"(voS) :  : "memory"); SB();
  asm volatile("s_add_u32 m0, %0, 131072" :  : "s"(mA) : "memory", "scc"); SB();
  asm volatile("s_nop 0\n\tbuffer_load_dwordx4 %0, %1, %2 offen lds" :  : "v"(voS), "s"(rA), "s"(ao[0]) : "memory"); SB();
  asm volatile("s_barrier" :  :  : "memory"); SB();
  asm volatile("ds_read_b64_tr_b16 v[184:185], %0 offset:0" :  : "v"(aW[0][0][0]) : "memory"); SB();
  asm volatile("v_bfi_b32 %0, %1, v228, v242" : "=v"(im[1][3]) : "s"(0xFFFFu) : ); SB();
  asm volatile("v_mfma_f32_16x16x32_bf16 %0, v[160:163], a[84:87], %0" : "+v"(a1[1][0]) :  : ); SB();
  asm volatile("ds_read_b64_tr_b16 v[186:187], %0 offset:768" :  : "v"(aW[0][0][0]) : "memory"); SB();
  asm volatile("v_mov_b32_dpp v243, v235 wave_shr:1 row_mask:0xf bank_mask:0xf" :  :  : ); SB();
  asm volatile("v_mfma_f32_16x16x32_bf16 %0, v[160:163], %1, %0" : "+v"(c1[0]) : "v"(ones) : ); SB();
  asm volatile("ds_read_b64_tr_b16 v[188:189], %0 offset:0" :  : "v"(aW[0][0][1]) : "memory"); SB();
  asm volatile("v_alignbit_b32 %0, v235, v235, 16" : "=v"(im[2][0]) :  : ); SB();
  asm volatile("v_mfma_f32_16x16x32_bf16 %0, v[164:167], a[80:83], %0" : "+v"(a1[0][1]) :  : ); SB();
  asm volatile("ds_read_b64_tr_b16 v[190:191], %0 offset:768" :  : "v"(aW[0][0][1]) : "memory"); SB();
  asm volatile("v_alignbit_b32 %0, v234, v234, 16" : "=v"(im[2][1]) :  : ); SB();
  asm volatile("v_mfma_f32_16x16x32_bf16 %0, v[164:167], a[84:87], %0" : "+v"(a1[1][1]) :  : ); SB();
  asm volatile("ds_read_b64_tr_b16 v[192:193], %0 offset:0" :  : "v"(aW[0][0][2]) : "memory"); SB();
  asm volatile("v_alignbit_b32 %0, v233, v233, 16" : "=v"(im[2][2]) :  : ); SB();
  asm volatile("v_mfma_f32_16x16x32_bf16 %0, v[164:167], %1, %0" : "+v"(c1[1]) : "v"(ones) : ); SB();
  asm volatile("ds_read_b64_tr_b16 v[194:195], %0 offset:768" :  : "v"(aW[0][0][2]) : "memory"); SB();
  asm volatile("v_alignbit_b32 %0, v232, v232, 16" : "=v"(im[2][3]) :  : ); SB();
  asm volatile("v_mfma_f32_16x16x32_bf16 %0, v[168:171], a[80:83], %0" : "+v"(a1[0][2]) :  : ); SB();
  asm volatile("buffer_load_dwordx4 %0, %1, %2 offen offset:1024 lds" :  : "v"(voS), "s"(rA), "s"(ao[1]) : "memory"); SB();
  asm volatile("v_bfi_b32 %0, %1, v235, v234" : "=v"(im[3][0]) : "s"(0xFFFFu) : ); SB();
  asm volatile("v_mfma_f32_16x16x32_bf16 %0, v[168:171], a[84:87], %0" : "+v"(a1[1][2]) :  : ); SB();
  asm volatile("buffer_load_dwordx4 %0, %1, %2 offen offset:2048 lds" :  : "v"(voS), "s"(rA), "s"(ao[2]) : "memory"); SB();
  asm volatile("v_bfi_b32 %0, %1, v234, v233" : "=v"(im[3][1]) : "s"(0xFFFFu) : ); SB();
  asm volatile("v_mfma_f32_16x16x32_bf16 %0, v[168:171], %1, %0" : "+v"(c1[2]) : "v"(ones) : ); SB();
  asm volatile("v_bfi_b32 %0, %1, v233, v232" : "=v"(im[3][2]) : "s"(0xFFFFu) : ); SB();
  asm volatile("s_waitcnt lgkmcnt(5)\n\tv_mfma_f32_16x16x32_bf16 %0, v[172:175], a[88:91], %0" : "+v"(a1[0][0]) :  : ); SB();
  asm volatile("ds_read_b64_tr_b16 v[160:161], %0 offset:0" :  : "v"(aW[0][1][0]) : "memory"); SB();
  asm volatile("v_bfi_b32 %0, %1, v232, v243" : "=v"(im[3][3]) : "s"(0xFFFFu) : ); SB();
  asm volatile("v_mfma_f32_16x16x32_bf16 %0, v[172:175], a[92:95], %0" : "+v"(a1[1][0]) :  : ); SB();
  asm volatile("ds_read_b64_tr_b16 v[162:163], %0 offset:768" :  : "v"(aW[0][1][0]) : "memory"); SB();
  asm volatile("v_mov_b32_dpp v244, v239 wave_shr:1 row_mask:0xf bank_mask:0xf" :  :  : ); SB();
  asm volatile("v_mfma_f32_16x16x32_bf16 %0, v[176:179], a[88:91], %0" : "+v"(a1[0][1]) :  : ); SB();
  asm volatile("ds_read_b64_tr_b16 v[164:165], %0 offset:0" :  : "v"(aW[0][1][1]) : "memory"); SB();
  asm volatile("v_alignbit_b32 %0, v239, v239, 16" : "=v"(im[4][0]) :  : ); SB();
  asm volatile("v_mfma_f32_16x16x32_bf16 %0, v[176:179], a[92:95], %0" : "+v"(a1[1][1]) :  : ); SB();
  asm volatile("ds_read_b64_tr_b16 v[166:167], %0 offset:768" :  : "v"(aW[0][1][1]) : "memory"); SB();
  asm volatile("v_alignbit_b32 %0, v238, v238, 16" : "=v"(im[4][1]) :  : ); SB();
  asm volatile("v_mfma_f32_16x16x32_bf16 %0, v[180:183], a[88:91], %0" : "+v"(a1[0][2]) :  : ); SB();
  asm volatile("ds_read_b64_tr_b16 v[168:169], %0 offset:0" :  : "v"(aW[0][1][2]) : "memory"); SB();
  asm volatile("v_alignbit_b32 %0, v237, v237, 16" : "=v"(im[4][2]) :  : ); SB();
  asm volatile("v_mfma_f32_16x16x32_bf16 %0, v[180:183], a[92:95], %0" : "+v"(a1[1][2]) :  : ); SB();
  asm volatile("ds_read_b64_tr_b16 v[170:171], %0 offset:768" :  : "v"(aW[0][1][2]) : "memory"); SB();
  asm volatile("s_waitcnt lgkmcnt(6)\n\tv_mfma_f32_16x16x32_bf16 %0, v[184:187], a[96:99], %0" : "+v"(a1[0][0]) :  : ); SB();
  asm volatile("ds_read_b64_tr_b16 v[172:173], %0 offset:12288" :  : "v"(aW[0][0][0]) : "memory"); SB();
  asm volatile("v_alignbit_b32 %0, v236, v236, 16" : "=v"(im[4][3]) :  : ); SB();
  asm volatile("v_mfma_f32_16x16x32_bf16 %0, v[184:187], a[100:103], %0" : "+v"(a1[1][0]) :  : ); SB();
  asm volatile("ds_read_b64_tr_b16 v[174:175], %0 offset:13056" :  : "v"(aW[0][0][0]) : "memory"); SB();
  asm volatile("v_bfi_b32 %0, %1, v239, v238" : "=v"(im[5][0]) : "s"(0xFFFFu) : ); SB();
  asm volatile("v_mfma_f32_16x16x32_bf16 %0, v[184:187], %1, %0" : "+v"(c1[0]) : "v"(ones) : ); SB();
  asm volatile("ds_read_b64_tr_b16 v[176:177], %0 offset:12288" :  : "v"(aW[0][0][1]) : "memory"); SB();
  asm volatile("v_bfi_b32 %0, %1, v238, v237" : "=v"(im[5][1]) : "s"(0xFFFFu) : ); SB();
  asm volatile("v_mfma_f32_16x16x32_bf16 %0, v[188:191], a[96:99], %0" : "+v"(a1[0][1]) :  : ); SB();
  asm volatile("ds_read_b64_tr_b16 v[178:179], %0 offset:13056" :  : "v"(aW[0][0][1]) : "memory"); SB();
  asm volatile("v_bfi_b32 %0, %1, v237, v236" : "=v"(im[5][2]) : "s"(0xFFFFu) : ); SB();
  asm volatile("v_mfma_f32_16x16x32_bf16 %0, v[188:191], a[100:103], %0" : "+v"(a1[1][1]) :  : ); SB();
  asm volatile("ds_read_b64_tr_b16 v[180:181], %0 offset:12288" :  : "v"(aW[0][0][2]) : "memory"); SB();
  asm volatile("v_bfi_b32 %0, %1, v236, v244" : "=v"(im[5][3]) : "s"(0xFFFFu) : ); SB();
  asm volatile("v_mfma_f32_16x16x32_bf16 %0, v[188:191], %1, %0" : "+v"(c1[1]) : "v"(ones) : ); SB();
  asm volatile("ds_read_b64_tr_b16 v[182:183], %0 offset:13056" :  : "v"(aW[0][0][2]) : "memory"); SB();
  asm volatile("v_mov_b32 %0, 0" : "=v"(zero4[0]) :  : ); SB();
  asm volatile("v_mfma_f32_16x16x32_bf16 %0, v[192:195], a[96:99], %0" : "+v"(a1[0][2]) :  : ); SB();
  asm volatile("buffer_load_dwordx4 %0, %1, %2 offen offset:3072 lds" :  : "v"(voS), "s"(rA), "s"(ao[3]) : "memory"); SB();
  asm volatile("s_add_u32 m0, %0, 135168" :  : "s"(mA) : "memory", "scc"); SB();
  asm volatile("v_mfma_f32_16x16x32_bf16 %0, v[192:195], a[100:103], %0" : "+v"(a1[1][2]) :  : ); SB();
  asm volatile("buffer_load_dwordx4 %0, %1, %2 offen lds" :  : "v"(voS), "s"(rA), "s"(ao[4]) : "memory"); SB();
  asm volatile("s_waitcnt vmcnt(12)\n\ts_mov_b64 exec, 1\n\tbuffer_store_dwordx2 %0, off, %1, %2 sc1\n\ts_mov_b64 exec, -1" :  : "v"(((u32x2){tg0, tg1})), "s"(rWS), "s"(sFZ) : "memory"); SB();
  asm volatile("v_mfma_f32_16x16x32_bf16 %0, v[192:195], %1, %0" : "+v"(c1[2]) : "v"(ones) : ); SB();
  asm volatile("v_mov_b32 %0, 0" : "=v"(zero4[1]) :  : ); SB();
  asm volatile("v_mov_b32 %0, 0" : "=v"(zero4[2]) :  : ); SB();
  asm volatile("v_mov_b32 %0, 0" : "=v"(zero4[3]) :  : ); SB();
  asm volatile("s_waitcnt lgkmcnt(4)\n\tv_mfma_f32_16x16x32_bf16 %0, v[160:163], a[104:107], %0" : "+v"(a1[0][0]) :  : ); SB();
  asm volatile("ds_read_b64_tr_b16 v[184:185], %0 offset:12288" :  : "v"(aW[0][1][0]) : "memory"); SB();
  asm volatile("v_mfma_f32_16x16x32_bf16 %0, v[160:163], a[108:111], %0" : "+v"(a1[1][0]) :  : ); SB();
  asm volatile("s_mov_b32 exec_lo, 0x10001\n\ts_mov_b32 exec_hi, 0x10001\n\tds_write_b128 %0, %1 offset:0\n\ts_mov_b64 exec, -1" :  : "v"(ca), "v"(c1[0]) : "memory"); SB();
  asm volatile("ds_read_b64_tr_b16 v[186:187], %0 offset:13056" :  : "v"(aW[0][1][0]) : "memory"); SB();
  asm volatile("v_mfma_f32_16x16x32_bf16 %0, v[164:167], a[104:107], %0" : "+v"(a1[0][1]) :  : ); SB();
  asm volatile("ds_read_b64_tr_b16 v[188:189], %0 offset:12288" :  : "v"(aW[0][1][1]) : "memory"); SB();
  asm volatile("v_mfma_f32_16x16x32_bf16 %0, v[164:167], a[108:111], %0" : "+v"(a1[1][1]) :  : ); SB();
  asm volatile("s_mov_b32 exec_lo, 0x10001\n\ts_mov_b32 exec_hi, 0x10001\n\tds_write_b128 %0, %1 offset:64\n\ts_mov_b64 exec, -1" :  : "v"(ca), "v"(c1[1]) : "memory"); SB();
  asm volatile("ds_read_b64_tr_b16 v[190:191], %0 offset:13056" :  : "v"(aW[0][1][1]) : "memory"); SB();
  asm volatile("buffer_load_dwordx4 %0, %1, %2 offen offset:1024 lds" :  : "v"(voS), "s"(rA), "s"(ao[5]) : "memory"); SB();
  asm volatile("v_mfma_f32_16x16x32_bf16 %0, v[168:171], a[104:107], %0" : "+v"(a1[0][2]) :  : ); SB();
  asm volatile("ds_read_b64_tr_b16 v[192:193], %0 offset:12288" :  : "v"(aW[0][1][2]) : "memory"); SB();
  asm volatile("v_mfma_f32_16x16x32_bf16 %0, v[168:171], a[108:111], %0" : "+v"(a1[1][2]) :  : ); SB();
  asm volatile("s_mov_b32 exec_lo, 0x10001\n\ts_mov_b32 exec_hi, 0x10001\n\tds_write_b128 %0, %1 offset:128\n\ts_mov_b64 exec, -1" :  : "v"(ca), "v"(c1[2]) : "memory"); SB();
  asm volatile("ds_read_b64_tr_b16 v[194:195], %0 offset:13056" :  : "v"(aW[0][1][2]) : "memory"); SB();
  asm volatile("s_waitcnt vmcnt(11)" :  :  : "memory"); SB();
  asm volatile("v_mfma_f32_16x16x32_bf16 %0, v[172:175], a[112:115], %0" : "+v"(a1[0][0]) :  : ); SB();
  asm volatile("ds_read_b128 a[196:199], %0 offset:2048" :  : "v"(aA[0]) : "memory"); SB();
  asm volatile("v_mfma_f32_16x16x32_bf16 %0, v[172:175], a[116:119], %0" : "+v"(a1[1][0]) :  : ); SB();
  asm volatile("ds_read_b128 a[212:215], %0 offset:2048" :  : "v"(aA[1]) : "memory"); SB();
  asm volatile("v_mfma_f32_16x16x32_bf16 %0, v[172:175], %1, %0" : "+v"(c1[0]) : "v"(ones) : ); SB();
  asm volatile("buffer_load_dwordx4 %0, %1, %2 offen offset:2048 lds" :  : "v"(voS), "s"(rA), "s"(ao[6]) : "memory"); SB();
  asm volatile("s_waitcnt lgkmcnt(7)\n\tv_mfma_f32_16x16x32_bf16 %0, v[176:179], a[112:115], %0" : "+v"(a1[0][1]) :  : ); SB();
  asm volatile("s_waitcnt lgkmcnt(3)\n\ts_barrier" :  :  : "memory"); SB();
  asm volatile("ds_read_b64_tr_b16 v[160:161], %0 offset:24576" :  : "v"(aW[0][0][0]) : "memory"); SB();
  asm volatile("s_waitcnt vmcnt(10)" :  :  : "memory"); SB();
  asm volatile("v_mfma_f32_16x16x32_bf16 %0, v[176:179], a[116:119], %0" : "+v"(a1[1][1]) :  : ); SB();
  asm volatile("ds_read_b64_tr_b16 v[162:163], %0 offset:25344" :  : "v"(aW[0][0][0]) : "memory"); SB();
  asm volatile("ds_read_b128 a[200:203], %0 offset:4096" :  : "v"(aA[0]) : "memory"); SB();
  asm volatile("v_mfma_f32_16x16x32_bf16 %0, v[176:179], %1, %0" : "+v"(c1[1]) : "v"(ones) : ); SB();
  asm volatile("ds_read_b64_tr_b16 v[164:165], %0 offset:24576" :  : "v"(aW[0][0][1]) : "memory"); SB();
  asm volatile("ds_read_b128 a[216:219], %0 offset:4096" :  : "v"(aA[1]) : "memory"); SB();
  asm volatile("v_mfma_f32_16x16x32_bf16 %0, v[180:183], a[112:115], %0" : "+v"(a1[0][2]) :  : ); SB();
  asm volatile("ds_read_b64_tr_b16 v[166:167], %0 offset:25344" :  : "v"(aW[0][0][1]) : "memory"); SB();
  asm volatile("s_waitcnt vmcnt(8)" :  :  : "memory"); SB();
  asm volatile("v_mfma_f32_16x16x32_bf16 %0, v[180:183], a[116:119], %0" : "+v"(a1[1][2]) :  : ); SB();
  asm volatile("ds_read_b64_tr_b16 v[168:169], %0 offset:24576" :  : "v"(aW[0][0][2]) : "memory"); SB();
  asm volatile("ds_read_b128 a[204:207], %0 offset:6144" :  : "v"(aA[0]) : "memory"); SB();
  asm volatile("v_mfma_f32_16x16x32_bf16 %0, v[180:183], %1, %0" : "+v"(c1[2]) : "v"(ones) : ); SB();
  asm volatile("ds_read_b64_tr_b16 v[170:171], %0 offset:25344" :  : "v"(aW[0][0][2]) : "memory"); SB();
  asm volatile("ds_read_b128 a[220:223], %0 offset:6144" :  : "v"(aA[1]) : "memory"); SB();
  asm volatile("v_mfma_f32_16x16x32_bf16 %0, v[184:187], a[120:123], %0" : "+v"(a1[0][0]) :  : ); SB();
  asm volatile("buffer_load_dwordx4 %0, %1, %2 offen offset:3072 lds" :  : "v"(voS), "s"(rA), "s"(ao[7]) : "memory"); SB();
  asm volatile("v_mfma_f32_16x16x32_bf16 %0, v[184:187], a[124:127], %0" : "+v"(a1[1][0]) :  : ); SB();
  asm volatile("v_xor_b32 %0, 128, %0" : "+v"(voS) :  : "memory"); SB();
  asm volatile("s_add_u32 m0, %0, 98304" :  : "s"(mA) : "memory", "scc"); SB();
  asm volatile("v_mfma_f32_16x16x32_bf16 %0, v[184:187], %1, %0" : "+v"(c1[0]) : "v"(ones) : ); SB();
  asm volatile("buffer_load_dwordx4 %0, %1, %2 offen lds" :  : "v"(voS), "s"(rA), "s"(ao[0]) : "memory"); SB();
  asm volatile("v_mfma_f32_16x16x32_bf16 %0, v[188:191], a[120:123], %0" : "+v"(a1[0][1]) :  : ); SB();
  asm volatile("ds_read_b64_tr_b16 v[172:173], %0 offset:24576" :  : "v"(aW[0][1][0]) : "memory"); SB();
  asm volatile("v_mfma_f32_16x16x32_bf16 %0, v[188:191], a[124:127], %0" : "+v"(a1[1][1]) :  : ); SB();
  asm volatile("ds_read_b64_tr_b16 v[174:175], %0 offset:25344" :  : "v"(aW[0][1][0]) : "memory"); SB();
  asm volatile("s_waitcnt vmcnt(8)" :  :  : "memory"); SB();
  asm volatile("v_mfma_f32_16x16x32_bf16 %0, v[188:191], %1, %0" : "+v"(c1[1]) : "v"(ones) : ); SB();
  asm volatile("ds_read_b64_tr_b16 v[176:177], %0 offset:24576" :  : "v"(aW[0][1][1]) : "memory"); SB();
  asm volatile("ds_read_b128 a[224:227], %0 offset:32768" :  : "v"(aA[0]) : "memory"); SB();
  asm volatile("s_waitcnt lgkmcnt(8)\n\tv_mfma_f32_16x16x32_bf16 %0, v[192:195], a[120:123], %0" : "+v"(a1[0][2]) :  : ); SB();
  asm volatile("ds_read_b64_tr_b16 v[178:179], %0 offset:25344" :  : "v"(aW[0][1][1]) : "memory"); SB();
  asm volatile("ds_read_b128 a[240:243], %0 offset:32768" :  : "v"(aA[1]) : "memory"); SB();
  asm volatile("v_mfma_f32_16x16x32_bf16 %0, v[192:195], a[124:127], %0" : "+v"(a1[1][2]) :  : ); SB();
  asm volatile("ds_read_b64_tr_b16 v[180:181], %0 offset:24576" :  : "v"(aW[0][1][2]) : "memory"); SB();
  asm volatile("buffer_load_dwordx4 %0, %1, %2 offen offset:1024 lds" :  : "v"(voS), "s"(rA), "s"(ao[1]) : "memory"); SB();
  asm volatile("v_mfma_f32_16x16x32_bf16 %0, v[192:195], %1, %0" : "+v"(c1[2]) : "v"(ones) : ); SB();
  asm volatile("ds_read_b64_tr_b16 v[182:183], %0 offset:25344" :  : "v"(aW[0][1][2]) : "memory"); SB();
  asm volatile("buffer_load_dwordx4 %0, %1, %2 offen offset:2048 lds" :  : "v"(voS), "s"(rA), "s"(ao[2]) : "memory"); SB();
  asm volatile("v_mfma_f32_16x16x32_bf16 %0, v[160:163], a[128:131], %0" : "+v"(a1[0][0]) :  : ); SB();
  asm volatile("ds_read_b128 %0, %1 offset:0" : "=&v"(co[0]) : "v"(cr) : "memory"); SB();
  asm volatile("v_mfma_f32_16x16x32_bf16 %0, v[160:163], a[132:135], %0" : "+v"(a1[1][0]) :  : ); SB();
  asm volatile("ds_read_b128 %0, %1 offset:64" : "=&v"(co[1]) : "v"(cr) : "memory"); SB();
  asm volatile("v_mfma_f32_16x16x32_bf16 %0, v[160:163], %1, %0" : "+v"(c1[0]) : "v"(ones) : ); SB();
  asm volatile("buffer_load_dwordx4 %0, %1, %2 offen offset:3072 lds" :  : "v"(voS), "s"(rA), "s"(ao[3]) : "memory"); SB();
  asm volatile("v_mfma_f32_16x16x32_bf16 %0, v[164:167], a[128:131], %0" : "+v"(a1[0][1]) :  : ); SB();
  asm volatile("ds_read_b64_tr_b16 v[184:185], %0 offset:36864" :  : "v"(aW[0][0][0]) : "memory"); SB();
  asm volatile("v_mfma_f32_16x16x32_bf16 %0, v[164:167], a[132:135], %0" : "+v"(a1[1][1]) :  : ); SB();
  asm volatile("ds_read_b64_tr_b16 v[186:187], %0 offset:37632" :  : "v"(aW[0][0][0]) : "memory"); SB();
  asm volatile("v_mfma_f32_16x16x32_bf16 %0, v[164:167], %1, %0" : "+v"(c1[1]) : "v"(ones) : ); SB();
  asm volatile("ds_read_b64_tr_b16 v[188:189], %0 offset:36864" :  : "v"(aW[0][0][1]) : "memory"); SB();
  asm volatile("s_add_u32 m0, %0, 102400" :  : "s"(mA) : "memory", "scc"); SB();
  asm volatile("s_waitcnt lgkmcnt(8)\n\tv_mfma_f32_16x16x32_bf16 %0, v[168:171], a[128:131], %0" : "+v"(a1[0][2]) :  : ); SB();
  asm volatile("ds_read_b64_tr_b16 v[190:191], %0 offset:37632" :  : "v"(aW[0][0][1]) : "memory"); SB();
  asm volatile("buffer_load_dwordx4 %0, %1, %2 offen lds" :  : "v"(voS), "s"(rA), "s"(ao[4]) : "memory"); SB();
  asm volatile("v_mfma_f32_16x16x32_bf16 %0, v[168:171], a[132:135], %0" : "+v"(a1[1][2]) :  : ); SB();
  asm volatile("ds_read_b64_tr_b16 v[192:193], %0 offset:36864" :  : "v"(aW[0][0][2]) : "memory"); SB();
  asm volatile("v_mfma_f32_16x16x32_bf16 %0, v[168:171], %1, %0" : "+v"(c1[2]) : "v"(ones) : ); SB();
  asm volatile("ds_read_b64_tr_b16 v[194:195], %0 offset:37632" :  : "v"(aW[0][0][2]) : "memory"); SB();
  asm volatile("buffer_load_dwordx4 %0, %1, %2 offen offset:1024 lds" :  : "v"(voS), "s"(rA), "s"(ao[5]) : "memory"); SB();
  asm volatile("v_mfma_f32_16x16x32_bf16 %0, v[172:175], a[136:139], %0" : "+v"(a1[0][0]) :  : ); SB();
  asm volatile("ds_read_b128 %0, %1 offset:128" : "=&v"(co[2]) : "v"(cr) : "memory"); SB();
  asm volatile("v_mfma_f32_16x16x32_bf16 %0, v[172:175], a[140:143], %0" : "+v"(a1[1][0]) :  : ); SB();
  asm volatile("ds_write_b128 %0, %1 offset:0" :  : "v"(ha), "v"(im[0]) : "memory"); SB();
  asm volatile("ds_write_b128 %0, %1 offset:4160" :  : "v"(ha), "v"(im[1]) : "memory"); SB();
  asm volatile("buffer_load_dwordx2 %0, %1, %2, %3 offen sc1" : "=&v"(fk) : "v"(vFZ), "s"(rWS), "s"(8192u) : "memory"); SB();
  asm volatile("s_waitcnt vmcnt(12)" :  :  : "memory"); SB();
  asm volatile("v_mfma_f32_16x16x32_bf16 %0, v[172:175], %1, %0" : "+v"(c1[0]) : "v"(ones) : ); SB();
  asm volatile("ds_write_b128 %0, %1 offset:12928" :  : "v"(ha), "v"(im[2]) : "memory"); SB();
  asm volatile("ds_write_b128 %0, %1 offset:17088" :  : "v"(ha), "v"(im[3]) : "memory"); SB();
  asm volatile("ds_read_b128 a[228:231], %0 offset:34816" :  : "v"(aA[0]) : "memory"); SB();
  asm volatile("v_mfma_f32_16x16x32_bf16 %0, v[176:179], a[136:139], %0" : "+v"(a1[0][1]) :  : ); SB();
  asm volatile("ds_read_b64_tr_b16 v[160:161], %0 offset:36864" :  : "v"(aW[0][1][0]) : "memory"); SB();
  asm volatile("ds_read_b128 a[244:247], %0 offset:34816" :  : "v"(aA[1]) : "memory"); SB();
  asm volatile("v_mfma_f32_16x16x32_bf16 %0, v[176:179], a[140:143], %0" : "+v"(a1[1][1]) :  : ); SB();
  asm volatile("ds_read_b64_tr_b16 v[162:163], %0 offset:37632" :  : "v"(aW[0][1][0]) : "memory"); SB();
  asm volatile("buffer_load_dwordx4 %0, %1, %2 offen offset:2048 lds" :  : "v"(voS), "s"(rA), "s"(ao[6]) : "memory"); SB();
  asm volatile("v_mfma_f32_16x16x32_bf16 %0, v[176:179], %1, %0" : "+v"(c1[1]) : "v"(ones) : ); SB();
  asm volatile("ds_write_b128 %0, %1 offset:25856" :  : "v"(ha), "v"(im[4]) : "memory"); SB();
  asm volatile("ds_read_b64_tr_b16 v[164:165], %0 offset:36864" :  : "v"(aW[0][1][1]) : "memory"); SB();
  asm volatile("s_waitcnt vmcnt(10)" :  :  : "memory"); SB();
  asm volatile("s_waitcnt lgkmcnt(8)\n\tv_mfma_f32_16x16x32_bf16 %0, v[180:183], a[136:139], %0" : "+v"(a1[0][2]) :  : ); SB();
  asm volatile("ds_read_b64_tr_b16 v[166:167], %0 offset:37632" :  : "v"(aW[0][1][1]) : "memory"); SB();
  asm volatile("ds_read_b128 a[232:235], %0 offset:36864" :  : "v"(aA[0]) : "memory"); SB();
  asm volatile("v_mfma_f32_16x16x32_bf16 %0, v[180:183], a[140:143], %0" : "+v"(a1[1][2]) :  : ); SB();
  asm volatile("ds_read_b64_tr_b16 v[168:169], %0 offset:36864" :  : "v"(aW[0][1][2]) : "memory"); SB();
  asm volatile("ds_read_b128 a[248:251], %0 offset:36864" :  : "v"(aA[1]) : "memory"); SB();
  asm volatile("v_mfma_f32_16x16x32_bf16 %0, v[180:183], %1, %0" : "+v"(c1[2]) : "v"(ones) : ); SB();
  asm volatile("ds_read_b64_tr_b16 v[170:171], %0 offset:37632" :  : "v"(aW[0][1][2]) : "memory"); SB();
  asm volatile("buffer_load_dwordx4 %0, %1, %2 offen offset:3072 lds" :  : "v"(voS), "s"(rA), "s"(ao[7]) : "memory"); SB();
  asm volatile("v_mfma_f32_16x16x32_bf16 %0, v[184:187], a[144:147], %0" : "+v"(a1[0][0]) :  : ); SB();
  asm volatile("v_xor_b32 %0, 896, %0" : "+v"(voS) :  : "memory"); SB();
  asm volatile("s_add_u32 m0, %0, 131072" :  : "s"(mA) : "memory", "scc"); SB();
  asm volatile("v_mfma_f32_16x16x32_bf16 %0, v[184:187], a[148:151], %0" : "+v"(a1[1][0]) :  : ); SB();
  asm volatile("ds_write_b128 %0, %1 offset:30016" :  : "v"(ha), "v"(im[5]) : "memory"); SB();
  asm volatile("buffer_load_dwordx4 %0, %1, %2 offen lds" :  : "v"(voS), "s"(rA), "s"(ao[0]) : "memory"); SB();
  asm volatile("v_mfma_f32_16x16x32_bf16 %0, v[184:187], %1, %0" : "+v"(c1[0]) : "v"(ones) : ); SB();
  asm volatile("buffer_load_dwordx4 %0, %1, %2 offen offset:1024 lds" :  : "v"(voS), "s"(rA), "s"(ao[1]) : "memory"); SB();
  asm volatile("s_waitcnt lgkmcnt(6)" :  :  : "memory"); SB();
  asm volatile("v_mfma_f32_16x16x32_bf16 %0, v[188:191], a[144:147], %0" : "+v"(a1[0][1]) :  : ); SB();
  asm volatile("v_mfma_f32_16x16x32_bf16 %0, v[188:191], a[148:151], %0" : "+v"(a1[1][1]) :  : ); SB();
  asm volatile("v_mfma_f32_16x16x32_bf16 %0, v[188:191], %1, %0" : "+v"(c1[1]) : "v"(ones) : ); SB();
  asm volatile("buffer_load_dwordx4 %0, %1, %2 offen offset:2048 lds" :  : "v"(voS), "s"(rA), "s"(ao[2]) : "memory"); SB();
  asm volatile("v_mfma_f32_16x16x32_bf16 %0, v[192:195], a[144:147], %0" : "+v"(a1[0][2]) :  : ); SB();
  asm volatile("ds_write_b128 %0, %1 offset:0" :  : "v"(za), "v"(zero4) : "memory"); SB();
  asm volatile("v_mfma_f32_16x16x32_bf16 %0, v[192:195], a[148:151], %0" : "+v"(a1[1][2]) :  : ); SB();
  asm volatile("buffer_load_dwordx4 %0, %1, %2 offen offset:3072 lds" :  : "v"(voS), "s"(rA), "s"(ao[3]) : "memory"); SB();
  asm volatile("s_waitcnt lgkmcnt(2)" :  :  : "memory"); SB();
  asm volatile("v_mfma_f32_16x16x32_bf16 %0, v[192:195], %1, %0" : "+v"(c1[2]) : "v"(ones) : ); SB();
  asm volatile("v_mfma_f32_16x16x32_bf16 %0, v[160:163], a[152:155], %0" : "+v"(a1[0][0]) :  : ); SB();
  asm volatile("v_mfma_f32_16x16x32_bf16 %0, v[160:163], a[156:159], %0" : "+v"(a1[1][0]) :  : ); SB();
  asm volatile("v_mfma_f32_16x16x32_bf16 %0, v[160:163], %1, %0" : "+v"(c1[0]) : "v"(ones) : ); SB();
  asm volatile("s_add_u32 m0, %0, 135168" :  : "s"(mA) : "memory", "scc"); SB();
  asm volatile("v_mfma_f32_16x16x32_bf16 %0, v[164:167], a[152:155], %0" : "+v"(a1[0][1]) :  : ); SB();
  asm volatile("buffer_load_dwordx4 %0, %1, %2 offen lds" :  : "v"(voS), "s"(rA), "s"(ao[4]) : "memory"); SB();
  asm volatile("s_waitcnt vmcnt(14)" :  :  : "memory"); SB();
  asm volatile("v_mfma_f32_16x16x32_bf16 %0, v[164:167], a[156:159], %0" : "+v"(a1[1][1]) :  : ); SB();
  asm volatile("ds_read_b128 a[236:239], %0 offset:38912" :  : "v"(aA[0]) : "memory"); SB();
  asm volatile("v_mfma_f32_16x16x32_bf16 %0, v[164:167], %1, %0" : "+v"(c1[1]) : "v"(ones) : ); SB();
  asm volatile("ds_read_b128 a[252:255], %0 offset:38912" :  : "v"(aA[1]) : "memory"); SB();
  asm volatile("v_mfma_f32_16x16x32_bf16 %0, v[168:171], a[152:155], %0" : "+v"(a1[0][2]) :  : ); SB();
  asm volatile("buffer_load_dwordx4 %0, %1, %2 offen offset:1024 lds" :  : "v"(voS), "s"(rA), "s"(ao[5]) : "memory"); SB();
  asm volatile("v_mfma_f32_16x16x32_bf16 %0, v[168:171], a[156:159], %0" : "+v"(a1[1][2]) :  : ); SB();
  asm volatile("v_mfma_f32_16x16x32_bf16 %0, v[168:171], %1, %0" : "+v"(c1[2]) : "v"(ones) : ); SB();
  ST(11);
  f32x4 ac[8];
  u32x2 cA[2][8][2];
  asm volatile("s_waitcnt vmcnt(13)" :  :  : "memory"); SB();
  asm volatile("ds_read_b128 a[0:3], %0 offset:0" :  : "v"(aA[0]) : "memory"); SB();
  asm volatile("ds_read_b128 a[16:19], %0 offset:0" :  : "v"(aA[1]) : "memory"); SB();
  asm volatile("s_waitcnt lgkmcnt(2)" :  :  : "memory"); SB();
  asm volatile("buffer_load_dwordx4 %0, %1, %2 offen offset:2048 lds" :  : "v"(voS), "s"(rA), "s"(ao[6]) : "memory"); SB();
  asm volatile("buffer_load_dwordx4 %0, %1, %2 offen offset:3072 lds" :  : "v"(voS), "s"(rA), "s"(ao[7]) : "memory"); SB();
  asm volatile("buffer_load_dwordx2 %0, %1, %2, %3 offen sc1" : "=&v"(fz) : "v"(vFZ), "s"(rWS), "s"(0u) : "memory"); SB();
  asm volatile("s_waitcnt vmcnt(14)" :  :  : "memory"); SB();
  asm volatile("ds_read_b128 a[4:7], %0 offset:2048" :  : "v"(aA[0]) : "memory"); SB();
  asm volatile("ds_read_b128 a[20:23], %0 offset:2048" :  : "v"(aA[1]) : "memory"); SB();
  asm volatile("s_waitcnt lgkmcnt(2)" :  :  : "memory"); SB();
  asm volatile("v_xor_b32 %0, 128, %0" : "+v"(voS) :  : "memory"); SB();
  asm volatile("s_add_u32 m0, %0, 98304" :  : "s"(mA) : "memory", "scc"); SB();
  asm volatile("s_nop 0\n\tbuffer_load_dwordx4 %0, %1, %2 offen lds" :  : "v"(voS), "s"(rA), "s"(ao[0]) : "memory"); SB();
  asm volatile("buffer_load_dwordx4 %0, %1, %2 offen offset:1024 lds" :  : "v"(voS), "s"(rA), "s"(ao[1]) : "memory"); SB();
  asm volatile("s_waitcnt vmcnt(14)" :  :  : "memory"); SB();
  asm volatile("ds_read_b128 a[8:11], %0 offset:4096" :  : "v"(aA[0]) : "memory"); SB();
  asm volatile("ds_read_b128 a[24:27], %0 offset:4096" :  : "v"(aA[1]) : "memory"); SB();
  asm volatile("s_waitcnt vmcnt(13)\n\t.Lc_%=:\n\tv_cmp_ne_u64 vcc, %2, %1\n\ts_cbranch_vccnz .Lr_%=\n\t.Ld_%=:\n\t.subsection 1\n\t.Lr_%=:\n\tv_add_u32 %0, -1, %0\n\ts_nop 1\n\tv_readfirstlane_b32 vcc_hi, %0\n\ts_nop 3\n\ts_cmp_lt_i32 vcc_hi, 1\n\ts_cbranch_scc1 .Ld_%=\n\ts_sleep 2\n\ts_mov_b32 vcc_lo, 8192\n\tbuffer_load_dwordx2 %1, %3, %4, vcc_lo offen sc1\n\ts_waitcnt vmcnt(0)\n\ts_branch .Lc_%=\n\t.subsection 0" : "+v"(tmo), "+v"(fk) : "s"(tg), "v"(vFZ), "s"(rWS) : "memory", "vcc", "scc"); SB();
  asm volatile("s_add_u32 m0, %0, 85888" :  : "s"(mU) : "memory", "scc"); SB();
  asm volatile("s_nop 0\n\tbuffer_load_dwordx4 %0, %1, %2 offen sc1 lds" :  : "v"(vF), "s"(rWS), "s"(uso) : "memory"); SB();
  asm volatile("s_waitcnt lgkmcnt(2)" :  :  : "memory"); SB();
  asm volatile("s_add_u32 m0, %0, 98304" :  : "s"(mA) : "memory", "scc"); SB();
  asm volatile("s_nop 0\n\tbuffer_load_dwordx4 %0, %1, %2 offen offset:2048 lds" :  : "v"(voS), "s"(rA), "s"(ao[2]) : "memory"); SB();
  asm volatile("buffer_load_dwordx4 %0, %1, %2 offen offset:3072 lds" :  : "v"(voS), "s"(rA), "s"(ao[3]) : "memory"); SB();
  asm volatile("s_waitcnt lgkmcnt(0)" :  :  : "memory"); SB();
  asm volatile("s_waitcnt vmcnt(14)" :  :  : "memory"); SB();
  asm volatile("ds_read_b128 a[12:15], %0 offset:6144" :  : "v"(aA[0]) : "memory"); SB();
  asm volatile("ds_read_b128 a[28:31], %0 offset:6144" :  : "v"(aA[1]) : "memory"); SB();
  asm volatile("s_add_u32 m0, %0, 102400" :  : "s"(mA) : "memory", "scc"); SB();
  asm volatile("s_nop 0\n\tbuffer_load_dwordx4 %0, %1, %2 offen lds" :  : "v"(voS), "s"(rA), "s"(ao[4]) : "memory"); SB();
  asm volatile("buffer_load_dwordx4 %0, %1, %2 offen offset:1024 lds" :  : "v"(voS), "s"(rA), "s"(ao[5]) : "memory"); SB();
  asm volatile("s_waitcnt lgkmcnt(0)" :  :  : "memory"); SB();
  asm volatile("s_waitcnt vmcnt(14)" :  :  : "memory"); SB();
  asm volatile("ds_read_b128 a[32:35], %0 offset:32768" :  : "v"(aA[0]) : "memory"); SB();
  asm volatile("ds_read_b128 a[48:51], %0 offset:32768" :  : "v"(aA[1]) : "memory"); SB();
  asm volatile("buffer_load_dwordx4 %0, %1, %2 offen offset:2048 lds" :  : "v"(voS), "s"(rA), "s"(ao[6]) : "memory"); SB();
  asm volatile("buffer_load_dwordx4 %0, %1, %2 offen offset:3072 lds" :  : "v"(voS), "s"(rA), "s"(ao[7]) : "memory"); SB();
  asm volatile("s_waitcnt vmcnt(14)" :  :  : "memory"); SB();
  asm volatile("ds_read_b128 a[36:39], %0 offset:34816" :  : "v"(aA[0]) : "memory"); SB();
  asm volatile("ds_read_b128 a[52:55], %0 offset:34816" :  : "v"(aA[1]) : "memory"); SB();
  asm volatile("s_waitcnt lgkmcnt(2)" :  :  : "memory"); SB();
  asm volatile("v_xor_b32 %0, 384, %0" : "+v"(voS) :  : "memory"); SB();
  asm volatile("s_add_u32 m0, %0, 131072" :  : "s"(mA) : "memory", "scc"); SB();
  asm volatile("s_nop 0\n\tbuffer_load_dwordx4 %0, %1, %2 offen lds" :  : "v"(voS), "s"(rA), "s"(ao[0]) : "memory"); SB();
  asm volatile("buffer_load_dwordx4 %0, %1, %2 offen offset:1024 lds" :  : "v"(voS), "s"(rA), "s"(ao[1]) : "memory"); SB();
  asm volatile("s_waitcnt vmcnt(14)" :  :  : "memory"); SB();
  asm volatile("ds_read_b128 a[40:43], %0 offset:36864" :  : "v"(aA[0]) : "memory"); SB();
  asm volatile("ds_read_b128 a[56:59], %0 offset:36864" :  : "v"(aA[1]) : "memory"); SB();
  asm volatile("s_waitcnt lgkmcnt(2)" :  :  : "memory"); SB();
  asm volatile("buffer_load_dwordx4 %0, %1, %2 offen offset:2048 lds" :  : "v"(voS), "s"(rA), "s"(ao[2]) : "memory"); SB();
  asm volatile("buffer_load_dwordx4 %0, %1, %2 offen offset:3072 lds" :  : "v"(voS), "s"(rA), "s"(ao[3]) : "memory"); SB();
  asm volatile("s_waitcnt vmcnt(14)" :  :  : "memory"); SB();
  asm volatile("ds_read_b128 a[44:47], %0 offset:38912" :  : "v"(aA[0]) : "memory"); SB();
  asm volatile("ds_read_b128 a[60:63], %0 offset:38912" :  : "v"(aA[1]) : "memory"); SB();
  asm volatile("s_waitcnt vmcnt(13)\n\t.Lc_%=:\n\tv_cmp_ne_u64 vcc, %2, %1\n\ts_cbranch_vccnz .Lr_%=\n\t.Ld_%=:\n\t.subsection 1\n\t.Lr_%=:\n\tv_add_u32 %0, -1, %0\n\ts_nop 1\n\tv_readfirstlane_b32 vcc_hi, %0\n\ts_nop 3\n\ts_cmp_lt_i32 vcc_hi, 1\n\ts_cbranch_scc1 .Ld_%=\n\ts_sleep 2\n\ts_mov_b32 vcc_lo, 0\n\tbuffer_load_dwordx2 %1, %3, %4, vcc_lo offen sc1\n\ts_waitcnt vmcnt(0)\n\ts_branch .Lc_%=\n\t.subsection 0" : "+v"(tmo), "+v"(fz) : "s"(tg), "v"(vFZ), "s"(rWS) : "memory", "vcc", "scc"); SB();
  asm volatile("buffer_load_dwordx4 v[160:163], %0, %1, %2 offen offset:0 sc1" :  : "v"(vB), "s"(rWS), "s"(0u) : "memory"); SB();
  asm volatile("s_waitcnt lgkmcnt(2)" :  :  : "memory"); SB();
  asm volatile("buffer_load_dwordx4 v[164:167], %0, %1, %2 offen offset:1024 sc1" :  : "v"(vB), "s"(rWS), "s"(0u) : "memory"); SB();
  asm volatile("s_waitcnt vmcnt(12) lgkmcnt(0)\n\ts_barrier" :  :  : "memory"); SB();
  ST(14);
  asm volatile("ds_read2_b32 %0, %1 offset0:176 offset1:177" : "=&v"(cA[0][0][0]) : "v"(aa[0]) : "memory"); SB();
  asm volatile("ds_read2_b32 %0, %1 offset0:178 offset1:179" : "=&v"(cA[0][0][1]) : "v"(aa[0]) : "memory"); SB();
  asm volatile("ds_read_b128 a[124:127], %0 offset:3776" :  : "v"(bb) : "memory"); SB();
  asm volatile("ds_read_b128 a[120:123], %0 offset:3264" :  : "v"(bb) : "memory"); SB();
  asm volatile("ds_read_b128 a[116:119], %0 offset:2752" :  : "v"(bb) : "memory"); SB();
  asm volatile("ds_read_b128 a[112:115], %0 offset:2240" :  : "v"(bb) : "memory"); SB();
  asm volatile("ds_read2_b32 %0, %1 offset0:48 offset1:49" : "=&v"(cA[0][1][0]) : "v"(aa[0]) : "memory"); SB();
  asm volatile("ds_read2_b32 %0, %1 offset0:50 offset1:51" : "=&v"(cA[0][1][1]) : "v"(aa[0]) : "memory"); SB();
  asm volatile("ds_read_b128 a[108:111], %0 offset:1728" :  : "v"(bb) : "memory"); SB();
  asm volatile("ds_read2_b32 %0, %1 offset0:176 offset1:177" : "=&v"(cA[0][2][0]) : "v"(aa[1]) : "memory"); SB();
  asm volatile("" : "+v"(a1[0][0]), "+v"(a1[0][1]), "+v"(a1[0][2]), "+v"(a1[1][0]), "+v"(a1[1][1]), "+v"(a1[1][2]), "+v"(c1[0]), "+v"(c1[1]), "+v"(c1[2]), "+v"(co[0]), "+v"(co[1]), "+v"(co[2]) :  : ); SB();
  c1[0] += co[0]; c1[1] += co[1]; c1[2] += co[2];
#pragma unroll
  for (int n = 0; n < 6; ++n) { a1[n / 3][n % 3] = __builtin_elementwise_fma((f32x4){-xm[n / 3], -xm[n / 3], -xm[n / 3], -xm[n / 3]}, c1[n % 3], a1[n / 3][n % 3]); asm volatile("" : "+v"(a1[n / 3][n % 3])); }
  SB();
  asm volatile("ds_read_b128 a[64:67], %0 offset:0" :  : "v"(aA[0]) : "memory"); SB();
  asm volatile("ds_read_b128 a[80:83], %0 offset:0" :  : "v"(aA[1]) : "memory"); SB();
  asm volatile("s_waitcnt vmcnt(10)" :  :  : "memory"); SB();
  asm volatile("ds_read_b128 a[68:71], %0 offset:2048" :  : "v"(aA[0]) : "memory"); SB();
  asm volatile("ds_read_b128 a[84:87], %0 offset:2048" :  : "v"(aA[1]) : "memory"); SB();
  asm volatile("buffer_load_dwordx4 v[168:171], %0, %1, %2 offen offset:0 sc1" :  : "v"(vB), "s"(rWS), "s"(65536u) : "memory"); SB();
  asm volatile("buffer_load_dwordx4 v[172:175], %0, %1, %2 offen offset:1024 sc1" :  : "v"(vB), "s"(rWS), "s"(65536u) : "memory"); SB();
  asm volatile("v_xor_b32 %0, 131072, %0" : "+v"(vB) :  : "memory"); SB();
  if (nch == 3) {
  asm volatile("s_waitcnt lgkmcnt(5)\n\tv_mfma_f32_16x16x32_bf16 %0, %1, a[124:127], 0" : "=&v"(ac[7]) : "v"(((u32x4){cA[0][0][0][0], cA[0][0][0][1], cA[0][0][1][0], cA[0][0][1][1]})) : ); SB();
  asm volatile("ds_read2_b32 %0, %1 offset0:178 offset1:179" : "=&v"(cA[0][2][1]) : "v"(aa[1]) : "memory"); SB();
  asm volatile("s_waitcnt vmcnt(10)" :  :  : "memory"); SB();
  asm volatile("v_mfma_f32_16x16x32_bf16 %0, %1, a[120:123], 0" : "=&v"(ac[6]) : "v"(((u32x4){cA[0][0][0][0], cA[0][0][0][1], cA[0][0][1][0], cA[0][0][1][1]})) : ); SB();
  asm volatile("ds_read2_b32 %0, %1 offset0:48 offset1:49" : "=&v"(cA[0][3][0]) : "v"(aa[1]) : "memory"); SB();
  asm volatile("ds_read_b128 a[72:75], %0 offset:4096" :  : "v"(aA[0]) : "memory"); SB();
  asm volatile("v_mfma_f32_16x16x32_bf16 %0, %1, a[116:119], 0" : "=&v"(ac[5]) : "v"(((u32x4){cA[0][0][0][0], cA[0][0][0][1], cA[0][0][1][0], cA[0][0][1][1]})) : ); SB();
  asm volatile("ds_read2_b32 %0, %1 offset0:50 offset1:51" : "=&v"(cA[0][3][1]) : "v"(aa[1]) : "memory"); SB();
  asm volatile("ds_read_b128 a[88:91], %0 offset:4096" :  : "v"(aA[1]) : "memory"); SB();
  asm volatile("v_mfma_f32_16x16x32_bf16 %0, %1, a[112:115], 0" : "=&v"(ac[4]) : "v"(((u32x4){cA[0][0][0][0], cA[0][0][0][1], cA[0][0][1][0], cA[0][0][1][1]})) : ); SB();
  asm volatile("ds_read_b128 a[104:107], %0 offset:1216" :  : "v"(bb) : "memory"); SB();
  asm volatile("buffer_load_dwordx4 v[176:179], %0, %1, %2 offen offset:0 sc1" :  : "v"(vB), "s"(rWS), "s"(0u) : "memory"); SB();
  asm volatile("v_mfma_f32_16x16x32_bf16 %0, %1, a[120:123], %0" : "+v"(ac[7]) : "v"(((u32x4){cA[0][1][0][0], cA[0][1][0][1], cA[0][1][1][0], cA[0][1][1][1]})) : ); SB();
  asm volatile("ds_read2_b32 %0, %1 offset0:176 offset1:177" : "=&v"(cA[0][4][0]) : "v"(aa[2]) : "memory"); SB();
  asm volatile("v_mfma_f32_16x16x32_bf16 %0, %1, a[116:119], %0" : "+v"(ac[6]) : "v"(((u32x4){cA[0][1][0][0], cA[0][1][0][1], cA[0][1][1][0], cA[0][1][1][1]})) : ); SB();
  asm volatile("ds_read2_b32 %0, %1 offset0:178 offset1:179" : "=&v"(cA[0][4][1]) : "v"(aa[2]) : "memory"); SB();
  asm volatile("buffer_load_dwordx4 v[180:183], %0, %1, %2 offen offset:1024 sc1" :  : "v"(vB), "s"(rWS), "s"(0u) : "memory"); SB();
  asm volatile("v_mfma_f32_16x16x32_bf16 %0, %1, a[112:115], %0" : "+v"(ac[5]) : "v"(((u32x4){cA[0][1][0][0], cA[0][1][0][1], cA[0][1][1][0], cA[0][1][1][1]})) : ); SB();
  asm volatile("ds_read2_b32 %0, %1 offset0:48 offset1:49" : "=&v"(cA[0][5][0]) : "v"(aa[2]) : "memory"); SB();
  asm volatile("v_mfma_f32_16x16x32_bf16 %0, %1, a[108:111], %0" : "+v"(ac[4]) : "v"(((u32x4){cA[0][1][0][0], cA[0][1][0][1], cA[0][1][1][0], cA[0][1][1][1]})) : ); SB();
  asm volatile("ds_read2_b32 %0, %1 offset0:50 offset1:51" : "=&v"(cA[0][5][1]) : "v"(aa[2]) : "memory"); SB();
  asm volatile("s_waitcnt lgkmcnt(9)\n\tv_mfma_f32_16x16x32_bf16 %0, %1, a[116:119], %0" : "+v"(ac[7]) : "v"(((u32x4){cA[0][2][0][0], cA[0][2][0][1], cA[0][2][1][0], cA[0][2][1][1]})) : ); SB();
  asm volatile("ds_read_b128 a[100:103], %0 offset:704" :  : "v"(bb) : "memory"); SB();
  asm volatile("s_waitcnt vmcnt(10)" :  :  : "memory"); SB();
  asm volatile("v_mfma_f32_16x16x32_bf16 %0, %1, a[112:115], %0" : "+v"(ac[6]) : "v"(((u32x4){cA[0][2][0][0], cA[0][2][0][1], cA[0][2][1][0], cA[0][2][1][1]})) : ); SB();
  asm volatile("ds_read2_b32 %0, %1 offset0:176 offset1:177" : "=&v"(cA[0][6][0]) : "v"(aa[3]) : "memory"); SB();
  asm volatile("ds_read_b128 a[76:79], %0 offset:6144" :  : "v"(aA[0]) : "memory"); SB();
  asm volatile("v_mfma_f32_16x16x32_bf16 %0, %1, a[108:111], %0" : "+v"(ac[5]) : "v"(((u32x4){cA[0][2][0][0], cA[0][2][0][1], cA[0][2][1][0], cA[0][2][1][1]})) : ); SB();
  asm volatile("ds_read2_b32 %0, %1 offset0:178 offset1:179" : "=&v"(cA[0][6][1]) : "v"(aa[3]) : "memory"); SB();
  asm volatile("ds_read_b128 a[92:95], %0 offset:6144" :  : "v"(aA[1]) : "memory"); SB();
  asm volatile("v_mfma_f32_16x16x32_bf16 %0, %1, a[108:111], 0" : "=&v"(ac[3]) : "v"(((u32x4){cA[0][0][0][0], cA[0][0][0][1], cA[0][0][1][0], cA[0][0][1][1]})) : ); SB();
  asm volatile("ds_read2_b32 %0, %1 offset0:48 offset1:49" : "=&v"(cA[0][7][0]) : "v"(aa[3]) : "memory"); SB();
  asm volatile("buffer_load_dwordx4 v[184:187], %0, %1, %2 offen offset:0 sc1" :  : "v"(vB), "s"(rWS), "s"(65536u) : "memory"); SB();
  asm volatile("s_waitcnt lgkmcnt(10)\n\tv_mfma_f32_16x16x32_bf16 %0, %1, a[112:115], %0" : "+v"(ac[7]) : "v"(((u32x4){cA[0][3][0][0], cA[0][3][0][1], cA[0][3][1][0], cA[0][3][1][1]})) : ); SB();
  asm volatile("ds_read2_b32 %0, %1 offset0:50 offset1:51" : "=&v"(cA[0][7][1]) : "v"(aa[3]) : "memory"); SB();
  asm volatile("buffer_load_dwordx4 v[188:191], %0, %1, %2 offen offset:1024 sc1" :  : "v"(vB), "s"(rWS), "s"(65536u) : "memory"); SB();
  asm volatile("v_mfma_f32_16x16x32_bf16 %0, %1, a[108:111], %0" : "+v"(ac[6]) : "v"(((u32x4){cA[0][3][0][0], cA[0][3][0][1], cA[0][3][1][0], cA[0][3][1][1]})) : ); SB();
  asm volatile("ds_read_b128 a[96:99], %0 offset:192" :  : "v"(bb) : "memory"); SB();
  asm volatile("v_mfma_f32_16x16x32_bf16 %0, %1, a[104:107], %0" : "+v"(ac[5]) : "v"(((u32x4){cA[0][3][0][0], cA[0][3][0][1], cA[0][3][1][0], cA[0][3][1][1]})) : ); SB();
  asm volatile("ds_read2_b32 %0, %1 offset0:160 offset1:161" : "=&v"(cA[1][0][0]) : "v"(aa[0]) : "memory"); SB();
  asm volatile("v_mfma_f32_16x16x32_bf16 %0, %1, a[104:107], %0" : "+v"(ac[4]) : "v"(((u32x4){cA[0][2][0][0], cA[0][2][0][1], cA[0][2][1][0], cA[0][2][1][1]})) : ); SB();
  asm volatile("v_xor_b32 %0, 393216, %0" : "+v"(vB) :  : "memory"); SB();
  asm volatile("s_waitcnt lgkmcnt(9)\n\tv_mfma_f32_16x16x32_bf16 %0, %1, a[108:111], %0" : "+v"(ac[7]) : "v"(((u32x4){cA[0][4][0][0], cA[0][4][0][1], cA[0][4][1][0], cA[0][4][1][1]})) : ); SB();
  asm volatile("ds_read2_b32 %0, %1 offset0:162 offset1:163" : "=&v"(cA[1][0][1]) : "v"(aa[0]) : "memory"); SB();
  asm volatile("buffer_load_dwordx4 v[192:195], %0, %1, %2 offen offset:0 sc1" :  : "v"(vB), "s"(rWS), "s"(0u) : "memory"); SB();
  asm volatile("v_mfma_f32_16x16x32_bf16 %0, %1, a[104:107], %0" : "+v"(ac[6]) : "v"(((u32x4){cA[0][4][0][0], cA[0][4][0][1], cA[0][4][1][0], cA[0][4][1][1]})) : ); SB();
  asm volatile("ds_read_b128 a[156:159], %0 offset:3712" :  : "v"(bb) : "memory"); SB();
  asm volatile("v_mfma_f32_16x16x32_bf16 %0, %1, a[104:107], %0" : "+v"(ac[3]) : "v"(((u32x4){cA[0][1][0][0], cA[0][1][0][1], cA[0][1][1][0], cA[0][1][1][1]})) : ); SB();
  asm volatile("ds_read_b128 a[152:155], %0 offset:3200" :  : "v"(bb) : "memory"); SB();
  asm volatile("buffer_load_dwordx4 v[196:199], %0, %1, %2 offen offset:1024 sc1" :  : "v"(vB), "s"(rWS), "s"(0u) : "memory"); SB();
  asm volatile("v_mfma_f32_16x16x32_bf16 %0, %1, a[104:107], 0" : "=&v"(ac[2]) : "v"(((u32x4){cA[0][0][0][0], cA[0][0][0][1], cA[0][0][1][0], cA[0][0][1][1]})) : ); SB();
  asm volatile("ds_write_b128 %0, %1 offset:0" :  : "v"(ew[0]), "v"(a1[0][0]) : "memory"); SB();
  asm volatile("v_mfma_f32_16x16x32_bf16 %0, %1, a[104:107], %0" : "+v"(ac[7]) : "v"(((u32x4){cA[0][5][0][0], cA[0][5][0][1], cA[0][5][1][0], cA[0][5][1][1]})) : ); SB();
  asm volatile("ds_read_b128 a[128:131], %0 offset:128" :  : "v"(bb) : "memory"); SB();
  asm volatile("s_waitcnt lgkmcnt(7)\n\tv_mfma_f32_16x16x32_bf16 %0, %1, a[100:103], %0" : "+v"(ac[6]) : "v"(((u32x4){cA[0][5][0][0], cA[0][5][0][1], cA[0][5][1][0], cA[0][5][1][1]})) : ); SB();
  asm volatile("ds_read_b128 a[132:135], %0 offset:640" :  : "v"(bb) : "memory"); SB();
  asm volatile("buffer_load_dwordx4 v[200:203], %0, %1, %2 offen offset:0 sc1" :  : "v"(vB), "s"(rWS), "s"(65536u) : "memory"); SB();
  asm volatile("v_mfma_f32_16x16x32_bf16 %0, %1, a[100:103], %0" : "+v"(ac[5]) : "v"(((u32x4){cA[0][4][0][0], cA[0][4][0][1], cA[0][4][1][0], cA[0][4][1][1]})) : ); SB();
  asm volatile("ds_read2_b32 %0, %1 offset0:32 offset1:33" : "=&v"(cA[1][1][0]) : "v"(aa[0]) : "memory"); SB();
  asm volatile("v_mfma_f32_16x16x32_bf16 %0, %1, a[100:103], %0" : "+v"(ac[4]) : "v"(((u32x4){cA[0][3][0][0], cA[0][3][0][1], cA[0][3][1][0], cA[0][3][1][1]})) : ); SB();
  asm volatile("ds_write_b128 %0, %1 offset:1024" :  : "v"(ew[0]), "v"(a1[0][1]) : "memory"); SB();
  asm volatile("v_mfma_f32_16x16x32_bf16 %0, %1, a[100:103], %0" : "+v"(ac[7]) : "v"(((u32x4){cA[0][6][0][0], cA[0][6][0][1], cA[0][6][1][0], cA[0][6][1][1]})) : ); SB();
  asm volatile("ds_read2_b32 %0, %1 offset0:34 offset1:35" : "=&v"(cA[1][1][1]) : "v"(aa[0]) : "memory"); SB();
  asm volatile("buffer_load_dwordx4 v[204:207], %0, %1, %2 offen offset:1024 sc1" :  : "v"(vB), "s"(rWS), "s"(65536u) : "memory"); SB();
  asm volatile("v_mfma_f32_16x16x32_bf16 %0, %1, a[100:103], %0" : "+v"(ac[3]) : "v"(((u32x4){cA[0][2][0][0], cA[0][2][0][1], cA[0][2][1][0], cA[0][2][1][1]})) : ); SB();
  asm volatile("ds_read_b128 a[148:151], %0 offset:2688" :  : "v"(bb) : "memory"); SB();
  asm volatile("v_mfma_f32_16x16x32_bf16 %0, %1, a[100:103], %0" : "+v"(ac[2]) : "v"(((u32x4){cA[0][1][0][0], cA[0][1][0][1], cA[0][1][1][0], cA[0][1][1][1]})) : ); SB();
  asm volatile("ds_read_b128 a[136:139], %0 offset:1152" :  : "v"(bb) : "memory"); SB();
  asm volatile("v_mfma_f32_16x16x32_bf16 %0, %1, a[100:103], 0" : "=&v"(ac[1]) : "v"(((u32x4){cA[0][0][0][0], cA[0][0][0][1], cA[0][0][1][0], cA[0][0][1][1]})) : ); SB();
  asm volatile("v_xor_b32 %0, 262144, %0" : "+v"(vB) :  : "memory"); SB();
  asm volatile("s_waitcnt lgkmcnt(7)\n\tv_mfma_f32_16x16x32_bf16 %0, %1, a[96:99], %0" : "+v"(ac[7]) : "v"(((u32x4){cA[0][7][0][0], cA[0][7][0][1], cA[0][7][1][0], cA[0][7][1][1]})) : ); SB();
  asm volatile("ds_read2_b32 %0, %1 offset0:160 offset1:161" : "=&v"(cA[1][2][0]) : "v"(aa[1]) : "memory"); SB();
  asm volatile("buffer_load_dwordx4 v[208:211], %0, %1, %2 offen offset:0 sc1" :  : "v"(vB), "s"(rWS), "s"(2048u) : "memory"); SB();
  asm volatile("v_mfma_f32_16x16x32_bf16 %0, %1, a[96:99], %0" : "+v"(ac[6]) : "v"(((u32x4){cA[0][6][0][0], cA[0][6][0][1], cA[0][6][1][0], cA[0][6][1][1]})) : ); SB();
  asm volatile("ds_read2_b32 %0, %1 offset0:162 offset1:163" : "=&v"(cA[1][2][1]) : "v"(aa[1]) : "memory"); SB();
  asm volatile("v_mfma_f32_16x16x32_bf16 %0, %1, a[96:99], %0" : "+v"(ac[5]) : "v"(((u32x4){cA[0][5][0][0], cA[0][5][0][1], cA[0][5][1][0], cA[0][5][1][1]})) : ); SB();
  asm volatile("ds_read_b128 a[140:143], %0 offset:1664" :  : "v"(bb) : "memory"); SB();
  asm volatile("v_mfma_f32_16x16x32_bf16 %0, %1, a[96:99], %0" : "+v"(ac[4]) : "v"(((u32x4){cA[0][4][0][0], cA[0][4][0][1], cA[0][4][1][0], cA[0][4][1][1]})) : ); SB();
  asm volatile("buffer_load_dwordx4 v[212:215], %0, %1, %2 offen offset:1024 sc1" :  : "v"(vB), "s"(rWS), "s"(2048u) : "memory"); SB();
  asm volatile("v_mfma_f32_16x16x32_bf16 %0, %1, a[96:99], %0" : "+v"(ac[3]) : "v"(((u32x4){cA[0][3][0][0], cA[0][3][0][1], cA[0][3][1][0], cA[0][3][1][1]})) : ); SB();
  asm volatile("ds_read_b128 a[144:147], %0 offset:2176" :  : "v"(bb) : "memory"); SB();
  asm volatile("v_mfma_f32_16x16x32_bf16 %0, %1, a[96:99], %0" : "+v"(ac[2]) : "v"(((u32x4){cA[0][2][0][0], cA[0][2][0][1], cA[0][2][1][0], cA[0][2][1][1]})) : ); SB();
  asm volatile("ds_read2_b32 %0, %1 offset0:32 offset1:33" : "=&v"(cA[1][3][0]) : "v"(aa[1]) : "memory"); SB();
  asm volatile("v_mfma_f32_16x16x32_bf16 %0, %1, a[96:99], %0" : "+v"(ac[1]) : "v"(((u32x4){cA[0][1][0][0], cA[0][1][0][1], cA[0][1][1][0], cA[0][1][1][1]})) : ); SB();
  asm volatile("ds_read2_b32 %0, %1 offset0:34 offset1:35" : "=&v"(cA[1][3][1]) : "v"(aa[1]) : "memory"); SB();
  asm volatile("v_mfma_f32_16x16x32_bf16 %0, %1, a[96:99], 0" : "=&v"(ac[0]) : "v"(((u32x4){cA[0][0][0][0], cA[0][0][0][1], cA[0][0][1][0], cA[0][0][1][1]})) : ); SB();
  asm volatile("buffer_load_dwordx4 v[216:219], %0, %1, %2 offen offset:0 sc1" :  : "v"(vB), "s"(rWS), "s"(67584u) : "memory"); SB();
  asm volatile("v_mfma_f32_16x16x32_bf16 %0, %1, a[156:159], %0" : "+v"(ac[7]) : "v"(((u32x4){cA[1][0][0][0], cA[1][0][0][1], cA[1][0][1][0], cA[1][0][1][1]})) : ); SB();
  asm volatile("ds_read2_b32 %0, %1 offset0:160 offset1:161" : "=&v"(cA[1][4][0]) : "v"(aa[2]) : "memory"); SB();
  asm volatile("v_mfma_f32_16x16x32_bf16 %0, %1, a[152:155], %0" : "+v"(ac[6]) : "v"(((u32x4){cA[1][0][0][0], cA[1][0][0][1], cA[1][0][1][0], cA[1][0][1][1]})) : ); SB();
  asm volatile("ds_read2_b32 %0, %1 offset0:162 offset1:163" : "=&v"(cA[1][4][1]) : "v"(aa[2]) : "memory"); SB();
  asm volatile("s_waitcnt lgkmcnt(7)\n\tv_mfma_f32_16x16x32_bf16 %0, %1, a[128:131], %0" : "+v"(ac[0]) : "v"(((u32x4){cA[1][0][0][0], cA[1][0][0][1], cA[1][0][1][0], cA[1][0][1][1]})) : ); SB();
  asm volatile("ds_read2_b32 %0, %1 offset0:32 offset1:33" : "=&v"(cA[1][5][0]) : "v"(aa[2]) : "memory"); SB();
  asm volatile("v_mfma_f32_16x16x32_bf16 %0, %1, a[132:135], %0" : "+v"(ac[1]) : "v"(((u32x4){cA[1][0][0][0], cA[1][0][0][1], cA[1][0][1][0], cA[1][0][1][1]})) : ); SB();
  asm volatile("buffer_load_dwordx4 v[220:223], %0, %1, %2 offen offset:1024 sc1" :  : "v"(vB), "s"(rWS), "s"(67584u) : "memory"); SB();
  asm volatile("v_mfma_f32_16x16x32_bf16 %0, %1, a[152:155], %0" : "+v"(ac[7]) : "v"(((u32x4){cA[1][1][0][0], cA[1][1][0][1], cA[1][1][1][0], cA[1][1][1][1]})) : ); SB();
  asm volatile("ds_read2_b32 %0, %1 offset0:34 offset1:35" : "=&v"(cA[1][5][1]) : "v"(aa[2]) : "memory"); SB();
  asm volatile("v_mfma_f32_16x16x32_bf16 %0, %1, a[148:151], %0" : "+v"(ac[6]) : "v"(((u32x4){cA[1][1][0][0], cA[1][1][0][1], cA[1][1][1][0], cA[1][1][1][1]})) : ); SB();
  asm volatile("ds_read2_b32 %0, %1 offset0:160 offset1:161" : "=&v"(cA[1][6][0]) : "v"(aa[3]) : "memory"); SB();
  asm volatile("v_mfma_f32_16x16x32_bf16 %0, %1, a[136:139], %0" : "+v"(ac[2]) : "v"(((u32x4){cA[1][0][0][0], cA[1][0][0][1], cA[1][0][1][0], cA[1][0][1][1]})) : ); SB();
  asm volatile("ds_read2_b32 %0, %1 offset0:162 offset1:163" : "=&v"(cA[1][6][1]) : "v"(aa[3]) : "memory"); SB();
  asm volatile("v_mfma_f32_16x16x32_bf16 %0, %1, a[128:131], %0" : "+v"(ac[1]) : "v"(((u32x4){cA[1][1][0][0], cA[1][1][0][1], cA[1][1][1][0], cA[1][1][1][1]})) : ); SB();
  asm volatile("ds_write_b128 %0, %1 offset:0" :  : "v"(dd), "v"(ac[0]) : "memory"); SB();
  asm volatile("s_waitcnt lgkmcnt(7)\n\tv_mfma_f32_16x16x32_bf16 %0, %1, a[148:151], %0" : "+v"(ac[7]) : "v"(((u32x4){cA[1][2][0][0], cA[1][2][0][1], cA[1][2][1][0], cA[1][2][1][1]})) : ); SB();
  asm volatile("ds_read2_b32 %0, %1 offset0:32 offset1:33" : "=&v"(cA[1][7][0]) : "v"(aa[3]) : "memory"); SB();
  asm volatile("v_mfma_f32_16x16x32_bf16 %0, %1, a[140:143], %0" : "+v"(ac[3]) : "v"(((u32x4){cA[1][0][0][0], cA[1][0][0][1], cA[1][0][1][0], cA[1][0][1][1]})) : ); SB();
  asm volatile("ds_read2_b32 %0, %1 offset0:34 offset1:35" : "=&v"(cA[1][7][1]) : "v"(aa[3]) : "memory"); SB();
  asm volatile("v_mfma_f32_16x16x32_bf16 %0, %1, a[132:135], %0" : "+v"(ac[2]) : "v"(((u32x4){cA[1][1][0][0], cA[1][1][0][1], cA[1][1][1][0], cA[1][1][1][1]})) : ); SB();
  asm volatile("v_xor_b32 %0, 131072, %0" : "+v"(vB) :  : "memory"); SB();
  asm volatile("v_mfma_f32_16x16x32_bf16 %0, %1, a[144:147], %0" : "+v"(ac[4]) : "v"(((u32x4){cA[1][0][0][0], cA[1][0][0][1], cA[1][0][1][0], cA[1][0][1][1]})) : ); SB();
  asm volatile("ds_write_b128 %0, %1 offset:1024" :  : "v"(dd), "v"(ac[1]) : "memory"); SB();
  asm volatile("v_mfma_f32_16x16x32_bf16 %0, %1, a[144:147], %0" : "+v"(ac[7]) : "v"(((u32x4){cA[1][3][0][0], cA[1][3][0][1], cA[1][3][1][0], cA[1][3][1][1]})) : ); SB();
  asm volatile("buffer_load_dwordx4 v[224:227], %0, %1, %2 offen offset:0 sc1" :  : "v"(vB), "s"(rWS), "s"(2048u) : "memory"); SB();
  asm volatile("v_mfma_f32_16x16x32_bf16 %0, %1, a[136:139], %0" : "+v"(ac[3]) : "v"(((u32x4){cA[1][1][0][0], cA[1][1][0][1], cA[1][1][1][0], cA[1][1][1][1]})) : ); SB();
  asm volatile("buffer_load_dwordx4 v[228:231], %0, %1, %2 offen offset:1024 sc1" :  : "v"(vB), "s"(rWS), "s"(2048u) : "memory"); SB();
  asm volatile("v_mfma_f32_16x16x32_bf16 %0, %1, a[128:131], %0" : "+v"(ac[2]) : "v"(((u32x4){cA[1][2][0][0], cA[1][2][0][1], cA[1][2][1][0], cA[1][2][1][1]})) : ); SB();
  asm volatile("ds_write_b128 %0, %1 offset:2048" :  : "v"(ew[0]), "v"(a1[0][2]) : "memory"); SB();
  asm volatile("v_mfma_f32_16x16x32_bf16 %0, %1, a[140:143], %0" : "+v"(ac[4]) : "v"(((u32x4){cA[1][1][0][0], cA[1][1][0][1], cA[1][1][1][0], cA[1][1][1][1]})) : ); SB();
  asm volatile("buffer_load_dwordx4 v[232:235], %0, %1, %2 offen offset:0 sc1" :  : "v"(vB), "s"(rWS), "s"(67584u) : "memory"); SB();
  asm volatile("v_mfma_f32_16x16x32_bf16 %0, %1, a[148:151], %0" : "+v"(ac[5]) : "v"(((u32x4){cA[1][0][0][0], cA[1][0][0][1], cA[1][0][1][0], cA[1][0][1][1]})) : ); SB();
  asm volatile("ds_write_b128 %0, %1 offset:0" :  : "v"(ew[1]), "v"(a1[1][0]) : "memory"); SB();
  asm volatile("v_mfma_f32_16x16x32_bf16 %0, %1, a[132:135], %0" : "+v"(ac[3]) : "v"(((u32x4){cA[1][2][0][0], cA[1][2][0][1], cA[1][2][1][0], cA[1][2][1][1]})) : ); SB();
  asm volatile("buffer_load_dwordx4 v[236:239], %0, %1, %2 offen offset:1024 sc1" :  : "v"(vB), "s"(rWS), "s"(67584u) : "memory"); SB();
  asm volatile("v_mfma_f32_16x16x32_bf16 %0, %1, a[144:147], %0" : "+v"(ac[6]) : "v"(((u32x4){cA[1][2][0][0], cA[1][2][0][1], cA[1][2][1][0], cA[1][2][1][1]})) : ); SB();
  asm volatile("ds_write_b128 %0, %1 offset:2048" :  : "v"(dd), "v"(ac[2]) : "memory"); SB();
  asm volatile("v_mfma_f32_16x16x32_bf16 %0, %1, a[136:139], %0" : "+v"(ac[4]) : "v"(((u32x4){cA[1][2][0][0], cA[1][2][0][1], cA[1][2][1][0], cA[1][2][1][1]})) : ); SB();
  asm volatile("v_xor_b32 %0, 393216, %0" : "+v"(vB) :  : "memory"); SB();
  asm volatile("v_mfma_f32_16x16x32_bf16 %0, %1, a[144:147], %0" : "+v"(ac[5]) : "v"(((u32x4){cA[1][1][0][0], cA[1][1][0][1], cA[1][1][1][0], cA[1][1][1][1]})) : ); SB();
  asm volatile("ds_write_b128 %0, %1 offset:1024" :  : "v"(ew[1]), "v"(a1[1][1]) : "memory"); SB();
  asm volatile("v_mfma_f32_16x16x32_bf16 %0, %1, a[128:131], %0" : "+v"(ac[3]) : "v"(((u32x4){cA[1][3][0][0], cA[1][3][0][1], cA[1][3][1][0], cA[1][3][1][1]})) : ); SB();
  asm volatile("buffer_load_dwordx4 v[240:243], %0, %1, %2 offen offset:0 sc1" :  : "v"(vB), "s"(rWS), "s"(2048u) : "memory"); SB();
  asm volatile("v_mfma_f32_16x16x32_bf16 %0, %1, a[140:143], %0" : "+v"(ac[6]) : "v"(((u32x4){cA[1][3][0][0], cA[1][3][0][1], cA[1][3][1][0], cA[1][3][1][1]})) : ); SB();
  asm volatile("ds_write_b128 %0, %1 offset:2048" :  : "v"(ew[1]), "v"(a1[1][2]) : "memory"); SB();
  asm volatile("v_mfma_f32_16x16x32_bf16 %0, %1, a[132:135], %0" : "+v"(ac[4]) : "v"(((u32x4){cA[1][3][0][0], cA[1][3][0][1], cA[1][3][1][0], cA[1][3][1][1]})) : ); SB();
  asm volatile("buffer_load_dwordx4 v[244:247], %0, %1, %2 offen offset:1024 sc1" :  : "v"(vB), "s"(rWS), "s"(2048u) : "memory"); SB();
  asm volatile("v_mfma_f32_16x16x32_bf16 %0, %1, a[140:143], %0" : "+v"(ac[5]) : "v"(((u32x4){cA[1][2][0][0], cA[1][2][0][1], cA[1][2][1][0], cA[1][2][1][1]})) : ); SB();
  asm volatile("s_waitcnt lgkmcnt(3)\n\tv_mfma_f32_16x16x32_bf16 %0, %1, a[140:143], %0" : "+v"(ac[7]) : "v"(((u32x4){cA[1][4][0][0], cA[1][4][0][1], cA[1][4][1][0], cA[1][4][1][1]})) : ); SB();
  asm volatile("ds_write_b128 %0, %1 offset:3072" :  : "v"(dd), "v"(ac[3]) : "memory"); SB();
  asm volatile("v_mfma_f32_16x16x32_bf16 %0, %1, a[136:139], %0" : "+v"(ac[6]) : "v"(((u32x4){cA[1][4][0][0], cA[1][4][0][1], cA[1][4][1][0], cA[1][4][1][1]})) : ); SB();
  asm volatile("buffer_load_dwordx4 v[248:251], %0, %1, %2 offen offset:0 sc1" :  : "v"(vB), "s"(rWS), "s"(67584u) : "memory"); SB();
  asm volatile("v_mfma_f32_16x16x32_bf16 %0, %1, a[128:131], %0" : "+v"(ac[4]) : "v"(((u32x4){cA[1][4][0][0], cA[1][4][0][1], cA[1][4][1][0], cA[1][4][1][1]})) : ); SB();
  asm volatile("v_mfma_f32_16x16x32_bf16 %0, %1, a[136:139], %0" : "+v"(ac[5]) : "v"(((u32x4){cA[1][3][0][0], cA[1][3][0][1], cA[1][3][1][0], cA[1][3][1][1]})) : ); SB();
  asm volatile("v_mfma_f32_16x16x32_bf16 %0, %1, a[136:139], %0" : "+v"(ac[7]) : "v"(((u32x4){cA[1][5][0][0], cA[1][5][0][1], cA[1][5][1][0], cA[1][5][1][1]})) : ); SB();
  asm volatile("buffer_load_dwordx4 v[252:255], %0, %1, %2 offen offset:1024 sc1" :  : "v"(vB), "s"(rWS), "s"(67584u) : "memory"); SB();
  asm volatile("v_mfma_f32_16x16x32_bf16 %0, %1, a[132:135], %0" : "+v"(ac[6]) : "v"(((u32x4){cA[1][5][0][0], cA[1][5][0][1], cA[1][5][1][0], cA[1][5][1][1]})) : ); SB();
  asm volatile("v_xor_b32 %0, 131072, %0" : "+v"(vB) :  : "memory"); SB();
  asm volatile("v_mfma_f32_16x16x32_bf16 %0, %1, a[132:135], %0" : "+v"(ac[5]) : "v"(((u32x4){cA[1][4][0][0], cA[1][4][0][1], cA[1][4][1][0], cA[1][4][1][1]})) : ); SB();
  asm volatile("ds_write_b128 %0, %1 offset:4096" :  : "v"(dd), "v"(ac[4]) : "memory"); SB();
  asm volatile("v_mfma_f32_16x16x32_bf16 %0, %1, a[132:135], %0" : "+v"(ac[7]) : "v"(((u32x4){cA[1][6][0][0], cA[1][6][0][1], cA[1][6][1][0], cA[1][6][1][1]})) : ); SB();
  asm volatile("s_add_u32 m0, %0, 135168" :  : "s"(mA) : "memory", "scc"); SB();
  asm volatile("v_mfma_f32_16x16x32_bf16 %0, %1, a[128:131], %0" : "+v"(ac[6]) : "v"(((u32x4){cA[1][6][0][0], cA[1][6][0][1], cA[1][6][1][0], cA[1][6][1][1]})) : ); SB();
  asm volatile("buffer_load_dwordx4 %0, %1, %2 offen lds" :  : "v"(voS), "s"(rA), "s"(ao[4]) : "memory"); SB();
  asm volatile("v_mfma_f32_16x16x32_bf16 %0, %1, a[128:131], %0" : "+v"(ac[5]) : "v"(((u32x4){cA[1][5][0][0], cA[1][5][0][1], cA[1][5][1][0], cA[1][5][1][1]})) : ); SB();
  asm volatile("v_mfma_f32_16x16x32_bf16 %0, %1, a[128:131], %0" : "+v"(ac[7]) : "v"(((u32x4){cA[1][7][0][0], cA[1][7][0][1], cA[1][7][1][0], cA[1][7][1][1]})) : ); SB();
  asm volatile("buffer_load_dwordx4 %0, %1, %2 offen offset:1024 lds" :  : "v"(voS), "s"(rA), "s"(ao[5]) : "memory"); SB();
  asm volatile("s_nop 5" : "+v"(ac[5]), "+v"(ac[6]), "+v"(ac[7]) :  : ); SB();
  asm volatile("ds_write_b128 %0, %1 offset:6144" :  : "v"(dd), "v"(ac[6]) : "memory"); SB();
  asm volatile("ds_write_b128 %0, %1 offset:5120" :  : "v"(dd), "v"(ac[5]) : "memory"); SB();
  asm volatile("ds_write_b128 %0, %1 offset:7168" :  : "v"(dd), "v"(ac[7]) : "memory"); SB();
  } else {
  asm volatile("; ELSE_BEGIN"); SB();
  asm volatile("s_waitcnt lgkmcnt(0)" ::: "memory"); SB();
  asm volatile("s_waitcnt vmcnt(10)" :  :  : "memory"); SB();
  asm volatile("s_waitcnt lgkmcnt(0)" ::: "memory"); SB();
  asm volatile("ds_read_b128 a[72:75], %0 offset:4096" :  : "v"(aA[0]) : "memory"); SB();
  asm volatile("s_waitcnt lgkmcnt(0)" ::: "memory"); SB();
  asm volatile("ds_read_b128 a[88:91], %0 offset:4096" :  : "v"(aA[1]) : "memory"); SB();
  asm volatile("s_waitcnt lgkmcnt(0)" ::: "memory"); SB();
  asm volatile("buffer_load_dwordx4 v[176:179], %0, %1, %2 offen offset:0 sc1" :  : "v"(vB), "s"(rWS), "s"(0u) : "memory"); SB();
  asm volatile("s_waitcnt lgkmcnt(0)" ::: "memory"); SB();
  asm volatile("buffer_load_dwordx4 v[180:183], %0, %1, %2 offen offset:1024 sc1" :  : "v"(vB), "s"(rWS), "s"(0u) : "memory"); SB();
  asm volatile("s_waitcnt lgkmcnt(0)" ::: "memory"); SB();
  asm volatile("s_waitcnt vmcnt(10)" :  :  : "memory"); SB();
  asm volatile("s_waitcnt lgkmcnt(0)" ::: "memory"); SB();
  asm volatile("ds_read_b128 a[76:79], %0 offset:6144" :  : "v"(aA[0]) : "memory"); SB();
  asm volatile("s_waitcnt lgkmcnt(0)" ::: "memory"); SB();
  asm volatile("ds_read_b128 a[92:95], %0 offset:6144" :  : "v"(aA[1]) : "memory"); SB();
  asm volatile("s_waitcnt lgkmcnt(0)" ::: "memory"); SB();
  asm volatile("buffer_load_dwordx4 v[184:187], %0, %1, %2 offen offset:0 sc1" :  : "v"(vB), "s"(rWS), "s"(65536u) : "memory"); SB();
  asm volatile("s_waitcnt lgkmcnt(0)" ::: "memory"); SB();
  asm volatile("buffer_load_dwordx4 v[188:191], %0, %1, %2 offen offset:1024 sc1" :  : "v"(vB), "s"(rWS), "s"(65536u) : "memory"); SB();
  asm volatile("s_waitcnt lgkmcnt(0)" ::: "memory"); SB();
  asm volatile("v_xor_b32 %0, 393216, %0" : "+v"(vB) :  : "memory"); SB();
  asm volatile("s_waitcnt lgkmcnt(0)" ::: "memory"); SB();
  asm volatile("buffer_load_dwordx4 v[192:195], %0, %1, %2 offen offset:0 sc1" :  : "v"(vB), "s"(rWS), "s"(0u) : "memory"); SB();
  asm volatile("s_waitcnt lgkmcnt(0)" ::: "memory"); SB();
  asm volatile("buffer_load_dwordx4 v[196:199], %0, %1, %2 offen offset:1024 sc1" :  : "v"(vB), "s"(rWS), "s"(0u) : "memory"); SB();
  asm volatile("s_waitcnt lgkmcnt(0)" ::: "memory"); SB();
  asm volatile("ds_write_b128 %0, %1 offset:0" :  : "v"(ew[0]), "v"(a1[0][0]) : "memory"); SB();
  asm volatile("s_waitcnt lgkmcnt(0)" ::: "memory"); SB();
  asm volatile("buffer_load_dwordx4 v[200:203], %0, %1, %2 offen offset:0 sc1" :  : "v"(vB), "s"(rWS), "s"(65536u) : "memory"); SB();
  asm volatile("s_waitcnt lgkmcnt(0)" ::: "memory"); SB();
  asm volatile("ds_write_b128 %0, %1 offset:1024" :  : "v"(ew[0]), "v"(a1[0][1]) : "memory"); SB();
  asm volatile("s_waitcnt lgkmcnt(0)" ::: "memory"); SB();
  asm volatile("buffer_load_dwordx4 v[204:207], %0, %1, %2 offen offset:1024 sc1" :  : "v"(vB), "s"(rWS), "s"(65536u) : "memory"); SB();
  asm volatile("s_waitcnt lgkmcnt(0)" ::: "memory"); SB();
  asm volatile("v_xor_b32 %0, 262144, %0" : "+v"(vB) :  : "memory"); SB();
  asm volatile("s_waitcnt lgkmcnt(0)" ::: "memory"); SB();
  asm volatile("buffer_load_dwordx4 v[208:211], %0, %1, %2 offen offset:0 sc1" :  : "v"(vB), "s"(rWS), "s"(2048u) : "memory"); SB();
  asm volatile("s_waitcnt lgkmcnt(0)" ::: "memory"); SB();
  asm volatile("buffer_load_dwordx4 v[212:215], %0, %1, %2 offen offset:1024 sc1" :  : "v"(vB), "s"(rWS), "s"(2048u) : "memory"); SB();
  asm volatile("s_waitcnt lgkmcnt(0)" ::: "memory"); SB();
  asm volatile("buffer_load_dwordx4 v[216:219], %0, %1, %2 offen offset:0 sc1" :  : "v"(vB), "s"(rWS), "s"(67584u) : "memory"); SB();
  asm volatile("s_waitcnt lgkmcnt(0)" ::: "memory"); SB();
  asm volatile("buffer_load_dwordx4 v[220:223], %0, %1, %2 offen offset:1024 sc1" :  : "v"(vB), "s"(rWS), "s"(67584u) : "memory"); SB();
  asm volatile("s_waitcnt lgkmcnt(0)" ::: "memory"); SB();
  asm volatile("v_xor_b32 %0, 131072, %0" : "+v"(vB) :  : "memory"); SB();
  asm volatile("s_waitcnt lgkmcnt(0)" ::: "memory"); SB();
  asm volatile("buffer_load_dwordx4 v[224:227], %0, %1, %2 offen offset:0 sc1" :  : "v"(vB), "s"(rWS), "s"(2048u) : "memory"); SB();
  asm volatile("s_waitcnt lgkmcnt(0)" ::: "memory"); SB();
  asm volatile("buffer_load_dwordx4 v[228:231], %0, %1, %2 offen offset:1024 sc1" :  : "v"(vB), "s"(rWS), "s"(2048u) : "memory"); SB();
  asm volatile("s_waitcnt lgkmcnt(0)" ::: "memory"); SB();
  asm volatile("ds_write_b128 %0, %1 offset:2048" :  : "v"(ew[0]), "v"(a1[0][2]) : "memory"); SB();
  asm volatile("s_waitcnt lgkmcnt(0)" ::: "memory"); SB();
  asm volatile("buffer_load_dwordx4 v[232:235], %0, %1, %2 offen offset:0 sc1" :  : "v"(vB), "s"(rWS), "s"(67584u) : "memory"); SB();
  asm volatile("s_waitcnt lgkmcnt(0)" ::: "memory"); SB();
  asm volatile("ds_write_b128 %0, %1 offset:0" :  : "v"(ew[1]), "v"(a1[1][0]) : "memory"); SB();
  asm volatile("s_waitcnt lgkmcnt(0)" ::: "memory"); SB();
  asm volatile("buffer_load_dwordx4 v[236:239], %0, %1, %2 offen offset:1024 sc1" :  : "v"(vB), "s"(rWS), "s"(67584u) : "memory"); SB();
  asm volatile("s_waitcnt lgkmcnt(0)" ::: "memory"); SB();
  asm volatile("v_xor_b32 %0, 393216, %0" : "+v"(vB) :  : "memory"); SB();
  asm volatile("s_waitcnt lgkmcnt(0)" ::: "memory"); SB();
  asm volatile("ds_write_b128 %0, %1 offset:1024" :  : "v"(ew[1]), "v"(a1[1][1]) : "memory"); SB();
  asm volatile("s_waitcnt lgkmcnt(0)" ::: "memory"); SB();
  asm volatile("buffer_load_dwordx4 v[240:243], %0, %1, %2 offen offset:0 sc1" :  : "v"(vB), "s"(rWS), "s"(2048u) : "memory"); SB();
  asm volatile("s_waitcnt lgkmcnt(0)" ::: "memory"); SB();
  asm volatile("ds_write_b128 %0, %1 offset:2048" :  : "v"(ew[1]), "v"(a1[1][2]) : "memory"); SB();
  asm volatile("s_waitcnt lgkmcnt(0)" ::: "memory"); SB();
  asm volatile("buffer_load_dwordx4 v[244:247], %0, %1, %2 offen offset:1024 sc1" :  : "v"(vB), "s"(rWS), "s"(2048u) : "memory"); SB();
  asm volatile("s_waitcnt lgkmcnt(0)" ::: "memory"); SB();
  asm volatile("buffer_load_dwordx4 v[248:251], %0, %1, %2 offen offset:0 sc1" :  : "v"(vB), "s"(rWS), "s"(67584u) : "memory"); SB();
  asm volatile("s_waitcnt lgkmcnt(0)" ::: "memory"); SB();
  asm volatile("buffer_load_dwordx4 v[252:255], %0, %1, %2 offen offset:1024 sc1" :  : "v"(vB), "s"(rWS), "s"(67584u) : "memory"); SB();
  asm volatile("s_waitcnt lgkmcnt(0)" ::: "memory"); SB();
  asm volatile("v_xor_b32 %0, 131072, %0" : "+v"(vB) :  : "memory"); SB();
  asm volatile("s_waitcnt lgkmcnt(0)" ::: "memory"); SB();
  asm volatile("s_add_u32 m0, %0, 135168" :  : "s"(mA) : "memory", "scc"); SB();
  asm volatile("s_waitcnt lgkmcnt(0)" ::: "memory"); SB();
  asm volatile("buffer_load_dwordx4 %0, %1, %2 offen lds" :  : "v"(voS), "s"(rA), "s"(ao[4]) : "memory"); SB();
  asm volatile("s_waitcnt lgkmcnt(0)" ::: "memory"); SB();
  asm volatile("s_waitcnt lgkmcnt(0)" ::: "memory"); SB();
  asm volatile("buffer_load_dwordx4 %0, %1, %2 offen offset:1024 lds" :  : "v"(voS), "s"(rA), "s"(ao[5]) : "memory"); SB();
  asm volatile("s_waitcnt lgkmcnt(0)" ::: "memory"); SB();
  asm volatile("s_waitcnt lgkmcnt(0) ; ELSE_END" ::: "memory"); SB();
  }
  ST(15);
  f32x4 g[2][4][2], pr[6], yq[8], bs4 = {bs, bs, bs, bs};
  u32x4 ho[3], ykv, y2;
  float gt[4], ge[4];
  unsigned vH_, pw2, pe[3], vX, vO, vO2, vP, vFH, vFY, vHr, hso[8], ys2, ydr, ykr, er, sFH, sFY;
  int dl;
  asm volatile("" : "+v"(bs4)); SB();
  asm volatile("s_waitcnt vmcnt(24)" :  :  : "memory"); SB();
  asm volatile("v_mfma_f32_16x16x32_bf16 %0, v[160:163], a[160:163], %1" : "=&v"(g[0][0][0]) : "v"(bs4) : ); SB();
  { unsigned lane = lane0, w = w0, rb = rb0, cb = cb0;
  asm volatile("" : "+v"(lane)); asm volatile("" : "+s"(w), "+s"(rb), "+s"(cb)); SB();

  asm volatile("v_mfma_f32_16x16x32_bf16 %0, v[160:163], a[164:167], 0" : "=&v"(g[0][1][0]) :  : ); SB();
  er = lb + 32768 + w * 6144 + l16; asm volatile("" : "+v"(er)); SB();
  asm volatile("v_mfma_f32_16x16x32_bf16 %0, v[160:163], a[168:171], 0" : "=&v"(g[0][2][0]) :  : ); SB();
  asm volatile("s_waitcnt lgkmcnt(0)\n\ts_barrier" :  :  : "memory"); SB();
  asm volatile("ds_read_b128 %0, %1 offset:0" : "=&v"(a1[0][0]) : "v"(er) : "memory"); SB();
  asm volatile("v_mfma_f32_16x16x32_bf16 %0, v[160:163], a[172:175], 0" : "=&v"(g[0][3][0]) :  : ); SB();
  asm volatile("ds_read_b128 a[96:99], %0 offset:32768" :  : "v"(aA[0]) : "memory"); SB();
  asm volatile("v_mfma_f32_16x16x32_bf16 %0, v[164:167], a[160:163], %1" : "=&v"(g[0][0][1]) : "v"(bs4) : ); SB();
  asm volatile("ds_read_b128 a[112:115], %0 offset:32768" :  : "v"(aA[1]) : "memory"); SB();
  asm volatile("v_mfma_f32_16x16x32_bf16 %0, v[164:167], a[164:167], 0" : "=&v"(g[0][1][1]) :  : ); SB();
  asm volatile("ds_read_b128 a[100:103], %0 offset:34816" :  : "v"(aA[0]) : "memory"); SB();
  asm volatile("v_mfma_f32_16x16x32_bf16 %0, v[164:167], a[168:171], 0" : "=&v"(g[0][2][1]) :  : ); SB();
  asm volatile("ds_read_b128 a[116:119], %0 offset:34816" :  : "v"(aA[1]) : "memory"); SB();
  asm volatile("v_mfma_f32_16x16x32_bf16 %0, v[164:167], a[172:175], 0" : "=&v"(g[0][3][1]) :  : ); SB();
  asm volatile("buffer_load_dwordx4 %0, %1, %2 offen offset:2048 lds" :  : "v"(voS), "s"(rA), "s"(ao[6]) : "memory"); SB();
  ST(25);
  asm volatile("s_waitcnt vmcnt(23)" :  :  : "memory"); SB();
  asm volatile("v_mfma_f32_16x16x32_bf16 %0, v[168:171], a[176:179], %0" : "+v"(g[0][0][0]) :  : ); SB();
  asm volatile("ds_read_b128 %0, %1 offset:1024" : "=&v"(a1[0][1]) : "v"(er) : "memory"); SB();
  asm volatile("v_mfma_f32_16x16x32_bf16 %0, v[168:171], a[180:183], %0" : "+v"(g[0][1][0]) :  : ); SB();
  asm volatile("ds_read_b128 %0, %1 offset:2048" : "=&v"(a1[0][2]) : "v"(er) : "memory"); SB();
  asm volatile("v_mfma_f32_16x16x32_bf16 %0, v[168:171], a[184:187], %0" : "+v"(g[0][2][0]) :  : ); SB();
  asm volatile("ds_read_b128 %0, %1 offset:3072" : "=&v"(a1[1][0]) : "v"(er) : "memory"); SB();
  asm volatile("v_mfma_f32_16x16x32_bf16 %0, v[168:171], a[188:191], %0" : "+v"(g[0][3][0]) :  : ); SB();
  asm volatile("buffer_load_dwordx4 v[160:163], %0, %1, %2 offen offset:0 sc1" :  : "v"(vB), "s"(rWS), "s"(0u) : "memory"); SB();
  asm volatile("v_mfma_f32_16x16x32_bf16 %0, v[172:175], a[176:179], %0" : "+v"(g[0][0][1]) :  : ); SB();
  asm volatile("ds_read_b128 %0, %1 offset:4096" : "=&v"(a1[1][1]) : "v"(er) : "memory"); SB();
  asm volatile("v_mfma_f32_16x16x32_bf16 %0, v[172:175], a[180:183], %0" : "+v"(g[0][1][1]) :  : ); SB();
  asm volatile("ds_read_b128 %0, %1 offset:5120" : "=&v"(a1[1][2]) : "v"(er) : "memory"); SB();
  asm volatile("v_mfma_f32_16x16x32_bf16 %0, v[172:175], a[184:187], %0" : "+v"(g[0][2][1]) :  : ); SB();
  ydr = lb + 0 + (w * 2 + (lane >> 5)) * 1024 + (lane & 1) * 512; asm volatile("" : "+v"(ydr)); SB();
  asm volatile("v_mfma_f32_16x16x32_bf16 %0, v[172:175], a[188:191], %0" : "+v"(g[0][3][1]) :  : ); SB();
  asm volatile("buffer_load_dwordx4 v[164:167], %0, %1, %2 offen offset:1024 sc1" :  : "v"(vB), "s"(rWS), "s"(0u) : "memory"); SB();
  asm volatile("s_waitcnt vmcnt(23)" :  :  : "memory"); SB();
  asm volatile("v_mfma_f32_16x16x32_bf16 %0, v[176:179], a[192:195], %0" : "+v"(g[0][0][0]) :  : ); SB();
  ydr = ydr + ((((lane >> 1) & 15) ^ ((lane & 1) << 3)) << 4); asm volatile("" : "+v"(ydr)); SB();
  asm volatile("v_mfma_f32_16x16x32_bf16 %0, v[176:179], a[196:199], %0" : "+v"(g[0][1][0]) :  : ); SB();
  ykr = lb + 59520 + 512 + w * 1024 + l16; asm volatile("" : "+v"(ykr)); SB();
  asm volatile("v_mfma_f32_16x16x32_bf16 %0, v[176:179], a[200:203], %0" : "+v"(g[0][2][0]) :  : ); SB();
  asm volatile("buffer_load_dwordx4 v[168:171], %0, %1, %2 offen offset:0 sc1" :  : "v"(vB), "s"(rWS), "s"(65536u) : "memory"); SB();
  pw2 = er + (122880 - 32768); asm volatile("" : "+v"(pw2)); SB();
  asm volatile("v_mfma_f32_16x16x32_bf16 %0, v[176:179], a[204:207], %0" : "+v"(g[0][3][0]) :  : ); SB();
  pe[0] = lb + 122880 + (w ^ 1) * 6144 + l16; asm volatile("" : "+v"(pe[0])); SB();
  asm volatile("v_mfma_f32_16x16x32_bf16 %0, v[180:183], a[192:195], %0" : "+v"(g[0][0][1]) :  : ); SB();
  pe[1] = lb + 122880 + (w ^ 2) * 6144 + l16; asm volatile("" : "+v"(pe[1])); SB();
  asm volatile("v_mfma_f32_16x16x32_bf16 %0, v[180:183], a[196:199], %0" : "+v"(g[0][1][1]) :  : ); SB();
  pe[2] = lb + 122880 + (w ^ 3) * 6144 + l16; asm volatile("" : "+v"(pe[2])); SB();
  asm volatile("v_mfma_f32_16x16x32_bf16 %0, v[180:183], a[200:203], %0" : "+v"(g[0][2][1]) :  : ); SB();
  asm volatile("buffer_load_dwordx4 v[172:175], %0, %1, %2 offen offset:1024 sc1" :  : "v"(vB), "s"(rWS), "s"(65536u) : "memory"); SB();
  asm volatile("s_waitcnt lgkmcnt(0)" : "+v"(a1[0][0]), "+v"(a1[0][1]), "+v"(a1[0][2]), "+v"(a1[1][0]), "+v"(a1[1][1]), "+v"(a1[1][2]) :  : "memory"); SB();
  asm volatile("v_mfma_f32_16x16x32_bf16 %0, v[180:183], a[204:207], %0" : "+v"(g[0][3][1]) :  : ); SB();
  dl = (int)(wm * 12928 - wn * 256 + w * 128) - 2 * 12928; asm volatile("" : "+s"(dl)); SB();
  asm volatile("s_waitcnt vmcnt(23)" :  :  : "memory"); SB();
  asm volatile("v_mfma_f32_16x16x32_bf16 %0, v[184:187], a[208:211], %0" : "+v"(g[0][0][0]) :  : ); SB();
  asm volatile("v_xor_b32 %0, 917504, %0" : "+v"(vB) :  : "memory"); SB();
  asm volatile("v_add_u32 %0, %1, %0" : "+v"(aa[0]) : "s"(dl) : ); SB();
  asm volatile("v_mfma_f32_16x16x32_bf16 %0, v[184:187], a[212:215], %0" : "+v"(g[0][1][0]) :  : ); SB();
  asm volatile("buffer_load_dwordx4 v[176:179], %0, %1, %2 offen offset:0 sc1" :  : "v"(vB), "s"(rWS), "s"(0u) : "memory"); SB();
  asm volatile("v_add_u32 %0, %1, %0" : "+v"(aa[1]) : "s"(dl) : ); SB();
  asm volatile("v_mfma_f32_16x16x32_bf16 %0, v[184:187], a[216:219], %0" : "+v"(g[0][2][0]) :  : ); SB();
  asm volatile("buffer_load_dwordx4 v[180:183], %0, %1, %2 offen offset:1024 sc1" :  : "v"(vB), "s"(rWS), "s"(0u) : "memory"); SB();
  asm volatile("v_add_u32 %0, %1, %0" : "+v"(aa[2]) : "s"(dl) : ); SB();
  asm volatile("v_mfma_f32_16x16x32_bf16 %0, v[184:187], a[220:223], %0" : "+v"(g[0][3][0]) :  : ); SB();
  asm volatile("v_add_u32 %0, %1, %0" : "+v"(aa[3]) : "s"(dl) : ); SB();
  asm volatile("v_mfma_f32_16x16x32_bf16 %0, v[188:191], a[208:211], %0" : "+v"(g[0][0][1]) :  : ); SB();
  asm volatile("v_add_u32 %0, %1, %0" : "+v"(bb) : "s"(dl) : ); SB();
  asm volatile("v_mfma_f32_16x16x32_bf16 %0, v[188:191], a[212:215], %0" : "+v"(g[0][1][1]) :  : ); SB();
  asm volatile("buffer_load_dwordx4 %0, %1, %2 offen offset:3072 lds" :  : "v"(voS), "s"(rA), "s"(ao[7]) : "memory"); SB();
  asm volatile("v_mul_f32 %0, %1, %1" : "=v"(gt[0]) : "v"(a1[0][0][0]) : ); SB();
  asm volatile("v_mfma_f32_16x16x32_bf16 %0, v[188:191], a[216:219], %0" : "+v"(g[0][2][1]) :  : ); SB();
  asm volatile("v_xor_b32 %0, 128, %0" : "+v"(voS) :  : "memory"); SB();
  asm volatile("v_mul_f32 %0, %1, %1" : "=v"(gt[1]) : "v"(a1[0][0][1]) : ); SB();
  asm volatile("v_mfma_f32_16x16x32_bf16 %0, v[188:191], a[220:223], %0" : "+v"(g[0][3][1]) :  : ); SB();
  asm volatile("v_mul_f32 %0, %1, %1" : "=v"(gt[2]) : "v"(a1[0][0][2]) : ); SB();
  asm volatile("s_waitcnt vmcnt(24)" :  :  : "memory"); SB();
  asm volatile("v_mfma_f32_16x16x32_bf16 %0, v[192:195], a[224:227], %0" : "+v"(g[0][0][0]) :  : ); SB();
  asm volatile("v_mul_f32 %0, %1, %1" : "=v"(gt[3]) : "v"(a1[0][0][3]) : ); SB();
  asm volatile("v_mfma_f32_16x16x32_bf16 %0, v[192:195], a[228:231], %0" : "+v"(g[0][1][0]) :  : ); SB();
  asm volatile("buffer_load_dwordx4 v[184:187], %0, %1, %2 offen offset:0 sc1" :  : "v"(vB), "s"(rWS), "s"(65536u) : "memory"); SB();
  asm volatile("v_fma_f32 %0, %0, %1, %2" : "+v"(gt[0]) : "v"(k2[0]), "v"(k1[0]) : ); SB();
  asm volatile("v_mfma_f32_16x16x32_bf16 %0, v[192:195], a[232:235], %0" : "+v"(g[0][2][0]) :  : ); SB();
  asm volatile("v_fma_f32 %0, %0, %1, %2" : "+v"(gt[1]) : "v"(k2[0]), "v"(k1[0]) : ); SB();
  asm volatile("v_mfma_f32_16x16x32_bf16 %0, v[192:195], a[236:239], %0" : "+v"(g[0][3][0]) :  : ); SB();
  asm volatile("v_fma_f32 %0, %0, %1, %2" : "+v"(gt[2]) : "v"(k2[0]), "v"(k1[0]) : ); SB();
  asm volatile("v_mfma_f32_16x16x32_bf16 %0, v[196:199], a[224:227], %0" : "+v"(g[0][0][1]) :  : ); SB();
  asm volatile("buffer_load_dwordx4 v[188:191], %0, %1, %2 offen offset:1024 sc1" :  : "v"(vB), "s"(rWS), "s"(65536u) : "memory"); SB();
  asm volatile("v_fma_f32 %0, %0, %1, %2" : "+v"(gt[3]) : "v"(k2[0]), "v"(k1[0]) : ); SB();
  asm volatile("v_mfma_f32_16x16x32_bf16 %0, v[196:199], a[228:231], %0" : "+v"(g[0][1][1]) :  : ); SB();
  asm volatile("v_xor_b32 %0, 131072, %0" : "+v"(vB) :  : "memory"); SB();
  asm volatile("v_mul_f32 %0, %0, %1" : "+v"(gt[0]) : "v"(a1[0][0][0]) : ); SB();
  asm volatile("v_mfma_f32_16x16x32_bf16 %0, v[196:199], a[232:235], %0" : "+v"(g[0][2][1]) :  : ); SB();
  asm volatile("v_mul_f32 %0, %0, %1" : "+v"(gt[1]) : "v"(a1[0][0][1]) : ); SB();
  asm volatile("v_mfma_f32_16x16x32_bf16 %0, v[196:199], a[236:239], %0" : "+v"(g[0][3][1]) :  : ); SB();
  asm volatile("v_mul_f32 %0, %0, %1" : "+v"(gt[2]) : "v"(a1[0][0][2]) : ); SB();
  asm volatile("s_waitcnt vmcnt(24)" :  :  : "memory"); SB();
  asm volatile("v_mfma_f32_16x16x32_bf16 %0, v[200:203], a[240:243], %0" : "+v"(g[0][0][0]) :  : ); SB();
  asm volatile("buffer_load_dwordx4 v[192:195], %0, %1, %2 offen offset:0 sc1" :  : "v"(vB), "s"(rWS), "s"(0u) : "memory"); SB();
  asm volatile("v_mul_f32 %0, %0, %1" : "+v"(gt[3]) : "v"(a1[0][0][3]) : ); SB();
  asm volatile("v_mfma_f32_16x16x32_bf16 %0, v[200:203], a[244:247], %0" : "+v"(g[0][1][0]) :  : ); SB();
  asm volatile("v_exp_f32 %0, %1" : "=v"(ge[0]) : "v"(gt[0]) : ); SB();
  asm volatile("v_mfma_f32_16x16x32_bf16 %0, v[200:203], a[248:251], %0" : "+v"(g[0][2][0]) :  : ); SB();
  asm volatile("v_exp_f32 %0, %1" : "=v"(ge[1]) : "v"(gt[1]) : ); SB();
  asm volatile("v_mfma_f32_16x16x32_bf16 %0, v[200:203], a[252:255], %0" : "+v"(g[0][3][0]) :  : ); SB();
  asm volatile("s_waitcnt vmcnt(11)" :  :  : "memory"); SB();
  asm volatile("ds_read_b128 a[104:107], %0 offset:36864" :  : "v"(aA[0]) : "memory"); SB();
  asm volatile("v_mfma_f32_16x16x32_bf16 %0, v[204:207], a[240:243], %0" : "+v"(g[0][0][1]) :  : ); SB();
  asm volatile("ds_read_b128 a[120:123], %0 offset:36864" :  : "v"(aA[1]) : "memory"); SB();
  asm volatile("v_exp_f32 %0, %1" : "=v"(ge[2]) : "v"(gt[2]) : ); SB();
  asm volatile("v_mfma_f32_16x16x32_bf16 %0, v[204:207], a[244:247], %0" : "+v"(g[0][1][1]) :  : ); SB();
  asm volatile("buffer_load_dwordx4 v[196:199], %0, %1, %2 offen offset:1024 sc1" :  : "v"(vB), "s"(rWS), "s"(0u) : "memory"); SB();
  asm volatile("v_exp_f32 %0, %1" : "=v"(ge[3]) : "v"(gt[3]) : ); SB();
  asm volatile("v_mfma_f32_16x16x32_bf16 %0, v[204:207], a[248:251], %0" : "+v"(g[0][2][1]) :  : ); SB();
  asm volatile("s_add_u32 m0, %0, 98304" :  : "s"(mA) : "memory", "scc"); SB();
  asm volatile("v_fma_f32 %0, %0, %1, %1" : "+v"(ge[0]) : "v"(xd[0]) : ); SB();
  asm volatile("v_mfma_f32_16x16x32_bf16 %0, v[204:207], a[252:255], %0" : "+v"(g[0][3][1]) :  : ); SB();
  asm volatile("buffer_load_dwordx4 %0, %1, %2 offen lds" :  : "v"(voS), "s"(rA), "s"(ao[0]) : "memory"); SB();
  asm volatile("v_mfma_f32_16x16x32_bf16 %0, v[208:211], a[160:163], %1" : "=&v"(g[1][0][0]) : "v"(bs4) : ); SB();
  asm volatile("v_fma_f32 %0, %0, %1, %1" : "+v"(ge[1]) : "v"(xd[0]) : ); SB();
  asm volatile("v_mfma_f32_16x16x32_bf16 %0, v[208:211], a[164:167], 0" : "=&v"(g[1][1][0]) :  : ); SB();
  asm volatile("v_fma_f32 %0, %0, %1, %1" : "+v"(ge[2]) : "v"(xd[0]) : ); SB();
  asm volatile("v_mfma_f32_16x16x32_bf16 %0, v[208:211], a[168:171], 0" : "=&v"(g[1][2][0]) :  : ); SB();
  asm volatile("buffer_load_dwordx4 v[200:203], %0, %1, %2 offen offset:0 sc1" :  : "v"(vB), "s"(rWS), "s"(65536u) : "memory"); SB();
  asm volatile("v_fma_f32 %0, %0, %1, %1" : "+v"(ge[3]) : "v"(xd[0]) : ); SB();
  asm volatile("v_mfma_f32_16x16x32_bf16 %0, v[208:211], a[172:175], 0" : "=&v"(g[1][3][0]) :  : ); SB();
  asm volatile("v_rcp_f32 %0, %1" : "=v"(gt[0]) : "v"(ge[0]) : ); SB();
  asm volatile("v_mfma_f32_16x16x32_bf16 %0, v[212:215], a[160:163], %1" : "=&v"(g[1][0][1]) : "v"(bs4) : ); SB();
  asm volatile("v_rcp_f32 %0, %1" : "=v"(gt[1]) : "v"(ge[1]) : ); SB();
  asm volatile("v_mfma_f32_16x16x32_bf16 %0, v[212:215], a[164:167], 0" : "=&v"(g[1][1][1]) :  : ); SB();
  asm volatile("v_rcp_f32 %0, %1" : "=v"(gt[2]) : "v"(ge[2]) : ); SB();
  asm volatile("v_mfma_f32_16x16x32_bf16 %0, v[212:215], a[168:171], 0" : "=&v"(g[1][2][1]) :  : ); SB();
  asm volatile("v_rcp_f32 %0, %1" : "=v"(gt[3]) : "v"(ge[3]) : ); SB();
  asm volatile("v_mfma_f32_16x16x32_bf16 %0, v[212:215], a[172:175], 0" : "=&v"(g[1][3][1]) :  : ); SB();
  asm volatile("v_mul_f32 %0, %0, %1" : "+v"(a1[0][0][0]) : "v"(gt[0]) : ); SB();
  asm volatile("v_mfma_f32_16x16x32_bf16 %0, v[216:219], a[176:179], %0" : "+v"(g[1][0][0]) :  : ); SB();
  asm volatile("buffer_load_dwordx4 v[204:207], %0, %1, %2 offen offset:1024 sc1" :  : "v"(vB), "s"(rWS), "s"(65536u) : "memory"); SB();
  asm volatile("v_mul_f32 %0, %0, %1" : "+v"(a1[0][0][1]) : "v"(gt[1]) : ); SB();
  asm volatile("v_mfma_f32_16x16x32_bf16 %0, v[216:219], a[180:183], %0" : "+v"(g[1][1][0]) :  : ); SB();
  asm volatile("v_xor_b32 %0, 393216, %0" : "+v"(vB) :  : "memory"); SB();
  asm volatile("v_mul_f32 %0, %0, %1" : "+v"(a1[0][0][2]) : "v"(gt[2]) : ); SB();
  asm volatile("v_mfma_f32_16x16x32_bf16 %0, v[216:219], a[184:187], %0" : "+v"(g[1][2][0]) :  : ); SB();
  asm volatile("v_mul_f32 %0, %0, %1" : "+v"(a1[0][0][3]) : "v"(gt[3]) : ); SB();
  asm volatile("v_mfma_f32_16x16x32_bf16 %0, v[216:219], a[188:191], %0" : "+v"(g[1][3][0]) :  : ); SB();
  asm volatile("v_mul_f32 %0, %1, %1" : "=v"(gt[0]) : "v"(a1[0][1][0]) : ); SB();
  asm volatile("v_mfma_f32_16x16x32_bf16 %0, v[220:223], a[176:179], %0" : "+v"(g[1][0][1]) :  : ); SB();
  asm volatile("buffer_load_dwordx4 v[208:211], %0, %1, %2 offen offset:0 sc1" :  : "v"(vB), "s"(rWS), "s"(0u) : "memory"); SB();
  asm volatile("v_mul_f32 %0, %1, %1" : "=v"(gt[1]) : "v"(a1[0][1][1]) : ); SB();
  asm volatile("v_mfma_f32_16x16x32_bf16 %0, v[220:223], a[180:183], %0" : "+v"(g[1][1][1]) :  : ); SB();
  asm volatile("v_mul_f32 %0, %1, %1" : "=v"(gt[2]) : "v"(a1[0][1][2]) : ); SB();
  asm volatile("v_mfma_f32_16x16x32_bf16 %0, v[220:223], a[184:187], %0" : "+v"(g[1][2][1]) :  : ); SB();
  asm volatile("v_mul_f32 %0, %1, %1" : "=v"(gt[3]) : "v"(a1[0][1][3]) : ); SB();
  asm volatile("v_mfma_f32_16x16x32_bf16 %0, v[220:223], a[188:191], %0" : "+v"(g[1][3][1]) :  : ); SB();
  asm volatile("v_fma_f32 %0, %0, %1, %2" : "+v"(gt[0]) : "v"(k2[0]), "v"(k1[0]) : ); SB();
  ST(26);
  asm volatile("v_mfma_f32_16x16x32_bf16 %0, v[224:227], a[192:195], %0" : "+v"(g[1][0][0]) :  : ); SB();
  vH_ = vH; asm volatile("" : "+v"(vH_)); SB();
  asm volatile("v_mfma_f32_16x16x32_bf16 %0, v[224:227], a[196:199], %0" : "+v"(g[1][1][0]) :  : ); SB();
  vO = W_WO + (cb * 120 + w * 30 + 0u * 15) * 1024 + l16; asm volatile("" : "+v"(vO)); SB();
  asm volatile("v_mfma_f32_16x16x32_bf16 %0, v[224:227], a[200:203], %0" : "+v"(g[1][2][0]) :  : ); SB();
  vO2 = vO + 15360 - 0u * 30720; asm volatile("" : "+v"(vO2)); SB();
  asm volatile("v_mfma_f32_16x16x32_bf16 %0, v[224:227], a[204:207], %0" : "+v"(g[1][3][0]) :  : ); SB();
  vFH = W_FH + rb * 256 + w * 64 + (lane & 7) * 8; asm volatile("" : "+v"(vFH)); SB();
  asm volatile("v_mfma_f32_16x16x32_bf16 %0, v[228:231], a[192:195], %0" : "+v"(g[1][0][1]) :  : ); SB();
  vHr = W_HH + (rb * 96 + w * 6) * 1024 + l16; asm volatile("" : "+v"(vHr)); SB();
  asm volatile("v_mfma_f32_16x16x32_bf16 %0, v[228:231], a[196:199], %0" : "+v"(g[1][1][1]) :  : ); SB();
  vX = (rb * 64 + w * 16 + l15) * 1280; asm volatile("" : "+v"(vX)); SB();
  asm volatile("v_mfma_f32_16x16x32_bf16 %0, v[228:231], a[200:203], %0" : "+v"(g[1][2][1]) :  : ); SB();
  vX = vX + cb * 160 + 8 * g4; asm volatile("" : "+v"(vX)); SB();
  asm volatile("v_mfma_f32_16x16x32_bf16 %0, v[228:231], a[204:207], %0" : "+v"(g[1][3][1]) :  : ); SB();
  sFH = W_FH + ((rb * 8 + cb) * 4 + w) * 8; asm volatile("" : "+s"(sFH)); SB();
  asm volatile("v_mfma_f32_16x16x32_bf16 %0, v[232:235], a[208:211], %0" : "+v"(g[1][0][0]) :  : ); SB();
  asm volatile("buffer_load_dwordx4 v[212:215], %0, %1, %2 offen offset:1024 sc1" :  : "v"(vB), "s"(rWS), "s"(0u) : "memory"); SB();
  asm volatile("v_mov_b32 %0, -1" : "=v"(ho[2][0]) :  : ); SB();
  asm volatile("v_mfma_f32_16x16x32_bf16 %0, v[232:235], a[212:215], %0" : "+v"(g[1][1][0]) :  : ); SB();
  asm volatile("buffer_load_dwordx4 %0, %1, %2 offen offset:1024 lds" :  : "v"(voS), "s"(rA), "s"(ao[1]) : "memory"); SB();
  asm volatile("v_mov_b32 %0, -1" : "=v"(ho[2][1]) :  : ); SB();
  asm volatile("v_mfma_f32_16x16x32_bf16 %0, v[232:235], a[216:219], %0" : "+v"(g[1][2][0]) :  : ); SB();
  asm volatile("v_mov_b32 %0, -1" : "=v"(ho[2][2]) :  : ); SB();
  asm volatile("v_mfma_f32_16x16x32_bf16 %0, v[232:235], a[220:223], %0" : "+v"(g[1][3][0]) :  : ); SB();
  asm volatile("v_mov_b32 %0, -1" : "=v"(ho[2][3]) :  : ); SB();
  asm volatile("v_mfma_f32_16x16x32_bf16 %0, v[236:239], a[208:211], %0" : "+v"(g[1][0][1]) :  : ); SB();
  hso[0] = (w ^ 0) * 24576 + 0u * 3072; asm volatile("" : "+s"(hso[0])); SB();
  hso[1] = hso[0] ^ 3072; asm volatile("" : "+s"(hso[1])); SB();
  asm volatile("v_mfma_f32_16x16x32_bf16 %0, v[236:239], a[212:215], %0" : "+v"(g[1][1][1]) :  : ); SB();
  hso[2] = (w ^ 1) * 24576 + 0u * 3072; asm volatile("" : "+s"(hso[2])); SB();
  hso[3] = hso[2] ^ 3072; asm volatile("" : "+s"(hso[3])); SB();
  asm volatile("v_mfma_f32_16x16x32_bf16 %0, v[236:239], a[216:219], %0" : "+v"(g[1][2][1]) :  : ); SB();
  hso[4] = (w ^ 2) * 24576 + 0u * 3072; asm volatile("" : "+s"(hso[4])); SB();
  hso[5] = hso[4] ^ 3072; asm volatile("" : "+s"(hso[5])); SB();
  asm volatile("v_mfma_f32_16x16x32_bf16 %0, v[236:239], a[220:223], %0" : "+v"(g[1][3][1]) :  : ); SB();
  hso[6] = (w ^ 3) * 24576 + 0u * 3072; asm volatile("" : "+s"(hso[6])); SB();
  asm volatile("v_mfma_f32_16x16x32_bf16 %0, v[240:243], a[224:227], %0" : "+v"(g[1][0][0]) :  : ); SB();
  hso[7] = hso[6] ^ 3072; asm volatile("" : "+s"(hso[7])); SB();
  asm volatile("buffer_load_dwordx4 %0, %1, %2 offen offset:2048 lds" :  : "v"(voS), "s"(rA), "s"(ao[2]) : "memory"); SB();
  asm volatile("v_mfma_f32_16x16x32_bf16 %0, v[240:243], a[228:231], %0" : "+v"(g[1][1][0]) :  : ); SB();
  asm volatile("buffer_load_dwordx4 %0, %1, %2 offen offset:3072 lds" :  : "v"(voS), "s"(rA), "s"(ao[3]) : "memory"); SB();
  asm volatile("s_waitcnt vmcnt(12)" :  :  : "memory"); SB();
  asm volatile("v_mfma_f32_16x16x32_bf16 %0, v[240:243], a[232:235], %0" : "+v"(g[1][2][0]) :  : ); SB();
  asm volatile("ds_read_b128 a[108:111], %0 offset:38912" :  : "v"(aA[0]) : "memory"); SB();
  asm volatile("v_fma_f32 %0, %0, %1, %2" : "+v"(gt[1]) : "v"(k2[0]), "v"(k1[0]) : ); SB();
  asm volatile("v_mfma_f32_16x16x32_bf16 %0, v[240:243], a[236:239], %0" : "+v"(g[1][3][0]) :  : ); SB();
  asm volatile("ds_read_b128 a[124:127], %0 offset:38912" :  : "v"(aA[1]) : "memory"); SB();
  asm volatile("s_add_u32 m0, %0, 102400" :  : "s"(mA) : "memory", "scc"); SB();
  asm volatile("v_mfma_f32_16x16x32_bf16 %0, v[244:247], a[224:227], %0" : "+v"(g[1][0][1]) :  : ); SB();
  asm volatile("buffer_load_dwordx4 %0, %1, %2 offen lds" :  : "v"(voS), "s"(rA), "s"(ao[4]) : "memory"); SB();
  asm volatile("v_fma_f32 %0, %0, %1, %2" : "+v"(gt[2]) : "v"(k2[0]), "v"(k1[0]) : ); SB();
  asm volatile("v_mfma_f32_16x16x32_bf16 %0, v[244:247], a[228:231], %0" : "+v"(g[1][1][1]) :  : ); SB();
  asm volatile("v_fma_f32 %0, %0, %1, %2" : "+v"(gt[3]) : "v"(k2[0]), "v"(k1[0]) : ); SB();
  asm volatile("v_mfma_f32_16x16x32_bf16 %0, v[244:247], a[232:235], %0" : "+v"(g[1][2][1]) :  : ); SB();
  asm volatile("v_mul_f32 %0, %0, %1" : "+v"(gt[0]) : "v"(a1[0][1][0]) : ); SB();
  asm volatile("v_mfma_f32_16x16x32_bf16 %0, v[244:247], a[236:239], %0" : "+v"(g[1][3][1]) :  : ); SB();
  asm volatile("buffer_load_dwordx4 %0, %1, %2 offen offset:1024 lds" :  : "v"(voS), "s"(rA), "s"(ao[5]) : "memory"); SB();
  asm volatile("v_mfma_f32_16x16x32_bf16 %0, v[248:251], a[240:243], %0" : "+v"(g[1][0][0]) :  : ); SB();
  asm volatile("v_mul_f32 %0, %0, %1" : "+v"(gt[1]) : "v"(a1[0][1][1]) : ); SB();
  asm volatile("v_mfma_f32_16x16x32_bf16 %0, v[248:251], a[244:247], %0" : "+v"(g[1][1][0]) :  : ); SB();
  asm volatile("v_mul_f32 %0, %0, %1" : "+v"(gt[2]) : "v"(a1[0][1][2]) : ); SB();
  asm volatile("v_mfma_f32_16x16x32_bf16 %0, v[248:251], a[248:251], %0" : "+v"(g[1][2][0]) :  : ); SB();
  asm volatile("v_mul_f32 %0, %0, %1" : "+v"(gt[3]) : "v"(a1[0][1][3]) : ); SB();
  asm volatile("v_mfma_f32_16x16x32_bf16 %0, v[248:251], a[252:255], %0" : "+v"(g[1][3][0]) :  : ); SB();
  asm volatile("buffer_load_dwordx4 %0, %1, %2 offen offset:2048 lds" :  : "v"(voS), "s"(rA), "s"(ao[6]) : "memory"); SB();
  asm volatile("v_exp_f32 %0, %1" : "=v"(ge[0]) : "v"(gt[0]) : ); SB();
  asm volatile("v_mfma_f32_16x16x32_bf16 %0, v[252:255], a[240:243], %0" : "+v"(g[1][0][1]) :  : ); SB();
  asm volatile("v_exp_f32 %0, %1" : "=v"(ge[1]) : "v"(gt[1]) : ); SB();
  asm volatile("v_mfma_f32_16x16x32_bf16 %0, v[252:255], a[244:247], %0" : "+v"(g[1][1][1]) :  : ); SB();
  asm volatile("v_exp_f32 %0, %1" : "=v"(ge[2]) : "v"(gt[2]) : ); SB();
  asm volatile("v_mfma_f32_16x16x32_bf16 %0, v[252:255], a[248:251], %0" : "+v"(g[1][2][1]) :  : ); SB();
  asm volatile("buffer_load_dwordx4 %0, %1, %2 offen offset:3072 lds" :  : "v"(voS), "s"(rA), "s"(ao[7]) : "memory"); SB();
  asm volatile("v_exp_f32 %0, %1" : "=v"(ge[3]) : "v"(gt[3]) : ); SB();
  asm volatile("v_mfma_f32_16x16x32_bf16 %0, v[252:255], a[252:255], %0" : "+v"(g[1][3][1]) :  : ); SB();
  asm volatile("v_fma_f32 %0, %0, %1, %1" : "+v"(ge[0]) : "v"(xd[0]) : ); SB();
  asm volatile("v_mfma_f32_16x16x32_bf16 %0, v[160:163], a[0:3], %0" : "+v"(g[0][0][0]) :  : ); SB();
  asm volatile("v_fma_f32 %0, %0, %1, %1" : "+v"(ge[1]) : "v"(xd[0]) : ); SB();
  asm volatile("v_mfma_f32_16x16x32_bf16 %0, v[160:163], a[4:7], %0" : "+v"(g[0][1][0]) :  : ); SB();
  asm volatile("buffer_load_dwordx4 v[216:219], %0, %1, %2 offen offset:0 sc1" :  : "v"(vB), "s"(rWS), "s"(65536u) : "memory"); SB();
  asm volatile("v_fma_f32 %0, %0, %1, %1" : "+v"(ge[2]) : "v"(xd[0]) : ); SB();
  asm volatile("v_mfma_f32_16x16x32_bf16 %0, v[160:163], a[8:11], %0" : "+v"(g[0][2][0]) :  : ); SB();
  asm volatile("v_fma_f32 %0, %0, %1, %1" : "+v"(ge[3]) : "v"(xd[0]) : ); SB();
  asm volatile("v_mfma_f32_16x16x32_bf16 %0, v[160:163], a[12:15], %0" : "+v"(g[0][3][0]) :  : ); SB();
  asm volatile("v_rcp_f32 %0, %1" : "=v"(gt[0]) : "v"(ge[0]) : ); SB();
  asm volatile("v_mfma_f32_16x16x32_bf16 %0, v[164:167], a[0:3], %0" : "+v"(g[0][0][1]) :  : ); SB();
  asm volatile("v_rcp_f32 %0, %1" : "=v"(gt[1]) : "v"(ge[1]) : ); SB();
  asm volatile("v_mfma_f32_16x16x32_bf16 %0, v[164:167], a[4:7], %0" : "+v"(g[0][1][1]) :  : ); SB();
  asm volatile("buffer_load_dwordx4 v[220:223], %0, %1, %2 offen offset:1024 sc1" :  : "v"(vB), "s"(rWS), "s"(65536u) : "memory"); SB();
  asm volatile("v_rcp_f32 %0, %1" : "=v"(gt[2]) : "v"(ge[2]) : ); SB();
  asm volatile("v_mfma_f32_16x16x32_bf16 %0, v[164:167], a[8:11], %0" : "+v"(g[0][2][1]) :  : ); SB();
  asm volatile("v_xor_b32 %0, 131072, %0" : "+v"(vB) :  : "memory"); SB();
  asm volatile("v_rcp_f32 %0, %1" : "=v"(gt[3]) : "v"(ge[3]) : ); SB();
  asm volatile("v_mfma_f32_16x16x32_bf16 %0, v[164:167], a[12:15], %0" : "+v"(g[0][3][1]) :  : ); SB();
  asm volatile("v_mul_f32 %0, %0, %1" : "+v"(a1[0][1][0]) : "v"(gt[0]) : ); SB();
  asm volatile("v_mfma_f32_16x16x32_bf16 %0, v[168:171], a[16:19], %0" : "+v"(g[0][0][0]) :  : ); SB();
  asm volatile("buffer_load_dwordx4 v[224:227], %0, %1, %2 offen offset:0 sc1" :  : "v"(vB), "s"(rWS), "s"(0u) : "memory"); SB();
  asm volatile("v_mul_f32 %0, %0, %1" : "+v"(a1[0][1][1]) : "v"(gt[1]) : ); SB();
  asm volatile("v_mfma_f32_16x16x32_bf16 %0, v[168:171], a[20:23], %0" : "+v"(g[0][1][0]) :  : ); SB();
  asm volatile("v_mul_f32 %0, %0, %1" : "+v"(a1[0][1][2]) : "v"(gt[2]) : ); SB();
  asm volatile("v_mfma_f32_16x16x32_bf16 %0, v[168:171], a[24:27], %0" : "+v"(g[0][2][0]) :  : ); SB();
  asm volatile("v_mul_f32 %0, %0, %1" : "+v"(a1[0][1][3]) : "v"(gt[3]) : ); SB();
  asm volatile("v_mfma_f32_16x16x32_bf16 %0, v[168:171], a[28:31], %0" : "+v"(g[0][3][0]) :  : ); SB();
  asm volatile("v_mul_f32 %0, %1, %1" : "=v"(gt[0]) : "v"(a1[0][2][0]) : ); SB();
  asm volatile("v_mfma_f32_16x16x32_bf16 %0, v[172:175], a[16:19], %0" : "+v"(g[0][0][1]) :  : ); SB();
  asm volatile("buffer_load_dwordx4 v[228:231], %0, %1, %2 offen offset:1024 sc1" :  : "v"(vB), "s"(rWS), "s"(0u) : "memory"); SB();
  asm volatile("v_mul_f32 %0, %1, %1" : "=v"(gt[1]) : "v"(a1[0][2][1]) : ); SB();
  asm volatile("v_mfma_f32_16x16x32_bf16 %0, v[172:175], a[20:23], %0" : "+v"(g[0][1][1]) :  : ); SB();
  asm volatile("v_mul_f32 %0, %1, %1" : "=v"(gt[2]) : "v"(a1[0][2][2]) : ); SB();
  asm volatile("v_mfma_f32_16x16x32_bf16 %0, v[172:175], a[24:27], %0" : "+v"(g[0][2][1]) :  : ); SB();
  asm volatile("v_mul_f32 %0, %1, %1" : "=v"(gt[3]) : "v"(a1[0][2][3]) : ); SB();
  asm volatile("v_mfma_f32_16x16x32_bf16 %0, v[172:175], a[28:31], %0" : "+v"(g[0][3][1]) :  : ); SB();
  asm volatile("buffer_load_dwordx4 v[232:235], %0, %1, %2 offen offset:0 sc1" :  : "v"(vB), "s"(rWS), "s"(65536u) : "memory"); SB();
  asm volatile("v_mfma_f32_16x16x32_bf16 %0, v[176:179], a[32:35], %0" : "+v"(g[0][0][0]) :  : ); SB();
  asm volatile("v_fma_f32 %0, %0, %1, %2" : "+v"(gt[0]) : "v"(k2[0]), "v"(k1[0]) : ); SB();
  asm volatile("v_mfma_f32_16x16x32_bf16 %0, v[176:179], a[36:39], %0" : "+v"(g[0][1][0]) :  : ); SB();
  asm volatile("v_fma_f32 %0, %0, %1, %2" : "+v"(gt[1]) : "v"(k2[0]), "v"(k1[0]) : ); SB();
  asm volatile("v_mfma_f32_16x16x32_bf16 %0, v[176:179], a[40:43], %0" : "+v"(g[0][2][0]) :  : ); SB();
  asm volatile("v_fma_f32 %0, %0, %1, %2" : "+v"(gt[2]) : "v"(k2[0]), "v"(k1[0]) : ); SB();
  asm volatile("v_mfma_f32_16x16x32_bf16 %0, v[176:179], a[44:47], %0" : "+v"(g[0][3][0]) :  : ); SB();
  asm volatile("buffer_load_dwordx4 v[236:239], %0, %1, %2 offen offset:1024 sc1" :  : "v"(vB), "s"(rWS), "s"(65536u) : "memory"); SB();
  asm volatile("v_fma_f32 %0, %0, %1, %2" : "+v"(gt[3]) : "v"(k2[0]), "v"(k1[0]) : ); SB();
  asm volatile("v_mfma_f32_16x16x32_bf16 %0, v[180:183], a[32:35], %0" : "+v"(g[0][0][1]) :  : ); SB();
  asm volatile("v_xor_b32 %0, 524288, %0" : "+v"(vB) :  : "memory"); SB();
  asm volatile("v_mul_f32 %0, %0, %1" : "+v"(gt[0]) : "v"(a1[0][2][0]) : ); SB();
  asm volatile("v_mfma_f32_16x16x32_bf16 %0, v[180:183], a[36:39], %0" : "+v"(g[0][1][1]) :  : ); SB();
  asm volatile("v_mul_f32 %0, %0, %1" : "+v"(gt[1]) : "v"(a1[0][2][1]) : ); SB();
  asm volatile("v_mfma_f32_16x16x32_bf16 %0, v[180:183], a[40:43], %0" : "+v"(g[0][2][1]) :  : ); SB();
  asm volatile("v_mul_f32 %0, %0, %1" : "+v"(gt[2]) : "v"(a1[0][2][2]) : ); SB();
  asm volatile("v_mfma_f32_16x16x32_bf16 %0, v[180:183], a[44:47], %0" : "+v"(g[0][3][1]) :  : ); SB();
  asm volatile("v_mul_f32 %0, %0, %1" : "+v"(gt[3]) : "v"(a1[0][2][3]) : ); SB();
  asm volatile("s_waitcnt vmcnt(20)" :  :  : "memory"); SB();
  asm volatile("v_mfma_f32_16x16x32_bf16 %0, v[184:187], a[48:51], %0" : "+v"(g[0][0][0]) :  : ); SB();
  asm volatile("buffer_load_dwordx4 v[240:243], %0, %1, %2 offen offset:0 sc1" :  : "v"(vB), "s"(rWS), "s"(2048u) : "memory"); SB();
  asm volatile("v_exp_f32 %0, %1" : "=v"(ge[0]) : "v"(gt[0]) : ); SB();
  asm volatile("v_mfma_f32_16x16x32_bf16 %0, v[184:187], a[52:55], %0" : "+v"(g[0][1][0]) :  : ); SB();
  asm volatile("v_exp_f32 %0, %1" : "=v"(ge[1]) : "v"(gt[1]) : ); SB();
  asm volatile("v_mfma_f32_16x16x32_bf16 %0, v[184:187], a[56:59], %0" : "+v"(g[0][2][0]) :  : ); SB();
  asm volatile("v_exp_f32 %0, %1" : "=v"(ge[2]) : "v"(gt[2]) : ); SB();
  asm volatile("v_mfma_f32_16x16x32_bf16 %0, v[184:187], a[60:63], %0" : "+v"(g[0][3][0]) :  : ); SB();
  asm volatile("s_waitcnt vmcnt(13)" :  :  : "memory"); SB();
  asm volatile("ds_read_b128 a[128:131], %0 offset:0" :  : "v"(aA[0]) : "memory"); SB();
  asm volatile("v_mfma_f32_16x16x32_bf16 %0, v[188:191], a[48:51], %0" : "+v"(g[0][0][1]) :  : ); SB();
  asm volatile("ds_read_b128 a[144:147], %0 offset:0" :  : "v"(aA[1]) : "memory"); SB();
  asm volatile("v_exp_f32 %0, %1" : "=v"(ge[3]) : "v"(gt[3]) : ); SB();
  asm volatile("v_mfma_f32_16x16x32_bf16 %0, v[188:191], a[52:55], %0" : "+v"(g[0][1][1]) :  : ); SB();
  asm volatile("buffer_load_dwordx4 v[244:247], %0, %1, %2 offen offset:1024 sc1" :  : "v"(vB), "s"(rWS), "s"(2048u) : "memory"); SB();
  asm volatile("v_fma_f32 %0, %0, %1, %1" : "+v"(ge[0]) : "v"(xd[0]) : ); SB();
  asm volatile("v_mfma_f32_16x16x32_bf16 %0, v[188:191], a[56:59], %0" : "+v"(g[0][2][1]) :  : ); SB();
  asm volatile("v_fma_f32 %0, %0, %1, %1" : "+v"(ge[1]) : "v"(xd[0]) : ); SB();
  asm volatile("v_mfma_f32_16x16x32_bf16 %0, v[188:191], a[60:63], %0" : "+v"(g[0][3][1]) :  : ); SB();
  asm volatile("v_fma_f32 %0, %0, %1, %1" : "+v"(ge[2]) : "v"(xd[0]) : ); SB();
  ST(27);
  asm volatile("v_mfma_f32_16x16x32_bf16 %0, v[192:195], a[64:67], %0" : "+v"(g[0][0][0]) :  : ); SB();
  asm volatile("buffer_load_dwordx4 v[248:251], %0, %1, %2 offen offset:0 sc1" :  : "v"(vB), "s"(rWS), "s"(67584u) : "memory"); SB();
  asm volatile("v_fma_f32 %0, %0, %1, %1" : "+v"(ge[3]) : "v"(xd[0]) : ); SB();
  asm volatile("v_mfma_f32_16x16x32_bf16 %0, v[192:195], a[68:71], %0" : "+v"(g[0][1][0]) :  : ); SB();
  asm volatile("v_rcp_f32 %0, %1" : "=v"(gt[0]) : "v"(ge[0]) : ); SB();
  asm volatile("v_mfma_f32_16x16x32_bf16 %0, v[192:195], a[72:75], %0" : "+v"(g[0][2][0]) :  : ); SB();
  asm volatile("v_rcp_f32 %0, %1" : "=v"(gt[1]) : "v"(ge[1]) : ); SB();
  asm volatile("v_mfma_f32_16x16x32_bf16 %0, v[192:195], a[76:79], %0" : "+v"(g[0][3][0]) :  : ); SB();
  asm volatile("v_rcp_f32 %0, %1" : "=v"(gt[2]) : "v"(ge[2]) : ); SB();
  asm volatile("v_mfma_f32_16x16x32_bf16 %0, v[196:199], a[64:67], %0" : "+v"(g[0][0][1]) :  : ); SB();
  asm volatile("s_waitcnt vmcnt(13)" :  :  : "memory"); SB();
  asm volatile("ds_read_b128 a[132:135], %0 offset:2048" :  : "v"(aA[0]) : "memory"); SB();
  asm volatile("v_mfma_f32_16x16x32_bf16 %0, v[196:199], a[68:71], %0" : "+v"(g[0][1][1]) :  : ); SB();
  asm volatile("ds_read_b128 a[148:151], %0 offset:2048" :  : "v"(aA[1]) : "memory"); SB();
  asm volatile("v_rcp_f32 %0, %1" : "=v"(gt[3]) : "v"(ge[3]) : ); SB();
  asm volatile("v_mfma_f32_16x16x32_bf16 %0, v[196:199], a[72:75], %0" : "+v"(g[0][2][1]) :  : ); SB();
  asm volatile("buffer_load_dwordx4 v[252:255], %0, %1, %2 offen offset:1024 sc1" :  : "v"(vB), "s"(rWS), "s"(67584u) : "memory"); SB();
  asm volatile("v_mul_f32 %0, %0, %1" : "+v"(a1[0][2][0]) : "v"(gt[0]) : ); SB();
  asm volatile("v_mfma_f32_16x16x32_bf16 %0, v[196:199], a[76:79], %0" : "+v"(g[0][3][1]) :  : ); SB();
  asm volatile("v_xor_b32 %0, 917504, %0" : "+v"(vB) :  : "memory"); SB();
  asm volatile("v_mfma_f32_16x16x32_bf16 %0, v[200:203], a[80:83], %0" : "+v"(g[0][0][0]) :  : ); SB();
  asm volatile("buffer_load_dwordx4 v[160:163], %0, %1, %2 offen offset:0 sc1" :  : "v"(vB), "s"(rWS), "s"(2048u) : "memory"); SB();
  asm volatile("v_mul_f32 %0, %0, %1" : "+v"(a1[0][2][1]) : "v"(gt[1]) : ); SB();
  asm volatile("v_mfma_f32_16x16x32_bf16 %0, v[200:203], a[84:87], %0" : "+v"(g[0][1][0]) :  : ); SB();
  asm volatile("v_mul_f32 %0, %0, %1" : "+v"(a1[0][2][2]) : "v"(gt[2]) : ); SB();
  asm volatile("v_mfma_f32_16x16x32_bf16 %0, v[200:203], a[88:91], %0" : "+v"(g[0][2][0]) :  : ); SB();
  asm volatile("v_mul_f32 %0, %0, %1" : "+v"(a1[0][2][3]) : "v"(gt[3]) : ); SB();
  asm volatile("v_mfma_f32_16x16x32_bf16 %0, v[200:203], a[92:95], %0" : "+v"(g[0][3][0]) :  : ); SB();
  asm volatile("s_waitcnt vmcnt(13)" :  :  : "memory"); SB();
  asm volatile("ds_read_b128 a[136:139], %0 offset:4096" :  : "v"(aA[0]) : "memory"); SB();
  asm volatile("v_mfma_f32_16x16x32_bf16 %0, v[204:207], a[80:83], %0" : "+v"(g[0][0][1]) :  : ); SB();
  asm volatile("ds_read_b128 a[152:155], %0 offset:4096" :  : "v"(aA[1]) : "memory"); SB();
  asm volatile("v_mul_f32 %0, %1, %1" : "=v"(gt[0]) : "v"(a1[1][0][0]) : ); SB();
  asm volatile("v_mfma_f32_16x16x32_bf16 %0, v[204:207], a[84:87], %0" : "+v"(g[0][1][1]) :  : ); SB();
  asm volatile("buffer_load_dwordx4 v[164:167], %0, %1, %2 offen offset:1024 sc1" :  : "v"(vB), "s"(rWS), "s"(2048u) : "memory"); SB();
  asm volatile("v_mul_f32 %0, %1, %1" : "=v"(gt[1]) : "v"(a1[1][0][1]) : ); SB();
  asm volatile("v_mfma_f32_16x16x32_bf16 %0, v[204:207], a[88:91], %0" : "+v"(g[0][2][1]) :  : ); SB();
  asm volatile("buffer_load_dwordx4 v[168:171], %0, %1, %2 offen offset:0 sc1" :  : "v"(vB), "s"(rWS), "s"(67584u) : "memory"); SB();
  asm volatile("v_mul_f32 %0, %1, %1" : "=v"(gt[2]) : "v"(a1[1][0][2]) : ); SB();
  asm volatile("v_mfma_f32_16x16x32_bf16 %0, v[204:207], a[92:95], %0" : "+v"(g[0][3][1]) :  : ); SB();
  asm volatile("v_mul_f32 %0, %1, %1" : "=v"(gt[3]) : "v"(a1[1][0][3]) : ); SB();
  asm volatile("v_mfma_f32_16x16x32_bf16 %0, v[208:211], a[96:99], %0" : "+v"(g[0][0][0]) :  : ); SB();
  asm volatile("v_fma_f32 %0, %0, %1, %2" : "+v"(gt[0]) : "v"(k2[0]), "v"(k1[0]) : ); SB();
  asm volatile("v_mfma_f32_16x16x32_bf16 %0, v[208:211], a[100:103], %0" : "+v"(g[0][1][0]) :  : ); SB();
  asm volatile("s_waitcnt vmcnt(13)" :  :  : "memory"); SB();
  asm volatile("v_fma_f32 %0, %0, %1, %2" : "+v"(gt[1]) : "v"(k2[0]), "v"(k1[0]) : ); SB();
  asm volatile("s_waitcnt lgkmcnt(2)\n\tv_mfma_f32_16x16x32_bf16 %0, v[208:211], a[104:107], %0" : "+v"(g[0][2][0]) :  : ); SB();
  asm volatile("ds_read_b128 a[140:143], %0 offset:6144" :  : "v"(aA[0]) : "memory"); SB();
  asm volatile("v_fma_f32 %0, %0, %1, %2" : "+v"(gt[2]) : "v"(k2[0]), "v"(k1[0]) : ); SB();
  asm volatile("v_mfma_f32_16x16x32_bf16 %0, v[208:211], a[108:111], %0" : "+v"(g[0][3][0]) :  : ); SB();
  asm volatile("ds_read_b128 a[156:159], %0 offset:6144" :  : "v"(aA[1]) : "memory"); SB();
  asm volatile("v_fma_f32 %0, %0, %1, %2" : "+v"(gt[3]) : "v"(k2[0]), "v"(k1[0]) : ); SB();
  asm volatile("v_mfma_f32_16x16x32_bf16 %0, v[212:215], a[96:99], %0" : "+v"(g[0][0][1]) :  : ); SB();
  asm volatile("buffer_load_dwordx4 v[172:175], %0, %1, %2 offen offset:1024 sc1" :  : "v"(vB), "s"(rWS), "s"(67584u) : "memory"); SB();
  asm volatile("v_mul_f32 %0, %0, %1" : "+v"(gt[0]) : "v"(a1[1][0][0]) : ); SB();
  asm volatile("v_mfma_f32_16x16x32_bf16 %0, v[212:215], a[100:103], %0" : "+v"(g[0][1][1]) :  : ); SB();
  asm volatile("v_xor_b32 %0, 131072, %0" : "+v"(vB) :  : "memory"); SB();
  asm volatile("v_mul_f32 %0, %0, %1" : "+v"(gt[1]) : "v"(a1[1][0][1]) : ); SB();
  asm volatile("v_mfma_f32_16x16x32_bf16 %0, v[212:215], a[104:107], %0" : "+v"(g[0][2][1]) :  : ); SB();
  asm volatile("buffer_load_dwordx4 v[176:179], %0, %1, %2 offen offset:0 sc1" :  : "v"(vB), "s"(rWS), "s"(2048u) : "memory"); SB();
  asm volatile("v_mul_f32 %0, %0, %1" : "+v"(gt[2]) : "v"(a1[1][0][2]) : ); SB();
  asm volatile("v_mfma_f32_16x16x32_bf16 %0, v[212:215], a[108:111], %0" : "+v"(g[0][3][1]) :  : ); SB();
  asm volatile("v_mul_f32 %0, %0, %1" : "+v"(gt[3]) : "v"(a1[1][0][3]) : ); SB();
  asm volatile("s_waitcnt vmcnt(13)" :  :  : "memory"); SB();
  asm volatile("v_mfma_f32_16x16x32_bf16 %0, v[216:219], a[112:115], %0" : "+v"(g[0][0][0]) :  : ); SB();
  asm volatile("buffer_load_dwordx4 v[180:183], %0, %1, %2 offen offset:1024 sc1" :  : "v"(vB), "s"(rWS), "s"(2048u) : "memory"); SB();
  asm volatile("v_exp_f32 %0, %1" : "=v"(ge[0]) : "v"(gt[0]) : ); SB();
  asm volatile("v_mfma_f32_16x16x32_bf16 %0, v[216:219], a[116:119], %0" : "+v"(g[0][1][0]) :  : ); SB();
  asm volatile("v_exp_f32 %0, %1" : "=v"(ge[1]) : "v"(gt[1]) : ); SB();
  asm volatile("v_mfma_f32_16x16x32_bf16 %0, v[216:219], a[120:123], %0" : "+v"(g[0][2][0]) :  : ); SB();
  asm volatile("v_exp_f32 %0, %1" : "=v"(ge[2]) : "v"(gt[2]) : ); SB();
  asm volatile("v_mfma_f32_16x16x32_bf16 %0, v[216:219], a[124:127], %0" : "+v"(g[0][3][0]) :  : ); SB();
  asm volatile("buffer_load_dwordx4 v[184:187], %0, %1, %2 offen offset:0 sc1" :  : "v"(vB), "s"(rWS), "s"(67584u) : "memory"); SB();
  asm volatile("v_exp_f32 %0, %1" : "=v"(ge[3]) : "v"(gt[3]) : ); SB();
  asm volatile("v_mfma_f32_16x16x32_bf16 %0, v[220:223], a[112:115], %0" : "+v"(g[0][0][1]) :  : ); SB();
  asm volatile("v_fma_f32 %0, %0, %1, %1" : "+v"(ge[0]) : "v"(xd[0]) : ); SB();
  asm volatile("v_mfma_f32_16x16x32_bf16 %0, v[220:223], a[116:119], %0" : "+v"(g[0][1][1]) :  : ); SB();
  asm volatile("v_fma_f32 %0, %0, %1, %1" : "+v"(ge[1]) : "v"(xd[0]) : ); SB();
  asm volatile("v_mfma_f32_16x16x32_bf16 %0, v[220:223], a[120:123], %0" : "+v"(g[0][2][1]) :  : ); SB();
  asm volatile("v_fma_f32 %0, %0, %1, %1" : "+v"(ge[2]) : "v"(xd[0]) : ); SB();
  asm volatile("v_mfma_f32_16x16x32_bf16 %0, v[220:223], a[124:127], %0" : "+v"(g[0][3][1]) :  : ); SB();
  asm volatile("buffer_load_dwordx4 v[188:191], %0, %1, %2 offen offset:1024 sc1" :  : "v"(vB), "s"(rWS), "s"(67584u) : "memory"); SB();
  asm volatile("s_waitcnt vmcnt(14)" :  :  : "memory"); SB();
  asm volatile("v_mfma_f32_16x16x32_bf16 %0, v[224:227], a[128:131], %0" : "+v"(g[0][0][0]) :  : ); SB();
  asm volatile("v_xor_b32 %0, 393216, %0" : "+v"(vB) :  : "memory"); SB();
  asm volatile("v_fma_f32 %0, %0, %1, %1" : "+v"(ge[3]) : "v"(xd[0]) : ); SB();
  asm volatile("v_mfma_f32_16x16x32_bf16 %0, v[224:227], a[132:135], %0" : "+v"(g[0][1][0]) :  : ); SB();
  asm volatile("v_rcp_f32 %0, %1" : "=v"(gt[0]) : "v"(ge[0]) : ); SB();
  asm volatile("s_waitcnt lgkmcnt(0)\n\tv_mfma_f32_16x16x32_bf16 %0, v[224:227], a[136:139], %0" : "+v"(g[0][2][0]) :  : ); SB();
  asm volatile("buffer_load_dwordx4 v[192:195], %0, %1, %2 offen offset:0 sc1" :  : "v"(vB), "s"(rWS), "s"(2048u) : "memory"); SB();
  asm volatile("v_rcp_f32 %0, %1" : "=v"(gt[1]) : "v"(ge[1]) : ); SB();
  asm volatile("v_mfma_f32_16x16x32_bf16 %0, v[224:227], a[140:143], %0" : "+v"(g[0][3][0]) :  : ); SB();
  asm volatile("v_rcp_f32 %0, %1" : "=v"(gt[2]) : "v"(ge[2]) : ); SB();
  asm volatile("v_mfma_f32_16x16x32_bf16 %0, v[228:231], a[128:131], %0" : "+v"(g[0][0][1]) :  : ); SB();
  asm volatile("v_rcp_f32 %0, %1" : "=v"(gt[3]) : "v"(ge[3]) : ); SB();
  asm volatile("v_mfma_f32_16x16x32_bf16 %0, v[228:231], a[132:135], %0" : "+v"(g[0][1][1]) :  : ); SB();
  asm volatile("v_mul_f32 %0, %0, %1" : "+v"(a1[1][0][0]) : "v"(gt[0]) : ); SB();
  asm volatile("v_mfma_f32_16x16x32_bf16 %0, v[228:231], a[136:139], %0" : "+v"(g[0][2][1]) :  : ); SB();
  asm volatile("buffer_load_dwordx4 v[196:199], %0, %1, %2 offen offset:1024 sc1" :  : "v"(vB), "s"(rWS), "s"(2048u) : "memory"); SB();
  asm volatile("v_mul_f32 %0, %0, %1" : "+v"(a1[1][0][1]) : "v"(gt[1]) : ); SB();
  asm volatile("v_mfma_f32_16x16x32_bf16 %0, v[228:231], a[140:143], %0" : "+v"(g[0][3][1]) :  : ); SB();
  asm volatile("v_mul_f32 %0, %0, %1" : "+v"(a1[1][0][2]) : "v"(gt[2]) : ); SB();
  asm volatile("s_waitcnt vmcnt(14)" :  :  : "memory"); SB();
  asm volatile("v_mfma_f32_16x16x32_bf16 %0, v[232:235], a[144:147], %0" : "+v"(g[0][0][0]) :  : ); SB();
  asm volatile("v_mul_f32 %0, %0, %1" : "+v"(a1[1][0][3]) : "v"(gt[3]) : ); SB();
  asm volatile("v_mfma_f32_16x16x32_bf16 %0, v[232:235], a[148:151], %0" : "+v"(g[0][1][0]) :  : ); SB();
  asm volatile("buffer_load_dwordx4 v[200:203], %0, %1, %2 offen offset:0 sc1" :  : "v"(vB), "s"(rWS), "s"(67584u) : "memory"); SB();
  asm volatile("v_mul_f32 %0, %1, %1" : "=v"(gt[0]) : "v"(a1[1][1][0]) : ); SB();
  asm volatile("v_mfma_f32_16x16x32_bf16 %0, v[232:235], a[152:155], %0" : "+v"(g[0][2][0]) :  : ); SB();
  asm volatile("v_mul_f32 %0, %1, %1" : "=v"(gt[1]) : "v"(a1[1][1][1]) : ); SB();
  asm volatile("v_mfma_f32_16x16x32_bf16 %0, v[232:235], a[156:159], %0" : "+v"(g[0][3][0]) :  : ); SB();
  asm volatile("v_mul_f32 %0, %1, %1" : "=v"(gt[2]) : "v"(a1[1][1][2]) : ); SB();
  asm volatile("v_mfma_f32_16x16x32_bf16 %0, v[236:239], a[144:147], %0" : "+v"(g[0][0][1]) :  : ); SB();
  asm volatile("v_mul_f32 %0, %1, %1" : "=v"(gt[3]) : "v"(a1[1][1][3]) : ); SB();
  asm volatile("v_mfma_f32_16x16x32_bf16 %0, v[236:239], a[148:151], %0" : "+v"(g[0][1][1]) :  : ); SB();
  asm volatile("buffer_load_dwordx4 v[204:207], %0, %1, %2 offen offset:1024 sc1" :  : "v"(vB), "s"(rWS), "s"(67584u) : "memory"); SB();
  asm volatile("v_fma_f32 %0, %0, %1, %2" : "+v"(gt[0]) : "v"(k2[0]), "v"(k1[0]) : ); SB();
  asm volatile("v_mfma_f32_16x16x32_bf16 %0, v[236:239], a[152:155], %0" : "+v"(g[0][2][1]) :  : ); SB();
  asm volatile("v_xor_b32 %0, 131072, %0" : "+v"(vB) :  : "memory"); SB();
  asm volatile("v_fma_f32 %0, %0, %1, %2" : "+v"(gt[1]) : "v"(k2[0]), "v"(k1[0]) : ); SB();
  asm volatile("v_mfma_f32_16x16x32_bf16 %0, v[236:239], a[156:159], %0" : "+v"(g[0][3][1]) :  : ); SB();
  asm volatile("v_fma_f32 %0, %0, %1, %2" : "+v"(gt[2]) : "v"(k2[0]), "v"(k1[0]) : ); SB();
  asm volatile("s_waitcnt vmcnt(14)" :  :  : "memory"); SB();
  asm volatile("v_mfma_f32_16x16x32_bf16 %0, v[240:243], a[0:3], %0" : "+v"(g[1][0][0]) :  : ); SB();
  asm volatile("s_barrier" :  :  : "memory"); SB();
  asm volatile("buffer_load_dwordx4 v[208:211], %0, %1, %2 offen offset:0 sc1" :  : "v"(vB), "s"(rWS), "s"(2048u) : "memory"); SB();
  asm volatile("v_mfma_f32_16x16x32_bf16 %0, v[240:243], a[4:7], %0" : "+v"(g[1][1][0]) :  : ); SB();
  asm volatile("v_fma_f32 %0, %0, %1, %2" : "+v"(gt[3]) : "v"(k2[0]), "v"(k1[0]) : ); SB();
  asm volatile("v_mfma_f32_16x16x32_bf16 %0, v[240:243], a[8:11], %0" : "+v"(g[1][2][0]) :  : ); SB();
  asm volatile("ds_write_b128 %0, %1 offset:0" :  : "v"(pw2), "v"(g[0][1][0]) : "memory"); SB();
  asm volatile("v_mfma_f32_16x16x32_bf16 %0, v[240:243], a[12:15], %0" : "+v"(g[1][3][0]) :  : ); SB();
  asm volatile("buffer_load_dwordx4 v[212:215], %0, %1, %2 offen offset:1024 sc1" :  : "v"(vB), "s"(rWS), "s"(2048u) : "memory"); SB();
  asm volatile("v_mul_f32 %0, %0, %1" : "+v"(gt[0]) : "v"(a1[1][1][0]) : ); SB();
  asm volatile("v_mfma_f32_16x16x32_bf16 %0, v[244:247], a[0:3], %0" : "+v"(g[1][0][1]) :  : ); SB();
  asm volatile("v_mul_f32 %0, %0, %1" : "+v"(gt[1]) : "v"(a1[1][1][1]) : ); SB();
  asm volatile("v_mfma_f32_16x16x32_bf16 %0, v[244:247], a[4:7], %0" : "+v"(g[1][1][1]) :  : ); SB();
  asm volatile("ds_write_b128 %0, %1 offset:1024" :  : "v"(pw2), "v"(g[0][1][1]) : "memory"); SB();
  asm volatile("v_mfma_f32_16x16x32_bf16 %0, v[244:247], a[8:11], %0" : "+v"(g[1][2][1]) :  : ); SB();
  asm volatile("buffer_load_dwordx4 v[216:219], %0, %1, %2 offen offset:0 sc1" :  : "v"(vB), "s"(rWS), "s"(67584u) : "memory"); SB();
  asm volatile("v_mul_f32 %0, %0, %1" : "+v"(gt[2]) : "v"(a1[1][1][2]) : ); SB();
  asm volatile("v_mfma_f32_16x16x32_bf16 %0, v[244:247], a[12:15], %0" : "+v"(g[1][3][1]) :  : ); SB();
  asm volatile("v_mul_f32 %0, %0, %1" : "+v"(gt[3]) : "v"(a1[1][1][3]) : ); SB();
  asm volatile("s_waitcnt vmcnt(15)" :  :  : "memory"); SB();
  asm volatile("v_mfma_f32_16x16x32_bf16 %0, v[248:251], a[16:19], %0" : "+v"(g[1][0][0]) :  : ); SB();
  asm volatile("ds_write_b128 %0, %1 offset:2048" :  : "v"(pw2), "v"(g[0][2][0]) : "memory"); SB();
  asm volatile("v_mfma_f32_16x16x32_bf16 %0, v[248:251], a[20:23], %0" : "+v"(g[1][1][0]) :  : ); SB();
  asm volatile("buffer_store_dwordx4 %1, %0, %2, %3 offen offset:2048 sc1\n\ts_nop 1" :  : "v"(vH_), "v"(ho[2]), "s"(rWS), "s"(0u) : "memory"); SB();
  asm volatile("v_exp_f32 %0, %1" : "=v"(ge[0]) : "v"(gt[0]) : ); SB();
  asm volatile("v_mfma_f32_16x16x32_bf16 %0, v[248:251], a[24:27], %0" : "+v"(g[1][2][0]) :  : ); SB();
  asm volatile("v_exp_f32 %0, %1" : "=v"(ge[1]) : "v"(gt[1]) : ); SB();
  asm volatile("v_mfma_f32_16x16x32_bf16 %0, v[248:251], a[28:31], %0" : "+v"(g[1][3][0]) :  : ); SB();
  asm volatile("ds_write_b128 %0, %1 offset:3072" :  : "v"(pw2), "v"(g[0][2][1]) : "memory"); SB();
  asm volatile("v_mfma_f32_16x16x32_bf16 %0, v[252:255], a[16:19], %0" : "+v"(g[1][0][1]) :  : ); SB();
  asm volatile("buffer_load_dwordx4 v[220:223], %0, %1, %2 offen offset:1024 sc1" :  : "v"(vB), "s"(rWS), "s"(67584u) : "memory"); SB();
  asm volatile("v_exp_f32 %0, %1" : "=v"(ge[2]) : "v"(gt[2]) : ); SB();
  asm volatile("v_mfma_f32_16x16x32_bf16 %0, v[252:255], a[20:23], %0" : "+v"(g[1][1][1]) :  : ); SB();
  asm volatile("v_xor_b32 %0, 917504, %0" : "+v"(vB) :  : "memory"); SB();
  asm volatile("v_exp_f32 %0, %1" : "=v"(ge[3]) : "v"(gt[3]) : ); SB();
  asm volatile("v_mfma_f32_16x16x32_bf16 %0, v[252:255], a[24:27], %0" : "+v"(g[1][2][1]) :  : ); SB();
  asm volatile("ds_write_b128 %0, %1 offset:4096" :  : "v"(pw2), "v"(g[0][3][0]) : "memory"); SB();
  asm volatile("v_mfma_f32_16x16x32_bf16 %0, v[252:255], a[28:31], %0" : "+v"(g[1][3][1]) :  : ); SB();
  asm volatile("buffer_load_dwordx4 v[224:227], %0, %1, %2 offen offset:0 sc1" :  : "v"(vB), "s"(rWS), "s"(4096u) : "memory"); SB();
  ST(28);
  asm volatile("s_waitcnt vmcnt(16)" :  :  : "memory"); SB();
  asm volatile("v_mfma_f32_16x16x32_bf16 %0, v[160:163], a[32:35], %0" : "+v"(g[1][0][0]) :  : ); SB();
  asm volatile("buffer_load_dwordx4 v[228:231], %0, %1, %2 offen offset:1024 sc1" :  : "v"(vB), "s"(rWS), "s"(4096u) : "memory"); SB();
  asm volatile("v_fma_f32 %0, %0, %1, %1" : "+v"(ge[0]) : "v"(xd[0]) : ); SB();
  asm volatile("v_mfma_f32_16x16x32_bf16 %0, v[160:163], a[36:39], %0" : "+v"(g[1][1][0]) :  : ); SB();
  asm volatile("ds_write_b128 %0, %1 offset:5120" :  : "v"(pw2), "v"(g[0][3][1]) : "memory"); SB();
  asm volatile("v_mfma_f32_16x16x32_bf16 %0, v[160:163], a[40:43], %0" : "+v"(g[1][2][0]) :  : ); SB();
  asm volatile("buffer_load_dwordx4 v[232:235], %0, %1, %2 offen offset:0 sc1" :  : "v"(vB), "s"(rWS), "s"(69632u) : "memory"); SB();
  asm volatile("v_fma_f32 %0, %0, %1, %1" : "+v"(ge[1]) : "v"(xd[0]) : ); SB();
  asm volatile("v_mfma_f32_16x16x32_bf16 %0, v[160:163], a[44:47], %0" : "+v"(g[1][3][0]) :  : ); SB();
  asm volatile("v_fma_f32 %0, %0, %1, %1" : "+v"(ge[2]) : "v"(xd[0]) : ); SB();
  asm volatile("v_mfma_f32_16x16x32_bf16 %0, v[164:167], a[32:35], %0" : "+v"(g[1][0][1]) :  : ); SB();
  asm volatile("s_waitcnt lgkmcnt(0)\n\ts_barrier" :  :  : "memory"); SB();
  asm volatile("ds_read_b128 %0, %1 offset:0" : "=&v"(pr[0]) : "v"(pe[0]) : "memory"); SB();
  asm volatile("v_mfma_f32_16x16x32_bf16 %0, v[164:167], a[36:39], %0" : "+v"(g[1][1][1]) :  : ); SB();
  asm volatile("ds_read_b128 %0, %1 offset:1024" : "=&v"(pr[1]) : "v"(pe[0]) : "memory"); SB();
  asm volatile("v_fma_f32 %0, %0, %1, %1" : "+v"(ge[3]) : "v"(xd[0]) : ); SB();
  asm volatile("v_mfma_f32_16x16x32_bf16 %0, v[164:167], a[40:43], %0" : "+v"(g[1][2][1]) :  : ); SB();
  asm volatile("ds_read_b128 %0, %1 offset:2048" : "=&v"(pr[2]) : "v"(pe[1]) : "memory"); SB();
  asm volatile("v_rcp_f32 %0, %1" : "=v"(gt[0]) : "v"(ge[0]) : ); SB();
  asm volatile("v_mfma_f32_16x16x32_bf16 %0, v[164:167], a[44:47], %0" : "+v"(g[1][3][1]) :  : ); SB();
  asm volatile("ds_read_b128 %0, %1 offset:3072" : "=&v"(pr[3]) : "v"(pe[1]) : "memory"); SB();
  asm volatile("s_waitcnt vmcnt(16)" :  :  : "memory"); SB();
  asm volatile("v_mfma_f32_16x16x32_bf16 %0, v[168:171], a[48:51], %0" : "+v"(g[1][0][0]) :  : ); SB();
  asm volatile("ds_read_b128 %0, %1 offset:4096" : "=&v"(pr[4]) : "v"(pe[2]) : "memory"); SB();
  asm volatile("v_rcp_f32 %0, %1" : "=v"(gt[1]) : "v"(ge[1]) : ); SB();
  asm volatile("v_mfma_f32_16x16x32_bf16 %0, v[168:171], a[52:55], %0" : "+v"(g[1][1][0]) :  : ); SB();
  asm volatile("buffer_load_dwordx4 v[236:239], %0, %1, %2 offen offset:1024 sc1" :  : "v"(vB), "s"(rWS), "s"(69632u) : "memory"); SB();
  asm volatile("v_rcp_f32 %0, %1" : "=v"(gt[2]) : "v"(ge[2]) : ); SB();
  asm volatile("v_mfma_f32_16x16x32_bf16 %0, v[168:171], a[56:59], %0" : "+v"(g[1][2][0]) :  : ); SB();
  asm volatile("ds_read_b128 %0, %1 offset:5120" : "=&v"(pr[5]) : "v"(pe[2]) : "memory"); SB();
  asm volatile("v_rcp_f32 %0, %1" : "=v"(gt[3]) : "v"(ge[3]) : ); SB();
  asm volatile("v_mfma_f32_16x16x32_bf16 %0, v[168:171], a[60:63], %0" : "+v"(g[1][3][0]) :  : ); SB();
  asm volatile("v_xor_b32 %0, 131072, %0" : "+v"(vB) :  : "memory"); SB();
  asm volatile("v_mul_f32 %0, %0, %1" : "+v"(a1[1][1][0]) : "v"(gt[0]) : ); SB();
  asm volatile("v_mfma_f32_16x16x32_bf16 %0, v[172:175], a[48:51], %0" : "+v"(g[1][0][1]) :  : ); SB();
  asm volatile("buffer_load_dwordx4 v[240:243], %0, %1, %2 offen offset:0 sc1" :  : "v"(vB), "s"(rWS), "s"(4096u) : "memory"); SB();
  asm volatile("v_mul_f32 %0, %0, %1" : "+v"(a1[1][1][1]) : "v"(gt[1]) : ); SB();
  asm volatile("v_mfma_f32_16x16x32_bf16 %0, v[172:175], a[52:55], %0" : "+v"(g[1][1][1]) :  : ); SB();
  asm volatile("buffer_load_dwordx4 v[244:247], %0, %1, %2 offen offset:1024 sc1" :  : "v"(vB), "s"(rWS), "s"(4096u) : "memory"); SB();
  asm volatile("v_mul_f32 %0, %0, %1" : "+v"(a1[1][1][2]) : "v"(gt[2]) : ); SB();
  asm volatile("v_mfma_f32_16x16x32_bf16 %0, v[172:175], a[56:59], %0" : "+v"(g[1][2][1]) :  : ); SB();
  asm volatile("v_mul_f32 %0, %0, %1" : "+v"(a1[1][1][3]) : "v"(gt[3]) : ); SB();
  asm volatile("v_mfma_f32_16x16x32_bf16 %0, v[172:175], a[60:63], %0" : "+v"(g[1][3][1]) :  : ); SB();
  asm volatile("v_mul_f32 %0, %1, %1" : "=v"(gt[0]) : "v"(a1[1][2][0]) : ); SB();
  asm volatile("s_waitcnt vmcnt(17)" :  :  : "memory"); SB();
  asm volatile("v_mfma_f32_16x16x32_bf16 %0, v[176:179], a[64:67], %0" : "+v"(g[1][0][0]) :  : ); SB();
  asm volatile("s_waitcnt lgkmcnt(0)" : "+v"(pr[0]), "+v"(pr[1]), "+v"(pr[2]), "+v"(pr[3]), "+v"(pr[4]), "+v"(pr[5]) :  : "memory"); SB();
  asm volatile("buffer_load_dwordx4 v[248:251], %0, %1, %2 offen offset:0 sc1" :  : "v"(vB), "s"(rWS), "s"(69632u) : "memory"); SB();
  asm volatile("v_mfma_f32_16x16x32_bf16 %0, v[176:179], a[68:71], %0" : "+v"(g[1][1][0]) :  : ); SB();
  asm volatile("v_add_f32 %0, %0, %1" : "+v"(pr[0][0]) : "v"(g[0][0][0][0]) : ); SB();
  asm volatile("v_mfma_f32_16x16x32_bf16 %0, v[176:179], a[72:75], %0" : "+v"(g[1][2][0]) :  : ); SB();
  asm volatile("v_add_f32 %0, %0, %1" : "+v"(pr[0][1]) : "v"(g[0][0][0][1]) : ); SB();
  asm volatile("v_mfma_f32_16x16x32_bf16 %0, v[176:179], a[76:79], %0" : "+v"(g[1][3][0]) :  : ); SB();
  asm volatile("buffer_load_dwordx4 v[252:255], %0, %1, %2 offen offset:1024 sc1" :  : "v"(vB), "s"(rWS), "s"(69632u) : "memory"); SB();
  asm volatile("v_add_f32 %0, %0, %1" : "+v"(pr[0][2]) : "v"(g[0][0][0][2]) : ); SB();
  asm volatile("v_mfma_f32_16x16x32_bf16 %0, v[180:183], a[64:67], %0" : "+v"(g[1][0][1]) :  : ); SB();
  asm volatile("v_xor_b32 %0, 393216, %0" : "+v"(vB) :  : "memory"); SB();
  asm volatile("v_add_f32 %0, %0, %1" : "+v"(pr[0][3]) : "v"(g[0][0][0][3]) : ); SB();
  asm volatile("v_mfma_f32_16x16x32_bf16 %0, v[180:183], a[68:71], %0" : "+v"(g[1][1][1]) :  : ); SB();
  asm volatile("v_add_f32 %0, %0, %1" : "+v"(pr[1][0]) : "v"(g[0][0][1][0]) : ); SB();
  asm volatile("v_mfma_f32_16x16x32_bf16 %0, v[180:183], a[72:75], %0" : "+v"(g[1][2][1]) :  : ); SB();
  asm volatile("buffer_load_dwordx4 v[160:163], %0, %1, %2 offen offset:0 sc1" :  : "v"(vB), "s"(rWS), "s"(4096u) : "memory"); SB();
  asm volatile("v_add_f32 %0, %0, %1" : "+v"(pr[1][1]) : "v"(g[0][0][1][1]) : ); SB();
  asm volatile("v_mfma_f32_16x16x32_bf16 %0, v[180:183], a[76:79], %0" : "+v"(g[1][3][1]) :  : ); SB();
  asm volatile("v_add_f32 %0, %0, %1" : "+v"(pr[1][2]) : "v"(g[0][0][1][2]) : ); SB();
  asm volatile("s_waitcnt vmcnt(18)" :  :  : "memory"); SB();
  asm volatile("v_mfma_f32_16x16x32_bf16 %0, v[184:187], a[80:83], %0" : "+v"(g[1][0][0]) :  : ); SB();
  asm volatile("v_add_f32 %0, %0, %1" : "+v"(pr[1][3]) : "v"(g[0][0][1][3]) : ); SB();
  asm volatile("v_mfma_f32_16x16x32_bf16 %0, v[184:187], a[84:87], %0" : "+v"(g[1][1][0]) :  : ); SB();
  asm volatile("v_add_f32 %0, %0, %1" : "+v"(pr[2][0]) : "v"(pr[4][0]) : ); SB();
  asm volatile("v_mfma_f32_16x16x32_bf16 %0, v[184:187], a[88:91], %0" : "+v"(g[1][2][0]) :  : ); SB();
  asm volatile("buffer_load_dwordx4 v[164:167], %0, %1, %2 offen offset:1024 sc1" :  : "v"(vB), "s"(rWS), "s"(4096u) : "memory"); SB();
  asm volatile("v_add_f32 %0, %0, %1" : "+v"(pr[2][1]) : "v"(pr[4][1]) : ); SB();
  asm volatile("v_mfma_f32_16x16x32_bf16 %0, v[184:187], a[92:95], %0" : "+v"(g[1][3][0]) :  : ); SB();
  asm volatile("v_add_f32 %0, %0, %1" : "+v"(pr[2][2]) : "v"(pr[4][2]) : ); SB();
  asm volatile("v_mfma_f32_16x16x32_bf16 %0, v[188:191], a[80:83], %0" : "+v"(g[1][0][1]) :  : ); SB();
  asm volatile("v_add_f32 %0, %0, %1" : "+v"(pr[2][3]) : "v"(pr[4][3]) : ); SB();
  asm volatile("v_mfma_f32_16x16x32_bf16 %0, v[188:191], a[84:87], %0" : "+v"(g[1][1][1]) :  : ); SB();
  asm volatile("buffer_load_dwordx4 v[168:171], %0, %1, %2 offen offset:0 sc1" :  : "v"(vB), "s"(rWS), "s"(69632u) : "memory"); SB();
  asm volatile("v_add_f32 %0, %0, %1" : "+v"(pr[3][0]) : "v"(pr[5][0]) : ); SB();
  asm volatile("v_mfma_f32_16x16x32_bf16 %0, v[188:191], a[88:91], %0" : "+v"(g[1][2][1]) :  : ); SB();
  asm volatile("v_add_f32 %0, %0, %1" : "+v"(pr[3][1]) : "v"(pr[5][1]) : ); SB();
  asm volatile("v_mfma_f32_16x16x32_bf16 %0, v[188:191], a[92:95], %0" : "+v"(g[1][3][1]) :  : ); SB();
  asm volatile("v_add_f32 %0, %0, %1" : "+v"(pr[3][2]) : "v"(pr[5][2]) : ); SB();
  asm volatile("s_waitcnt vmcnt(18)" :  :  : "memory"); SB();
  asm volatile("v_mfma_f32_16x16x32_bf16 %0, v[192:195], a[96:99], %0" : "+v"(g[1][0][0]) :  : ); SB();
  asm volatile("v_add_f32 %0, %0, %1" : "+v"(pr[3][3]) : "v"(pr[5][3]) : ); SB();
  asm volatile("v_mfma_f32_16x16x32_bf16 %0, v[192:195], a[100:103], %0" : "+v"(g[1][1][0]) :  : ); SB();
  asm volatile("buffer_load_dwordx4 v[172:175], %0, %1, %2 offen offset:1024 sc1" :  : "v"(vB), "s"(rWS), "s"(69632u) : "memory"); SB();
  asm volatile("v_add_f32 %0, %0, %1" : "+v"(pr[0][0]) : "v"(pr[2][0]) : ); SB();
  asm volatile("v_mfma_f32_16x16x32_bf16 %0, v[192:195], a[104:107], %0" : "+v"(g[1][2][0]) :  : ); SB();
  asm volatile("v_xor_b32 %0, 131072, %0" : "+v"(vB) :  : "memory"); SB();
  asm volatile("v_add_f32 %0, %0, %1" : "+v"(pr[0][1]) : "v"(pr[2][1]) : ); SB();
  asm volatile("v_mfma_f32_16x16x32_bf16 %0, v[192:195], a[108:111], %0" : "+v"(g[1][3][0]) :  : ); SB();
  asm volatile("v_add_f32 %0, %0, %1" : "+v"(pr[0][2]) : "v"(pr[2][2]) : ); SB();
  asm volatile("v_mfma_f32_16x16x32_bf16 %0, v[196:199], a[96:99], %0" : "+v"(g[1][0][1]) :  : ); SB();
  asm volatile("v_add_f32 %0, %0, %1" : "+v"(pr[0][3]) : "v"(pr[2][3]) : ); SB();
  asm volatile("v_mfma_f32_16x16x32_bf16 %0, v[196:199], a[100:103], %0" : "+v"(g[1][1][1]) :  : ); SB();
  asm volatile("v_add_f32 %0, %0, %1" : "+v"(pr[1][0]) : "v"(pr[3][0]) : ); SB();
  asm volatile("v_mfma_f32_16x16x32_bf16 %0, v[196:199], a[104:107], %0" : "+v"(g[1][2][1]) :  : ); SB();
  asm volatile("v_add_f32 %0, %0, %1" : "+v"(pr[1][1]) : "v"(pr[3][1]) : ); SB();
  asm volatile("v_mfma_f32_16x16x32_bf16 %0, v[196:199], a[108:111], %0" : "+v"(g[1][3][1]) :  : ); SB();
  asm volatile("v_add_f32 %0, %0, %1" : "+v"(pr[1][2]) : "v"(pr[3][2]) : ); SB();
  asm volatile("s_waitcnt vmcnt(17)" :  :  : "memory"); SB();
  asm volatile("v_mfma_f32_16x16x32_bf16 %0, v[200:203], a[112:115], %0" : "+v"(g[1][0][0]) :  : ); SB();
  asm volatile("v_add_f32 %0, %0, %1" : "+v"(pr[1][3]) : "v"(pr[3][3]) : ); SB();
  asm volatile("v_mfma_f32_16x16x32_bf16 %0, v[200:203], a[116:119], %0" : "+v"(g[1][1][0]) :  : ); SB();
  asm volatile("v_mul_f32 %0, %0, %1" : "+v"(pr[0][0]) : "v"(a1[0][0][0]) : ); SB();
  asm volatile("v_mfma_f32_16x16x32_bf16 %0, v[200:203], a[120:123], %0" : "+v"(g[1][2][0]) :  : ); SB();
  asm volatile("v_mul_f32 %0, %0, %1" : "+v"(pr[0][1]) : "v"(a1[0][0][1]) : ); SB();
  asm volatile("v_mfma_f32_16x16x32_bf16 %0, v[200:203], a[124:127], %0" : "+v"(g[1][3][0]) :  : ); SB();
  asm volatile("v_mul_f32 %0, %0, %1" : "+v"(pr[0][2]) : "v"(a1[0][0][2]) : ); SB();
  asm volatile("v_mfma_f32_16x16x32_bf16 %0, v[204:207], a[112:115], %0" : "+v"(g[1][0][1]) :  : ); SB();
  asm volatile("v_mul_f32 %0, %0, %1" : "+v"(pr[0][3]) : "v"(a1[0][0][3]) : ); SB();
  asm volatile("v_mfma_f32_16x16x32_bf16 %0, v[204:207], a[116:119], %0" : "+v"(g[1][1][1]) :  : ); SB();
  asm volatile("v_mul_f32 %0, %0, %1" : "+v"(pr[1][0]) : "v"(a1[0][1][0]) : ); SB();
  asm volatile("v_mfma_f32_16x16x32_bf16 %0, v[204:207], a[120:123], %0" : "+v"(g[1][2][1]) :  : ); SB();
  asm volatile("buffer_load_dwordx4 v[176:179], %0, %1, %2 offen offset:0 sc1" :  : "v"(vB), "s"(rWS), "s"(4096u) : "memory"); SB();
  asm volatile("v_mul_f32 %0, %0, %1" : "+v"(pr[1][1]) : "v"(a1[0][1][1]) : ); SB();
  asm volatile("v_mfma_f32_16x16x32_bf16 %0, v[204:207], a[124:127], %0" : "+v"(g[1][3][1]) :  : ); SB();
  asm volatile("v_mul_f32 %0, %0, %1" : "+v"(pr[1][2]) : "v"(a1[0][1][2]) : ); SB();
  asm volatile("s_waitcnt vmcnt(16)" :  :  : "memory"); SB();
  asm volatile("v_mfma_f32_16x16x32_bf16 %0, v[208:211], a[128:131], %0" : "+v"(g[1][0][0]) :  : ); SB();
  asm volatile("v_mul_f32 %0, %0, %1" : "+v"(pr[1][3]) : "v"(a1[0][1][3]) : ); SB();
  asm volatile("v_mfma_f32_16x16x32_bf16 %0, v[208:211], a[132:135], %0" : "+v"(g[1][1][0]) :  : ); SB();
  asm volatile("buffer_load_dwordx4 v[180:183], %0, %1, %2 offen offset:1024 sc1" :  : "v"(vB), "s"(rWS), "s"(4096u) : "memory"); SB();
  asm volatile("v_cvt_pk_bf16_f32 %0, %1, %2" : "=v"(ho[0][0]) : "v"(pr[0][0]), "v"(pr[0][1]) : ); SB();
  asm volatile("v_mfma_f32_16x16x32_bf16 %0, v[208:211], a[136:139], %0" : "+v"(g[1][2][0]) :  : ); SB();
  asm volatile("v_cvt_pk_bf16_f32 %0, %1, %2" : "=v"(ho[0][1]) : "v"(pr[0][2]), "v"(pr[0][3]) : ); SB();
  asm volatile("v_mfma_f32_16x16x32_bf16 %0, v[208:211], a[140:143], %0" : "+v"(g[1][3][0]) :  : ); SB();
  asm volatile("v_cvt_pk_bf16_f32 %0, %1, %2" : "=v"(ho[0][2]) : "v"(pr[1][0]), "v"(pr[1][1]) : ); SB();
  asm volatile("v_mfma_f32_16x16x32_bf16 %0, v[212:215], a[128:131], %0" : "+v"(g[1][0][1]) :  : ); SB();
  asm volatile("v_cvt_pk_bf16_f32 %0, %1, %2" : "=v"(ho[0][3]) : "v"(pr[1][2]), "v"(pr[1][3]) : ); SB();
  asm volatile("v_mfma_f32_16x16x32_bf16 %0, v[212:215], a[132:135], %0" : "+v"(g[1][1][1]) :  : ); SB();
  asm volatile("buffer_store_dwordx4 %1, %0, %2, %3 offen offset:0 sc1\n\ts_nop 1" :  : "v"(vH_), "v"(ho[0]), "s"(rWS), "s"(0u) : "memory"); SB();
  asm volatile("v_mul_f32 %0, %1, %1" : "=v"(gt[1]) : "v"(a1[1][2][1]) : ); SB();
  asm volatile("v_mfma_f32_16x16x32_bf16 %0, v[212:215], a[136:139], %0" : "+v"(g[1][2][1]) :  : ); SB();
  asm volatile("v_mul_f32 %0, %1, %1" : "=v"(gt[2]) : "v"(a1[1][2][2]) : ); SB();
  asm volatile("v_mfma_f32_16x16x32_bf16 %0, v[212:215], a[140:143], %0" : "+v"(g[1][3][1]) :  : ); SB();
  asm volatile("v_mul_f32 %0, %1, %1" : "=v"(gt[3]) : "v"(a1[1][2][3]) : ); SB();
  asm volatile("s_waitcnt vmcnt(15)" :  :  : "memory"); SB();
  asm volatile("v_mfma_f32_16x16x32_bf16 %0, v[216:219], a[144:147], %0" : "+v"(g[1][0][0]) :  : ); SB();
  asm volatile("buffer_load_dwordx4 v[184:187], %0, %1, %2 offen offset:0 sc1" :  : "v"(vB), "s"(rWS), "s"(69632u) : "memory"); SB();
  asm volatile("v_fma_f32 %0, %0, %1, %2" : "+v"(gt[0]) : "v"(k2[0]), "v"(k1[0]) : ); SB();
  asm volatile("v_mfma_f32_16x16x32_bf16 %0, v[216:219], a[148:151], %0" : "+v"(g[1][1][0]) :  : ); SB();
  asm volatile("v_fma_f32 %0, %0, %1, %2" : "+v"(gt[1]) : "v"(k2[0]), "v"(k1[0]) : ); SB();
  asm volatile("v_mfma_f32_16x16x32_bf16 %0, v[216:219], a[152:155], %0" : "+v"(g[1][2][0]) :  : ); SB();
  asm volatile("v_fma_f32 %0, %0, %1, %2" : "+v"(gt[2]) : "v"(k2[0]), "v"(k1[0]) : ); SB();
  asm volatile("v_mfma_f32_16x16x32_bf16 %0, v[216:219], a[156:159], %0" : "+v"(g[1][3][0]) :  : ); SB();
  asm volatile("v_fma_f32 %0, %0, %1, %2" : "+v"(gt[3]) : "v"(k2[0]), "v"(k1[0]) : ); SB();
  asm volatile("v_mfma_f32_16x16x32_bf16 %0, v[220:223], a[144:147], %0" : "+v"(g[1][0][1]) :  : ); SB();
  asm volatile("buffer_load_dwordx4 v[188:191], %0, %1, %2 offen offset:1024 sc1" :  : "v"(vB), "s"(rWS), "s"(69632u) : "memory"); SB();
  asm volatile("v_mul_f32 %0, %0, %1" : "+v"(gt[0]) : "v"(a1[1][2][0]) : ); SB();
  asm volatile("v_mfma_f32_16x16x32_bf16 %0, v[220:223], a[148:151], %0" : "+v"(g[1][1][1]) :  : ); SB();
  asm volatile("v_xor_b32 %0, 917504, %0" : "+v"(vB) :  : "memory"); SB();
  asm volatile("v_mul_f32 %0, %0, %1" : "+v"(gt[1]) : "v"(a1[1][2][1]) : ); SB();
  asm volatile("v_mfma_f32_16x16x32_bf16 %0, v[220:223], a[152:155], %0" : "+v"(g[1][2][1]) :  : ); SB();
  asm volatile("v_mul_f32 %0, %0, %1" : "+v"(gt[2]) : "v"(a1[1][2][2]) : ); SB();
  asm volatile("v_mfma_f32_16x16x32_bf16 %0, v[220:223], a[156:159], %0" : "+v"(g[1][3][1]) :  : ); SB();
  asm volatile("buffer_load_dwordx4 v[192:195], %0, %1, %2 offen offset:0 sc1" :  : "v"(vB), "s"(rWS), "s"(4096u) : "memory"); SB();
  ST(29);
  asm volatile("s_waitcnt vmcnt(16)" :  :  : "memory"); SB();
  asm volatile("v_mfma_f32_16x16x32_bf16 %0, v[224:227], a[160:163], %1" : "=&v"(g[0][0][0]) : "v"(bs4) : ); SB();
  asm volatile("s_barrier" :  :  : "memory"); SB();
  asm volatile("v_mul_f32 %0, %0, %1" : "+v"(gt[3]) : "v"(a1[1][2][3]) : ); SB();
  asm volatile("v_mfma_f32_16x16x32_bf16 %0, v[224:227], a[164:167], 0" : "=&v"(g[0][1][0]) :  : ); SB();
  asm volatile("v_exp_f32 %0, %1" : "=v"(ge[0]) : "v"(gt[0]) : ); SB();
  asm volatile("v_mfma_f32_16x16x32_bf16 %0, v[224:227], a[168:171], 0" : "=&v"(g[0][2][0]) :  : ); SB();
  asm volatile("ds_write_b128 %0, %1 offset:0" :  : "v"(pw2), "v"(g[1][1][0]) : "memory"); SB();
  asm volatile("v_mfma_f32_16x16x32_bf16 %0, v[224:227], a[172:175], 0" : "=&v"(g[0][3][0]) :  : ); SB();
  asm volatile("buffer_load_dwordx4 v[196:199], %0, %1, %2 offen offset:1024 sc1" :  : "v"(vB), "s"(rWS), "s"(4096u) : "memory"); SB();
  asm volatile("v_exp_f32 %0, %1" : "=v"(ge[1]) : "v"(gt[1]) : ); SB();
  asm volatile("v_mfma_f32_16x16x32_bf16 %0, v[228:231], a[160:163], %1" : "=&v"(g[0][0][1]) : "v"(bs4) : ); SB();
  asm volatile("buffer_load_dwordx4 v[200:203], %0, %1, %2 offen offset:0 sc1" :  : "v"(vB), "s"(rWS), "s"(69632u) : "memory"); SB();
  asm volatile("v_exp_f32 %0, %1" : "=v"(ge[2]) : "v"(gt[2]) : ); SB();
  asm volatile("v_mfma_f32_16x16x32_bf16 %0, v[228:231], a[164:167], 0" : "=&v"(g[0][1][1]) :  : ); SB();
  asm volatile("ds_write_b128 %0, %1 offset:1024" :  : "v"(pw2), "v"(g[1][1][1]) : "memory"); SB();
  asm volatile("v_mfma_f32_16x16x32_bf16 %0, v[228:231], a[168:171], 0" : "=&v"(g[0][2][1]) :  : ); SB();
  asm volatile("buffer_load_dwordx4 v[204:207], %0, %1, %2 offen offset:1024 sc1" :  : "v"(vB), "s"(rWS), "s"(69632u) : "memory"); SB();
  asm volatile("v_exp_f32 %0, %1" : "=v"(ge[3]) : "v"(gt[3]) : ); SB();
  asm volatile("v_mfma_f32_16x16x32_bf16 %0, v[228:231], a[172:175], 0" : "=&v"(g[0][3][1]) :  : ); SB();
  asm volatile("v_xor_b32 %0, 131072, %0" : "+v"(vB) :  : "memory"); SB();
  asm volatile("s_waitcnt vmcnt(17)" :  :  : "memory"); SB();
  asm volatile("v_mfma_f32_16x16x32_bf16 %0, v[232:235], a[176:179], %0" : "+v"(g[0][0][0]) :  : ); SB();
  asm volatile("ds_write_b128 %0, %1 offset:2048" :  : "v"(pw2), "v"(g[1][2][0]) : "memory"); SB();
  asm volatile("v_mfma_f32_16x16x32_bf16 %0, v[232:235], a[180:183], %0" : "+v"(g[0][1][0]) :  : ); SB();
  asm volatile("buffer_load_dwordx4 v[208:211], %0, %1, %2 offen offset:0 sc1" :  : "v"(vB), "s"(rWS), "s"(4096u) : "memory"); SB();
  asm volatile("v_fma_f32 %0, %0, %1, %1" : "+v"(ge[0]) : "v"(xd[0]) : ); SB();
  asm volatile("v_mfma_f32_16x16x32_bf16 %0, v[232:235], a[184:187], %0" : "+v"(g[0][2][0]) :  : ); SB();
  asm volatile("v_fma_f32 %0, %0, %1, %1" : "+v"(ge[1]) : "v"(xd[0]) : ); SB();
  asm volatile("v_mfma_f32_16x16x32_bf16 %0, v[232:235], a[188:191], %0" : "+v"(g[0][3][0]) :  : ); SB();
  asm volatile("ds_write_b128 %0, %1 offset:3072" :  : "v"(pw2), "v"(g[1][2][1]) : "memory"); SB();
  asm volatile("v_mfma_f32_16x16x32_bf16 %0, v[236:239], a[176:179], %0" : "+v"(g[0][0][1]) :  : ); SB();
  asm volatile("buffer_load_dwordx4 v[212:215], %0, %1, %2 offen offset:1024 sc1" :  : "v"(vB), "s"(rWS), "s"(4096u) : "memory"); SB();
  asm volatile("v_fma_f32 %0, %0, %1, %1" : "+v"(ge[2]) : "v"(xd[0]) : ); SB();
  asm volatile("v_mfma_f32_16x16x32_bf16 %0, v[236:239], a[180:183], %0" : "+v"(g[0][1][1]) :  : ); SB();
  asm volatile("v_fma_f32 %0, %0, %1, %1" : "+v"(ge[3]) : "v"(xd[0]) : ); SB();
  asm volatile("v_mfma_f32_16x16x32_bf16 %0, v[236:239], a[184:187], %0" : "+v"(g[0][2][1]) :  : ); SB();
  asm volatile("ds_write_b128 %0, %1 offset:4096" :  : "v"(pw2), "v"(g[1][3][0]) : "memory"); SB();
  asm volatile("v_mfma_f32_16x16x32_bf16 %0, v[236:239], a[188:191], %0" : "+v"(g[0][3][1]) :  : ); SB();
  asm volatile("buffer_load_dwordx4 v[216:219], %0, %1, %2 offen offset:0 sc1" :  : "v"(vB), "s"(rWS), "s"(69632u) : "memory"); SB();
  asm volatile("s_waitcnt vmcnt(18)" :  :  : "memory"); SB();
  asm volatile("v_mfma_f32_16x16x32_bf16 %0, v[240:243], a[192:195], %0" : "+v"(g[0][0][0]) :  : ); SB();
  asm volatile("buffer_load_dwordx4 v[220:223], %0, %1, %2 offen offset:1024 sc1" :  : "v"(vB), "s"(rWS), "s"(69632u) : "memory"); SB();
  asm volatile("v_rcp_f32 %0, %1" : "=v"(gt[0]) : "v"(ge[0]) : ); SB();
  asm volatile("v_mfma_f32_16x16x32_bf16 %0, v[240:243], a[196:199], %0" : "+v"(g[0][1][0]) :  : ); SB();
  asm volatile("ds_write_b128 %0, %1 offset:5120" :  : "v"(pw2), "v"(g[1][3][1]) : "memory"); SB();
  asm volatile("v_mfma_f32_16x16x32_bf16 %0, v[240:243], a[200:203], %0" : "+v"(g[0][2][0]) :  : ); SB();
  asm volatile("v_xor_b32 %0, 393216, %0" : "+v"(vB) :  : "memory"); SB();
  asm volatile("v_rcp_f32 %0, %1" : "=v"(gt[1]) : "v"(ge[1]) : ); SB();
  asm volatile("v_mfma_f32_16x16x32_bf16 %0, v[240:243], a[204:207], %0" : "+v"(g[0][3][0]) :  : ); SB();
  asm volatile("buffer_load_dwordx4 v[224:227], %0, %1, %2 offen offset:0 sc1" :  : "v"(vB), "s"(rWS), "s"(4096u) : "memory"); SB();
  asm volatile("v_rcp_f32 %0, %1" : "=v"(gt[2]) : "v"(ge[2]) : ); SB();
  asm volatile("v_mfma_f32_16x16x32_bf16 %0, v[244:247], a[192:195], %0" : "+v"(g[0][0][1]) :  : ); SB();
  asm volatile("s_waitcnt lgkmcnt(0)\n\ts_barrier" :  :  : "memory"); SB();
  asm volatile("ds_read_b128 %0, %1 offset:0" : "=&v"(pr[0]) : "v"(pe[0]) : "memory"); SB();
  asm volatile("v_mfma_f32_16x16x32_bf16 %0, v[244:247], a[196:199], %0" : "+v"(g[0][1][1]) :  : ); SB();
  asm volatile("ds_read_b128 %0, %1 offset:1024" : "=&v"(pr[1]) : "v"(pe[0]) : "memory"); SB();
  asm volatile("v_rcp_f32 %0, %1" : "=v"(gt[3]) : "v"(ge[3]) : ); SB();
  asm volatile("v_mfma_f32_16x16x32_bf16 %0, v[244:247], a[200:203], %0" : "+v"(g[0][2][1]) :  : ); SB();
  asm volatile("ds_read_b128 %0, %1 offset:2048" : "=&v"(pr[2]) : "v"(pe[1]) : "memory"); SB();
  asm volatile("v_mul_f32 %0, %0, %1" : "+v"(a1[1][2][0]) : "v"(gt[0]) : ); SB();
  asm volatile("v_mfma_f32_16x16x32_bf16 %0, v[244:247], a[204:207], %0" : "+v"(g[0][3][1]) :  : ); SB();
  asm volatile("ds_read_b128 %0, %1 offset:3072" : "=&v"(pr[3]) : "v"(pe[1]) : "memory"); SB();
  asm volatile("s_waitcnt vmcnt(18)" :  :  : "memory"); SB();
  asm volatile("v_mfma_f32_16x16x32_bf16 %0, v[248:251], a[208:211], %0" : "+v"(g[0][0][0]) :  : ); SB();
  asm volatile("ds_read_b128 %0, %1 offset:4096" : "=&v"(pr[4]) : "v"(pe[2]) : "memory"); SB();
  asm volatile("v_mul_f32 %0, %0, %1" : "+v"(a1[1][2][1]) : "v"(gt[1]) : ); SB();
  asm volatile("v_mfma_f32_16x16x32_bf16 %0, v[248:251], a[212:215], %0" : "+v"(g[0][1][0]) :  : ); SB();
  asm volatile("ds_read_b128 %0, %1 offset:5120" : "=&v"(pr[5]) : "v"(pe[2]) : "memory"); SB();
  asm volatile("v_mul_f32 %0, %0, %1" : "+v"(a1[1][2][2]) : "v"(gt[2]) : ); SB();
  asm volatile("v_mfma_f32_16x16x32_bf16 %0, v[248:251], a[216:219], %0" : "+v"(g[0][2][0]) :  : ); SB();
  asm volatile("buffer_load_dwordx4 v[228:231], %0, %1, %2 offen offset:1024 sc1" :  : "v"(vB), "s"(rWS), "s"(4096u) : "memory"); SB();
  asm volatile("v_mul_f32 %0, %0, %1" : "+v"(a1[1][2][3]) : "v"(gt[3]) : ); SB();
  asm volatile("v_mfma_f32_16x16x32_bf16 %0, v[248:251], a[220:223], %0" : "+v"(g[0][3][0]) :  : ); SB();
  asm volatile("buffer_load_dwordx4 v[232:235], %0, %1, %2 offen offset:0 sc1" :  : "v"(vB), "s"(rWS), "s"(69632u) : "memory"); SB();
  asm volatile("v_mfma_f32_16x16x32_bf16 %0, v[252:255], a[208:211], %0" : "+v"(g[0][0][1]) :  : ); SB();
  asm volatile("v_mfma_f32_16x16x32_bf16 %0, v[252:255], a[212:215], %0" : "+v"(g[0][1][1]) :  : ); SB();
  asm volatile("v_mfma_f32_16x16x32_bf16 %0, v[252:255], a[216:219], %0" : "+v"(g[0][2][1]) :  : ); SB();
  asm volatile("v_mfma_f32_16x16x32_bf16 %0, v[252:255], a[220:223], %0" : "+v"(g[0][3][1]) :  : ); SB();
  asm volatile("s_waitcnt lgkmcnt(0)" : "+v"(pr[0]), "+v"(pr[1]), "+v"(pr[2]), "+v"(pr[3]), "+v"(pr[4]), "+v"(pr[5]) :  : "memory"); SB();
  asm volatile("v_add_f32 %0, %0, %1" : "+v"(pr[0][0]) : "v"(g[1][0][0][0]) : ); SB();
  asm volatile("s_waitcnt vmcnt(18)" :  :  : "memory"); SB();
  asm volatile("v_mfma_f32_16x16x32_bf16 %0, v[160:163], a[224:227], %0" : "+v"(g[0][0][0]) :  : ); SB();
  asm volatile("v_add_f32 %0, %0, %1" : "+v"(pr[0][1]) : "v"(g[1][0][0][1]) : ); SB();
  asm volatile("v_mfma_f32_16x16x32_bf16 %0, v[160:163], a[228:231], %0" : "+v"(g[0][1][0]) :  : ); SB();
  asm volatile("v_add_f32 %0, %0, %1" : "+v"(pr[0][2]) : "v"(g[1][0][0][2]) : ); SB();
  asm volatile("v_mfma_f32_16x16x32_bf16 %0, v[160:163], a[232:235], %0" : "+v"(g[0][2][0]) :  : ); SB();
  asm volatile("v_add_f32 %0, %0, %1" : "+v"(pr[0][3]) : "v"(g[1][0][0][3]) : ); SB();
  asm volatile("v_mfma_f32_16x16x32_bf16 %0, v[160:163], a[236:239], %0" : "+v"(g[0][3][0]) :  : ); SB();
  asm volatile("v_add_f32 %0, %0, %1" : "+v"(pr[1][0]) : "v"(g[1][0][1][0]) : ); SB();
  asm volatile("v_mfma_f32_16x16x32_bf16 %0, v[164:167], a[224:227], %0" : "+v"(g[0][0][1]) :  : ); SB();
  asm volatile("v_add_f32 %0, %0, %1" : "+v"(pr[1][1]) : "v"(g[1][0][1][1]) : ); SB();
  asm volatile("v_mfma_f32_16x16x32_bf16 %0, v[164:167], a[228:231], %0" : "+v"(g[0][1][1]) :  : ); SB();
  asm volatile("buffer_load_dwordx4 v[236:239], %0, %1, %2 offen offset:1024 sc1" :  : "v"(vB), "s"(rWS), "s"(69632u) : "memory"); SB();
  asm volatile("v_add_f32 %0, %0, %1" : "+v"(pr[1][2]) : "v"(g[1][0][1][2]) : ); SB();
  asm volatile("v_mfma_f32_16x16x32_bf16 %0, v[164:167], a[232:235], %0" : "+v"(g[0][2][1]) :  : ); SB();
  asm volatile("v_xor_b32 %0, 131072, %0" : "+v"(vB) :  : "memory"); SB();
  asm volatile("v_add_f32 %0, %0, %1" : "+v"(pr[1][3]) : "v"(g[1][0][1][3]) : ); SB();
  asm volatile("v_mfma_f32_16x16x32_bf16 %0, v[164:167], a[236:239], %0" : "+v"(g[0][3][1]) :  : ); SB();
  asm volatile("v_add_f32 %0, %0, %1" : "+v"(pr[2][0]) : "v"(pr[4][0]) : ); SB();
  asm volatile("s_waitcnt vmcnt(17)" :  :  : "memory"); SB();
  asm volatile("v_mfma_f32_16x16x32_bf16 %0, v[168:171], a[240:243], %0" : "+v"(g[0][0][0]) :  : ); SB();
  asm volatile("buffer_load_dwordx4 v[240:243], %0, %1, %2 offen offset:0 sc1" :  : "v"(vB), "s"(rWS), "s"(4096u) : "memory"); SB();
  asm volatile("v_add_f32 %0, %0, %1" : "+v"(pr[2][1]) : "v"(pr[4][1]) : ); SB();
  asm volatile("v_mfma_f32_16x16x32_bf16 %0, v[168:171], a[244:247], %0" : "+v"(g[0][1][0]) :  : ); SB();
  asm volatile("v_add_f32 %0, %0, %1" : "+v"(pr[2][2]) : "v"(pr[4][2]) : ); SB();
  asm volatile("v_mfma_f32_16x16x32_bf16 %0, v[168:171], a[248:251], %0" : "+v"(g[0][2][0]) :  : ); SB();
  asm volatile("v_add_f32 %0, %0, %1" : "+v"(pr[2][3]) : "v"(pr[4][3]) : ); SB();
  asm volatile("v_mfma_f32_16x16x32_bf16 %0, v[168:171], a[252:255], %0" : "+v"(g[0][3][0]) :  : ); SB();
  asm volatile("v_add_f32 %0, %0, %1" : "+v"(pr[3][0]) : "v"(pr[5][0]) : ); SB();
  asm volatile("v_mfma_f32_16x16x32_bf16 %0, v[172:175], a[240:243], %0" : "+v"(g[0][0][1]) :  : ); SB();
  asm volatile("buffer_load_dwordx4 v[244:247], %0, %1, %2 offen offset:1024 sc1" :  : "v"(vB), "s"(rWS), "s"(4096u) : "memory"); SB();
  asm volatile("v_add_f32 %0, %0, %1" : "+v"(pr[3][1]) : "v"(pr[5][1]) : ); SB();
  asm volatile("v_mfma_f32_16x16x32_bf16 %0, v[172:175], a[244:247], %0" : "+v"(g[0][1][1]) :  : ); SB();
  asm volatile("v_add_f32 %0, %0, %1" : "+v"(pr[3][2]) : "v"(pr[5][2]) : ); SB();
  asm volatile("v_mfma_f32_16x16x32_bf16 %0, v[172:175], a[248:251], %0" : "+v"(g[0][2][1]) :  : ); SB();
  asm volatile("v_add_f32 %0, %0, %1" : "+v"(pr[3][3]) : "v"(pr[5][3]) : ); SB();
  asm volatile("v_mfma_f32_16x16x32_bf16 %0, v[172:175], a[252:255], %0" : "+v"(g[0][3][1]) :  : ); SB();
  asm volatile("s_add_u32 m0, %0, 60032" :  : "s"(mU) : "memory", "scc"); SB();
  asm volatile("v_add_f32 %0, %0, %1" : "+v"(pr[0][0]) : "v"(pr[2][0]) : ); SB();
  asm volatile("s_waitcnt vmcnt(17)" :  :  : "memory"); SB();
  asm volatile("v_mfma_f32_16x16x32_bf16 %0, v[176:179], a[0:3], %0" : "+v"(g[0][0][0]) :  : ); SB();
  asm volatile("buffer_load_dwordx4 %0, %1, %2 offen sc1 lds" :  : "v"(vF), "s"(rWS), "s"(W_KV) : "memory"); SB();
  asm volatile("v_add_f32 %0, %0, %1" : "+v"(pr[0][1]) : "v"(pr[2][1]) : ); SB();
  asm volatile("v_mfma_f32_16x16x32_bf16 %0, v[176:179], a[4:7], %0" : "+v"(g[0][1][0]) :  : ); SB();
  asm volatile("v_add_f32 %0, %0, %1" : "+v"(pr[0][2]) : "v"(pr[2][2]) : ); SB();
  asm volatile("v_mfma_f32_16x16x32_bf16 %0, v[176:179], a[8:11], %0" : "+v"(g[0][2][0]) :  : ); SB();
  asm volatile("v_add_f32 %0, %0, %1" : "+v"(pr[0][3]) : "v"(pr[2][3]) : ); SB();
  asm volatile("v_mfma_f32_16x16x32_bf16 %0, v[176:179], a[12:15], %0" : "+v"(g[0][3][0]) :  : ); SB();
  asm volatile("s_add_u32 m0, %0, 72960" :  : "s"(mU) : "memory", "scc"); SB();
  asm volatile("v_add_f32 %0, %0, %1" : "+v"(pr[1][0]) : "v"(pr[3][0]) : ); SB();
  asm volatile("v_mfma_f32_16x16x32_bf16 %0, v[180:183], a[0:3], %0" : "+v"(g[0][0][1]) :  : ); SB();
  asm volatile("buffer_load_dwordx4 %0, %1, %2 offen sc1 lds" :  : "v"(vF), "s"(rWS), "s"(W_KV + 4096) : "memory"); SB();
  asm volatile("v_add_f32 %0, %0, %1" : "+v"(pr[1][1]) : "v"(pr[3][1]) : ); SB();
  asm volatile("v_mfma_f32_16x16x32_bf16 %0, v[180:183], a[4:7], %0" : "+v"(g[0][1][1]) :  : ); SB();
  asm volatile("v_add_f32 %0, %0, %1" : "+v"(pr[1][2]) : "v"(pr[3][2]) : ); SB();
  asm volatile("v_mfma_f32_16x16x32_bf16 %0, v[180:183], a[8:11], %0" : "+v"(g[0][2][1]) :  : ); SB();
  asm volatile("buffer_load_dwordx4 v[248:251], %0, %1, %2 offen offset:0 sc1" :  : "v"(vB), "s"(rWS), "s"(69632u) : "memory"); SB();
  asm volatile("v_add_f32 %0, %0, %1" : "+v"(pr[1][3]) : "v"(pr[3][3]) : ); SB();
  asm volatile("v_mfma_f32_16x16x32_bf16 %0, v[180:183], a[12:15], %0" : "+v"(g[0][3][1]) :  : ); SB();
  asm volatile("v_mul_f32 %0, %0, %1" : "+v"(pr[0][0]) : "v"(a1[0][2][0]) : ); SB();
  asm volatile("s_waitcnt vmcnt(17)" :  :  : "memory"); SB();
  asm volatile("v_mfma_f32_16x16x32_bf16 %0, v[184:187], a[16:19], %0" : "+v"(g[0][0][0]) :  : ); SB();
  asm volatile("v_mul_f32 %0, %0, %1" : "+v"(pr[0][1]) : "v"(a1[0][2][1]) : ); SB();
  asm volatile("v_mfma_f32_16x16x32_bf16 %0, v[184:187], a[20:23], %0" : "+v"(g[0][1][0]) :  : ); SB();
  asm volatile("v_mul_f32 %0, %0, %1" : "+v"(pr[0][2]) : "v"(a1[0][2][2]) : ); SB();
  asm volatile("v_mfma_f32_16x16x32_bf16 %0, v[184:187], a[24:27], %0" : "+v"(g[0][2][0]) :  : ); SB();
  asm volatile("buffer_load_dwordx4 v[252:255], %0, %1, %2 offen offset:1024 sc1" :  : "v"(vB), "s"(rWS), "s"(69632u) : "memory"); SB();
  asm volatile("v_mul_f32 %0, %0, %1" : "+v"(pr[0][3]) : "v"(a1[0][2][3]) : ); SB();
  asm volatile("v_mfma_f32_16x16x32_bf16 %0, v[184:187], a[28:31], %0" : "+v"(g[0][3][0]) :  : ); SB();
  asm volatile("v_mul_f32 %0, %0, %1" : "+v"(pr[1][0]) : "v"(a1[1][0][0]) : ); SB();
  asm volatile("v_mfma_f32_16x16x32_bf16 %0, v[188:191], a[16:19], %0" : "+v"(g[0][0][1]) :  : ); SB();
  asm volatile("v_mul_f32 %0, %0, %1" : "+v"(pr[1][1]) : "v"(a1[1][0][1]) : ); SB();
  asm volatile("v_mfma_f32_16x16x32_bf16 %0, v[188:191], a[20:23], %0" : "+v"(g[0][1][1]) :  : ); SB();
  asm volatile("v_mul_f32 %0, %0, %1" : "+v"(pr[1][2]) : "v"(a1[1][0][2]) : ); SB();
  asm volatile("v_mfma_f32_16x16x32_bf16 %0, v[188:191], a[24:27], %0" : "+v"(g[0][2][1]) :  : ); SB();
  asm volatile("buffer_load_dwordx4 a[160:163], %0, %1, %2 offen offset:0 sc1" :  : "v"(vO), "s"(rWS), "s"(0u) : "memory"); SB();
  asm volatile("v_mul_f32 %0, %0, %1" : "+v"(pr[1][3]) : "v"(a1[1][0][3]) : ); SB();
  asm volatile("v_mfma_f32_16x16x32_bf16 %0, v[188:191], a[28:31], %0" : "+v"(g[0][3][1]) :  : ); SB();
  asm volatile("v_cvt_pk_bf16_f32 %0, %1, %2" : "=v"(ho[1][0]) : "v"(pr[0][0]), "v"(pr[0][1]) : ); SB();
  asm volatile("s_waitcnt vmcnt(17)" :  :  : "memory"); SB();
  asm volatile("v_mfma_f32_16x16x32_bf16 %0, v[192:195], a[32:35], %0" : "+v"(g[0][0][0]) :  : ); SB();
  asm volatile("v_cvt_pk_bf16_f32 %0, %1, %2" : "=v"(ho[1][1]) : "v"(pr[0][2]), "v"(pr[0][3]) : ); SB();
  asm volatile("v_mfma_f32_16x16x32_bf16 %0, v[192:195], a[36:39], %0" : "+v"(g[0][1][0]) :  : ); SB();
  asm volatile("buffer_load_dwordx4 a[164:167], %0, %1, %2 offen offset:1024 sc1" :  : "v"(vO), "s"(rWS), "s"(0u) : "memory"); SB();
  asm volatile("v_cvt_pk_bf16_f32 %0, %1, %2" : "=v"(ho[1][2]) : "v"(pr[1][0]), "v"(pr[1][1]) : ); SB();
  asm volatile("v_mfma_f32_16x16x32_bf16 %0, v[192:195], a[40:43], %0" : "+v"(g[0][2][0]) :  : ); SB();
  asm volatile("v_cvt_pk_bf16_f32 %0, %1, %2" : "=v"(ho[1][3]) : "v"(pr[1][2]), "v"(pr[1][3]) : ); SB();
  asm volatile("v_mfma_f32_16x16x32_bf16 %0, v[192:195], a[44:47], %0" : "+v"(g[0][3][0]) :  : ); SB();
  asm volatile("v_mfma_f32_16x16x32_bf16 %0, v[196:199], a[32:35], %0" : "+v"(g[0][0][1]) :  : ); SB();
  asm volatile("v_mfma_f32_16x16x32_bf16 %0, v[196:199], a[36:39], %0" : "+v"(g[0][1][1]) :  : ); SB();
  asm volatile("buffer_store_dwordx4 %1, %0, %2, %3 offen offset:1024 sc1\n\ts_nop 1" :  : "v"(vH_), "v"(ho[1]), "s"(rWS), "s"(0u) : "memory"); SB();
  asm volatile("v_mfma_f32_16x16x32_bf16 %0, v[196:199], a[40:43], %0" : "+v"(g[0][2][1]) :  : ); SB();
  asm volatile("v_mfma_f32_16x16x32_bf16 %0, v[196:199], a[44:47], %0" : "+v"(g[0][3][1]) :  : ); SB();
  asm volatile("s_waitcnt vmcnt(17)" :  :  : "memory"); SB();
  asm volatile("v_mfma_f32_16x16x32_bf16 %0, v[200:203], a[48:51], %0" : "+v"(g[0][0][0]) :  : ); SB();
  asm volatile("buffer_load_dwordx4 a[168:171], %0, %1, %2 offen offset:2048 sc1" :  : "v"(vO), "s"(rWS), "s"(0u) : "memory"); SB();
  asm volatile("v_mfma_f32_16x16x32_bf16 %0, v[200:203], a[52:55], %0" : "+v"(g[0][1][0]) :  : ); SB();
  asm volatile("v_mfma_f32_16x16x32_bf16 %0, v[200:203], a[56:59], %0" : "+v"(g[0][2][0]) :  : ); SB();
  asm volatile("v_mfma_f32_16x16x32_bf16 %0, v[200:203], a[60:63], %0" : "+v"(g[0][3][0]) :  : ); SB();
  asm volatile("v_mfma_f32_16x16x32_bf16 %0, v[204:207], a[48:51], %0" : "+v"(g[0][0][1]) :  : ); SB();
  asm volatile("buffer_load_dwordx4 a[172:175], %0, %1, %2 offen offset:3072 sc1" :  : "v"(vO), "s"(rWS), "s"(0u) : "memory"); SB();
  asm volatile("v_mfma_f32_16x16x32_bf16 %0, v[204:207], a[52:55], %0" : "+v"(g[0][1][1]) :  : ); SB();
  asm volatile("v_mfma_f32_16x16x32_bf16 %0, v[204:207], a[56:59], %0" : "+v"(g[0][2][1]) :  : ); SB();
  asm volatile("v_mfma_f32_16x16x32_bf16 %0, v[204:207], a[60:63], %0" : "+v"(g[0][3][1]) :  : ); SB();
  asm volatile("s_waitcnt vmcnt(17)" :  :  : "memory"); SB();
  asm volatile("v_mfma_f32_16x16x32_bf16 %0, v[208:211], a[64:67], %0" : "+v"(g[0][0][0]) :  : ); SB();
  asm volatile("ds_read_b128 %0, %1 offset:0" : "=&v"(yq[0]) : "v"(ydr) : "memory"); SB();
  asm volatile("v_mfma_f32_16x16x32_bf16 %0, v[208:211], a[68:71], %0" : "+v"(g[0][1][0]) :  : ); SB();
  asm volatile("ds_read_b128 %0, %1 offset:256" : "=&v"(yq[1]) : "v"(ydr) : "memory"); SB();
  asm volatile("v_mfma_f32_16x16x32_bf16 %0, v[208:211], a[72:75], %0" : "+v"(g[0][2][0]) :  : ); SB();
  asm volatile("ds_read_b128 %0, %1 offset:8192" : "=&v"(yq[2]) : "v"(ydr) : "memory"); SB();
  asm volatile("v_mfma_f32_16x16x32_bf16 %0, v[208:211], a[76:79], %0" : "+v"(g[0][3][0]) :  : ); SB();
  asm volatile("ds_read_b128 %0, %1 offset:8448" : "=&v"(yq[3]) : "v"(ydr) : "memory"); SB();
  asm volatile("v_mfma_f32_16x16x32_bf16 %0, v[212:215], a[64:67], %0" : "+v"(g[0][0][1]) :  : ); SB();
  asm volatile("ds_read_b128 %0, %1 offset:16384" : "=&v"(yq[4]) : "v"(ydr) : "memory"); SB();
  asm volatile("v_mfma_f32_16x16x32_bf16 %0, v[212:215], a[68:71], %0" : "+v"(g[0][1][1]) :  : ); SB();
  asm volatile("ds_read_b128 %0, %1 offset:16640" : "=&v"(yq[5]) : "v"(ydr) : "memory"); SB();
  asm volatile("v_mfma_f32_16x16x32_bf16 %0, v[212:215], a[72:75], %0" : "+v"(g[0][2][1]) :  : ); SB();
  asm volatile("ds_read_b128 %0, %1 offset:24576" : "=&v"(yq[6]) : "v"(ydr) : "memory"); SB();
  asm volatile("v_mfma_f32_16x16x32_bf16 %0, v[212:215], a[76:79], %0" : "+v"(g[0][3][1]) :  : ); SB();
  asm volatile("ds_read_b128 %0, %1 offset:24832" : "=&v"(yq[7]) : "v"(ydr) : "memory"); SB();
  asm volatile("s_waitcnt vmcnt(15)" :  :  : "memory"); SB();
  asm volatile("v_mfma_f32_16x16x32_bf16 %0, v[216:219], a[80:83], %0" : "+v"(g[0][0][0]) :  : ); SB();
  asm volatile("ds_read_b128 %0, %1 offset:25856" : "=&v"(ykv) : "v"(ykr) : "memory"); SB();
  asm volatile("v_mfma_f32_16x16x32_bf16 %0, v[216:219], a[84:87], %0" : "+v"(g[0][1][0]) :  : ); SB();
  asm volatile("buffer_load_dwordx4 a[176:179], %0, %1, %2 offen offset:0 sc1" :  : "v"(vO), "s"(rWS), "s"(4096u) : "memory"); SB();
  asm volatile("v_mfma_f32_16x16x32_bf16 %0, v[216:219], a[88:91], %0" : "+v"(g[0][2][0]) :  : ); SB();
  asm volatile("buffer_load_dwordx4 a[180:183], %0, %1, %2 offen offset:1024 sc1" :  : "v"(vO), "s"(rWS), "s"(4096u) : "memory"); SB();
  asm volatile("v_mfma_f32_16x16x32_bf16 %0, v[216:219], a[92:95], %0" : "+v"(g[0][3][0]) :  : ); SB();
  asm volatile("v_mfma_f32_16x16x32_bf16 %0, v[220:223], a[80:83], %0" : "+v"(g[0][0][1]) :  : ); SB();
  asm volatile("v_mfma_f32_16x16x32_bf16 %0, v[220:223], a[84:87], %0" : "+v"(g[0][1][1]) :  : ); SB();
  asm volatile("buffer_load_dwordx4 a[184:187], %0, %1, %2 offen offset:2048 sc1" :  : "v"(vO), "s"(rWS), "s"(4096u) : "memory"); SB();
  asm volatile("v_mfma_f32_16x16x32_bf16 %0, v[220:223], a[88:91], %0" : "+v"(g[0][2][1]) :  : ); SB();
  asm volatile("v_mfma_f32_16x16x32_bf16 %0, v[220:223], a[92:95], %0" : "+v"(g[0][3][1]) :  : ); SB();
  asm volatile("buffer_load_dwordx4 a[188:191], %0, %1, %2 offen offset:3072 sc1" :  : "v"(vO), "s"(rWS), "s"(4096u) : "memory"); SB();
  asm volatile("buffer_load_dwordx4 a[192:195], %0, %1, %2 offen offset:0 sc1" :  : "v"(vO), "s"(rWS), "s"(8192u) : "memory"); SB();
  asm volatile("s_waitcnt vmcnt(12)\n\ts_barrier" :  :  : "memory"); SB();
  asm volatile("s_waitcnt vmcnt(10)" :  :  : "memory"); SB();
  asm volatile("v_mfma_f32_16x16x32_bf16 %0, v[224:227], a[100:103], %0" : "+v"(g[0][1][0]) :  : ); SB();
  asm volatile("buffer_load_dwordx4 a[196:199], %0, %1, %2 offen offset:1024 sc1" :  : "v"(vO), "s"(rWS), "s"(8192u) : "memory"); SB();
  asm volatile("v_mfma_f32_16x16x32_bf16 %0, v[228:231], a[100:103], %0" : "+v"(g[0][1][1]) :  : ); SB();
  asm volatile("ds_read2_b32 a[64:65], %0 offset0:176 offset1:177" :  : "v"(aa[0]) : "memory"); SB();
  asm volatile("v_mfma_f32_16x16x32_bf16 %0, v[232:235], a[116:119], %0" : "+v"(g[0][1][0]) :  : ); SB();
  asm volatile("ds_read2_b32 a[66:67], %0 offset0:178 offset1:179" :  : "v"(aa[0]) : "memory"); SB();
  asm volatile("v_mfma_f32_16x16x32_bf16 %0, v[236:239], a[116:119], %0" : "+v"(g[0][1][1]) :  : ); SB();
  asm volatile("ds_read_b128 a[60:63], %0 offset:3776" :  : "v"(bb) : "memory"); SB();
  asm volatile("v_mfma_f32_16x16x32_bf16 %0, v[240:243], a[132:135], %0" : "+v"(g[0][1][0]) :  : ); SB();
  asm volatile("buffer_load_dwordx4 a[200:203], %0, %1, %2 offen offset:0 sc1" :  : "v"(vO2), "s"(rWS), "s"(0u) : "memory"); SB();
  asm volatile("v_mfma_f32_16x16x32_bf16 %0, v[244:247], a[132:135], %0" : "+v"(g[0][1][1]) :  : ); SB();
  asm volatile("ds_read_b128 a[56:59], %0 offset:3264" :  : "v"(bb) : "memory"); SB();
  asm volatile("v_mfma_f32_16x16x32_bf16 %0, v[248:251], a[148:151], %0" : "+v"(g[0][1][0]) :  : ); SB();
  asm volatile("ds_read_b128 a[52:55], %0 offset:2752" :  : "v"(bb) : "memory"); SB();
  asm volatile("v_mfma_f32_16x16x32_bf16 %0, v[252:255], a[148:151], %0" : "+v"(g[0][1][1]) :  : ); SB();
  asm volatile("buffer_load_dwordx4 a[204:207], %0, %1, %2 offen offset:1024 sc1" :  : "v"(vO2), "s"(rWS), "s"(0u) : "memory"); SB();
  asm volatile("v_mfma_f32_16x16x32_bf16 %0, v[224:227], a[104:107], %0" : "+v"(g[0][2][0]) :  : ); SB();
  asm volatile("ds_read_b128 a[48:51], %0 offset:2240" :  : "v"(bb) : "memory"); SB();
  asm volatile("v_mfma_f32_16x16x32_bf16 %0, v[228:231], a[104:107], %0" : "+v"(g[0][2][1]) :  : ); SB();
  asm volatile("ds_read2_b32 a[68:69], %0 offset0:48 offset1:49" :  : "v"(aa[0]) : "memory"); SB();
  asm volatile("v_mfma_f32_16x16x32_bf16 %0, v[232:235], a[120:123], %0" : "+v"(g[0][2][0]) :  : ); SB();
  asm volatile("ds_write_b128 %0, %1 offset:0" :  : "v"(pw2), "v"(g[0][1][0]) : "memory"); SB();
  asm volatile("v_mfma_f32_16x16x32_bf16 %0, v[236:239], a[120:123], %0" : "+v"(g[0][2][1]) :  : ); SB();
  asm volatile("ds_read2_b32 a[70:71], %0 offset0:50 offset1:51" :  : "v"(aa[0]) : "memory"); SB();
  asm volatile("v_mfma_f32_16x16x32_bf16 %0, v[240:243], a[136:139], %0" : "+v"(g[0][2][0]) :  : ); SB();
  asm volatile("ds_read_b128 a[44:47], %0 offset:1728" :  : "v"(bb) : "memory"); SB();
  asm volatile("v_mfma_f32_16x16x32_bf16 %0, v[244:247], a[136:139], %0" : "+v"(g[0][2][1]) :  : ); SB();
  asm volatile("ds_write_b128 %0, %1 offset:1024" :  : "v"(pw2), "v"(g[0][1][1]) : "memory"); SB();
  asm volatile("v_mfma_f32_16x16x32_bf16 %0, v[248:251], a[152:155], %0" : "+v"(g[0][2][0]) :  : ); SB();
  asm volatile("ds_read2_b32 a[72:73], %0 offset0:176 offset1:177" :  : "v"(aa[1]) : "memory"); SB();
  asm volatile("v_mfma_f32_16x16x32_bf16 %0, v[252:255], a[152:155], %0" : "+v"(g[0][2][1]) :  : ); SB();
  asm volatile("ds_read2_b32 a[74:75], %0 offset0:178 offset1:179" :  : "v"(aa[1]) : "memory"); SB();
  asm volatile("v_mfma_f32_16x16x32_bf16 %0, v[224:227], a[108:111], %0" : "+v"(g[0][3][0]) :  : ); SB();
  asm volatile("ds_read2_b32 a[76:77], %0 offset0:48 offset1:49" :  : "v"(aa[1]) : "memory"); SB();
  asm volatile("v_mfma_f32_16x16x32_bf16 %0, v[228:231], a[108:111], %0" : "+v"(g[0][3][1]) :  : ); SB();
  asm volatile("ds_read2_b32 a[78:79], %0 offset0:50 offset1:51" :  : "v"(aa[1]) : "memory"); SB();
  asm volatile("v_mfma_f32_16x16x32_bf16 %0, v[232:235], a[124:127], %0" : "+v"(g[0][3][0]) :  : ); SB();
  asm volatile("ds_write_b128 %0, %1 offset:2048" :  : "v"(pw2), "v"(g[0][2][0]) : "memory"); SB();
  asm volatile("v_mfma_f32_16x16x32_bf16 %0, v[236:239], a[124:127], %0" : "+v"(g[0][3][1]) :  : ); SB();
  asm volatile("ds_read_b128 a[40:43], %0 offset:1216" :  : "v"(bb) : "memory"); SB();
  asm volatile("v_mfma_f32_16x16x32_bf16 %0, v[240:243], a[140:143], %0" : "+v"(g[0][3][0]) :  : ); SB();
  asm volatile("ds_read2_b32 a[80:81], %0 offset0:176 offset1:177" :  : "v"(aa[2]) : "memory"); SB();
  asm volatile("v_mfma_f32_16x16x32_bf16 %0, v[244:247], a[140:143], %0" : "+v"(g[0][3][1]) :  : ); SB();
  asm volatile("ds_write_b128 %0, %1 offset:3072" :  : "v"(pw2), "v"(g[0][2][1]) : "memory"); SB();
  asm volatile("v_mfma_f32_16x16x32_bf16 %0, v[248:251], a[156:159], %0" : "+v"(g[0][3][0]) :  : ); SB();
  asm volatile("s_waitcnt vmcnt(10)\n\ts_mov_b64 exec, 1\n\tbuffer_store_dwordx2 %0, off, %1, %2 sc1\n\ts_mov_b64 exec, -1" :  : "v"(((u32x2){tg0, tg1})), "s"(rWS), "s"(sFH) : "memory"); SB();
  asm volatile("buffer_load_dwordx4 a[208:211], %0, %1, %2 offen offset:2048 sc1" :  : "v"(vO2), "s"(rWS), "s"(0u) : "memory"); SB();
  asm volatile("v_mfma_f32_16x16x32_bf16 %0, v[252:255], a[156:159], %0" : "+v"(g[0][3][1]) :  : ); SB();
  asm volatile("ds_read2_b32 a[82:83], %0 offset0:178 offset1:179" :  : "v"(aa[2]) : "memory"); SB();
  asm volatile("v_mfma_f32_16x16x32_bf16 %0, v[224:227], a[96:99], %0" : "+v"(g[0][0][0]) :  : ); SB();
  asm volatile("ds_read2_b32 a[84:85], %0 offset0:48 offset1:49" :  : "v"(aa[2]) : "memory"); SB();
  asm volatile("v_mfma_f32_16x16x32_bf16 %0, v[228:231], a[96:99], %0" : "+v"(g[0][0][1]) :  : ); SB();
  asm volatile("buffer_load_dwordx4 a[212:215], %0, %1, %2 offen offset:3072 sc1" :  : "v"(vO2), "s"(rWS), "s"(0u) : "memory"); SB();
  asm volatile("v_mfma_f32_16x16x32_bf16 %0, v[232:235], a[112:115], %0" : "+v"(g[0][0][0]) :  : ); SB();
  asm volatile("ds_write_b128 %0, %1 offset:4096" :  : "v"(pw2), "v"(g[0][3][0]) : "memory"); SB();
  asm volatile("v_mfma_f32_16x16x32_bf16 %0, v[236:239], a[112:115], %0" : "+v"(g[0][0][1]) :  : ); SB();
  asm volatile("ds_read2_b32 a[86:87], %0 offset0:50 offset1:51" :  : "v"(aa[2]) : "memory"); SB();
  asm volatile("v_mfma_f32_16x16x32_bf16 %0, v[240:243], a[128:131], %0" : "+v"(g[0][0][0]) :  : ); SB();
  asm volatile("buffer_load_dwordx4 a[216:219], %0, %1, %2 offen offset:0 sc1" :  : "v"(vO2), "s"(rWS), "s"(4096u) : "memory"); SB();
  asm volatile("v_mfma_f32_16x16x32_bf16 %0, v[244:247], a[128:131], %0" : "+v"(g[0][0][1]) :  : ); SB();
  asm volatile("ds_write_b128 %0, %1 offset:5120" :  : "v"(pw2), "v"(g[0][3][1]) : "memory"); SB();
  asm volatile("v_mfma_f32_16x16x32_bf16 %0, v[248:251], a[144:147], %0" : "+v"(g[0][0][0]) :  : ); SB();
  asm volatile("buffer_load_dwordx4 a[220:223], %0, %1, %2 offen offset:1024 sc1" :  : "v"(vO2), "s"(rWS), "s"(4096u) : "memory"); SB();
  asm volatile("v_mfma_f32_16x16x32_bf16 %0, v[252:255], a[144:147], %0" : "+v"(g[0][0][1]) :  : ); SB();
  asm volatile("ds_read_b128 a[36:39], %0 offset:704" :  : "v"(bb) : "memory"); SB();
  if ((rb | cb | w) == 0) asm volatile("buffer_store_dwordx2 %0, off, %1, 0 sc1" :: "v"(((u32x2){tg0, tg1})), "s"(rWS) : "memory");
  SB();
  ST(17);
  unsigned pw3, p3[3], yw, ys[2], vYr, ya, po;
  asm volatile("s_waitcnt lgkmcnt(4)\n\tv_mfma_f32_16x16x32_bf16 %0, a[64:67], a[60:63], 0" : "=&v"(ac[7]) :  : ); SB();
  asm volatile("ds_read2_b32 a[88:89], %0 offset0:176 offset1:177" :  : "v"(aa[3]) : "memory"); SB();
  asm volatile("" : "+v"(yq[0]), "+v"(yq[1]), "+v"(yq[2]), "+v"(yq[3]), "+v"(yq[4]), "+v"(yq[5]), "+v"(yq[6]), "+v"(yq[7]), "+v"(ykv) :  : "memory"); SB();
  asm volatile("v_mfma_f32_16x16x32_bf16 %0, a[64:67], a[56:59], 0" : "=&v"(ac[6]) :  : ); SB();
  asm volatile("ds_read2_b32 a[90:91], %0 offset0:178 offset1:179" :  : "v"(aa[3]) : "memory"); SB();
  asm volatile("buffer_load_dwordx4 a[224:227], %0, %1, %2 offen offset:2048 sc1" :  : "v"(vO2), "s"(rWS), "s"(4096u) : "memory"); SB();
  asm volatile("v_mfma_f32_16x16x32_bf16 %0, a[64:67], a[52:55], 0" : "=&v"(ac[5]) :  : ); SB();
  asm volatile("ds_read2_b32 a[92:93], %0 offset0:48 offset1:49" :  : "v"(aa[3]) : "memory"); SB();
  asm volatile("v_add_f32 %0, %0, %1" : "+v"(yq[0][0]) : "v"(yq[2][0]) : ); SB();
  asm volatile("v_mfma_f32_16x16x32_bf16 %0, a[64:67], a[48:51], 0" : "=&v"(ac[4]) :  : ); SB();
  asm volatile("ds_read2_b32 a[94:95], %0 offset0:50 offset1:51" :  : "v"(aa[3]) : "memory"); SB();
  asm volatile("buffer_load_dwordx4 a[228:231], %0, %1, %2 offen offset:3072 sc1" :  : "v"(vO2), "s"(rWS), "s"(4096u) : "memory"); SB();
  asm volatile("v_mfma_f32_16x16x32_bf16 %0, a[68:71], a[56:59], %0" : "+v"(ac[7]) :  : ); SB();
  asm volatile("ds_read_b128 a[32:35], %0 offset:192" :  : "v"(bb) : "memory"); SB();
  asm volatile("s_waitcnt lgkmcnt(6)\n\ts_barrier" :  :  : "memory"); SB();
  asm volatile("v_mfma_f32_16x16x32_bf16 %0, a[68:71], a[52:55], %0" : "+v"(ac[6]) :  : ); SB();
  asm volatile("ds_read2_b32 a[128:129], %0 offset0:160 offset1:161" :  : "v"(aa[0]) : "memory"); SB();
  asm volatile("buffer_load_dwordx4 a[232:235], %0, %1, %2 offen offset:0 sc1" :  : "v"(vO2), "s"(rWS), "s"(8192u) : "memory"); SB();
  asm volatile("v_mfma_f32_16x16x32_bf16 %0, a[68:71], a[48:51], %0" : "+v"(ac[5]) :  : ); SB();
  asm volatile("ds_read2_b32 a[130:131], %0 offset0:162 offset1:163" :  : "v"(aa[0]) : "memory"); SB();
  asm volatile("v_add_f32 %0, %0, %1" : "+v"(yq[0][1]) : "v"(yq[2][1]) : ); SB();
  asm volatile("v_mfma_f32_16x16x32_bf16 %0, a[68:71], a[44:47], %0" : "+v"(ac[4]) :  : ); SB();
  asm volatile("ds_read_b128 %0, %1 offset:0" : "=&v"(pr[0]) : "v"(pe[0]) : "memory"); SB();
  asm volatile("v_add_f32 %0, %0, %1" : "+v"(yq[0][2]) : "v"(yq[2][2]) : ); SB();
  asm volatile("v_mfma_f32_16x16x32_bf16 %0, a[72:75], a[52:55], %0" : "+v"(ac[7]) :  : ); SB();
  asm volatile("ds_read_b128 a[124:127], %0 offset:3712" :  : "v"(bb) : "memory"); SB();
  asm volatile("buffer_load_dwordx4 a[236:239], %0, %1, %2 offen offset:1024 sc1" :  : "v"(vO2), "s"(rWS), "s"(8192u) : "memory"); SB();
  asm volatile("v_mfma_f32_16x16x32_bf16 %0, a[72:75], a[48:51], %0" : "+v"(ac[6]) :  : ); SB();
  asm volatile("ds_read_b128 a[120:123], %0 offset:3200" :  : "v"(bb) : "memory"); SB();
  asm volatile("v_add_f32 %0, %0, %1" : "+v"(yq[0][3]) : "v"(yq[2][3]) : ); SB();
  asm volatile("v_mfma_f32_16x16x32_bf16 %0, a[72:75], a[44:47], %0" : "+v"(ac[5]) :  : ); SB();
  asm volatile("ds_read_b128 a[116:119], %0 offset:2688" :  : "v"(bb) : "memory"); SB();
  asm volatile("v_add_f32 %0, %0, %1" : "+v"(yq[1][0]) : "v"(yq[3][0]) : ); SB();
  asm volatile("v_mfma_f32_16x16x32_bf16 %0, a[64:67], a[44:47], 0" : "=&v"(ac[3]) :  : ); SB();
  asm volatile("ds_read_b128 %0, %1 offset:1024" : "=&v"(pr[1]) : "v"(pe[0]) : "memory"); SB();
  asm volatile("v_add_f32 %0, %0, %1" : "+v"(yq[1][1]) : "v"(yq[3][1]) : ); SB();
  asm volatile("v_mfma_f32_16x16x32_bf16 %0, a[76:79], a[48:51], %0" : "+v"(ac[7]) :  : ); SB();
  asm volatile("ds_read_b128 a[112:115], %0 offset:2176" :  : "v"(bb) : "memory"); SB();
  asm volatile("buffer_load_dwordx4 a[240:243], %0, %1, %2 offen offset:2048 sc1" :  : "v"(vO), "s"(rWS), "s"(8192u) : "memory"); SB();
  asm volatile("v_mfma_f32_16x16x32_bf16 %0, a[76:79], a[44:47], %0" : "+v"(ac[6]) :  : ); SB();
  asm volatile("ds_read2_b32 a[132:133], %0 offset0:32 offset1:33" :  : "v"(aa[0]) : "memory"); SB();
  asm volatile("v_add_f32 %0, %0, %1" : "+v"(yq[1][2]) : "v"(yq[3][2]) : ); SB();
  asm volatile("v_mfma_f32_16x16x32_bf16 %0, a[76:79], a[40:43], %0" : "+v"(ac[5]) :  : ); SB();
  asm volatile("ds_read2_b32 a[134:135], %0 offset0:34 offset1:35" :  : "v"(aa[0]) : "memory"); SB();
  asm volatile("buffer_load_dwordx4 a[244:247], %0, %1, %2 offen offset:3072 sc1" :  : "v"(vO), "s"(rWS), "s"(8192u) : "memory"); SB();
  asm volatile("v_mfma_f32_16x16x32_bf16 %0, a[72:75], a[40:43], %0" : "+v"(ac[4]) :  : ); SB();
  asm volatile("ds_read_b128 %0, %1 offset:2048" : "=&v"(pr[2]) : "v"(pe[1]) : "memory"); SB();
  asm volatile("v_add_f32 %0, %0, %1" : "+v"(yq[1][3]) : "v"(yq[3][3]) : ); SB();
  asm volatile("v_mfma_f32_16x16x32_bf16 %0, a[80:83], a[44:47], %0" : "+v"(ac[7]) :  : ); SB();
  asm volatile("ds_read_b128 a[108:111], %0 offset:1664" :  : "v"(bb) : "memory"); SB();
  asm volatile("v_add_f32 %0, %0, %1" : "+v"(yq[4][0]) : "v"(yq[6][0]) : ); SB();
  asm volatile("v_mfma_f32_16x16x32_bf16 %0, a[80:83], a[40:43], %0" : "+v"(ac[6]) :  : ); SB();
  asm volatile("ds_read2_b32 a[136:137], %0 offset0:160 offset1:161" :  : "v"(aa[1]) : "memory"); SB();
  asm volatile("v_add_f32 %0, %0, %1" : "+v"(yq[4][1]) : "v"(yq[6][1]) : ); SB();
  asm volatile("v_mfma_f32_16x16x32_bf16 %0, a[68:71], a[40:43], %0" : "+v"(ac[3]) :  : ); SB();
  asm volatile("ds_read2_b32 a[138:139], %0 offset0:162 offset1:163" :  : "v"(aa[1]) : "memory"); SB();
  asm volatile("buffer_load_dwordx4 a[248:251], %0, %1, %2 offen offset:0 sc1" :  : "v"(vO), "s"(rWS), "s"(12288u) : "memory"); SB();
  asm volatile("v_mfma_f32_16x16x32_bf16 %0, a[64:67], a[40:43], 0" : "=&v"(ac[2]) :  : ); SB();
  asm volatile("ds_read_b128 %0, %1 offset:3072" : "=&v"(pr[3]) : "v"(pe[1]) : "memory"); SB();
  asm volatile("v_add_f32 %0, %0, %1" : "+v"(yq[4][2]) : "v"(yq[6][2]) : ); SB();
  asm volatile("v_mfma_f32_16x16x32_bf16 %0, a[84:87], a[40:43], %0" : "+v"(ac[7]) :  : ); SB();
  asm volatile("ds_read2_b32 a[140:141], %0 offset0:32 offset1:33" :  : "v"(aa[1]) : "memory"); SB();
  asm volatile("s_waitcnt lgkmcnt(8)\n\tv_mfma_f32_16x16x32_bf16 %0, a[84:87], a[36:39], %0" : "+v"(ac[6]) :  : ); SB();
  asm volatile("ds_read2_b32 a[142:143], %0 offset0:34 offset1:35" :  : "v"(aa[1]) : "memory"); SB();
  asm volatile("v_add_f32 %0, %0, %1" : "+v"(yq[4][3]) : "v"(yq[6][3]) : ); SB();
  asm volatile("v_mfma_f32_16x16x32_bf16 %0, a[80:83], a[36:39], %0" : "+v"(ac[5]) :  : ); SB();
  asm volatile("ds_read_b128 a[104:107], %0 offset:1152" :  : "v"(bb) : "memory"); SB();
  asm volatile("v_add_f32 %0, %0, %1" : "+v"(yq[5][0]) : "v"(yq[7][0]) : ); SB();
  asm volatile("v_mfma_f32_16x16x32_bf16 %0, a[76:79], a[36:39], %0" : "+v"(ac[4]) :  : ); SB();
  asm volatile("ds_read_b128 %0, %1 offset:4096" : "=&v"(pr[4]) : "v"(pe[2]) : "memory"); SB();
  asm volatile("v_add_f32 %0, %0, %1" : "+v"(yq[5][1]) : "v"(yq[7][1]) : ); SB();
  asm volatile("v_mfma_f32_16x16x32_bf16 %0, a[88:91], a[36:39], %0" : "+v"(ac[7]) :  : ); SB();
  asm volatile("ds_read2_b32 a[144:145], %0 offset0:160 offset1:161" :  : "v"(aa[2]) : "memory"); SB();
  asm volatile("v_add_f32 %0, %0, %1" : "+v"(yq[5][2]) : "v"(yq[7][2]) : ); SB();
  asm volatile("v_mfma_f32_16x16x32_bf16 %0, a[72:75], a[36:39], %0" : "+v"(ac[3]) :  : ); SB();
  asm volatile("ds_read2_b32 a[146:147], %0 offset0:162 offset1:163" :  : "v"(aa[2]) : "memory"); SB();
  asm volatile("v_add_f32 %0, %0, %1" : "+v"(yq[5][3]) : "v"(yq[7][3]) : ); SB();
  asm volatile("v_mfma_f32_16x16x32_bf16 %0, a[68:71], a[36:39], %0" : "+v"(ac[2]) :  : ); SB();
  asm volatile("ds_read2_b32 a[148:149], %0 offset0:32 offset1:33" :  : "v"(aa[2]) : "memory"); SB();
  asm volatile("v_add_f32 %0, %0, %1" : "+v"(yq[0][0]) : "v"(yq[4][0]) : ); SB();
  asm volatile("v_mfma_f32_16x16x32_bf16 %0, a[64:67], a[36:39], 0" : "=&v"(ac[1]) :  : ); SB();
  asm volatile("ds_read_b128 %0, %1 offset:5120" : "=&v"(pr[5]) : "v"(pe[2]) : "memory"); SB();
  asm volatile("v_add_f32 %0, %0, %1" : "+v"(yq[0][1]) : "v"(yq[4][1]) : ); SB();
  asm volatile("v_mfma_f32_16x16x32_bf16 %0, a[92:95], a[32:35], %0" : "+v"(ac[7]) :  : ); SB();
  asm volatile("ds_read2_b32 a[150:151], %0 offset0:34 offset1:35" :  : "v"(aa[2]) : "memory"); SB();
  asm volatile("v_add_f32 %0, %0, %1" : "+v"(yq[0][2]) : "v"(yq[4][2]) : ); SB();
  asm volatile("v_mfma_f32_16x16x32_bf16 %0, a[88:91], a[32:35], %0" : "+v"(ac[6]) :  : ); SB();
  asm volatile("ds_read_b128 a[100:103], %0 offset:640" :  : "v"(bb) : "memory"); SB();
  asm volatile("v_add_f32 %0, %0, %1" : "+v"(yq[0][3]) : "v"(yq[4][3]) : ); SB();
  asm volatile("v_mfma_f32_16x16x32_bf16 %0, a[84:87], a[32:35], %0" : "+v"(ac[5]) :  : ); SB();
  asm volatile("ds_read2_b32 a[152:153], %0 offset0:160 offset1:161" :  : "v"(aa[3]) : "memory"); SB();
  asm volatile("v_add_f32 %0, %0, %1" : "+v"(yq[1][0]) : "v"(yq[5][0]) : ); SB();
  asm volatile("v_mfma_f32_16x16x32_bf16 %0, a[80:83], a[32:35], %0" : "+v"(ac[4]) :  : ); SB();
  pw3 = lb + 98304 + w * 15360 + l16; asm volatile("" : "+v"(pw3)); SB();
  asm volatile("v_mfma_f32_16x16x32_bf16 %0, a[76:79], a[32:35], %0" : "+v"(ac[3]) :  : ); SB();
  asm volatile("ds_read2_b32 a[154:155], %0 offset0:162 offset1:163" :  : "v"(aa[3]) : "memory"); SB();
  asm volatile("v_add_f32 %0, %0, %1" : "+v"(yq[1][1]) : "v"(yq[5][1]) : ); SB();
  asm volatile("v_mfma_f32_16x16x32_bf16 %0, a[72:75], a[32:35], %0" : "+v"(ac[2]) :  : ); SB();
  asm volatile("ds_read2_b32 a[156:157], %0 offset0:32 offset1:33" :  : "v"(aa[3]) : "memory"); SB();
  asm volatile("s_waitcnt lgkmcnt(5)" : "+v"(pr[0]), "+v"(pr[1]), "+v"(pr[2]), "+v"(pr[3]), "+v"(pr[4]), "+v"(pr[5]) :  : "memory"); SB();
  asm volatile("v_mfma_f32_16x16x32_bf16 %0, a[68:71], a[32:35], %0" : "+v"(ac[1]) :  : ); SB();
  asm volatile("ds_read2_b32 a[158:159], %0 offset0:34 offset1:35" :  : "v"(aa[3]) : "memory"); SB();
  asm volatile("buffer_load_dwordx4 a[252:255], %0, %1, %2 offen offset:1024 sc1" :  : "v"(vO), "s"(rWS), "s"(12288u) : "memory"); SB();
  asm volatile("v_mfma_f32_16x16x32_bf16 %0, a[64:67], a[32:35], 0" : "=&v"(ac[0]) :  : ); SB();
  {
    const f32x4 h0 = ((g[0][0][0] + pr[0]) + (pr[2] + pr[4])) * a1[1][1], h1 = ((g[0][0][1] + pr[1]) + (pr[3] + pr[5])) * a1[1][2];
    ho[2] = (u32x4){pack2(h0[0], h0[1]), pack2(h0[2], h0[3]), pack2(h1[0], h1[1]), nohole(pack2(h1[2], h1[3]))};
    SB();
  }
  asm volatile("v_mfma_f32_16x16x32_bf16 %0, a[128:131], a[124:127], %0" : "+v"(ac[7]) :  : ); SB();
  asm volatile("ds_read_b128 a[96:99], %0 offset:128" :  : "v"(bb) : "memory"); SB();
  asm volatile("buffer_load_dwordx4 a[0:3], %0, %1, %2 offen offset:2048 sc1" :  : "v"(vO), "s"(rWS), "s"(12288u) : "memory"); SB();
  asm volatile("v_mfma_f32_16x16x32_bf16 %0, a[128:131], a[120:123], %0" : "+v"(ac[6]) :  : ); SB();
  asm volatile("ds_read2_b32 a[64:65], %0 offset0:144 offset1:145" :  : "v"(aa[0]) : "memory"); SB();
  asm volatile("buffer_load_dwordx4 a[4:7], %0, %1, %2 offen offset:2048 sc1" :  : "v"(vO2), "s"(rWS), "s"(8192u) : "memory"); SB();
  asm volatile("v_mfma_f32_16x16x32_bf16 %0, a[128:131], a[116:119], %0" : "+v"(ac[5]) :  : ); SB();
  asm volatile("ds_read2_b32 a[66:67], %0 offset0:146 offset1:147" :  : "v"(aa[0]) : "memory"); SB();
  asm volatile("buffer_load_dwordx4 a[8:11], %0, %1, %2 offen offset:3072 sc1" :  : "v"(vO2), "s"(rWS), "s"(8192u) : "memory"); SB();
  asm volatile("v_mfma_f32_16x16x32_bf16 %0, a[128:131], a[112:115], %0" : "+v"(ac[4]) :  : ); SB();
  asm volatile("buffer_store_dwordx4 %1, %0, %2, %3 offen offset:2048 sc1\n\ts_nop 1" :  : "v"(vH_), "v"(ho[2]), "s"(rWS), "s"(0u) : "memory"); SB();
  asm volatile("v_mfma_f32_16x16x32_bf16 %0, a[132:135], a[120:123], %0" : "+v"(ac[7]) :  : ); SB();
  asm volatile("ds_read_b128 a[60:63], %0 offset:3648" :  : "v"(bb) : "memory"); SB();
  asm volatile("v_add_f32 %0, %0, %1" : "+v"(yq[1][2]) : "v"(yq[5][2]) : ); SB();
  asm volatile("v_mfma_f32_16x16x32_bf16 %0, a[132:135], a[116:119], %0" : "+v"(ac[6]) :  : ); SB();
  asm volatile("ds_read_b128 a[56:59], %0 offset:3136" :  : "v"(bb) : "memory"); SB();
  asm volatile("v_add_f32 %0, %0, %1" : "+v"(yq[1][3]) : "v"(yq[5][3]) : ); SB();
  asm volatile("v_mfma_f32_16x16x32_bf16 %0, a[132:135], a[112:115], %0" : "+v"(ac[5]) :  : ); SB();
  asm volatile("ds_read_b128 a[52:55], %0 offset:2624" :  : "v"(bb) : "memory"); SB();
  asm volatile("v_lshlrev_b32 %0, 16, %1" : "=v"(yq[2][0]) : "v"(ykv[0]) : ); SB();
  asm volatile("v_mfma_f32_16x16x32_bf16 %0, a[132:135], a[108:111], %0" : "+v"(ac[4]) :  : ); SB();
  p3[0] = lb + 98304 + (w ^ 1) * 15360 + l16; asm volatile("" : "+v"(p3[0])); SB();
  asm volatile("v_mfma_f32_16x16x32_bf16 %0, a[136:139], a[116:119], %0" : "+v"(ac[7]) :  : ); SB();
  asm volatile("ds_read_b128 a[48:51], %0 offset:2112" :  : "v"(bb) : "memory"); SB();
  asm volatile("buffer_load_dwordx4 a[12:15], %0, %1, %2 offen offset:0 sc1" :  : "v"(vO2), "s"(rWS), "s"(12288u) : "memory"); SB();
  asm volatile("v_mfma_f32_16x16x32_bf16 %0, a[136:139], a[112:115], %0" : "+v"(ac[6]) :  : ); SB();
  asm volatile("ds_read2_b32 a[68:69], %0 offset0:16 offset1:17" :  : "v"(aa[0]) : "memory"); SB();
  asm volatile("v_and_b32 %0, 0xffff0000, %1" : "=v"(yq[2][1]) : "v"(ykv[0]) : ); SB();
  asm volatile("v_mfma_f32_16x16x32_bf16 %0, a[136:139], a[108:111], %0" : "+v"(ac[5]) :  : ); SB();
  asm volatile("ds_read2_b32 a[70:71], %0 offset0:18 offset1:19" :  : "v"(aa[0]) : "memory"); SB();
  asm volatile("v_lshlrev_b32 %0, 16, %1" : "=v"(yq[2][2]) : "v"(ykv[1]) : ); SB();
  asm volatile("v_mfma_f32_16x16x32_bf16 %0, a[128:131], a[108:111], %0" : "+v"(ac[3]) :  : ); SB();
  p3[1] = lb + 98304 + (w ^ 2) * 15360 + l16; asm volatile("" : "+v"(p3[1])); SB();
  asm volatile("v_mfma_f32_16x16x32_bf16 %0, a[140:143], a[112:115], %0" : "+v"(ac[7]) :  : ); SB();
  asm volatile("ds_read_b128 a[44:47], %0 offset:1600" :  : "v"(bb) : "memory"); SB();
  asm volatile("buffer_load_dwordx4 a[16:19], %0, %1, %2 offen offset:1024 sc1" :  : "v"(vO2), "s"(rWS), "s"(12288u) : "memory"); SB();
  asm volatile("v_mfma_f32_16x16x32_bf16 %0, a[140:143], a[108:111], %0" : "+v"(ac[6]) :  : ); SB();
  asm volatile("ds_read2_b32 a[72:73], %0 offset0:144 offset1:145" :  : "v"(aa[1]) : "memory"); SB();
  asm volatile("v_and_b32 %0, 0xffff0000, %1" : "=v"(yq[2][3]) : "v"(ykv[1]) : ); SB();
  asm volatile("v_mfma_f32_16x16x32_bf16 %0, a[140:143], a[104:107], %0" : "+v"(ac[5]) :  : ); SB();
  asm volatile("ds_read2_b32 a[74:75], %0 offset0:146 offset1:147" :  : "v"(aa[1]) : "memory"); SB();
  asm volatile("buffer_load_dwordx4 a[20:23], %0, %1, %2 offen offset:2048 sc1" :  : "v"(vO2), "s"(rWS), "s"(12288u) : "memory"); SB();
  asm volatile("v_mfma_f32_16x16x32_bf16 %0, a[136:139], a[104:107], %0" : "+v"(ac[4]) :  : ); SB();
  p3[2] = lb + 98304 + (w ^ 3) * 15360 + l16; asm volatile("" : "+v"(p3[2])); SB();
  asm volatile("v_mfma_f32_16x16x32_bf16 %0, a[144:147], a[108:111], %0" : "+v"(ac[7]) :  : ); SB();
  asm volatile("ds_read2_b32 a[76:77], %0 offset0:16 offset1:17" :  : "v"(aa[1]) : "memory"); SB();
  asm volatile("v_lshlrev_b32 %0, 16, %1" : "=v"(yq[3][0]) : "v"(ykv[2]) : ); SB();
  asm volatile("v_mfma_f32_16x16x32_bf16 %0, a[144:147], a[104:107], %0" : "+v"(ac[6]) :  : ); SB();
  asm volatile("ds_read2_b32 a[78:79], %0 offset0:18 offset1:19" :  : "v"(aa[1]) : "memory"); SB();
  asm volatile("v_and_b32 %0, 0xffff0000, %1" : "=v"(yq[3][1]) : "v"(ykv[2]) : ); SB();
  asm volatile("v_mfma_f32_16x16x32_bf16 %0, a[132:135], a[104:107], %0" : "+v"(ac[3]) :  : ); SB();
  asm volatile("ds_read_b128 a[40:43], %0 offset:1088" :  : "v"(bb) : "memory"); SB();
  asm volatile("buffer_load_dwordx2 %0, %1, %2, %3 offen sc1" : "=&v"(fh) : "v"(vFH), "s"(rWS), "s"(0u) : "memory"); SB();
  asm volatile("v_mfma_f32_16x16x32_bf16 %0, a[128:131], a[104:107], %0" : "+v"(ac[2]) :  : ); SB();
  yw = (lane ^ ((lane >> 3) & 6)) * 16; asm volatile("" : "+v"(yw)); SB();
  asm volatile("s_waitcnt lgkmcnt(6)\n\tv_mfma_f32_16x16x32_bf16 %0, a[148:151], a[104:107], %0" : "+v"(ac[7]) :  : ); SB();
  asm volatile("ds_read2_b32 a[80:81], %0 offset0:144 offset1:145" :  : "v"(aa[2]) : "memory"); SB();
  asm volatile("v_lshlrev_b32 %0, 16, %1" : "=v"(yq[3][2]) : "v"(ykv[3]) : ); SB();
  asm volatile("v_mfma_f32_16x16x32_bf16 %0, a[148:151], a[100:103], %0" : "+v"(ac[6]) :  : ); SB();
  asm volatile("ds_read2_b32 a[82:83], %0 offset0:146 offset1:147" :  : "v"(aa[2]) : "memory"); SB();
  asm volatile("v_and_b32 %0, 0xffff0000, %1" : "=v"(yq[3][3]) : "v"(ykv[3]) : ); SB();
  asm volatile("v_mfma_f32_16x16x32_bf16 %0, a[144:147], a[100:103], %0" : "+v"(ac[5]) :  : ); SB();
  asm volatile("ds_read2_b32 a[84:85], %0 offset0:16 offset1:17" :  : "v"(aa[2]) : "memory"); SB();
  asm volatile("v_fmac_f32 %0, %1, %2" : "+v"(yq[0][0]) : "v"(yq[2][0]), "v"(dD[2]) : ); SB();
  asm volatile("v_mfma_f32_16x16x32_bf16 %0, a[140:143], a[100:103], %0" : "+v"(ac[4]) :  : ); SB();
  yw = yw + lb + 32768 + w * 3072; asm volatile("" : "+v"(yw)); SB();
  asm volatile("v_mfma_f32_16x16x32_bf16 %0, a[152:155], a[100:103], %0" : "+v"(ac[7]) :  : ); SB();
  asm volatile("ds_read2_b32 a[86:87], %0 offset0:18 offset1:19" :  : "v"(aa[2]) : "memory"); SB();
  asm volatile("v_fmac_f32 %0, %1, %2" : "+v"(yq[0][1]) : "v"(yq[2][1]), "v"(dD[2]) : ); SB();
  asm volatile("v_mfma_f32_16x16x32_bf16 %0, a[136:139], a[100:103], %0" : "+v"(ac[3]) :  : ); SB();
  asm volatile("ds_read_b128 a[36:39], %0 offset:576" :  : "v"(bb) : "memory"); SB();
  asm volatile("v_fmac_f32 %0, %1, %2" : "+v"(yq[0][2]) : "v"(yq[2][2]), "v"(dD[2]) : ); SB();
  asm volatile("v_mfma_f32_16x16x32_bf16 %0, a[132:135], a[100:103], %0" : "+v"(ac[2]) :  : ); SB();
  asm volatile("ds_read2_b32 a[88:89], %0 offset0:144 offset1:145" :  : "v"(aa[3]) : "memory"); SB();
  asm volatile("v_fmac_f32 %0, %1, %2" : "+v"(yq[0][3]) : "v"(yq[2][3]), "v"(dD[2]) : ); SB();
  asm volatile("v_mfma_f32_16x16x32_bf16 %0, a[128:131], a[100:103], %0" : "+v"(ac[1]) :  : ); SB();
  ya = lb + 32768 + (4 * g4 + q4) * 128 + 8 * p4; asm volatile("" : "+v"(ya)); SB();
  asm volatile("v_mfma_f32_16x16x32_bf16 %0, a[156:159], a[96:99], %0" : "+v"(ac[7]) :  : ); SB();
  asm volatile("ds_read2_b32 a[90:91], %0 offset0:146 offset1:147" :  : "v"(aa[3]) : "memory"); SB();
  asm volatile("v_fmac_f32 %0, %1, %2" : "+v"(yq[1][0]) : "v"(yq[3][0]), "v"(dD[2]) : ); SB();
  asm volatile("v_mfma_f32_16x16x32_bf16 %0, a[152:155], a[96:99], %0" : "+v"(ac[6]) :  : ); SB();
  asm volatile("ds_read2_b32 a[92:93], %0 offset0:16 offset1:17" :  : "v"(aa[3]) : "memory"); SB();
  asm volatile("v_fmac_f32 %0, %1, %2" : "+v"(yq[1][1]) : "v"(yq[3][1]), "v"(dD[2]) : ); SB();
  asm volatile("v_mfma_f32_16x16x32_bf16 %0, a[148:151], a[96:99], %0" : "+v"(ac[5]) :  : ); SB();
  asm volatile("ds_read2_b32 a[94:95], %0 offset0:18 offset1:19" :  : "v"(aa[3]) : "memory"); SB();
  asm volatile("v_fmac_f32 %0, %1, %2" : "+v"(yq[1][2]) : "v"(yq[3][2]), "v"(dD[2]) : ); SB();
  asm volatile("v_mfma_f32_16x16x32_bf16 %0, a[144:147], a[96:99], %0" : "+v"(ac[4]) :  : ); SB();
  po = (2 * g4 + (q4 >> 1)) & 3; asm volatile("" : "+v"(po)); SB();
  asm volatile("v_mfma_f32_16x16x32_bf16 %0, a[140:143], a[96:99], %0" : "+v"(ac[3]) :  : ); SB();
  asm volatile("ds_read_b128 a[32:35], %0 offset:64" :  : "v"(bb) : "memory"); SB();
  asm volatile("v_fmac_f32 %0, %1, %2" : "+v"(yq[1][3]) : "v"(yq[3][3]), "v"(dD[2]) : ); SB();
  asm volatile("v_mfma_f32_16x16x32_bf16 %0, a[136:139], a[96:99], %0" : "+v"(ac[2]) :  : ); SB();
  ya = ya + ((w ^ po) << 5); asm volatile("" : "+v"(ya)); SB();
  asm volatile("v_mfma_f32_16x16x32_bf16 %0, a[132:135], a[96:99], %0" : "+v"(ac[1]) :  : ); SB();
  po = vX + 24 * (g4 & 1); asm volatile("" : "+v"(po)); SB();
  asm volatile("v_mfma_f32_16x16x32_bf16 %0, a[128:131], a[96:99], %0" : "+v"(ac[0]) :  : ); SB();
  asm volatile("v_cvt_pk_bf16_f32 %0, %1, %2" : "=v"(y2[0]) : "v"(yq[0][0]), "v"(yq[0][1]) : ); SB();
  asm volatile("v_mfma_f32_16x16x32_bf16 %0, a[64:67], a[60:63], %0" : "+v"(ac[7]) :  : ); SB();
  asm volatile("ds_read2_b32 a[128:129], %0 offset0:128 offset1:129" :  : "v"(aa[0]) : "memory"); SB();
  asm volatile("v_cvt_pk_bf16_f32 %0, %1, %2" : "=v"(y2[1]) : "v"(yq[0][2]), "v"(yq[0][3]) : ); SB();
  asm volatile("v_mfma_f32_16x16x32_bf16 %0, a[64:67], a[56:59], %0" : "+v"(ac[6]) :  : ); SB();
  asm volatile("ds_read2_b32 a[130:131], %0 offset0:130 offset1:131" :  : "v"(aa[0]) : "memory"); SB();
  asm volatile("v_cvt_pk_bf16_f32 %0, %1, %2" : "=v"(y2[2]) : "v"(yq[1][0]), "v"(yq[1][1]) : ); SB();
  asm volatile("v_mfma_f32_16x16x32_bf16 %0, a[64:67], a[52:55], %0" : "+v"(ac[5]) :  : ); SB();
  asm volatile("ds_read_b128 a[124:127], %0 offset:3584" :  : "v"(bb) : "memory"); SB();
  asm volatile("v_cvt_pk_bf16_f32 %0, %1, %2" : "=v"(y2[3]) : "v"(yq[1][2]), "v"(yq[1][3]) : ); SB();
  asm volatile("v_mfma_f32_16x16x32_bf16 %0, a[64:67], a[48:51], %0" : "+v"(ac[4]) :  : ); SB();
  asm volatile("v_min_u32 %0, -2, %0" : "+v"(y2[3]) :  : ); SB();
  asm volatile("v_mfma_f32_16x16x32_bf16 %0, a[68:71], a[56:59], %0" : "+v"(ac[7]) :  : ); SB();
  asm volatile("ds_read_b128 a[120:123], %0 offset:3072" :  : "v"(bb) : "memory"); SB();
  asm volatile("v_xnor_b32 %0, %0, %1" : "+v"(y2[3]) : "v"(ykv[3]) : ); SB();
  asm volatile("v_mfma_f32_16x16x32_bf16 %0, a[68:71], a[52:55], %0" : "+v"(ac[6]) :  : ); SB();
  asm volatile("ds_read_b128 a[96:99], %0 offset:0" :  : "v"(bb) : "memory"); SB();
  asm volatile("v_mfma_f32_16x16x32_bf16 %0, a[68:71], a[48:51], %0" : "+v"(ac[5]) :  : ); SB();
  asm volatile("ds_read_b128 a[100:103], %0 offset:512" :  : "v"(bb) : "memory"); SB();
  asm volatile("buffer_store_dwordx4 %1, %0, %2, %3 offen offset:0 sc1\n\ts_nop 1" :  : "v"(vF), "v"(y2), "s"(rWS), "s"(uso) : "memory"); SB();
  asm volatile("s_waitcnt lgkmcnt(6)\n\tv_mfma_f32_16x16x32_bf16 %0, a[68:71], a[44:47], %0" : "+v"(ac[4]) :  : ); SB();
  asm volatile("v_mfma_f32_16x16x32_bf16 %0, a[72:75], a[52:55], %0" : "+v"(ac[7]) :  : ); SB();
  asm volatile("ds_read2_b32 a[132:133], %0 offset0:0 offset1:1" :  : "v"(aa[0]) : "memory"); SB();
  asm volatile("v_mfma_f32_16x16x32_bf16 %0, a[72:75], a[48:51], %0" : "+v"(ac[6]) :  : ); SB();
  asm volatile("ds_read2_b32 a[134:135], %0 offset0:2 offset1:3" :  : "v"(aa[0]) : "memory"); SB();
  asm volatile("v_mfma_f32_16x16x32_bf16 %0, a[72:75], a[44:47], %0" : "+v"(ac[5]) :  : ); SB();
  asm volatile("ds_read_b128 a[116:119], %0 offset:2560" :  : "v"(bb) : "memory"); SB();
  asm volatile("v_mfma_f32_16x16x32_bf16 %0, a[64:67], a[44:47], %0" : "+v"(ac[3]) :  : ); SB();
  asm volatile("v_mfma_f32_16x16x32_bf16 %0, a[76:79], a[48:51], %0" : "+v"(ac[7]) :  : ); SB();
  asm volatile("ds_read_b128 a[104:107], %0 offset:1024" :  : "v"(bb) : "memory"); SB();
  asm volatile("v_mfma_f32_16x16x32_bf16 %0, a[76:79], a[44:47], %0" : "+v"(ac[6]) :  : ); SB();
  asm volatile("ds_read2_b32 a[136:137], %0 offset0:128 offset1:129" :  : "v"(aa[1]) : "memory"); SB();
  asm volatile("v_mfma_f32_16x16x32_bf16 %0, a[76:79], a[40:43], %0" : "+v"(ac[5]) :  : ); SB();
  asm volatile("ds_read2_b32 a[138:139], %0 offset0:130 offset1:131" :  : "v"(aa[1]) : "memory"); SB();
  asm volatile("v_mfma_f32_16x16x32_bf16 %0, a[72:75], a[40:43], %0" : "+v"(ac[4]) :  : ); SB();
  asm volatile("v_mfma_f32_16x16x32_bf16 %0, a[80:83], a[44:47], %0" : "+v"(ac[7]) :  : ); SB();
  asm volatile("ds_read_b128 a[108:111], %0 offset:1536" :  : "v"(bb) : "memory"); SB();
  asm volatile("v_mfma_f32_16x16x32_bf16 %0, a[80:83], a[40:43], %0" : "+v"(ac[6]) :  : ); SB();
  asm volatile("ds_read_b128 a[112:115], %0 offset:2048" :  : "v"(bb) : "memory"); SB();
  asm volatile("v_mfma_f32_16x16x32_bf16 %0, a[68:71], a[40:43], %0" : "+v"(ac[3]) :  : ); SB();
  asm volatile("ds_read2_b32 a[140:141], %0 offset0:0 offset1:1" :  : "v"(aa[1]) : "memory"); SB();
  asm volatile("v_mfma_f32_16x16x32_bf16 %0, a[64:67], a[40:43], %0" : "+v"(ac[2]) :  : ); SB();
  asm volatile("v_mfma_f32_16x16x32_bf16 %0, a[84:87], a[40:43], %0" : "+v"(ac[7]) :  : ); SB();
  asm volatile("ds_read2_b32 a[142:143], %0 offset0:2 offset1:3" :  : "v"(aa[1]) : "memory"); SB();
  asm volatile("v_mfma_f32_16x16x32_bf16 %0, a[84:87], a[36:39], %0" : "+v"(ac[6]) :  : ); SB();
  asm volatile("ds_read2_b32 a[144:145], %0 offset0:128 offset1:129" :  : "v"(aa[2]) : "memory"); SB();
  asm volatile("v_mfma_f32_16x16x32_bf16 %0, a[80:83], a[36:39], %0" : "+v"(ac[5]) :  : ); SB();
  asm volatile("ds_read2_b32 a[146:147], %0 offset0:130 offset1:131" :  : "v"(aa[2]) : "memory"); SB();
  asm volatile("v_mfma_f32_16x16x32_bf16 %0, a[76:79], a[36:39], %0" : "+v"(ac[4]) :  : ); SB();
  asm volatile("v_mfma_f32_16x16x32_bf16 %0, a[88:91], a[36:39], %0" : "+v"(ac[7]) :  : ); SB();
  asm volatile("ds_read2_b32 a[148:149], %0 offset0:0 offset1:1" :  : "v"(aa[2]) : "memory"); SB();
  asm volatile("v_mfma_f32_16x16x32_bf16 %0, a[72:75], a[36:39], %0" : "+v"(ac[3]) :  : ); SB();
  asm volatile("ds_read2_b32 a[150:151], %0 offset0:2 offset1:3" :  : "v"(aa[2]) : "memory"); SB();
  asm volatile("v_mfma_f32_16x16x32_bf16 %0, a[68:71], a[36:39], %0" : "+v"(ac[2]) :  : ); SB();
  asm volatile("ds_read2_b32 a[152:153], %0 offset0:128 offset1:129" :  : "v"(aa[3]) : "memory"); SB();
  asm volatile("v_mfma_f32_16x16x32_bf16 %0, a[64:67], a[36:39], %0" : "+v"(ac[1]) :  : ); SB();
  asm volatile("v_mfma_f32_16x16x32_bf16 %0, a[92:95], a[32:35], %0" : "+v"(ac[7]) :  : ); SB();
  asm volatile("ds_read2_b32 a[154:155], %0 offset0:130 offset1:131" :  : "v"(aa[3]) : "memory"); SB();
  asm volatile("v_mfma_f32_16x16x32_bf16 %0, a[88:91], a[32:35], %0" : "+v"(ac[6]) :  : ); SB();
  asm volatile("ds_read2_b32 a[156:157], %0 offset0:0 offset1:1" :  : "v"(aa[3]) : "memory"); SB();
  asm volatile("v_mfma_f32_16x16x32_bf16 %0, a[84:87], a[32:35], %0" : "+v"(ac[5]) :  : ); SB();
  asm volatile("ds_read2_b32 a[158:159], %0 offset0:2 offset1:3" :  : "v"(aa[3]) : "memory"); SB();
  asm volatile("v_mfma_f32_16x16x32_bf16 %0, a[80:83], a[32:35], %0" : "+v"(ac[4]) :  : ); SB();
  asm volatile("v_mfma_f32_16x16x32_bf16 %0, a[76:79], a[32:35], %0" : "+v"(ac[3]) :  : ); SB();
  asm volatile("v_mfma_f32_16x16x32_bf16 %0, a[72:75], a[32:35], %0" : "+v"(ac[2]) :  : ); SB();
  asm volatile("v_mfma_f32_16x16x32_bf16 %0, a[68:71], a[32:35], %0" : "+v"(ac[1]) :  : ); SB();
  asm volatile("v_mfma_f32_16x16x32_bf16 %0, a[64:67], a[32:35], %0" : "+v"(ac[0]) :  : ); SB();
  asm volatile("s_waitcnt lgkmcnt(4)\n\tv_mfma_f32_16x16x32_bf16 %0, a[128:131], a[124:127], %0" : "+v"(ac[7]) :  : ); SB();
  asm volatile("v_mfma_f32_16x16x32_bf16 %0, a[128:131], a[120:123], %0" : "+v"(ac[6]) :  : ); SB();
  asm volatile("v_mfma_f32_16x16x32_bf16 %0, a[128:131], a[96:99], %0" : "+v"(ac[0]) :  : ); SB();
  asm volatile("v_mfma_f32_16x16x32_bf16 %0, a[128:131], a[100:103], %0" : "+v"(ac[1]) :  : ); SB();
  asm volatile("v_mfma_f32_16x16x32_bf16 %0, a[132:135], a[120:123], %0" : "+v"(ac[7]) :  : ); SB();
  asm volatile("v_mfma_f32_16x16x32_bf16 %0, a[132:135], a[116:119], %0" : "+v"(ac[6]) :  : ); SB();
  asm volatile("v_mfma_f32_16x16x32_bf16 %0, a[128:131], a[104:107], %0" : "+v"(ac[2]) :  : ); SB();
  asm volatile("ds_write_b128 %0, %1 offset:0" :  : "v"(dd), "v"(ac[0]) : "memory"); SB();
  asm volatile("v_mfma_f32_16x16x32_bf16 %0, a[132:135], a[96:99], %0" : "+v"(ac[1]) :  : ); SB();
  asm volatile("s_waitcnt vmcnt(1)\n\t.Lc_%=:\n\tv_cmp_ne_u64 vcc, %2, %1\n\ts_cbranch_vccnz .Lr_%=\n\t.Ld_%=:\n\t.subsection 1\n\t.Lr_%=:\n\tv_add_u32 %0, -1, %0\n\ts_nop 1\n\tv_readfirstlane_b32 vcc_hi, %0\n\ts_nop 3\n\ts_cmp_lt_i32 vcc_hi, 1\n\ts_cbranch_scc1 .Ld_%=\n\ts_sleep 2\n\ts_mov_b32 vcc_lo, 0\n\tbuffer_load_dwordx2 %1, %3, %4, vcc_lo offen sc1\n\ts_waitcnt vmcnt(0)\n\ts_branch .Lc_%=\n\t.subsection 0" : "+v"(tmo), "+v"(fh) : "s"(tg), "v"(vFH), "s"(rWS) : "memory", "vcc", "scc"); SB();
  asm volatile("buffer_load_dwordx4 v[164:167], %0, %1, %2 offen offset:0 sc1" :  : "v"(vHr), "s"(rWS), "s"(hso[2]) : "memory"); SB();
  asm volatile("v_mfma_f32_16x16x32_bf16 %0, a[136:139], a[116:119], %0" : "+v"(ac[7]) :  : ); SB();
  asm volatile("buffer_load_dwordx4 v[168:171], %0, %1, %2 offen offset:0 sc1" :  : "v"(vHr), "s"(rWS), "s"(hso[4]) : "memory"); SB();
  asm volatile("v_mfma_f32_16x16x32_bf16 %0, a[128:131], a[108:111], %0" : "+v"(ac[3]) :  : ); SB();
  asm volatile("buffer_load_dwordx4 v[172:175], %0, %1, %2 offen offset:0 sc1" :  : "v"(vHr), "s"(rWS), "s"(hso[6]) : "memory"); SB();
  asm volatile("v_mfma_f32_16x16x32_bf16 %0, a[132:135], a[100:103], %0" : "+v"(ac[2]) :  : ); SB();
  asm volatile("v_mfma_f32_16x16x32_bf16 %0, a[128:131], a[112:115], %0" : "+v"(ac[4]) :  : ); SB();
  asm volatile("buffer_load_dwordx4 v[160:163], %0, %1, %2 offen offset:0 sc1" :  : "v"(vHr), "s"(rWS), "s"(hso[0]) : "memory"); SB();
  asm volatile("v_mfma_f32_16x16x32_bf16 %0, a[140:143], a[112:115], %0" : "+v"(ac[7]) :  : ); SB();
  asm volatile("ds_write_b128 %0, %1 offset:1024" :  : "v"(dd), "v"(ac[1]) : "memory"); SB();
  asm volatile("v_mfma_f32_16x16x32_bf16 %0, a[132:135], a[104:107], %0" : "+v"(ac[3]) :  : ); SB();
  asm volatile("buffer_load_dwordx4 v[180:183], %0, %1, %2 offen offset:1024 sc1" :  : "v"(vHr), "s"(rWS), "s"(hso[2]) : "memory"); SB();
  asm volatile("v_mfma_f32_16x16x32_bf16 %0, a[136:139], a[96:99], %0" : "+v"(ac[2]) :  : ); SB();
  asm volatile("v_mfma_f32_16x16x32_bf16 %0, a[132:135], a[108:111], %0" : "+v"(ac[4]) :  : ); SB();
  asm volatile("v_mfma_f32_16x16x32_bf16 %0, a[128:131], a[116:119], %0" : "+v"(ac[5]) :  : ); SB();
  asm volatile("v_mfma_f32_16x16x32_bf16 %0, a[136:139], a[100:103], %0" : "+v"(ac[3]) :  : ); SB();
  asm volatile("buffer_load_dwordx4 v[184:187], %0, %1, %2 offen offset:1024 sc1" :  : "v"(vHr), "s"(rWS), "s"(hso[4]) : "memory"); SB();
  asm volatile("v_mfma_f32_16x16x32_bf16 %0, a[136:139], a[112:115], %0" : "+v"(ac[6]) :  : ); SB();
  asm volatile("ds_write_b128 %0, %1 offset:2048" :  : "v"(dd), "v"(ac[2]) : "memory"); SB();
  asm volatile("v_mfma_f32_16x16x32_bf16 %0, a[136:139], a[104:107], %0" : "+v"(ac[4]) :  : ); SB();
  asm volatile("buffer_load_dwordx4 v[188:191], %0, %1, %2 offen offset:1024 sc1" :  : "v"(vHr), "s"(rWS), "s"(hso[6]) : "memory"); SB();
  asm volatile("v_mfma_f32_16x16x32_bf16 %0, a[132:135], a[112:115], %0" : "+v"(ac[5]) :  : ); SB();
  asm volatile("v_mfma_f32_16x16x32_bf16 %0, a[140:143], a[96:99], %0" : "+v"(ac[3]) :  : ); SB();
  asm volatile("v_mfma_f32_16x16x32_bf16 %0, a[140:143], a[108:111], %0" : "+v"(ac[6]) :  : ); SB();
  asm volatile("v_mfma_f32_16x16x32_bf16 %0, a[140:143], a[100:103], %0" : "+v"(ac[4]) :  : ); SB();
  asm volatile("buffer_load_dwordx4 v[176:179], %0, %1, %2 offen offset:1024 sc1" :  : "v"(vHr), "s"(rWS), "s"(hso[0]) : "memory"); SB();
  asm volatile("v_mfma_f32_16x16x32_bf16 %0, a[136:139], a[108:111], %0" : "+v"(ac[5]) :  : ); SB();
  asm volatile("v_mfma_f32_16x16x32_bf16 %0, a[144:147], a[108:111], %0" : "+v"(ac[7]) :  : ); SB();
  asm volatile("ds_write_b128 %0, %1 offset:3072" :  : "v"(dd), "v"(ac[3]) : "memory"); SB();
  asm volatile("v_mfma_f32_16x16x32_bf16 %0, a[144:147], a[104:107], %0" : "+v"(ac[6]) :  : ); SB();
  asm volatile("buffer_load_dwordx4 v[196:199], %0, %1, %2 offen offset:0 sc1" :  : "v"(vHr), "s"(rWS), "s"(hso[3]) : "memory"); SB();
  asm volatile("v_mfma_f32_16x16x32_bf16 %0, a[144:147], a[96:99], %0" : "+v"(ac[4]) :  : ); SB();
  asm volatile("v_mfma_f32_16x16x32_bf16 %0, a[140:143], a[104:107], %0" : "+v"(ac[5]) :  : ); SB();
  asm volatile("v_mfma_f32_16x16x32_bf16 %0, a[148:151], a[104:107], %0" : "+v"(ac[7]) :  : ); SB();
  asm volatile("buffer_load_dwordx4 v[200:203], %0, %1, %2 offen offset:0 sc1" :  : "v"(vHr), "s"(rWS), "s"(hso[5]) : "memory"); SB();
  asm volatile("v_mfma_f32_16x16x32_bf16 %0, a[148:151], a[100:103], %0" : "+v"(ac[6]) :  : ); SB();
  asm volatile("v_mfma_f32_16x16x32_bf16 %0, a[144:147], a[100:103], %0" : "+v"(ac[5]) :  : ); SB();
  asm volatile("ds_write_b128 %0, %1 offset:4096" :  : "v"(dd), "v"(ac[4]) : "memory"); SB();
  asm volatile("s_waitcnt lgkmcnt(2)\n\tv_mfma_f32_16x16x32_bf16 %0, a[152:155], a[100:103], %0" : "+v"(ac[7]) :  : ); SB();
  asm volatile("buffer_load_dwordx4 v[204:207], %0, %1, %2 offen offset:0 sc1" :  : "v"(vHr), "s"(rWS), "s"(hso[7]) : "memory"); SB();
  asm volatile("v_mfma_f32_16x16x32_bf16 %0, a[152:155], a[96:99], %0" : "+v"(ac[6]) :  : ); SB();
  asm volatile("v_mfma_f32_16x16x32_bf16 %0, a[148:151], a[96:99], %0" : "+v"(ac[5]) :  : ); SB();
  asm volatile("buffer_load_dwordx4 v[192:195], %0, %1, %2 offen offset:0 sc1" :  : "v"(vHr), "s"(rWS), "s"(hso[1]) : "memory"); SB();
  asm volatile("v_mfma_f32_16x16x32_bf16 %0, a[156:159], a[96:99], %0" : "+v"(ac[7]) :  : ); SB();
  asm volatile("s_nop 5" : "+v"(ac[5]), "+v"(ac[6]), "+v"(ac[7]) :  : ); SB();
  asm volatile("ds_write_b128 %0, %1 offset:6144" :  : "v"(dd), "v"(ac[6]) : "memory"); SB();
  asm volatile("ds_write_b128 %0, %1 offset:5120" :  : "v"(dd), "v"(ac[5]) : "memory"); SB();
  asm volatile("ds_write_b128 %0, %1 offset:7168" :  : "v"(dd), "v"(ac[7]) : "memory"); SB();
  ST(18);
  f32x4 yp[2][4], o3[4][5];
  u32x4 yk[2], yo[2];
  unsigned vtmp;
  asm volatile("ds_read_b128 %0, %1 offset:0" : "=&v"(yk[0]) : "v"(ykr) : "memory"); SB();
  asm volatile("ds_read_b128 %0, %1 offset:12928" : "=&v"(yk[1]) : "v"(ykr) : "memory"); SB();
  asm volatile("s_waitcnt lgkmcnt(0)\n\ts_barrier" :  :  : "memory"); SB();
  asm volatile("ds_read_b128 %0, %1 offset:0" : "=&v"(yp[0][0]) : "v"(ydr) : "memory"); SB();
  asm volatile("ds_read_b128 %0, %1 offset:256" : "=&v"(yp[0][1]) : "v"(ydr) : "memory"); SB();
  asm volatile("ds_read_b128 %0, %1 offset:8192" : "=&v"(yp[0][2]) : "v"(ydr) : "memory"); SB();
  asm volatile("ds_read_b128 %0, %1 offset:8448" : "=&v"(yp[0][3]) : "v"(ydr) : "memory"); SB();
  asm volatile("ds_read_b128 %0, %1 offset:16384" : "=&v"(yp[1][0]) : "v"(ydr) : "memory"); SB();
  asm volatile("ds_read_b128 %0, %1 offset:16640" : "=&v"(yp[1][1]) : "v"(ydr) : "memory"); SB();
  asm volatile("ds_read_b128 %0, %1 offset:24576" : "=&v"(yp[1][2]) : "v"(ydr) : "memory"); SB();
  asm volatile("ds_read_b128 %0, %1 offset:24832" : "=&v"(yp[1][3]) : "v"(ydr) : "memory"); SB();
  asm volatile("buffer_load_dwordx4 v[212:215], %0, %1, %2 offen offset:1024 sc1" :  : "v"(vHr), "s"(rWS), "s"(hso[3]) : "memory"); SB();
  asm volatile("buffer_load_dwordx4 v[216:219], %0, %1, %2 offen offset:1024 sc1" :  : "v"(vHr), "s"(rWS), "s"(hso[5]) : "memory"); SB();
  asm volatile("buffer_load_dwordx4 v[220:223], %0, %1, %2 offen offset:1024 sc1" :  : "v"(vHr), "s"(rWS), "s"(hso[7]) : "memory"); SB();
  asm volatile("" : "+v"(yk[0]), "+v"(yk[1]) :  : ); SB();
  f32x2 yd[2][4];
#pragma unroll
  for (int n = 0; n < 8; ++n) { const float Dv = dD[n >> 2]; yd[n >> 2][n & 3] = unpk(yk[n >> 2][n & 3]) * (f32x2){Dv, Dv}; asm volatile("" : "+v"(yd[n >> 2][n & 3])); }
  SB();
  asm volatile("s_waitcnt lgkmcnt(0)" : "+v"(yp[0][0]), "+v"(yp[0][1]), "+v"(yp[0][2]), "+v"(yp[0][3]) :  : "memory"); SB();
  {
    const f32x4 s0 = yp[0][0] + yp[0][2], s1 = yp[0][1] + yp[0][3];
    yo[0] = (u32x4){pk2((f32x2){s0[0], s0[1]} + yd[0][0]), pk2((f32x2){s0[2], s0[3]} + yd[0][1]), pk2((f32x2){s1[0], s1[1]} + yd[0][2]), ~(nohole(pk2((f32x2){s1[2], s1[3]} + yd[0][3])) ^ yk[0][3])};
    SB();
  }
  asm volatile("buffer_store_dwordx4 %1, %0, %2, %3 offen offset:0 sc1\n\ts_nop 1" :  : "v"(vF), "v"(yo[0]), "s"(rWS), "s"(W_KV + 0) : "memory"); SB();
  asm volatile("" : "+v"(yp[1][0]), "+v"(yp[1][1]), "+v"(yp[1][2]), "+v"(yp[1][3]) :  : "memory"); SB();
  {
    const f32x4 s0 = yp[1][0] + yp[1][2], s1 = yp[1][1] + yp[1][3];
    yo[1] = (u32x4){pk2((f32x2){s0[0], s0[1]} + yd[1][0]), pk2((f32x2){s0[2], s0[3]} + yd[1][1]), pk2((f32x2){s1[0], s1[1]} + yd[1][2]), ~(nohole(pk2((f32x2){s1[2], s1[3]} + yd[1][3])) ^ yk[1][3])};
    SB();
  }
  asm volatile("buffer_store_dwordx4 %1, %0, %2, %3 offen offset:0 sc1\n\ts_nop 1" :  : "v"(vF), "v"(yo[1]), "s"(rWS), "s"(W_KV + 4096) : "memory"); SB();
  ST(19);
  ST(20);
  f32x2 xf[5][2], sv[5][2];
  float rd, mr;
  asm volatile("s_waitcnt vmcnt(13)" :  :  : "memory"); SB();
  asm volatile("v_mfma_f32_16x16x32_bf16 %0, a[160:163], v[164:167], 0" : "=&v"(o3[1][0]) :  : ); SB();
  asm volatile("buffer_load_dwordx4 v[208:211], %0, %1, %2 offen offset:1024 sc1" :  : "v"(vHr), "s"(rWS), "s"(hso[1]) : "memory"); SB();
  asm volatile("v_mfma_f32_16x16x32_bf16 %0, a[164:167], v[164:167], 0" : "=&v"(o3[1][1]) :  : ); SB();
  asm volatile("buffer_load_dwordx4 v[228:231], %0, %1, %2 offen offset:2048 sc1" :  : "v"(vHr), "s"(rWS), "s"(hso[2]) : "memory"); SB();
  asm volatile("v_mfma_f32_16x16x32_bf16 %0, a[168:171], v[164:167], 0" : "=&v"(o3[1][2]) :  : ); SB();
  asm volatile("v_mfma_f32_16x16x32_bf16 %0, a[172:175], v[164:167], 0" : "=&v"(o3[1][3]) :  : ); SB();
  asm volatile("buffer_load_dwordx4 v[232:235], %0, %1, %2 offen offset:2048 sc1" :  : "v"(vHr), "s"(rWS), "s"(hso[4]) : "memory"); SB();
  asm volatile("v_mfma_f32_16x16x32_bf16 %0, a[176:179], v[164:167], 0" : "=&v"(o3[1][4]) :  : ); SB();
  asm volatile("v_mfma_f32_16x16x32_bf16 %0, a[160:163], v[168:171], 0" : "=&v"(o3[2][0]) :  : ); SB();
  asm volatile("v_mfma_f32_16x16x32_bf16 %0, a[164:167], v[168:171], 0" : "=&v"(o3[2][1]) :  : ); SB();
  asm volatile("buffer_load_dwordx4 v[236:239], %0, %1, %2 offen offset:2048 sc1" :  : "v"(vHr), "s"(rWS), "s"(hso[6]) : "memory"); SB();
  asm volatile("v_mfma_f32_16x16x32_bf16 %0, a[168:171], v[168:171], 0" : "=&v"(o3[2][2]) :  : ); SB();
  asm volatile("v_mfma_f32_16x16x32_bf16 %0, a[172:175], v[168:171], 0" : "=&v"(o3[2][3]) :  : ); SB();
  asm volatile("v_mfma_f32_16x16x32_bf16 %0, a[176:179], v[168:171], 0" : "=&v"(o3[2][4]) :  : ); SB();
  asm volatile("v_mfma_f32_16x16x32_bf16 %0, a[160:163], v[172:175], 0" : "=&v"(o3[3][0]) :  : ); SB();
  asm volatile("buffer_load_dwordx4 v[224:227], %0, %1, %2 offen offset:2048 sc1" :  : "v"(vHr), "s"(rWS), "s"(hso[0]) : "memory"); SB();
  asm volatile("v_mfma_f32_16x16x32_bf16 %0, a[164:167], v[172:175], 0" : "=&v"(o3[3][1]) :  : ); SB();
  asm volatile("v_mfma_f32_16x16x32_bf16 %0, a[168:171], v[172:175], 0" : "=&v"(o3[3][2]) :  : ); SB();
  asm volatile("v_mfma_f32_16x16x32_bf16 %0, a[172:175], v[172:175], 0" : "=&v"(o3[3][3]) :  : ); SB();
  asm volatile("buffer_load_dwordx4 v[244:247], %0, %1, %2 offen offset:2048 sc1" :  : "v"(vHr), "s"(rWS), "s"(hso[3]) : "memory"); SB();
  asm volatile("v_mfma_f32_16x16x32_bf16 %0, a[176:179], v[172:175], 0" : "=&v"(o3[3][4]) :  : ); SB();
  asm volatile("s_waitcnt vmcnt(15)" :  :  : "memory"); SB();
  asm volatile("v_mfma_f32_16x16x32_bf16 %0, a[180:183], v[180:183], %0" : "+v"(o3[1][0]) :  : ); SB();
  asm volatile("v_rcp_f32 %0, %1" : "=v"(rd) : "v"(xd[0]) : ); SB();
  asm volatile("v_mfma_f32_16x16x32_bf16 %0, a[184:187], v[180:183], %0" : "+v"(o3[1][1]) :  : ); SB();
  asm volatile("v_mul_f32 %0, -%1, %2" : "=v"(mr) : "v"(xm[0]), "v"(rd) : ); SB();
  asm volatile("v_mfma_f32_16x16x32_bf16 %0, a[188:191], v[180:183], %0" : "+v"(o3[1][2]) :  : ); SB();
  asm volatile("buffer_load_dwordx4 v[248:251], %0, %1, %2 offen offset:2048 sc1" :  : "v"(vHr), "s"(rWS), "s"(hso[5]) : "memory"); SB();
  asm volatile("v_lshlrev_b32 %0, 16, %1" : "=v"(xf[0][0][0]) : "v"(xv[0][0]) : ); SB();
  asm volatile("v_mfma_f32_16x16x32_bf16 %0, a[192:195], v[180:183], %0" : "+v"(o3[1][3]) :  : ); SB();
  asm volatile("v_and_b32 %0, 0xffff0000, %1" : "=v"(xf[0][0][1]) : "v"(xv[0][0]) : ); SB();
  asm volatile("v_mfma_f32_16x16x32_bf16 %0, a[196:199], v[180:183], %0" : "+v"(o3[1][4]) :  : ); SB();
  asm volatile("v_lshlrev_b32 %0, 16, %1" : "=v"(xf[0][1][0]) : "v"(xv[0][1]) : ); SB();
  asm volatile("v_mfma_f32_16x16x32_bf16 %0, a[180:183], v[184:187], %0" : "+v"(o3[2][0]) :  : ); SB();
  asm volatile("buffer_load_dwordx4 v[252:255], %0, %1, %2 offen offset:2048 sc1" :  : "v"(vHr), "s"(rWS), "s"(hso[7]) : "memory"); SB();
  asm volatile("v_and_b32 %0, 0xffff0000, %1" : "=v"(xf[0][1][1]) : "v"(xv[0][1]) : ); SB();
  asm volatile("v_mfma_f32_16x16x32_bf16 %0, a[184:187], v[184:187], %0" : "+v"(o3[2][1]) :  : ); SB();
  asm volatile("v_lshlrev_b32 %0, 16, %1" : "=v"(xf[1][0][0]) : "v"(xv[1][0]) : ); SB();
  asm volatile("v_mfma_f32_16x16x32_bf16 %0, a[188:191], v[184:187], %0" : "+v"(o3[2][2]) :  : ); SB();
  asm volatile("v_and_b32 %0, 0xffff0000, %1" : "=v"(xf[1][0][1]) : "v"(xv[1][0]) : ); SB();
  asm volatile("v_mfma_f32_16x16x32_bf16 %0, a[192:195], v[184:187], %0" : "+v"(o3[2][3]) :  : ); SB();
  asm volatile("v_lshlrev_b32 %0, 16, %1" : "=v"(xf[1][1][0]) : "v"(xv[1][1]) : ); SB();
  asm volatile("v_mfma_f32_16x16x32_bf16 %0, a[196:199], v[184:187], %0" : "+v"(o3[2][4]) :  : ); SB();
  asm volatile("buffer_load_dwordx4 v[240:243], %0, %1, %2 offen offset:2048 sc1" :  : "v"(vHr), "s"(rWS), "s"(hso[1]) : "memory"); SB();
  asm volatile("v_and_b32 %0, 0xffff0000, %1" : "=v"(xf[1][1][1]) : "v"(xv[1][1]) : ); SB();
  asm volatile("v_mfma_f32_16x16x32_bf16 %0, a[180:183], v[188:191], %0" : "+v"(o3[3][0]) :  : ); SB();
  asm volatile("v_lshlrev_b32 %0, 16, %1" : "=v"(xf[2][0][0]) : "v"(xv[2][0]) : ); SB();
  asm volatile("v_mfma_f32_16x16x32_bf16 %0, a[184:187], v[188:191], %0" : "+v"(o3[3][1]) :  : ); SB();
  asm volatile("v_and_b32 %0, 0xffff0000, %1" : "=v"(xf[2][0][1]) : "v"(xv[2][0]) : ); SB();
  asm volatile("v_mfma_f32_16x16x32_bf16 %0, a[188:191], v[188:191], %0" : "+v"(o3[3][2]) :  : ); SB();
  asm volatile("v_lshlrev_b32 %0, 16, %1" : "=v"(xf[2][1][0]) : "v"(xv[2][1]) : ); SB();
  asm volatile("v_mfma_f32_16x16x32_bf16 %0, a[192:195], v[188:191], %0" : "+v"(o3[3][3]) :  : ); SB();
  asm volatile("v_and_b32 %0, 0xffff0000, %1" : "=v"(xf[2][1][1]) : "v"(xv[2][1]) : ); SB();
  asm volatile("v_mfma_f32_16x16x32_bf16 %0, a[196:199], v[188:191], %0" : "+v"(o3[3][4]) :  : ); SB();
  asm volatile("v_lshlrev_b32 %0, 16, %1" : "=v"(xf[3][0][0]) : "v"(xv[3][0]) : ); SB();
  asm volatile("s_waitcnt vmcnt(14)" :  :  : "memory"); SB();
  asm volatile("v_mfma_f32_16x16x32_bf16 %0, a[200:203], v[196:199], %0" : "+v"(o3[1][0]) :  : ); SB();
  asm volatile("v_and_b32 %0, 0xffff0000, %1" : "=v"(xf[3][0][1]) : "v"(xv[3][0]) : ); SB();
  asm volatile("v_mfma_f32_16x16x32_bf16 %0, a[204:207], v[196:199], %0" : "+v"(o3[1][1]) :  : ); SB();
  asm volatile("v_lshlrev_b32 %0, 16, %1" : "=v"(xf[3][1][0]) : "v"(xv[3][1]) : ); SB();
  asm volatile("v_mfma_f32_16x16x32_bf16 %0, a[208:211], v[196:199], %0" : "+v"(o3[1][2]) :  : ); SB();
  asm volatile("v_and_b32 %0, 0xffff0000, %1" : "=v"(xf[3][1][1]) : "v"(xv[3][1]) : ); SB();
  asm volatile("v_mfma_f32_16x16x32_bf16 %0, a[212:215], v[196:199], %0" : "+v"(o3[1][3]) :  : ); SB();
  asm volatile("v_lshlrev_b32 %0, 16, %1" : "=v"(xf[4][0][0]) : "v"(xv[4][0]) : ); SB();
  asm volatile("v_mfma_f32_16x16x32_bf16 %0, a[216:219], v[196:199], %0" : "+v"(o3[1][4]) :  : ); SB();
  asm volatile("v_and_b32 %0, 0xffff0000, %1" : "=v"(xf[4][0][1]) : "v"(xv[4][0]) : ); SB();
  asm volatile("v_mfma_f32_16x16x32_bf16 %0, a[200:203], v[200:203], %0" : "+v"(o3[2][0]) :  : ); SB();
  asm volatile("v_lshlrev_b32 %0, 16, %1" : "=v"(xf[4][1][0]) : "v"(xv[4][1]) : ); SB();
  asm volatile("v_mfma_f32_16x16x32_bf16 %0, a[204:207], v[200:203], %0" : "+v"(o3[2][1]) :  : ); SB();
  asm volatile("v_and_b32 %0, 0xffff0000, %1" : "=v"(xf[4][1][1]) : "v"(xv[4][1]) : ); SB();
  asm volatile("v_mfma_f32_16x16x32_bf16 %0, a[208:211], v[200:203], %0" : "+v"(o3[2][2]) :  : ); SB();
  asm volatile("v_fma_f32 %0, %1, %2, %3" : "=v"(sv[0][0][0]) : "v"(xf[0][0][0]), "v"(rd), "v"(mr) : ); SB();
  asm volatile("v_mfma_f32_16x16x32_bf16 %0, a[212:215], v[200:203], %0" : "+v"(o3[2][3]) :  : ); SB();
  asm volatile("v_fma_f32 %0, %1, %2, %3" : "=v"(sv[0][0][1]) : "v"(xf[0][0][1]), "v"(rd), "v"(mr) : ); SB();
  asm volatile("v_mfma_f32_16x16x32_bf16 %0, a[216:219], v[200:203], %0" : "+v"(o3[2][4]) :  : ); SB();
  asm volatile("v_fma_f32 %0, %1, %2, %3" : "=v"(sv[0][1][0]) : "v"(xf[0][1][0]), "v"(rd), "v"(mr) : ); SB();
  asm volatile("v_mfma_f32_16x16x32_bf16 %0, a[200:203], v[204:207], %0" : "+v"(o3[3][0]) :  : ); SB();
  asm volatile("v_fma_f32 %0, %1, %2, %3" : "=v"(sv[0][1][1]) : "v"(xf[0][1][1]), "v"(rd), "v"(mr) : ); SB();
  asm volatile("v_mfma_f32_16x16x32_bf16 %0, a[204:207], v[204:207], %0" : "+v"(o3[3][1]) :  : ); SB();
  asm volatile("v_fma_f32 %0, %1, %2, %3" : "=v"(sv[1][0][0]) : "v"(xf[1][0][0]), "v"(rd), "v"(mr) : ); SB();
  asm volatile("v_mfma_f32_16x16x32_bf16 %0, a[208:211], v[204:207], %0" : "+v"(o3[3][2]) :  : ); SB();
  asm volatile("v_fma_f32 %0, %1, %2, %3" : "=v"(sv[1][0][1]) : "v"(xf[1][0][1]), "v"(rd), "v"(mr) : ); SB();
  asm volatile("v_mfma_f32_16x16x32_bf16 %0, a[212:215], v[204:207], %0" : "+v"(o3[3][3]) :  : ); SB();
  asm volatile("v_fma_f32 %0, %1, %2, %3" : "=v"(sv[1][1][0]) : "v"(xf[1][1][0]), "v"(rd), "v"(mr) : ); SB();
  asm volatile("v_mfma_f32_16x16x32_bf16 %0, a[216:219], v[204:207], %0" : "+v"(o3[3][4]) :  : ); SB();
  asm volatile("v_fma_f32 %0, %1, %2, %3" : "=v"(sv[1][1][1]) : "v"(xf[1][1][1]), "v"(rd), "v"(mr) : ); SB();
  asm volatile("s_waitcnt vmcnt(8)" :  :  : "memory"); SB();
  asm volatile("v_mfma_f32_16x16x32_bf16 %0, a[220:223], v[212:215], %0" : "+v"(o3[1][0]) :  : ); SB();
  asm volatile("v_fma_f32 %0, %1, %2, %3" : "=v"(sv[2][0][0]) : "v"(xf[2][0][0]), "v"(rd), "v"(mr) : ); SB();
  asm volatile("v_mfma_f32_16x16x32_bf16 %0, a[224:227], v[212:215], %0" : "+v"(o3[1][1]) :  : ); SB();
  asm volatile("v_fma_f32 %0, %1, %2, %3" : "=v"(sv[2][0][1]) : "v"(xf[2][0][1]), "v"(rd), "v"(mr) : ); SB();
  asm volatile("v_mfma_f32_16x16x32_bf16 %0, a[228:231], v[212:215], %0" : "+v"(o3[1][2]) :  : ); SB();
  asm volatile("v_fma_f32 %0, %1, %2, %3" : "=v"(sv[2][1][0]) : "v"(xf[2][1][0]), "v"(rd), "v"(mr) : ); SB();
  asm volatile("v_mfma_f32_16x16x32_bf16 %0, a[232:235], v[212:215], %0" : "+v"(o3[1][3]) :  : ); SB();
  asm volatile("v_fma_f32 %0, %1, %2, %3" : "=v"(sv[2][1][1]) : "v"(xf[2][1][1]), "v"(rd), "v"(mr) : ); SB();
  asm volatile("v_mfma_f32_16x16x32_bf16 %0, a[236:239], v[212:215], %0" : "+v"(o3[1][4]) :  : ); SB();
  asm volatile("v_fma_f32 %0, %1, %2, %3" : "=v"(sv[3][0][0]) : "v"(xf[3][0][0]), "v"(rd), "v"(mr) : ); SB();
  asm volatile("v_mfma_f32_16x16x32_bf16 %0, a[220:223], v[216:219], %0" : "+v"(o3[2][0]) :  : ); SB();
  asm volatile("v_fma_f32 %0, %1, %2, %3" : "=v"(sv[3][0][1]) : "v"(xf[3][0][1]), "v"(rd), "v"(mr) : ); SB();
  asm volatile("v_mfma_f32_16x16x32_bf16 %0, a[224:227], v[216:219], %0" : "+v"(o3[2][1]) :  : ); SB();
  asm volatile("v_fma_f32 %0, %1, %2, %3" : "=v"(sv[3][1][0]) : "v"(xf[3][1][0]), "v"(rd), "v"(mr) : ); SB();
  asm volatile("v_mfma_f32_16x16x32_bf16 %0, a[228:231], v[216:219], %0" : "+v"(o3[2][2]) :  : ); SB();
  asm volatile("v_fma_f32 %0, %1, %2, %3" : "=v"(sv[3][1][1]) : "v"(xf[3][1][1]), "v"(rd), "v"(mr) : ); SB();
  asm volatile("v_mfma_f32_16x16x32_bf16 %0, a[232:235], v[216:219], %0" : "+v"(o3[2][3]) :  : ); SB();
  asm volatile("v_fma_f32 %0, %1, %2, %3" : "=v"(sv[4][0][0]) : "v"(xf[4][0][0]), "v"(rd), "v"(mr) : ); SB();
  asm volatile("v_mfma_f32_16x16x32_bf16 %0, a[236:239], v[216:219], %0" : "+v"(o3[2][4]) :  : ); SB();
  asm volatile("v_fma_f32 %0, %1, %2, %3" : "=v"(sv[4][0][1]) : "v"(xf[4][0][1]), "v"(rd), "v"(mr) : ); SB();
  asm volatile("v_mfma_f32_16x16x32_bf16 %0, a[220:223], v[220:223], %0" : "+v"(o3[3][0]) :  : ); SB();
  asm volatile("v_fma_f32 %0, %1, %2, %3" : "=v"(sv[4][1][0]) : "v"(xf[4][1][0]), "v"(rd), "v"(mr) : ); SB();
  asm volatile("v_mfma_f32_16x16x32_bf16 %0, a[224:227], v[220:223], %0" : "+v"(o3[3][1]) :  : ); SB();
  asm volatile("v_fma_f32 %0, %1, %2, %3" : "=v"(sv[4][1][1]) : "v"(xf[4][1][1]), "v"(rd), "v"(mr) : ); SB();
  asm volatile("v_mfma_f32_16x16x32_bf16 %0, a[228:231], v[220:223], %0" : "+v"(o3[3][2]) :  : ); SB();
  asm volatile("v_mfma_f32_16x16x32_bf16 %0, a[232:235], v[220:223], %0" : "+v"(o3[3][3]) :  : ); SB();
  asm volatile("v_mfma_f32_16x16x32_bf16 %0, a[236:239], v[220:223], %0" : "+v"(o3[3][4]) :  : ); SB();
  ST(30);
  asm volatile("s_waitcnt vmcnt(0)\n\t.Lc_%=:\n\tv_max3_u32 %1, v231, v235, v239\n\tv_max3_u32 %1, %1, v227, v247\n\tv_max3_u32 %1, %1, v251, v255\n\tv_max3_u32 %1, %1, v243, v243\n\tv_cmp_eq_u32 vcc, -1, %1\n\ts_cbranch_vccnz .Lr_%=\n\t.Ld_%=:\n\t.subsection 1\n\t.Lr_%=:\n\tv_add_u32 %0, -1, %0\n\ts_nop 1\n\tv_readfirstlane_b32 vcc_hi, %0\n\ts_nop 3\n\ts_cmp_lt_i32 vcc_hi, 1\n\ts_cbranch_scc1 .Ld_%=\n\ts_sleep 2\n\tbuffer_load_dwordx4 v[228:231], %2, %3, %6 offen offset:2048 sc1\n\tbuffer_load_dwordx4 v[232:235], %2, %3, %8 offen offset:2048 sc1\n\tbuffer_load_dwordx4 v[236:239], %2, %3, %10 offen offset:2048 sc1\n\tbuffer_load_dwordx4 v[224:227], %2, %3, %4 offen offset:2048 sc1\n\tbuffer_load_dwordx4 v[244:247], %2, %3, %7 offen offset:2048 sc1\n\tbuffer_load_dwordx4 v[248:251], %2, %3, %9 offen offset:2048 sc1\n\tbuffer_load_dwordx4 v[252:255], %2, %3, %11 offen offset:2048 sc1\n\tbuffer_load_dwordx4 v[240:243], %2, %3, %5 offen offset:2048 sc1\n\ts_waitcnt vmcnt(0)\n\ts_branch .Lc_%=\n\t.subsection 0" : "+v"(tmo), "=&v"(vtmp) : "v"(vHr), "s"(rWS), "s"(hso[0]), "s"(hso[1]), "s"(hso[2]), "s"(hso[3]), "s"(hso[4]), "s"(hso[5]), "s"(hso[6]), "s"(hso[7]) : "memory", "vcc", "scc"); SB();
  asm volatile("v_mfma_f32_16x16x32_bf16 %0, a[240:243], v[228:231], %0" : "+v"(o3[1][0]) :  : ); SB();
  asm volatile("v_mfma_f32_16x16x32_bf16 %0, a[244:247], v[228:231], %0" : "+v"(o3[1][1]) :  : ); SB();
  asm volatile("v_mfma_f32_16x16x32_bf16 %0, a[248:251], v[228:231], %0" : "+v"(o3[1][2]) :  : ); SB();
  asm volatile("v_mfma_f32_16x16x32_bf16 %0, a[252:255], v[228:231], %0" : "+v"(o3[1][3]) :  : ); SB();
  asm volatile("v_mfma_f32_16x16x32_bf16 %0, a[0:3], v[228:231], %0" : "+v"(o3[1][4]) :  : ); SB();
  asm volatile("v_mfma_f32_16x16x32_bf16 %0, a[4:7], v[244:247], %0" : "+v"(o3[1][0]) :  : ); SB();
  asm volatile("v_mfma_f32_16x16x32_bf16 %0, a[8:11], v[244:247], %0" : "+v"(o3[1][1]) :  : ); SB();
  asm volatile("v_mfma_f32_16x16x32_bf16 %0, a[12:15], v[244:247], %0" : "+v"(o3[1][2]) :  : ); SB();
  asm volatile("v_mfma_f32_16x16x32_bf16 %0, a[16:19], v[244:247], %0" : "+v"(o3[1][3]) :  : ); SB();
  asm volatile("v_mfma_f32_16x16x32_bf16 %0, a[20:23], v[244:247], %0" : "+v"(o3[1][4]) :  : ); SB();
  asm volatile("v_mfma_f32_16x16x32_bf16 %0, a[240:243], v[232:235], %0" : "+v"(o3[2][0]) :  : ); SB();
  asm volatile("ds_write_b128 %0, %1 offset:0" :  : "v"(pw3), "v"(o3[1][0]) : "memory"); SB();
  asm volatile("v_mfma_f32_16x16x32_bf16 %0, a[244:247], v[232:235], %0" : "+v"(o3[2][1]) :  : ); SB();
  asm volatile("v_mfma_f32_16x16x32_bf16 %0, a[248:251], v[232:235], %0" : "+v"(o3[2][2]) :  : ); SB();
  asm volatile("v_mfma_f32_16x16x32_bf16 %0, a[252:255], v[232:235], %0" : "+v"(o3[2][3]) :  : ); SB();
  asm volatile("ds_write_b128 %0, %1 offset:1024" :  : "v"(pw3), "v"(o3[1][1]) : "memory"); SB();
  asm volatile("v_mfma_f32_16x16x32_bf16 %0, a[0:3], v[232:235], %0" : "+v"(o3[2][4]) :  : ); SB();
  asm volatile("v_mfma_f32_16x16x32_bf16 %0, a[4:7], v[248:251], %0" : "+v"(o3[2][0]) :  : ); SB();
  asm volatile("buffer_load_dwordx4 v[164:167], %0, %1, %2 offen offset:0 sc1" :  : "v"(vKV), "s"(rWS), "s"(0u) : "memory"); SB();
  asm volatile("v_mfma_f32_16x16x32_bf16 %0, a[8:11], v[248:251], %0" : "+v"(o3[2][1]) :  : ); SB();
  asm volatile("buffer_load_dwordx4 v[168:171], %0, %1, %2 offen offset:0 sc1" :  : "v"(vKV), "s"(rWS), "s"(w3k[0]) : "memory"); SB();
  asm volatile("v_mfma_f32_16x16x32_bf16 %0, a[12:15], v[248:251], %0" : "+v"(o3[2][2]) :  : ); SB();
  asm volatile("buffer_load_dwordx4 v[172:175], %0, %1, %2 offen offset:0 sc1" :  : "v"(vKV), "s"(rWS), "s"(w3k[1]) : "memory"); SB();
  asm volatile("v_mfma_f32_16x16x32_bf16 %0, a[16:19], v[248:251], %0" : "+v"(o3[2][3]) :  : ); SB();
  asm volatile("ds_write_b128 %0, %1 offset:2048" :  : "v"(pw3), "v"(o3[1][2]) : "memory"); SB();
  asm volatile("v_mfma_f32_16x16x32_bf16 %0, a[20:23], v[248:251], %0" : "+v"(o3[2][4]) :  : ); SB();
  asm volatile("v_mfma_f32_16x16x32_bf16 %0, a[240:243], v[236:239], %0" : "+v"(o3[3][0]) :  : ); SB();
  asm volatile("v_mfma_f32_16x16x32_bf16 %0, a[244:247], v[236:239], %0" : "+v"(o3[3][1]) :  : ); SB();
  asm volatile("ds_write_b128 %0, %1 offset:3072" :  : "v"(pw3), "v"(o3[1][3]) : "memory"); SB();
  asm volatile("v_mfma_f32_16x16x32_bf16 %0, a[248:251], v[236:239], %0" : "+v"(o3[3][2]) :  : ); SB();
  asm volatile("v_mfma_f32_16x16x32_bf16 %0, a[252:255], v[236:239], %0" : "+v"(o3[3][3]) :  : ); SB();
  asm volatile("v_mfma_f32_16x16x32_bf16 %0, a[0:3], v[236:239], %0" : "+v"(o3[3][4]) :  : ); SB();
  asm volatile("ds_write_b128 %0, %1 offset:4096" :  : "v"(pw3), "v"(o3[1][4]) : "memory"); SB();
  asm volatile("v_mfma_f32_16x16x32_bf16 %0, a[4:7], v[252:255], %0" : "+v"(o3[3][0]) :  : ); SB();
  asm volatile("v_mfma_f32_16x16x32_bf16 %0, a[8:11], v[252:255], %0" : "+v"(o3[3][1]) :  : ); SB();
  asm volatile("v_mfma_f32_16x16x32_bf16 %0, a[12:15], v[252:255], %0" : "+v"(o3[3][2]) :  : ); SB();
  asm volatile("ds_write_b128 %0, %1 offset:5120" :  : "v"(pw3), "v"(o3[2][0]) : "memory"); SB();
  asm volatile("v_mfma_f32_16x16x32_bf16 %0, a[16:19], v[252:255], %0" : "+v"(o3[3][3]) :  : ); SB();
  asm volatile("v_mfma_f32_16x16x32_bf16 %0, a[20:23], v[252:255], %0" : "+v"(o3[3][4]) :  : ); SB();
  asm volatile("v_mfma_f32_16x16x32_bf16 %0, a[160:163], v[160:163], 0" : "=&v"(o3[0][0]) :  : ); SB();
  asm volatile("ds_write_b128 %0, %1 offset:6144" :  : "v"(pw3), "v"(o3[2][1]) : "memory"); SB();
  asm volatile("v_mfma_f32_16x16x32_bf16 %0, a[164:167], v[160:163], 0" : "=&v"(o3[0][1]) :  : ); SB();
  asm volatile("v_mfma_f32_16x16x32_bf16 %0, a[168:171], v[160:163], 0" : "=&v"(o3[0][2]) :  : ); SB();
  asm volatile("v_mfma_f32_16x16x32_bf16 %0, a[172:175], v[160:163], 0" : "=&v"(o3[0][3]) :  : ); SB();
  asm volatile("ds_write_b128 %0, %1 offset:7168" :  : "v"(pw3), "v"(o3[2][2]) : "memory"); SB();
  asm volatile("v_mfma_f32_16x16x32_bf16 %0, a[176:179], v[160:163], 0" : "=&v"(o3[0][4]) :  : ); SB();
  asm volatile("v_mfma_f32_16x16x32_bf16 %0, a[180:183], v[176:179], %0" : "+v"(o3[0][0]) :  : ); SB();
  asm volatile("v_mfma_f32_16x16x32_bf16 %0, a[184:187], v[176:179], %0" : "+v"(o3[0][1]) :  : ); SB();
  asm volatile("ds_write_b128 %0, %1 offset:8192" :  : "v"(pw3), "v"(o3[2][3]) : "memory"); SB();
  asm volatile("v_mfma_f32_16x16x32_bf16 %0, a[188:191], v[176:179], %0" : "+v"(o3[0][2]) :  : ); SB();
  asm volatile("v_mfma_f32_16x16x32_bf16 %0, a[192:195], v[176:179], %0" : "+v"(o3[0][3]) :  : ); SB();
  asm volatile("v_mfma_f32_16x16x32_bf16 %0, a[196:199], v[176:179], %0" : "+v"(o3[0][4]) :  : ); SB();
  asm volatile("ds_write_b128 %0, %1 offset:9216" :  : "v"(pw3), "v"(o3[2][4]) : "memory"); SB();
  asm volatile("v_mfma_f32_16x16x32_bf16 %0, a[200:203], v[192:195], %0" : "+v"(o3[0][0]) :  : ); SB();
  asm volatile("v_mfma_f32_16x16x32_bf16 %0, a[204:207], v[192:195], %0" : "+v"(o3[0][1]) :  : ); SB();
  asm volatile("v_mfma_f32_16x16x32_bf16 %0, a[208:211], v[192:195], %0" : "+v"(o3[0][2]) :  : ); SB();
  asm volatile("ds_write_b128 %0, %1 offset:10240" :  : "v"(pw3), "v"(o3[3][0]) : "memory"); SB();
  asm volatile("v_mfma_f32_16x16x32_bf16 %0, a[212:215], v[192:195], %0" : "+v"(o3[0][3]) :  : ); SB();
  asm volatile("v_mfma_f32_16x16x32_bf16 %0, a[216:219], v[192:195], %0" : "+v"(o3[0][4]) :  : ); SB();
  asm volatile("v_mfma_f32_16x16x32_bf16 %0, a[220:223], v[208:211], %0" : "+v"(o3[0][0]) :  : ); SB();
  asm volatile("ds_write_b128 %0, %1 offset:11264" :  : "v"(pw3), "v"(o3[3][1]) : "memory"); SB();
  asm volatile("v_mfma_f32_16x16x32_bf16 %0, a[224:227], v[208:211], %0" : "+v"(o3[0][1]) :  : ); SB();
  asm volatile("v_mfma_f32_16x16x32_bf16 %0, a[228:231], v[208:211], %0" : "+v"(o3[0][2]) :  : ); SB();
  asm volatile("v_mfma_f32_16x16x32_bf16 %0, a[232:235], v[208:211], %0" : "+v"(o3[0][3]) :  : ); SB();
  asm volatile("ds_write_b128 %0, %1 offset:12288" :  : "v"(pw3), "v"(o3[3][2]) : "memory"); SB();
  asm volatile("v_mfma_f32_16x16x32_bf16 %0, a[236:239], v[208:211], %0" : "+v"(o3[0][4]) :  : ); SB();
  asm volatile("v_mfma_f32_16x16x32_bf16 %0, a[240:243], v[224:227], %0" : "+v"(o3[0][0]) :  : ); SB();
  asm volatile("v_mfma_f32_16x16x32_bf16 %0, a[244:247], v[224:227], %0" : "+v"(o3[0][1]) :  : ); SB();
  asm volatile("ds_write_b128 %0, %1 offset:13312" :  : "v"(pw3), "v"(o3[3][3]) : "memory"); SB();
  asm volatile("v_mfma_f32_16x16x32_bf16 %0, a[248:251], v[224:227], %0" : "+v"(o3[0][2]) :  : ); SB();
  asm volatile("v_mfma_f32_16x16x32_bf16 %0, a[252:255], v[224:227], %0" : "+v"(o3[0][3]) :  : ); SB();
  asm volatile("v_mfma_f32_16x16x32_bf16 %0, a[0:3], v[224:227], %0" : "+v"(o3[0][4]) :  : ); SB();
  asm volatile("ds_write_b128 %0, %1 offset:14336" :  : "v"(pw3), "v"(o3[3][4]) : "memory"); SB();
  asm volatile("v_mfma_f32_16x16x32_bf16 %0, a[4:7], v[240:243], %0" : "+v"(o3[0][0]) :  : ); SB();
  asm volatile("v_mfma_f32_16x16x32_bf16 %0, a[8:11], v[240:243], %0" : "+v"(o3[0][1]) :  : ); SB();
  asm volatile("v_mfma_f32_16x16x32_bf16 %0, a[12:15], v[240:243], %0" : "+v"(o3[0][2]) :  : ); SB();
  asm volatile("v_mfma_f32_16x16x32_bf16 %0, a[16:19], v[240:243], %0" : "+v"(o3[0][3]) :  : ); SB();
  asm volatile("v_mfma_f32_16x16x32_bf16 %0, a[20:23], v[240:243], %0" : "+v"(o3[0][4]) :  : ); SB();
  asm volatile("s_waitcnt lgkmcnt(0)\n\ts_barrier" :  :  : "memory"); SB();
  asm volatile("ds_read_b128 %0, %1 offset:0" : "=&v"(o3[1][0]) : "v"(p3[0]) : "memory"); SB();
  asm volatile("ds_read_b128 %0, %1 offset:5120" : "=&v"(o3[2][0]) : "v"(p3[1]) : "memory"); SB();
  asm volatile("ds_read_b128 %0, %1 offset:10240" : "=&v"(o3[3][0]) : "v"(p3[2]) : "memory"); SB();
  asm volatile("ds_read_b128 %0, %1 offset:1024" : "=&v"(o3[1][1]) : "v"(p3[0]) : "memory"); SB();
  asm volatile("ds_read_b128 %0, %1 offset:6144" : "=&v"(o3[2][1]) : "v"(p3[1]) : "memory"); SB();
  asm volatile("ds_read_b128 %0, %1 offset:11264" : "=&v"(o3[3][1]) : "v"(p3[2]) : "memory"); SB();
  asm volatile("ds_read_b128 %0, %1 offset:2048" : "=&v"(o3[1][2]) : "v"(p3[0]) : "memory"); SB();
  asm volatile("ds_read_b128 %0, %1 offset:7168" : "=&v"(o3[2][2]) : "v"(p3[1]) : "memory"); SB();
  asm volatile("ds_read_b128 %0, %1 offset:12288" : "=&v"(o3[3][2]) : "v"(p3[2]) : "memory"); SB();
  asm volatile("ds_read_b128 %0, %1 offset:3072" : "=&v"(o3[1][3]) : "v"(p3[0]) : "memory"); SB();
  asm volatile("ds_read_b128 %0, %1 offset:8192" : "=&v"(o3[2][3]) : "v"(p3[1]) : "memory"); SB();
  asm volatile("ds_read_b128 %0, %1 offset:13312" : "=&v"(o3[3][3]) : "v"(p3[2]) : "memory"); SB();
  asm volatile("ds_read_b128 %0, %1 offset:4096" : "=&v"(o3[1][4]) : "v"(p3[0]) : "memory"); SB();
  asm volatile("ds_read_b128 %0, %1 offset:9216" : "=&v"(o3[2][4]) : "v"(p3[1]) : "memory"); SB();
  asm volatile("ds_read_b128 %0, %1 offset:14336" : "=&v"(o3[3][4]) : "v"(p3[2]) : "memory"); SB();
  ST(21);
  u32x2 yr[5];
  asm volatile("s_waitcnt lgkmcnt(9)" : "+v"(o3[0][0]), "+v"(o3[0][1]), "+v"(o3[1][0]), "+v"(o3[1][1]), "+v"(o3[2][0]), "+v"(o3[2][1]), "+v"(o3[3][0]), "+v"(o3[3][1]) :  : "memory"); SB();
#pragma unroll
  for (int j = 0; j <= 1; ++j) {
    const f32x4 o4 = (o3[0][j] + o3[1][j]) + (o3[2][j] + o3[3][j]);
#pragma unroll
    for (int h = 0; h < 2; ++h) {
      const f32x2 o = {o4[2 * h], o4[2 * h + 1]};
      const f32x2 a = o * (f32x2){-1.4426950f, -1.4426950f};
      const f32x2 e = (f32x2){__builtin_amdgcn_exp2f(a[0]), __builtin_amdgcn_exp2f(a[1])} + (f32x2){1.0f, 1.0f};
      const f32x2 r = {__builtin_amdgcn_rcpf(e[0]), __builtin_amdgcn_rcpf(e[1])};
      sv[j][h] = (sv[j][h] * o) * r;
    }
  }
  asm volatile("" : "+v"(sv[0][0]), "+v"(sv[0][1]), "+v"(sv[1][0]), "+v"(sv[1][1]));
  SB();
  asm volatile("s_waitcnt lgkmcnt(0)" : "+v"(o3[0][2]), "+v"(o3[0][3]), "+v"(o3[1][2]), "+v"(o3[1][3]), "+v"(o3[2][2]), "+v"(o3[2][3]), "+v"(o3[3][2]), "+v"(o3[3][3]) :  : "memory"); SB();
#pragma unroll
  for (int j = 2; j <= 3; ++j) {
    const f32x4 o4 = (o3[0][j] + o3[1][j]) + (o3[2][j] + o3[3][j]);
#pragma unroll
    for (int h = 0; h < 2; ++h) {
      const f32x2 o = {o4[2 * h], o4[2 * h + 1]};
      const f32x2 a = o * (f32x2){-1.4426950f, -1.4426950f};
      const f32x2 e = (f32x2){__builtin_amdgcn_exp2f(a[0]), __builtin_amdgcn_exp2f(a[1])} + (f32x2){1.0f, 1.0f};
      const f32x2 r = {__builtin_amdgcn_rcpf(e[0]), __builtin_amdgcn_rcpf(e[1])};
      sv[j][h] = (sv[j][h] * o) * r;
    }
  }
  asm volatile("" : "+v"(sv[2][0]), "+v"(sv[2][1]), "+v"(sv[3][0]), "+v"(sv[3][1]));
  SB();
  ST(22);
  asm volatile("s_waitcnt vmcnt(0)\n\t.Lc_%=:\n\tv_xnor_b32 v167, v167, %6\n\tv_xnor_b32 v171, v171, %7\n\tv_xnor_b32 v175, v175, %8\n\tv_max3_u32 %1, v167, v171, v175\n\tv_cmp_eq_u32 vcc, -1, %1\n\ts_cbranch_vccnz .Lr_%=\n\t.Ld_%=:\n\t.subsection 1\n\t.Lr_%=:\n\tv_add_u32 %0, -1, %0\n\ts_nop 1\n\tv_readfirstlane_b32 vcc_hi, %0\n\ts_nop 3\n\ts_cmp_lt_i32 vcc_hi, 1\n\ts_cbranch_scc1 .Ld_%=\n\ts_sleep 2\n\tbuffer_load_dwordx4 v[164:167], %2, %3, 0 offen sc1\n\tbuffer_load_dwordx4 v[168:171], %2, %3, %4 offen sc1\n\tbuffer_load_dwordx4 v[172:175], %2, %3, %5 offen sc1\n\ts_waitcnt vmcnt(0)\n\ts_branch .Lc_%=\n\t.subsection 0" : "+v"(tmo), "=&v"(vtmp) : "v"(vKV), "s"(rWS), "s"(w3k[0]), "s"(w3k[1]), "v"(kz[0]), "v"(kz[1]), "v"(kz[2]) : "memory", "vcc", "scc"); SB();
  ST(31);
  asm volatile("ds_write_b128 %0, v[164:167] offset:0" :  : "v"(yw) : "memory"); SB();
  asm volatile("ds_write_b128 %0, v[168:171] offset:1024" :  : "v"(yw) : "memory"); SB();
  asm volatile("ds_write_b128 %0, v[172:175] offset:2048" :  : "v"(yw) : "memory"); SB();
  asm volatile("s_waitcnt lgkmcnt(0)\n\ts_barrier" :  :  : "memory"); SB();
  asm volatile("ds_read_b64_tr_b16 %0, %1 offset:0" : "=&v"(yr[0]) : "v"(ya) : "memory"); SB();
  asm volatile("ds_read_b64_tr_b16 %0, %1 offset:2048" : "=&v"(yr[1]) : "v"(ya) : "memory"); SB();
  asm volatile("ds_read_b64_tr_b16 %0, %1 offset:4096" : "=&v"(yr[2]) : "v"(ya) : "memory"); SB();
  asm volatile("ds_read_b64_tr_b16 %0, %1 offset:6144" : "=&v"(yr[3]) : "v"(ya) : "memory"); SB();
  asm volatile("ds_read_b64_tr_b16 %0, %1 offset:8192" : "=&v"(yr[4]) : "v"(ya) : "memory"); SB();
  asm volatile("" : "+v"(o3[0][4]), "+v"(o3[1][4]), "+v"(o3[2][4]), "+v"(o3[3][4]) :  : "memory"); SB();
#pragma unroll
  for (int j = 4; j <= 4; ++j) {
    const f32x4 o4 = (o3[0][j] + o3[1][j]) + (o3[2][j] + o3[3][j]);
#pragma unroll
    for (int h = 0; h < 2; ++h) {
      const f32x2 o = {o4[2 * h], o4[2 * h + 1]};
      const f32x2 a = o * (f32x2){-1.4426950f, -1.4426950f};
      const f32x2 e = (f32x2){__builtin_amdgcn_exp2f(a[0]), __builtin_amdgcn_exp2f(a[1])} + (f32x2){1.0f, 1.0f};
      const f32x2 r = {__builtin_amdgcn_rcpf(e[0]), __builtin_amdgcn_rcpf(e[1])};
      sv[j][h] = (sv[j][h] * o) * r;
    }
  }
  asm volatile("" : "+v"(sv[4][0]), "+v"(sv[4][1]));
  SB();
  asm volatile("s_waitcnt lgkmcnt(0)" : "+v"(yr[0]), "+v"(yr[1]), "+v"(yr[2]), "+v"(yr[3]), "+v"(yr[4]) :  : "memory"); SB();
  {
    unsigned pk[5][2];
#pragma unroll
    for (int n = 0; n < 10; ++n) pk[n >> 1][n & 1] = pk2(__builtin_elementwise_fma(sv[n >> 1][n & 1], unpk(yr[n >> 1][n & 1]), xf[n >> 1][n & 1]));
#pragma unroll
    for (int q = 0; q < 2; ++q) {
      const auto s0 = __builtin_amdgcn_permlane16_swap(pk[2 * q][0], pk[2 * q + 1][0], false, false);
      const auto s1 = __builtin_amdgcn_permlane16_swap(pk[2 * q][1], pk[2 * q + 1][1], false, false);
      asm volatile("s_nop 1\n\tglobal_store_dwordx4 %0, %1, %2 offset:%3 " :: "v"(po), "v"((u32x4){s0[0], s1[0], s0[1], s1[1]}), "s"(out), "i"(64 * q) : "memory");
    }
    asm volatile("global_store_dwordx2 %0, %1, %2 offset:128 " :: "v"(vX), "v"((u32x2){pk[4][0], pk[4][1]}), "s"(out) : "memory");
  }
  ST(23);
  ST_OUT();
  }}}
}

extern "C" __global__ void k_end() { asm volatile(".subsection 2\n.Lcode_end:\n\ts_endpgm\n\t.subsection 0"); }

extern "C" void kernel_launch(void* const* d_in, const int* in_sizes, int n_in,
                              void* d_out, int out_size, void* d_ws, size_t ws_size,
                              hipStream_t stream) {
  (void)hipFuncSetAttribute((const void*)k_all, hipFuncAttributeMaxDynamicSharedMemorySize, 163840);
  k_all<<<256, 256, 163840, stream>>>((const char*)d_in[0], (const char*)d_in[1], (const char*)d_in[2], (const char*)d_in[3], (const char*)d_in[4],
                                      (const char*)d_in[5], (const char*)d_in[7], (const char*)d_in[8], (const char*)d_in[9], (const char*)d_in[10],
                                      (char*)d_out, (char*)d_ws);
}
```

```cpp
#include <hip/hip_runtime.h>
#include <stdint.h>

typedef unsigned short u16;
typedef __attribute__((ext_vector_type(4))) float f32x4;
typedef __attribute__((ext_vector_type(2))) float f32x2;
typedef __attribute__((ext_vector_type(4))) unsigned int u32x4;
typedef __attribute__((ext_vector_type(2))) unsigned int u32x2;
typedef __attribute__((ext_vector_type(2))) __bf16 bf16x2n;
typedef __attribute__((ext_vector_type(8))) unsigned long long u64x8;
typedef __attribute__((ext_vector_type(4))) unsigned long long u64x4;
#define LDS_AS __attribute__((address_space(3)))
#define GAS __attribute__((address_space(1)))
#define SB() __builtin_amdgcn_sched_barrier(0)
#define DI __device__ __forceinline__

#define SGA
#define ST_DECL do {} while (0)
#define ST(i) do {} while (0)
#define ST_OUT() do {} while (0)

constexpr int S_ = 2048, D_ = 640;
constexpr unsigned HOLE = 0xFFFFFFFFu;
constexpr int W_ST = 4096, W_FZ = 768 << 10, W_FK = W_FZ + 8192, W_FH = W_FK + 8192, W_KV = 1 << 20, W_Z2 = 4 << 20, W_HH = 11 << 20, W_WO = 14 << 20;

#define wm (w >> 1)
#define wn (w & 1)
#define g4 (lane >> 4)
#define l15 (lane & 15)
#define q4 ((lane >> 2) & 3)
#define p4 (lane & 3)
#define l16 (lane * 16)
#define nch (3 - (rb >> 4))
#define ch0 (cb * 80 + rb * 2 + min(rb, 16u))
#define c2o ((nch - 2) * 2)
#define c2s (8192u + ((rb >> 4) << 30))
#define w3s ((w + 1) >> 2 << 30)
#define vZ (W_Z2 + ((rb * 2 + wm) * 64 + cb * 6 + wn * 3) * 1024 + l16)
#define vH (W_HH + ((rb * 4 + w) * 24 + cb * 3) * 1024 + l16)

DI unsigned pack2(float lo, float hi) {
  f32x2 v = {lo, hi};
  return __builtin_bit_cast(unsigned, __builtin_convertvector(v, bf16x2n));
}
DI float lo16(unsigned w) { return __uint_as_float(w << 16); }
DI float hi16(unsigned w) { return __uint_as_float(w & 0xFFFF0000u); }
DI float rbf(float f) { return lo16(pack2(f, f)); }
DI f32x2 unpk(unsigned w) { f32x2 r = {lo16(w), hi16(w)}; return r; }
DI unsigned pk2(f32x2 v) { return pack2(v[0], v[1]); }
DI unsigned nohole(unsigned a) { return a < HOLE - 1 ? a : HOLE - 1; }
template <int CTRL> DI float dppf(float v) {
  return __builtin_bit_cast(float, __builtin_amdgcn_update_dpp(0, __builtin_bit_cast(int, v), CTRL, 0xF, 0xF, true));
}
DI float red16(float v) {
  const auto a = __builtin_amdgcn_permlane16_swap(__float_as_uint(v), __float_as_uint(v), false, false);
  return __uint_as_float(a[0]) + __uint_as_float(a[1]);
}
DI float red32(float v) {
  const auto c = __builtin_amdgcn_permlane32_swap(__float_as_uint(v), __float_as_uint(v), false, false);
  return __uint_as_float(c[0]) + __uint_as_float(c[1]);
}
DI float sel32(float a, float b) {
  const auto c = __builtin_amdgcn_permlane32_swap(__float_as_uint(a), __float_as_uint(b), false, false);
  return __uint_as_float(c[0]) + __uint_as_float(c[1]);
}
DI unsigned m10(unsigned a) { return a >= 10 ? a - 10 : a; }
DI unsigned lds_addr(const void* p) { return (unsigned)(uintptr_t)(LDS_AS const char*)p; }

__global__ __launch_bounds__(256, 1) __attribute__((amdgpu_num_vgpr(160))) SGA void k_all(
    const char*, const char*, const char*, const char*, const char*, const char*, const char*, const char*, const char*, const char*, char*, char*) {
  extern __shared__ __attribute__((aligned(16))) char smem[];
  ST_DECL;
  ST(0);
  u64x8 ka0; u64x4 ka1;
  asm volatile("s_load_dwordx16 %0, %2, 0x0\n\ts_load_dwordx8 %1, %2, 0x40" : "=&s"(ka0), "=&s"(ka1) : "s"(__builtin_amdgcn_kernarg_segment_ptr()) : "memory"); SB();
  const unsigned tid = threadIdx.x, lane0 = tid & 63, w0 = __builtin_amdgcn_readfirstlane(tid >> 6);
  const unsigned b = blockIdx.x, xcd = b & 7, idx = b >> 3;
  const unsigned rb0 = 8 * (xcd >> 1) + (idx >> 2), cb0 = 4 * (xcd & 1) + (idx & 3);
  const unsigned lb = lds_addr(smem);
  const unsigned lane = lane0, w = w0, rb = rb0, cb = cb0;
  {
    unsigned long long pc; unsigned clen;
    asm volatile("s_getpc_b64 %0\n.Lpc_here_%=:\n\ts_mov_b32 %1, .Lcode_end-.Lpc_here_%=" : "=s"(pc), "=s"(clen));
    const unsigned o = min((((lane & 3) * 4 + w) * 32 + idx) * 128, clen - 4u) & ~3u;
    asm volatile("s_mov_b64 exec, 15\n\tglobal_load_dword v254, %0, %1\n\ts_mov_b64 exec, -1" :: "v"(o), "s"(pc) : "memory");
    SB();
  }
  unsigned voA = (rb * 64 + w * 16) * 1280 + __umul24(lane >> 3, 1280) + (((lane & 7) ^ (lane >> 3)) << 4), voB[3];
#pragma unroll
  for (int i = 0; i < 3; ++i) { const unsigned r = __umul24(i * 64 + lane, 171) >> 11; voB[i] = w * 49152 + cb * 192 + __umul24(r, 2880) + ((lane ^ ((r >> 3) << 1)) << 4); }
  unsigned mX = lb + w * 2048, mW = lb + w * 3072, mA;
  const unsigned rot = m10((cb * 5 >> 2) + 2 + ((rb & 7) * 7 >> 3));
  unsigned kx[10], kw_[10], voA2 = voA + 9216;
  auto kofs = [&](int s) {
    asm volatile("s_add_u32 %0, %2, %3\n\ts_cmp_ge_u32 %0, 10\n\ts_cselect_b32 %1, 10, 0\n\ts_sub_u32 %0, %0, %1\n\ts_mul_i32 %1, %0, 0x30000\n\ts_lshl_b32 %0, %0, 7"
                 : "=&s"(kx[s]), "=&s"(kw_[s]) : "s"(rot), "i"(s) : "scc");
  };
  kofs(0); kofs(1);
  asm volatile("" : "+v"(voA), "+v"(voA2), "+v"(voB[0]), "+v"(voB[1]), "+v"(voB[2]));
  asm volatile("" : "+s"(mX), "+s"(mW)); SB();
  asm volatile("s_waitcnt lgkmcnt(0)" : "+s"(ka0), "+s"(ka1) :: "memory"); SB();
  const char *x = (const char*)ka0[0], *kin = (const char*)ka0[1], *vin = (const char*)ka0[2], *shift_k = (const char*)ka0[3], *ssm_k = (const char*)ka0[4];
  const char *ssm_D = (const char*)ka0[5], *w_in = (const char*)ka0[6], *w_sp = (const char*)ka0[7], *b_sp = (const char*)ka1[0], *w_out = (const char*)ka1[1];
  char *out = (char*)ka1[2], *ws = (char*)ka1[3];
  const auto rX = __builtin_amdgcn_make_buffer_rsrc((void*)x, 0, S_ * D_ * 2, 0x00020000);
  const auto rW = __builtin_amdgcn_make_buffer_rsrc((void*)(w_in + 1536), 0, 640 * 3072 - 1536, 0x00020000);
  asm volatile("; named registers" :  :  : "v255", "a255"); SB();
  asm volatile("s_add_u32 m0, %0, 81920" :  : "s"(mX) : "memory", "scc"); SB();
  asm volatile("s_nop 0\n\tbuffer_load_dwordx4 %0, %1, %2 offen lds" :  : "v"(voA), "s"(rX), "s"(kx[0]) : "memory"); SB();
  asm volatile("buffer_load_dwordx4 %0, %1, %2 offen offset:1024 lds" :  : "v"(voA2), "s"(rX), "s"(kx[0]) : "memory"); SB();
  asm volatile("s_add_u32 m0, %0, 0" :  : "s"(mW) : "memory", "scc"); SB();
  asm volatile("s_nop 0\n\tbuffer_load_dwordx4 %0, %1, %2 offen lds" :  : "v"(voB[0]), "s"(rW), "s"(kw_[0]) : "memory"); SB();
  asm volatile("buffer_load_dwordx4 %0, %1, %2 offen offset:1024 lds" :  : "v"(voB[1]), "s"(rW), "s"(kw_[0]) : "memory"); SB();
  asm volatile("buffer_load_dwordx4 %0, %1, %2 offen offset:2048 lds" :  : "v"(voB[2]), "s"(rW), "s"(kw_[0]) : "memory"); SB();
  asm volatile("s_add_u32 m0, %0, 90112" :  : "s"(mX) : "memory", "scc"); SB();
  asm volatile("s_nop 0\n\tbuffer_load_dwordx4 %0, %1, %2 offen lds" :  : "v"(voA), "s"(rX), "s"(kx[1]) : "memory"); SB();
  asm volatile("buffer_load_dwordx4 %0, %1, %2 offen offset:1024 lds" :  : "v"(voA2), "s"(rX), "s"(kx[1]) : "memory"); SB();
  kofs(2); kofs(3); kofs(4); kofs(5); kofs(6); kofs(7); kofs(8); kofs(9);
  const auto rWS = __builtin_amdgcn_make_buffer_rsrc((void*)ws, 0, 32 << 20, 0x00020000);
  const auto rW1 = __builtin_amdgcn_make_buffer_rsrc((void*)w_in, 0, 640 * 3072, 0x00020000);
  mA = lb + w * 8192; asm volatile("" : "+s"(mA));
  { unsigned lane = lane0, w = w0, rb = rb0, cb = cb0;
  asm volatile("" : "+v"(lane)); asm volatile("" : "+s"(w), "+s"(rb), "+s"(cb)); SB();

  unsigned ao[8];
  u32x2 kp[5], kc[5], vv[5], sw[5], wt[2], ko[5];
  u32x4 kq[3];
  f32x2 kt[2], kf[2];
  unsigned ku[5];
  unsigned aX[2][2], aW[2][2][3];
#pragma unroll
  for (int n = 0; n < 4; ++n) aX[n >> 1][n & 1] = lb + 98304 + (wm * 32 + ((n & 1) ^ wn) * 16 + l15) * 128 + (((((n >> 1) ^ wm) * 4 + g4) ^ (l15 & 7)) << 4);
#pragma unroll
  for (int n = 0; n < 6; ++n) {
    aW[0][n / 3][n % 3] = lb + ((n / 3 ^ wm) * 32 + 8 * g4 + q4) * 192 + (((wn * 3 + n % 3) ^ (g4 & 1)) << 5) + 8 * p4;
    aW[1][n / 3][n % 3] = aW[0][n / 3][n % 3] + 49152;
    asm volatile("" : "+v"(aW[0][n / 3][n % 3]), "+v"(aW[1][n / 3][n % 3]));
  }
  f32x4 co[3];
  u32x4 ones = {0x3F803F80u, 0x3F803F80u, 0x3F803F80u, 0x3F803F80u};
  f32x4 a2[2][3], a1[2][3], rs, gr, c2[3], c1[3];
  u32x2 zt[3][2];
  asm volatile("" : "+v"(ones), "+v"(aX[0][0]), "+v"(aX[0][1]), "+v"(aX[1][0]), "+v"(aX[1][1]));
  int tmo = (1 << 18);
  u32x2 fk, fz, fh, fi;
  unsigned tg0, tg1;
  float xm[2], xd[2], k1[2], k2[2];
  f32x4 xs[2];
  float xt[4];
  u32x2 xv[5];
  unsigned mU, uso, w3k[2], sFK, sFZ;
  float dD[3], bs;
  u32x4 im[6], zero4;
  SB();
  asm volatile("s_add_u32 m0, %0, 12288" :  : "s"(mW) : "memory", "scc"); SB();
  asm volatile("s_nop 0\n\tbuffer_load_dwordx4 %0, %1, %2 offen lds" :  : "v"(voB[0]), "s"(rW), "s"(kw_[1]) : "memory"); SB();
  asm volatile("buffer_load_dwordx4 %0, %1, %2 offen offset:1024 lds" :  : "v"(voB[1]), "s"(rW), "s"(kw_[1]) : "memory"); SB();
  asm volatile("buffer_load_dwordx4 %0, %1, %2 offen offset:2048 lds" :  : "v"(voB[2]), "s"(rW), "s"(kw_[1]) : "memory"); SB();
  const auto rK = __builtin_amdgcn_make_buffer_rsrc((void*)kin, 0, S_ * D_ * 2, 0x00020000);
  const auto rV = __builtin_amdgcn_make_buffer_rsrc((void*)vin, 0, S_ * D_ * 2, 0x00020000);
  const auto rS = __builtin_amdgcn_make_buffer_rsrc((void*)(shift_k - 3328), 0, D_ * 8 + 3328, 0x00020000);
  const auto rO = __builtin_amdgcn_make_buffer_rsrc((void*)(w_out - 3072), 0, 768 * 1280 + 3072, 0x00020000);
  unsigned voK[4], voV[3], voH = cb * 640 + l16;
#pragma unroll
  for (int n = 0; n < 4; ++n) { const unsigned o = n * 64 + lane, r = (o * 205) >> 11; voK[n] = (rb * 64 + w * 16 - 4) * 1280 + cb * 160 + r * 1120 + lane * 16; }
#pragma unroll
  for (int n = 0; n < 3; ++n) voV[n] = voK[n] + 5120;
  const unsigned wof = (rb >> 3) * 30 + min((rb & 7) * 4 + w, 29u), wok = (wof * 205) >> 10;
  unsigned voO = (lane >> 1) * 1280 + (lane & 1) * 16 + wok * 40960 + (wof - 5 * wok) * 32 + cb * 160, vWO = W_WO + (cb * 120 + wof) * 1024 + l16;
  asm volatile("" : "+v"(vWO), "+v"(voK[0]), "+v"(voK[1]), "+v"(voK[2]), "+v"(voK[3]), "+v"(voV[0]), "+v"(voV[1]), "+v"(voV[2]), "+v"(voH), "+v"(voO));
  SB();
  const auto rF = __builtin_amdgcn_make_buffer_rsrc((void*)ssm_k, 0, S_ * D_ * 2, 0x00020000);
  unsigned vF = ch0 * 4096 + w * 1024 + l16, c2s_ = c2s;
  unsigned hpo[3], vD = ch0 * 2, vD2 = vD + c2o * 2 - 4, vBs = (rb * 64 + w * 16 + l15) * 2;
  hpo[0] = w ? ch0 * 4096 + w * 1024 - 4 : 0x40000000u; hpo[1] = hpo[0] + 4096; hpo[2] = hpo[0] + c2s;
  asm volatile("" : "+s"(hpo[0]), "+s"(hpo[1]), "+s"(hpo[2]), "+v"(vD), "+v"(vD2), "+v"(vBs), "+v"(vF), "+s"(c2s_));
  SB();
  unsigned ka = lb + 98304 + w * 8192 + (4 * p4 + q4) * 160 + 8 * g4, oa = lb + 98304 + 7168 + w * 8192 + (4 * g4 + q4) * 32 + 8 * p4;
  unsigned sa = lb + 98304 + 3328 + w * 8192 + (4 * g4 + p4) * 8;
  unsigned kw = lb + 12288 + (4 * g4 + p4) * 144 + w * 32 + q4 * 8, kr = lb + 12288 + (w * 24 + (lane >> 3)) * 144 + (lane & 7) * 16;
  unsigned vKV = W_KV + (cb * 80 + w * 24 + (lane >> 3)) * 4096 + rb * 128 + (lane & 7) * 16;
  unsigned vS = W_ST + (rb * 2 + wm) * 8192 + wn * 512 + (lane >> 5) * 512 + l16;
  asm volatile("" : "+v"(ka), "+v"(oa), "+v"(sa), "+v"(kw), "+v"(kr), "+v"(vKV), "+v"(vS));
  SB();
  ST(1);
  SB();
  asm volatile("s_add_u32 m0, %0, 98304" :  : "s"(mX) : "memory", "scc"); SB();
  asm volatile("s_nop 0\n\tbuffer_load_dwordx4 %0, %1, %2 offen lds" :  : "v"(voA), "s"(rX), "s"(kx[2]) : "memory"); SB();
  asm volatile("buffer_load_dwordx4 %0, %1, %2 offen offset:1024 lds" :  : "v"(voA2), "s"(rX), "s"(kx[2]) : "memory"); SB();
  asm volatile("buffer_load_dwordx2 v[252:253], off, %0, 0 sc1" :  : "s"(rWS) : "memory"); SB();
  asm volatile("s_add_u32 m0, %0, 106496" :  : "s"(mX) : "memory", "scc"); SB();
  asm volatile("s_nop 0\n\tbuffer_load_dwordx4 %0, %1, %2 offen lds" :  : "v"(voA), "s"(rX), "s"(kx[3]) : "memory"); SB();
  asm volatile("buffer_load_dwordx4 %0, %1, %2 offen offset:1024 lds" :  : "v"(voA2), "s"(rX), "s"(kx[3]) : "memory"); SB();
  asm volatile("s_add_u32 m0, %0, 24576" :  : "s"(mW) : "memory", "scc"); SB();
  asm volatile("s_nop 0\n\tbuffer_load_dwordx4 %0, %1, %2 offen lds" :  : "v"(voB[0]), "s"(rW), "s"(kw_[2]) : "memory"); SB();
  asm volatile("s_waitcnt vmcnt(11) lgkmcnt(0)\n\ts_barrier" :  :  : "memory"); SB();
  ST(4);
  asm volatile("ds_read_b128 a[0:3], %0 offset:0" :  : "v"((aX[0][0] - 16384)) : "memory"); SB();
  asm volatile("ds_read_b128 a[4:7], %0 offset:0" :  : "v"((aX[0][1] - 16384)) : "memory"); SB();
  asm volatile("ds_read_b64_tr_b16 v[160:161], %0 offset:0" :  : "v"(aW[0][0][0]) : "memory"); SB();
  asm volatile("ds_read_b64_tr_b16 v[162:163], %0 offset:768" :  : "v"(aW[0][0][0]) : "memory"); SB();
  asm volatile("ds_read_b64_tr_b16 v[164:165], %0 offset:0" :  : "v"(aW[0][0][1]) : "memory"); SB();
  asm volatile("ds_read_b64_tr_b16 v[166:167], %0 offset:768" :  : "v"(aW[0][0][1]) : "memory"); SB();
  asm volatile("ds_read_b64_tr_b16 v[168:169], %0 offset:0" :  : "v"(aW[0][0][2]) : "memory"); SB();
  asm volatile("ds_read_b64_tr_b16 v[170:171], %0 offset:768" :  : "v"(aW[0][0][2]) : "memory"); SB();
  asm volatile("ds_read_b128 a[8:11], %0 offset:0" :  : "v"((aX[1][0] - 16384)) : "memory"); SB();
  asm volatile("ds_read_b128 a[12:15], %0 offset:0" :  : "v"((aX[1][1] - 16384)) : "memory"); SB();
  asm volatile("s_waitcnt lgkmcnt(6)\n\tv_mfma_f32_16x16x32_bf16 %0, v[160:163], a[0:3], 0" : "=&v"(a2[0][0]) :  : ); SB();
  asm volatile("ds_read_b64_tr_b16 v[172:173], %0 offset:0" :  : "v"(aW[0][1][0]) : "memory"); SB();
  asm volatile("buffer_load_dwordx4 %0, %1, %2 offen offset:1024 lds" :  : "v"(voB[1]), "s"(rW), "s"(kw_[2]) : "memory"); SB();
  asm volatile("v_mfma_f32_16x16x32_bf16 %0, v[160:163], a[4:7], 0" : "=&v"(a2[1][0]) :  : ); SB();
  asm volatile("ds_read_b64_tr_b16 v[174:175], %0 offset:768" :  : "v"(aW[0][1][0]) : "memory"); SB();
  asm volatile("buffer_load_dwordx4 %0, %1, %2 offen offset:2048 lds" :  : "v"(voB[2]), "s"(rW), "s"(kw_[2]) : "memory"); SB();
  asm volatile("v_mfma_f32_16x16x32_bf16 %0, v[160:163], %1, 0" : "=&v"(c2[0]) : "v"(ones) : ); SB();
  asm volatile("ds_read_b64_tr_b16 v[176:177], %0 offset:0" :  : "v"(aW[0][1][1]) : "memory"); SB();
  asm volatile("s_waitcnt lgkmcnt(5)\n\tv_mfma_f32_16x16x32_bf16 %0, v[164:167], a[0:3], 0" : "=&v"(a2[0][1]) :  : ); SB();
  asm volatile("ds_read_b64_tr_b16 v[178:179], %0 offset:768" :  : "v"(aW[0][1][1]) : "memory"); SB();
  asm volatile("v_mfma_f32_16x16x32_bf16 %0, v[164:167], a[4:7], 0" : "=&v"(a2[1][1]) :  : ); SB();
  asm volatile("ds_read_b64_tr_b16 v[180:181], %0 offset:0" :  : "v"(aW[0][1][2]) : "memory"); SB();
  asm volatile("s_add_u32 m0, %0, 114688" :  : "s"(mX) : "memory", "scc"); SB();
  asm volatile("v_mfma_f32_16x16x32_bf16 %0, v[164:167], %1, 0" : "=&v"(c2[1]) : "v"(ones) : ); SB();
  asm volatile("ds_read_b64_tr_b16 v[182:183], %0 offset:768" :  : "v"(aW[0][1][2]) : "memory"); SB();
  asm volatile("buffer_load_dwordx4 %0, %1, %2 offen lds" :  : "v"(voA), "s"(rX), "s"(kx[4]) : "memory"); SB();
  asm volatile("v_mfma_f32_16x16x32_bf16 %0, v[168:171], a[0:3], 0" : "=&v"(a2[0][2]) :  : ); SB();
  unsigned aV[5];
#pragma unroll
  for (int j = 0; j < 5; ++j) {
    const unsigned c = cb * 10 + 2 * j, t = 0 ? (c >> 3) ^ (c < 64 ? rot : rot & 1) : m10((c >> 3) + 10 - rot);
    aV[j] = lb + 81920 + t * 8192 + w * 2048 + l15 * 128 + ((((c & 7) + (g4 >> 1)) ^ (l15 & 7)) << 4) + (g4 & 1) * 8;
    asm volatile("" : "+v"(aV[j]));
  }
  SB();
  asm volatile("buffer_load_dwordx4 %0, %1, %2 offen offset:1024 lds" :  : "v"(voA2), "s"(rX), "s"(kx[4]) : "memory"); SB();
  asm volatile("s_add_u32 m0, %0, 122880" :  : "s"(mX) : "memory", "scc"); SB();
  asm volatile("s_nop 0\n\tbuffer_load_dwordx4 %0, %1, %2 offen lds" :  : "v"(voA), "s"(rX), "s"(kx[5]) : "memory"); SB();
  asm volatile("s_waitcnt vmcnt(11)\n\ts_barrier" :  :  : "memory"); SB();
  asm volatile("ds_read_b64_tr_b16 v[184:185], %0 offset:12288" :  : "v"(aW[0][0][0]) : "memory"); SB();
  asm volatile("v_mfma_f32_16x16x32_bf16 %0, v[168:171], a[4:7], 0" : "=&v"(a2[1][2]) :  : ); SB();
  asm volatile("ds_read_b64_tr_b16 v[186:187], %0 offset:13056" :  : "v"(aW[0][0][0]) : "memory"); SB();
  asm volatile("v_mfma_f32_16x16x32_bf16 %0, v[168:171], %1, 0" : "=&v"(c2[2]) : "v"(ones) : ); SB();
  asm volatile("ds_read_b64_tr_b16 v[188:189], %0 offset:12288" :  : "v"(aW[0][0][1]) : "memory"); SB();
  asm volatile("v_mfma_f32_16x16x32_bf16 %0, %1, a[0:3], 0" : "=&v"(rs) : "v"(ones) : ); SB();
  asm volatile("ds_read_b128 a[16:19], %0 offset:8192" :  : "v"((aX[0][0] - 16384)) : "memory"); SB();
  asm volatile("ds_read_b128 a[20:23], %0 offset:8192" :  : "v"((aX[0][1] - 16384)) : "memory"); SB();
  asm volatile("ds_read_b64_tr_b16 v[190:191], %0 offset:13056" :  : "v"(aW[0][0][1]) : "memory"); SB();
  asm volatile("buffer_load_dwordx4 %0, %1, %2 offen offset:1024 lds" :  : "v"(voA2), "s"(rX), "s"(kx[5]) : "memory"); SB();
  asm volatile("v_mfma_f32_16x16x32_bf16 %0, a[0:3], a[0:3], 0" : "=&v"(gr) :  : ); SB();
  asm volatile("ds_read_b64_tr_b16 v[192:193], %0 offset:12288" :  : "v"(aW[0][0][2]) : "memory"); SB();
  asm volatile("s_waitcnt lgkmcnt(7)\n\tv_mfma_f32_16x16x32_bf16 %0, v[172:175], a[8:11], %0" : "+v"(a2[0][0]) :  : ); SB();
  asm volatile("ds_read_b64_tr_b16 v[194:195], %0 offset:13056" :  : "v"(aW[0][0][2]) : "memory"); SB();
  asm volatile("s_add_u32 m0, %0, 36864" :  : "s"(mW) : "memory", "scc"); SB();
  asm volatile("v_mfma_f32_16x16x32_bf16 %0, v[172:175], a[12:15], %0" : "+v"(a2[1][0]) :  : ); SB();
  asm volatile("ds_read_b128 a[24:27], %0 offset:8192" :  : "v"((aX[1][0] - 16384)) : "memory"); SB();
  asm volatile("v_mfma_f32_16x16x32_bf16 %0, v[176:179], a[8:11], %0" : "+v"(a2[0][1]) :  : ); SB();
  asm volatile("ds_read_b128 a[28:31], %0 offset:8192" :  : "v"((aX[1][1] - 16384)) : "memory"); SB();
  asm volatile("v_mfma_f32_16x16x32_bf16 %0, v[176:179], a[12:15], %0" : "+v"(a2[1][1]) :  : ); SB();
  asm volatile("buffer_load_dwordx4 %0, %1, %2 offen lds" :  : "v"(voB[0]), "s"(rW), "s"(kw_[3]) : "memory"); SB();
  asm volatile("v_mfma_f32_16x16x32_bf16 %0, v[180:183], a[8:11], %0" : "+v"(a2[0][2]) :  : ); SB();
  asm volatile("ds_read_b64_tr_b16 v[160:161], %0 offset:12288" :  : "v"(aW[0][1][0]) : "memory"); SB();
  asm volatile("v_mfma_f32_16x16x32_bf16 %0, v[180:183], a[12:15], %0" : "+v"(a2[1][2]) :  : ); SB();
  asm volatile("ds_read_b64_tr_b16 v[162:163], %0 offset:13056" :  : "v"(aW[0][1][0]) : "memory"); SB();
  asm volatile("v_mfma_f32_16x16x32_bf16 %0, %1, a[8:11], %0" : "+v"(rs) : "v"(ones) : ); SB();
  asm volatile("ds_read_b64_tr_b16 v[164:165], %0 offset:12288" :  : "v"(aW[0][1][1]) : "memory"); SB();
  asm volatile("v_mfma_f32_16x16x32_bf16 %0, a[8:11], a[8:11], %0" : "+v"(gr) :  : ); SB();
  asm volatile("ds_read_b64_tr_b16 v[166:167], %0 offset:13056" :  : "v"(aW[0][1][1]) : "memory"); SB();
  asm volatile("s_waitcnt lgkmcnt(8)\n\tv_mfma_f32_16x16x32_bf16 %0, v[184:187], a[16:19], %0" : "+v"(a2[0][0]) :  : ); SB();
  asm volatile("ds_read_b64_tr_b16 v[168:169], %0 offset:12288" :  : "v"(aW[0][1][2]) : "memory"); SB();
  asm volatile("v_mfma_f32_16x16x32_bf16 %0, v[184:187], a[20:23], %0" : "+v"(a2[1][0]) :  : ); SB();
  asm volatile("ds_read_b64_tr_b16 v[170:171], %0 offset:13056" :  : "v"(aW[0][1][2]) : "memory"); SB();
  asm volatile("v_mfma_f32_16x16x32_bf16 %0, v[184:187], %1, %0" : "+v"(c2[0]) : "v"(ones) : ); SB();
  asm volatile("buffer_load_dwordx4 %0, %1, %2 offen offset:1024 lds" :  : "v"(voB[1]), "s"(rW), "s"(kw_[3]) : "memory"); SB();
  asm volatile("v_mfma_f32_16x16x32_bf16 %0, v[188:191], a[16:19], %0" : "+v"(a2[0][1]) :  : ); SB();
  asm volatile("buffer_load_dwordx4 %0, %1, %2 offen offset:2048 lds" :  : "v"(voB[2]), "s"(rW), "s"(kw_[3]) : "memory"); SB();
  asm volatile("v_mfma_f32_16x16x32_bf16 %0, v[188:191], a[20:23], %0" : "+v"(a2[1][1]) :  : ); SB();
  asm volatile("s_add_u32 m0, %0, 131072" :  : "s"(mX) : "memory", "scc"); SB();
  asm volatile("s_nop 0\n\tbuffer_load_dwordx4 %0, %1, %2 offen lds" :  : "v"(voA), "s"(rX), "s"(kx[6]) : "memory"); SB();
  asm volatile("buffer_load_dwordx4 %0, %1, %2 offen offset:1024 lds" :  : "v"(voA2), "s"(rX), "s"(kx[6]) : "memory"); SB();
  asm volatile("s_add_u32 m0, %0, 139264" :  : "s"(mX) : "memory", "scc"); SB();
  asm volatile("s_nop 0\n\tbuffer_load_dwordx4 %0, %1, %2 offen lds" :  : "v"(voA), "s"(rX), "s"(kx[7]) : "memory"); SB();
  asm volatile("s_waitcnt vmcnt(10)\n\ts_barrier" :  :  : "memory"); SB();
  asm volatile("ds_read_b64_tr_b16 v[172:173], %0 offset:24576" :  : "v"(aW[0][0][0]) : "memory"); SB();
  asm volatile("v_mfma_f32_16x16x32_bf16 %0, v[188:191], %1, %0" : "+v"(c2[1]) : "v"(ones) : ); SB();
  asm volatile("ds_read_b64_tr_b16 v[174:175], %0 offset:25344" :  : "v"(aW[0][0][0]) : "memory"); SB();
  asm volatile("s_waitcnt lgkmcnt(2)\n\tv_mfma_f32_16x16x32_bf16 %0, v[192:195], a[16:19], %0" : "+v"(a2[0][2]) :  : ); SB();
  asm volatile("ds_read_b64_tr_b16 v[176:177], %0 offset:24576" :  : "v"(aW[0][0][1]) : "memory"); SB();
  asm volatile("v_mfma_f32_16x16x32_bf16 %0, v[192:195], a[20:23], %0" : "+v"(a2[1][2]) :  : ); SB();
  asm volatile("ds_read_b64_tr_b16 v[178:179], %0 offset:25344" :  : "v"(aW[0][0][1]) : "memory"); SB();
  asm volatile("v_mfma_f32_16x16x32_bf16 %0, v[192:195], %1, %0" : "+v"(c2[2]) : "v"(ones) : ); SB();
  asm volatile("ds_read_b64_tr_b16 v[180:181], %0 offset:24576" :  : "v"(aW[0][0][2]) : "memory"); SB();
  asm volatile("v_mfma_f32_16x16x32_bf16 %0, %1, a[16:19], %0" : "+v"(rs) : "v"(ones) : ); SB();
  asm volatile("ds_read_b128 a[32:35], %0 offset:0" :  : "v"(aX[0][0]) : "memory"); SB();
  asm volatile("ds_read_b128 a[36:39], %0 offset:0" :  : "v"(aX[0][1]) : "memory"); SB();
  asm volatile("ds_read_b64_tr_b16 v[182:183], %0 offset:25344" :  : "v"(aW[0][0][2]) : "memory"); SB();
  asm volatile("v_mfma_f32_16x16x32_bf16 %0, a[16:19], a[16:19], %0" : "+v"(gr) :  : ); SB();
  asm volatile("ds_read_b128 a[40:43], %0 offset:0" :  : "v"(aX[1][0]) : "memory"); SB();
  asm volatile("v_mfma_f32_16x16x32_bf16 %0, v[160:163], a[24:27], %0" : "+v"(a2[0][0]) :  : ); SB();
  asm volatile("ds_read_b128 a[44:47], %0 offset:0" :  : "v"(aX[1][1]) : "memory"); SB();
  asm volatile("v_mfma_f32_16x16x32_bf16 %0, v[160:163], a[28:31], %0" : "+v"(a2[1][0]) :  : ); SB();
  asm volatile("ds_read_b128 a[48:51], %0 offset:8192" :  : "v"(aX[0][0]) : "memory"); SB();
  asm volatile("v_mfma_f32_16x16x32_bf16 %0, v[164:167], a[24:27], %0" : "+v"(a2[0][1]) :  : ); SB();
  asm volatile("ds_read_b128 a[52:55], %0 offset:8192" :  : "v"(aX[0][1]) : "memory"); SB();
  asm volatile("v_mfma_f32_16x16x32_bf16 %0, v[164:167], a[28:31], %0" : "+v"(a2[1][1]) :  : ); SB();
  asm volatile("ds_read_b128 a[56:59], %0 offset:8192" :  : "v"(aX[1][0]) : "memory"); SB();
  asm volatile("v_mfma_f32_16x16x32_bf16 %0, v[168:171], a[24:27], %0" : "+v"(a2[0][2]) :  : ); SB();
  asm volatile("ds_read_b128 a[60:63], %0 offset:8192" :  : "v"(aX[1][1]) : "memory"); SB();
  asm volatile("v_mfma_f32_16x16x32_bf16 %0, v[168:171], a[28:31], %0" : "+v"(a2[1][2]) :  : ); SB();
  asm volatile("buffer_load_dwordx4 %0, %1, %2 offen offset:1024 lds" :  : "v"(voA2), "s"(rX), "s"(kx[7]) : "memory"); SB();
  asm volatile("s_add_u32 m0, %0, 49152" :  : "s"(mW) : "memory", "scc"); SB();
  asm volatile("v_mfma_f32_16x16x32_bf16 %0, %1, a[24:27], %0" : "+v"(rs) : "v"(ones) : ); SB();
  asm volatile("ds_read_b64_tr_b16 v[184:185], %0 offset:24576" :  : "v"(aW[0][1][0]) : "memory"); SB();
  asm volatile("v_mfma_f32_16x16x32_bf16 %0, a[24:27], a[24:27], %0" : "+v"(gr) :  : ); SB();
  asm volatile("ds_read_b64_tr_b16 v[186:187], %0 offset:25344" :  : "v"(aW[0][1][0]) : "memory"); SB();
  asm volatile("s_waitcnt lgkmcnt(9)\n\tv_mfma_f32_16x16x32_bf16 %0, v[172:175], a[32:35], %0" : "+v"(a2[0][0]) :  : ); SB();
  asm volatile("ds_read_b64_tr_b16 v[188:189], %0 offset:24576" :  : "v"(aW[0][1][1]) : "memory"); SB();
  asm volatile("v_mfma_f32_16x16x32_bf16 %0, v[172:175], a[36:39], %0" : "+v"(a2[1][0]) :  : ); SB();
  asm volatile("ds_read_b64_tr_b16 v[190:191], %0 offset:25344" :  : "v"(aW[0][1][1]) : "memory"); SB();
  asm volatile("v_mfma_f32_16x16x32_bf16 %0, v[172:175], %1, %0" : "+v"(c2[0]) : "v"(ones) : ); SB();
  asm volatile("ds_read_b64_tr_b16 v[192:193], %0 offset:24576" :  : "v"(aW[0][1][2]) : "memory"); SB();
  asm volatile("buffer_load_dwordx4 %0, %1, %2 offen lds" :  : "v"(voB[0]), "s"(rW), "s"(kw_[4]) : "memory"); SB();
  asm volatile("v_mfma_f32_16x16x32_bf16 %0, v[176:179], a[32:35], %0" : "+v"(a2[0][1]) :  : ); SB();
  asm volatile("ds_read_b64_tr_b16 v[194:195], %0 offset:25344" :  : "v"(aW[0][1][2]) : "memory"); SB();
  asm volatile("v_mfma_f32_16x16x32_bf16 %0, v[176:179], a[36:39], %0" : "+v"(a2[1][1]) :  : ); SB();
  asm volatile("buffer_load_dwordx4 %0, %1, %2 offen offset:1024 lds" :  : "v"(voB[1]), "s"(rW), "s"(kw_[4]) : "memory"); SB();
  asm volatile("v_mfma_f32_16x16x32_bf16 %0, v[176:179], %1, %0" : "+v"(c2[1]) : "v"(ones) : ); SB();
  asm volatile("s_waitcnt lgkmcnt(7)\n\tv_mfma_f32_16x16x32_bf16 %0, v[180:183], a[32:35], %0" : "+v"(a2[0][2]) :  : ); SB();
  asm volatile("v_mfma_f32_16x16x32_bf16 %0, v[180:183], a[36:39], %0" : "+v"(a2[1][2]) :  : ); SB();
  asm volatile("buffer_load_dwordx4 %0, %1, %2 offen offset:2048 lds" :  : "v"(voB[2]), "s"(rW), "s"(kw_[4]) : "memory"); SB();
  asm volatile("v_mfma_f32_16x16x32_bf16 %0, v[180:183], %1, %0" : "+v"(c2[2]) : "v"(ones) : ); SB();
  const auto rA = __builtin_amdgcn_make_buffer_rsrc((void*)(w_sp - 3072), 0, S_ * S_ * 2 + 3072, 0x00020000);
  unsigned ca = lb + 98304 + 6912 + w * 8192 + g4 * 16, cr = lb + 98304 + 6912 + (w ^ 2) * 8192 + g4 * 16;
  unsigned voS = (rb * 64 + (lane >> 3)) * 4096 + w * 1024 + (cb & 5) * 128 + (((lane & 7) ^ (lane >> 3)) << 4), aA[2];
#pragma unroll
  for (int k = 0; k < 2; ++k) aA[k] = lb + 98304 + w * 8192 + l15 * 128 + (((k * 4 + g4) ^ (l15 & 7)) << 4);
  unsigned xw = lb + 98304 + 6656 + w * 8192 + l15 * 16, xr_ = lb + 98304 + 6656 + (w ^ 1) * 8192 + l15 * 16;
  unsigned vo = W_ST + ((rb * 2 + wm) * 8 + cb) * 1024 + wn * 128 + ((lane >> 5) ^ wn) * 512 + (l15 >> 3) * 256 + (l15 & 7) * 16;
  asm volatile("" : "+v"(ca), "+v"(cr), "+v"(voS), "+v"(aA[0]), "+v"(aA[1]), "+v"(xw), "+v"(xr_), "+v"(vo));
  SB();
  asm volatile("s_add_u32 m0, %0, 147456" :  : "s"(mX) : "memory", "scc"); SB();
  asm volatile("s_nop 0\n\tbuffer_load_dwordx4 %0, %1, %2 offen lds" :  : "v"(voA), "s"(rX), "s"(kx[8]) : "memory"); SB();
  asm volatile("buffer_load_dwordx4 %0, %1, %2 offen offset:1024 lds" :  : "v"(voA2), "s"(rX), "s"(kx[8]) : "memory"); SB();
  asm volatile("s_waitcnt vmcnt(9)\n\ts_barrier" :  :  : "memory"); SB();
  asm volatile("ds_read_b64 %0, %1 offset:0" : "=&v"(xv[0]) : "v"(aV[0]) : "memory"); SB();
  asm volatile("ds_read_b64 %0, %1 offset:0" : "=&v"(xv[1]) : "v"(aV[1]) : "memory"); SB();
  asm volatile("ds_read_b64_tr_b16 v[160:161], %0 offset:36864" :  : "v"(aW[0][0][0]) : "memory"); SB();
  asm volatile("s_add_u32 m0, %0, 155648" :  : "s"(mX) : "memory", "scc"); SB();
  asm volatile("v_mfma_f32_16x16x32_bf16 %0, %1, a[32:35], %0" : "+v"(rs) : "v"(ones) : ); SB();
  asm volatile("ds_read_b64 %0, %1 offset:0" : "=&v"(xv[2]) : "v"(aV[2]) : "memory"); SB();
  asm volatile("ds_read_b64 %0, %1 offset:0" : "=&v"(xv[3]) : "v"(aV[3]) : "memory"); SB();
  asm volatile("ds_read_b64_tr_b16 v[162:163], %0 offset:37632" :  : "v"(aW[0][0][0]) : "memory"); SB();
  asm volatile("buffer_load_dwordx4 %0, %1, %2 offen lds" :  : "v"(voA), "s"(rX), "s"(kx[9]) : "memory"); SB();
  asm volatile("v_mfma_f32_16x16x32_bf16 %0, a[32:35], a[32:35], %0" : "+v"(gr) :  : ); SB();
  asm volatile("ds_read_b64 %0, %1 offset:0" : "=&v"(xv[4]) : "v"(aV[4]) : "memory"); SB();
  asm volatile("ds_read_b64_tr_b16 v[164:165], %0 offset:36864" :  : "v"(aW[0][0][1]) : "memory"); SB();
  asm volatile("buffer_load_dwordx4 %0, %1, %2 offen offset:1024 lds" :  : "v"(voA2), "s"(rX), "s"(kx[9]) : "memory"); SB();
  asm volatile("v_mfma_f32_16x16x32_bf16 %0, %1, a[40:43], %0" : "+v"(rs) : "v"(ones) : ); SB();
  asm volatile("ds_read_b64_tr_b16 v[166:167], %0 offset:37632" :  : "v"(aW[0][0][1]) : "memory"); SB();
  asm volatile("v_mfma_f32_16x16x32_bf16 %0, a[40:43], a[40:43], %0" : "+v"(gr) :  : ); SB();
  asm volatile("ds_read_b64_tr_b16 v[168:169], %0 offset:36864" :  : "v"(aW[0][0][2]) : "memory"); SB();
  asm volatile("s_add_u32 m0, %0, 61440" :  : "s"(mW) : "memory", "scc"); SB();
  asm volatile("s_waitcnt lgkmcnt(10)\n\tv_mfma_f32_16x16x32_bf16 %0, v[184:187], a[40:43], %0" : "+v"(a2[0][0]) :  : ); SB();
  asm volatile("ds_read_b64_tr_b16 v[170:171], %0 offset:37632" :  : "v"(aW[0][0][2]) : "memory"); SB();
  asm volatile("buffer_load_dwordx4 %0, %1, %2 offen lds" :  : "v"(voB[0]), "s"(rW), "s"(kw_[5]) : "memory"); SB();
  asm volatile("v_mfma_f32_16x16x32_bf16 %0, v[184:187], a[44:47], %0" : "+v"(a2[1][0]) :  : ); SB();
  asm volatile("ds_read_b128 a[64:67], %0 offset:16384" :  : "v"(aX[0][0]) : "memory"); SB();
  asm volatile("v_mfma_f32_16x16x32_bf16 %0, v[188:191], a[40:43], %0" : "+v"(a2[0][1]) :  : ); SB();
  asm volatile("ds_read_b128 a[68:71], %0 offset:16384" :  : "v"(aX[0][1]) : "memory"); SB();
  asm volatile("v_mfma_f32_16x16x32_bf16 %0, v[188:191], a[44:47], %0" : "+v"(a2[1][1]) :  : ); SB();
  asm volatile("ds_read_b128 a[72:75], %0 offset:16384" :  : "v"(aX[1][0]) : "memory"); SB();
  asm volatile("v_mfma_f32_16x16x32_bf16 %0, v[192:195], a[40:43], %0" : "+v"(a2[0][2]) :  : ); SB();
  asm volatile("ds_read_b128 a[76:79], %0 offset:16384" :  : "v"(aX[1][1]) : "memory"); SB();
  asm volatile("v_mfma_f32_16x16x32_bf16 %0, v[192:195], a[44:47], %0" : "+v"(a2[1][2]) :  : ); SB();
  asm volatile("ds_read_b128 a[80:83], %0 offset:24576" :  : "v"(aX[0][0]) : "memory"); SB();
  asm volatile("v_mfma_f32_16x16x32_bf16 %0, %1, a[48:51], %0" : "+v"(rs) : "v"(ones) : ); SB();
  asm volatile("ds_read_b128 a[84:87], %0 offset:24576" :  : "v"(aX[0][1]) : "memory"); SB();
  asm volatile("v_mfma_f32_16x16x32_bf16 %0, a[48:51], a[48:51], %0" : "+v"(gr) :  : ); SB();
  asm volatile("ds_read_b128 a[88:91], %0 offset:24576" :  : "v"(aX[1][0]) : "memory"); SB();
  asm volatile("v_mfma_f32_16x16x32_bf16 %0, %1, a[56:59], %0" : "+v"(rs) : "v"(ones) : ); SB();
  asm volatile("ds_read_b64_tr_b16 v[172:173], %0 offset:36864" :  : "v"(aW[0][1][0]) : "memory"); SB();
  asm volatile("v_mfma_f32_16x16x32_bf16 %0, a[56:59], a[56:59], %0" : "+v"(gr) :  : ); SB();
  asm volatile("ds_read_b64_tr_b16 v[174:175], %0 offset:37632" :  : "v"(aW[0][1][0]) : "memory"); SB();
  asm volatile("s_waitcnt lgkmcnt(9)\n\tv_mfma_f32_16x16x32_bf16 %0, v[160:163], a[48:51], %0" : "+v"(a2[0][0]) :  : ); SB();
  asm volatile("ds_read_b64_tr_b16 v[176:177], %0 offset:36864" :  : "v"(aW[0][1][1]) : "memory"); SB();
  asm volatile("v_mfma_f32_16x16x32_bf16 %0, v[160:163], a[52:55], %0" : "+v"(a2[1][0]) :  : ); SB();
  asm volatile("ds_read_b64_tr_b16 v[178:179], %0 offset:37632" :  : "v"(aW[0][1][1]) : "memory"); SB();
  asm volatile("v_mfma_f32_16x16x32_bf16 %0, v[160:163], %1, %0" : "+v"(c2[0]) : "v"(ones) : ); SB();
  asm volatile("ds_read_b64_tr_b16 v[180:181], %0 offset:36864" :  : "v"(aW[0][1][2]) : "memory"); SB();
  asm volatile("v_mfma_f32_16x16x32_bf16 %0, v[164:167], a[48:51], %0" : "+v"(a2[0][1]) :  : ); SB();
  asm volatile("ds_read_b64_tr_b16 v[182:183], %0 offset:37632" :  : "v"(aW[0][1][2]) : "memory"); SB();
  asm volatile("v_mfma_f32_16x16x32_bf16 %0, v[164:167], a[52:55], %0" : "+v"(a2[1][1]) :  : ); SB();
  asm volatile("ds_read_b128 a[92:95], %0 offset:24576" :  : "v"(aX[1][1]) : "memory"); SB();
  asm volatile("v_mfma_f32_16x16x32_bf16 %0, v[164:167], %1, %0" : "+v"(c2[1]) : "v"(ones) : ); SB();
  asm volatile("buffer_load_dwordx4 %0, %1, %2 offen offset:1024 lds" :  : "v"(voB[1]), "s"(rW), "s"(kw_[5]) : "memory"); SB();
  asm volatile("v_mfma_f32_16x16x32_bf16 %0, v[168:171], a[48:51], %0" : "+v"(a2[0][2]) :  : ); SB();
  asm volatile("buffer_load_dwordx4 %0, %1, %2 offen offset:2048 lds" :  : "v"(voB[2]), "s"(rW), "s"(kw_[5]) : "memory"); SB();
  asm volatile("v_mfma_f32_16x16x32_bf16 %0, v[168:171], a[52:55], %0" : "+v"(a2[1][2]) :  : ); SB();
  asm volatile("v_mfma_f32_16x16x32_bf16 %0, v[168:171], %1, %0" : "+v"(c2[2]) : "v"(ones) : ); SB();
#pragma unroll
  for (int n = 0; n < 8; ++n) { ao[n] = (n ^ (2 * w)) * 32768 + (3 - (n & 3)) * 1024; asm volatile("" : "+s"(ao[n])); }
  w3k[0] = w3s + 32768; w3k[1] = w3s + 65536; sFZ = W_FZ + ((cb * 32 + rb) * 4 + w) * 8; sFK = sFZ + 8192; mU = lb + w * 1024; uso = W_KV + c2s;
  asm volatile("" : "+s"(w3k[0]), "+s"(w3k[1]), "+s"(sFZ), "+s"(sFK), "+s"(mU), "+s"(uso));
  SB();
  asm volatile("s_add_u32 m0, %0, 73728" :  : "s"(mW) : "memory", "scc"); SB();
  asm volatile("s_nop 0\n\tbuffer_load_dwordx4 %0, %1, %2 offen lds" :  : "v"(voB[0]), "s"(rW), "s"(kw_[6]) : "memory"); SB();
  asm volatile("v_readfirstlane_b32 %0, v252\n\tv_readfirstlane_b32 %1, v253\n\ts_nop 0\n\ts_add_u32 %0, %0, 1\n\ts_addc_u32 %1, %1, 0" : "=&s"(tg0), "=&s"(tg1) :  : "memory", "scc"); SB();
  asm volatile("buffer_load_dwordx4 %0, %1, %2 offen offset:1024 lds" :  : "v"(voB[1]), "s"(rW), "s"(kw_[6]) : "memory"); SB();
  asm volatile("buffer_load_dwordx4 %0, %1, %2 offen offset:2048 lds" :  : "v"(voB[2]), "s"(rW), "s"(kw_[6]) : "memory"); SB();
  asm volatile("s_waitcnt vmcnt(10)\n\ts_barrier" :  :  : "memory"); SB();
  asm volatile("ds_read_b64_tr_b16 v[184:185], %0 offset:0" :  : "v"(aW[1][0][0]) : "memory"); SB();
  asm volatile("s_waitcnt lgkmcnt(1)\n\tv_mfma_f32_16x16x32_bf16 %0, %1, a[64:67], %0" : "+v"(rs) : "v"(ones) : ); SB();
  asm volatile("ds_read_b64_tr_b16 v[186:187], %0 offset:768" :  : "v"(aW[1][0][0]) : "memory"); SB();
  asm volatile("s_add_u32 m0, %0, 86016" :  : "s"(mW) : "memory", "scc"); SB();
  asm volatile("v_mfma_f32_16x16x32_bf16 %0, a[64:67], a[64:67], %0" : "+v"(gr) :  : ); SB();
  asm volatile("ds_read_b64_tr_b16 v[188:189], %0 offset:0" :  : "v"(aW[1][0][1]) : "memory"); SB();
  asm volatile("v_mfma_f32_16x16x32_bf16 %0, %1, a[72:75], %0" : "+v"(rs) : "v"(ones) : ); SB();
  asm volatile("ds_read_b64_tr_b16 v[190:191], %0 offset:768" :  : "v"(aW[1][0][1]) : "memory"); SB();
  asm volatile("v_mfma_f32_16x16x32_bf16 %0, a[72:75], a[72:75], %0" : "+v"(gr) :  : ); SB();
  asm volatile("ds_read_b64_tr_b16 v[192:193], %0 offset:0" :  : "v"(aW[1][0][2]) : "memory"); SB();
  asm volatile("v_mfma_f32_16x16x32_bf16 %0, v[172:175], a[56:59], %0" : "+v"(a2[0][0]) :  : ); SB();
  asm volatile("ds_read_b64_tr_b16 v[194:195], %0 offset:768" :  : "v"(aW[1][0][2]) : "memory"); SB();
  asm volatile("v_mfma_f32_16x16x32_bf16 %0, v[172:175], a[60:63], %0" : "+v"(a2[1][0]) :  : ); SB();
  asm volatile("ds_read_b128 a[96:99], %0 offset:32768" :  : "v"(aX[0][0]) : "memory"); SB();
  asm volatile("v_mfma_f32_16x16x32_bf16 %0, v[176:179], a[56:59], %0" : "+v"(a2[0][1]) :  : ); SB();
  asm volatile("ds_read_b128 a[100:103], %0 offset:32768" :  : "v"(aX[0][1]) : "memory"); SB();
  asm volatile("v_mfma_f32_16x16x32_bf16 %0, v[176:179], a[60:63], %0" : "+v"(a2[1][1]) :  : ); SB();
  asm volatile("ds_read_b128 a[104:107], %0 offset:32768" :  : "v"(aX[1][0]) : "memory"); SB();
  asm volatile("v_mfma_f32_16x16x32_bf16 %0, v[180:183], a[56:59], %0" : "+v"(a2[0][2]) :  : ); SB();
  asm volatile("ds_read_b128 a[108:111], %0 offset:32768" :  : "v"(aX[1][1]) : "memory"); SB();
  asm volatile("v_mfma_f32_16x16x32_bf16 %0, v[180:183], a[60:63], %0" : "+v"(a2[1][2]) :  : ); SB();
  asm volatile("ds_read_b128 a[112:115], %0 offset:40960" :  : "v"(aX[0][0]) : "memory"); SB();
  asm volatile("v_mfma_f32_16x16x32_bf16 %0, %1, a[80:83], %0" : "+v"(rs) : "v"(ones) : ); SB();
  asm volatile("ds_read_b128 a[116:119], %0 offset:40960" :  : "v"(aX[0][1]) : "memory"); SB();
  asm volatile("v_mfma_f32_16x16x32_bf16 %0, a[80:83], a[80:83], %0" : "+v"(gr) :  : ); SB();
  asm volatile("ds_read_b128 a[120:123], %0 offset:40960" :  : "v"(aX[1][0]) : "memory"); SB();
  asm volatile("v_mfma_f32_16x16x32_bf16 %0, %1, a[88:91], %0" : "+v"(rs) : "v"(ones) : ); SB();
  asm volatile("ds_read_b64_tr_b16 v[160:161], %0 offset:0" :  : "v"(aW[1][1][0]) : "memory"); SB();
  asm volatile("buffer_load_dwordx4 %0, %1, %2 offen lds" :  : "v"(voB[0]), "s"(rW), "s"(kw_[7]) : "memory"); SB();
  asm volatile("v_mfma_f32_16x16x32_bf16 %0, a[88:91], a[88:91], %0" : "+v"(gr) :  : ); SB();
  asm volatile("ds_read_b64_tr_b16 v[162:163], %0 offset:768" :  : "v"(aW[1][1][0]) : "memory"); SB();
  asm volatile("s_waitcnt lgkmcnt(9)\n\tv_mfma_f32_16x16x32_bf16 %0, v[184:187], a[64:67], %0" : "+v"(a2[0][0]) :  : ); SB();
  asm volatile("ds_read_b64_tr_b16 v[164:165], %0 offset:0" :  : "v"(aW[1][1][1]) : "memory"); SB();
  asm volatile("v_mfma_f32_16x16x32_bf16 %0, v[184:187], a[68:71], %0" : "+v"(a2[1][0]) :  : ); SB();
  asm volatile("ds_read_b64_tr_b16 v[166:167], %0 offset:768" :  : "v"(aW[1][1][1]) : "memory"); SB();
  asm volatile("buffer_load_dwordx4 %0, %1, %2 offen offset:1024 lds" :  : "v"(voB[1]), "s"(rW), "s"(kw_[7]) : "memory"); SB();
  asm volatile("v_mfma_f32_16x16x32_bf16 %0, v[184:187], %1, %0" : "+v"(c2[0]) : "v"(ones) : ); SB();
  asm volatile("ds_read_b64_tr_b16 v[168:169], %0 offset:0" :  : "v"(aW[1][1][2]) : "memory"); SB();
  asm volatile("v_mfma_f32_16x16x32_bf16 %0, v[188:191], a[64:67], %0" : "+v"(a2[0][1]) :  : ); SB();
  asm volatile("ds_read_b64_tr_b16 v[170:171], %0 offset:768" :  : "v"(aW[1][1][2]) : "memory"); SB();
  asm volatile("v_mfma_f32_16x16x32_bf16 %0, v[188:191], a[68:71], %0" : "+v"(a2[1][1]) :  : ); SB();
  asm volatile("ds_read_b128 a[124:127], %0 offset:40960" :  : "v"(aX[1][1]) : "memory"); SB();
  asm volatile("v_mfma_f32_16x16x32_bf16 %0, v[188:191], %1, %0" : "+v"(c2[1]) : "v"(ones) : ); SB();
  asm volatile("buffer_load_dwordx4 %0, %1, %2 offen offset:2048 lds" :  : "v"(voB[2]), "s"(rW), "s"(kw_[7]) : "memory"); SB();
  asm volatile("v_mfma_f32_16x16x32_bf16 %0, v[192:195], a[64:67], %0" : "+v"(a2[0][2]) :  : ); SB();
  asm volatile("s_add_u32 m0, %0, 0" :  : "s"(mW) : "memory", "scc"); SB();
  asm volatile("v_mfma_f32_16x16x32_bf16 %0, v[192:195], a[68:71], %0" : "+v"(a2[1][2]) :  : ); SB();
  asm volatile("buffer_load_dwordx4 %0, %1, %2 offen lds" :  : "v"(voB[0]), "s"(rW), "s"(kw_[8]) : "memory"); SB();
  asm volatile("v_mfma_f32_16x16x32_bf16 %0, v[192:195], %1, %0" : "+v"(c2[2]) : "v"(ones) : ); SB();
  asm volatile("buffer_load_dwordx4 %0, %1, %2 offen offset:1024 lds" :  : "v"(voB[1]), "s"(rW), "s"(kw_[8]) : "memory"); SB();
  asm volatile("buffer_load_dwordx4 %0, %1, %2 offen offset:2048 lds" :  : "v"(voB[2]), "s"(rW), "s"(kw_[8]) : "memory"); SB();
  asm volatile("s_add_u32 m0, %0, 12288" :  : "s"(mW) : "memory", "scc"); SB();
  asm volatile("s_nop 0\n\tbuffer_load_dwordx4 %0, %1, %2 offen lds" :  : "v"(voB[0]), "s"(rW), "s"(kw_[9]) : "memory"); SB();
  asm volatile("s_waitcnt vmcnt(10)\n\ts_barrier" :  :  : "memory"); SB();
  asm volatile("ds_read_b64_tr_b16 v[172:173], %0 offset:12288" :  : "v"(aW[1][0][0]) : "memory"); SB();
  asm volatile("s_waitcnt lgkmcnt(1)\n\tv_mfma_f32_16x16x32_bf16 %0, %1, a[96:99], %0" : "+v"(rs) : "v"(ones) : ); SB();
  asm volatile("ds_read_b64_tr_b16 v[174:175], %0 offset:13056" :  : "v"(aW[1][0][0]) : "memory"); SB();
  asm volatile("v_mfma_f32_16x16x32_bf16 %0, a[96:99], a[96:99], %0" : "+v"(gr) :  : ); SB();
  asm volatile("ds_read_b64_tr_b16 v[176:177], %0 offset:12288" :  : "v"(aW[1][0][1]) : "memory"); SB();
  asm volatile("v_mfma_f32_16x16x32_bf16 %0, %1, a[104:107], %0" : "+v"(rs) : "v"(ones) : ); SB();
  asm volatile("ds_read_b64_tr_b16 v[178:179], %0 offset:13056" :  : "v"(aW[1][0][1]) : "memory"); SB();
  asm volatile("v_mfma_f32_16x16x32_bf16 %0, a[104:107], a[104:107], %0" : "+v"(gr) :  : ); SB();
  asm volatile("ds_read_b64_tr_b16 v[180:181], %0 offset:12288" :  : "v"(aW[1][0][2]) : "memory"); SB();
  asm volatile("v_mfma_f32_16x16x32_bf16 %0, v[160:163], a[72:75], %0" : "+v"(a2[0][0]) :  : ); SB();
  asm volatile("ds_read_b64_tr_b16 v[182:183], %0 offset:13056" :  : "v"(aW[1][0][2]) : "memory"); SB();
  asm volatile("v_mfma_f32_16x16x32_bf16 %0, v[160:163], a[76:79], %0" : "+v"(a2[1][0]) :  : ); SB();
  asm volatile("ds_read_b128 a[128:131], %0 offset:49152" :  : "v"(aX[0][0]) : "memory"); SB();
  asm volatile("v_mfma_f32_16x16x32_bf16 %0, v[164:167], a[72:75], %0" : "+v"(a2[0][1]) :  : ); SB();
  asm volatile("ds_read_b128 a[132:135], %0 offset:49152" :  : "v"(aX[0][1]) : "memory"); SB();
  asm volatile("v_mfma_f32_16x16x32_bf16 %0, v[164:167], a[76:79], %0" : "+v"(a2[1][1]) :  : ); SB();
  asm volatile("ds_read_b128 a[136:139], %0 offset:49152" :  : "v"(aX[1][0]) : "memory"); SB();
  asm volatile("v_mfma_f32_16x16x32_bf16 %0, v[168:171], a[72:75], %0" : "+v"(a2[0][2]) :  : ); SB();
  asm volatile("ds_read_b128 a[140:143], %0 offset:49152" :  : "v"(aX[1][1]) : "memory"); SB();
  asm volatile("v_mfma_f32_16x16x32_bf16 %0, v[168:171], a[76:79], %0" : "+v"(a2[1][2]) :  : ); SB();
  asm volatile("ds_read_b128 a[144:147], %0 offset:57344" :  : "v"(aX[0][0]) : "memory"); SB();
  asm volatile("v_mfma_f32_16x16x32_bf16 %0, %1, a[112:115], %0" : "+v"(rs) : "v"(ones) : ); SB();
  asm volatile("buffer_load_dwordx4 %0, %1, %2 offen offset:1024 lds" :  : "v"(voB[1]), "s"(rW), "s"(kw_[9]) : "memory"); SB();
  asm volatile("v_mfma_f32_16x16x32_bf16 %0, a[112:115], a[112:115], %0" : "+v"(gr) :  : ); SB();
  asm volatile("ds_read_b128 a[148:151], %0 offset:57344" :  : "v"(aX[0][1]) : "memory"); SB();
  asm volatile("v_mfma_f32_16x16x32_bf16 %0, %1, a[120:123], %0" : "+v"(rs) : "v"(ones) : ); SB();
  asm volatile("ds_read_b64_tr_b16 v[184:185], %0 offset:12288" :  : "v"(aW[1][1][0]) : "memory"); SB();
  asm volatile("v_mfma_f32_16x16x32_bf16 %0, a[120:123], a[120:123], %0" : "+v"(gr) :  : ); SB();
  asm volatile("ds_read_b64_tr_b16 v[186:187], %0 offset:13056" :  : "v"(aW[1][1][0]) : "memory"); SB();
  asm volatile("s_waitcnt lgkmcnt(8)\n\tv_mfma_f32_16x16x32_bf16 %0, v[172:175], a[80:83], %0" : "+v"(a2[0][0]) :  : ); SB();
  asm volatile("ds_read_b64_tr_b16 v[188:189], %0 offset:12288" :  : "v"(aW[1][1][1]) : "memory"); SB();
  asm volatile("buffer_load_dwordx4 %0, %1, %2 offen offset:2048 lds" :  : "v"(voB[2]), "s"(rW), "s"(kw_[9]) : "memory"); SB();
  asm volatile("v_mfma_f32_16x16x32_bf16 %0, v[172:175], a[84:87], %0" : "+v"(a2[1][0]) :  : ); SB();
  asm volatile("ds_read_b64_tr_b16 v[190:191], %0 offset:13056" :  : "v"(aW[1][1][1]) : "memory"); SB();
  asm volatile("v_mfma_f32_16x16x32_bf16 %0, v[172:175], %1, %0" : "+v"(c2[0]) : "v"(ones) : ); SB();
  asm volatile("ds_read_b64_tr_b16 v[192:193], %0 offset:12288" :  : "v"(aW[1][1][2]) : "memory"); SB();
  asm volatile("v_mfma_f32_16x16x32_bf16 %0, v[176:179], a[80:83], %0" : "+v"(a2[0][1]) :  : ); SB();
  asm volatile("ds_read_b64_tr_b16 v[194:195], %0 offset:13056" :  : "v"(aW[1][1][2]) : "memory"); SB();
  asm volatile("v_mfma_f32_16x16x32_bf16 %0, v[176:179], a[84:87], %0" : "+v"(a2[1][1]) :  : ); SB();
  asm volatile("ds_read_b128 a[152:155], %0 offset:57344" :  : "v"(aX[1][0]) : "memory"); SB();
  asm volatile("v_mfma_f32_16x16x32_bf16 %0, v[176:179], %1, %0" : "+v"(c2[1]) : "v"(ones) : ); SB();
  asm volatile("ds_read_b128 a[156:159], %0 offset:57344" :  : "v"(aX[1][1]) : "memory"); SB();
  asm volatile("v_mfma_f32_16x16x32_bf16 %0, v[180:183], a[80:83], %0" : "+v"(a2[0][2]) :  : ); SB();
  asm volatile("v_cmp_le_u32 vcc, %2, %1\n\ts_mov_b64 exec, vcc\n\tds_read_b64 %0, %1\n\ts_mov_b64 exec, -1" : "+v"(xv[0]) : "v"(aV[0]), "s"(lb + 131072) : "memory", "vcc"); SB();
  asm volatile("v_mfma_f32_16x16x32_bf16 %0, v[180:183], a[84:87], %0" : "+v"(a2[1][2]) :  : ); SB();
  asm volatile("v_cmp_le_u32 vcc, %2, %1\n\ts_mov_b64 exec, vcc\n\tds_read_b64 %0, %1\n\ts_mov_b64 exec, -1" : "+v"(xv[1]) : "v"(aV[1]), "s"(lb + 131072) : "memory", "vcc"); SB();
  asm volatile("v_mfma_f32_16x16x32_bf16 %0, v[180:183], %1, %0" : "+v"(c2[2]) : "v"(ones) : ); SB();
  asm volatile("s_waitcnt vmcnt(9)\n\ts_barrier" :  :  : "memory"); SB();
  asm volatile("ds_read_b64_tr_b16 v[160:161], %0 offset:24576" :  : "v"(aW[1][0][0]) : "memory"); SB();
  asm volatile("s_waitcnt lgkmcnt(6)\n\tv_mfma_f32_16x16x32_bf16 %0, %1, a[128:131], %0" : "+v"(rs) : "v"(ones) : ); SB();
  asm volatile("ds_read_b64_tr_b16 v[162:163], %0 offset:25344" :  : "v"(aW[1][0][0]) : "memory"); SB();
  asm volatile("v_mfma_f32_16x16x32_bf16 %0, a[128:131], a[128:131], %0" : "+v"(gr) :  : ); SB();
  asm volatile("ds_read_b64_tr_b16 v[164:165], %0 offset:24576" :  : "v"(aW[1][0][1]) : "memory"); SB();
  asm volatile("v_mfma_f32_16x16x32_bf16 %0, %1, a[136:139], %0" : "+v"(rs) : "v"(ones) : ); SB();
  asm volatile("ds_read_b64_tr_b16 v[166:167], %0 offset:25344" :  : "v"(aW[1][0][1]) : "memory"); SB();
  asm volatile("v_mfma_f32_16x16x32_bf16 %0, a[136:139], a[136:139], %0" : "+v"(gr) :  : ); SB();
  asm volatile("ds_read_b64_tr_b16 v[168:169], %0 offset:24576" :  : "v"(aW[1][0][2]) : "memory"); SB();
  asm volatile("v_mfma_f32_16x16x32_bf16 %0, v[184:187], a[88:91], %0" : "+v"(a2[0][0]) :  : ); SB();
  asm volatile("ds_read_b64_tr_b16 v[170:171], %0 offset:25344" :  : "v"(aW[1][0][2]) : "memory"); SB();
  asm volatile("v_mfma_f32_16x16x32_bf16 %0, v[184:187], a[92:95], %0" : "+v"(a2[1][0]) :  : ); SB();
  asm volatile("v_cmp_le_u32 vcc, %2, %1\n\ts_mov_b64 exec, vcc\n\tds_read_b64 %0, %1\n\ts_mov_b64 exec, -1" : "+v"(xv[2]) : "v"(aV[2]), "s"(lb + 131072) : "memory", "vcc"); SB();
  asm volatile("v_mfma_f32_16x16x32_bf16 %0, v[188:191], a[88:91], %0" : "+v"(a2[0][1]) :  : ); SB();
  asm volatile("v_cmp_le_u32 vcc, %2, %1\n\ts_mov_b64 exec, vcc\n\tds_read_b64 %0, %1\n\ts_mov_b64 exec, -1" : "+v"(xv[3]) : "v"(aV[3]), "s"(lb + 131072) : "memory", "vcc"); SB();
  asm volatile("v_mfma_f32_16x16x32_bf16 %0, v[188:191], a[92:95], %0" : "+v"(a2[1][1]) :  : ); SB();
  asm volatile("v_cmp_le_u32 vcc, %2, %1\n\ts_mov_b64 exec, vcc\n\tds_read_b64 %0, %1\n\ts_mov_b64 exec, -1" : "+v"(xv[4]) : "v"(aV[4]), "s"(lb + 131072) : "memory", "vcc"); SB();
  asm volatile("s_waitcnt lgkmcnt(7)\n\tv_mfma_f32_16x16x32_bf16 %0, v[192:195], a[88:91], %0" : "+v"(a2[0][2]) :  : ); SB();
  asm volatile("ds_read_b64_tr_b16 v[172:173], %0 offset:24576" :  : "v"(aW[1][1][0]) : "memory"); SB();
  asm volatile("v_mfma_f32_16x16x32_bf16 %0, v[192:195], a[92:95], %0" : "+v"(a2[1][2]) :  : ); SB();
  asm volatile("ds_read_b64_tr_b16 v[174:175], %0 offset:25344" :  : "v"(aW[1][1][0]) : "memory"); SB();
  asm volatile("v_mfma_f32_16x16x32_bf16 %0, %1, a[144:147], %0" : "+v"(rs) : "v"(ones) : ); SB();
  asm volatile("ds_read_b64_tr_b16 v[176:177], %0 offset:24576" :  : "v"(aW[1][1][1]) : "memory"); SB();
  asm volatile("buffer_load_dword v242, off, %0, %1" :  : "s"(rF), "s"(hpo[0]) : "memory"); SB();
  asm volatile("v_mfma_f32_16x16x32_bf16 %0, a[144:147], a[144:147], %0" : "+v"(gr) :  : ); SB();
  asm volatile("ds_read_b64_tr_b16 v[178:179], %0 offset:25344" :  : "v"(aW[1][1][1]) : "memory"); SB();
  asm volatile("s_add_u32 m0, %0, 98304" :  : "s"(mA) : "memory", "scc"); SB();
  asm volatile("v_mfma_f32_16x16x32_bf16 %0, %1, a[152:155], %0" : "+v"(rs) : "v"(ones) : ); SB();
  asm volatile("ds_read_b64_tr_b16 v[180:181], %0 offset:24576" :  : "v"(aW[1][1][2]) : "memory"); SB();
  asm volatile("buffer_load_dwordx4 %0, %1, %2 offen lds" :  : "v"(voK[0]), "s"(rK), "s"(0u) : "memory"); SB();
  asm volatile("v_mfma_f32_16x16x32_bf16 %0, a[152:155], a[152:155], %0" : "+v"(gr) :  : ); SB();
  asm volatile("ds_read_b64_tr_b16 v[182:183], %0 offset:25344" :  : "v"(aW[1][1][2]) : "memory"); SB();
  asm volatile("v_mfma_f32_16x16x32_bf16 %0, v[160:163], a[96:99], %0" : "+v"(a2[0][0]) :  : ); SB();
  asm volatile("buffer_load_dwordx4 %0, %1, %2 offen offset:1024 lds" :  : "v"(voK[1]), "s"(rK), "s"(0u) : "memory"); SB();
  asm volatile("buffer_load_dwordx4 %0, %1, %2 offen offset:2048 lds" :  : "v"(voK[2]), "s"(rK), "s"(0u) : "memory"); SB();
  asm volatile("s_waitcnt vmcnt(10)\n\ts_barrier" :  :  : "memory"); SB();
  asm volatile("ds_read_b64_tr_b16 v[184:185], %0 offset:36864" :  : "v"(aW[1][0][0]) : "memory"); SB();
  asm volatile("v_mfma_f32_16x16x32_bf16 %0, v[160:163], a[100:103], %0" : "+v"(a2[1][0]) :  : ); SB();
  asm volatile("ds_read_b64_tr_b16 v[186:187], %0 offset:37632" :  : "v"(aW[1][0][0]) : "memory"); SB();
  asm volatile("v_mfma_f32_16x16x32_bf16 %0, v[160:163], %1, %0" : "+v"(c2[0]) : "v"(ones) : ); SB();
  asm volatile("ds_read_b64_tr_b16 v[188:189], %0 offset:36864" :  : "v"(aW[1][0][1]) : "memory"); SB();
  asm volatile("s_waitcnt lgkmcnt(3)\n\tv_mfma_f32_16x16x32_bf16 %0, v[164:167], a[96:99], %0" : "+v"(a2[0][1]) :  : ); SB();
  asm volatile("ds_read_b64_tr_b16 v[190:191], %0 offset:37632" :  : "v"(aW[1][0][1]) : "memory"); SB();
  asm volatile("global_load_ushort v245, %0, %1 offset:0" :  : "v"(vD), "s"(ssm_D) : "memory"); SB();
  asm volatile("v_mfma_f32_16x16x32_bf16 %0, v[164:167], a[100:103], %0" : "+v"(a2[1][1]) :  : ); SB();
  asm volatile("ds_read_b64_tr_b16 v[192:193], %0 offset:36864" :  : "v"(aW[1][0][2]) : "memory"); SB();
  asm volatile("s_mov_b32 exec_lo, 0xff\n\ts_mov_b32 exec_hi, 0\n\tbuffer_load_dwordx4 %0, %1, %2 offen offset:3072 lds\n\ts_mov_b64 exec, -1" :  : "v"(voK[3]), "s"(rK), "s"(0u) : "memory"); SB();
  asm volatile("v_mfma_f32_16x16x32_bf16 %0, v[164:167], %1, %0" : "+v"(c2[1]) : "v"(ones) : ); SB();
  asm volatile("ds_read_b64_tr_b16 v[194:195], %0 offset:37632" :  : "v"(aW[1][0][2]) : "memory"); SB();
  asm volatile("v_mfma_f32_16x16x32_bf16 %0, v[168:171], a[96:99], %0" : "+v"(a2[0][2]) :  : ); SB();
  asm volatile("v_mfma_f32_16x16x32_bf16 %0, v[168:171], a[100:103], %0" : "+v"(a2[1][2]) :  : ); SB();
  asm volatile("s_mov_b32 exec_lo, -1\n\ts_mov_b32 exec_hi, 0xff\n\tbuffer_load_dwordx4 %0, %1, %2 offen offset:3328 lds\n\ts_mov_b64 exec, -1" :  : "v"(voH), "s"(rS), "s"(0u) : "memory"); SB();
  asm volatile("v_mfma_f32_16x16x32_bf16 %0, v[168:171], %1, %0" : "+v"(c2[2]) : "v"(ones) : ); SB();
  asm volatile("v_mfma_f32_16x16x32_bf16 %0, v[172:175], a[104:107], %0" : "+v"(a2[0][0]) :  : ); SB();
  asm volatile("ds_read_b64_tr_b16 v[160:161], %0 offset:36864" :  : "v"(aW[1][1][0]) : "memory"); SB();
  asm volatile("s_add_u32 m0, %0, 102400" :  : "s"(mA) : "memory", "scc"); SB();
  asm volatile("v_mfma_f32_16x16x32_bf16 %0, v[172:175], a[108:111], %0" : "+v"(a2[1][0]) :  : ); SB();
  asm volatile("s_mov_b32 exec_lo, 0x10001\n\ts_mov_b32 exec_hi, 0x10001\n\tds_write_b128 %0, %1 offset:0\n\ts_mov_b64 exec, -1" :  : "v"(ca), "v"(c2[0]) : "memory"); SB();
  asm volatile("ds_read_b64_tr_b16 v[162:163], %0 offset:37632" :  : "v"(aW[1][1][0]) : "memory"); SB();
  asm volatile("buffer_load_dwordx4 %0, %1, %2 offen lds" :  : "v"(voV[0]), "s"(rV), "s"(0u) : "memory"); SB();
  asm volatile("v_mfma_f32_16x16x32_bf16 %0, v[176:179], a[104:107], %0" : "+v"(a2[0][1]) :  : ); SB();
  asm volatile("ds_read_b64_tr_b16 v[164:165], %0 offset:36864" :  : "v"(aW[1][1][1]) : "memory"); SB();
  asm volatile("v_cndmask_b32 %0, %1, %2, %3" : "=v"(xt[0]) : "v"(gr[0]), "v"(gr[1]), "s"(0xAAAAAAAAAAAAAAAAull) : ); SB();
  asm volatile("v_mfma_f32_16x16x32_bf16 %0, v[176:179], a[108:111], %0" : "+v"(a2[1][1]) :  : ); SB();
  asm volatile("s_mov_b32 exec_lo, 0x10001\n\ts_mov_b32 exec_hi, 0x10001\n\tds_write_b128 %0, %1 offset:64\n\ts_mov_b64 exec, -1" :  : "v"(ca), "v"(c2[1]) : "memory"); SB();
  asm volatile("ds_read_b64_tr_b16 v[166:167], %0 offset:37632" :  : "v"(aW[1][1][1]) : "memory"); SB();
  asm volatile("buffer_load_dword v243, off, %0, %1" :  : "s"(rF), "s"(hpo[1]) : "memory"); SB();
  asm volatile("v_mfma_f32_16x16x32_bf16 %0, v[180:183], a[104:107], %0" : "+v"(a2[0][2]) :  : ); SB();
  asm volatile("ds_read_b64_tr_b16 v[168:169], %0 offset:36864" :  : "v"(aW[1][1][2]) : "memory"); SB();
  asm volatile("buffer_load_dwordx4 %0, %1, %2 offen offset:1024 lds" :  : "v"(voV[1]), "s"(rV), "s"(0u) : "memory"); SB();
  asm volatile("v_mfma_f32_16x16x32_bf16 %0, v[180:183], a[108:111], %0" : "+v"(a2[1][2]) :  : ); SB();
  asm volatile("s_mov_b32 exec_lo, 0x10001\n\ts_mov_b32 exec_hi, 0x10001\n\tds_write_b128 %0, %1 offset:128\n\ts_mov_b64 exec, -1" :  : "v"(ca), "v"(c2[2]) : "memory"); SB();
  asm volatile("ds_read_b64_tr_b16 v[170:171], %0 offset:37632" :  : "v"(aW[1][1][2]) : "memory"); SB();
  asm volatile("s_waitcnt lgkmcnt(5)\n\tv_mfma_f32_16x16x32_bf16 %0, v[184:187], a[112:115], %0" : "+v"(a2[0][0]) :  : ); SB();
  asm volatile("s_mov_b32 exec_lo, -1\n\ts_mov_b32 exec_hi, 0\n\tbuffer_load_dwordx4 %0, %1, %2 offen offset:2048 lds\n\ts_mov_b64 exec, -1" :  : "v"(voV[2]), "s"(rV), "s"(0u) : "memory"); SB();
  asm volatile("v_cndmask_b32 %0, %1, %2, %3" : "=v"(xt[1]) : "v"(gr[2]), "v"(gr[3]), "s"(0xAAAAAAAAAAAAAAAAull) : ); SB();
  asm volatile("v_mfma_f32_16x16x32_bf16 %0, v[184:187], a[116:119], %0" : "+v"(a2[1][0]) :  : ); SB();
  asm volatile("buffer_load_dwordx4 %0, %1, %2 offen offset:3072 lds" :  : "v"(voO), "s"(rO), "s"(0u) : "memory"); SB();
  asm volatile("v_mul_f32 %0, 0x3acccccd, %1" : "=v"(xs[0][0]) : "v"(rs[0]) : ); SB();
  asm volatile("v_mfma_f32_16x16x32_bf16 %0, v[184:187], %1, %0" : "+v"(c2[0]) : "v"(ones) : ); SB();
  asm volatile("global_load_ushort v246, %0, %1 offset:2" :  : "v"(vD), "s"(ssm_D) : "memory"); SB();
  asm volatile("v_cndmask_b32 %0, %0, %1, %2" : "+v"(xt[0]) : "v"(xt[1]), "s"(0xCCCCCCCCCCCCCCCCull) : ); SB();
  asm volatile("v_mfma_f32_16x16x32_bf16 %0, v[188:191], a[112:115], %0" : "+v"(a2[0][1]) :  : ); SB();
  asm volatile("s_waitcnt vmcnt(16)\n\ts_barrier" :  :  : "memory"); SB();
  asm volatile("ds_read_b64_tr_b16 v[172:173], %0 offset:0" :  : "v"(aW[0][0][0]) : "memory"); SB();
  asm volatile("v_mul_f32 %0, 0x3a83126f, %1" : "=v"(xt[1]) : "v"(rs[0]) : ); SB();
  asm volatile("v_mfma_f32_16x16x32_bf16 %0, v[188:191], a[116:119], %0" : "+v"(a2[1][1]) :  : ); SB();
  asm volatile("ds_read_b64_tr_b16 v[174:175], %0 offset:768" :  : "v"(aW[0][0][0]) : "memory"); SB();
  asm volatile("v_fma_f32 %0, -%1, %2, %3" : "=v"(xs[0][1]) : "v"(rs[0]), "v"(xs[0][0]), "v"(xt[0]) : ); SB();
  asm volatile("v_mfma_f32_16x16x32_bf16 %0, v[188:191], %1, %0" : "+v"(c2[1]) : "v"(ones) : ); SB();
  asm volatile("ds_read_b64_tr_b16 v[176:177], %0 offset:0" :  : "v"(aW[0][0][1]) : "memory"); SB();
  asm volatile("v_mul_f32 %0, %0, %1" : "+v"(xt[1]) : "v"(xs[0][0]) : ); SB();
  asm volatile("v_mfma_f32_16x16x32_bf16 %0, v[192:195], a[112:115], %0" : "+v"(a2[0][2]) :  : ); SB();
  asm volatile("ds_read_b64_tr_b16 v[178:179], %0 offset:768" :  : "v"(aW[0][0][1]) : "memory"); SB();
  asm volatile("v_mfma_f32_16x16x32_bf16 %0, v[192:195], a[116:119], %0" : "+v"(a2[1][2]) :  : ); SB();
  asm volatile("ds_read_b64_tr_b16 v[180:181], %0 offset:0" :  : "v"(aW[0][0][2]) : "memory"); SB();
  asm volatile("v_mfma_f32_16x16x32_bf16 %0, v[192:195], %1, %0" : "+v"(c2[2]) : "v"(ones) : ); SB();
  asm volatile("ds_read_b64_tr_b16 v[182:183], %0 offset:768" :  : "v"(aW[0][0][2]) : "memory"); SB();
  asm volatile("v_mfma_f32_16x16x32_bf16 %0, v[160:163], a[120:123], %0" : "+v"(a2[0][0]) :  : ); SB();
  if (__builtin_expect((__builtin_amdgcn_ballot_w64(!(xs[0][1] >= xt[1])) & 0xF0000F0000F0000Full) != 0, 0)) {
    const f32x2 mm = {xs[0][0], xs[0][0]};
    f32x2 q2 = {0.f, 0.f};
    const u32x4* xp = (const u32x4*)(x + (size_t)(rb * 64 + w * 16 + l15) * 1280 + g4 * 320);
#pragma unroll 1
    for (int c = 0; c < 20; ++c) {
      u32x4 xq;
      asm volatile("global_load_dwordx4 %0, %1, off\n\ts_waitcnt vmcnt(0)" : "=&v"(xq) : "v"(xp + c) : "memory");
#pragma unroll
      for (int e = 0; e < 4; ++e) { const f32x2 d = unpk(xq[e]) - mm; q2 = __builtin_elementwise_fma(d, d, q2); }
    }
    xs[0][1] = red32(red16(q2[0] + q2[1]));
  }
  SB();
  asm volatile("v_mfma_f32_16x16x32_bf16 %0, v[160:163], a[124:127], %0" : "+v"(a2[1][0]) :  : ); SB();
  asm volatile("buffer_load_dword v244, off, %0, %1" :  : "s"(rF), "s"(hpo[2]) : "memory"); SB();
  asm volatile("v_max_f32 %0, 0, %0" : "+v"(xs[0][1]) :  : ); SB();
  asm volatile("v_mfma_f32_16x16x32_bf16 %0, v[160:163], %1, %0" : "+v"(c2[0]) : "v"(ones) : ); SB();
  asm volatile("buffer_load_dwordx4 v[228:231], %0, %1, %2 offen" :  : "v"(vF), "s"(rF), "s"(0u) : "memory"); SB();
  asm volatile("s_waitcnt lgkmcnt(5)\n\tv_mfma_f32_16x16x32_bf16 %0, v[164:167], a[120:123], %0" : "+v"(a2[0][1]) :  : ); SB();
  asm volatile("ds_read_b64_tr_b16 v[184:185], %0 offset:0" :  : "v"(aW[0][1][0]) : "memory"); SB();
  asm volatile("v_mul_f32 %0, 0x3acccccd, %0" : "+v"(xs[0][1]) :  : ); SB();
  asm volatile("v_mfma_f32_16x16x32_bf16 %0, v[164:167], a[124:127], %0" : "+v"(a2[1][1]) :  : ); SB();
  asm volatile("ds_read_b64_tr_b16 v[186:187], %0 offset:768" :  : "v"(aW[0][1][0]) : "memory"); SB();
  asm volatile("v_cvt_pk_bf16_f32 %0, %0, %0" : "+v"(xs[0][1]) :  : ); SB();
  asm volatile("v_mfma_f32_16x16x32_bf16 %0, v[164:167], %1, %0" : "+v"(c2[1]) : "v"(ones) : ); SB();
  asm volatile("ds_read_b64_tr_b16 v[188:189], %0 offset:0" :  : "v"(aW[0][1][1]) : "memory"); SB();
  asm volatile("v_lshlrev_b32 %0, 16, %0" : "+v"(xs[0][1]) :  : ); SB();
  asm volatile("v_mfma_f32_16x16x32_bf16 %0, v[168:171], a[120:123], %0" : "+v"(a2[0][2]) :  : ); SB();
  asm volatile("ds_read_b64_tr_b16 v[190:191], %0 offset:768" :  : "v"(aW[0][1][1]) : "memory"); SB();
  asm volatile("v_add_f32 %0, 0x3727c5ac, %0" : "+v"(xs[0][1]) :  : ); SB();
  asm volatile("v_mfma_f32_16x16x32_bf16 %0, v[168:171], a[124:127], %0" : "+v"(a2[1][2]) :  : ); SB();
  asm volatile("ds_read_b64_tr_b16 v[192:193], %0 offset:0" :  : "v"(aW[0][1][2]) : "memory"); SB();
  asm volatile("v_cvt_pk_bf16_f32 %0, %0, %0" : "+v"(xs[0][1]) :  : ); SB();
  asm volatile("v_mfma_f32_16x16x32_bf16 %0, v[168:171], %1, %0" : "+v"(c2[2]) : "v"(ones) : ); SB();
  asm volatile("ds_read_b64_tr_b16 v[194:195], %0 offset:768" :  : "v"(aW[0][1][2]) : "memory"); SB();
  asm volatile("s_waitcnt lgkmcnt(6)\n\tv_mfma_f32_16x16x32_bf16 %0, v[172:175], a[128:131], %0" : "+v"(a2[0][0]) :  : ); SB();
  asm volatile("global_load_ushort v247, %0, %1 offset:4" :  : "v"(vD2), "s"(ssm_D) : "memory"); SB();
  asm volatile("v_lshlrev_b32 %0, 16, %0" : "+v"(xs[0][1]) :  : ); SB();
  asm volatile("v_mfma_f32_16x16x32_bf16 %0, v[172:175], a[132:135], %0" : "+v"(a2[1][0]) :  : ); SB();
  asm volatile("buffer_load_dwordx4 v[232:235], %0, %1, %2 offen" :  : "v"(vF), "s"(rF), "s"(4096u) : "memory"); SB();
  asm volatile("v_mfma_f32_16x16x32_bf16 %0, v[172:175], %1, %0" : "+v"(c2[0]) : "v"(ones) : ); SB();
  asm volatile("v_sqrt_f32 %0, %0" : "+v"(xs[0][1]) :  : ); SB();
  asm volatile("v_mfma_f32_16x16x32_bf16 %0, v[176:179], a[128:131], %0" : "+v"(a2[0][1]) :  : ); SB();
  asm volatile("v_cvt_pk_bf16_f32 %0, %0, %0" : "+v"(xs[0][0]) :  : ); SB();
  asm volatile("v_lshlrev_b32 %0, 16, %0" : "+v"(xs[0][0]) :  : ); SB();
  asm volatile("v_cvt_pk_bf16_f32 %0, %0, %0" : "+v"(xs[0][1]) :  : ); SB();
  asm volatile("v_lshlrev_b32 %0, 16, %0" : "+v"(xs[0][1]) :  : ); SB();
  asm volatile("v_rcp_f32 %0, %1" : "=v"(xt[0]) : "v"(xs[0][1]) : ); SB();
  asm volatile("s_nop 0\n\tv_mul_f32 %0, 0xc0135761, %1" : "=v"(xs[0][2]) : "v"(xt[0]) : ); SB();
  asm volatile("v_mul_f32 %0, %1, %1" : "=v"(xt[1]) : "v"(xt[0]) : ); SB();
  asm volatile("v_mul_f32 %0, 0xbdd2d3e8, %0" : "+v"(xt[0]) :  : ); SB();
  asm volatile("v_mul_f32 %0, %1, %2" : "=v"(xs[0][3]) : "v"(xt[0]), "v"(xt[1]) : ); SB();
  asm volatile("s_mov_b32 exec_lo, 0xf0000f\n\ts_mov_b32 exec_hi, 0xf0000f00\n\tds_write_b128 %0, %1\n\ts_mov_b64 exec, -1" :  : "v"(xw), "v"(xs[0]) : "memory"); SB();
  asm volatile("" : "+v"(xv[0]), "+v"(xv[1]), "+v"(xv[2]), "+v"(xv[3]), "+v"(xv[4]) :  : "memory"); SB();
  asm volatile("s_waitcnt vmcnt(17) lgkmcnt(0)\n\ts_barrier" :  :  : "memory"); SB();
  asm volatile("ds_read_b64_tr_b16 v[160:161], %0 offset:12288" :  : "v"(aW[0][0][0]) : "memory"); SB();
  asm volatile("v_mfma_f32_16x16x32_bf16 %0, v[176:179], a[132:135], %0" : "+v"(a2[1][1]) :  : ); SB();
  asm volatile("ds_read_b64_tr_b16 v[162:163], %0 offset:13056" :  : "v"(aW[0][0][0]) : "memory"); SB();
  asm volatile("v_mfma_f32_16x16x32_bf16 %0, v[176:179], %1, %0" : "+v"(c2[1]) : "v"(ones) : ); SB();
  asm volatile("ds_read_b64_tr_b16 v[164:165], %0 offset:12288" :  : "v"(aW[0][0][1]) : "memory"); SB();
  asm volatile("v_mfma_f32_16x16x32_bf16 %0, v[180:183], a[128:131], %0" : "+v"(a2[0][2]) :  : ); SB();
  asm volatile("ds_read_b64_tr_b16 v[166:167], %0 offset:13056" :  : "v"(aW[0][0][1]) : "memory"); SB();
  asm volatile("v_mfma_f32_16x16x32_bf16 %0, v[180:183], a[132:135], %0" : "+v"(a2[1][2]) :  : ); SB();
  asm volatile("ds_read_b64_tr_b16 v[168:169], %0 offset:12288" :  : "v"(aW[0][0][2]) : "memory"); SB();
  asm volatile("v_mfma_f32_16x16x32_bf16 %0, v[180:183], %1, %0" : "+v"(c2[2]) : "v"(ones) : ); SB();
  asm volatile("ds_read_b64_tr_b16 v[170:171], %0 offset:13056" :  : "v"(aW[0][0][2]) : "memory"); SB();
  asm volatile("v_mfma_f32_16x16x32_bf16 %0, v[184:187], a[136:139], %0" : "+v"(a2[0][0]) :  : ); SB();
  asm volatile("ds_read_b128 %0, %1 offset:0" : "=&v"(xs[0]) : "v"(xw) : "memory"); SB();
  asm volatile("v_mfma_f32_16x16x32_bf16 %0, v[184:187], a[140:143], %0" : "+v"(a2[1][0]) :  : ); SB();
  asm volatile("ds_read_b128 %0, %1 offset:0" : "=&v"(xs[1]) : "v"(xr_) : "memory"); SB();
  asm volatile("v_mfma_f32_16x16x32_bf16 %0, v[184:187], %1, %0" : "+v"(c2[0]) : "v"(ones) : ); SB();
  asm volatile("ds_read_b128 %0, %1 offset:0" : "=&v"(co[0]) : "v"(cr) : "memory"); SB();
  asm volatile("v_mfma_f32_16x16x32_bf16 %0, v[188:191], a[136:139], %0" : "+v"(a2[0][1]) :  : ); SB();
  asm volatile("ds_read_b64_tr_b16 v[172:173], %0 offset:12288" :  : "v"(aW[0][1][0]) : "memory"); SB();
  asm volatile("v_mfma_f32_16x16x32_bf16 %0, v[188:191], a[140:143], %0" : "+v"(a2[1][1]) :  : ); SB();
  asm volatile("ds_read_b64_tr_b16 v[174:175], %0 offset:13056" :  : "v"(aW[0][1][0]) : "memory"); SB();
  asm volatile("v_mfma_f32_16x16x32_bf16 %0, v[188:191], %1, %0" : "+v"(c2[1]) : "v"(ones) : ); SB();
  asm volatile("ds_read_b64_tr_b16 v[176:177], %0 offset:12288" :  : "v"(aW[0][1][1]) : "memory"); SB();
  asm volatile("v_mfma_f32_16x16x32_bf16 %0, v[192:195], a[136:139], %0" : "+v"(a2[0][2]) :  : ); SB();
  asm volatile("ds_read_b64_tr_b16 v[178:179], %0 offset:13056" :  : "v"(aW[0][1][1]) : "memory"); SB();
  asm volatile("v_mfma_f32_16x16x32_bf16 %0, v[192:195], a[140:143], %0" : "+v"(a2[1][2]) :  : ); SB();
  asm volatile("ds_read_b64_tr_b16 v[180:181], %0 offset:12288" :  : "v"(aW[0][1][2]) : "memory"); SB();
  asm volatile("v_mfma_f32_16x16x32_bf16 %0, v[192:195], %1, %0" : "+v"(c2[2]) : "v"(ones) : ); SB();
  asm volatile("ds_read_b64_tr_b16 v[182:183], %0 offset:13056" :  : "v"(aW[0][1][2]) : "memory"); SB();
  asm volatile("s_waitcnt lgkmcnt(9)\n\tv_mfma_f32_16x16x32_bf16 %0, v[160:163], a[144:147], %0" : "+v"(a2[0][0]) :  : ); SB();
  asm volatile("ds_read_b128 %0, %1 offset:64" : "=&v"(co[1]) : "v"(cr) : "memory"); SB();
  asm volatile("v_mfma_f32_16x16x32_bf16 %0, v[160:163], a[148:151], %0" : "+v"(a2[1][0]) :  : ); SB();
  asm volatile("ds_read_b128 %0, %1 offset:128" : "=&v"(co[2]) : "v"(cr) : "memory"); SB();
  asm volatile("v_mfma_f32_16x16x32_bf16 %0, v[160:163], %1, %0" : "+v"(c2[0]) : "v"(ones) : ); SB();
  asm volatile("global_load_ushort v248, %0, %1" :  : "v"(vBs), "s"(b_sp) : "memory"); SB();
  asm volatile("v_mfma_f32_16x16x32_bf16 %0, v[164:167], a[144:147], %0" : "+v"(a2[0][1]) :  : ); SB();
  asm volatile("buffer_load_dwordx4 v[236:239], %0, %1, %2 offen" :  : "v"(vF), "s"(rF), "s"(c2s_) : "memory"); SB();
  asm volatile("v_mfma_f32_16x16x32_bf16 %0, v[164:167], a[148:151], %0" : "+v"(a2[1][1]) :  : ); SB();
  asm volatile("v_mfma_f32_16x16x32_bf16 %0, v[164:167], %1, %0" : "+v"(c2[1]) : "v"(ones) : ); SB();
  asm volatile("v_mfma_f32_16x16x32_bf16 %0, v[168:171], a[144:147], %0" : "+v"(a2[0][2]) :  : ); SB();
  asm volatile("v_mfma_f32_16x16x32_bf16 %0, v[168:171], a[148:151], %0" : "+v"(a2[1][2]) :  : ); SB();
  asm volatile("v_mfma_f32_16x16x32_bf16 %0, v[168:171], %1, %0" : "+v"(c2[2]) : "v"(ones) : ); SB();
  asm volatile("s_waitcnt lgkmcnt(3)\n\tv_mfma_f32_16x16x32_bf16 %0, v[172:175], a[152:155], %0" : "+v"(a2[0][0]) :  : ); SB();
  asm volatile("v_mfma_f32_16x16x32_bf16 %0, v[172:175], a[156:159], %0" : "+v"(a2[1][0]) :  : ); SB();
  asm volatile("v_mfma_f32_16x16x32_bf16 %0, v[172:175], %1, %0" : "+v"(c2[0]) : "v"(ones) : ); SB();
  asm volatile("v_mfma_f32_16x16x32_bf16 %0, v[176:179], a[152:155], %0" : "+v"(a2[0][1]) :  : ); SB();
  asm volatile("v_mfma_f32_16x16x32_bf16 %0, v[176:179], a[156:159], %0" : "+v"(a2[1][1]) :  : ); SB();
  asm volatile("v_mfma_f32_16x16x32_bf16 %0, v[176:179], %1, %0" : "+v"(c2[1]) : "v"(ones) : ); SB();
  asm volatile("s_waitcnt lgkmcnt(0)\n\tv_mfma_f32_16x16x32_bf16 %0, v[180:183], a[152:155], %0" : "+v"(a2[0][2]) :  : ); SB();
  asm volatile("v_mfma_f32_16x16x32_bf16 %0, v[180:183], a[156:159], %0" : "+v"(a2[1][2]) :  : ); SB();
  asm volatile("v_mfma_f32_16x16x32_bf16 %0, v[180:183], %1, %0" : "+v"(c2[2]) : "v"(ones) : ); SB();
  ST(3);
  ST(5);
  const unsigned long long tg = ((unsigned long long)tg1 << 32) | tg0;
  asm volatile("" : "+v"(xs[0]), "+v"(xs[1]), "+v"(co[0]), "+v"(co[1]), "+v"(co[2]) :  : "memory"); SB();
  asm volatile("s_nop 3" : "+v"(a2[0][0]), "+v"(a2[0][1]), "+v"(a2[0][2]), "+v"(a2[1][0]), "+v"(a2[1][1]), "+v"(a2[1][2]), "+v"(c2[0]), "+v"(c2[1]), "+v"(c2[2]) :  : ); SB();
  for (int i = 0; i < 2; ++i) { xm[i] = xs[i][0]; xd[i] = xs[i][1]; k1[i] = xs[i][2]; k2[i] = xs[i][3]; }
  c2[0] += co[0]; c2[1] += co[1]; c2[2] += co[2];
  f32x2 ge2[2];
  auto act = [&](f32x2& t, f32x2& e, const f32x4& a, const f32x4& cs, int i, int h) {
    t = __builtin_elementwise_fma((f32x2){-xm[i], -xm[i]}, (f32x2){cs[2 * h], cs[2 * h + 1]}, (f32x2){a[2 * h], a[2 * h + 1]});
    const f32x2 u = t * __builtin_elementwise_fma(t * t, (f32x2){k2[i], k2[i]}, (f32x2){k1[i], k1[i]});
    e = (f32x2){__builtin_amdgcn_exp2f(u[0]), __builtin_amdgcn_exp2f(u[1])};
  };
  auto act2 = [&](f32x2& t, f32x2 e, int i) {
    e = __builtin_elementwise_fma(e, (f32x2){xd[i], xd[i]}, (f32x2){xd[i], xd[i]});
    t *= (f32x2){__builtin_amdgcn_rcpf(e[0]), __builtin_amdgcn_rcpf(e[1])};
  };
  f32x2 gv[2][3][2];
  float sv[2][2], pv[2];
  act(gv[0][0][0], ge2[0], a2[0][0], c2[0], 0, 0); act(gv[0][0][1], ge2[1], a2[0][0], c2[0], 0, 1);
  asm volatile("" : "+v"(gv[0][0][0]), "+v"(gv[0][0][1]), "+v"(ge2[0]), "+v"(ge2[1]));
  SB();
  asm volatile("s_add_u32 m0, %0, 24576" :  : "s"(mW) : "memory", "scc"); SB();
  asm volatile("s_nop 0\n\tbuffer_load_dwordx4 %0, %1, %2 offen lds" :  : "v"(voB[0]), "s"(rW1), "s"(kw_[0]) : "memory"); SB();
  asm volatile("buffer_load_dwordx4 %0, %1, %2 offen offset:1024 lds" :  : "v"(voB[1]), "s"(rW1), "s"(kw_[0]) : "memory"); SB();
  act2(gv[0][0][0], ge2[0], 0); act2(gv[0][0][1], ge2[1], 0);
  asm volatile("" : "+v"(gv[0][0][0]), "+v"(gv[0][0][1]));
  SB();
  asm volatile("buffer_load_dwordx4 %0, %1, %2 offen offset:2048 lds" :  : "v"(voB[2]), "s"(rW1), "s"(kw_[0]) : "memory"); SB();
  act(gv[0][1][0], ge2[0], a2[0][1], c2[1], 0, 0); act(gv[0][1][1], ge2[1], a2[0][1], c2[1], 0, 1);
  asm volatile("" : "+v"(gv[0][1][0]), "+v"(gv[0][1][1]), "+v"(ge2[0]), "+v"(ge2[1]));
  SB();
  asm volatile("s_add_u32 m0, %0, 36864" :  : "s"(mW) : "memory", "scc"); SB();
  asm volatile("s_nop 0\n\tbuffer_load_dwordx4 %0, %1, %2 offen lds" :  : "v"(voB[0]), "s"(rW1), "s"(kw_[1]) : "memory"); SB();
  asm volatile("buffer_load_dwordx4 %0, %1, %2 offen offset:1024 lds" :  : "v"(voB[1]), "s"(rW1), "s"(kw_[1]) : "memory"); SB();
  act2(gv[0][1][0], ge2[0], 0); act2(gv[0][1][1], ge2[1], 0);
  asm volatile("" : "+v"(gv[0][1][0]), "+v"(gv[0][1][1]));
  SB();
  asm volatile("buffer_load_dwordx4 %0, %1, %2 offen offset:2048 lds" :  : "v"(voB[2]), "s"(rW1), "s"(kw_[1]) : "memory"); SB();
  act(gv[0][2][0], ge2[0], a2[0][2], c2[2], 0, 0); act(gv[0][2][1], ge2[1], a2[0][2], c2[2], 0, 1);
  asm volatile("" : "+v"(gv[0][2][0]), "+v"(gv[0][2][1]), "+v"(ge2[0]), "+v"(ge2[1]));
  SB();
  asm volatile("s_add_u32 m0, %0, 49152" :  : "s"(mW) : "memory", "scc"); SB();
  asm volatile("s_nop 0\n\tbuffer_load_dwordx4 %0, %1, %2 offen lds" :  : "v"(voB[0]), "s"(rW1), "s"(kw_[2]) : "memory"); SB();
  act2(gv[0][2][0], ge2[0], 0); act2(gv[0][2][1], ge2[1], 0);
  asm volatile("" : "+v"(gv[0][2][0]), "+v"(gv[0][2][1]));
  SB();
  asm volatile("buffer_load_dwordx4 %0, %1, %2 offen offset:1024 lds" :  : "v"(voB[1]), "s"(rW1), "s"(kw_[2]) : "memory"); SB();
  act(gv[1][0][0], ge2[0], a2[1][0], c2[0], 1, 0); act(gv[1][0][1], ge2[1], a2[1][0], c2[0], 1, 1);
  asm volatile("" : "+v"(gv[1][0][0]), "+v"(gv[1][0][1]), "+v"(ge2[0]), "+v"(ge2[1]));
  SB();
  asm volatile("buffer_load_dwordx4 %0, %1, %2 offen offset:2048 lds" :  : "v"(voB[2]), "s"(rW1), "s"(kw_[2]) : "memory"); SB();
  act2(gv[1][0][0], ge2[0], 1); act2(gv[1][0][1], ge2[1], 1);
  asm volatile("" : "+v"(gv[1][0][0]), "+v"(gv[1][0][1]));
  SB();
  act(gv[1][1][0], ge2[0], a2[1][1], c2[1], 1, 0); act(gv[1][1][1], ge2[1], a2[1][1], c2[1], 1, 1);
  asm volatile("" : "+v"(gv[1][1][0]), "+v"(gv[1][1][1]), "+v"(ge2[0]), "+v"(ge2[1]));
  SB();
  asm volatile("s_add_u32 m0, %0, 61440" :  : "s"(mW) : "memory", "scc"); SB();
  asm volatile("s_nop 0\n\tbuffer_load_dwordx4 %0, %1, %2 offen lds" :  : "v"(voB[0]), "s"(rW1), "s"(kw_[3]) : "memory"); SB();
  act2(gv[1][1][0], ge2[0], 1); act2(gv[1][1][1], ge2[1], 1);
  asm volatile("" : "+v"(gv[1][1][0]), "+v"(gv[1][1][1]));
  SB();
  asm volatile("buffer_load_dwordx4 %0, %1, %2 offen offset:1024 lds" :  : "v"(voB[1]), "s"(rW1), "s"(kw_[3]) : "memory"); SB();
  act(gv[1][2][0], ge2[0], a2[1][2], c2[2], 1, 0); act(gv[1][2][1], ge2[1], a2[1][2], c2[2], 1, 1);
  asm volatile("" : "+v"(gv[1][2][0]), "+v"(gv[1][2][1]), "+v"(ge2[0]), "+v"(ge2[1]));
  SB();
  asm volatile("buffer_load_dwordx4 %0, %1, %2 offen offset:2048 lds" :  : "v"(voB[2]), "s"(rW1), "s"(kw_[3]) : "memory"); SB();
  act2(gv[1][2][0], ge2[0], 1); act2(gv[1][2][1], ge2[1], 1);
  asm volatile("" : "+v"(gv[1][2][0]), "+v"(gv[1][2][1]));
  SB();
#pragma unroll
  for (int i = 0; i < 2; ++i) {
    const unsigned p0 = __float_as_uint(gv[i][0][0][0]);
    const auto pa = __builtin_amdgcn_permlane16_swap(p0, p0, false, false);
    const auto pb = __builtin_amdgcn_permlane32_swap(pa[0], pa[0], false, false);
    pv[i] = __uint_as_float(pb[0]);
  }
  asm volatile("" : "+v"(pv[0]), "+v"(pv[1]));
  f32x2 s1[2], s2[2];
  SB();
  asm volatile("s_add_u32 m0, %0, 73728" :  : "s"(mW) : "memory", "scc"); SB();
  asm volatile("s_nop 0\n\tbuffer_load_dwordx4 %0, %1, %2 offen lds" :  : "v"(voB[0]), "s"(rW1), "s"(kw_[4]) : "memory"); SB();
#pragma unroll
  for (int m = 0; m < 4; ++m) {
    const int i = m & 1, n = 0 + (m >> 1);
    const f32x2 d = gv[i][n >> 1][n & 1] - (f32x2){pv[i], pv[i]};
    s1[i] = n ? s1[i] + d : d; s2[i] = n ? __builtin_elementwise_fma(d, d, s2[i]) : d * d;
  }
  asm volatile("" : "+v"(s1[0]), "+v"(s1[1]), "+v"(s2[0]), "+v"(s2[1]));
  SB();
  asm volatile("buffer_load_dwordx4 %0, %1, %2 offen offset:1024 lds" :  : "v"(voB[1]), "s"(rW1), "s"(kw_[4]) : "memory"); SB();
#pragma unroll
  for (int m = 0; m < 4; ++m) {
    const int i = m & 1, n = 2 + (m >> 1);
    const f32x2 d = gv[i][n >> 1][n & 1] - (f32x2){pv[i], pv[i]};
    s1[i] = n ? s1[i] + d : d; s2[i] = n ? __builtin_elementwise_fma(d, d, s2[i]) : d * d;
  }
  asm volatile("" : "+v"(s1[0]), "+v"(s1[1]), "+v"(s2[0]), "+v"(s2[1]));
  SB();
#pragma unroll
  for (int m = 0; m < 4; ++m) {
    const int i = m & 1, n = 4 + (m >> 1);
    const f32x2 d = gv[i][n >> 1][n & 1] - (f32x2){pv[i], pv[i]};
    s1[i] = n ? s1[i] + d : d; s2[i] = n ? __builtin_elementwise_fma(d, d, s2[i]) : d * d;
  }
  asm volatile("" : "+v"(s1[0]), "+v"(s1[1]), "+v"(s2[0]), "+v"(s2[1]));
  SB();
  for (int i = 0; i < 2; ++i) { sv[i][0] = s1[i][0] + s1[i][1]; sv[i][1] = s2[i][0] + s2[i][1]; }
  {
    const float t1 = red16(sel32(sv[0][0], sv[1][0])), t2 = red16(sel32(sv[0][1], sv[1][1]));
    const float dl = t1 * (1.0f / 48.0f);
    const u32x4 so = {__float_as_uint((lane < 32 ? pv[0] : pv[1]) + dl), __float_as_uint(fmaxf(t2 - t1 * dl, 0.f)), tg0, tg1};
    SB();
  asm volatile("s_mov_b32 exec_lo, 0xffff\n\ts_mov_b32 exec_hi, 0xffff\n\tbuffer_store_dwordx4 %1, %0, %2, %3 offen offset:0 sc1\n\ts_mov_b64 exec, -1\n\ts_nop 0" :  : "v"(vo), "v"(so), "s"(rWS), "s"(0u) : "memory"); SB();
  }
  ST(6);
  unsigned zp[2][3][2];
#pragma unroll
  for (int n = 0; n < 12; ++n) { zp[n / 6][(n >> 1) % 3][n & 1] = pk2(gv[n / 6][(n >> 1) % 3][n & 1]); asm volatile("" : "+v"(zp[n / 6][(n >> 1) % 3][n & 1])); }
  SB();
  asm volatile("s_waitcnt vmcnt(22)\n\ts_barrier" :  :  : "memory"); SB();
  ST(2);
  asm volatile("ds_read_b64_tr_b16 %0, %1 offset:0" : "=&v"(wt[0]) : "v"(oa) : "memory"); SB();
  asm volatile("ds_read_b64_tr_b16 %0, %1 offset:512" : "=&v"(wt[1]) : "v"(oa) : "memory"); SB();
  asm volatile("ds_read_b64_tr_b16 %0, %1 offset:0" : "=&v"(kp[0]) : "v"(ka) : "memory"); SB();
  asm volatile("ds_read_b64_tr_b16 %0, %1 offset:640" : "=&v"(kc[0]) : "v"(ka) : "memory"); SB();
  asm volatile("ds_read_b64_tr_b16 %0, %1 offset:4096" : "=&v"(vv[0]) : "v"(ka) : "memory"); SB();
  asm volatile("ds_read_b64 %0, %1 offset:0" : "=&v"(sw[0]) : "v"(sa) : "memory"); SB();
  asm volatile("ds_read_b64_tr_b16 %0, %1 offset:32" : "=&v"(kp[1]) : "v"(ka) : "memory"); SB();
  asm volatile("ds_read_b64_tr_b16 %0, %1 offset:672" : "=&v"(kc[1]) : "v"(ka) : "memory"); SB();
  asm volatile("ds_read_b64_tr_b16 %0, %1 offset:4128" : "=&v"(vv[1]) : "v"(ka) : "memory"); SB();
  asm volatile("ds_read_b64 %0, %1 offset:128" : "=&v"(sw[1]) : "v"(sa) : "memory"); SB();
  asm volatile("ds_read_b64_tr_b16 %0, %1 offset:64" : "=&v"(kp[2]) : "v"(ka) : "memory"); SB();
  asm volatile("ds_read_b64_tr_b16 %0, %1 offset:704" : "=&v"(kc[2]) : "v"(ka) : "memory"); SB();
  asm volatile("ds_read_b64_tr_b16 %0, %1 offset:4160" : "=&v"(vv[2]) : "v"(ka) : "memory"); SB();
  asm volatile("ds_read_b64 %0, %1 offset:256" : "=&v"(sw[2]) : "v"(sa) : "memory"); SB();
  asm volatile("ds_read_b64_tr_b16 %0, %1 offset:96" : "=&v"(kp[3]) : "v"(ka) : "memory"); SB();
  asm volatile("ds_read_b64_tr_b16 %0, %1 offset:736" : "=&v"(kc[3]) : "v"(ka) : "memory"); SB();
  asm volatile("ds_read_b64_tr_b16 %0, %1 offset:4192" : "=&v"(vv[3]) : "v"(ka) : "memory"); SB();
  asm volatile("ds_read_b64 %0, %1 offset:384" : "=&v"(sw[3]) : "v"(sa) : "memory"); SB();
  asm volatile("ds_read_b64_tr_b16 %0, %1 offset:128" : "=&v"(kp[4]) : "v"(ka) : "memory"); SB();
  asm volatile("ds_read_b64_tr_b16 %0, %1 offset:768" : "=&v"(kc[4]) : "v"(ka) : "memory"); SB();
  asm volatile("ds_read_b64_tr_b16 %0, %1 offset:4224" : "=&v"(vv[4]) : "v"(ka) : "memory"); SB();
  asm volatile("ds_read_b64 %0, %1 offset:512" : "=&v"(sw[4]) : "v"(sa) : "memory"); SB();
  asm volatile("s_waitcnt lgkmcnt(10)" : "+v"(kp[0]), "+v"(kc[0]), "+v"(vv[0]), "+v"(sw[0]), "+v"(wt[0]), "+v"(wt[1]) :  : "memory"); SB();
  asm volatile("v_alignbit_b32 %4, %14, %14, 16\n\tv_alignbit_b32 %5, %10, %9, 16\n\tv_alignbit_b32 %6, %11, %10, 16\n\tv_alignbit_b32 %7, %13, %13, 16\n\tv_alignbit_b32 %8, %12, %11, 16\n\tv_dot2_f32_bf16 %0, %5, %4, 0\n\tv_dot2_f32_bf16 %1, %10, %4, 0\n\tv_dot2_f32_bf16 %2, %6, %4, 0\n\tv_dot2_f32_bf16 %3, %11, %4, 0\n\tv_dot2_f32_bf16 %0, %6, %7, %0\n\tv_dot2_f32_bf16 %1, %11, %7, %1\n\tv_dot2_f32_bf16 %2, %8, %7, %2\n\tv_dot2_f32_bf16 %3, %12, %7, %3" : "=&v"(kt[0][0]), "=&v"(kt[0][1]), "=&v"(kt[1][0]), "=&v"(kt[1][1]), "=&v"(ku[0]), "=&v"(ku[1]), "=&v"(ku[2]), "=&v"(ku[3]), "=&v"(ku[4]) : "v"(kp[0][0]), "v"(kp[0][1]), "v"(kc[0][0]), "v"(kc[0][1]), "v"(sw[0][0]), "v"(sw[0][1]) : ); SB();
  asm volatile("v_lshlrev_b32 %0, 16, %4\n\tv_and_b32 %1, 0xffff0000, %4\n\tv_lshlrev_b32 %2, 16, %5\n\tv_and_b32 %3, 0xffff0000, %5" : "=&v"(kf[0][0]), "=&v"(kf[0][1]), "=&v"(kf[1][0]), "=&v"(kf[1][1]) : "v"(vv[0][0]), "v"(vv[0][1]) : ); SB();
  kt[0] *= kf[0]; kt[1] *= kf[1]; SB();
  ko[0] = (u32x2){pk2(kt[0]), pk2(kt[1])};
  asm volatile("" : "+v"(ko[0]));
  SB();
  asm volatile("ds_write_b64 %0, %1 offset:0" :  : "v"(kw), "v"(ko[0]) : "memory"); SB();
  asm volatile("" : "+v"(kp[1]), "+v"(kc[1]), "+v"(vv[1]), "+v"(sw[1]) :  : "memory"); SB();
  asm volatile("v_alignbit_b32 %4, %14, %14, 16\n\tv_alignbit_b32 %5, %10, %9, 16\n\tv_alignbit_b32 %6, %11, %10, 16\n\tv_alignbit_b32 %7, %13, %13, 16\n\tv_alignbit_b32 %8, %12, %11, 16\n\tv_dot2_f32_bf16 %0, %5, %4, 0\n\tv_dot2_f32_bf16 %1, %10, %4, 0\n\tv_dot2_f32_bf16 %2, %6, %4, 0\n\tv_dot2_f32_bf16 %3, %11, %4, 0\n\tv_dot2_f32_bf16 %0, %6, %7, %0\n\tv_dot2_f32_bf16 %1, %11, %7, %1\n\tv_dot2_f32_bf16 %2, %8, %7, %2\n\tv_dot2_f32_bf16 %3, %12, %7, %3" : "=&v"(kt[0][0]), "=&v"(kt[0][1]), "=&v"(kt[1][0]), "=&v"(kt[1][1]), "=&v"(ku[0]), "=&v"(ku[1]), "=&v"(ku[2]), "=&v"(ku[3]), "=&v"(ku[4]) : "v"(kp[1][0]), "v"(kp[1][1]), "v"(kc[1][0]), "v"(kc[1][1]), "v"(sw[1][0]), "v"(sw[1][1]) : ); SB();
  asm volatile("buffer_load_dwordx4 %0, %1, %2 offen offset:2048 lds" :  : "v"(voB[2]), "s"(rW1), "s"(kw_[4]) : "memory"); SB();
  asm volatile("v_lshlrev_b32 %0, 16, %4\n\tv_and_b32 %1, 0xffff0000, %4\n\tv_lshlrev_b32 %2, 16, %5\n\tv_and_b32 %3, 0xffff0000, %5" : "=&v"(kf[0][0]), "=&v"(kf[0][1]), "=&v"(kf[1][0]), "=&v"(kf[1][1]) : "v"(vv[1][0]), "v"(vv[1][1]) : ); SB();
  kt[0] *= kf[0]; kt[1] *= kf[1]; SB();
  ko[1] = (u32x2){pk2(kt[0]), pk2(kt[1])};
  asm volatile("" : "+v"(ko[1]));
  SB();
  asm volatile("s_add_u32 m0, %0, 86016" :  : "s"(mW) : "memory", "scc"); SB();
  asm volatile("s_nop 0\n\tbuffer_load_dwordx4 %0, %1, %2 offen lds" :  : "v"(voB[0]), "s"(rW1), "s"(kw_[5]) : "memory"); SB();
  asm volatile("ds_write_b64 %0, %1 offset:2304" :  : "v"(kw), "v"(ko[1]) : "memory"); SB();
  asm volatile("s_waitcnt lgkmcnt(2)" : "+v"(kp[2]), "+v"(kc[2]), "+v"(vv[2]), "+v"(sw[2]) :  : "memory"); SB();
  asm volatile("v_alignbit_b32 %4, %14, %14, 16\n\tv_alignbit_b32 %5, %10, %9, 16\n\tv_alignbit_b32 %6, %11, %10, 16\n\tv_alignbit_b32 %7, %13, %13, 16\n\tv_alignbit_b32 %8, %12, %11, 16\n\tv_dot2_f32_bf16 %0, %5, %4, 0\n\tv_dot2_f32_bf16 %1, %10, %4, 0\n\tv_dot2_f32_bf16 %2, %6, %4, 0\n\tv_dot2_f32_bf16 %3, %11, %4, 0\n\tv_dot2_f32_bf16 %0, %6, %7, %0\n\tv_dot2_f32_bf16 %1, %11, %7, %1\n\tv_dot2_f32_bf16 %2, %8, %7, %2\n\tv_dot2_f32_bf16 %3, %12, %7, %3" : "=&v"(kt[0][0]), "=&v"(kt[0][1]), "=&v"(kt[1][0]), "=&v"(kt[1][1]), "=&v"(ku[0]), "=&v"(ku[1]), "=&v"(ku[2]), "=&v"(ku[3]), "=&v"(ku[4]) : "v"(kp[2][0]), "v"(kp[2][1]), "v"(kc[2][0]), "v"(kc[2][1]), "v"(sw[2][0]), "v"(sw[2][1]) : ); SB();
  asm volatile("buffer_load_dwordx4 %0, %1, %2 offen offset:1024 lds" :  : "v"(voB[1]), "s"(rW1), "s"(kw_[5]) : "memory"); SB();
  asm volatile("v_lshlrev_b32 %0, 16, %4\n\tv_and_b32 %1, 0xffff0000, %4\n\tv_lshlrev_b32 %2, 16, %5\n\tv_and_b32 %3, 0xffff0000, %5" : "=&v"(kf[0][0]), "=&v"(kf[0][1]), "=&v"(kf[1][0]), "=&v"(kf[1][1]) : "v"(vv[2][0]), "v"(vv[2][1]) : ); SB();
  kt[0] *= kf[0]; kt[1] *= kf[1]; SB();
  ko[2] = (u32x2){pk2(kt[0]), pk2(kt[1])};
  asm volatile("" : "+v"(ko[2]));
  SB();
  asm volatile("buffer_load_dwordx4 %0, %1, %2 offen offset:2048 lds" :  : "v"(voB[2]), "s"(rW1), "s"(kw_[5]) : "memory"); SB();
  asm volatile("ds_write_b64 %0, %1 offset:4608" :  : "v"(kw), "v"(ko[2]) : "memory"); SB();
  asm volatile("" : "+v"(kp[3]), "+v"(kc[3]), "+v"(vv[3]), "+v"(sw[3]) :  : "memory"); SB();
  asm volatile("v_alignbit_b32 %4, %14, %14, 16\n\tv_alignbit_b32 %5, %10, %9, 16\n\tv_alignbit_b32 %6, %11, %10, 16\n\tv_alignbit_b32 %7, %13, %13, 16\n\tv_alignbit_b32 %8, %12, %11, 16\n\tv_dot2_f32_bf16 %0, %5, %4, 0\n\tv_dot2_f32_bf16 %1, %10, %4, 0\n\tv_dot2_f32_bf16 %2, %6, %4, 0\n\tv_dot2_f32_bf16 %3, %11, %4, 0\n\tv_dot2_f32_bf16 %0, %6, %7, %0\n\tv_dot2_f32_bf16 %1, %11, %7, %1\n\tv_dot2_f32_bf16 %2, %8, %7, %2\n\tv_dot2_f32_bf16 %3, %12, %7, %3" : "=&v"(kt[0][0]), "=&v"(kt[0][1]), "=&v"(kt[1][0]), "=&v"(kt[1][1]), "=&v"(ku[0]), "=&v"(ku[1]), "=&v"(ku[2]), "=&v"(ku[3]), "=&v"(ku[4]) : "v"(kp[3][0]), "v"(kp[3][1]), "v"(kc[3][0]), "v"(kc[3][1]), "v"(sw[3][0]), "v"(sw[3][1]) : ); SB();
  asm volatile("s_add_u32 m0, %0, 0" :  : "s"(mW) : "memory", "scc"); SB();
  asm volatile("s_nop 0\n\tbuffer_load_dwordx4 %0, %1, %2 offen lds" :  : "v"(voB[0]), "s"(rW1), "s"(kw_[6]) : "memory"); SB();
  asm volatile("v_lshlrev_b32 %0, 16, %4\n\tv_and_b32 %1, 0xffff0000, %4\n\tv_lshlrev_b32 %2, 16, %5\n\tv_and_b32 %3, 0xffff0000, %5" : "=&v"(kf[0][0]), "=&v"(kf[0][1]), "=&v"(kf[1][0]), "=&v"(kf[1][1]) : "v"(vv[3][0]), "v"(vv[3][1]) : ); SB();
  kt[0] *= kf[0]; kt[1] *= kf[1]; SB();
  ko[3] = (u32x2){pk2(kt[0]), pk2(kt[1])};
  asm volatile("" : "+v"(ko[3]));
  SB();
  asm volatile("buffer_load_dwordx4 %0, %1, %2 offen offset:1024 lds" :  : "v"(voB[1]), "s"(rW1), "s"(kw_[6]) : "memory"); SB();
  asm volatile("ds_write_b64 %0, %1 offset:6912" :  : "v"(kw), "v"(ko[3]) : "memory"); SB();
  asm volatile("" : "+v"(kp[4]), "+v"(kc[4]), "+v"(vv[4]), "+v"(sw[4]) :  : "memory"); SB();
  asm volatile("v_alignbit_b32 %4, %14, %14, 16\n\tv_alignbit_b32 %5, %10, %9, 16\n\tv_alignbit_b32 %6, %11, %10, 16\n\tv_alignbit_b32 %7, %13, %13, 16\n\tv_alignbit_b32 %8, %12, %11, 16\n\tv_dot2_f32_bf16 %0, %5, %4, 0\n\tv_dot2_f32_bf16 %1, %10, %4, 0\n\tv_dot2_f32_bf16 %2, %6, %4, 0\n\tv_dot2_f32_bf16 %3, %11, %4, 0\n\tv_dot2_f32_bf16 %0, %6, %7, %0\n\tv_dot2_f32_bf16 %1, %11, %7, %1\n\tv_dot2_f32_bf16 %2, %8, %7, %2\n\tv_dot2_f32_bf16 %3, %12, %7, %3" : "=&v"(kt[0][0]), "=&v"(kt[0][1]), "=&v"(kt[1][0]), "=&v"(kt[1][1]), "=&v"(ku[0]), "=&v"(ku[1]), "=&v"(ku[2]), "=&v"(ku[3]), "=&v"(ku[4]) : "v"(kp[4][0]), "v"(kp[4][1]), "v"(kc[4][0]), "v"(kc[4][1]), "v"(sw[4][0]), "v"(sw[4][1]) : ); SB();
  asm volatile("buffer_load_dwordx4 %0, %1, %2 offen offset:2048 lds" :  : "v"(voB[2]), "s"(rW1), "s"(kw_[6]) : "memory"); SB();
  asm volatile("v_lshlrev_b32 %0, 16, %4\n\tv_and_b32 %1, 0xffff0000, %4\n\tv_lshlrev_b32 %2, 16, %5\n\tv_and_b32 %3, 0xffff0000, %5" : "=&v"(kf[0][0]), "=&v"(kf[0][1]), "=&v"(kf[1][0]), "=&v"(kf[1][1]) : "v"(vv[4][0]), "v"(vv[4][1]) : ); SB();
  kt[0] *= kf[0]; kt[1] *= kf[1]; SB();
  ko[4] = (u32x2){pk2(kt[0]), pk2(kt[1])};
  asm volatile("" : "+v"(ko[4]));
  SB();
  asm volatile("ds_write_b64 %0, %1 offset:9216" :  : "v"(kw), "v"(ko[4]) : "memory"); SB();
  ST(16);
  asm volatile("s_waitcnt vmcnt(16) lgkmcnt(0)\n\ts_barrier" :  :  : "memory"); SB();
  asm volatile("ds_read_b128 %0, %1 offset:0" : "=&v"(kq[0]) : "v"(kr) : "memory"); SB();
  asm volatile("ds_read_b128 %0, %1 offset:1152" : "=&v"(kq[1]) : "v"(kr) : "memory"); SB();
  asm volatile("ds_read_b128 %0, %1 offset:2304" : "=&v"(kq[2]) : "v"(kr) : "memory"); SB();
  asm volatile("ds_read_b64_tr_b16 v[184:185], %0 offset:24576" :  : "v"(aW[0][0][0]) : "memory"); SB();
  asm volatile("ds_read_b64_tr_b16 v[186:187], %0 offset:25344" :  : "v"(aW[0][0][0]) : "memory"); SB();
  asm volatile("ds_read_b64_tr_b16 v[188:189], %0 offset:24576" :  : "v"(aW[0][0][1]) : "memory"); SB();
  asm volatile("ds_read_b64_tr_b16 v[190:191], %0 offset:25344" :  : "v"(aW[0][0][1]) : "memory"); SB();
  asm volatile("ds_read_b64_tr_b16 v[192:193], %0 offset:24576" :  : "v"(aW[0][0][2]) : "memory"); SB();
  asm volatile("ds_read_b64_tr_b16 v[194:195], %0 offset:25344" :  : "v"(aW[0][0][2]) : "memory"); SB();
  ST(7);
  asm volatile("s_waitcnt lgkmcnt(6)" : "+v"(kq[0]), "+v"(kq[1]), "+v"(kq[2]) :  : "memory"); SB();
  unsigned kz[3] = {kq[0][3], kq[1][3] | (w + 1) >> 2, kq[2][3] | (w + 1) >> 2};
  asm volatile("" : "+v"(kz[0]), "+v"(kz[1]), "+v"(kz[2])); SB();
  asm volatile("buffer_load_dwordx4 v[196:199], %0, %1, %2 offen offset:0 sc1" :  : "v"(vS), "s"(rWS), "s"(0u) : "memory"); SB();
  asm volatile("buffer_load_dwordx4 v[200:203], %0, %1, %2 offen offset:2048 sc1" :  : "v"(vS), "s"(rWS), "s"(0u) : "memory"); SB();
  asm volatile("s_waitcnt lgkmcnt(3)\n\tv_mfma_f32_16x16x32_bf16 %0, v[184:187], a[0:3], 0" : "=&v"(a1[0][0]) :  : ); SB();
  asm volatile("ds_read_b64_tr_b16 v[160:161], %0 offset:24576" :  : "v"(aW[0][1][0]) : "memory"); SB();
  asm volatile("v_mfma_f32_16x16x32_bf16 %0, v[184:187], a[4:7], 0" : "=&v"(a1[1][0]) :  : ); SB();
  asm volatile("ds_read_b64_tr_b16 v[162:163], %0 offset:25344" :  : "v"(aW[0][1][0]) : "memory"); SB();
  asm volatile("v_mfma_f32_16x16x32_bf16 %0, v[184:187], %1, 0" : "=&v"(c1[0]) : "v"(ones) : ); SB();
  asm volatile("ds_read_b64_tr_b16 v[164:165], %0 offset:24576" :  : "v"(aW[0][1][1]) : "memory"); SB();
  asm volatile("s_waitcnt lgkmcnt(3)\n\tv_mfma_f32_16x16x32_bf16 %0, v[188:191], a[0:3], 0" : "=&v"(a1[0][1]) :  : ); SB();
  asm volatile("ds_read_b64_tr_b16 v[166:167], %0 offset:25344" :  : "v"(aW[0][1][1]) : "memory"); SB();
  asm volatile("v_mfma_f32_16x16x32_bf16 %0, v[188:191], a[4:7], 0" : "=&v"(a1[1][1]) :  : ); SB();
  asm volatile("ds_read_b64_tr_b16 v[168:169], %0 offset:24576" :  : "v"(aW[0][1][2]) : "memory"); SB();
  asm volatile("v_mfma_f32_16x16x32_bf16 %0, v[188:191], %1, 0" : "=&v"(c1[1]) : "v"(ones) : ); SB();
  asm volatile("ds_read_b64_tr_b16 v[170:171], %0 offset:25344" :  : "v"(aW[0][1][2]) : "memory"); SB();
  asm volatile("v_mfma_f32_16x16x32_bf16 %0, v[192:195], a[0:3], 0" : "=&v"(a1[0][2]) :  : ); SB();
  asm volatile("ds_read_b64_tr_b16 v[172:173], %0 offset:36864" :  : "v"(aW[0][0][0]) : "memory"); SB();
  asm volatile("v_mfma_f32_16x16x32_bf16 %0, v[192:195], a[4:7], 0" : "=&v"(a1[1][2]) :  : ); SB();
  asm volatile("ds_read_b64_tr_b16 v[174:175], %0 offset:37632" :  : "v"(aW[0][0][0]) : "memory"); SB();
  asm volatile("v_mfma_f32_16x16x32_bf16 %0, v[192:195], %1, 0" : "=&v"(c1[2]) : "v"(ones) : ); SB();
  asm volatile("ds_read_b64_tr_b16 v[176:177], %0 offset:36864" :  : "v"(aW[0][0][1]) : "memory"); SB();
  asm volatile("s_waitcnt lgkmcnt(7)\n\tv_mfma_f32_16x16x32_bf16 %0, v[160:163], a[8:11], %0" : "+v"(a1[0][0]) :  : ); SB();
  asm volatile("ds_read_b64_tr_b16 v[178:179], %0 offset:37632" :  : "v"(aW[0][0][1]) : "memory"); SB();
  asm volatile("v_mfma_f32_16x16x32_bf16 %0, v[160:163], a[12:15], %0" : "+v"(a1[1][0]) :  : ); SB();
  asm volatile("ds_read_b64_tr_b16 v[180:181], %0 offset:36864" :  : "v"(aW[0][0][2]) : "memory"); SB();
  asm volatile("s_waitcnt lgkmcnt(7)\n\tv_mfma_f32_16x16x32_bf16 %0, v[164:167], a[8:11], %0" : "+v"(a1[0][1]) :  : ); SB();
  asm volatile("ds_read_b64_tr_b16 v[182:183], %0 offset:37632" :  : "v"(aW[0][0][2]) : "memory"); SB();
  asm volatile("v_mfma_f32_16x16x32_bf16 %0, v[164:167], a[12:15], %0" : "+v"(a1[1][1]) :  : ); SB();
  asm volatile("ds_read_b64_tr_b16 v[184:185], %0 offset:36864" :  : "v"(aW[0][1][0]) : "memory"); SB();
  asm volatile("s_waitcnt lgkmcnt(7)\n\tv_mfma_f32_16x16x32_bf16 %0, v[168:171], a[8:11], %0" : "+v"(a1[0][2]) :  : ); SB();
  asm volatile("ds_read_b64_tr_b16 v[186:187], %0 offset:37632" :  : "v"(aW[0][1][0]) : "memory"); SB();
  asm volatile("buffer_load_dwordx4 v[204:207], %0, %1, %2 offen offset:0 sc1" :  : "v"(vS), "s"(rWS), "s"(4096u) : "memory"); SB();
  asm volatile("v_mfma_f32_16x16x32_bf16 %0, v[168:171], a[12:15], %0" : "+v"(a1[1][2]) :  : ); SB();
  asm volatile("ds_read_b64_tr_b16 v[188:189], %0 offset:36864" :  : "v"(aW[0][1][1]) : "memory"); SB();
  asm volatile("s_waitcnt lgkmcnt(7)\n\tv_mfma_f32_16x16x32_bf16 %0, v[172:175], a[16:19], %0" : "+v"(a1[0][0]) :  : ); SB();
  asm volatile("ds_read_b64_tr_b16 v[190:191], %0 offset:37632" :  : "v"(aW[0][1][1]) : "memory"); SB();
  asm volatile("v_mfma_f32_16x16x32_bf16 %0, v[172:175], a[20:23], %0" : "+v"(a1[1][0]) :  : ); SB();
  asm volatile("ds_read_b64_tr_b16 v[192:193], %0 offset:36864" :  : "v"(aW[0][1][2]) : "memory"); SB();
  asm volatile("v_mfma_f32_16x16x32_bf16 %0, v[172:175], %1, %0" : "+v"(c1[0]) : "v"(ones) : ); SB();
  asm volatile("ds_read_b64_tr_b16 v[194:195], %0 offset:37632" :  : "v"(aW[0][1][2]) : "memory"); SB();
  asm volatile("s_waitcnt lgkmcnt(8)\n\tv_mfma_f32_16x16x32_bf16 %0, v[176:179], a[16:19], %0" : "+v"(a1[0][1]) :  : ); SB();
  asm volatile("s_waitcnt vmcnt(13)\n\ts_barrier" :  :  : "memory"); SB();
  asm volatile("ds_read_b64_tr_b16 v[160:161], %0 offset:0" :  : "v"(aW[1][0][0]) : "memory"); SB();
  asm volatile("buffer_load_dwordx4 v[208:211], %0, %1, %2 offen offset:2048 sc1" :  : "v"(vS), "s"(rWS), "s"(4096u) : "memory"); SB();
  asm volatile("v_mfma_f32_16x16x32_bf16 %0, v[176:179], a[20:23], %0" : "+v"(a1[1][1]) :  : ); SB();
  asm volatile("ds_read_b64_tr_b16 v[162:163], %0 offset:768" :  : "v"(aW[1][0][0]) : "memory"); SB();
  asm volatile("v_mfma_f32_16x16x32_bf16 %0, v[176:179], %1, %0" : "+v"(c1[1]) : "v"(ones) : ); SB();
  asm volatile("ds_read_b64_tr_b16 v[164:165], %0 offset:0" :  : "v"(aW[1][0][1]) : "memory"); SB();
  asm volatile("buffer_store_dwordx4 %1, %0, %2, %3 offen offset:0 sc1\n\ts_nop 1" :  : "v"(vKV), "v"(kq[0]), "s"(rWS), "s"(0u) : "memory"); SB();
  asm volatile("s_waitcnt lgkmcnt(6)\n\tv_mfma_f32_16x16x32_bf16 %0, v[180:183], a[16:19], %0" : "+v"(a1[0][2]) :  : ); SB();
  asm volatile("ds_read_b64_tr_b16 v[166:167], %0 offset:768" :  : "v"(aW[1][0][1]) : "memory"); SB();
  asm volatile("v_mfma_f32_16x16x32_bf16 %0, v[180:183], a[20:23], %0" : "+v"(a1[1][2]) :  : ); SB();
  asm volatile("ds_read_b64_tr_b16 v[168:169], %0 offset:0" :  : "v"(aW[1][0][2]) : "memory"); SB();
  asm volatile("v_mfma_f32_16x16x32_bf16 %0, v[180:183], %1, %0" : "+v"(c1[2]) : "v"(ones) : ); SB();
  asm volatile("ds_read_b64_tr_b16 v[170:171], %0 offset:768" :  : "v"(aW[1][0][2]) : "memory"); SB();
  asm volatile("v_mfma_f32_16x16x32_bf16 %0, v[184:187], a[24:27], %0" : "+v"(a1[0][0]) :  : ); SB();
  asm volatile("ds_read_b64_tr_b16 v[172:173], %0 offset:0" :  : "v"(aW[1][1][0]) : "memory"); SB();
  asm volatile("buffer_store_dwordx4 %1, %0, %2, %3 offen offset:0 sc1\n\ts_nop 1" :  : "v"(vKV), "v"(kq[1]), "s"(rWS), "s"(w3k[0]) : "memory"); SB();
  asm volatile("v_mfma_f32_16x16x32_bf16 %0, v[184:187], a[28:31], %0" : "+v"(a1[1][0]) :  : ); SB();
  asm volatile("ds_read_b64_tr_b16 v[174:175], %0 offset:768" :  : "v"(aW[1][1][0]) : "memory"); SB();
  asm volatile("s_waitcnt lgkmcnt(8)\n\tv_mfma_f32_16x16x32_bf16 %0, v[188:191], a[24:27], %0" : "+v"(a1[0][1]) :  : ); SB();
  asm volatile("ds_read_b64_tr_b16 v[176:177], %0 offset:0" :  : "v"(aW[1][1][1]) : "memory"); SB();
  asm volatile("v_mfma_f32_16x16x32_bf16 %0, v[188:191], a[28:31], %0" : "+v"(a1[1][1]) :  : ); SB();
  asm volatile("ds_read_b64_tr_b16 v[178:179], %0 offset:768" :  : "v"(aW[1][1][1]) : "memory"); SB();
  asm volatile("v_mfma_f32_16x16x32_bf16 %0, v[192:195], a[24:27], %0" : "+v"(a1[0][2]) :  : ); SB();
  asm volatile("ds_read_b64_tr_b16 v[180:181], %0 offset:0" :  : "v"(aW[1][1][2]) : "memory"); SB();
  asm volatile("buffer_store_dwordx4 %1, %0, %2, %3 offen offset:0 sc1\n\ts_nop 1" :  : "v"(vKV), "v"(kq[2]), "s"(rWS), "s"(w3k[1]) : "memory"); SB();
  asm volatile("v_mfma_f32_16x16x32_bf16 %0, v[192:195], a[28:31], %0" : "+v"(a1[1][2]) :  : ); SB();
  asm volatile("ds_read_b64_tr_b16 v[182:183], %0 offset:768" :  : "v"(aW[1][1][2]) : "memory"); SB();
  asm volatile("s_waitcnt lgkmcnt(8)\n\tv_mfma_f32_16x16x32_bf16 %0, v[160:163], a[32:35], %0" : "+v"(a1[0][0]) :  : ); SB();
  asm volatile("ds_read_b64_tr_b16 v[184:185], %0 offset:12288" :  : "v"(aW[1][0][0]) : "memory"); SB();
  asm volatile("v_mfma_f32_16x16x32_bf16 %0, v[160:163], a[36:39], %0" : "+v"(a1[1][0]) :  : ); SB();
  asm volatile("ds_read_b64_tr_b16 v[186:187], %0 offset:13056" :  : "v"(aW[1][0][0]) : "memory"); SB();
  asm volatile("v_mfma_f32_16x16x32_bf16 %0, v[160:163], %1, %0" : "+v"(c1[0]) : "v"(ones) : ); SB();
  asm volatile("ds_read_b64_tr_b16 v[188:189], %0 offset:12288" :  : "v"(aW[1][0][1]) : "memory"); SB();
  asm volatile("v_mfma_f32_16x16x32_bf16 %0, v[164:167], a[32:35], %0" : "+v"(a1[0][1]) :  : ); SB();
  asm volatile("ds_read_b64_tr_b16 v[190:191], %0 offset:13056" :  : "v"(aW[1][0][1]) : "memory"); SB();
  asm volatile("buffer_store_dwordx4 %1, %0, %2, %3 offen offset:0 sc1\n\ts_nop 1" :  : "v"(vWO), "v"(((u32x4){wt[0][0], wt[0][1], wt[1][0], wt[1][1]})), "s"(rWS), "s"(0u) : "memory"); SB();
  asm volatile("v_mfma_f32_16x16x32_bf16 %0, v[164:167], a[36:39], %0" : "+v"(a1[1][1]) :  : ); SB();
  asm volatile("ds_read_b64_tr_b16 v[192:193], %0 offset:12288" :  : "v"(aW[1][0][2]) : "memory"); SB();
  asm volatile("s_add_u32 m0, %0, 12288" :  : "s"(mW) : "memory", "scc"); SB();
  asm volatile("v_mfma_f32_16x16x32_bf16 %0, v[164:167], %1, %0" : "+v"(c1[1]) : "v"(ones) : ); SB();
  asm volatile("ds_read_b64_tr_b16 v[194:195], %0 offset:13056" :  : "v"(aW[1][0][2]) : "memory"); SB();
  asm volatile("s_waitcnt lgkmcnt(8)\n\tv_mfma_f32_16x16x32_bf16 %0, v[168:171], a[32:35], %0" : "+v"(a1[0][2]) :  : ); SB();
  ca = lb + 57344 + w * 192 + g4 * 16; asm volatile("" : "+v"(ca)); SB();
  asm volatile("v_mfma_f32_16x16x32_bf16 %0, v[168:171], a[36:39], %0" : "+v"(a1[1][2]) :  : ); SB();
  asm volatile("buffer_load_dwordx4 %0, %1, %2 offen lds" :  : "v"(voB[0]), "s"(rW1), "s"(kw_[7]) : "memory"); SB();
  asm volatile("v_mfma_f32_16x16x32_bf16 %0, v[168:171], %1, %0" : "+v"(c1[2]) : "v"(ones) : ); SB();
  cr = lb + 57344 + (w ^ 2) * 192 + g4 * 16; asm volatile("" : "+v"(cr)); SB();
  asm volatile("v_mfma_f32_16x16x32_bf16 %0, v[172:175], a[40:43], %0" : "+v"(a1[0][0]) :  : ); SB();
  asm volatile("ds_read_b64_tr_b16 v[160:161], %0 offset:12288" :  : "v"(aW[1][1][0]) : "memory"); SB();
  asm volatile("v_mfma_f32_16x16x32_bf16 %0, v[172:175], a[44:47], %0" : "+v"(a1[1][0]) :  : ); SB();
  asm volatile("ds_read_b64_tr_b16 v[162:163], %0 offset:13056" :  : "v"(aW[1][1][0]) : "memory"); SB();
  asm volatile("v_lshlrev_b32 %0, 16, v245" : "=v"(dD[0]) :  : ); SB();
  asm volatile("v_mfma_f32_16x16x32_bf16 %0, v[176:179], a[40:43], %0" : "+v"(a1[0][1]) :  : ); SB();
  asm volatile("ds_read_b64_tr_b16 v[164:165], %0 offset:12288" :  : "v"(aW[1][1][1]) : "memory"); SB();
  asm volatile("v_lshlrev_b32 %0, 16, v246" : "=v"(dD[1]) :  : ); SB();
  asm volatile("v_mfma_f32_16x16x32_bf16 %0, v[176:179], a[44:47], %0" : "+v"(a1[1][1]) :  : ); SB();
  asm volatile("ds_read_b64_tr_b16 v[166:167], %0 offset:13056" :  : "v"(aW[1][1][1]) : "memory"); SB();
  asm volatile("s_waitcnt lgkmcnt(6)\n\tv_mfma_f32_16x16x32_bf16 %0, v[180:183], a[40:43], %0" : "+v"(a1[0][2]) :  : ); SB();
  asm volatile("ds_read_b64_tr_b16 v[168:169], %0 offset:12288" :  : "v"(aW[1][1][2]) : "memory"); SB();
  asm volatile("v_lshlrev_b32 %0, 16, v247" : "=v"(dD[2]) :  : ); SB();
  asm volatile("v_mfma_f32_16x16x32_bf16 %0, v[180:183], a[44:47], %0" : "+v"(a1[1][2]) :  : ); SB();
  asm volatile("ds_read_b64_tr_b16 v[170:171], %0 offset:13056" :  : "v"(aW[1][1][2]) : "memory"); SB();
  asm volatile("v_lshlrev_b32 %0, 16, v248" : "=v"(bs) :  : ); SB();
  ST(8);
  { unsigned lane = lane0, w = w0, rb = rb0, cb = cb0;
  asm volatile("" : "+v"(lane)); asm volatile("" : "+s"(w), "+s"(rb), "+s"(cb)); SB();

  unsigned zwa[2] = {lb + 98304 + 4096 + w * 8192 + (wn * 16 + l15) * 104 + g4 * 8, lb + 98304 + 4096 + w * 8192 + ((wn ^ 1) * 16 + l15) * 104 + g4 * 8};
  unsigned zra = lb + 98304 + 4096 + w * 8192 + (8 * g4 + q4) * 104 + 8 * p4, vZ_ = vZ;
  unsigned nw = lb + 98304 + 7424 + wm * 16384 + wn * 64 + (g4 & 1) * 32 + (lane & 7) * 4, nr = lb + 98304 + 7424 + wm * 16384 + g4 * 32;
  asm volatile("" : "+v"(zwa[0]), "+v"(zwa[1]), "+v"(zra), "+v"(vZ_), "+v"(nw), "+v"(nr));
  SB();
  asm volatile("buffer_load_dwordx4 %0, %1, %2 offen offset:1024 lds" :  : "v"(voB[1]), "s"(rW1), "s"(kw_[7]) : "memory"); SB();
  asm volatile("buffer_load_dwordx4 %0, %1, %2 offen offset:2048 lds" :  : "v"(voB[2]), "s"(rW1), "s"(kw_[7]) : "memory"); SB();
  asm volatile("s_add_u32 m0, %0, 24576" :  : "s"(mW) : "memory", "scc"); SB();
  asm volatile("s_nop 0\n\tbuffer_load_dwordx4 %0, %1, %2 offen lds" :  : "v"(voB[0]), "s"(rW1), "s"(kw_[8]) : "memory"); SB();
  asm volatile("buffer_load_dwordx4 %0, %1, %2 offen offset:1024 lds" :  : "v"(voB[1]), "s"(rW1), "s"(kw_[8]) : "memory"); SB();
  asm volatile("buffer_load_dwordx4 %0, %1, %2 offen offset:2048 lds" :  : "v"(voB[2]), "s"(rW1), "s"(kw_[8]) : "memory"); SB();
  asm volatile("s_add_u32 m0, %0, 131072" :  : "s"(mA) : "memory", "scc"); SB();
  asm volatile("s_nop 0\n\tbuffer_load_dwordx4 %0, %1, %2 offen lds" :  : "v"(voS), "s"(rA), "s"(ao[0]) : "memory"); SB();
  asm volatile("s_waitcnt vmcnt(11)\n\t.Lc_%=:\n\tv_cmpx_eq_u64 vcc, %1, v[198:199]\n\tv_cmpx_eq_u64 vcc, %1, v[202:203]\n\tv_cmpx_eq_u64 vcc, %1, v[206:207]\n\tv_cmpx_eq_u64 vcc, %1, v[210:211]\n\ts_cmp_eq_u64 vcc, -1\n\ts_mov_b64 exec, -1\n\ts_cbranch_scc0 .Lr_%=\n\t.Ld_%=:\n\t.subsection 1\n\t.Lr_%=:\n\tv_add_u32 %0, -1, %0\n\ts_nop 1\n\tv_readfirstlane_b32 vcc_hi, %0\n\ts_nop 3\n\ts_cmp_lt_i32 vcc_hi, 1\n\ts_cbranch_scc1 .Ld_%=\n\ts_sleep 2\n\ts_movk_i32 vcc_lo, 0x1000\n\tbuffer_load_dwordx4 v[196:199], %2, %3, 0 offen offset:0 sc1\n\tbuffer_load_dwordx4 v[200:203], %2, %3, 0 offen offset:2048 sc1\n\tbuffer_load_dwordx4 v[204:207], %2, %3, vcc_lo offen offset:0 sc1\n\tbuffer_load_dwordx4 v[208:211], %2, %3, vcc_lo offen offset:2048 sc1\n\ts_waitcnt vmcnt(0)\n\ts_branch .Lc_%=\n\t.subsection 0" : "+v"(tmo) : "s"(tg), "v"(vS), "s"(rWS) : "memory", "vcc", "scc"); SB();
  ST(9);
  {
    f32x2 s4[2], dm[2];
  asm volatile("v_pk_add_f32 %0, v[196:197], v[200:201]\n\tv_pk_add_f32 %1, v[204:205], v[208:209]" : "=&v"(s4[0]), "=&v"(s4[1]) :  : ); SB();
    const f32x2 sm = s4[0] + s4[1];
    const float mean = red32(sm[0] + dppf<0x128>(sm[0])) * (1.0f / 16.0f);
  asm volatile("v_sub_f32 %0, v196, %4\n\tv_sub_f32 %1, v200, %4\n\tv_sub_f32 %2, v204, %4\n\tv_sub_f32 %3, v208, %4" : "=&v"(dm[0][0]), "=&v"(dm[0][1]), "=&v"(dm[1][0]), "=&v"(dm[1][1]) : "v"(mean) : ); SB();
    const f32x2 dq = __builtin_elementwise_fma(dm[0], dm[0], dm[1] * dm[1]);
    float qs = __builtin_fmaf(dq[0] + dq[1], 48.0f, sm[1]);
    qs = red32(qs + dppf<0x128>(qs));
    float r = __builtin_amdgcn_rcpf(rbf(__builtin_amdgcn_sqrtf(rbf(rbf(qs * (1.0f / 768.0f)) + 1e-5f))));
    float mr = -rbf(mean) * r;
    f32x4 rm[4];
    asm volatile("" : "+v"(r), "+v"(mr)); SB();
  asm volatile("s_mov_b64 exec, 0xff00ff\n\tds_write2_b32 %0, %1, %2 offset1:32\n\ts_mov_b64 exec, -1" :  : "v"(nw), "v"(r), "v"(mr) : "memory"); SB();
  asm volatile("ds_write_b64 %0, %1 offset:0" :  : "v"(zwa[0]), "v"(((u32x2){zp[0][0][0], zp[0][0][1]})) : "memory"); SB();
  asm volatile("ds_write_b64 %0, %1 offset:32" :  : "v"(zwa[0]), "v"(((u32x2){zp[0][1][0], zp[0][1][1]})) : "memory"); SB();
  asm volatile("ds_write_b64 %0, %1 offset:64" :  : "v"(zwa[0]), "v"(((u32x2){zp[0][2][0], zp[0][2][1]})) : "memory"); SB();
  asm volatile("buffer_load_dwordx4 %0, %1, %2 offen offset:1024 lds" :  : "v"(voS), "s"(rA), "s"(ao[1]) : "memory"); SB();
  asm volatile("buffer_load_dwordx4 %0, %1, %2 offen offset:2048 lds" :  : "v"(voS), "s"(rA), "s"(ao[2]) : "memory"); SB();
  asm volatile("ds_write_b64 %0, %1 offset:0" :  : "v"(zwa[1]), "v"(((u32x2){zp[1][0][0], zp[1][0][1]})) : "memory"); SB();
  asm volatile("ds_write_b64 %0, %1 offset:32" :  : "v"(zwa[1]), "v"(((u32x2){zp[1][1][0], zp[1][1][1]})) : "memory"); SB();
  asm volatile("ds_write_b64 %0, %1 offset:64" :  : "v"(zwa[1]), "v"(((u32x2){zp[1][2][0], zp[1][2][1]})) : "memory"); SB();
  asm volatile("buffer_load_dwordx4 %0, %1, %2 offen offset:3072 lds" :  : "v"(voS), "s"(rA), "s"(ao[3]) : "memory"); SB();
  asm volatile("ds_read_b64_tr_b16 %0, %1 offset:0" : "=&v"(zt[0][0]) : "v"(zra) : "memory"); SB();
  asm volatile("ds_read_b64_tr_b16 %0, %1 offset:416" : "=&v"(zt[0][1]) : "v"(zra) : "memory"); SB();
  asm volatile("ds_read_b64_tr_b16 %0, %1 offset:32" : "=&v"(zt[1][0]) : "v"(zra) : "memory"); SB();
  asm volatile("ds_read_b64_tr_b16 %0, %1 offset:448" : "=&v"(zt[1][1]) : "v"(zra) : "memory"); SB();
  asm volatile("ds_read_b64_tr_b16 %0, %1 offset:64" : "=&v"(zt[2][0]) : "v"(zra) : "memory"); SB();
  asm volatile("ds_read_b64_tr_b16 %0, %1 offset:480" : "=&v"(zt[2][1]) : "v"(zra) : "memory"); SB();
  asm volatile("s_waitcnt lgkmcnt(6)\n\ts_barrier" :  :  : "memory"); SB();
  asm volatile("ds_read_b128 %0, %1 offset:0" : "=&v"(rm[0]) : "v"(nr) : "memory"); SB();
  asm volatile("ds_read_b128 %0, %1 offset:16" : "=&v"(rm[1]) : "v"(nr) : "memory"); SB();
  asm volatile("ds_read_b128 %0, %1 offset:128" : "=&v"(rm[2]) : "v"(nr) : "memory"); SB();
  asm volatile("ds_read_b128 %0, %1 offset:144" : "=&v"(rm[3]) : "v"(nr) : "memory"); SB();
  asm volatile("s_add_u32 m0, %0, 36864" :  : "s"(mW) : "memory", "scc"); SB();
  asm volatile("s_nop 0\n\tbuffer_load_dwordx4 %0, %1, %2 offen lds" :  : "v"(voB[0]), "s"(rW1), "s"(kw_[9]) : "memory"); SB();
  asm volatile("s_waitcnt lgkmcnt(4)" : "+v"(zt[0][0]), "+v"(zt[0][1]), "+v"(zt[1][0]), "+v"(zt[1][1]), "+v"(zt[2][0]), "+v"(zt[2][1]) :  : "memory"); SB();
  asm volatile("s_waitcnt lgkmcnt(0)" : "+v"(rm[0]), "+v"(rm[1]), "+v"(rm[2]), "+v"(rm[3]) :  : "memory"); SB();
    const f32x2 re[4] = {{rm[0][0], rm[0][1]}, {rm[0][2], rm[0][3]}, {rm[1][0], rm[1][1]}, {rm[1][2], rm[1][3]}};
    const f32x2 me[4] = {{rm[2][0], rm[2][1]}, {rm[2][2], rm[2][3]}, {rm[3][0], rm[3][1]}, {rm[3][2], rm[3][3]}};
    u32x4 zo[3];
#pragma unroll
    for (int e = 0; e < 4; ++e) zo[0][e] = pk2(__builtin_elementwise_fma(unpk(zt[0][e >> 1][e & 1]), re[e], me[e]));
    SB();
  asm volatile("buffer_store_dwordx4 %1, %0, %2, %3 offen offset:0 sc1\n\ts_nop 1" :  : "v"(vZ_), "v"(zo[0]), "s"(rWS), "s"(0u) : "memory"); SB();
#pragma unroll
    for (int e = 0; e < 4; ++e) zo[1][e] = pk2(__builtin_elementwise_fma(unpk(zt[1][e >> 1][e & 1]), re[e], me[e]));
    SB();
  asm volatile("buffer_store_dwordx4 %1, %0, %2, %3 offen offset:1024 sc1\n\ts_nop 1" :  : "v"(vZ_), "v"(zo[1]), "s"(rWS), "s"(0u) : "memory"); SB();
#pragma unroll
    for (int e = 0; e < 4; ++e) zo[2][e] = pk2(__builtin_elementwise_fma(unpk(zt[2][e >> 1][e & 1]), re[e], me[e]));
    SB();
  asm volatile("buffer_store_dwordx4 %1, %0, %2, %3 offen offset:2048 sc1\n\ts_nop 1" :  : "v"(vZ_), "v"(zo[2]), "s"(rWS), "s"(0u) : "memory"); SB();
  }
  ST(10);
  asm volatile("v_mfma_f32_16x16x32_bf16 %0, v[184:187], a[48:51], %0" : "+v"(a1[0][0]) :  : ); SB();
  asm volatile("s_barrier" :  :  : "memory"); SB();
  asm volatile("ds_read_b64_tr_b16 v[172:173], %0 offset:24576" :  : "v"(aW[1][0][0]) : "memory"); SB();
  asm volatile("s_waitcnt vmcnt(14)\n\ts_mov_b64 exec, 1\n\tbuffer_store_dwordx2 %0, off, %1, %2 sc1\n\ts_mov_b64 exec, -1" :  : "v"(((u32x2){tg0, tg1})), "s"(rWS), "s"(sFK) : "memory"); SB();
  asm volatile("v_mfma_f32_16x16x32_bf16 %0, v[184:187], a[52:55], %0" : "+v"(a1[1][0]) :  : ); SB();
  asm volatile("ds_read_b64_tr_b16 v[174:175], %0 offset:25344" :  : "v"(aW[1][0][0]) : "memory"); SB();
  asm volatile("v_mov_b32_dpp v242, v231 wave_shr:1 row_mask:0xf bank_mask:0xf" :  :  : ); SB();
  asm volatile("v_mfma_f32_16x16x32_bf16 %0, v[184:187], %1, %0" : "+v"(c1[0]) : "v"(ones) : ); SB();
  asm volatile("ds_read_b64_tr_b16 v[176:177], %0 offset:24576" :  : "v"(aW[1][0][1]) : "memory"); SB();
  asm volatile("v_alignbit_b32 %0, v231, v231, 16" : "=v"(im[0][0]) :  : ); SB();
  asm volatile("v_mfma_f32_16x16x32_bf16 %0, v[188:191], a[48:51], %0" : "+v"(a1[0][1]) :  : ); SB();
  asm volatile("ds_read_b64_tr_b16 v[178:179], %0 offset:25344" :  : "v"(aW[1][0][1]) : "memory"); SB();
  asm volatile("v_alignbit_b32 %0, v230, v230, 16" : "=v"(im[0][1]) :  : ); SB();
  asm volatile("v_mfma_f32_16x16x32_bf16 %0, v[188:191], a[52:55], %0" : "+v"(a1[1][1]) :  : ); SB();
  asm volatile("ds_read_b64_tr_b16 v[180:181], %0 offset:24576" :  : "v"(aW[1][0][2]) : "memory"); SB();
  asm volatile("v_alignbit_b32 %0, v229, v229, 16" : "=v"(im[0][2]) :  : ); SB();
  asm volatile("v_mfma_f32_16x16x32_bf16 %0, v[188:191], %1, %0" : "+v"(c1[1]) : "v"(ones) : ); SB();
  asm volatile("ds_read_b64_tr_b16 v[182:183], %0 offset:25344" :  : "v"(aW[1][0][2]) : "memory"); SB();
  asm volatile("v_alignbit_b32 %0, v228, v228, 16" : "=v"(im[0][3]) :  : ); SB();
  asm volatile("v_mfma_f32_16x16x32_bf16 %0, v[192:195], a[48:51], %0" : "+v"(a1[0][2]) :  : ); SB();
  asm volatile("buffer_load_dwordx4 %0, %1, %2 offen offset:1024 lds" :  : "v"(voB[1]), "s"(rW1), "s"(kw_[9]) : "memory"); SB();
  asm volatile("v_bfi_b32 %0, %1, v231, v230" : "=v"(im[1][0]) : "s"(0xFFFFu) : ); SB();
  asm volatile("v_mfma_f32_16x16x32_bf16 %0, v[192:195], a[52:55], %0" : "+v"(a1[1][2]) :  : ); SB();
  asm volatile("buffer_load_dwordx4 %0, %1, %2 offen offset:2048 lds" :  : "v"(voB[2]), "s"(rW1), "s"(kw_[9]) : "memory"); SB();
  asm volatile("v_bfi_b32 %0, %1, v230, v229" : "=v"(im[1][1]) : "s"(0xFFFFu) : ); SB();
  asm volatile("v_mfma_f32_16x16x32_bf16 %0, v[192:195], %1, %0" : "+v"(c1[2]) : "v"(ones) : ); SB();
  asm volatile("v_bfi_b32 %0, %1, v229, v228" : "=v"(im[1][2]) : "s"(0xFFFFu) : ); SB();
  asm volatile("v_mfma_f32_16x16x32_bf16 %0, v[160:163], a[56:59], %0" : "+v"(a1[0][0]) :  : ); SB();
  asm volatile("ds_read_b64_tr_b16 v[184:185], %0 offset:24576" :  : "v"(aW[1][1][0]) : "memory"); SB();
  asm volatile("s_add_u32 m0, %0, 135168" :  : "s"(mA) : "memory", "scc"); SB();
  asm volatile("v_mfma_f32_16x16x32_bf16 %0, v[160:163], a[60:63], %0" : "+v"(a1[1][0]) :  : ); SB();
  asm volatile("ds_read_b64_tr_b16 v[186:187], %0 offset:25344" :  : "v"(aW[1][1][0]) : "memory"); SB();
  asm volatile("v_bfi_b32 %0, %1, v228, v242" : "=v"(im[1][3]) : "s"(0xFFFFu) : ); SB();
  asm volatile("v_mfma_f32_16x16x32_bf16 %0, v[164:167], a[56:59], %0" : "+v"(a1[0][1]) :  : ); SB();
  asm volatile("ds_read_b64_tr_b16 v[188:189], %0 offset:24576" :  : "v"(aW[1][1][1]) : "memory"); SB();
  asm volatile("v_mov_b32_dpp v243, v235 wave_shr:1 row_mask:0xf bank_mask:0xf" :  :  : ); SB();
  asm volatile("v_mfma_f32_16x16x32_bf16 %0, v[164:167], a[60:63], %0" : "+v"(a1[1][1]) :  : ); SB();
  asm volatile("ds_read_b64_tr_b16 v[190:191], %0 offset:25344" :  : "v"(aW[1][1][1]) : "memory"); SB();
  asm volatile("v_alignbit_b32 %0, v235, v235, 16" : "=v"(im[2][0]) :  : ); SB();
  asm volatile("v_mfma_f32_16x16x32_bf16 %0, v[168:171], a[56:59], %0" : "+v"(a1[0][2]) :  : ); SB();
  asm volatile("ds_read_b64_tr_b16 v[192:193], %0 offset:24576" :  : "v"(aW[1][1][2]) : "memory"); SB();
  asm volatile("v_alignbit_b32 %0, v234, v234, 16" : "=v"(im[2][1]) :  : ); SB();
  asm volatile("v_mfma_f32_16x16x32_bf16 %0, v[168:171], a[60:63], %0" : "+v"(a1[1][2]) :  : ); SB();
  asm volatile("ds_read_b64_tr_b16 v[194:195], %0 offset:25344" :  : "v"(aW[1][1][2]) : "memory"); SB();
  asm volatile("s_waitcnt lgkmcnt(6)\n\tv_mfma_f32_16x16x32_bf16 %0, v[172:175], a[64:67], %0" : "+v"(a1[0][0]) :  : ); SB();
  asm volatile("ds_read_b64_tr_b16 v[160:161], %0 offset:36864" :  : "v"(aW[1][0][0]) : "memory"); SB();
  asm volatile("v_alignbit_b32 %0, v233, v233, 16" : "=v"(im[2][2]) :  : ); SB();
  asm volatile("v_mfma_f32_16x16x32_bf16 %0, v[172:175], a[68:71], %0" : "+v"(a1[1][0]) :  : ); SB();
  asm volatile("ds_read_b64_tr_b16 v[162:163], %0 offset:37632" :  : "v"(aW[1][0][0]) : "memory"); SB();
  asm volatile("v_alignbit_b32 %0, v232, v232, 16" : "=v"(im[2][3]) :  : ); SB();
  asm volatile("v_mfma_f32_16x16x32_bf16 %0, v[172:175], %1, %0" : "+v"(c1[0]) : "v"(ones) : ); SB();
  asm volatile("ds_read_b64_tr_b16 v[164:165], %0 offset:36864" :  : "v"(aW[1][0][1]) : "memory"); SB();
  asm volatile("v_bfi_b32 %0, %1, v235, v234" : "=v"(im[3][0]) : "s"(0xFFFFu) : ); SB();
  asm volatile("v_mfma_f32_16x16x32_bf16 %0, v[176:179], a[64:67], %0" : "+v"(a1[0][1]) :  : ); SB();
  asm volatile("ds_read_b64_tr_b16 v[166:167], %0 offset:37632" :  : "v"(aW[1][0][1]) : "memory"); SB();
  asm volatile("v_bfi_b32 %0, %1, v234, v233" : "=v"(im[3][1]) : "s"(0xFFFFu) : ); SB();
  asm volatile("v_mfma_f32_16x16x32_bf16 %0, v[176:179], a[68:71], %0" : "+v"(a1[1][1]) :  : ); SB();
  asm volatile("ds_read_b64_tr_b16 v[168:169], %0 offset:36864" :  : "v"(aW[1][0][2]) : "memory"); SB();
  asm volatile("v_bfi_b32 %0, %1, v233, v232" : "=v"(im[3][2]) : "s"(0xFFFFu) : ); SB();
  asm volatile("v_mfma_f32_16x16x32_bf16 %0, v[176:179], %1, %0" : "+v"(c1[1]) : "v"(ones) : ); SB();
  asm volatile("ds_read_b64_tr_b16 v[170:171], %0 offset:37632" :  : "v"(aW[1][0][2]) : "memory"); SB();
  asm volatile("v_bfi_b32 %0, %1, v232, v243" : "=v"(im[3][3]) : "s"(0xFFFFu) : ); SB();
  asm volatile("v_mfma_f32_16x16x32_bf16 %0, v[180:183], a[64:67], %0" : "+v"(a1[0][2]) :  : ); SB();
  asm volatile("buffer_load_dwordx4 %0, %1, %2 offen lds" :  : "v"(voS), "s"(rA), "s"(ao[4]) : "memory"); SB();
  asm volatile("s_waitcnt vmcnt(10)" :  :  : "memory"); SB();
  asm volatile("v_mfma_f32_16x16x32_bf16 %0, v[180:183], a[68:71], %0" : "+v"(a1[1][2]) :  : ); SB();
  asm volatile("ds_read_b128 a[160:163], %0 offset:32768" :  : "v"(aA[0]) : "memory"); SB();
  asm volatile("v_mov_b32_dpp v244, v239 wave_shr:1 row_mask:0xf bank_mask:0xf" :  :  : ); SB();
  asm volatile("v_mfma_f32_16x16x32_bf16 %0, v[180:183], %1, %0" : "+v"(c1[2]) : "v"(ones) : ); SB();
  asm volatile("ds_read_b128 a[176:179], %0 offset:32768" :  : "v"(aA[1]) : "memory"); SB();
  asm volatile("s_waitcnt lgkmcnt(8)\n\tv_mfma_f32_16x16x32_bf16 %0, v[184:187], a[72:75], %0" : "+v"(a1[0][0]) :  : ); SB();
  asm volatile("ds_read_b64_tr_b16 v[172:173], %0 offset:36864" :  : "v"(aW[1][1][0]) : "memory"); SB();
  asm volatile("buffer_load_dwordx4 %0, %1, %2 offen offset:1024 lds" :  : "v"(voS), "s"(rA), "s"(ao[5]) : "memory"); SB();
  asm volatile("v_mfma_f32_16x16x32_bf16 %0, v[184:187], a[76:79], %0" : "+v"(a1[1][0]) :  : ); SB();
  asm volatile("ds_read_b64_tr_b16 v[174:175], %0 offset:37632" :  : "v"(aW[1][1][0]) : "memory"); SB();
  asm volatile("v_alignbit_b32 %0, v239, v239, 16" : "=v"(im[4][0]) :  : ); SB();
  asm volatile("v_mfma_f32_16x16x32_bf16 %0, v[188:191], a[72:75], %0" : "+v"(a1[0][1]) :  : ); SB();
  asm volatile("ds_read_b64_tr_b16 v[176:177], %0 offset:36864" :  : "v"(aW[1][1][1]) : "memory"); SB();
  asm volatile("v_alignbit_b32 %0, v238, v238, 16" : "=v"(im[4][1]) :  : ); SB();
  asm volatile("v_mfma_f32_16x16x32_bf16 %0, v[188:191], a[76:79], %0" : "+v"(a1[1][1]) :  : ); SB();
  asm volatile("ds_read_b64_tr_b16 v[178:179], %0 offset:37632" :  : "v"(aW[1][1][1]) : "memory"); SB();
  asm volatile("s_waitcnt vmcnt(9)" :  :  : "memory"); SB();
  asm volatile("v_mfma_f32_16x16x32_bf16 %0, v[192:195], a[72:75], %0" : "+v"(a1[0][2]) :  : ); SB();
  asm volatile("ds_read_b64_tr_b16 v[180:181], %0 offset:36864" :  : "v"(aW[1][1][2]) : "memory"); SB();
  asm volatile("ds_read_b128 a[164:167], %0 offset:34816" :  : "v"(aA[0]) : "memory"); SB();
  asm volatile("v_mfma_f32_16x16x32_bf16 %0, v[192:195], a[76:79], %0" : "+v"(a1[1][2]) :  : ); SB();
  asm volatile("ds_read_b64_tr_b16 v[182:183], %0 offset:37632" :  : "v"(aW[1][1][2]) : "memory"); SB();
  asm volatile("ds_read_b128 a[180:183], %0 offset:34816" :  : "v"(aA[1]) : "memory"); SB();
  asm volatile("s_waitcnt lgkmcnt(10)\n\tv_mfma_f32_16x16x32_bf16 %0, v[160:163], a[80:83], %0" : "+v"(a1[0][0]) :  : ); SB();
  unsigned vFZ = W_FZ + cb * 1024 + w * 256 + (lane & 31) * 8;
  unsigned vB = W_Z2 + (w * 1024 + (cb & 5) * 128 + cb * 6) * 1024 + l16;
  unsigned ha = lb + 59520 + 4608 + (2040 - 8 * (w * 64 + lane)) * 2;
  unsigned za = lb + 59520 + min(w, 2u) * 12928 + (lane < 32 ? lane * 16 : 4608 + ((lane >> 1) & 1) * 4160 + 4096 + (lane & 1) * 16);
  unsigned ew[2] = {lb + 32768 + w * 6144 + wn * 3072 + l16, lb + 32768 + (w ^ 1) * 6144 + wn * 3072 + l16};
  unsigned aa[4], bb;
  {
    const unsigned c = (l15 + 1) & 1;
    const unsigned o = lb + 59520 + 2 * 12928 - w * 128 - 192;
#pragma unroll
    for (int k = 0; k < 4; ++k) aa[k] = o + 4608 + c * 4160 + (2031 - l15 + 8 * g4 - c) * 2 - 512 - 1024 * k;
    bb = o + 512 + 32 * l15 - 32 + 16 * g4;
  }
  unsigned dd = lb + 0 + w * 8192 + g4 * 256 + ((l15 ^ ((g4 >> 1) << 3)) << 4);
  asm volatile("" : "+v"(aa[0]), "+v"(aa[1]), "+v"(aa[2]), "+v"(aa[3]), "+v"(bb), "+v"(dd), "+v"(vFZ), "+v"(vB), "+v"(ha), "+v"(za), "+v"(ew[0]), "+v"(ew[1]));
  SB();
  asm volatile("buffer_load_dwordx4 %0, %1, %2 offen offset:2048 lds" :  : "v"(voS), "s"(rA), "s"(ao[6]) : "memory"); SB();
  asm volatile("buffer_load_dwordx4 %0, %1, %2 offen offset:3072 lds" :  : "v"(voS), "s"(rA), "s"(ao[7]) : "memory"); SB();
  asm volatile("v_xor_b32 %0, 128, %0" : "+v"(voS) :  : "memory"); SB();
  asm volatile("s_barrier" :  :  : "memory"); SB();
  asm volatile("ds_read_b64_tr_b16 v[184:185], %0 offset:0" :  : "v"(aW[0][0][0]) : "memory"); SB();
  asm volatile("s_add_u32 m0, %0, 98304" :  : "s"(mA) : "memory", "scc"); SB();
  asm volatile("v_mfma_f32_16x16x32_bf16 %0, v[160:163], a[84:87], %0" : "+v"(a1[1][0]) :  : ); SB();
  asm volatile("ds_read_b64_tr_b16 v[186:187], %0 offset:768" :  : "v"(aW[0][0][0]) : "memory"); SB();
  asm volatile("buffer_load_dwordx4 %0, %1, %2 offen lds" :  : "v"(voS), "s"(rA), "s"(ao[0]) : "memory"); SB();
  asm volatile("v_mfma_f32_16x16x32_bf16 %0, v[160:163], %1, %0" : "+v"(c1[0]) : "v"(ones) : ); SB();
  asm volatile("ds_read_b64_tr_b16 v[188:189], %0 offset:0" :  : "v"(aW[0][0][1]) : "memory"); SB();
  asm volatile("v_alignbit_b32 %0, v237, v237, 16" : "=v"(im[4][2]) :  : ); SB();
  asm volatile("v_mfma_f32_16x16x32_bf16 %0, v[164:167], a[80:83], %0" : "+v"(a1[0][1]) :  : ); SB();
  asm volatile("ds_read_b64_tr_b16 v[190:191], %0 offset:768" :  : "v"(aW[0][0][1]) : "memory"); SB();
  asm volatile("v_alignbit_b32 %0, v236, v236, 16" : "=v"(im[4][3]) :  : ); SB();
  asm volatile("v_mfma_f32_16x16x32_bf16 %0, v[164:167], a[84:87], %0" : "+v"(a1[1][1]) :  : ); SB();
  asm volatile("ds_read_b64_tr_b16 v[192:193], %0 offset:0" :  : "v"(aW[0][0][2]) : "memory"); SB();
  asm volatile("v_bfi_b32 %0, %1, v239, v238" : "=v"(im[5][0]) : "s"(0xFFFFu) : ); SB();
  asm volatile("v_mfma_f32_16x16x32_bf16 %0, v[164:167], %1, %0" : "+v"(c1[1]) : "v"(ones) : ); SB();
  asm volatile("ds_read_b64_tr_b16 v[194:195], %0 offset:768" :  : "v"(aW[0][0][2]) : "memory"); SB();
  asm volatile("v_bfi_b32 %0, %1, v238, v237" : "=v"(im[5][1]) : "s"(0xFFFFu) : ); SB();
  asm volatile("v_mfma_f32_16x16x32_bf16 %0, v[168:171], a[80:83], %0" : "+v"(a1[0][2]) :  : ); SB();
  asm volatile("buffer_load_dwordx4 %0, %1, %2 offen offset:1024 lds" :  : "v"(voS), "s"(rA), "s"(ao[1]) : "memory"); SB();
  asm volatile("v_bfi_b32 %0, %1, v237, v236" : "=v"(im[5][2]) : "s"(0xFFFFu) : ); SB();
  asm volatile("v_mfma_f32_16x16x32_bf16 %0, v[168:171], a[84:87], %0" : "+v"(a1[1][2]) :  : ); SB();
  asm volatile("buffer_load_dwordx4 %0, %1, %2 offen offset:2048 lds" :  : "v"(voS), "s"(rA), "s"(ao[2]) : "memory"); SB();
  asm volatile("v_bfi_b32 %0, %1, v236, v244" : "=v"(im[5][3]) : "s"(0xFFFFu) : ); SB();
  asm volatile("v_mfma_f32_16x16x32_bf16 %0, v[168:171], %1, %0" : "+v"(c1[2]) : "v"(ones) : ); SB();
  asm volatile("v_mov_b32 %0, 0" : "=v"(zero4[0]) :  : ); SB();
  asm volatile("s_waitcnt lgkmcnt(5)\n\tv_mfma_f32_16x16x32_bf16 %0, v[172:175], a[88:91], %0" : "+v"(a1[0][0]) :  : ); SB();
  asm volatile("ds_read_b64_tr_b16 v[160:161], %0 offset:0" :  : "v"(aW[0][1][0]) : "memory"); SB();
  asm volatile("v_mov_b32 %0, 0" : "=v"(zero4[1]) :  : ); SB();
  asm volatile("v_mfma_f32_16x16x32_bf16 %0, v[172:175], a[92:95], %0" : "+v"(a1[1][0]) :  : ); SB();
  asm volatile("ds_read_b64_tr_b16 v[162:163], %0 offset:768" :  : "v"(aW[0][1][0]) : "memory"); SB();
  asm volatile("v_mov_b32 %0, 0" : "=v"(zero4[2]) :  : ); SB();
  asm volatile("v_mfma_f32_16x16x32_bf16 %0, v[176:179], a[88:91], %0" : "+v"(a1[0][1]) :  : ); SB();
  asm volatile("ds_read_b64_tr_b16 v[164:165], %0 offset:0" :  : "v"(aW[0][1][1]) : "memory"); SB();
  asm volatile("v_mov_b32 %0, 0" : "=v"(zero4[3]) :  : ); SB();
  asm volatile("v_mfma_f32_16x16x32_bf16 %0, v[176:179], a[92:95], %0" : "+v"(a1[1][1]) :  : ); SB();
  asm volatile("ds_read_b64_tr_b16 v[166:167], %0 offset:768" :  : "v"(aW[0][1][1]) : "memory"); SB();
  asm volatile("v_mfma_f32_16x16x32_bf16 %0, v[180:183], a[88:91], %0" : "+v"(a1[0][2]) :  : ); SB();
  asm volatile("ds_read_b64_tr_b16 v[168:169], %0 offset:0" :  : "v"(aW[0][1][2]) : "memory"); SB();
  asm volatile("v_mfma_f32_16x16x32_bf16 %0, v[180:183], a[92:95], %0" : "+v"(a1[1][2]) :  : ); SB();
  asm volatile("ds_read_b64_tr_b16 v[170:171], %0 offset:768" :  : "v"(aW[0][1][2]) : "memory"); SB();
  asm volatile("s_waitcnt lgkmcnt(6)\n\tv_mfma_f32_16x16x32_bf16 %0, v[184:187], a[96:99], %0" : "+v"(a1[0][0]) :  : ); SB();
  asm volatile("ds_read_b64_tr_b16 v[172:173], %0 offset:12288" :  : "v"(aW[0][0][0]) : "memory"); SB();
  asm volatile("v_mfma_f32_16x16x32_bf16 %0, v[184:187], a[100:103], %0" : "+v"(a1[1][0]) :  : ); SB();
  asm volatile("ds_read_b64_tr_b16 v[174:175], %0 offset:13056" :  : "v"(aW[0][0][0]) : "memory"); SB();
  asm volatile("v_mfma_f32_16x16x32_bf16 %0, v[184:187], %1, %0" : "+v"(c1[0]) : "v"(ones) : ); SB();
  asm volatile("ds_read_b64_tr_b16 v[176:177], %0 offset:12288" :  : "v"(aW[0][0][1]) : "memory"); SB();
  asm volatile("v_mfma_f32_16x16x32_bf16 %0, v[188:191], a[96:99], %0" : "+v"(a1[0][1]) :  : ); SB();
  asm volatile("ds_read_b64_tr_b16 v[178:179], %0 offset:13056" :  : "v"(aW[0][0][1]) : "memory"); SB();
  asm volatile("v_mfma_f32_16x16x32_bf16 %0, v[188:191], a[100:103], %0" : "+v"(a1[1][1]) :  : ); SB();
  asm volatile("ds_read_b64_tr_b16 v[180:181], %0 offset:12288" :  : "v"(aW[0][0][2]) : "memory"); SB();
  asm volatile("v_mfma_f32_16x16x32_bf16 %0, v[188:191], %1, %0" : "+v"(c1[1]) : "v"(ones) : ); SB();
  asm volatile("ds_read_b64_tr_b16 v[182:183], %0 offset:13056" :  : "v"(aW[0][0][2]) : "memory"); SB();
  asm volatile("v_mfma_f32_16x16x32_bf16 %0, v[192:195], a[96:99], %0" : "+v"(a1[0][2]) :  : ); SB();
  asm volatile("buffer_load_dwordx4 %0, %1, %2 offen offset:3072 lds" :  : "v"(voS), "s"(rA), "s"(ao[3]) : "memory"); SB();
  asm volatile("s_add_u32 m0, %0, 102400" :  : "s"(mA) : "memory", "scc"); SB();
  asm volatile("v_mfma_f32_16x16x32_bf16 %0, v[192:195], a[100:103], %0" : "+v"(a1[1][2]) :  : ); SB();
  asm volatile("buffer_load_dwordx4 %0, %1, %2 offen lds" :  : "v"(voS), "s"(rA), "s"(ao[4]) : "memory"); SB();
  asm volatile("s_waitcnt vmcnt(12)\n\ts_mov_b64 exec, 1\n\tbuffer_store_dwordx2 %0, off, %1, %2 sc1\n\ts_mov_b64 exec, -1" :  : "v"(((u32x2){tg0, tg1})), "s"(rWS), "s"(sFZ) : "memory"); SB();
  asm volatile("v_mfma_f32_16x16x32_bf16 %0, v[192:195], %1, %0" : "+v"(c1[2]) : "v"(ones) : ); SB();
  asm volatile("s_waitcnt lgkmcnt(5)\n\tv_mfma_f32_16x16x32_bf16 %0, v[160:163], a[104:107], %0" : "+v"(a1[0][0]) :  : ); SB();
  asm volatile("ds_read_b64_tr_b16 v[184:185], %0 offset:12288" :  : "v"(aW[0][1][0]) : "memory"); SB();
  asm volatile("v_mfma_f32_16x16x32_bf16 %0, v[160:163], a[108:111], %0" : "+v"(a1[1][0]) :  : ); SB();
  asm volatile("s_mov_b32 exec_lo, 0x10001\n\ts_mov_b32 exec_hi, 0x10001\n\tds_write_b128 %0, %1 offset:0\n\ts_mov_b64 exec, -1" :  : "v"(ca), "v"(c1[0]) : "memory"); SB();
  asm volatile("ds_read_b64_tr_b16 v[186:187], %0 offset:13056" :  : "v"(aW[0][1][0]) : "memory"); SB();
  asm volatile("v_mfma_f32_16x16x32_bf16 %0, v[164:167], a[104:107], %0" : "+v"(a1[0][1]) :  : ); SB();
  asm volatile("ds_read_b64_tr_b16 v[188:189], %0 offset:12288" :  : "v"(aW[0][1][1]) : "memory"); SB();
  asm volatile("v_mfma_f32_16x16x32_bf16 %0, v[164:167], a[108:111], %0" : "+v"(a1[1][1]) :  : ); SB();
  asm volatile("s_mov_b32 exec_lo, 0x10001\n\ts_mov_b32 exec_hi, 0x10001\n\tds_write_b128 %0, %1 offset:64\n\ts_mov_b64 exec, -1" :  : "v"(ca), "v"(c1[1]) : "memory"); SB();
  asm volatile("ds_read_b64_tr_b16 v[190:191], %0 offset:13056" :  : "v"(aW[0][1][1]) : "memory"); SB();
  asm volatile("buffer_load_dwordx4 %0, %1, %2 offen offset:1024 lds" :  : "v"(voS), "s"(rA), "s"(ao[5]) : "memory"); SB();
  asm volatile("v_mfma_f32_16x16x32_bf16 %0, v[168:171], a[104:107], %0" : "+v"(a1[0][2]) :  : ); SB();
  asm volatile("ds_read_b64_tr_b16 v[192:193], %0 offset:12288" :  : "v"(aW[0][1][2]) : "memory"); SB();
  asm volatile("v_mfma_f32_16x16x32_bf16 %0, v[168:171], a[108:111], %0" : "+v"(a1[1][2]) :  : ); SB();
  asm volatile("s_mov_b32 exec_lo, 0x10001\n\ts_mov_b32 exec_hi, 0x10001\n\tds_write_b128 %0, %1 offset:128\n\ts_mov_b64 exec, -1" :  : "v"(ca), "v"(c1[2]) : "memory"); SB();
  asm volatile("ds_read_b64_tr_b16 v[194:195], %0 offset:13056" :  : "v"(aW[0][1][2]) : "memory"); SB();
  asm volatile("buffer_load_dwordx4 %0, %1, %2 offen offset:2048 lds" :  : "v"(voS), "s"(rA), "s"(ao[6]) : "memory"); SB();
  asm volatile("s_waitcnt lgkmcnt(5)\n\tv_mfma_f32_16x16x32_bf16 %0, v[172:175], a[112:115], %0" : "+v"(a1[0][0]) :  : ); SB();
  asm volatile("buffer_load_dwordx4 %0, %1, %2 offen offset:3072 lds" :  : "v"(voS), "s"(rA), "s"(ao[7]) : "memory"); SB();
  asm volatile("v_mfma_f32_16x16x32_bf16 %0, v[172:175], a[116:119], %0" : "+v"(a1[1][0]) :  : ); SB();
  asm volatile("v_xor_b32 %0, 384, %0" : "+v"(voS) :  : "memory"); SB();
  asm volatile("v_mfma_f32_16x16x32_bf16 %0, v[172:175], %1, %0" : "+v"(c1[0]) : "v"(ones) : ); SB();
  asm volatile("v_mfma_f32_16x16x32_bf16 %0, v[176:179], a[112:115], %0" : "+v"(a1[0][1]) :  : ); SB();
  asm volatile("s_waitcnt vmcnt(13) lgkmcnt(1)\n\ts_barrier" :  :  : "memory"); SB();
  asm volatile("ds_read_b64_tr_b16 v[160:161], %0 offset:24576" :  : "v"(aW[0][0][0]) : "memory"); SB();
  asm volatile("s_waitcnt vmcnt(11)" :  :  : "memory"); SB();
  asm volatile("v_mfma_f32_16x16x32_bf16 %0, v[176:179], a[116:119], %0" : "+v"(a1[1][1]) :  : ); SB();
  asm volatile("ds_read_b64_tr_b16 v[162:163], %0 offset:25344" :  : "v"(aW[0][0][0]) : "memory"); SB();
  asm volatile("ds_read_b128 a[168:171], %0 offset:36864" :  : "v"(aA[0]) : "memory"); SB();
  asm volatile("v_mfma_f32_16x16x32_bf16 %0, v[176:179], %1, %0" : "+v"(c1[1]) : "v"(ones) : ); SB();
  asm volatile("ds_read_b64_tr_b16 v[164:165], %0 offset:24576" :  : "v"(aW[0][0][1]) : "memory"); SB();
  asm volatile("ds_read_b128 a[184:187], %0 offset:36864" :  : "v"(aA[1]) : "memory"); SB();
  asm volatile("v_mfma_f32_16x16x32_bf16 %0, v[180:183], a[112:115], %0" : "+v"(a1[0][2]) :  : ); SB();
  asm volatile("ds_read_b64_tr_b16 v[166:167], %0 offset:25344" :  : "v"(aW[0][0][1]) : "memory"); SB();
  asm volatile("s_add_u32 m0, %0, 131072" :  : "s"(mA) : "memory", "scc"); SB();
  asm volatile("v_mfma_f32_16x16x32_bf16 %0, v[180:183], a[116:119], %0" : "+v"(a1[1][2]) :  : ); SB();
  asm volatile("ds_read_b64_tr_b16 v[168:169], %0 offset:24576" :  : "v"(aW[0][0][2]) : "memory"); SB();
  asm volatile("buffer_load_dwordx4 %0, %1, %2 offen lds" :  : "v"(voS), "s"(rA), "s"(ao[0]) : "memory"); SB();
  asm volatile("v_mfma_f32_16x16x32_bf16 %0, v[180:183], %1, %0" : "+v"(c1[2]) : "v"(ones) : ); SB();
  asm volatile("ds_read_b64_tr_b16 v[170:171], %0 offset:25344" :  : "v"(aW[0][0][2]) : "memory"); SB();
  asm volatile("v_mfma_f32_16x16x32_bf16 %0, v[184:187], a[120:123], %0" : "+v"(a1[0][0]) :  : ); SB();
  asm volatile("ds_read_b128 %0, %1 offset:0" : "=&v"(co[0]) : "v"(cr) : "memory"); SB();
  asm volatile("v_mfma_f32_16x16x32_bf16 %0, v[184:187], a[124:127], %0" : "+v"(a1[1][0]) :  : ); SB();
  asm volatile("ds_read_b128 %0, %1 offset:64" : "=&v"(co[1]) : "v"(cr) : "memory"); SB();
  asm volatile("v_mfma_f32_16x16x32_bf16 %0, v[184:187], %1, %0" : "+v"(c1[0]) : "v"(ones) : ); SB();
  asm volatile("ds_read_b128 %0, %1 offset:128" : "=&v"(co[2]) : "v"(cr) : "memory"); SB();
  asm volatile("v_mfma_f32_16x16x32_bf16 %0, v[188:191], a[120:123], %0" : "+v"(a1[0][1]) :  : ); SB();
  asm volatile("ds_write_b128 %0, %1 offset:0" :  : "v"(ha), "v"(im[0]) : "memory"); SB();
  asm volatile("ds_read_b64_tr_b16 v[172:173], %0 offset:24576" :  : "v"(aW[0][1][0]) : "memory"); SB();
  asm volatile("v_mfma_f32_16x16x32_bf16 %0, v[188:191], a[124:127], %0" : "+v"(a1[1][1]) :  : ); SB();
  asm volatile("ds_read_b64_tr_b16 v[174:175], %0 offset:25344" :  : "v"(aW[0][1][0]) : "memory"); SB();
  asm volatile("buffer_load_dwordx4 %0, %1, %2 offen offset:1024 lds" :  : "v"(voS), "s"(rA), "s"(ao[1]) : "memory"); SB();
  asm volatile("v_mfma_f32_16x16x32_bf16 %0, v[188:191], %1, %0" : "+v"(c1[1]) : "v"(ones) : ); SB();
  asm volatile("ds_read_b64_tr_b16 v[176:177], %0 offset:24576" :  : "v"(aW[0][1][1]) : "memory"); SB();
  asm volatile("s_waitcnt lgkmcnt(8)\n\tv_mfma_f32_16x16x32_bf16 %0, v[192:195], a[120:123], %0" : "+v"(a1[0][2]) :  : ); SB();
  asm volatile("ds_read_b64_tr_b16 v[178:179], %0 offset:25344" :  : "v"(aW[0][1][1]) : "memory"); SB();
  asm volatile("v_mfma_f32_16x16x32_bf16 %0, v[192:195], a[124:127], %0" : "+v"(a1[1][2]) :  : ); SB();
  asm volatile("ds_read_b64_tr_b16 v[180:181], %0 offset:24576" :  : "v"(aW[0][1][2]) : "memory"); SB();
  asm volatile("v_mfma_f32_16x16x32_bf16 %0, v[192:195], %1, %0" : "+v"(c1[2]) : "v"(ones) : ); SB();
  asm volatile("ds_write_b128 %0, %1 offset:4160" :  : "v"(ha), "v"(im[1]) : "memory"); SB();
  asm volatile("ds_read_b64_tr_b16 v[182:183], %0 offset:25344" :  : "v"(aW[0][1][2]) : "memory"); SB();
  asm volatile("s_waitcnt vmcnt(11)" :  :  : "memory"); SB();
  asm volatile("v_mfma_f32_16x16x32_bf16 %0, v[160:163], a[128:131], %0" : "+v"(a1[0][0]) :  : ); SB();
  asm volatile("ds_read_b128 a[172:175], %0 offset:38912" :  : "v"(aA[0]) : "memory"); SB();
  asm volatile("v_mfma_f32_16x16x32_bf16 %0, v[160:163], a[132:135], %0" : "+v"(a1[1][0]) :  : ); SB();
  asm volatile("ds_read_b128 a[188:191], %0 offset:38912" :  : "v"(aA[1]) : "memory"); SB();
  asm volatile("v_mfma_f32_16x16x32_bf16 %0, v[160:163], %1, %0" : "+v"(c1[0]) : "v"(ones) : ); SB();
  asm volatile("buffer_load_dwordx4 %0, %1, %2 offen offset:2048 lds" :  : "v"(voS), "s"(rA), "s"(ao[2]) : "memory"); SB();
  asm volatile("v_mfma_f32_16x16x32_bf16 %0, v[164:167], a[128:131], %0" : "+v"(a1[0][1]) :  : ); SB();
  asm volatile("ds_read_b64_tr_b16 v[184:185], %0 offset:36864" :  : "v"(aW[0][0][0]) : "memory"); SB();
  asm volatile("buffer_load_dwordx4 %0, %1, %2 offen offset:3072 lds" :  : "v"(voS), "s"(rA), "s"(ao[3]) : "memory"); SB();
  asm volatile("v_mfma_f32_16x16x32_bf16 %0, v[164:167], a[132:135], %0" : "+v"(a1[1][1]) :  : ); SB();
  asm volatile("ds_write_b128 %0, %1 offset:12928" :  : "v"(ha), "v"(im[2]) : "memory"); SB();
  asm volatile("ds_read_b64_tr_b16 v[186:187], %0 offset:37632" :  : "v"(aW[0][0][0]) : "memory"); SB();
  asm volatile("s_waitcnt vmcnt(11)" :  :  : "memory"); SB();
  asm volatile("v_mfma_f32_16x16x32_bf16 %0, v[164:167], %1, %0" : "+v"(c1[1]) : "v"(ones) : ); SB();
  asm volatile("ds_read_b64_tr_b16 v[188:189], %0 offset:36864" :  : "v"(aW[0][0][1]) : "memory"); SB();
  asm volatile("buffer_load_dwordx2 %0, %1, %2, %3 offen sc1" : "=&v"(fk) : "v"(vFZ), "s"(rWS), "s"(8192u) : "memory"); SB();
  asm volatile("s_waitcnt lgkmcnt(7)\n\tv_mfma_f32_16x16x32_bf16 %0, v[168:171], a[128:131], %0" : "+v"(a1[0][2]) :  : ); SB();
  asm volatile("ds_read_b64_tr_b16 v[190:191], %0 offset:37632" :  : "v"(aW[0][0][1]) : "memory"); SB();
  asm volatile("ds_read_b128 a[192:195], %0 offset:0" :  : "v"(aA[0]) : "memory"); SB();
  asm volatile("v_mfma_f32_16x16x32_bf16 %0, v[168:171], a[132:135], %0" : "+v"(a1[1][2]) :  : ); SB();
  asm volatile("ds_read_b64_tr_b16 v[192:193], %0 offset:36864" :  : "v"(aW[0][0][2]) : "memory"); SB();
  asm volatile("ds_read_b128 a[208:211], %0 offset:0" :  : "v"(aA[1]) : "memory"); SB();
  asm volatile("v_mfma_f32_16x16x32_bf16 %0, v[168:171], %1, %0" : "+v"(c1[2]) : "v"(ones) : ); SB();
  asm volatile("ds_read_b64_tr_b16 v[194:195], %0 offset:37632" :  : "v"(aW[0][0][2]) : "memory"); SB();
  asm volatile("s_waitcnt vmcnt(10)" :  :  : "memory"); SB();
  asm volatile("v_mfma_f32_16x16x32_bf16 %0, v[172:175], a[136:139], %0" : "+v"(a1[0][0]) :  : ); SB();
  asm volatile("ds_write_b128 %0, %1 offset:17088" :  : "v"(ha), "v"(im[3]) : "memory"); SB();
  asm volatile("ds_read_b128 a[196:199], %0 offset:2048" :  : "v"(aA[0]) : "memory"); SB();
  asm volatile("v_mfma_f32_16x16x32_bf16 %0, v[172:175], a[140:143], %0" : "+v"(a1[1][0]) :  : ); SB();
  asm volatile("ds_read_b128 a[212:215], %0 offset:2048" :  : "v"(aA[1]) : "memory"); SB();
  asm volatile("s_add_u32 m0, %0, 135168" :  : "s"(mA) : "memory", "scc"); SB();
  asm volatile("v_mfma_f32_16x16x32_bf16 %0, v[172:175], %1, %0" : "+v"(c1[0]) : "v"(ones) : ); SB();
  asm volatile("buffer_load_dwordx4 %0, %1, %2 offen lds" :  : "v"(voS), "s"(rA), "s"(ao[4]) : "memory"); SB();
  asm volatile("v_mfma_f32_16x16x32_bf16 %0, v[176:179], a[136:139], %0" : "+v"(a1[0][1]) :  : ); SB();
  asm volatile("ds_read_b64_tr_b16 v[160:161], %0 offset:36864" :  : "v"(aW[0][1][0]) : "memory"); SB();
  asm volatile("buffer_load_dwordx4 %0, %1, %2 offen offset:1024 lds" :  : "v"(voS), "s"(rA), "s"(ao[5]) : "memory"); SB();
  asm volatile("v_mfma_f32_16x16x32_bf16 %0, v[176:179], a[140:143], %0" : "+v"(a1[1][1]) :  : ); SB();
  asm volatile("ds_read_b64_tr_b16 v[162:163], %0 offset:37632" :  : "v"(aW[0][1][0]) : "memory"); SB();
  asm volatile("s_waitcnt lgkmcnt(8)" :  :  : "memory"); SB();
  asm volatile("v_mfma_f32_16x16x32_bf16 %0, v[176:179], %1, %0" : "+v"(c1[1]) : "v"(ones) : ); SB();
  asm volatile("ds_write_b128 %0, %1 offset:25856" :  : "v"(ha), "v"(im[4]) : "memory"); SB();
  asm volatile("ds_read_b64_tr_b16 v[164:165], %0 offset:36864" :  : "v"(aW[0][1][1]) : "memory"); SB();
  asm volatile("buffer_load_dwordx4 %0, %1, %2 offen offset:2048 lds" :  : "v"(voS), "s"(rA), "s"(ao[6]) : "memory"); SB();
  asm volatile("v_mfma_f32_16x16x32_bf16 %0, v[180:183], a[136:139], %0" : "+v"(a1[0][2]) :  : ); SB();
  asm volatile("ds_read_b64_tr_b16 v[166:167], %0 offset:37632" :  : "v"(aW[0][1][1]) : "memory"); SB();
  asm volatile("v_mfma_f32_16x16x32_bf16 %0, v[180:183], a[140:143], %0" : "+v"(a1[1][2]) :  : ); SB();
  asm volatile("ds_read_b64_tr_b16 v[168:169], %0 offset:36864" :  : "v"(aW[0][1][2]) : "memory"); SB();
  asm volatile("buffer_load_dwordx4 %0, %1, %2 offen offset:3072 lds" :  : "v"(voS), "s"(rA), "s"(ao[7]) : "memory"); SB();
  asm volatile("v_mfma_f32_16x16x32_bf16 %0, v[180:183], %1, %0" : "+v"(c1[2]) : "v"(ones) : ); SB();
  asm volatile("ds_read_b64_tr_b16 v[170:171], %0 offset:37632" :  : "v"(aW[0][1][2]) : "memory"); SB();
  asm volatile("s_waitcnt vmcnt(11)" :  :  : "memory"); SB();
  asm volatile("v_mfma_f32_16x16x32_bf16 %0, v[184:187], a[144:147], %0" : "+v"(a1[0][0]) :  : ); SB();
  asm volatile("ds_read_b128 a[200:203], %0 offset:4096" :  : "v"(aA[0]) : "memory"); SB();
  asm volatile("v_mfma_f32_16x16x32_bf16 %0, v[184:187], a[148:151], %0" : "+v"(a1[1][0]) :  : ); SB();
  asm volatile("ds_write_b128 %0, %1 offset:30016" :  : "v"(ha), "v"(im[5]) : "memory"); SB();
  asm volatile("ds_read_b128 a[216:219], %0 offset:4096" :  : "v"(aA[1]) : "memory"); SB();
  asm volatile("s_waitcnt lgkmcnt(8)" :  :  : "memory"); SB();
  asm volatile("v_mfma_f32_16x16x32_bf16 %0, v[184:187], %1, %0" : "+v"(c1[0]) : "v"(ones) : ); SB();
  asm volatile("v_xor_b32 %0, 128, %0" : "+v"(voS) :  : "memory"); SB();
  asm volatile("s_add_u32 m0, %0, 98304" :  : "s"(mA) : "memory", "scc"); SB();
  asm volatile("v_mfma_f32_16x16x32_bf16 %0, v[188:191], a[144:147], %0" : "+v"(a1[0][1]) :  : ); SB();
  asm volatile("buffer_load_dwordx4 %0, %1, %2 offen lds" :  : "v"(voS), "s"(rA), "s"(ao[0]) : "memory"); SB();
  asm volatile("v_mfma_f32_16x16x32_bf16 %0, v[188:191], a[148:151], %0" : "+v"(a1[1][1]) :  : ); SB();
  asm volatile("buffer_load_dwordx4 %0, %1, %2 offen offset:1024 lds" :  : "v"(voS), "s"(rA), "s"(ao[1]) : "memory"); SB();
  asm volatile("s_waitcnt vmcnt(11)" :  :  : "memory"); SB();
  asm volatile("v_mfma_f32_16x16x32_bf16 %0, v[188:191], %1, %0" : "+v"(c1[1]) : "v"(ones) : ); SB();
  asm volatile("ds_read_b128 a[204:207], %0 offset:6144" :  : "v"(aA[0]) : "memory"); SB();
  asm volatile("v_mfma_f32_16x16x32_bf16 %0, v[192:195], a[144:147], %0" : "+v"(a1[0][2]) :  : ); SB();
  asm volatile("ds_write_b128 %0, %1 offset:0" :  : "v"(za), "v"(zero4) : "memory"); SB();
  asm volatile("ds_read_b128 a[220:223], %0 offset:6144" :  : "v"(aA[1]) : "memory"); SB();
  asm volatile("v_mfma_f32_16x16x32_bf16 %0, v[192:195], a[148:151], %0" : "+v"(a1[1][2]) :  : ); SB();
  asm volatile("buffer_load_dwordx4 %0, %1, %2 offen offset:2048 lds" :  : "v"(voS), "s"(rA), "s"(ao[2]) : "memory"); SB();
  asm volatile("v_mfma_f32_16x16x32_bf16 %0, v[192:195], %1, %0" : "+v"(c1[2]) : "v"(ones) : ); SB();
  asm volatile("v_mfma_f32_16x16x32_bf16 %0, v[160:163], a[152:155], %0" : "+v"(a1[0][0]) :  : ); SB();
  asm volatile("buffer_load_dwordx4 %0, %1, %2 offen offset:3072 lds" :  : "v"(voS), "s"(rA), "s"(ao[3]) : "memory"); SB();
  asm volatile("s_waitcnt lgkmcnt(3)" :  :  : "memory"); SB();
  asm volatile("v_mfma_f32_16x16x32_bf16 %0, v[160:163], a[156:159], %0" : "+v"(a1[1][0]) :  : ); SB();
  asm volatile("v_mfma_f32_16x16x32_bf16 %0, v[160:163], %1, %0" : "+v"(c1[0]) : "v"(ones) : ); SB();
  asm volatile("v_mfma_f32_16x16x32_bf16 %0, v[164:167], a[152:155], %0" : "+v"(a1[0][1]) :  : ); SB();
  asm volatile("s_add_u32 m0, %0, 102400" :  : "s"(mA) : "memory", "scc"); SB();
  asm volatile("v_mfma_f32_16x16x32_bf16 %0, v[164:167], a[156:159], %0" : "+v"(a1[1][1]) :  : ); SB();
  asm volatile("buffer_load_dwordx4 %0, %1, %2 offen lds" :  : "v"(voS), "s"(rA), "s"(ao[4]) : "memory"); SB();
  asm volatile("v_mfma_f32_16x16x32_bf16 %0, v[164:167], %1, %0" : "+v"(c1[1]) : "v"(ones) : ); SB();
  asm volatile("v_mfma_f32_16x16x32_bf16 %0, v[168:171], a[152:155], %0" : "+v"(a1[0][2]) :  : ); SB();
  asm volatile("v_mfma_f32_16x16x32_bf16 %0, v[168:171], a[156:159], %0" : "+v"(a1[1][2]) :  : ); SB();
  asm volatile("buffer_load_dwordx4 %0, %1, %2 offen offset:1024 lds" :  : "v"(voS), "s"(rA), "s"(ao[5]) : "memory"); SB();
  asm volatile("s_waitcnt lgkmcnt(0)" :  :  : "memory"); SB();
  asm volatile("v_mfma_f32_16x16x32_bf16 %0, v[168:171], %1, %0" : "+v"(c1[2]) : "v"(ones) : ); SB();
  ST(11);
  f32x4 ac[8];
  u32x2 cA[2][8][2];
  asm volatile("buffer_load_dwordx4 %0, %1, %2 offen offset:2048 lds" :  : "v"(voS), "s"(rA), "s"(ao[6]) : "memory"); SB();
  asm volatile("s_waitcnt vmcnt(14)" :  :  : "memory"); SB();
  asm volatile("ds_read_b128 a[224:227], %0 offset:32768" :  : "v"(aA[0]) : "memory"); SB();
  asm volatile("ds_read_b128 a[240:243], %0 offset:32768" :  : "v"(aA[1]) : "memory"); SB();
  asm volatile("buffer_load_dwordx4 %0, %1, %2 offen offset:3072 lds" :  : "v"(voS), "s"(rA), "s"(ao[7]) : "memory"); SB();
  asm volatile("buffer_load_dwordx2 %0, %1, %2, %3 offen sc1" : "=&v"(fz) : "v"(vFZ), "s"(rWS), "s"(0u) : "memory"); SB();
  asm volatile("s_waitcnt lgkmcnt(0)" :  :  : "memory"); SB();
  asm volatile("v_xor_b32 %0, 896, %0" : "+v"(voS) :  : "memory"); SB();
  asm volatile("s_add_u32 m0, %0, 131072" :  : "s"(mA) : "memory", "scc"); SB();
  asm volatile("s_nop 0\n\tbuffer_load_dwordx4 %0, %1, %2 offen lds" :  : "v"(voS), "s"(rA), "s"(ao[0]) : "memory"); SB();
  asm volatile("s_waitcnt vmcnt(15)" :  :  : "memory"); SB();
  asm volatile("ds_read_b128 a[228:231], %0 offset:34816" :  : "v"(aA[0]) : "memory"); SB();
  asm volatile("s_waitcnt vmcnt(14)\n\t.Lc_%=:\n\tv_cmp_ne_u64 vcc, %2, %1\n\ts_cbranch_vccnz .Lr_%=\n\t.Ld_%=:\n\t.subsection 1\n\t.Lr_%=:\n\tv_add_u32 %0, -1, %0\n\ts_nop 1\n\tv_readfirstlane_b32 vcc_hi, %0\n\ts_nop 3\n\ts_cmp_lt_i32 vcc_hi, 1\n\ts_cbranch_scc1 .Ld_%=\n\ts_sleep 2\n\ts_mov_b32 vcc_lo, 8192\n\tbuffer_load_dwordx2 %1, %3, %4, vcc_lo offen sc1\n\ts_waitcnt vmcnt(0)\n\ts_branch .Lc_%=\n\t.subsection 0" : "+v"(tmo), "+v"(fk) : "s"(tg), "v"(vFZ), "s"(rWS) : "memory", "vcc", "scc"); SB();
  asm volatile("s_add_u32 m0, %0, 85888" :  : "s"(mU) : "memory", "scc"); SB();
  asm volatile("s_nop 0\n\tbuffer_load_dwordx4 %0, %1, %2 offen sc1 lds" :  : "v"(vF), "s"(rWS), "s"(uso) : "memory"); SB();
  asm volatile("ds_read_b128 a[244:247], %0 offset:34816" :  : "v"(aA[1]) : "memory"); SB();
  asm volatile("s_add_u32 m0, %0, 131072" :  : "s"(mA) : "memory", "scc"); SB();
  asm volatile("s_nop 0\n\tbuffer_load_dwordx4 %0, %1, %2 offen offset:1024 lds" :  : "v"(voS), "s"(rA), "s"(ao[1]) : "memory"); SB();
  asm volatile("s_waitcnt vmcnt(14)" :  :  : "memory"); SB();
  asm volatile("ds_read_b128 a[232:235], %0 offset:36864" :  : "v"(aA[0]) : "memory"); SB();
  asm volatile("ds_read_b128 a[248:251], %0 offset:36864" :  : "v"(aA[1]) : "memory"); SB();
  asm volatile("s_waitcnt lgkmcnt(2)" :  :  : "memory"); SB();
  asm volatile("buffer_load_dwordx4 %0, %1, %2 offen offset:2048 lds" :  : "v"(voS), "s"(rA), "s"(ao[2]) : "memory"); SB();
  asm volatile("buffer_load_dwordx4 %0, %1, %2 offen offset:3072 lds" :  : "v"(voS), "s"(rA), "s"(ao[3]) : "memory"); SB();
  asm volatile("s_waitcnt vmcnt(14)" :  :  : "memory"); SB();
  asm volatile("ds_read_b128 a[236:239], %0 offset:38912" :  : "v"(aA[0]) : "memory"); SB();
  asm volatile("ds_read_b128 a[252:255], %0 offset:38912" :  : "v"(aA[1]) : "memory"); SB();
  asm volatile("s_waitcnt lgkmcnt(2)" :  :  : "memory"); SB();
  asm volatile("s_add_u32 m0, %0, 135168" :  : "s"(mA) : "memory", "scc"); SB();
  asm volatile("s_nop 0\n\tbuffer_load_dwordx4 %0, %1, %2 offen lds" :  : "v"(voS), "s"(rA), "s"(ao[4]) : "memory"); SB();
  asm volatile("buffer_load_dwordx4 %0, %1, %2 offen offset:1024 lds" :  : "v"(voS), "s"(rA), "s"(ao[5]) : "memory"); SB();
  asm volatile("s_waitcnt vmcnt(14)" :  :  : "memory"); SB();
  asm volatile("ds_read_b128 a[0:3], %0 offset:0" :  : "v"(aA[0]) : "memory"); SB();
  asm volatile("ds_read_b128 a[16:19], %0 offset:0" :  : "v"(aA[1]) : "memory"); SB();
  asm volatile("s_waitcnt lgkmcnt(2)" :  :  : "memory"); SB();
  asm volatile("buffer_load_dwordx4 %0, %1, %2 offen offset:2048 lds" :  : "v"(voS), "s"(rA), "s"(ao[6]) : "memory"); SB();
  asm volatile("buffer_load_dwordx4 %0, %1, %2 offen offset:3072 lds" :  : "v"(voS), "s"(rA), "s"(ao[7]) : "memory"); SB();
  asm volatile("s_waitcnt vmcnt(14)" :  :  : "memory"); SB();
  asm volatile("ds_read_b128 a[4:7], %0 offset:2048" :  : "v"(aA[0]) : "memory"); SB();
  asm volatile("ds_read_b128 a[20:23], %0 offset:2048" :  : "v"(aA[1]) : "memory"); SB();
  asm volatile("s_waitcnt lgkmcnt(2)" :  :  : "memory"); SB();
  asm volatile("v_xor_b32 %0, 128, %0" : "+v"(voS) :  : "memory"); SB();
  asm volatile("s_add_u32 m0, %0, 98304" :  : "s"(mA) : "memory", "scc"); SB();
  asm volatile("s_nop 0\n\tbuffer_load_dwordx4 %0, %1, %2 offen lds" :  : "v"(voS), "s"(rA), "s"(ao[0]) : "memory"); SB();
  asm volatile("buffer_load_dwordx4 %0, %1, %2 offen offset:1024 lds" :  : "v"(voS), "s"(rA), "s"(ao[1]) : "memory"); SB();
  asm volatile("s_waitcnt vmcnt(14)" :  :  : "memory"); SB();
  asm volatile("ds_read_b128 a[8:11], %0 offset:4096" :  : "v"(aA[0]) : "memory"); SB();
  asm volatile("ds_read_b128 a[24:27], %0 offset:4096" :  : "v"(aA[1]) : "memory"); SB();
  asm volatile("s_waitcnt lgkmcnt(2)" :  :  : "memory"); SB();
  asm volatile("buffer_load_dwordx4 %0, %1, %2 offen offset:2048 lds" :  : "v"(voS), "s"(rA), "s"(ao[2]) : "memory"); SB();
  asm volatile("buffer_load_dwordx4 %0, %1, %2 offen offset:3072 lds" :  : "v"(voS), "s"(rA), "s"(ao[3]) : "memory"); SB();
  asm volatile("s_waitcnt vmcnt(14)" :  :  : "memory"); SB();
  asm volatile("ds_read_b128 a[12:15], %0 offset:6144" :  : "v"(aA[0]) : "memory"); SB();
  asm volatile("ds_read_b128 a[28:31], %0 offset:6144" :  : "v"(aA[1]) : "memory"); SB();
  asm volatile("s_waitcnt vmcnt(13)\n\t.Lc_%=:\n\tv_cmp_ne_u64 vcc, %2, %1\n\ts_cbranch_vccnz .Lr_%=\n\t.Ld_%=:\n\t.subsection 1\n\t.Lr_%=:\n\tv_add_u32 %0, -1, %0\n\ts_nop 1\n\tv_readfirstlane_b32 vcc_hi, %0\n\ts_nop 3\n\ts_cmp_lt_i32 vcc_hi, 1\n\ts_cbranch_scc1 .Ld_%=\n\ts_sleep 2\n\ts_mov_b32 vcc_lo, 0\n\tbuffer_load_dwordx2 %1, %3, %4, vcc_lo offen sc1\n\ts_waitcnt vmcnt(0)\n\ts_branch .Lc_%=\n\t.subsection 0" : "+v"(tmo), "+v"(fz) : "s"(tg), "v"(vFZ), "s"(rWS) : "memory", "vcc", "scc"); SB();
  asm volatile("buffer_load_dwordx4 v[160:163], %0, %1, %2 offen offset:0 sc1" :  : "v"(vB), "s"(rWS), "s"(0u) : "memory"); SB();
  asm volatile("s_waitcnt lgkmcnt(2)" :  :  : "memory"); SB();
  asm volatile("s_waitcnt vmcnt(12) lgkmcnt(0)\n\ts_barrier" :  :  : "memory"); SB();
  ST(14);
  asm volatile("ds_read2_b32 %0, %1 offset0:176 offset1:177" : "=&v"(cA[0][0][0]) : "v"(aa[0]) : "memory"); SB();
  asm volatile("ds_read2_b32 %0, %1 offset0:178 offset1:179" : "=&v"(cA[0][0][1]) : "v"(aa[0]) : "memory"); SB();
  asm volatile("ds_read_b128 a[124:127], %0 offset:3776" :  : "v"(bb) : "memory"); SB();
  asm volatile("ds_read_b128 a[120:123], %0 offset:3264" :  : "v"(bb) : "memory"); SB();
  asm volatile("ds_read_b128 a[116:119], %0 offset:2752" :  : "v"(bb) : "memory"); SB();
  asm volatile("ds_read_b128 a[112:115], %0 offset:2240" :  : "v"(bb) : "memory"); SB();
  asm volatile("ds_read2_b32 %0, %1 offset0:48 offset1:49" : "=&v"(cA[0][1][0]) : "v"(aa[0]) : "memory"); SB();
  asm volatile("ds_read2_b32 %0, %1 offset0:50 offset1:51" : "=&v"(cA[0][1][1]) : "v"(aa[0]) : "memory"); SB();
  asm volatile("ds_read_b128 a[108:111], %0 offset:1728" :  : "v"(bb) : "memory"); SB();
  asm volatile("ds_read2_b32 %0, %1 offset0:176 offset1:177" : "=&v"(cA[0][2][0]) : "v"(aa[1]) : "memory"); SB();
  asm volatile("" : "+v"(a1[0][0]), "+v"(a1[0][1]), "+v"(a1[0][2]), "+v"(a1[1][0]), "+v"(a1[1][1]), "+v"(a1[1][2]), "+v"(c1[0]), "+v"(c1[1]), "+v"(c1[2]), "+v"(co[0]), "+v"(co[1]), "+v"(co[2]) :  : ); SB();
  c1[0] += co[0]; c1[1] += co[1]; c1[2] += co[2];
#pragma unroll
  for (int n = 0; n < 6; ++n) { a1[n / 3][n % 3] = __builtin_elementwise_fma((f32x4){-xm[n / 3], -xm[n / 3], -xm[n / 3], -xm[n / 3]}, c1[n % 3], a1[n / 3][n % 3]); asm volatile("" : "+v"(a1[n / 3][n % 3])); }
  SB();
  asm volatile("s_waitcnt vmcnt(11)" :  :  : "memory"); SB();
  asm volatile("ds_read_b128 a[32:35], %0 offset:32768" :  : "v"(aA[0]) : "memory"); SB();
  asm volatile("ds_read_b128 a[48:51], %0 offset:32768" :  : "v"(aA[1]) : "memory"); SB();
  asm volatile("buffer_load_dwordx4 v[164:167], %0, %1, %2 offen offset:1024 sc1" :  : "v"(vB), "s"(rWS), "s"(0u) : "memory"); SB();
  asm volatile("buffer_load_dwordx4 v[168:171], %0, %1, %2 offen offset:0 sc1" :  : "v"(vB), "s"(rWS), "s"(65536u) : "memory"); SB();
  if (nch == 3) {
  asm volatile("s_waitcnt lgkmcnt(6)\n\tv_mfma_f32_16x16x32_bf16 %0, %1, a[124:127], 0" : "=&v"(ac[7]) : "v"(((u32x4){cA[0][0][0][0], cA[0][0][0][1], cA[0][0][1][0], cA[0][0][1][1]})) : ); SB();
  asm volatile("ds_read2_b32 %0, %1 offset0:178 offset1:179" : "=&v"(cA[0][2][1]) : "v"(aa[1]) : "memory"); SB();
  asm volatile("v_mfma_f32_16x16x32_bf16 %0, %1, a[120:123], 0" : "=&v"(ac[6]) : "v"(((u32x4){cA[0][0][0][0], cA[0][0][0][1], cA[0][0][1][0], cA[0][0][1][1]})) : ); SB();
  asm volatile("ds_read2_b32 %0, %1 offset0:48 offset1:49" : "=&v"(cA[0][3][0]) : "v"(aa[1]) : "memory"); SB();
  asm volatile("v_mfma_f32_16x16x32_bf16 %0, %1, a[116:119], 0" : "=&v"(ac[5]) : "v"(((u32x4){cA[0][0][0][0], cA[0][0][0][1], cA[0][0][1][0], cA[0][0][1][1]})) : ); SB();
  asm volatile("ds_read2_b32 %0, %1 offset0:50 offset1:51" : "=&v"(cA[0][3][1]) : "v"(aa[1]) : "memory"); SB();
  asm volatile("s_waitcnt vmcnt(11)" :  :  : "memory"); SB();
  asm volatile("v_mfma_f32_16x16x32_bf16 %0, %1, a[112:115], 0" : "=&v"(ac[4]) : "v"(((u32x4){cA[0][0][0][0], cA[0][0][0][1], cA[0][0][1][0], cA[0][0][1][1]})) : ); SB();
  asm volatile("ds_read_b128 a[104:107], %0 offset:1216" :  : "v"(bb) : "memory"); SB();
  asm volatile("s_waitcnt lgkmcnt(6)\n\tv_mfma_f32_16x16x32_bf16 %0, %1, a[120:123], %0" : "+v"(ac[7]) : "v"(((u32x4){cA[0][1][0][0], cA[0][1][0][1], cA[0][1][1][0], cA[0][1][1][1]})) : ); SB();
  asm volatile("ds_read2_b32 %0, %1 offset0:176 offset1:177" : "=&v"(cA[0][4][0]) : "v"(aa[2]) : "memory"); SB();
  asm volatile("v_mfma_f32_16x16x32_bf16 %0, %1, a[116:119], %0" : "+v"(ac[6]) : "v"(((u32x4){cA[0][1][0][0], cA[0][1][0][1], cA[0][1][1][0], cA[0][1][1][1]})) : ); SB();
  asm volatile("ds_read2_b32 %0, %1 offset0:178 offset1:179" : "=&v"(cA[0][4][1]) : "v"(aa[2]) : "memory"); SB();
  asm volatile("v_mfma_f32_16x16x32_bf16 %0, %1, a[112:115], %0" : "+v"(ac[5]) : "v"(((u32x4){cA[0][1][0][0], cA[0][1][0][1], cA[0][1][1][0], cA[0][1][1][1]})) : ); SB();
  asm volatile("ds_read2_b32 %0, %1 offset0:48 offset1:49" : "=&v"(cA[0][5][0]) : "v"(aa[2]) : "memory"); SB();
  asm volatile("ds_read_b128 a[36:39], %0 offset:34816" :  : "v"(aA[0]) : "memory"); SB();
  asm volatile("v_mfma_f32_16x16x32_bf16 %0, %1, a[108:111], %0" : "+v"(ac[4]) : "v"(((u32x4){cA[0][1][0][0], cA[0][1][0][1], cA[0][1][1][0], cA[0][1][1][1]})) : ); SB();
  asm volatile("ds_read2_b32 %0, %1 offset0:50 offset1:51" : "=&v"(cA[0][5][1]) : "v"(aa[2]) : "memory"); SB();
  asm volatile("ds_read_b128 a[52:55], %0 offset:34816" :  : "v"(aA[1]) : "memory"); SB();
  asm volatile("s_waitcnt lgkmcnt(9)\n\tv_mfma_f32_16x16x32_bf16 %0, %1, a[116:119], %0" : "+v"(ac[7]) : "v"(((u32x4){cA[0][2][0][0], cA[0][2][0][1], cA[0][2][1][0], cA[0][2][1][1]})) : ); SB();
  asm volatile("ds_read_b128 a[100:103], %0 offset:704" :  : "v"(bb) : "memory"); SB();
  asm volatile("buffer_load_dwordx4 v[172:175], %0, %1, %2 offen offset:1024 sc1" :  : "v"(vB), "s"(rWS), "s"(65536u) : "memory"); SB();
  asm volatile("v_mfma_f32_16x16x32_bf16 %0, %1, a[112:115], %0" : "+v"(ac[6]) : "v"(((u32x4){cA[0][2][0][0], cA[0][2][0][1], cA[0][2][1][0], cA[0][2][1][1]})) : ); SB();
  asm volatile("ds_read2_b32 %0, %1 offset0:176 offset1:177" : "=&v"(cA[0][6][0]) : "v"(aa[3]) : "memory"); SB();
  asm volatile("s_waitcnt vmcnt(10)" :  :  : "memory"); SB();
  asm volatile("v_mfma_f32_16x16x32_bf16 %0, %1, a[108:111], %0" : "+v"(ac[5]) : "v"(((u32x4){cA[0][2][0][0], cA[0][2][0][1], cA[0][2][1][0], cA[0][2][1][1]})) : ); SB();
  asm volatile("ds_read2_b32 %0, %1 offset0:178 offset1:179" : "=&v"(cA[0][6][1]) : "v"(aa[3]) : "memory"); SB();
  asm volatile("ds_read_b128 a[40:43], %0 offset:36864" :  : "v"(aA[0]) : "memory"); SB();
  asm volatile("v_mfma_f32_16x16x32_bf16 %0, %1, a[108:111], 0" : "=&v"(ac[3]) : "v"(((u32x4){cA[0][0][0][0], cA[0][0][0][1], cA[0][0][1][0], cA[0][0][1][1]})) : ); SB();
  asm volatile("ds_read2_b32 %0, %1 offset0:48 offset1:49" : "=&v"(cA[0][7][0]) : "v"(aa[3]) : "memory"); SB();
  asm volatile("ds_read_b128 a[56:59], %0 offset:36864" :  : "v"(aA[1]) : "memory"); SB();
  asm volatile("s_waitcnt lgkmcnt(11)\n\tv_mfma_f32_16x16x32_bf16 %0, %1, a[112:115], %0" : "+v"(ac[7]) : "v"(((u32x4){cA[0][3][0][0], cA[0][3][0][1], cA[0][3][1][0], cA[0][3][1][1]})) : ); SB();
  asm volatile("ds_read2_b32 %0, %1 offset0:50 offset1:51" : "=&v"(cA[0][7][1]) : "v"(aa[3]) : "memory"); SB();
  asm volatile("v_xor_b32 %0, 131072, %0" : "+v"(vB) :  : "memory"); SB();
  asm volatile("v_mfma_f32_16x16x32_bf16 %0, %1, a[108:111], %0" : "+v"(ac[6]) : "v"(((u32x4){cA[0][3][0][0], cA[0][3][0][1], cA[0][3][1][0], cA[0][3][1][1]})) : ); SB();
  asm volatile("ds_read_b128 a[96:99], %0 offset:192" :  : "v"(bb) : "memory"); SB();
  asm volatile("buffer_load_dwordx4 v[176:179], %0, %1, %2 offen offset:0 sc1" :  : "v"(vB), "s"(rWS), "s"(0u) : "memory"); SB();
  asm volatile("v_mfma_f32_16x16x32_bf16 %0, %1, a[104:107], %0" : "+v"(ac[5]) : "v"(((u32x4){cA[0][3][0][0], cA[0][3][0][1], cA[0][3][1][0], cA[0][3][1][1]})) : ); SB();
  asm volatile("ds_read2_b32 %0, %1 offset0:160 offset1:161" : "=&v"(cA[1][0][0]) : "v"(aa[0]) : "memory"); SB();
  asm volatile("buffer_load_dwordx4 v[180:183], %0, %1, %2 offen offset:1024 sc1" :  : "v"(vB), "s"(rWS), "s"(0u) : "memory"); SB();
  asm volatile("v_mfma_f32_16x16x32_bf16 %0, %1, a[104:107], %0" : "+v"(ac[4]) : "v"(((u32x4){cA[0][2][0][0], cA[0][2][0][1], cA[0][2][1][0], cA[0][2][1][1]})) : ); SB();
  asm volatile("s_waitcnt vmcnt(10)" :  :  : "memory"); SB();
  asm volatile("s_waitcnt lgkmcnt(9)\n\tv_mfma_f32_16x16x32_bf16 %0, %1, a[108:111], %0" : "+v"(ac[7]) : "v"(((u32x4){cA[0][4][0][0], cA[0][4][0][1], cA[0][4][1][0], cA[0][4][1][1]})) : ); SB();
  asm volatile("ds_read2_b32 %0, %1 offset0:162 offset1:163" : "=&v"(cA[1][0][1]) : "v"(aa[0]) : "memory"); SB();
  asm volatile("ds_read_b128 a[44:47], %0 offset:38912" :  : "v"(aA[0]) : "memory"); SB();
  asm volatile("v_mfma_f32_16x16x32_bf16 %0, %1, a[104:107], %0" : "+v"(ac[6]) : "v"(((u32x4){cA[0][4][0][0], cA[0][4][0][1], cA[0][4][1][0], cA[0][4][1][1]})) : ); SB();
  asm volatile("ds_read_b128 a[156:159], %0 offset:3712" :  : "v"(bb) : "memory"); SB();
  asm volatile("ds_read_b128 a[60:63], %0 offset:38912" :  : "v"(aA[1]) : "memory"); SB();
  asm volatile("v_mfma_f32_16x16x32_bf16 %0, %1, a[104:107], %0" : "+v"(ac[3]) : "v"(((u32x4){cA[0][1][0][0], cA[0][1][0][1], cA[0][1][1][0], cA[0][1][1][1]})) : ); SB();
  asm volatile("ds_read_b128 a[152:155], %0 offset:3200" :  : "v"(bb) : "memory"); SB();
  asm volatile("buffer_load_dwordx4 v[184:187], %0, %1, %2 offen offset:0 sc1" :  : "v"(vB), "s"(rWS), "s"(65536u) : "memory"); SB();
  asm volatile("v_mfma_f32_16x16x32_bf16 %0, %1, a[104:107], 0" : "=&v"(ac[2]) : "v"(((u32x4){cA[0][0][0][0], cA[0][0][0][1], cA[0][0][1][0], cA[0][0][1][1]})) : ); SB();
  asm volatile("ds_write_b128 %0, %1 offset:0" :  : "v"(ew[0]), "v"(a1[0][0]) : "memory"); SB();
  asm volatile("v_mfma_f32_16x16x32_bf16 %0, %1, a[104:107], %0" : "+v"(ac[7]) : "v"(((u32x4){cA[0][5][0][0], cA[0][5][0][1], cA[0][5][1][0], cA[0][5][1][1]})) : ); SB();
  asm volatile("ds_read_b128 a[128:131], %0 offset:128" :  : "v"(bb) : "memory"); SB();
  asm volatile("s_waitcnt vmcnt(9)" :  :  : "memory"); SB();
  asm volatile("s_waitcnt lgkmcnt(8)\n\tv_mfma_f32_16x16x32_bf16 %0, %1, a[100:103], %0" : "+v"(ac[6]) : "v"(((u32x4){cA[0][5][0][0], cA[0][5][0][1], cA[0][5][1][0], cA[0][5][1][1]})) : ); SB();
  asm volatile("ds_read_b128 a[132:135], %0 offset:640" :  : "v"(bb) : "memory"); SB();
  asm volatile("ds_read_b128 a[64:67], %0 offset:0" :  : "v"(aA[0]) : "memory"); SB();
  asm volatile("v_mfma_f32_16x16x32_bf16 %0, %1, a[100:103], %0" : "+v"(ac[5]) : "v"(((u32x4){cA[0][4][0][0], cA[0][4][0][1], cA[0][4][1][0], cA[0][4][1][1]})) : ); SB();
  asm volatile("ds_read2_b32 %0, %1 offset0:32 offset1:33" : "=&v"(cA[1][1][0]) : "v"(aa[0]) : "memory"); SB();
  asm volatile("ds_read_b128 a[80:83], %0 offset:0" :  : "v"(aA[1]) : "memory"); SB();
  asm volatile("v_mfma_f32_16x16x32_bf16 %0, %1, a[100:103], %0" : "+v"(ac[4]) : "v"(((u32x4){cA[0][3][0][0], cA[0][3][0][1], cA[0][3][1][0], cA[0][3][1][1]})) : ); SB();
  asm volatile("buffer_load_dwordx4 v[188:191], %0, %1, %2 offen offset:1024 sc1" :  : "v"(vB), "s"(rWS), "s"(65536u) : "memory"); SB();
  asm volatile("v_mfma_f32_16x16x32_bf16 %0, %1, a[100:103], %0" : "+v"(ac[7]) : "v"(((u32x4){cA[0][6][0][0], cA[0][6][0][1], cA[0][6][1][0], cA[0][6][1][1]})) : ); SB();
  asm volatile("ds_read2_b32 %0, %1 offset0:34 offset1:35" : "=&v"(cA[1][1][1]) : "v"(aa[0]) : "memory"); SB();
  asm volatile("v_xor_b32 %0, 393216, %0" : "+v"(vB) :  : "memory"); SB();
  asm volatile("v_mfma_f32_16x16x32_bf16 %0, %1, a[100:103], %0" : "+v"(ac[3]) : "v"(((u32x4){cA[0][2][0][0], cA[0][2][0][1], cA[0][2][1][0], cA[0][2][1][1]})) : ); SB();
  asm volatile("ds_read_b128 a[148:151], %0 offset:2688" :  : "v"(bb) : "memory"); SB();
  asm volatile("buffer_load_dwordx4 v[192:195], %0, %1, %2 offen offset:0 sc1" :  : "v"(vB), "s"(rWS), "s"(0u) : "memory"); SB();
  asm volatile("v_mfma_f32_16x16x32_bf16 %0, %1, a[100:103], %0" : "+v"(ac[2]) : "v"(((u32x4){cA[0][1][0][0], cA[0][1][0][1], cA[0][1][1][0], cA[0][1][1][1]})) : ); SB();
  asm volatile("ds_read_b128 a[136:139], %0 offset:1152" :  : "v"(bb) : "memory"); SB();
  asm volatile("s_waitcnt vmcnt(9)" :  :  : "memory"); SB();
  asm volatile("v_mfma_f32_16x16x32_bf16 %0, %1, a[100:103], 0" : "=&v"(ac[1]) : "v"(((u32x4){cA[0][0][0][0], cA[0][0][0][1], cA[0][0][1][0], cA[0][0][1][1]})) : ); SB();
  asm volatile("ds_read_b128 a[68:71], %0 offset:2048" :  : "v"(aA[0]) : "memory"); SB();
  asm volatile("v_mfma_f32_16x16x32_bf16 %0, %1, a[96:99], %0" : "+v"(ac[7]) : "v"(((u32x4){cA[0][7][0][0], cA[0][7][0][1], cA[0][7][1][0], cA[0][7][1][1]})) : ); SB();
  asm volatile("ds_read2_b32 %0, %1 offset0:160 offset1:161" : "=&v"(cA[1][2][0]) : "v"(aa[1]) : "memory"); SB();
  asm volatile("ds_read_b128 a[84:87], %0 offset:2048" :  : "v"(aA[1]) : "memory"); SB();
  asm volatile("v_mfma_f32_16x16x32_bf16 %0, %1, a[96:99], %0" : "+v"(ac[6]) : "v"(((u32x4){cA[0][6][0][0], cA[0][6][0][1], cA[0][6][1][0], cA[0][6][1][1]})) : ); SB();
  asm volatile("ds_read2_b32 %0, %1 offset0:162 offset1:163" : "=&v"(cA[1][2][1]) : "v"(aa[1]) : "memory"); SB();
  asm volatile("buffer_load_dwordx4 v[196:199], %0, %1, %2 offen offset:1024 sc1" :  : "v"(vB), "s"(rWS), "s"(0u) : "memory"); SB();
  asm volatile("v_mfma_f32_16x16x32_bf16 %0, %1, a[96:99], %0" : "+v"(ac[5]) : "v"(((u32x4){cA[0][5][0][0], cA[0][5][0][1], cA[0][5][1][0], cA[0][5][1][1]})) : ); SB();
  asm volatile("ds_read_b128 a[140:143], %0 offset:1664" :  : "v"(bb) : "memory"); SB();
  asm volatile("buffer_load_dwordx4 v[200:203], %0, %1, %2 offen offset:0 sc1" :  : "v"(vB), "s"(rWS), "s"(65536u) : "memory"); SB();
  asm volatile("v_mfma_f32_16x16x32_bf16 %0, %1, a[96:99], %0" : "+v"(ac[4]) : "v"(((u32x4){cA[0][4][0][0], cA[0][4][0][1], cA[0][4][1][0], cA[0][4][1][1]})) : ); SB();
  asm volatile("ds_write_b128 %0, %1 offset:1024" :  : "v"(ew[0]), "v"(a1[0][1]) : "memory"); SB();
  asm volatile("v_mfma_f32_16x16x32_bf16 %0, %1, a[96:99], %0" : "+v"(ac[3]) : "v"(((u32x4){cA[0][3][0][0], cA[0][3][0][1], cA[0][3][1][0], cA[0][3][1][1]})) : ); SB();
  asm volatile("ds_read_b128 a[144:147], %0 offset:2176" :  : "v"(bb) : "memory"); SB();
  asm volatile("buffer_load_dwordx4 v[204:207], %0, %1, %2 offen offset:1024 sc1" :  : "v"(vB), "s"(rWS), "s"(65536u) : "memory"); SB();
  asm volatile("v_mfma_f32_16x16x32_bf16 %0, %1, a[96:99], %0" : "+v"(ac[2]) : "v"(((u32x4){cA[0][2][0][0], cA[0][2][0][1], cA[0][2][1][0], cA[0][2][1][1]})) : ); SB();
  asm volatile("ds_read2_b32 %0, %1 offset0:32 offset1:33" : "=&v"(cA[1][3][0]) : "v"(aa[1]) : "memory"); SB();
  asm volatile("v_mfma_f32_16x16x32_bf16 %0, %1, a[96:99], %0" : "+v"(ac[1]) : "v"(((u32x4){cA[0][1][0][0], cA[0][1][0][1], cA[0][1][1][0], cA[0][1][1][1]})) : ); SB();
  asm volatile("ds_read2_b32 %0, %1 offset0:34 offset1:35" : "=&v"(cA[1][3][1]) : "v"(aa[1]) : "memory"); SB();
  asm volatile("v_mfma_f32_16x16x32_bf16 %0, %1, a[96:99], 0" : "=&v"(ac[0]) : "v"(((u32x4){cA[0][0][0][0], cA[0][0][0][1], cA[0][0][1][0], cA[0][0][1][1]})) : ); SB();
  asm volatile("v_xor_b32 %0, 262144, %0" : "+v"(vB) :  : "memory"); SB();
  asm volatile("s_waitcnt lgkmcnt(7)\n\tv_mfma_f32_16x16x32_bf16 %0, %1, a[156:159], %0" : "+v"(ac[7]) : "v"(((u32x4){cA[1][0][0][0], cA[1][0][0][1], cA[1][0][1][0], cA[1][0][1][1]})) : ); SB();
  asm volatile("ds_read2_b32 %0, %1 offset0:160 offset1:161" : "=&v"(cA[1][4][0]) : "v"(aa[2]) : "memory"); SB();
  asm volatile("v_mfma_f32_16x16x32_bf16 %0, %1, a[152:155], %0" : "+v"(ac[6]) : "v"(((u32x4){cA[1][0][0][0], cA[1][0][0][1], cA[1][0][1][0], cA[1][0][1][1]})) : ); SB();
  asm volatile("ds_read2_b32 %0, %1 offset0:162 offset1:163" : "=&v"(cA[1][4][1]) : "v"(aa[2]) : "memory"); SB();
  asm volatile("v_mfma_f32_16x16x32_bf16 %0, %1, a[128:131], %0" : "+v"(ac[0]) : "v"(((u32x4){cA[1][0][0][0], cA[1][0][0][1], cA[1][0][1][0], cA[1][0][1][1]})) : ); SB();
  asm volatile("ds_read2_b32 %0, %1 offset0:32 offset1:33" : "=&v"(cA[1][5][0]) : "v"(aa[2]) : "memory"); SB();
  asm volatile("v_mfma_f32_16x16x32_bf16 %0, %1, a[132:135], %0" : "+v"(ac[1]) : "v"(((u32x4){cA[1][0][0][0], cA[1][0][0][1], cA[1][0][1][0], cA[1][0][1][1]})) : ); SB();
  asm volatile("buffer_load_dwordx4 v[208:211], %0, %1, %2 offen offset:0 sc1" :  : "v"(vB), "s"(rWS), "s"(2048u) : "memory"); SB();
  asm volatile("v_mfma_f32_16x16x32_bf16 %0, %1, a[152:155], %0" : "+v"(ac[7]) : "v"(((u32x4){cA[1][1][0][0], cA[1][1][0][1], cA[1][1][1][0], cA[1][1][1][1]})) : ); SB();
  asm volatile("ds_read2_b32 %0, %1 offset0:34 offset1:35" : "=&v"(cA[1][5][1]) : "v"(aa[2]) : "memory"); SB();
  asm volatile("v_mfma_f32_16x16x32_bf16 %0, %1, a[148:151], %0" : "+v"(ac[6]) : "v"(((u32x4){cA[1][1][0][0], cA[1][1][0][1], cA[1][1][1][0], cA[1][1][1][1]})) : ); SB();
  asm volatile("ds_read2_b32 %0, %1 offset0:160 offset1:161" : "=&v"(cA[1][6][0]) : "v"(aa[3]) : "memory"); SB();
  asm volatile("v_mfma_f32_16x16x32_bf16 %0, %1, a[136:139], %0" : "+v"(ac[2]) : "v"(((u32x4){cA[1][0][0][0], cA[1][0][0][1], cA[1][0][1][0], cA[1][0][1][1]})) : ); SB();
  asm volatile("ds_read2_b32 %0, %1 offset0:162 offset1:163" : "=&v"(cA[1][6][1]) : "v"(aa[3]) : "memory"); SB();
  asm volatile("v_mfma_f32_16x16x32_bf16 %0, %1, a[128:131], %0" : "+v"(ac[1]) : "v"(((u32x4){cA[1][1][0][0], cA[1][1][0][1], cA[1][1][1][0], cA[1][1][1][1]})) : ); SB();
  asm volatile("ds_write_b128 %0, %1 offset:0" :  : "v"(dd), "v"(ac[0]) : "memory"); SB();
  asm volatile("s_waitcnt lgkmcnt(7)\n\tv_mfma_f32_16x16x32_bf16 %0, %1, a[148:151], %0" : "+v"(ac[7]) : "v"(((u32x4){cA[1][2][0][0], cA[1][2][0][1], cA[1][2][1][0], cA[1][2][1][1]})) : ); SB();
  asm volatile("ds_read2_b32 %0, %1 offset0:32 offset1:33" : "=&v"(cA[1][7][0]) : "v"(aa[3]) : "memory"); SB();
  asm volatile("v_mfma_f32_16x16x32_bf16 %0, %1, a[140:143], %0" : "+v"(ac[3]) : "v"(((u32x4){cA[1][0][0][0], cA[1][0][0][1], cA[1][0][1][0], cA[1][0][1][1]})) : ); SB();
  asm volatile("ds_read2_b32 %0, %1 offset0:34 offset1:35" : "=&v"(cA[1][7][1]) : "v"(aa[3]) : "memory"); SB();
  asm volatile("v_mfma_f32_16x16x32_bf16 %0, %1, a[132:135], %0" : "+v"(ac[2]) : "v"(((u32x4){cA[1][1][0][0], cA[1][1][0][1], cA[1][1][1][0], cA[1][1][1][1]})) : ); SB();
  asm volatile("buffer_load_dwordx4 v[212:215], %0, %1, %2 offen offset:1024 sc1" :  : "v"(vB), "s"(rWS), "s"(2048u) : "memory"); SB();
  asm volatile("v_mfma_f32_16x16x32_bf16 %0, %1, a[144:147], %0" : "+v"(ac[4]) : "v"(((u32x4){cA[1][0][0][0], cA[1][0][0][1], cA[1][0][1][0], cA[1][0][1][1]})) : ); SB();
  asm volatile("ds_write_b128 %0, %1 offset:1024" :  : "v"(dd), "v"(ac[1]) : "memory"); SB();
  asm volatile("v_mfma_f32_16x16x32_bf16 %0, %1, a[144:147], %0" : "+v"(ac[7]) : "v"(((u32x4){cA[1][3][0][0], cA[1][3][0][1], cA[1][3][1][0], cA[1][3][1][1]})) : ); SB();
  asm volatile("buffer_load_dwordx4 v[216:219], %0, %1, %2 offen offset:0 sc1" :  : "v"(vB), "s"(rWS), "s"(67584u) : "memory"); SB();
  asm volatile("v_mfma_f32_16x16x32_bf16 %0, %1, a[136:139], %0" : "+v"(ac[3]) : "v"(((u32x4){cA[1][1][0][0], cA[1][1][0][1], cA[1][1][1][0], cA[1][1][1][1]})) : ); SB();
  asm volatile("buffer_load_dwordx4 v[220:223], %0, %1, %2 offen offset:1024 sc1" :  : "v"(vB), "s"(rWS), "s"(67584u) : "memory"); SB();
  asm volatile("v_mfma_f32_16x16x32_bf16 %0, %1, a[128:131], %0" : "+v"(ac[2]) : "v"(((u32x4){cA[1][2][0][0], cA[1][2][0][1], cA[1][2][1][0], cA[1][2][1][1]})) : ); SB();
  asm volatile("v_xor_b32 %0, 131072, %0" : "+v"(vB) :  : "memory"); SB();
  asm volatile("v_mfma_f32_16x16x32_bf16 %0, %1, a[140:143], %0" : "+v"(ac[4]) : "v"(((u32x4){cA[1][1][0][0], cA[1][1][0][1], cA[1][1][1][0], cA[1][1][1][1]})) : ); SB();
  asm volatile("ds_write_b128 %0, %1 offset:2048" :  : "v"(ew[0]), "v"(a1[0][2]) : "memory"); SB();
  asm volatile("v_mfma_f32_16x16x32_bf16 %0, %1, a[148:151], %0" : "+v"(ac[5]) : "v"(((u32x4){cA[1][0][0][0], cA[1][0][0][1], cA[1][0][1][0], cA[1][0][1][1]})) : ); SB();
  asm volatile("buffer_load_dwordx4 v[224:227], %0, %1, %2 offen offset:0 sc1" :  : "v"(vB), "s"(rWS), "s"(2048u) : "memory"); SB();
  asm volatile("v_mfma_f32_16x16x32_bf16 %0, %1, a[132:135], %0" : "+v"(ac[3]) : "v"(((u32x4){cA[1][2][0][0], cA[1][2][0][1], cA[1][2][1][0], cA[1][2][1][1]})) : ); SB();
  asm volatile("ds_write_b128 %0, %1 offset:0" :  : "v"(ew[1]), "v"(a1[1][0]) : "memory"); SB();
  asm volatile("v_mfma_f32_16x16x32_bf16 %0, %1, a[144:147], %0" : "+v"(ac[6]) : "v"(((u32x4){cA[1][2][0][0], cA[1][2][0][1], cA[1][2][1][0], cA[1][2][1][1]})) : ); SB();
  asm volatile("ds_write_b128 %0, %1 offset:2048" :  : "v"(dd), "v"(ac[2]) : "memory"); SB();
  asm volatile("v_mfma_f32_16x16x32_bf16 %0, %1, a[136:139], %0" : "+v"(ac[4]) : "v"(((u32x4){cA[1][2][0][0], cA[1][2][0][1], cA[1][2][1][0], cA[1][2][1][1]})) : ); SB();
  asm volatile("buffer_load_dwordx4 v[228:231], %0, %1, %2 offen offset:1024 sc1" :  : "v"(vB), "s"(rWS), "s"(2048u) : "memory"); SB();
  asm volatile("v_mfma_f32_16x16x32_bf16 %0, %1, a[144:147], %0" : "+v"(ac[5]) : "v"(((u32x4){cA[1][1][0][0], cA[1][1][0][1], cA[1][1][1][0], cA[1][1][1][1]})) : ); SB();
  asm volatile("ds_write_b128 %0, %1 offset:1024" :  : "v"(ew[1]), "v"(a1[1][1]) : "memory"); SB();
  asm volatile("v_mfma_f32_16x16x32_bf16 %0, %1, a[128:131], %0" : "+v"(ac[3]) : "v"(((u32x4){cA[1][3][0][0], cA[1][3][0][1], cA[1][3][1][0], cA[1][3][1][1]})) : ); SB();
  asm volatile("buffer_load_dwordx4 v[232:235], %0, %1, %2 offen offset:0 sc1" :  : "v"(vB), "s"(rWS), "s"(67584u) : "memory"); SB();
  asm volatile("v_mfma_f32_16x16x32_bf16 %0, %1, a[140:143], %0" : "+v"(ac[6]) : "v"(((u32x4){cA[1][3][0][0], cA[1][3][0][1], cA[1][3][1][0], cA[1][3][1][1]})) : ); SB();
  asm volatile("ds_write_b128 %0, %1 offset:2048" :  : "v"(ew[1]), "v"(a1[1][2]) : "memory"); SB();
  asm volatile("v_mfma_f32_16x16x32_bf16 %0, %1, a[132:135], %0" : "+v"(ac[4]) : "v"(((u32x4){cA[1][3][0][0], cA[1][3][0][1], cA[1][3][1][0], cA[1][3][1][1]})) : ); SB();
  asm volatile("buffer_load_dwordx4 v[236:239], %0, %1, %2 offen offset:1024 sc1" :  : "v"(vB), "s"(rWS), "s"(67584u) : "memory"); SB();
  asm volatile("v_mfma_f32_16x16x32_bf16 %0, %1, a[140:143], %0" : "+v"(ac[5]) : "v"(((u32x4){cA[1][2][0][0], cA[1][2][0][1], cA[1][2][1][0], cA[1][2][1][1]})) : ); SB();
  asm volatile("v_xor_b32 %0, 393216, %0" : "+v"(vB) :  : "memory"); SB();
  asm volatile("s_waitcnt lgkmcnt(3)\n\tv_mfma_f32_16x16x32_bf16 %0, %1, a[140:143], %0" : "+v"(ac[7]) : "v"(((u32x4){cA[1][4][0][0], cA[1][4][0][1], cA[1][4][1][0], cA[1][4][1][1]})) : ); SB();
  asm volatile("ds_write_b128 %0, %1 offset:3072" :  : "v"(dd), "v"(ac[3]) : "memory"); SB();
  asm volatile("v_mfma_f32_16x16x32_bf16 %0, %1, a[136:139], %0" : "+v"(ac[6]) : "v"(((u32x4){cA[1][4][0][0], cA[1][4][0][1], cA[1][4][1][0], cA[1][4][1][1]})) : ); SB();
  asm volatile("buffer_load_dwordx4 v[240:243], %0, %1, %2 offen offset:0 sc1" :  : "v"(vB), "s"(rWS), "s"(2048u) : "memory"); SB();
  asm volatile("v_mfma_f32_16x16x32_bf16 %0, %1, a[128:131], %0" : "+v"(ac[4]) : "v"(((u32x4){cA[1][4][0][0], cA[1][4][0][1], cA[1][4][1][0], cA[1][4][1][1]})) : ); SB();
  asm volatile("v_mfma_f32_16x16x32_bf16 %0, %1, a[136:139], %0" : "+v"(ac[5]) : "v"(((u32x4){cA[1][3][0][0], cA[1][3][0][1], cA[1][3][1][0], cA[1][3][1][1]})) : ); SB();
  asm volatile("v_mfma_f32_16x16x32_bf16 %0, %1, a[136:139], %0" : "+v"(ac[7]) : "v"(((u32x4){cA[1][5][0][0], cA[1][5][0][1], cA[1][5][1][0], cA[1][5][1][1]})) : ); SB();
  asm volatile("v_mfma_f32_16x16x32_bf16 %0, %1, a[132:135], %0" : "+v"(ac[6]) : "v"(((u32x4){cA[1][5][0][0], cA[1][5][0][1], cA[1][5][1][0], cA[1][5][1][1]})) : ); SB();
  asm volatile("v_mfma_f32_16x16x32_bf16 %0, %1, a[132:135], %0" : "+v"(ac[5]) : "v"(((u32x4){cA[1][4][0][0], cA[1][4][0][1], cA[1][4][1][0], cA[1][4][1][1]})) : ); SB();
  asm volatile("ds_write_b128 %0, %1 offset:4096" :  : "v"(dd), "v"(ac[4]) : "memory"); SB();
  asm volatile("v_mfma_f32_16x16x32_bf16 %0, %1, a[132:135], %0" : "+v"(ac[7]) : "v"(((u32x4){cA[1][6][0][0], cA[1][6][0][1], cA[1][6][1][0], cA[1][6][1][1]})) : ); SB();
  asm volatile("buffer_load_dwordx4 v[244:247], %0, %1, %2 offen offset:1024 sc1" :  : "v"(vB), "s"(rWS), "s"(2048u) : "memory"); SB();
  asm volatile("v_mfma_f32_16x16x32_bf16 %0, %1, a[128:131], %0" : "+v"(ac[6]) : "v"(((u32x4){cA[1][6][0][0], cA[1][6][0][1], cA[1][6][1][0], cA[1][6][1][1]})) : ); SB();
  asm volatile("buffer_load_dwordx4 v[248:251], %0, %1, %2 offen offset:0 sc1" :  : "v"(vB), "s"(rWS), "s"(67584u) : "memory"); SB();
  asm volatile("v_mfma_f32_16x16x32_bf16 %0, %1, a[128:131], %0" : "+v"(ac[5]) : "v"(((u32x4){cA[1][5][0][0], cA[1][5][0][1], cA[1][5][1][0], cA[1][5][1][1]})) : ); SB();
  asm volatile("v_mfma_f32_16x16x32_bf16 %0, %1, a[128:131], %0" : "+v"(ac[7]) : "v"(((u32x4){cA[1][7][0][0], cA[1][7][0][1], cA[1][7][1][0], cA[1][7][1][1]})) : ); SB();
  asm volatile("s_nop 5" : "+v"(ac[5]), "+v"(ac[6]), "+v"(ac[7]) :  : ); SB();
  asm volatile("ds_write_b128 %0, %1 offset:6144" :  : "v"(dd), "v"(ac[6]) : "memory"); SB();
  asm volatile("ds_write_b128 %0, %1 offset:5120" :  : "v"(dd), "v"(ac[5]) : "memory"); SB();
  asm volatile("ds_write_b128 %0, %1 offset:7168" :  : "v"(dd), "v"(ac[7]) : "memory"); SB();
  } else {
  asm volatile("; ELSE_BEGIN"); SB();
  asm volatile("s_waitcnt lgkmcnt(0)" ::: "memory"); SB();
  asm volatile("s_waitcnt vmcnt(11)" :  :  : "memory"); SB();
  asm volatile("s_waitcnt lgkmcnt(0)" ::: "memory"); SB();
  asm volatile("ds_read_b128 a[36:39], %0 offset:34816" :  : "v"(aA[0]) : "memory"); SB();
  asm volatile("s_waitcnt lgkmcnt(0)" ::: "memory"); SB();
  asm volatile("ds_read_b128 a[52:55], %0 offset:34816" :  : "v"(aA[1]) : "memory"); SB();
  asm volatile("s_waitcnt lgkmcnt(0)" ::: "memory"); SB();
  asm volatile("buffer_load_dwordx4 v[172:175], %0, %1, %2 offen offset:1024 sc1" :  : "v"(vB), "s"(rWS), "s"(65536u) : "memory"); SB();
  asm volatile("s_waitcnt lgkmcnt(0)" ::: "memory"); SB();
  asm volatile("s_waitcnt vmcnt(10)" :  :  : "memory"); SB();
  asm volatile("s_waitcnt lgkmcnt(0)" ::: "memory"); SB();
  asm volatile("ds_read_b128 a[40:43], %0 offset:36864" :  : "v"(aA[0]) : "memory"); SB();
  asm volatile("s_waitcnt lgkmcnt(0)" ::: "memory"); SB();
  asm volatile("ds_read_b128 a[56:59], %0 offset:36864" :  : "v"(aA[1]) : "memory"); SB();
  asm volatile("s_waitcnt lgkmcnt(0)" ::: "memory"); SB();
  asm volatile("v_xor_b32 %0, 131072, %0" : "+v"(vB) :  : "memory"); SB();
  asm volatile("s_waitcnt lgkmcnt(0)" ::: "memory"); SB();
  asm volatile("buffer_load_dwordx4 v[176:179], %0, %1, %2 offen offset:0 sc1" :  : "v"(vB), "s"(rWS), "s"(0u) : "memory"); SB();
  asm volatile("s_waitcnt lgkmcnt(0)" ::: "memory"); SB();
  asm volatile("buffer_load_dwordx4 v[180:183], %0, %1, %2 offen offset:1024 sc1" :  : "v"(vB), "s"(rWS), "s"(0u) : "memory"); SB();
  asm volatile("s_waitcnt lgkmcnt(0)" ::: "memory"); SB();
  asm volatile("s_waitcnt vmcnt(10)" :  :  : "memory"); SB();
  asm volatile("s_waitcnt lgkmcnt(0)" ::: "memory"); SB();
  asm volatile("ds_read_b128 a[44:47], %0 offset:38912" :  : "v"(aA[0]) : "memory"); SB();
  asm volatile("s_waitcnt lgkmcnt(0)" ::: "memory"); SB();
  asm volatile("ds_read_b128 a[60:63], %0 offset:38912" :  : "v"(aA[1]) : "memory"); SB();
  asm volatile("s_waitcnt lgkmcnt(0)" ::: "memory"); SB();
  asm volatile("buffer_load_dwordx4 v[184:187], %0, %1, %2 offen offset:0 sc1" :  : "v"(vB), "s"(rWS), "s"(65536u) : "memory"); SB();
  asm volatile("s_waitcnt lgkmcnt(0)" ::: "memory"); SB();
  asm volatile("ds_write_b128 %0, %1 offset:0" :  : "v"(ew[0]), "v"(a1[0][0]) : "memory"); SB();
  asm volatile("s_waitcnt lgkmcnt(0)" ::: "memory"); SB();
  asm volatile("s_waitcnt vmcnt(9)" :  :  : "memory"); SB();
  asm volatile("s_waitcnt lgkmcnt(0)" ::: "memory"); SB();
  asm volatile("ds_read_b128 a[64:67], %0 offset:0" :  : "v"(aA[0]) : "memory"); SB();
  asm volatile("s_waitcnt lgkmcnt(0)" ::: "memory"); SB();
  asm volatile("ds_read_b128 a[80:83], %0 offset:0" :  : "v"(aA[1]) : "memory"); SB();
  asm volatile("s_waitcnt lgkmcnt(0)" ::: "memory"); SB();
  asm volatile("buffer_load_dwordx4 v[188:191], %0, %1, %2 offen offset:1024 sc1" :  : "v"(vB), "s"(rWS), "s"(65536u) : "memory"); SB();
  asm volatile("s_waitcnt lgkmcnt(0)" ::: "memory"); SB();
  asm volatile("v_xor_b32 %0, 393216, %0" : "+v"(vB) :  : "memory"); SB();
  asm volatile("s_waitcnt lgkmcnt(0)" ::: "memory"); SB();
  asm volatile("buffer_load_dwordx4 v[192:195], %0, %1, %2 offen offset:0 sc1" :  : "v"(vB), "s"(rWS), "s"(0u) : "memory"); SB();
  asm volatile("s_waitcnt lgkmcnt(0)" ::: "memory"); SB();
  asm volatile("s_waitcnt vmcnt(9)" :  :  : "memory"); SB();
  asm volatile("s_waitcnt lgkmcnt(0)" ::: "memory"); SB();
  asm volatile("ds_read_b128 a[68:71], %0 offset:2048" :  : "v"(aA[0]) : "memory"); SB();
  asm volatile("s_waitcnt lgkmcnt(0)" ::: "memory"); SB();
  asm volatile("ds_read_b128 a[84:87], %0 offset:2048" :  : "v"(aA[1]) : "memory"); SB();
  asm volatile("s_waitcnt lgkmcnt(0)" ::: "memory"); SB();
  asm volatile("buffer_load_dwordx4 v[196:199], %0, %1, %2 offen offset:1024 sc1" :  : "v"(vB), "s"(rWS), "s"(0u) : "memory"); SB();
  asm volatile("s_waitcnt lgkmcnt(0)" ::: "memory"); SB();
  asm volatile("buffer_load_dwordx4 v[200:203], %0, %1, %2 offen offset:0 sc1" :  : "v"(vB), "s"(rWS), "s"(65536u) : "memory"); SB();
  asm volatile("s_waitcnt lgkmcnt(0)" ::: "memory"); SB();
  asm volatile("ds_write_b128 %0, %1 offset:1024" :  : "v"(ew[0]), "v"(a1[0][1]) : "memory"); SB();
  asm volatile("s_waitcnt lgkmcnt(0)" ::: "memory"); SB();
  asm volatile("buffer_load_dwordx4 v[204:207], %0, %1, %2 offen offset:1024 sc1" :  : "v"(vB), "s"(rWS), "s"(65536u) : "memory"); SB();
  asm volatile("s_waitcnt lgkmcnt(0)" ::: "memory"); SB();
  asm volatile("v_xor_b32 %0, 262144, %0" : "+v"(vB) :  : "memory"); SB();
  asm volatile("s_waitcnt lgkmcnt(0)" ::: "memory"); SB();
  asm volatile("buffer_load_dwordx4 v[208:211], %0, %1, %2 offen offset:0 sc1" :  : "v"(vB), "s"(rWS), "s"(2048u) : "memory"); SB();
  asm volatile("s_waitcnt lgkmcnt(0)" ::: "memory"); SB();
  asm volatile("buffer_load_dwordx4 v[212:215], %0, %1, %2 offen offset:1024 sc1" :  : "v"(vB), "s"(rWS), "s"(2048u) : "memory"); SB();
  asm volatile("s_waitcnt lgkmcnt(0)" ::: "memory"); SB();
  asm volatile("buffer_load_dwordx4 v[216:219], %0, %1, %2 offen offset:0 sc1" :  : "v"(vB), "s"(rWS), "s"(67584u) : "memory"); SB();
  asm volatile("s_waitcnt lgkmcnt(0)" ::: "memory"); SB();
  asm volatile("buffer_load_dwordx4 v[220:223], %0, %1, %2 offen offset:1024 sc1" :  : "v"(vB), "s"(rWS), "s"(67584u) : "memory"); SB();
  asm volatile("s_waitcnt lgkmcnt(0)" ::: "memory"); SB();
  asm volatile("v_xor_b32 %0, 131072, %0" : "+v"(vB) :  : "memory"); SB();
  asm volatile("s_waitcnt lgkmcnt(0)" ::: "memory"); SB();
  asm volatile("ds_write_b128 %0, %1 offset:2048" :  : "v"(ew[0]), "v"(a1[0][2]) : "memory"); SB();
  asm volatile("s_waitcnt lgkmcnt(0)" ::: "memory"); SB();
  asm volatile("buffer_load_dwordx4 v[224:227], %0, %1, %2 offen offset:0 sc1" :  : "v"(vB), "s"(rWS), "s"(2048u) : "memory"); SB();
  asm volatile("s_waitcnt lgkmcnt(0)" ::: "memory"); SB();
  asm volatile("ds_write_b128 %0, %1 offset:0" :  : "v"(ew[1]), "v"(a1[1][0]) : "memory"); SB();
  asm volatile("s_waitcnt lgkmcnt(0)" ::: "memory"); SB();
  asm volatile("buffer_load_dwordx4 v[228:231], %0, %1, %2 offen offset:1024 sc1" :  : "v"(vB), "s"(rWS), "s"(2048u) : "memory"); SB();
  asm volatile("s_waitcnt lgkmcnt(0)" ::: "memory"); SB();
  asm volatile("ds_write_b128 %0, %1 offset:1024" :  : "v"(ew[1]), "v"(a1[1][1]) : "memory"); SB();
  asm volatile("s_waitcnt lgkmcnt(0)" ::: "memory"); SB();
  asm volatile("buffer_load_dwordx4 v[232:235], %0, %1, %2 offen offset:0 sc1" :  : "v"(vB), "s"(rWS), "s"(67584u) : "memory"); SB();
  asm volatile("s_waitcnt lgkmcnt(0)" ::: "memory"); SB();
  asm volatile("ds_write_b128 %0, %1 offset:2048" :  : "v"(ew[1]), "v"(a1[1][2]) : "memory"); SB();
  asm volatile("s_waitcnt lgkmcnt(0)" ::: "memory"); SB();
  asm volatile("buffer_load_dwordx4 v[236:239], %0, %1, %2 offen offset:1024 sc1" :  : "v"(vB), "s"(rWS), "s"(67584u) : "memory"); SB();
  asm volatile("s_waitcnt lgkmcnt(0)" ::: "memory"); SB();
  asm volatile("v_xor_b32 %0, 393216, %0" : "+v"(vB) :  : "memory"); SB();
  asm volatile("s_waitcnt lgkmcnt(0)" ::: "memory"); SB();
  asm volatile("buffer_load_dwordx4 v[240:243], %0, %1, %2 offen offset:0 sc1" :  : "v"(vB), "s"(rWS), "s"(2048u) : "memory"); SB();
  asm volatile("s_waitcnt lgkmcnt(0)" ::: "memory"); SB();
  asm volatile("buffer_load_dwordx4 v[244:247], %0, %1, %2 offen offset:1024 sc1" :  : "v"(vB), "s"(rWS), "s"(2048u) : "memory"); SB();
  asm volatile("s_waitcnt lgkmcnt(0)" ::: "memory"); SB();
  asm volatile("buffer_load_dwordx4 v[248:251], %0, %1, %2 offen offset:0 sc1" :  : "v"(vB), "s"(rWS), "s"(67584u) : "memory"); SB();
  asm volatile("s_waitcnt lgkmcnt(0) ; ELSE_END" ::: "memory"); SB();
  }
  ST(15);
  f32x4 g[2][4][2], pr[6], yq[8], bs4 = {bs, bs, bs, bs};
  u32x4 ho[3], ykv, y2;
  float gt[4], ge[4];
  unsigned vH_, pw2, pe[3], vX, vO, vO2, vP, vFH, vFY, vHr, hso[8], ys2, ydr, ykr, er, sFH, sFY;
  int dl;
  asm volatile("" : "+v"(bs4)); SB();
  asm volatile("s_waitcnt vmcnt(21)" :  :  : "memory"); SB();
  asm volatile("v_mfma_f32_16x16x32_bf16 %0, v[160:163], a[160:163], %1" : "=&v"(g[0][0][0]) : "v"(bs4) : ); SB();
  { unsigned lane = lane0, w = w0, rb = rb0, cb = cb0;
  asm volatile("" : "+v"(lane)); asm volatile("" : "+s"(w), "+s"(rb), "+s"(cb)); SB();

  asm volatile("v_mfma_f32_16x16x32_bf16 %0, v[160:163], a[164:167], 0" : "=&v"(g[0][1][0]) :  : ); SB();
  er = lb + 32768 + w * 6144 + l16; asm volatile("" : "+v"(er)); SB();
  asm volatile("v_mfma_f32_16x16x32_bf16 %0, v[160:163], a[168:171], 0" : "=&v"(g[0][2][0]) :  : ); SB();
  asm volatile("s_waitcnt lgkmcnt(0)\n\ts_barrier" :  :  : "memory"); SB();
  asm volatile("ds_read_b128 %0, %1 offset:0" : "=&v"(a1[0][0]) : "v"(er) : "memory"); SB();
  asm volatile("v_mfma_f32_16x16x32_bf16 %0, v[160:163], a[172:175], 0" : "=&v"(g[0][3][0]) :  : ); SB();
  asm volatile("buffer_load_dwordx4 v[252:255], %0, %1, %2 offen offset:1024 sc1" :  : "v"(vB), "s"(rWS), "s"(67584u) : "memory"); SB();
  asm volatile("v_mfma_f32_16x16x32_bf16 %0, v[164:167], a[160:163], %1" : "=&v"(g[0][0][1]) : "v"(bs4) : ); SB();
  asm volatile("ds_read_b128 %0, %1 offset:1024" : "=&v"(a1[0][1]) : "v"(er) : "memory"); SB();
  asm volatile("v_mfma_f32_16x16x32_bf16 %0, v[164:167], a[164:167], 0" : "=&v"(g[0][1][1]) :  : ); SB();
  asm volatile("ds_read_b128 %0, %1 offset:2048" : "=&v"(a1[0][2]) : "v"(er) : "memory"); SB();
  asm volatile("v_mfma_f32_16x16x32_bf16 %0, v[164:167], a[168:171], 0" : "=&v"(g[0][2][1]) :  : ); SB();
  asm volatile("ds_read_b128 %0, %1 offset:3072" : "=&v"(a1[1][0]) : "v"(er) : "memory"); SB();
  asm volatile("v_mfma_f32_16x16x32_bf16 %0, v[164:167], a[172:175], 0" : "=&v"(g[0][3][1]) :  : ); SB();
  asm volatile("ds_read_b128 %0, %1 offset:4096" : "=&v"(a1[1][1]) : "v"(er) : "memory"); SB();
  ST(25);
  asm volatile("s_waitcnt vmcnt(20)" :  :  : "memory"); SB();
  asm volatile("v_mfma_f32_16x16x32_bf16 %0, v[168:171], a[176:179], %0" : "+v"(g[0][0][0]) :  : ); SB();
  asm volatile("v_xor_b32 %0, 131072, %0" : "+v"(vB) :  : "memory"); SB();
  asm volatile("v_mfma_f32_16x16x32_bf16 %0, v[168:171], a[180:183], %0" : "+v"(g[0][1][0]) :  : ); SB();
  asm volatile("buffer_load_dwordx4 v[160:163], %0, %1, %2 offen offset:0 sc1" :  : "v"(vB), "s"(rWS), "s"(0u) : "memory"); SB();
  asm volatile("v_mfma_f32_16x16x32_bf16 %0, v[168:171], a[184:187], %0" : "+v"(g[0][2][0]) :  : ); SB();
  asm volatile("ds_read_b128 %0, %1 offset:5120" : "=&v"(a1[1][2]) : "v"(er) : "memory"); SB();
  asm volatile("v_mfma_f32_16x16x32_bf16 %0, v[168:171], a[188:191], %0" : "+v"(g[0][3][0]) :  : ); SB();
  ydr = lb + 0 + (w * 2 + (lane >> 5)) * 1024 + (lane & 1) * 512; asm volatile("" : "+v"(ydr)); SB();
  asm volatile("v_mfma_f32_16x16x32_bf16 %0, v[172:175], a[176:179], %0" : "+v"(g[0][0][1]) :  : ); SB();
  asm volatile("buffer_load_dwordx4 v[164:167], %0, %1, %2 offen offset:1024 sc1" :  : "v"(vB), "s"(rWS), "s"(0u) : "memory"); SB();
  asm volatile("v_mfma_f32_16x16x32_bf16 %0, v[172:175], a[180:183], %0" : "+v"(g[0][1][1]) :  : ); SB();
  ydr = ydr + ((((lane >> 1) & 15) ^ ((lane & 1) << 3)) << 4); asm volatile("" : "+v"(ydr)); SB();
  asm volatile("v_mfma_f32_16x16x32_bf16 %0, v[172:175], a[184:187], %0" : "+v"(g[0][2][1]) :  : ); SB();
  ykr = lb + 59520 + 512 + w * 1024 + l16; asm volatile("" : "+v"(ykr)); SB();
  asm volatile("v_mfma_f32_16x16x32_bf16 %0, v[172:175], a[188:191], %0" : "+v"(g[0][3][1]) :  : ); SB();
  asm volatile("s_add_u32 m0, %0, 102400" :  : "s"(mA) : "memory", "scc"); SB();
  pw2 = er + (122880 - 32768); asm volatile("" : "+v"(pw2)); SB();
  asm volatile("s_waitcnt vmcnt(20)" :  :  : "memory"); SB();
  asm volatile("v_mfma_f32_16x16x32_bf16 %0, v[176:179], a[192:195], %0" : "+v"(g[0][0][0]) :  : ); SB();
  asm volatile("buffer_load_dwordx4 %0, %1, %2 offen lds" :  : "v"(voS), "s"(rA), "s"(ao[4]) : "memory"); SB();
  asm volatile("v_mfma_f32_16x16x32_bf16 %0, v[176:179], a[196:199], %0" : "+v"(g[0][1][0]) :  : ); SB();
  pe[0] = lb + 122880 + (w ^ 1) * 6144 + l16; asm volatile("" : "+v"(pe[0])); SB();
  asm volatile("v_mfma_f32_16x16x32_bf16 %0, v[176:179], a[200:203], %0" : "+v"(g[0][2][0]) :  : ); SB();
  pe[1] = lb + 122880 + (w ^ 2) * 6144 + l16; asm volatile("" : "+v"(pe[1])); SB();
  asm volatile("v_mfma_f32_16x16x32_bf16 %0, v[176:179], a[204:207], %0" : "+v"(g[0][3][0]) :  : ); SB();
  asm volatile("buffer_load_dwordx4 v[168:171], %0, %1, %2 offen offset:0 sc1" :  : "v"(vB), "s"(rWS), "s"(65536u) : "memory"); SB();
  asm volatile("v_mfma_f32_16x16x32_bf16 %0, v[180:183], a[192:195], %0" : "+v"(g[0][0][1]) :  : ); SB();
  pe[2] = lb + 122880 + (w ^ 3) * 6144 + l16; asm volatile("" : "+v"(pe[2])); SB();
  asm volatile("v_mfma_f32_16x16x32_bf16 %0, v[180:183], a[196:199], %0" : "+v"(g[0][1][1]) :  : ); SB();
  asm volatile("s_waitcnt lgkmcnt(0)" : "+v"(a1[0][0]), "+v"(a1[0][1]), "+v"(a1[0][2]), "+v"(a1[1][0]), "+v"(a1[1][1]), "+v"(a1[1][2]) :  : "memory"); SB();
  asm volatile("buffer_load_dwordx4 v[172:175], %0, %1, %2 offen offset:1024 sc1" :  : "v"(vB), "s"(rWS), "s"(65536u) : "memory"); SB();
  asm volatile("v_mfma_f32_16x16x32_bf16 %0, v[180:183], a[200:203], %0" : "+v"(g[0][2][1]) :  : ); SB();
  dl = (int)(wm * 12928 - wn * 256 + w * 128) - 2 * 12928; asm volatile("" : "+s"(dl)); SB();
  asm volatile("v_mfma_f32_16x16x32_bf16 %0, v[180:183], a[204:207], %0" : "+v"(g[0][3][1]) :  : ); SB();
  asm volatile("v_xor_b32 %0, 917504, %0" : "+v"(vB) :  : "memory"); SB();
  asm volatile("s_waitcnt vmcnt(21)" :  :  : "memory"); SB();
  asm volatile("v_mfma_f32_16x16x32_bf16 %0, v[184:187], a[208:211], %0" : "+v"(g[0][0][0]) :  : ); SB();
  asm volatile("buffer_load_dwordx4 v[176:179], %0, %1, %2 offen offset:0 sc1" :  : "v"(vB), "s"(rWS), "s"(0u) : "memory"); SB();
  asm volatile("v_add_u32 %0, %1, %0" : "+v"(aa[0]) : "s"(dl) : ); SB();
  asm volatile("v_mfma_f32_16x16x32_bf16 %0, v[184:187], a[212:215], %0" : "+v"(g[0][1][0]) :  : ); SB();
  asm volatile("v_add_u32 %0, %1, %0" : "+v"(aa[1]) : "s"(dl) : ); SB();
  asm volatile("v_mfma_f32_16x16x32_bf16 %0, v[184:187], a[216:219], %0" : "+v"(g[0][2][0]) :  : ); SB();
  asm volatile("v_add_u32 %0, %1, %0" : "+v"(aa[2]) : "s"(dl) : ); SB();
  asm volatile("v_mfma_f32_16x16x32_bf16 %0, v[184:187], a[220:223], %0" : "+v"(g[0][3][0]) :  : ); SB();
  asm volatile("v_add_u32 %0, %1, %0" : "+v"(aa[3]) : "s"(dl) : ); SB();
  asm volatile("v_mfma_f32_16x16x32_bf16 %0, v[188:191], a[208:211], %0" : "+v"(g[0][0][1]) :  : ); SB();
  asm volatile("buffer_load_dwordx4 v[180:183], %0, %1, %2 offen offset:1024 sc1" :  : "v"(vB), "s"(rWS), "s"(0u) : "memory"); SB();
  asm volatile("v_add_u32 %0, %1, %0" : "+v"(bb) : "s"(dl) : ); SB();
  asm volatile("v_mfma_f32_16x16x32_bf16 %0, v[188:191], a[212:215], %0" : "+v"(g[0][1][1]) :  : ); SB();
  asm volatile("v_mul_f32 %0, %1, %1" : "=v"(gt[0]) : "v"(a1[0][0][0]) : ); SB();
  asm volatile("v_mfma_f32_16x16x32_bf16 %0, v[188:191], a[216:219], %0" : "+v"(g[0][2][1]) :  : ); SB();
  asm volatile("v_mul_f32 %0, %1, %1" : "=v"(gt[1]) : "v"(a1[0][0][1]) : ); SB();
  asm volatile("v_mfma_f32_16x16x32_bf16 %0, v[188:191], a[220:223], %0" : "+v"(g[0][3][1]) :  : ); SB();
  asm volatile("buffer_load_dwordx4 %0, %1, %2 offen offset:1024 lds" :  : "v"(voS), "s"(rA), "s"(ao[5]) : "memory"); SB();
  asm volatile("s_waitcnt vmcnt(22)" :  :  : "memory"); SB();
  asm volatile("v_mfma_f32_16x16x32_bf16 %0, v[192:195], a[224:227], %0" : "+v"(g[0][0][0]) :  : ); SB();
  asm volatile("v_mul_f32 %0, %1, %1" : "=v"(gt[2]) : "v"(a1[0][0][2]) : ); SB();
  asm volatile("v_mfma_f32_16x16x32_bf16 %0, v[192:195], a[228:231], %0" : "+v"(g[0][1][0]) :  : ); SB();
  asm volatile("v_mul_f32 %0, %1, %1" : "=v"(gt[3]) : "v"(a1[0][0][3]) : ); SB();
  asm volatile("v_mfma_f32_16x16x32_bf16 %0, v[192:195], a[232:235], %0" : "+v"(g[0][2][0]) :  : ); SB();
  asm volatile("v_fma_f32 %0, %0, %1, %2" : "+v"(gt[0]) : "v"(k2[0]), "v"(k1[0]) : ); SB();
  asm volatile("v_mfma_f32_16x16x32_bf16 %0, v[192:195], a[236:239], %0" : "+v"(g[0][3][0]) :  : ); SB();
  asm volatile("buffer_load_dwordx4 %0, %1, %2 offen offset:2048 lds" :  : "v"(voS), "s"(rA), "s"(ao[6]) : "memory"); SB();
  asm volatile("v_fma_f32 %0, %0, %1, %2" : "+v"(gt[1]) : "v"(k2[0]), "v"(k1[0]) : ); SB();
  asm volatile("v_mfma_f32_16x16x32_bf16 %0, v[196:199], a[224:227], %0" : "+v"(g[0][0][1]) :  : ); SB();
  asm volatile("v_fma_f32 %0, %0, %1, %2" : "+v"(gt[2]) : "v"(k2[0]), "v"(k1[0]) : ); SB();
  asm volatile("v_mfma_f32_16x16x32_bf16 %0, v[196:199], a[228:231], %0" : "+v"(g[0][1][1]) :  : ); SB();
  asm volatile("v_fma_f32 %0, %0, %1, %2" : "+v"(gt[3]) : "v"(k2[0]), "v"(k1[0]) : ); SB();
  asm volatile("v_mfma_f32_16x16x32_bf16 %0, v[196:199], a[232:235], %0" : "+v"(g[0][2][1]) :  : ); SB();
  asm volatile("buffer_load_dwordx4 %0, %1, %2 offen offset:3072 lds" :  : "v"(voS), "s"(rA), "s"(ao[7]) : "memory"); SB();
  asm volatile("v_mul_f32 %0, %0, %1" : "+v"(gt[0]) : "v"(a1[0][0][0]) : ); SB();
  asm volatile("v_mfma_f32_16x16x32_bf16 %0, v[196:199], a[236:239], %0" : "+v"(g[0][3][1]) :  : ); SB();
  asm volatile("v_xor_b32 %0, 384, %0" : "+v"(voS) :  : "memory"); SB();
  asm volatile("s_waitcnt vmcnt(22)" :  :  : "memory"); SB();
  asm volatile("v_mfma_f32_16x16x32_bf16 %0, v[200:203], a[240:243], %0" : "+v"(g[0][0][0]) :  : ); SB();
  asm volatile("v_mul_f32 %0, %0, %1" : "+v"(gt[1]) : "v"(a1[0][0][1]) : ); SB();
  asm volatile("v_mfma_f32_16x16x32_bf16 %0, v[200:203], a[244:247], %0" : "+v"(g[0][1][0]) :  : ); SB();
  asm volatile("v_mul_f32 %0, %0, %1" : "+v"(gt[2]) : "v"(a1[0][0][2]) : ); SB();
  asm volatile("v_mfma_f32_16x16x32_bf16 %0, v[200:203], a[248:251], %0" : "+v"(g[0][2][0]) :  : ); SB();
  asm volatile("buffer_load_dwordx4 v[184:187], %0, %1, %2 offen offset:0 sc1" :  : "v"(vB), "s"(rWS), "s"(65536u) : "memory"); SB();
  asm volatile("v_mul_f32 %0, %0, %1" : "+v"(gt[3]) : "v"(a1[0][0][3]) : ); SB();
  asm volatile("v_mfma_f32_16x16x32_bf16 %0, v[200:203], a[252:255], %0" : "+v"(g[0][3][0]) :  : ); SB();
  asm volatile("v_exp_f32 %0, %1" : "=v"(ge[0]) : "v"(gt[0]) : ); SB();
  asm volatile("v_mfma_f32_16x16x32_bf16 %0, v[204:207], a[240:243], %0" : "+v"(g[0][0][1]) :  : ); SB();
  asm volatile("v_exp_f32 %0, %1" : "=v"(ge[1]) : "v"(gt[1]) : ); SB();
  asm volatile("v_mfma_f32_16x16x32_bf16 %0, v[204:207], a[244:247], %0" : "+v"(g[0][1][1]) :  : ); SB();
  asm volatile("buffer_load_dwordx4 v[188:191], %0, %1, %2 offen offset:1024 sc1" :  : "v"(vB), "s"(rWS), "s"(65536u) : "memory"); SB();
  asm volatile("v_exp_f32 %0, %1" : "=v"(ge[2]) : "v"(gt[2]) : ); SB();
  asm volatile("v_mfma_f32_16x16x32_bf16 %0, v[204:207], a[248:251], %0" : "+v"(g[0][2][1]) :  : ); SB();
  asm volatile("v_xor_b32 %0, 131072, %0" : "+v"(vB) :  : "memory"); SB();
  asm volatile("v_exp_f32 %0, %1" : "=v"(ge[3]) : "v"(gt[3]) : ); SB();
  asm volatile("v_mfma_f32_16x16x32_bf16 %0, v[204:207], a[252:255], %0" : "+v"(g[0][3][1]) :  : ); SB();
  asm volatile("v_fma_f32 %0, %0, %1, %1" : "+v"(ge[0]) : "v"(xd[0]) : ); SB();
  asm volatile("s_waitcnt vmcnt(22)" :  :  : "memory"); SB();
  asm volatile("v_mfma_f32_16x16x32_bf16 %0, v[208:211], a[160:163], %1" : "=&v"(g[1][0][0]) : "v"(bs4) : ); SB();
  asm volatile("buffer_load_dwordx4 v[192:195], %0, %1, %2 offen offset:0 sc1" :  : "v"(vB), "s"(rWS), "s"(0u) : "memory"); SB();
  asm volatile("v_fma_f32 %0, %0, %1, %1" : "+v"(ge[1]) : "v"(xd[0]) : ); SB();
  asm volatile("v_mfma_f32_16x16x32_bf16 %0, v[208:211], a[164:167], 0" : "=&v"(g[1][1][0]) :  : ); SB();
  asm volatile("v_fma_f32 %0, %0, %1, %1" : "+v"(ge[2]) : "v"(xd[0]) : ); SB();
  asm volatile("v_mfma_f32_16x16x32_bf16 %0, v[208:211], a[168:171], 0" : "=&v"(g[1][2][0]) :  : ); SB();
  asm volatile("v_fma_f32 %0, %0, %1, %1" : "+v"(ge[3]) : "v"(xd[0]) : ); SB();
  asm volatile("v_mfma_f32_16x16x32_bf16 %0, v[208:211], a[172:175], 0" : "=&v"(g[1][3][0]) :  : ); SB();
  asm volatile("v_rcp_f32 %0, %1" : "=v"(gt[0]) : "v"(ge[0]) : ); SB();
  asm volatile("v_mfma_f32_16x16x32_bf16 %0, v[212:215], a[160:163], %1" : "=&v"(g[1][0][1]) : "v"(bs4) : ); SB();
  asm volatile("v_rcp_f32 %0, %1" : "=v"(gt[1]) : "v"(ge[1]) : ); SB();
  asm volatile("v_mfma_f32_16x16x32_bf16 %0, v[212:215], a[164:167], 0" : "=&v"(g[1][1][1]) :  : ); SB();
  asm volatile("buffer_load_dwordx4 v[196:199], %0, %1, %2 offen offset:1024 sc1" :  : "v"(vB), "s"(rWS), "s"(0u) : "memory"); SB();
  asm volatile("v_rcp_f32 %0, %1" : "=v"(gt[2]) : "v"(ge[2]) : ); SB();
  asm volatile("v_mfma_f32_16x16x32_bf16 %0, v[212:215], a[168:171], 0" : "=&v"(g[1][2][1]) :  : ); SB();
  asm volatile("v_rcp_f32 %0, %1" : "=v"(gt[3]) : "v"(ge[3]) : ); SB();
  asm volatile("v_mfma_f32_16x16x32_bf16 %0, v[212:215], a[172:175], 0" : "=&v"(g[1][3][1]) :  : ); SB();
  asm volatile("v_mul_f32 %0, %0, %1" : "+v"(a1[0][0][0]) : "v"(gt[0]) : ); SB();
  asm volatile("s_waitcnt vmcnt(22)" :  :  : "memory"); SB();
  asm volatile("v_mfma_f32_16x16x32_bf16 %0, v[216:219], a[176:179], %0" : "+v"(g[1][0][0]) :  : ); SB();
  asm volatile("v_mul_f32 %0, %0, %1" : "+v"(a1[0][0][1]) : "v"(gt[1]) : ); SB();
  asm volatile("v_mfma_f32_16x16x32_bf16 %0, v[216:219], a[180:183], %0" : "+v"(g[1][1][0]) :  : ); SB();
  asm volatile("v_mul_f32 %0, %0, %1" : "+v"(a1[0][0][2]) : "v"(gt[2]) : ); SB();
  asm volatile("v_mfma_f32_16x16x32_bf16 %0, v[216:219], a[184:187], %0" : "+v"(g[1][2][0]) :  : ); SB();
  asm volatile("v_mul_f32 %0, %0, %1" : "+v"(a1[0][0][3]) : "v"(gt[3]) : ); SB();
  asm volatile("v_mfma_f32_16x16x32_bf16 %0, v[216:219], a[188:191], %0" : "+v"(g[1][3][0]) :  : ); SB();
  asm volatile("s_add_u32 m0, %0, 131072" :  : "s"(mA) : "memory", "scc"); SB();
  asm volatile("v_mul_f32 %0, %1, %1" : "=v"(gt[0]) : "v"(a1[0][1][0]) : ); SB();
  asm volatile("v_mfma_f32_16x16x32_bf16 %0, v[220:223], a[176:179], %0" : "+v"(g[1][0][1]) :  : ); SB();
  asm volatile("buffer_load_dwordx4 %0, %1, %2 offen lds" :  : "v"(voS), "s"(rA), "s"(ao[0]) : "memory"); SB();
  asm volatile("v_mul_f32 %0, %1, %1" : "=v"(gt[1]) : "v"(a1[0][1][1]) : ); SB();
  asm volatile("v_mfma_f32_16x16x32_bf16 %0, v[220:223], a[180:183], %0" : "+v"(g[1][1][1]) :  : ); SB();
  asm volatile("v_mul_f32 %0, %1, %1" : "=v"(gt[2]) : "v"(a1[0][1][2]) : ); SB();
  asm volatile("v_mfma_f32_16x16x32_bf16 %0, v[220:223], a[184:187], %0" : "+v"(g[1][2][1]) :  : ); SB();
  asm volatile("buffer_load_dwordx4 %0, %1, %2 offen offset:1024 lds" :  : "v"(voS), "s"(rA), "s"(ao[1]) : "memory"); SB();
  asm volatile("v_mul_f32 %0, %1, %1" : "=v"(gt[3]) : "v"(a1[0][1][3]) : ); SB();
  asm volatile("v_mfma_f32_16x16x32_bf16 %0, v[220:223], a[188:191], %0" : "+v"(g[1][3][1]) :  : ); SB();
  asm volatile("v_fma_f32 %0, %0, %1, %2" : "+v"(gt[0]) : "v"(k2[0]), "v"(k1[0]) : ); SB();
  ST(26);
  asm volatile("s_waitcnt vmcnt(22)" :  :  : "memory"); SB();
  asm volatile("v_mfma_f32_16x16x32_bf16 %0, v[224:227], a[192:195], %0" : "+v"(g[1][0][0]) :  : ); SB();
  vH_ = vH; asm volatile("" : "+v"(vH_)); SB();
  asm volatile("v_mfma_f32_16x16x32_bf16 %0, v[224:227], a[196:199], %0" : "+v"(g[1][1][0]) :  : ); SB();
  vO = W_WO + (cb * 120 + w * 30 + 0u * 15) * 1024 + l16; asm volatile("" : "+v"(vO)); SB();
  asm volatile("v_mfma_f32_16x16x32_bf16 %0, v[224:227], a[200:203], %0" : "+v"(g[1][2][0]) :  : ); SB();
  vO2 = vO + 15360 - 0u * 30720; asm volatile("" : "+v"(vO2)); SB();
  asm volatile("v_mfma_f32_16x16x32_bf16 %0, v[224:227], a[204:207], %0" : "+v"(g[1][3][0]) :  : ); SB();
  vFH = W_FH + rb * 256 + w * 64 + (lane & 7) * 8; asm volatile("" : "+v"(vFH)); SB();
  asm volatile("v_mfma_f32_16x16x32_bf16 %0, v[228:231], a[192:195], %0" : "+v"(g[1][0][1]) :  : ); SB();
  vHr = W_HH + (rb * 96 + w * 6) * 1024 + l16; asm volatile("" : "+v"(vHr)); SB();
  asm volatile("v_mfma_f32_16x16x32_bf16 %0, v[228:231], a[196:199], %0" : "+v"(g[1][1][1]) :  : ); SB();
  vX = (rb * 64 + w * 16 + l15) * 1280; asm volatile("" : "+v"(vX)); SB();
  asm volatile("v_mfma_f32_16x16x32_bf16 %0, v[228:231], a[200:203], %0" : "+v"(g[1][2][1]) :  : ); SB();
  vX = vX + cb * 160 + 8 * g4; asm volatile("" : "+v"(vX)); SB();
  asm volatile("v_mfma_f32_16x16x32_bf16 %0, v[228:231], a[204:207], %0" : "+v"(g[1][3][1]) :  : ); SB();
  sFH = W_FH + ((rb * 8 + cb) * 4 + w) * 8; asm volatile("" : "+s"(sFH)); SB();
  asm volatile("s_waitcnt vmcnt(20)" :  :  : "memory"); SB();
  asm volatile("v_mfma_f32_16x16x32_bf16 %0, v[232:235], a[208:211], %0" : "+v"(g[1][0][0]) :  : ); SB();
  asm volatile("buffer_load_dwordx4 %0, %1, %2 offen offset:2048 lds" :  : "v"(voS), "s"(rA), "s"(ao[2]) : "memory"); SB();
  asm volatile("v_mov_b32 %0, -1" : "=v"(ho[2][0]) :  : ); SB();
  asm volatile("v_mfma_f32_16x16x32_bf16 %0, v[232:235], a[212:215], %0" : "+v"(g[1][1][0]) :  : ); SB();
  asm volatile("buffer_load_dwordx4 %0, %1, %2 offen offset:3072 lds" :  : "v"(voS), "s"(rA), "s"(ao[3]) : "memory"); SB();
  asm volatile("v_mov_b32 %0, -1" : "=v"(ho[2][1]) :  : ); SB();
  asm volatile("v_mfma_f32_16x16x32_bf16 %0, v[232:235], a[216:219], %0" : "+v"(g[1][2][0]) :  : ); SB();
  asm volatile("v_mov_b32 %0, -1" : "=v"(ho[2][2]) :  : ); SB();
  asm volatile("v_mfma_f32_16x16x32_bf16 %0, v[232:235], a[220:223], %0" : "+v"(g[1][3][0]) :  : ); SB();
  asm volatile("v_mov_b32 %0, -1" : "=v"(ho[2][3]) :  : ); SB();
  asm volatile("v_mfma_f32_16x16x32_bf16 %0, v[236:239], a[208:211], %0" : "+v"(g[1][0][1]) :  : ); SB();
  hso[0] = (w ^ 0) * 24576 + 0u * 3072; asm volatile("" : "+s"(hso[0])); SB();
  hso[1] = hso[0] ^ 3072; asm volatile("" : "+s"(hso[1])); SB();
  asm volatile("v_mfma_f32_16x16x32_bf16 %0, v[236:239], a[212:215], %0" : "+v"(g[1][1][1]) :  : ); SB();
  hso[2] = (w ^ 1) * 24576 + 0u * 3072; asm volatile("" : "+s"(hso[2])); SB();
  hso[3] = hso[2] ^ 3072; asm volatile("" : "+s"(hso[3])); SB();
  asm volatile("v_mfma_f32_16x16x32_bf16 %0, v[236:239], a[216:219], %0" : "+v"(g[1][2][1]) :  : ); SB();
  hso[4] = (w ^ 2) * 24576 + 0u * 3072; asm volatile("" : "+s"(hso[4])); SB();
  hso[5] = hso[4] ^ 3072; asm volatile("" : "+s"(hso[5])); SB();
  asm volatile("v_mfma_f32_16x16x32_bf16 %0, v[236:239], a[220:223], %0" : "+v"(g[1][3][1]) :  : ); SB();
  hso[6] = (w ^ 3) * 24576 + 0u * 3072; asm volatile("" : "+s"(hso[6])); SB();
  asm volatile("s_waitcnt vmcnt(20)" :  :  : "memory"); SB();
  asm volatile("v_mfma_f32_16x16x32_bf16 %0, v[240:243], a[224:227], %0" : "+v"(g[1][0][0]) :  : ); SB();
  hso[7] = hso[6] ^ 3072; asm volatile("" : "+s"(hso[7])); SB();
  asm volatile("s_add_u32 m0, %0, 135168" :  : "s"(mA) : "memory", "scc"); SB();
  asm volatile("v_fma_f32 %0, %0, %1, %2" : "+v"(gt[1]) : "v"(k2[0]), "v"(k1[0]) : ); SB();
  asm volatile("v_mfma_f32_16x16x32_bf16 %0, v[240:243], a[228:231], %0" : "+v"(g[1][1][0]) :  : ); SB();
  asm volatile("buffer_load_dwordx4 %0, %1, %2 offen lds" :  : "v"(voS), "s"(rA), "s"(ao[4]) : "memory"); SB();
  asm volatile("v_fma_f32 %0, %0, %1, %2" : "+v"(gt[2]) : "v"(k2[0]), "v"(k1[0]) : ); SB();
  asm volatile("v_mfma_f32_16x16x32_bf16 %0, v[240:243], a[232:235], %0" : "+v"(g[1][2][0]) :  : ); SB();
  asm volatile("buffer_load_dwordx4 %0, %1, %2 offen offset:1024 lds" :  : "v"(voS), "s"(rA), "s"(ao[5]) : "memory"); SB();
  asm volatile("s_waitcnt vmcnt(12)" :  :  : "memory"); SB();
  asm volatile("v_mfma_f32_16x16x32_bf16 %0, v[240:243], a[236:239], %0" : "+v"(g[1][3][0]) :  : ); SB();
  asm volatile("ds_read_b128 a[72:75], %0 offset:4096" :  : "v"(aA[0]) : "memory"); SB();
  asm volatile("v_fma_f32 %0, %0, %1, %2" : "+v"(gt[3]) : "v"(k2[0]), "v"(k1[0]) : ); SB();
  asm volatile("v_mfma_f32_16x16x32_bf16 %0, v[244:247], a[224:227], %0" : "+v"(g[1][0][1]) :  : ); SB();
  asm volatile("ds_read_b128 a[88:91], %0 offset:4096" :  : "v"(aA[1]) : "memory"); SB();
  asm volatile("v_mul_f32 %0, %0, %1" : "+v"(gt[0]) : "v"(a1[0][1][0]) : ); SB();
  asm volatile("v_mfma_f32_16x16x32_bf16 %0, v[244:247], a[228:231], %0" : "+v"(g[1][1][1]) :  : ); SB();
  asm volatile("buffer_load_dwordx4 %0, %1, %2 offen offset:2048 lds" :  : "v"(voS), "s"(rA), "s"(ao[6]) : "memory"); SB();
  asm volatile("v_mul_f32 %0, %0, %1" : "+v"(gt[1]) : "v"(a1[0][1][1]) : ); SB();
  asm volatile("v_mfma_f32_16x16x32_bf16 %0, v[244:247], a[232:235], %0" : "+v"(g[1][2][1]) :  : ); SB();
  asm volatile("v_mul_f32 %0, %0, %1" : "+v"(gt[2]) : "v"(a1[0][1][2]) : ); SB();
  asm volatile("v_mfma_f32_16x16x32_bf16 %0, v[244:247], a[236:239], %0" : "+v"(g[1][3][1]) :  : ); SB();
  asm volatile("v_mul_f32 %0, %0, %1" : "+v"(gt[3]) : "v"(a1[0][1][3]) : ); SB();
  asm volatile("v_mfma_f32_16x16x32_bf16 %0, v[248:251], a[240:243], %0" : "+v"(g[1][0][0]) :  : ); SB();
  asm volatile("buffer_load_dwordx4 %0, %1, %2 offen offset:3072 lds" :  : "v"(voS), "s"(rA), "s"(ao[7]) : "memory"); SB();
  asm volatile("v_exp_f32 %0, %1" : "=v"(ge[0]) : "v"(gt[0]) : ); SB();
  asm volatile("v_mfma_f32_16x16x32_bf16 %0, v[248:251], a[244:247], %0" : "+v"(g[1][1][0]) :  : ); SB();
  asm volatile("s_waitcnt vmcnt(12)" :  :  : "memory"); SB();
  asm volatile("ds_read_b128 a[76:79], %0 offset:6144" :  : "v"(aA[0]) : "memory"); SB();
  asm volatile("v_mfma_f32_16x16x32_bf16 %0, v[248:251], a[248:251], %0" : "+v"(g[1][2][0]) :  : ); SB();
  asm volatile("ds_read_b128 a[92:95], %0 offset:6144" :  : "v"(aA[1]) : "memory"); SB();
  asm volatile("v_exp_f32 %0, %1" : "=v"(ge[1]) : "v"(gt[1]) : ); SB();
  asm volatile("v_mfma_f32_16x16x32_bf16 %0, v[248:251], a[252:255], %0" : "+v"(g[1][3][0]) :  : ); SB();
  asm volatile("buffer_load_dwordx4 v[200:203], %0, %1, %2 offen offset:0 sc1" :  : "v"(vB), "s"(rWS), "s"(65536u) : "memory"); SB();
  asm volatile("v_exp_f32 %0, %1" : "=v"(ge[2]) : "v"(gt[2]) : ); SB();
  asm volatile("v_mfma_f32_16x16x32_bf16 %0, v[252:255], a[240:243], %0" : "+v"(g[1][0][1]) :  : ); SB();
  asm volatile("v_xor_b32 %0, 128, %0" : "+v"(voS) :  : "memory"); SB();
  asm volatile("v_exp_f32 %0, %1" : "=v"(ge[3]) : "v"(gt[3]) : ); SB();
  asm volatile("v_mfma_f32_16x16x32_bf16 %0, v[252:255], a[244:247], %0" : "+v"(g[1][1][1]) :  : ); SB();
  asm volatile("v_fma_f32 %0, %0, %1, %1" : "+v"(ge[0]) : "v"(xd[0]) : ); SB();
  asm volatile("v_mfma_f32_16x16x32_bf16 %0, v[252:255], a[248:251], %0" : "+v"(g[1][2][1]) :  : ); SB();
  asm volatile("buffer_load_dwordx4 v[204:207], %0, %1, %2 offen offset:1024 sc1" :  : "v"(vB), "s"(rWS), "s"(65536u) : "memory"); SB();
  asm volatile("v_fma_f32 %0, %0, %1, %1" : "+v"(ge[1]) : "v"(xd[0]) : ); SB();
  asm volatile("v_mfma_f32_16x16x32_bf16 %0, v[252:255], a[252:255], %0" : "+v"(g[1][3][1]) :  : ); SB();
  asm volatile("v_xor_b32 %0, 393216, %0" : "+v"(vB) :  : "memory"); SB();
  asm volatile("v_mfma_f32_16x16x32_bf16 %0, v[160:163], a[0:3], %0" : "+v"(g[0][0][0]) :  : ); SB();
  asm volatile("v_fma_f32 %0, %0, %1, %1" : "+v"(ge[2]) : "v"(xd[0]) : ); SB();
  asm volatile("v_mfma_f32_16x16x32_bf16 %0, v[160:163], a[4:7], %0" : "+v"(g[0][1][0]) :  : ); SB();
  asm volatile("v_fma_f32 %0, %0, %1, %1" : "+v"(ge[3]) : "v"(xd[0]) : ); SB();
  asm volatile("v_mfma_f32_16x16x32_bf16 %0, v[160:163], a[8:11], %0" : "+v"(g[0][2][0]) :  : ); SB();
  asm volatile("buffer_load_dwordx4 v[208:211], %0, %1, %2 offen offset:0 sc1" :  : "v"(vB), "s"(rWS), "s"(0u) : "memory"); SB();
  asm volatile("v_rcp_f32 %0, %1" : "=v"(gt[0]) : "v"(ge[0]) : ); SB();
  asm volatile("v_mfma_f32_16x16x32_bf16 %0, v[160:163], a[12:15], %0" : "+v"(g[0][3][0]) :  : ); SB();
  asm volatile("v_rcp_f32 %0, %1" : "=v"(gt[1]) : "v"(ge[1]) : ); SB();
  asm volatile("v_mfma_f32_16x16x32_bf16 %0, v[164:167], a[0:3], %0" : "+v"(g[0][0][1]) :  : ); SB();
  asm volatile("v_rcp_f32 %0, %1" : "=v"(gt[2]) : "v"(ge[2]) : ); SB();
  asm volatile("v_mfma_f32_16x16x32_bf16 %0, v[164:167], a[4:7], %0" : "+v"(g[0][1][1]) :  : ); SB();
  asm volatile("buffer_load_dwordx4 v[212:215], %0, %1, %2 offen offset:1024 sc1" :  : "v"(vB), "s"(rWS), "s"(0u) : "memory"); SB();
  asm volatile("v_rcp_f32 %0, %1" : "=v"(gt[3]) : "v"(ge[3]) : ); SB();
  asm volatile("v_mfma_f32_16x16x32_bf16 %0, v[164:167], a[8:11], %0" : "+v"(g[0][2][1]) :  : ); SB();
  asm volatile("v_mul_f32 %0, %0, %1" : "+v"(a1[0][1][0]) : "v"(gt[0]) : ); SB();
  asm volatile("v_mfma_f32_16x16x32_bf16 %0, v[164:167], a[12:15], %0" : "+v"(g[0][3][1]) :  : ); SB();
  asm volatile("v_mul_f32 %0, %0, %1" : "+v"(a1[0][1][1]) : "v"(gt[1]) : ); SB();
  asm volatile("v_mfma_f32_16x16x32_bf16 %0, v[168:171], a[16:19], %0" : "+v"(g[0][0][0]) :  : ); SB();
  asm volatile("s_add_u32 m0, %0, 98304" :  : "s"(mA) : "memory", "scc"); SB();
  asm volatile("v_mul_f32 %0, %0, %1" : "+v"(a1[0][1][2]) : "v"(gt[2]) : ); SB();
  asm volatile("v_mfma_f32_16x16x32_bf16 %0, v[168:171], a[20:23], %0" : "+v"(g[0][1][0]) :  : ); SB();
  asm volatile("buffer_load_dwordx4 %0, %1, %2 offen lds" :  : "v"(voS), "s"(rA), "s"(ao[0]) : "memory"); SB();
  asm volatile("v_mul_f32 %0, %0, %1" : "+v"(a1[0][1][3]) : "v"(gt[3]) : ); SB();
  asm volatile("v_mfma_f32_16x16x32_bf16 %0, v[168:171], a[24:27], %0" : "+v"(g[0][2][0]) :  : ); SB();
  asm volatile("v_mul_f32 %0, %1, %1" : "=v"(gt[0]) : "v"(a1[0][2][0]) : ); SB();
  asm volatile("v_mfma_f32_16x16x32_bf16 %0, v[168:171], a[28:31], %0" : "+v"(g[0][3][0]) :  : ); SB();
  asm volatile("v_mul_f32 %0, %1, %1" : "=v"(gt[1]) : "v"(a1[0][2][1]) : ); SB();
  asm volatile("v_mfma_f32_16x16x32_bf16 %0, v[172:175], a[16:19], %0" : "+v"(g[0][0][1]) :  : ); SB();
  asm volatile("buffer_load_dwordx4 %0, %1, %2 offen offset:1024 lds" :  : "v"(voS), "s"(rA), "s"(ao[1]) : "memory"); SB();
  asm volatile("v_mul_f32 %0, %1, %1" : "=v"(gt[2]) : "v"(a1[0][2][2]) : ); SB();
  asm volatile("v_mfma_f32_16x16x32_bf16 %0, v[172:175], a[20:23], %0" : "+v"(g[0][1][1]) :  : ); SB();
  asm volatile("v_mul_f32 %0, %1, %1" : "=v"(gt[3]) : "v"(a1[0][2][3]) : ); SB();
  asm volatile("v_mfma_f32_16x16x32_bf16 %0, v[172:175], a[24:27], %0" : "+v"(g[0][2][1]) :  : ); SB();
  asm volatile("v_fma_f32 %0, %0, %1, %2" : "+v"(gt[0]) : "v"(k2[0]), "v"(k1[0]) : ); SB();
  asm volatile("v_mfma_f32_16x16x32_bf16 %0, v[172:175], a[28:31], %0" : "+v"(g[0][3][1]) :  : ); SB();
  asm volatile("s_waitcnt vmcnt(12)" :  :  : "memory"); SB();
  asm volatile("v_fma_f32 %0, %0, %1, %2" : "+v"(gt[1]) : "v"(k2[0]), "v"(k1[0]) : ); SB();
  asm volatile("v_mfma_f32_16x16x32_bf16 %0, v[176:179], a[32:35], %0" : "+v"(g[0][0][0]) :  : ); SB();
  asm volatile("ds_read_b128 a[96:99], %0 offset:32768" :  : "v"(aA[0]) : "memory"); SB();
  asm volatile("v_fma_f32 %0, %0, %1, %2" : "+v"(gt[2]) : "v"(k2[0]), "v"(k1[0]) : ); SB();
  asm volatile("v_mfma_f32_16x16x32_bf16 %0, v[176:179], a[36:39], %0" : "+v"(g[0][1][0]) :  : ); SB();
  asm volatile("ds_read_b128 a[112:115], %0 offset:32768" :  : "v"(aA[1]) : "memory"); SB();
  asm volatile("v_fma_f32 %0, %0, %1, %2" : "+v"(gt[3]) : "v"(k2[0]), "v"(k1[0]) : ); SB();
  asm volatile("v_mfma_f32_16x16x32_bf16 %0, v[176:179], a[40:43], %0" : "+v"(g[0][2][0]) :  : ); SB();
  asm volatile("buffer_load_dwordx4 %0, %1, %2 offen offset:2048 lds" :  : "v"(voS), "s"(rA), "s"(ao[2]) : "memory"); SB();
  asm volatile("v_mul_f32 %0, %0, %1" : "+v"(gt[0]) : "v"(a1[0][2][0]) : ); SB();
  asm volatile("v_mfma_f32_16x16x32_bf16 %0, v[176:179], a[44:47], %0" : "+v"(g[0][3][0]) :  : ); SB();
  asm volatile("buffer_load_dwordx4 %0, %1, %2 offen offset:3072 lds" :  : "v"(voS), "s"(rA), "s"(ao[3]) : "memory"); SB();
  asm volatile("s_waitcnt lgkmcnt(2)" :  :  : "memory"); SB();
  asm volatile("v_mfma_f32_16x16x32_bf16 %0, v[180:183], a[32:35], %0" : "+v"(g[0][0][1]) :  : ); SB();
  asm volatile("v_mul_f32 %0, %0, %1" : "+v"(gt[1]) : "v"(a1[0][2][1]) : ); SB();
  asm volatile("v_mfma_f32_16x16x32_bf16 %0, v[180:183], a[36:39], %0" : "+v"(g[0][1][1]) :  : ); SB();
  asm volatile("v_mul_f32 %0, %0, %1" : "+v"(gt[2]) : "v"(a1[0][2][2]) : ); SB();
  asm volatile("v_mfma_f32_16x16x32_bf16 %0, v[180:183], a[40:43], %0" : "+v"(g[0][2][1]) :  : ); SB();
  asm volatile("v_mul_f32 %0, %0, %1" : "+v"(gt[3]) : "v"(a1[0][2][3]) : ); SB();
  asm volatile("v_mfma_f32_16x16x32_bf16 %0, v[180:183], a[44:47], %0" : "+v"(g[0][3][1]) :  : ); SB();
  asm volatile("s_add_u32 m0, %0, 102400" :  : "s"(mA) : "memory", "scc"); SB();
  asm volatile("v_exp_f32 %0, %1" : "=v"(ge[0]) : "v"(gt[0]) : ); SB();
  asm volatile("v_mfma_f32_16x16x32_bf16 %0, v[184:187], a[48:51], %0" : "+v"(g[0][0][0]) :  : ); SB();
  asm volatile("buffer_load_dwordx4 %0, %1, %2 offen lds" :  : "v"(voS), "s"(rA), "s"(ao[4]) : "memory"); SB();
  asm volatile("v_exp_f32 %0, %1" : "=v"(ge[1]) : "v"(gt[1]) : ); SB();
  asm volatile("v_mfma_f32_16x16x32_bf16 %0, v[184:187], a[52:55], %0" : "+v"(g[0][1][0]) :  : ); SB();
  asm volatile("v_exp_f32 %0, %1" : "=v"(ge[2]) : "v"(gt[2]) : ); SB();
  asm volatile("v_mfma_f32_16x16x32_bf16 %0, v[184:187], a[56:59], %0" : "+v"(g[0][2][0]) :  : ); SB();
  asm volatile("s_waitcnt vmcnt(13)" :  :  : "memory"); SB();
  asm volatile("ds_read_b128 a[100:103], %0 offset:34816" :  : "v"(aA[0]) : "memory"); SB();
  asm volatile("v_mfma_f32_16x16x32_bf16 %0, v[184:187], a[60:63], %0" : "+v"(g[0][3][0]) :  : ); SB();
  asm volatile("ds_read_b128 a[116:119], %0 offset:34816" :  : "v"(aA[1]) : "memory"); SB();
  asm volatile("v_exp_f32 %0, %1" : "=v"(ge[3]) : "v"(gt[3]) : ); SB();
  asm volatile("v_mfma_f32_16x16x32_bf16 %0, v[188:191], a[48:51], %0" : "+v"(g[0][0][1]) :  : ); SB();
  asm volatile("buffer_load_dwordx4 %0, %1, %2 offen offset:1024 lds" :  : "v"(voS), "s"(rA), "s"(ao[5]) : "memory"); SB();
  asm volatile("v_fma_f32 %0, %0, %1, %1" : "+v"(ge[0]) : "v"(xd[0]) : ); SB();
  asm volatile("v_mfma_f32_16x16x32_bf16 %0, v[188:191], a[52:55], %0" : "+v"(g[0][1][1]) :  : ); SB();
  asm volatile("v_fma_f32 %0, %0, %1, %1" : "+v"(ge[1]) : "v"(xd[0]) : ); SB();
  asm volatile("v_mfma_f32_16x16x32_bf16 %0, v[188:191], a[56:59], %0" : "+v"(g[0][2][1]) :  : ); SB();
  asm volatile("v_fma_f32 %0, %0, %1, %1" : "+v"(ge[2]) : "v"(xd[0]) : ); SB();
  asm volatile("v_mfma_f32_16x16x32_bf16 %0, v[188:191], a[60:63], %0" : "+v"(g[0][3][1]) :  : ); SB();
  asm volatile("v_fma_f32 %0, %0, %1, %1" : "+v"(ge[3]) : "v"(xd[0]) : ); SB();
  ST(27);
  asm volatile("v_mfma_f32_16x16x32_bf16 %0, v[192:195], a[64:67], %0" : "+v"(g[0][0][0]) :  : ); SB();
  asm volatile("buffer_load_dwordx4 %0, %1, %2 offen offset:2048 lds" :  : "v"(voS), "s"(rA), "s"(ao[6]) : "memory"); SB();
  asm volatile("v_rcp_f32 %0, %1" : "=v"(gt[0]) : "v"(ge[0]) : ); SB();
  asm volatile("v_mfma_f32_16x16x32_bf16 %0, v[192:195], a[68:71], %0" : "+v"(g[0][1][0]) :  : ); SB();
  asm volatile("v_rcp_f32 %0, %1" : "=v"(gt[1]) : "v"(ge[1]) : ); SB();
  asm volatile("v_mfma_f32_16x16x32_bf16 %0, v[192:195], a[72:75], %0" : "+v"(g[0][2][0]) :  : ); SB();
  asm volatile("v_rcp_f32 %0, %1" : "=v"(gt[2]) : "v"(ge[2]) : ); SB();
  asm volatile("v_mfma_f32_16x16x32_bf16 %0, v[192:195], a[76:79], %0" : "+v"(g[0][3][0]) :  : ); SB();
  asm volatile("s_waitcnt vmcnt(13)" :  :  : "memory"); SB();
  asm volatile("ds_read_b128 a[104:107], %0 offset:36864" :  : "v"(aA[0]) : "memory"); SB();
  asm volatile("v_mfma_f32_16x16x32_bf16 %0, v[196:199], a[64:67], %0" : "+v"(g[0][0][1]) :  : ); SB();
  asm volatile("ds_read_b128 a[120:123], %0 offset:36864" :  : "v"(aA[1]) : "memory"); SB();
  asm volatile("v_rcp_f32 %0, %1" : "=v"(gt[3]) : "v"(ge[3]) : ); SB();
  asm volatile("v_mfma_f32_16x16x32_bf16 %0, v[196:199], a[68:71], %0" : "+v"(g[0][1][1]) :  : ); SB();
  asm volatile("buffer_load_dwordx4 %0, %1, %2 offen offset:3072 lds" :  : "v"(voS), "s"(rA), "s"(ao[7]) : "memory"); SB();
  asm volatile("v_mul_f32 %0, %0, %1" : "+v"(a1[0][2][0]) : "v"(gt[0]) : ); SB();
  asm volatile("v_mfma_f32_16x16x32_bf16 %0, v[196:199], a[72:75], %0" : "+v"(g[0][2][1]) :  : ); SB();
  asm volatile("v_mul_f32 %0, %0, %1" : "+v"(a1[0][2][1]) : "v"(gt[1]) : ); SB();
  asm volatile("v_mfma_f32_16x16x32_bf16 %0, v[196:199], a[76:79], %0" : "+v"(g[0][3][1]) :  : ); SB();
  asm volatile("buffer_load_dwordx4 v[216:219], %0, %1, %2 offen offset:0 sc1" :  : "v"(vB), "s"(rWS), "s"(65536u) : "memory"); SB();
  asm volatile("s_waitcnt vmcnt(13)" :  :  : "memory"); SB();
  asm volatile("ds_read_b128 a[108:111], %0 offset:38912" :  : "v"(aA[0]) : "memory"); SB();
  asm volatile("ds_read_b128 a[124:127], %0 offset:38912" :  : "v"(aA[1]) : "memory"); SB();
  asm volatile("buffer_load_dwordx4 v[220:223], %0, %1, %2 offen offset:1024 sc1" :  : "v"(vB), "s"(rWS), "s"(65536u) : "memory"); SB();
  asm volatile("s_waitcnt vmcnt(12)" :  :  : "memory"); SB();
  asm volatile("v_mfma_f32_16x16x32_bf16 %0, v[200:203], a[80:83], %0" : "+v"(g[0][0][0]) :  : ); SB();
  asm volatile("v_xor_b32 %0, 131072, %0" : "+v"(vB) :  : "memory"); SB();
  asm volatile("v_mul_f32 %0, %0, %1" : "+v"(a1[0][2][2]) : "v"(gt[2]) : ); SB();
  asm volatile("v_mfma_f32_16x16x32_bf16 %0, v[200:203], a[84:87], %0" : "+v"(g[0][1][0]) :  : ); SB();
  asm volatile("buffer_load_dwordx4 v[224:227], %0, %1, %2 offen offset:0 sc1" :  : "v"(vB), "s"(rWS), "s"(0u) : "memory"); SB();
  asm volatile("v_mul_f32 %0, %0, %1" : "+v"(a1[0][2][3]) : "v"(gt[3]) : ); SB();
  asm volatile("v_mfma_f32_16x16x32_bf16 %0, v[200:203], a[88:91], %0" : "+v"(g[0][2][0]) :  : ); SB();
  asm volatile("buffer_load_dwordx4 v[228:231], %0, %1, %2 offen offset:1024 sc1" :  : "v"(vB), "s"(rWS), "s"(0u) : "memory"); SB();
  asm volatile("v_mul_f32 %0, %1, %1" : "=v"(gt[0]) : "v"(a1[1][0][0]) : ); SB();
  asm volatile("v_mfma_f32_16x16x32_bf16 %0, v[200:203], a[92:95], %0" : "+v"(g[0][3][0]) :  : ); SB();
  asm volatile("v_mul_f32 %0, %1, %1" : "=v"(gt[1]) : "v"(a1[1][0][1]) : ); SB();
  asm volatile("v_mfma_f32_16x16x32_bf16 %0, v[204:207], a[80:83], %0" : "+v"(g[0][0][1]) :  : ); SB();
  asm volatile("v_mul_f32 %0, %1, %1" : "=v"(gt[2]) : "v"(a1[1][0][2]) : ); SB();
  asm volatile("v_mfma_f32_16x16x32_bf16 %0, v[204:207], a[84:87], %0" : "+v"(g[0][1][1]) :  : ); SB();
  asm volatile("buffer_load_dwordx4 v[232:235], %0, %1, %2 offen offset:0 sc1" :  : "v"(vB), "s"(rWS), "s"(65536u) : "memory"); SB();
  asm volatile("v_mul_f32 %0, %1, %1" : "=v"(gt[3]) : "v"(a1[1][0][3]) : ); SB();
  asm volatile("v_mfma_f32_16x16x32_bf16 %0, v[204:207], a[88:91], %0" : "+v"(g[0][2][1]) :  : ); SB();
  asm volatile("v_fma_f32 %0, %0, %1, %2" : "+v"(gt[0]) : "v"(k2[0]), "v"(k1[0]) : ); SB();
  asm volatile("v_mfma_f32_16x16x32_bf16 %0, v[204:207], a[92:95], %0" : "+v"(g[0][3][1]) :  : ); SB();
  asm volatile("v_fma_f32 %0, %0, %1, %2" : "+v"(gt[1]) : "v"(k2[0]), "v"(k1[0]) : ); SB();
  asm volatile("s_waitcnt vmcnt(13)" :  :  : "memory"); SB();
  asm volatile("s_waitcnt lgkmcnt(0)\n\tv_mfma_f32_16x16x32_bf16 %0, v[208:211], a[96:99], %0" : "+v"(g[0][0][0]) :  : ); SB();
  asm volatile("buffer_load_dwordx4 v[236:239], %0, %1, %2 offen offset:1024 sc1" :  : "v"(vB), "s"(rWS), "s"(65536u) : "memory"); SB();
  asm volatile("v_fma_f32 %0, %0, %1, %2" : "+v"(gt[2]) : "v"(k2[0]), "v"(k1[0]) : ); SB();
  asm volatile("v_mfma_f32_16x16x32_bf16 %0, v[208:211], a[100:103], %0" : "+v"(g[0][1][0]) :  : ); SB();
  asm volatile("v_xor_b32 %0, 524288, %0" : "+v"(vB) :  : "memory"); SB();
  asm volatile("v_fma_f32 %0, %0, %1, %2" : "+v"(gt[3]) : "v"(k2[0]), "v"(k1[0]) : ); SB();
  asm volatile("v_mfma_f32_16x16x32_bf16 %0, v[208:211], a[104:107], %0" : "+v"(g[0][2][0]) :  : ); SB();
  asm volatile("v_mul_f32 %0, %0, %1" : "+v"(gt[0]) : "v"(a1[1][0][0]) : ); SB();
  asm volatile("v_mfma_f32_16x16x32_bf16 %0, v[208:211], a[108:111], %0" : "+v"(g[0][3][0]) :  : ); SB();
  asm volatile("v_mul_f32 %0, %0, %1" : "+v"(gt[1]) : "v"(a1[1][0][1]) : ); SB();
  asm volatile("v_mfma_f32_16x16x32_bf16 %0, v[212:215], a[96:99], %0" : "+v"(g[0][0][1]) :  : ); SB();
  asm volatile("buffer_load_dwordx4 v[240:243], %0, %1, %2 offen offset:0 sc1" :  : "v"(vB), "s"(rWS), "s"(2048u) : "memory"); SB();
  asm volatile("v_mul_f32 %0, %0, %1" : "+v"(gt[2]) : "v"(a1[1][0][2]) : ); SB();
  asm volatile("v_mfma_f32_16x16x32_bf16 %0, v[212:215], a[100:103], %0" : "+v"(g[0][1][1]) :  : ); SB();
  asm volatile("v_mul_f32 %0, %0, %1" : "+v"(gt[3]) : "v"(a1[1][0][3]) : ); SB();
  asm volatile("v_mfma_f32_16x16x32_bf16 %0, v[212:215], a[104:107], %0" : "+v"(g[0][2][1]) :  : ); SB();
  asm volatile("s_waitcnt vmcnt(13)" :  :  : "memory"); SB();
  asm volatile("ds_read_b128 a[128:131], %0 offset:0" :  : "v"(aA[0]) : "memory"); SB();
  asm volatile("v_mfma_f32_16x16x32_bf16 %0, v[212:215], a[108:111], %0" : "+v"(g[0][3][1]) :  : ); SB();
  asm volatile("ds_read_b128 a[144:147], %0 offset:0" :  : "v"(aA[1]) : "memory"); SB();
  asm volatile("buffer_load_dwordx4 v[244:247], %0, %1, %2 offen offset:1024 sc1" :  : "v"(vB), "s"(rWS), "s"(2048u) : "memory"); SB();
  asm volatile("buffer_load_dwordx4 v[248:251], %0, %1, %2 offen offset:0 sc1" :  : "v"(vB), "s"(rWS), "s"(67584u) : "memory"); SB();
  asm volatile("s_waitcnt vmcnt(13)" :  :  : "memory"); SB();
  asm volatile("ds_read_b128 a[132:135], %0 offset:2048" :  : "v"(aA[0]) : "memory"); SB();
  asm volatile("ds_read_b128 a[148:151], %0 offset:2048" :  : "v"(aA[1]) : "memory"); SB();
  asm volatile("buffer_load_dwordx4 v[252:255], %0, %1, %2 offen offset:1024 sc1" :  : "v"(vB), "s"(rWS), "s"(67584u) : "memory"); SB();
  asm volatile("v_xor_b32 %0, 917504, %0" : "+v"(vB) :  : "memory"); SB();
  asm volatile("buffer_load_dwordx4 v[160:163], %0, %1, %2 offen offset:0 sc1" :  : "v"(vB), "s"(rWS), "s"(2048u) : "memory"); SB();
  asm volatile("s_waitcnt vmcnt(13)" :  :  : "memory"); SB();
  asm volatile("ds_read_b128 a[136:139], %0 offset:4096" :  : "v"(aA[0]) : "memory"); SB();
  asm volatile("ds_read_b128 a[152:155], %0 offset:4096" :  : "v"(aA[1]) : "memory"); SB();
  asm volatile("buffer_load_dwordx4 v[164:167], %0, %1, %2 offen offset:1024 sc1" :  : "v"(vB), "s"(rWS), "s"(2048u) : "memory"); SB();
  asm volatile("buffer_load_dwordx4 v[168:171], %0, %1, %2 offen offset:0 sc1" :  : "v"(vB), "s"(rWS), "s"(67584u) : "memory"); SB();
  asm volatile("s_waitcnt vmcnt(13)" :  :  : "memory"); SB();
  asm volatile("ds_read_b128 a[140:143], %0 offset:6144" :  : "v"(aA[0]) : "memory"); SB();
  asm volatile("ds_read_b128 a[156:159], %0 offset:6144" :  : "v"(aA[1]) : "memory"); SB();
  asm volatile("buffer_load_dwordx4 v[172:175], %0, %1, %2 offen offset:1024 sc1" :  : "v"(vB), "s"(rWS), "s"(67584u) : "memory"); SB();
  asm volatile("s_waitcnt vmcnt(12)" :  :  : "memory"); SB();
  asm volatile("v_mfma_f32_16x16x32_bf16 %0, v[216:219], a[112:115], %0" : "+v"(g[0][0][0]) :  : ); SB();
  asm volatile("v_xor_b32 %0, 131072, %0" : "+v"(vB) :  : "memory"); SB();
  asm volatile("v_exp_f32 %0, %1" : "=v"(ge[0]) : "v"(gt[0]) : ); SB();
  asm volatile("v_mfma_f32_16x16x32_bf16 %0, v[216:219], a[116:119], %0" : "+v"(g[0][1][0]) :  : ); SB();
  asm volatile("buffer_load_dwordx4 v[176:179], %0, %1, %2 offen offset:0 sc1" :  : "v"(vB), "s"(rWS), "s"(2048u) : "memory"); SB();
  asm volatile("v_exp_f32 %0, %1" : "=v"(ge[1]) : "v"(gt[1]) : ); SB();
  asm volatile("v_mfma_f32_16x16x32_bf16 %0, v[216:219], a[120:123], %0" : "+v"(g[0][2][0]) :  : ); SB();
  asm volatile("buffer_load_dwordx4 v[180:183], %0, %1, %2 offen offset:1024 sc1" :  : "v"(vB), "s"(rWS), "s"(2048u) : "memory"); SB();
  asm volatile("v_exp_f32 %0, %1" : "=v"(ge[2]) : "v"(gt[2]) : ); SB();
  asm volatile("v_mfma_f32_16x16x32_bf16 %0, v[216:219], a[124:127], %0" : "+v"(g[0][3][0]) :  : ); SB();
  asm volatile("v_exp_f32 %0, %1" : "=v"(ge[3]) : "v"(gt[3]) : ); SB();
  asm volatile("v_mfma_f32_16x16x32_bf16 %0, v[220:223], a[112:115], %0" : "+v"(g[0][0][1]) :  : ); SB();
  asm volatile("v_fma_f32 %0, %0, %1, %1" : "+v"(ge[0]) : "v"(xd[0]) : ); SB();
  asm volatile("v_mfma_f32_16x16x32_bf16 %0, v[220:223], a[116:119], %0" : "+v"(g[0][1][1]) :  : ); SB();
  asm volatile("buffer_load_dwordx4 v[184:187], %0, %1, %2 offen offset:0 sc1" :  : "v"(vB), "s"(rWS), "s"(67584u) : "memory"); SB();
  asm volatile("v_fma_f32 %0, %0, %1, %1" : "+v"(ge[1]) : "v"(xd[0]) : ); SB();
  asm volatile("v_mfma_f32_16x16x32_bf16 %0, v[220:223], a[120:123], %0" : "+v"(g[0][2][1]) :  : ); SB();
  asm volatile("v_fma_f32 %0, %0, %1, %1" : "+v"(ge[2]) : "v"(xd[0]) : ); SB();
  asm volatile("v_mfma_f32_16x16x32_bf16 %0, v[220:223], a[124:127], %0" : "+v"(g[0][3][1]) :  : ); SB();
  asm volatile("v_fma_f32 %0, %0, %1, %1" : "+v"(ge[3]) : "v"(xd[0]) : ); SB();
  asm volatile("s_waitcnt vmcnt(13)" :  :  : "memory"); SB();
  asm volatile("s_waitcnt lgkmcnt(0)\n\tv_mfma_f32_16x16x32_bf16 %0, v[224:227], a[128:131], %0" : "+v"(g[0][0][0]) :  : ); SB();
  asm volatile("buffer_load_dwordx4 v[188:191], %0, %1, %2 offen offset:1024 sc1" :  : "v"(vB), "s"(rWS), "s"(67584u) : "memory"); SB();
  asm volatile("v_rcp_f32 %0, %1" : "=v"(gt[0]) : "v"(ge[0]) : ); SB();
  asm volatile("v_mfma_f32_16x16x32_bf16 %0, v[224:227], a[132:135], %0" : "+v"(g[0][1][0]) :  : ); SB();
  asm volatile("v_xor_b32 %0, 393216, %0" : "+v"(vB) :  : "memory"); SB();
  asm volatile("v_rcp_f32 %0, %1" : "=v"(gt[1]) : "v"(ge[1]) : ); SB();
  asm volatile("v_mfma_f32_16x16x32_bf16 %0, v[224:227], a[136:139], %0" : "+v"(g[0][2][0]) :  : ); SB();
  asm volatile("v_rcp_f32 %0, %1" : "=v"(gt[2]) : "v"(ge[2]) : ); SB();
  asm volatile("v_mfma_f32_16x16x32_bf16 %0, v[224:227], a[140:143], %0" : "+v"(g[0][3][0]) :  : ); SB();
  asm volatile("buffer_load_dwordx4 v[192:195], %0, %1, %2 offen offset:0 sc1" :  : "v"(vB), "s"(rWS), "s"(2048u) : "memory"); SB();
  asm volatile("v_rcp_f32 %0, %1" : "=v"(gt[3]) : "v"(ge[3]) : ); SB();
  asm volatile("v_mfma_f32_16x16x32_bf16 %0, v[228:231], a[128:131], %0" : "+v"(g[0][0][1]) :  : ); SB();
  asm volatile("v_mul_f32 %0, %0, %1" : "+v"(a1[1][0][0]) : "v"(gt[0]) : ); SB();
  asm volatile("v_mfma_f32_16x16x32_bf16 %0, v[228:231], a[132:135], %0" : "+v"(g[0][1][1]) :  : ); SB();
  asm volatile("v_mul_f32 %0, %0, %1" : "+v"(a1[1][0][1]) : "v"(gt[1]) : ); SB();
  asm volatile("v_mfma_f32_16x16x32_bf16 %0, v[228:231], a[136:139], %0" : "+v"(g[0][2][1]) :  : ); SB();
  asm volatile("buffer_load_dwordx4 v[196:199], %0, %1, %2 offen offset:1024 sc1" :  : "v"(vB), "s"(rWS), "s"(2048u) : "memory"); SB();
  asm volatile("v_mul_f32 %0, %0, %1" : "+v"(a1[1][0][2]) : "v"(gt[2]) : ); SB();
  asm volatile("v_mfma_f32_16x16x32_bf16 %0, v[228:231], a[140:143], %0" : "+v"(g[0][3][1]) :  : ); SB();
  asm volatile("v_mul_f32 %0, %0, %1" : "+v"(a1[1][0][3]) : "v"(gt[3]) : ); SB();
  asm volatile("s_waitcnt vmcnt(14)" :  :  : "memory"); SB();
  asm volatile("v_mfma_f32_16x16x32_bf16 %0, v[232:235], a[144:147], %0" : "+v"(g[0][0][0]) :  : ); SB();
  asm volatile("v_mul_f32 %0, %1, %1" : "=v"(gt[0]) : "v"(a1[1][1][0]) : ); SB();
  asm volatile("v_mfma_f32_16x16x32_bf16 %0, v[232:235], a[148:151], %0" : "+v"(g[0][1][0]) :  : ); SB();
  asm volatile("v_mul_f32 %0, %1, %1" : "=v"(gt[1]) : "v"(a1[1][1][1]) : ); SB();
  asm volatile("v_mfma_f32_16x16x32_bf16 %0, v[232:235], a[152:155], %0" : "+v"(g[0][2][0]) :  : ); SB();
  asm volatile("buffer_load_dwordx4 v[200:203], %0, %1, %2 offen offset:0 sc1" :  : "v"(vB), "s"(rWS), "s"(67584u) : "memory"); SB();
  asm volatile("v_mul_f32 %0, %1, %1" : "=v"(gt[2]) : "v"(a1[1][1][2]) : ); SB();
  asm volatile("v_mfma_f32_16x16x32_bf16 %0, v[232:235], a[156:159], %0" : "+v"(g[0][3][0]) :  : ); SB();
  asm volatile("v_mul_f32 %0, %1, %1" : "=v"(gt[3]) : "v"(a1[1][1][3]) : ); SB();
  asm volatile("v_mfma_f32_16x16x32_bf16 %0, v[236:239], a[144:147], %0" : "+v"(g[0][0][1]) :  : ); SB();
  asm volatile("v_fma_f32 %0, %0, %1, %2" : "+v"(gt[0]) : "v"(k2[0]), "v"(k1[0]) : ); SB();
  asm volatile("v_mfma_f32_16x16x32_bf16 %0, v[236:239], a[148:151], %0" : "+v"(g[0][1][1]) :  : ); SB();
  asm volatile("v_fma_f32 %0, %0, %1, %2" : "+v"(gt[1]) : "v"(k2[0]), "v"(k1[0]) : ); SB();
  asm volatile("v_mfma_f32_16x16x32_bf16 %0, v[236:239], a[152:155], %0" : "+v"(g[0][2][1]) :  : ); SB();
  asm volatile("buffer_load_dwordx4 v[204:207], %0, %1, %2 offen offset:1024 sc1" :  : "v"(vB), "s"(rWS), "s"(67584u) : "memory"); SB();
  asm volatile("v_fma_f32 %0, %0, %1, %2" : "+v"(gt[2]) : "v"(k2[0]), "v"(k1[0]) : ); SB();
  asm volatile("v_mfma_f32_16x16x32_bf16 %0, v[236:239], a[156:159], %0" : "+v"(g[0][3][1]) :  : ); SB();
  asm volatile("v_xor_b32 %0, 131072, %0" : "+v"(vB) :  : "memory"); SB();
  asm volatile("s_waitcnt vmcnt(14)" :  :  : "memory"); SB();
  asm volatile("v_mfma_f32_16x16x32_bf16 %0, v[240:243], a[0:3], %0" : "+v"(g[1][0][0]) :  : ); SB();
  asm volatile("s_barrier" :  :  : "memory"); SB();
  asm volatile("buffer_load_dwordx4 v[208:211], %0, %1, %2 offen offset:0 sc1" :  : "v"(vB), "s"(rWS), "s"(2048u) : "memory"); SB();
  asm volatile("v_mfma_f32_16x16x32_bf16 %0, v[240:243], a[4:7], %0" : "+v"(g[1][1][0]) :  : ); SB();
  asm volatile("v_fma_f32 %0, %0, %1, %2" : "+v"(gt[3]) : "v"(k2[0]), "v"(k1[0]) : ); SB();
  asm volatile("v_mfma_f32_16x16x32_bf16 %0, v[240:243], a[8:11], %0" : "+v"(g[1][2][0]) :  : ); SB();
  asm volatile("ds_write_b128 %0, %1 offset:0" :  : "v"(pw2), "v"(g[0][1][0]) : "memory"); SB();
  asm volatile("v_mfma_f32_16x16x32_bf16 %0, v[240:243], a[12:15], %0" : "+v"(g[1][3][0]) :  : ); SB();
  asm volatile("buffer_load_dwordx4 v[212:215], %0, %1, %2 offen offset:1024 sc1" :  : "v"(vB), "s"(rWS), "s"(2048u) : "memory"); SB();
  asm volatile("v_mul_f32 %0, %0, %1" : "+v"(gt[0]) : "v"(a1[1][1][0]) : ); SB();
  asm volatile("v_mfma_f32_16x16x32_bf16 %0, v[244:247], a[0:3], %0" : "+v"(g[1][0][1]) :  : ); SB();
  asm volatile("v_mul_f32 %0, %0, %1" : "+v"(gt[1]) : "v"(a1[1][1][1]) : ); SB();
  asm volatile("v_mfma_f32_16x16x32_bf16 %0, v[244:247], a[4:7], %0" : "+v"(g[1][1][1]) :  : ); SB();
  asm volatile("ds_write_b128 %0, %1 offset:1024" :  : "v"(pw2), "v"(g[0][1][1]) : "memory"); SB();
  asm volatile("v_mfma_f32_16x16x32_bf16 %0, v[244:247], a[8:11], %0" : "+v"(g[1][2][1]) :  : ); SB();
  asm volatile("buffer_load_dwordx4 v[216:219], %0, %1, %2 offen offset:0 sc1" :  : "v"(vB), "s"(rWS), "s"(67584u) : "memory"); SB();
  asm volatile("v_mul_f32 %0, %0, %1" : "+v"(gt[2]) : "v"(a1[1][1][2]) : ); SB();
  asm volatile("v_mfma_f32_16x16x32_bf16 %0, v[244:247], a[12:15], %0" : "+v"(g[1][3][1]) :  : ); SB();
  asm volatile("v_mul_f32 %0, %0, %1" : "+v"(gt[3]) : "v"(a1[1][1][3]) : ); SB();
  asm volatile("s_waitcnt vmcnt(15)" :  :  : "memory"); SB();
  asm volatile("v_mfma_f32_16x16x32_bf16 %0, v[248:251], a[16:19], %0" : "+v"(g[1][0][0]) :  : ); SB();
  asm volatile("ds_write_b128 %0, %1 offset:2048" :  : "v"(pw2), "v"(g[0][2][0]) : "memory"); SB();
  asm volatile("v_mfma_f32_16x16x32_bf16 %0, v[248:251], a[20:23], %0" : "+v"(g[1][1][0]) :  : ); SB();
  asm volatile("buffer_store_dwordx4 %1, %0, %2, %3 offen offset:2048 sc1\n\ts_nop 1" :  : "v"(vH_), "v"(ho[2]), "s"(rWS), "s"(0u) : "memory"); SB();
  asm volatile("v_exp_f32 %0, %1" : "=v"(ge[0]) : "v"(gt[0]) : ); SB();
  asm volatile("v_mfma_f32_16x16x32_bf16 %0, v[248:251], a[24:27], %0" : "+v"(g[1][2][0]) :  : ); SB();
  asm volatile("buffer_load_dwordx4 v[220:223], %0, %1, %2 offen offset:1024 sc1" :  : "v"(vB), "s"(rWS), "s"(67584u) : "memory"); SB();
  asm volatile("v_exp_f32 %0, %1" : "=v"(ge[1]) : "v"(gt[1]) : ); SB();
  asm volatile("v_mfma_f32_16x16x32_bf16 %0, v[248:251], a[28:31], %0" : "+v"(g[1][3][0]) :  : ); SB();
  asm volatile("ds_write_b128 %0, %1 offset:3072" :  : "v"(pw2), "v"(g[0][2][1]) : "memory"); SB();
  asm volatile("v_mfma_f32_16x16x32_bf16 %0, v[252:255], a[16:19], %0" : "+v"(g[1][0][1]) :  : ); SB();
  asm volatile("v_xor_b32 %0, 917504, %0" : "+v"(vB) :  : "memory"); SB();
  asm volatile("v_exp_f32 %0, %1" : "=v"(ge[2]) : "v"(gt[2]) : ); SB();
  asm volatile("v_mfma_f32_16x16x32_bf16 %0, v[252:255], a[20:23], %0" : "+v"(g[1][1][1]) :  : ); SB();
  asm volatile("buffer_load_dwordx4 v[224:227], %0, %1, %2 offen offset:0 sc1" :  : "v"(vB), "s"(rWS), "s"(4096u) : "memory"); SB();
  asm volatile("v_exp_f32 %0, %1" : "=v"(ge[3]) : "v"(gt[3]) : ); SB();
  asm volatile("v_mfma_f32_16x16x32_bf16 %0, v[252:255], a[24:27], %0" : "+v"(g[1][2][1]) :  : ); SB();
  asm volatile("ds_write_b128 %0, %1 offset:4096" :  : "v"(pw2), "v"(g[0][3][0]) : "memory"); SB();
  asm volatile("v_mfma_f32_16x16x32_bf16 %0, v[252:255], a[28:31], %0" : "+v"(g[1][3][1]) :  : ); SB();
  asm volatile("v_fma_f32 %0, %0, %1, %1" : "+v"(ge[0]) : "v"(xd[0]) : ); SB();
  ST(28);
  asm volatile("s_waitcnt vmcnt(16)" :  :  : "memory"); SB();
  asm volatile("v_mfma_f32_16x16x32_bf16 %0, v[160:163], a[32:35], %0" : "+v"(g[1][0][0]) :  : ); SB();
  asm volatile("buffer_load_dwordx4 v[228:231], %0, %1, %2 offen offset:1024 sc1" :  : "v"(vB), "s"(rWS), "s"(4096u) : "memory"); SB();
  asm volatile("v_fma_f32 %0, %0, %1, %1" : "+v"(ge[1]) : "v"(xd[0]) : ); SB();
  asm volatile("v_mfma_f32_16x16x32_bf16 %0, v[160:163], a[36:39], %0" : "+v"(g[1][1][0]) :  : ); SB();
  asm volatile("ds_write_b128 %0, %1 offset:5120" :  : "v"(pw2), "v"(g[0][3][1]) : "memory"); SB();
  asm volatile("v_mfma_f32_16x16x32_bf16 %0, v[160:163], a[40:43], %0" : "+v"(g[1][2][0]) :  : ); SB();
  asm volatile("buffer_load_dwordx4 v[232:235], %0, %1, %2 offen offset:0 sc1" :  : "v"(vB), "s"(rWS), "s"(69632u) : "memory"); SB();
  asm volatile("v_fma_f32 %0, %0, %1, %1" : "+v"(ge[2]) : "v"(xd[0]) : ); SB();
  asm volatile("v_mfma_f32_16x16x32_bf16 %0, v[160:163], a[44:47], %0" : "+v"(g[1][3][0]) :  : ); SB();
  asm volatile("v_fma_f32 %0, %0, %1, %1" : "+v"(ge[3]) : "v"(xd[0]) : ); SB();
  asm volatile("v_mfma_f32_16x16x32_bf16 %0, v[164:167], a[32:35], %0" : "+v"(g[1][0][1]) :  : ); SB();
  asm volatile("s_waitcnt lgkmcnt(0)\n\ts_barrier" :  :  : "memory"); SB();
  asm volatile("ds_read_b128 %0, %1 offset:0" : "=&v"(pr[0]) : "v"(pe[0]) : "memory"); SB();
  asm volatile("v_mfma_f32_16x16x32_bf16 %0, v[164:167], a[36:39], %0" : "+v"(g[1][1][1]) :  : ); SB();
  asm volatile("ds_read_b128 %0, %1 offset:1024" : "=&v"(pr[1]) : "v"(pe[0]) : "memory"); SB();
  asm volatile("v_rcp_f32 %0, %1" : "=v"(gt[0]) : "v"(ge[0]) : ); SB();
  asm volatile("v_mfma_f32_16x16x32_bf16 %0, v[164:167], a[40:43], %0" : "+v"(g[1][2][1]) :  : ); SB();
  asm volatile("ds_read_b128 %0, %1 offset:2048" : "=&v"(pr[2]) : "v"(pe[1]) : "memory"); SB();
  asm volatile("v_rcp_f32 %0, %1" : "=v"(gt[1]) : "v"(ge[1]) : ); SB();
  asm volatile("v_mfma_f32_16x16x32_bf16 %0, v[164:167], a[44:47], %0" : "+v"(g[1][3][1]) :  : ); SB();
  asm volatile("ds_read_b128 %0, %1 offset:3072" : "=&v"(pr[3]) : "v"(pe[1]) : "memory"); SB();
  asm volatile("s_waitcnt vmcnt(16)" :  :  : "memory"); SB();
  asm volatile("v_mfma_f32_16x16x32_bf16 %0, v[168:171], a[48:51], %0" : "+v"(g[1][0][0]) :  : ); SB();
  asm volatile("ds_read_b128 %0, %1 offset:4096" : "=&v"(pr[4]) : "v"(pe[2]) : "memory"); SB();
  asm volatile("v_rcp_f32 %0, %1" : "=v"(gt[2]) : "v"(ge[2]) : ); SB();
  asm volatile("v_mfma_f32_16x16x32_bf16 %0, v[168:171], a[52:55], %0" : "+v"(g[1][1][0]) :  : ); SB();
  asm volatile("buffer_load_dwordx4 v[236:239], %0, %1, %2 offen offset:1024 sc1" :  : "v"(vB), "s"(rWS), "s"(69632u) : "memory"); SB();
  asm volatile("v_rcp_f32 %0, %1" : "=v"(gt[3]) : "v"(ge[3]) : ); SB();
  asm volatile("v_mfma_f32_16x16x32_bf16 %0, v[168:171], a[56:59], %0" : "+v"(g[1][2][0]) :  : ); SB();
  asm volatile("ds_read_b128 %0, %1 offset:5120" : "=&v"(pr[5]) : "v"(pe[2]) : "memory"); SB();
  asm volatile("v_mul_f32 %0, %0, %1" : "+v"(a1[1][1][0]) : "v"(gt[0]) : ); SB();
  asm volatile("v_mfma_f32_16x16x32_bf16 %0, v[168:171], a[60:63], %0" : "+v"(g[1][3][0]) :  : ); SB();
  asm volatile("v_xor_b32 %0, 131072, %0" : "+v"(vB) :  : "memory"); SB();
  asm volatile("v_mul_f32 %0, %0, %1" : "+v"(a1[1][1][1]) : "v"(gt[1]) : ); SB();
  asm volatile("v_mfma_f32_16x16x32_bf16 %0, v[172:175], a[48:51], %0" : "+v"(g[1][0][1]) :  : ); SB();
  asm volatile("buffer_load_dwordx4 v[240:243], %0, %1, %2 offen offset:0 sc1" :  : "v"(vB), "s"(rWS), "s"(4096u) : "memory"); SB();
  asm volatile("v_mul_f32 %0, %0, %1" : "+v"(a1[1][1][2]) : "v"(gt[2]) : ); SB();
  asm volatile("v_mfma_f32_16x16x32_bf16 %0, v[172:175], a[52:55], %0" : "+v"(g[1][1][1]) :  : ); SB();
  asm volatile("buffer_load_dwordx4 v[244:247], %0, %1, %2 offen offset:1024 sc1" :  : "v"(vB), "s"(rWS), "s"(4096u) : "memory"); SB();
  asm volatile("v_mul_f32 %0, %0, %1" : "+v"(a1[1][1][3]) : "v"(gt[3]) : ); SB();
  asm volatile("v_mfma_f32_16x16x32_bf16 %0, v[172:175], a[56:59], %0" : "+v"(g[1][2][1]) :  : ); SB();
  asm volatile("v_mul_f32 %0, %1, %1" : "=v"(gt[0]) : "v"(a1[1][2][0]) : ); SB();
  asm volatile("v_mfma_f32_16x16x32_bf16 %0, v[172:175], a[60:63], %0" : "+v"(g[1][3][1]) :  : ); SB();
  asm volatile("v_mul_f32 %0, %1, %1" : "=v"(gt[1]) : "v"(a1[1][2][1]) : ); SB();
  asm volatile("s_waitcnt vmcnt(17)" :  :  : "memory"); SB();
  asm volatile("v_mfma_f32_16x16x32_bf16 %0, v[176:179], a[64:67], %0" : "+v"(g[1][0][0]) :  : ); SB();
  asm volatile("s_waitcnt lgkmcnt(0)" : "+v"(pr[0]), "+v"(pr[1]), "+v"(pr[2]), "+v"(pr[3]), "+v"(pr[4]), "+v"(pr[5]) :  : "memory"); SB();
  asm volatile("buffer_load_dwordx4 v[248:251], %0, %1, %2 offen offset:0 sc1" :  : "v"(vB), "s"(rWS), "s"(69632u) : "memory"); SB();
  asm volatile("v_mfma_f32_16x16x32_bf16 %0, v[176:179], a[68:71], %0" : "+v"(g[1][1][0]) :  : ); SB();
  asm volatile("v_add_f32 %0, %0, %1" : "+v"(pr[0][0]) : "v"(g[0][0][0][0]) : ); SB();
  asm volatile("v_mfma_f32_16x16x32_bf16 %0, v[176:179], a[72:75], %0" : "+v"(g[1][2][0]) :  : ); SB();
  asm volatile("v_add_f32 %0, %0, %1" : "+v"(pr[0][1]) : "v"(g[0][0][0][1]) : ); SB();
  asm volatile("v_mfma_f32_16x16x32_bf16 %0, v[176:179], a[76:79], %0" : "+v"(g[1][3][0]) :  : ); SB();
  asm volatile("buffer_load_dwordx4 v[252:255], %0, %1, %2 offen offset:1024 sc1" :  : "v"(vB), "s"(rWS), "s"(69632u) : "memory"); SB();
  asm volatile("v_add_f32 %0, %0, %1" : "+v"(pr[0][2]) : "v"(g[0][0][0][2]) : ); SB();
  asm volatile("v_mfma_f32_16x16x32_bf16 %0, v[180:183], a[64:67], %0" : "+v"(g[1][0][1]) :  : ); SB();
  asm volatile("v_xor_b32 %0, 393216, %0" : "+v"(vB) :  : "memory"); SB();
  asm volatile("v_add_f32 %0, %0, %1" : "+v"(pr[0][3]) : "v"(g[0][0][0][3]) : ); SB();
  asm volatile("v_mfma_f32_16x16x32_bf16 %0, v[180:183], a[68:71], %0" : "+v"(g[1][1][1]) :  : ); SB();
  asm volatile("v_add_f32 %0, %0, %1" : "+v"(pr[1][0]) : "v"(g[0][0][1][0]) : ); SB();
  asm volatile("v_mfma_f32_16x16x32_bf16 %0, v[180:183], a[72:75], %0" : "+v"(g[1][2][1]) :  : ); SB();
  asm volatile("buffer_load_dwordx4 v[160:163], %0, %1, %2 offen offset:0 sc1" :  : "v"(vB), "s"(rWS), "s"(4096u) : "memory"); SB();
  asm volatile("v_add_f32 %0, %0, %1" : "+v"(pr[1][1]) : "v"(g[0][0][1][1]) : ); SB();
  asm volatile("v_mfma_f32_16x16x32_bf16 %0, v[180:183], a[76:79], %0" : "+v"(g[1][3][1]) :  : ); SB();
  asm volatile("v_add_f32 %0, %0, %1" : "+v"(pr[1][2]) : "v"(g[0][0][1][2]) : ); SB();
  asm volatile("s_waitcnt vmcnt(18)" :  :  : "memory"); SB();
  asm volatile("v_mfma_f32_16x16x32_bf16 %0, v[184:187], a[80:83], %0" : "+v"(g[1][0][0]) :  : ); SB();
  asm volatile("v_add_f32 %0, %0, %1" : "+v"(pr[1][3]) : "v"(g[0][0][1][3]) : ); SB();
  asm volatile("v_mfma_f32_16x16x32_bf16 %0, v[184:187], a[84:87], %0" : "+v"(g[1][1][0]) :  : ); SB();
  asm volatile("v_add_f32 %0, %0, %1" : "+v"(pr[2][0]) : "v"(pr[4][0]) : ); SB();
  asm volatile("v_mfma_f32_16x16x32_bf16 %0, v[184:187], a[88:91], %0" : "+v"(g[1][2][0]) :  : ); SB();
  asm volatile("buffer_load_dwordx4 v[164:167], %0, %1, %2 offen offset:1024 sc1" :  : "v"(vB), "s"(rWS), "s"(4096u) : "memory"); SB();
  asm volatile("v_add_f32 %0, %0, %1" : "+v"(pr[2][1]) : "v"(pr[4][1]) : ); SB();
  asm volatile("v_mfma_f32_16x16x32_bf16 %0, v[184:187], a[92:95], %0" : "+v"(g[1][3][0]) :  : ); SB();
  asm volatile("v_add_f32 %0, %0, %1" : "+v"(pr[2][2]) : "v"(pr[4][2]) : ); SB();
  asm volatile("v_mfma_f32_16x16x32_bf16 %0, v[188:191], a[80:83], %0" : "+v"(g[1][0][1]) :  : ); SB();
  asm volatile("v_add_f32 %0, %0, %1" : "+v"(pr[2][3]) : "v"(pr[4][3]) : ); SB();
  asm volatile("v_mfma_f32_16x16x32_bf16 %0, v[188:191], a[84:87], %0" : "+v"(g[1][1][1]) :  : ); SB();
  asm volatile("buffer_load_dwordx4 v[168:171], %0, %1, %2 offen offset:0 sc1" :  : "v"(vB), "s"(rWS), "s"(69632u) : "memory"); SB();
  asm volatile("v_add_f32 %0, %0, %1" : "+v"(pr[3][0]) : "v"(pr[5][0]) : ); SB();
  asm volatile("v_mfma_f32_16x16x32_bf16 %0, v[188:191], a[88:91], %0" : "+v"(g[1][2][1]) :  : ); SB();
  asm volatile("v_add_f32 %0, %0, %1" : "+v"(pr[3][1]) : "v"(pr[5][1]) : ); SB();
  asm volatile("v_mfma_f32_16x16x32_bf16 %0, v[188:191], a[92:95], %0" : "+v"(g[1][3][1]) :  : ); SB();
  asm volatile("v_add_f32 %0, %0, %1" : "+v"(pr[3][2]) : "v"(pr[5][2]) : ); SB();
  asm volatile("s_waitcnt vmcnt(18)" :  :  : "memory"); SB();
  asm volatile("v_mfma_f32_16x16x32_bf16 %0, v[192:195], a[96:99], %0" : "+v"(g[1][0][0]) :  : ); SB();
  asm volatile("v_add_f32 %0, %0, %1" : "+v"(pr[3][3]) : "v"(pr[5][3]) : ); SB();
  asm volatile("v_mfma_f32_16x16x32_bf16 %0, v[192:195], a[100:103], %0" : "+v"(g[1][1][0]) :  : ); SB();
  asm volatile("buffer_load_dwordx4 v[172:175], %0, %1, %2 offen offset:1024 sc1" :  : "v"(vB), "s"(rWS), "s"(69632u) : "memory"); SB();
  asm volatile("v_add_f32 %0, %0, %1" : "+v"(pr[0][0]) : "v"(pr[2][0]) : ); SB();
  asm volatile("v_mfma_f32_16x16x32_bf16 %0, v[192:195], a[104:107], %0" : "+v"(g[1][2][0]) :  : ); SB();
  asm volatile("v_xor_b32 %0, 131072, %0" : "+v"(vB) :  : "memory"); SB();
  asm volatile("v_add_f32 %0, %0, %1" : "+v"(pr[0][1]) : "v"(pr[2][1]) : ); SB();
  asm volatile("v_mfma_f32_16x16x32_bf16 %0, v[192:195], a[108:111], %0" : "+v"(g[1][3][0]) :  : ); SB();
  asm volatile("v_add_f32 %0, %0, %1" : "+v"(pr[0][2]) : "v"(pr[2][2]) : ); SB();
  asm volatile("v_mfma_f32_16x16x32_bf16 %0, v[196:199], a[96:99], %0" : "+v"(g[1][0][1]) :  : ); SB();
  asm volatile("v_add_f32 %0, %0, %1" : "+v"(pr[0][3]) : "v"(pr[2][3]) : ); SB();
  asm volatile("v_mfma_f32_16x16x32_bf16 %0, v[196:199], a[100:103], %0" : "+v"(g[1][1][1]) :  : ); SB();
  asm volatile("v_add_f32 %0, %0, %1" : "+v"(pr[1][0]) : "v"(pr[3][0]) : ); SB();
  asm volatile("v_mfma_f32_16x16x32_bf16 %0, v[196:199], a[104:107], %0" : "+v"(g[1][2][1]) :  : ); SB();
  asm volatile("v_add_f32 %0, %0, %1" : "+v"(pr[1][1]) : "v"(pr[3][1]) : ); SB();
  asm volatile("v_mfma_f32_16x16x32_bf16 %0, v[196:199], a[108:111], %0" : "+v"(g[1][3][1]) :  : ); SB();
  asm volatile("v_add_f32 %0, %0, %1" : "+v"(pr[1][2]) : "v"(pr[3][2]) : ); SB();
  asm volatile("s_waitcnt vmcnt(17)" :  :  : "memory"); SB();
  asm volatile("v_mfma_f32_16x16x32_bf16 %0, v[200:203], a[112:115], %0" : "+v"(g[1][0][0]) :  : ); SB();
  asm volatile("v_add_f32 %0, %0, %1" : "+v"(pr[1][3]) : "v"(pr[3][3]) : ); SB();
  asm volatile("v_mfma_f32_16x16x32_bf16 %0, v[200:203], a[116:119], %0" : "+v"(g[1][1][0]) :  : ); SB();
  asm volatile("v_mul_f32 %0, %0, %1" : "+v"(pr[0][0]) : "v"(a1[0][0][0]) : ); SB();
  asm volatile("v_mfma_f32_16x16x32_bf16 %0, v[200:203], a[120:123], %0" : "+v"(g[1][2][0]) :  : ); SB();
  asm volatile("v_mul_f32 %0, %0, %1" : "+v"(pr[0][1]) : "v"(a1[0][0][1]) : ); SB();
  asm volatile("v_mfma_f32_16x16x32_bf16 %0, v[200:203], a[124:127], %0" : "+v"(g[1][3][0]) :  : ); SB();
  asm volatile("v_mul_f32 %0, %0, %1" : "+v"(pr[0][2]) : "v"(a1[0][0][2]) : ); SB();
  asm volatile("v_mfma_f32_16x16x32_bf16 %0, v[204:207], a[112:115], %0" : "+v"(g[1][0][1]) :  : ); SB();
  asm volatile("v_mul_f32 %0, %0, %1" : "+v"(pr[0][3]) : "v"(a1[0][0][3]) : ); SB();
  asm volatile("v_mfma_f32_16x16x32_bf16 %0, v[204:207], a[116:119], %0" : "+v"(g[1][1][1]) :  : ); SB();
  asm volatile("v_mul_f32 %0, %0, %1" : "+v"(pr[1][0]) : "v"(a1[0][1][0]) : ); SB();
  asm volatile("v_mfma_f32_16x16x32_bf16 %0, v[204:207], a[120:123], %0" : "+v"(g[1][2][1]) :  : ); SB();
  asm volatile("buffer_load_dwordx4 v[176:179], %0, %1, %2 offen offset:0 sc1" :  : "v"(vB), "s"(rWS), "s"(4096u) : "memory"); SB();
  asm volatile("v_mul_f32 %0, %0, %1" : "+v"(pr[1][1]) : "v"(a1[0][1][1]) : ); SB();
  asm volatile("v_mfma_f32_16x16x32_bf16 %0, v[204:207], a[124:127], %0" : "+v"(g[1][3][1]) :  : ); SB();
  asm volatile("v_mul_f32 %0, %0, %1" : "+v"(pr[1][2]) : "v"(a1[0][1][2]) : ); SB();
  asm volatile("s_waitcnt vmcnt(16)" :  :  : "memory"); SB();
  asm volatile("v_mfma_f32_16x16x32_bf16 %0, v[208:211], a[128:131], %0" : "+v"(g[1][0][0]) :  : ); SB();
  asm volatile("v_mul_f32 %0, %0, %1" : "+v"(pr[1][3]) : "v"(a1[0][1][3]) : ); SB();
  asm volatile("v_mfma_f32_16x16x32_bf16 %0, v[208:211], a[132:135], %0" : "+v"(g[1][1][0]) :  : ); SB();
  asm volatile("buffer_load_dwordx4 v[180:183], %0, %1, %2 offen offset:1024 sc1" :  : "v"(vB), "s"(rWS), "s"(4096u) : "memory"); SB();
  asm volatile("v_cvt_pk_bf16_f32 %0, %1, %2" : "=v"(ho[0][0]) : "v"(pr[0][0]), "v"(pr[0][1]) : ); SB();
  asm volatile("v_mfma_f32_16x16x32_bf16 %0, v[208:211], a[136:139], %0" : "+v"(g[1][2][0]) :  : ); SB();
  asm volatile("v_cvt_pk_bf16_f32 %0, %1, %2" : "=v"(ho[0][1]) : "v"(pr[0][2]), "v"(pr[0][3]) : ); SB();
  asm volatile("v_mfma_f32_16x16x32_bf16 %0, v[208:211], a[140:143], %0" : "+v"(g[1][3][0]) :  : ); SB();
  asm volatile("v_cvt_pk_bf16_f32 %0, %1, %2" : "=v"(ho[0][2]) : "v"(pr[1][0]), "v"(pr[1][1]) : ); SB();
  asm volatile("v_mfma_f32_16x16x32_bf16 %0, v[212:215], a[128:131], %0" : "+v"(g[1][0][1]) :  : ); SB();
  asm volatile("v_cvt_pk_bf16_f32 %0, %1, %2" : "=v"(ho[0][3]) : "v"(pr[1][2]), "v"(pr[1][3]) : ); SB();
  asm volatile("v_mfma_f32_16x16x32_bf16 %0, v[212:215], a[132:135], %0" : "+v"(g[1][1][1]) :  : ); SB();
  asm volatile("buffer_store_dwordx4 %1, %0, %2, %3 offen offset:0 sc1\n\ts_nop 1" :  : "v"(vH_), "v"(ho[0]), "s"(rWS), "s"(0u) : "memory"); SB();
  asm volatile("v_mul_f32 %0, %1, %1" : "=v"(gt[2]) : "v"(a1[1][2][2]) : ); SB();
  asm volatile("v_mfma_f32_16x16x32_bf16 %0, v[212:215], a[136:139], %0" : "+v"(g[1][2][1]) :  : ); SB();
  asm volatile("v_mul_f32 %0, %1, %1" : "=v"(gt[3]) : "v"(a1[1][2][3]) : ); SB();
  asm volatile("v_mfma_f32_16x16x32_bf16 %0, v[212:215], a[140:143], %0" : "+v"(g[1][3][1]) :  : ); SB();
  asm volatile("v_fma_f32 %0, %0, %1, %2" : "+v"(gt[0]) : "v"(k2[0]), "v"(k1[0]) : ); SB();
  asm volatile("s_waitcnt vmcnt(15)" :  :  : "memory"); SB();
  asm volatile("v_mfma_f32_16x16x32_bf16 %0, v[216:219], a[144:147], %0" : "+v"(g[1][0][0]) :  : ); SB();
  asm volatile("buffer_load_dwordx4 v[184:187], %0, %1, %2 offen offset:0 sc1" :  : "v"(vB), "s"(rWS), "s"(69632u) : "memory"); SB();
  asm volatile("v_fma_f32 %0, %0, %1, %2" : "+v"(gt[1]) : "v"(k2[0]), "v"(k1[0]) : ); SB();
  asm volatile("v_mfma_f32_16x16x32_bf16 %0, v[216:219], a[148:151], %0" : "+v"(g[1][1][0]) :  : ); SB();
  asm volatile("v_fma_f32 %0, %0, %1, %2" : "+v"(gt[2]) : "v"(k2[0]), "v"(k1[0]) : ); SB();
  asm volatile("v_mfma_f32_16x16x32_bf16 %0, v[216:219], a[152:155], %0" : "+v"(g[1][2][0]) :  : ); SB();
  asm volatile("v_fma_f32 %0, %0, %1, %2" : "+v"(gt[3]) : "v"(k2[0]), "v"(k1[0]) : ); SB();
  asm volatile("v_mfma_f32_16x16x32_bf16 %0, v[216:219], a[156:159], %0" : "+v"(g[1][3][0]) :  : ); SB();
  asm volatile("v_mul_f32 %0, %0, %1" : "+v"(gt[0]) : "v"(a1[1][2][0]) : ); SB();
  asm volatile("v_mfma_f32_16x16x32_bf16 %0, v[220:223], a[144:147], %0" : "+v"(g[1][0][1]) :  : ); SB();
  asm volatile("buffer_load_dwordx4 v[188:191], %0, %1, %2 offen offset:1024 sc1" :  : "v"(vB), "s"(rWS), "s"(69632u) : "memory"); SB();
  asm volatile("v_mul_f32 %0, %0, %1" : "+v"(gt[1]) : "v"(a1[1][2][1]) : ); SB();
  asm volatile("v_mfma_f32_16x16x32_bf16 %0, v[220:223], a[148:151], %0" : "+v"(g[1][1][1]) :  : ); SB();
  asm volatile("v_xor_b32 %0, 917504, %0" : "+v"(vB) :  : "memory"); SB();
  asm volatile("v_mul_f32 %0, %0, %1" : "+v"(gt[2]) : "v"(a1[1][2][2]) : ); SB();
  asm volatile("v_mfma_f32_16x16x32_bf16 %0, v[220:223], a[152:155], %0" : "+v"(g[1][2][1]) :  : ); SB();
  asm volatile("v_mul_f32 %0, %0, %1" : "+v"(gt[3]) : "v"(a1[1][2][3]) : ); SB();
  asm volatile("v_mfma_f32_16x16x32_bf16 %0, v[220:223], a[156:159], %0" : "+v"(g[1][3][1]) :  : ); SB();
  asm volatile("buffer_load_dwordx4 v[192:195], %0, %1, %2 offen offset:0 sc1" :  : "v"(vB), "s"(rWS), "s"(4096u) : "memory"); SB();
  ST(29);
  asm volatile("s_waitcnt vmcnt(16)" :  :  : "memory"); SB();
  asm volatile("v_mfma_f32_16x16x32_bf16 %0, v[224:227], a[160:163], %1" : "=&v"(g[0][0][0]) : "v"(bs4) : ); SB();
  asm volatile("s_barrier" :  :  : "memory"); SB();
  asm volatile("v_exp_f32 %0, %1" : "=v"(ge[0]) : "v"(gt[0]) : ); SB();
  asm volatile("v_mfma_f32_16x16x32_bf16 %0, v[224:227], a[164:167], 0" : "=&v"(g[0][1][0]) :  : ); SB();
  asm volatile("buffer_load_dwordx4 v[196:199], %0, %1, %2 offen offset:1024 sc1" :  : "v"(vB), "s"(rWS), "s"(4096u) : "memory"); SB();
  asm volatile("v_exp_f32 %0, %1" : "=v"(ge[1]) : "v"(gt[1]) : ); SB();
  asm volatile("v_mfma_f32_16x16x32_bf16 %0, v[224:227], a[168:171], 0" : "=&v"(g[0][2][0]) :  : ); SB();
  asm volatile("ds_write_b128 %0, %1 offset:0" :  : "v"(pw2), "v"(g[1][1][0]) : "memory"); SB();
  asm volatile("v_mfma_f32_16x16x32_bf16 %0, v[224:227], a[172:175], 0" : "=&v"(g[0][3][0]) :  : ); SB();
  asm volatile("buffer_load_dwordx4 v[200:203], %0, %1, %2 offen offset:0 sc1" :  : "v"(vB), "s"(rWS), "s"(69632u) : "memory"); SB();
  asm volatile("v_exp_f32 %0, %1" : "=v"(ge[2]) : "v"(gt[2]) : ); SB();
  asm volatile("v_mfma_f32_16x16x32_bf16 %0, v[228:231], a[160:163], %1" : "=&v"(g[0][0][1]) : "v"(bs4) : ); SB();
  asm volatile("v_exp_f32 %0, %1" : "=v"(ge[3]) : "v"(gt[3]) : ); SB();
  asm volatile("v_mfma_f32_16x16x32_bf16 %0, v[228:231], a[164:167], 0" : "=&v"(g[0][1][1]) :  : ); SB();
  asm volatile("ds_write_b128 %0, %1 offset:1024" :  : "v"(pw2), "v"(g[1][1][1]) : "memory"); SB();
  asm volatile("v_mfma_f32_16x16x32_bf16 %0, v[228:231], a[168:171], 0" : "=&v"(g[0][2][1]) :  : ); SB();
  asm volatile("buffer_load_dwordx4 v[204:207], %0, %1, %2 offen offset:1024 sc1" :  : "v"(vB), "s"(rWS), "s"(69632u) : "memory"); SB();
  asm volatile("v_fma_f32 %0, %0, %1, %1" : "+v"(ge[0]) : "v"(xd[0]) : ); SB();
  asm volatile("v_mfma_f32_16x16x32_bf16 %0, v[228:231], a[172:175], 0" : "=&v"(g[0][3][1]) :  : ); SB();
  asm volatile("v_xor_b32 %0, 131072, %0" : "+v"(vB) :  : "memory"); SB();
  asm volatile("s_waitcnt vmcnt(17)" :  :  : "memory"); SB();
  asm volatile("v_mfma_f32_16x16x32_bf16 %0, v[232:235], a[176:179], %0" : "+v"(g[0][0][0]) :  : ); SB();
  asm volatile("ds_write_b128 %0, %1 offset:2048" :  : "v"(pw2), "v"(g[1][2][0]) : "memory"); SB();
  asm volatile("v_mfma_f32_16x16x32_bf16 %0, v[232:235], a[180:183], %0" : "+v"(g[0][1][0]) :  : ); SB();
  asm volatile("buffer_load_dwordx4 v[208:211], %0, %1, %2 offen offset:0 sc1" :  : "v"(vB), "s"(rWS), "s"(4096u) : "memory"); SB();
  asm volatile("v_fma_f32 %0, %0, %1, %1" : "+v"(ge[1]) : "v"(xd[0]) : ); SB();
  asm volatile("v_mfma_f32_16x16x32_bf16 %0, v[232:235], a[184:187], %0" : "+v"(g[0][2][0]) :  : ); SB();
  asm volatile("v_fma_f32 %0, %0, %1, %1" : "+v"(ge[2]) : "v"(xd[0]) : ); SB();
  asm volatile("v_mfma_f32_16x16x32_bf16 %0, v[232:235], a[188:191], %0" : "+v"(g[0][3][0]) :  : ); SB();
  asm volatile("ds_write_b128 %0, %1 offset:3072" :  : "v"(pw2), "v"(g[1][2][1]) : "memory"); SB();
  asm volatile("v_mfma_f32_16x16x32_bf16 %0, v[236:239], a[176:179], %0" : "+v"(g[0][0][1]) :  : ); SB();
  asm volatile("buffer_load_dwordx4 v[212:215], %0, %1, %2 offen offset:1024 sc1" :  : "v"(vB), "s"(rWS), "s"(4096u) : "memory"); SB();
  asm volatile("v_fma_f32 %0, %0, %1, %1" : "+v"(ge[3]) : "v"(xd[0]) : ); SB();
  asm volatile("v_mfma_f32_16x16x32_bf16 %0, v[236:239], a[180:183], %0" : "+v"(g[0][1][1]) :  : ); SB();
  asm volatile("v_rcp_f32 %0, %1" : "=v"(gt[0]) : "v"(ge[0]) : ); SB();
  asm volatile("v_mfma_f32_16x16x32_bf16 %0, v[236:239], a[184:187], %0" : "+v"(g[0][2][1]) :  : ); SB();
  asm volatile("ds_write_b128 %0, %1 offset:4096" :  : "v"(pw2), "v"(g[1][3][0]) : "memory"); SB();
  asm volatile("v_mfma_f32_16x16x32_bf16 %0, v[236:239], a[188:191], %0" : "+v"(g[0][3][1]) :  : ); SB();
  asm volatile("buffer_load_dwordx4 v[216:219], %0, %1, %2 offen offset:0 sc1" :  : "v"(vB), "s"(rWS), "s"(69632u) : "memory"); SB();
  asm volatile("s_waitcnt vmcnt(18)" :  :  : "memory"); SB();
  asm volatile("v_mfma_f32_16x16x32_bf16 %0, v[240:243], a[192:195], %0" : "+v"(g[0][0][0]) :  : ); SB();
  asm volatile("v_rcp_f32 %0, %1" : "=v"(gt[1]) : "v"(ge[1]) : ); SB();
  asm volatile("v_mfma_f32_16x16x32_bf16 %0, v[240:243], a[196:199], %0" : "+v"(g[0][1][0]) :  : ); SB();
  asm volatile("ds_write_b128 %0, %1 offset:5120" :  : "v"(pw2), "v"(g[1][3][1]) : "memory"); SB();
  asm volatile("v_mfma_f32_16x16x32_bf16 %0, v[240:243], a[200:203], %0" : "+v"(g[0][2][0]) :  : ); SB();
  asm volatile("buffer_load_dwordx4 v[220:223], %0, %1, %2 offen offset:1024 sc1" :  : "v"(vB), "s"(rWS), "s"(69632u) : "memory"); SB();
  asm volatile("v_rcp_f32 %0, %1" : "=v"(gt[2]) : "v"(ge[2]) : ); SB();
  asm volatile("v_mfma_f32_16x16x32_bf16 %0, v[240:243], a[204:207], %0" : "+v"(g[0][3][0]) :  : ); SB();
  asm volatile("v_xor_b32 %0, 393216, %0" : "+v"(vB) :  : "memory"); SB();
  asm volatile("v_rcp_f32 %0, %1" : "=v"(gt[3]) : "v"(ge[3]) : ); SB();
  asm volatile("v_mfma_f32_16x16x32_bf16 %0, v[244:247], a[192:195], %0" : "+v"(g[0][0][1]) :  : ); SB();
  asm volatile("s_waitcnt lgkmcnt(0)\n\ts_barrier" :  :  : "memory"); SB();
  asm volatile("ds_read_b128 %0, %1 offset:0" : "=&v"(pr[0]) : "v"(pe[0]) : "memory"); SB();
  asm volatile("v_mfma_f32_16x16x32_bf16 %0, v[244:247], a[196:199], %0" : "+v"(g[0][1][1]) :  : ); SB();
  asm volatile("ds_read_b128 %0, %1 offset:1024" : "=&v"(pr[1]) : "v"(pe[0]) : "memory"); SB();
  asm volatile("v_mul_f32 %0, %0, %1" : "+v"(a1[1][2][0]) : "v"(gt[0]) : ); SB();
  asm volatile("v_mfma_f32_16x16x32_bf16 %0, v[244:247], a[200:203], %0" : "+v"(g[0][2][1]) :  : ); SB();
  asm volatile("ds_read_b128 %0, %1 offset:2048" : "=&v"(pr[2]) : "v"(pe[1]) : "memory"); SB();
  asm volatile("v_mul_f32 %0, %0, %1" : "+v"(a1[1][2][1]) : "v"(gt[1]) : ); SB();
  asm volatile("v_mfma_f32_16x16x32_bf16 %0, v[244:247], a[204:207], %0" : "+v"(g[0][3][1]) :  : ); SB();
  asm volatile("ds_read_b128 %0, %1 offset:3072" : "=&v"(pr[3]) : "v"(pe[1]) : "memory"); SB();
  asm volatile("s_waitcnt vmcnt(17)" :  :  : "memory"); SB();
  asm volatile("v_mfma_f32_16x16x32_bf16 %0, v[248:251], a[208:211], %0" : "+v"(g[0][0][0]) :  : ); SB();
  asm volatile("ds_read_b128 %0, %1 offset:4096" : "=&v"(pr[4]) : "v"(pe[2]) : "memory"); SB();
  asm volatile("v_mul_f32 %0, %0, %1" : "+v"(a1[1][2][2]) : "v"(gt[2]) : ); SB();
  asm volatile("v_mfma_f32_16x16x32_bf16 %0, v[248:251], a[212:215], %0" : "+v"(g[0][1][0]) :  : ); SB();
  asm volatile("ds_read_b128 %0, %1 offset:5120" : "=&v"(pr[5]) : "v"(pe[2]) : "memory"); SB();
  asm volatile("v_mul_f32 %0, %0, %1" : "+v"(a1[1][2][3]) : "v"(gt[3]) : ); SB();
  asm volatile("v_mfma_f32_16x16x32_bf16 %0, v[248:251], a[216:219], %0" : "+v"(g[0][2][0]) :  : ); SB();
  asm volatile("buffer_load_dwordx4 v[224:227], %0, %1, %2 offen offset:0 sc1" :  : "v"(vB), "s"(rWS), "s"(4096u) : "memory"); SB();
  asm volatile("v_mfma_f32_16x16x32_bf16 %0, v[248:251], a[220:223], %0" : "+v"(g[0][3][0]) :  : ); SB();
  asm volatile("buffer_load_dwordx4 v[228:231], %0, %1, %2 offen offset:1024 sc1" :  : "v"(vB), "s"(rWS), "s"(4096u) : "memory"); SB();
  asm volatile("v_mfma_f32_16x16x32_bf16 %0, v[252:255], a[208:211], %0" : "+v"(g[0][0][1]) :  : ); SB();
  asm volatile("v_mfma_f32_16x16x32_bf16 %0, v[252:255], a[212:215], %0" : "+v"(g[0][1][1]) :  : ); SB();
  asm volatile("v_mfma_f32_16x16x32_bf16 %0, v[252:255], a[216:219], %0" : "+v"(g[0][2][1]) :  : ); SB();
  asm volatile("buffer_load_dwordx4 v[232:235], %0, %1, %2 offen offset:0 sc1" :  : "v"(vB), "s"(rWS), "s"(69632u) : "memory"); SB();
  asm volatile("v_mfma_f32_16x16x32_bf16 %0, v[252:255], a[220:223], %0" : "+v"(g[0][3][1]) :  : ); SB();
  asm volatile("s_waitcnt lgkmcnt(0)" : "+v"(pr[0]), "+v"(pr[1]), "+v"(pr[2]), "+v"(pr[3]), "+v"(pr[4]), "+v"(pr[5]) :  : "memory"); SB();
  asm volatile("v_add_f32 %0, %0, %1" : "+v"(pr[0][0]) : "v"(g[1][0][0][0]) : ); SB();
  asm volatile("s_waitcnt vmcnt(18)" :  :  : "memory"); SB();
  asm volatile("v_mfma_f32_16x16x32_bf16 %0, v[160:163], a[224:227], %0" : "+v"(g[0][0][0]) :  : ); SB();
  asm volatile("v_add_f32 %0, %0, %1" : "+v"(pr[0][1]) : "v"(g[1][0][0][1]) : ); SB();
  asm volatile("v_mfma_f32_16x16x32_bf16 %0, v[160:163], a[228:231], %0" : "+v"(g[0][1][0]) :  : ); SB();
  asm volatile("v_add_f32 %0, %0, %1" : "+v"(pr[0][2]) : "v"(g[1][0][0][2]) : ); SB();
  asm volatile("v_mfma_f32_16x16x32_bf16 %0, v[160:163], a[232:235], %0" : "+v"(g[0][2][0]) :  : ); SB();
  asm volatile("v_add_f32 %0, %0, %1" : "+v"(pr[0][3]) : "v"(g[1][0][0][3]) : ); SB();
  asm volatile("v_mfma_f32_16x16x32_bf16 %0, v[160:163], a[236:239], %0" : "+v"(g[0][3][0]) :  : ); SB();
  asm volatile("v_add_f32 %0, %0, %1" : "+v"(pr[1][0]) : "v"(g[1][0][1][0]) : ); SB();
  asm volatile("v_mfma_f32_16x16x32_bf16 %0, v[164:167], a[224:227], %0" : "+v"(g[0][0][1]) :  : ); SB();
  asm volatile("v_add_f32 %0, %0, %1" : "+v"(pr[1][1]) : "v"(g[1][0][1][1]) : ); SB();
  asm volatile("v_mfma_f32_16x16x32_bf16 %0, v[164:167], a[228:231], %0" : "+v"(g[0][1][1]) :  : ); SB();
  asm volatile("v_add_f32 %0, %0, %1" : "+v"(pr[1][2]) : "v"(g[1][0][1][2]) : ); SB();
  asm volatile("v_mfma_f32_16x16x32_bf16 %0, v[164:167], a[232:235], %0" : "+v"(g[0][2][1]) :  : ); SB();
  asm volatile("buffer_load_dwordx4 v[236:239], %0, %1, %2 offen offset:1024 sc1" :  : "v"(vB), "s"(rWS), "s"(69632u) : "memory"); SB();
  asm volatile("v_add_f32 %0, %0, %1" : "+v"(pr[1][3]) : "v"(g[1][0][1][3]) : ); SB();
  asm volatile("v_mfma_f32_16x16x32_bf16 %0, v[164:167], a[236:239], %0" : "+v"(g[0][3][1]) :  : ); SB();
  asm volatile("v_xor_b32 %0, 131072, %0" : "+v"(vB) :  : "memory"); SB();
  asm volatile("s_waitcnt vmcnt(17)" :  :  : "memory"); SB();
  asm volatile("v_mfma_f32_16x16x32_bf16 %0, v[168:171], a[240:243], %0" : "+v"(g[0][0][0]) :  : ); SB();
  asm volatile("v_add_f32 %0, %0, %1" : "+v"(pr[2][0]) : "v"(pr[4][0]) : ); SB();
  asm volatile("v_mfma_f32_16x16x32_bf16 %0, v[168:171], a[244:247], %0" : "+v"(g[0][1][0]) :  : ); SB();
  asm volatile("buffer_load_dwordx4 v[240:243], %0, %1, %2 offen offset:0 sc1" :  : "v"(vB), "s"(rWS), "s"(4096u) : "memory"); SB();
  asm volatile("v_add_f32 %0, %0, %1" : "+v"(pr[2][1]) : "v"(pr[4][1]) : ); SB();
  asm volatile("v_mfma_f32_16x16x32_bf16 %0, v[168:171], a[248:251], %0" : "+v"(g[0][2][0]) :  : ); SB();
  asm volatile("v_add_f32 %0, %0, %1" : "+v"(pr[2][2]) : "v"(pr[4][2]) : ); SB();
  asm volatile("v_mfma_f32_16x16x32_bf16 %0, v[168:171], a[252:255], %0" : "+v"(g[0][3][0]) :  : ); SB();
  asm volatile("v_add_f32 %0, %0, %1" : "+v"(pr[2][3]) : "v"(pr[4][3]) : ); SB();
  asm volatile("v_mfma_f32_16x16x32_bf16 %0, v[172:175], a[240:243], %0" : "+v"(g[0][0][1]) :  : ); SB();
  asm volatile("v_add_f32 %0, %0, %1" : "+v"(pr[3][0]) : "v"(pr[5][0]) : ); SB();
  asm volatile("v_mfma_f32_16x16x32_bf16 %0, v[172:175], a[244:247], %0" : "+v"(g[0][1][1]) :  : ); SB();
  asm volatile("buffer_load_dwordx4 v[244:247], %0, %1, %2 offen offset:1024 sc1" :  : "v"(vB), "s"(rWS), "s"(4096u) : "memory"); SB();
  asm volatile("v_add_f32 %0, %0, %1" : "+v"(pr[3][1]) : "v"(pr[5][1]) : ); SB();
  asm volatile("v_mfma_f32_16x16x32_bf16 %0, v[172:175], a[248:251], %0" : "+v"(g[0][2][1]) :  : ); SB();
  asm volatile("v_add_f32 %0, %0, %1" : "+v"(pr[3][2]) : "v"(pr[5][2]) : ); SB();
  asm volatile("v_mfma_f32_16x16x32_bf16 %0, v[172:175], a[252:255], %0" : "+v"(g[0][3][1]) :  : ); SB();
  asm volatile("v_add_f32 %0, %0, %1" : "+v"(pr[3][3]) : "v"(pr[5][3]) : ); SB();
  asm volatile("s_waitcnt vmcnt(17)" :  :  : "memory"); SB();
  asm volatile("v_mfma_f32_16x16x32_bf16 %0, v[176:179], a[0:3], %0" : "+v"(g[0][0][0]) :  : ); SB();
  asm volatile("v_add_f32 %0, %0, %1" : "+v"(pr[0][0]) : "v"(pr[2][0]) : ); SB();
  asm volatile("v_mfma_f32_16x16x32_bf16 %0, v[176:179], a[4:7], %0" : "+v"(g[0][1][0]) :  : ); SB();
  asm volatile("s_add_u32 m0, %0, 60032" :  : "s"(mU) : "memory", "scc"); SB();
  asm volatile("v_add_f32 %0, %0, %1" : "+v"(pr[0][1]) : "v"(pr[2][1]) : ); SB();
  asm volatile("v_mfma_f32_16x16x32_bf16 %0, v[176:179], a[8:11], %0" : "+v"(g[0][2][0]) :  : ); SB();
  asm volatile("buffer_load_dwordx4 %0, %1, %2 offen sc1 lds" :  : "v"(vF), "s"(rWS), "s"(W_KV) : "memory"); SB();
  asm volatile("v_add_f32 %0, %0, %1" : "+v"(pr[0][2]) : "v"(pr[2][2]) : ); SB();
  asm volatile("v_mfma_f32_16x16x32_bf16 %0, v[176:179], a[12:15], %0" : "+v"(g[0][3][0]) :  : ); SB();
  asm volatile("v_add_f32 %0, %0, %1" : "+v"(pr[0][3]) : "v"(pr[2][3]) : ); SB();
  asm volatile("v_mfma_f32_16x16x32_bf16 %0, v[180:183], a[0:3], %0" : "+v"(g[0][0][1]) :  : ); SB();
  asm volatile("s_add_u32 m0, %0, 72960" :  : "s"(mU) : "memory", "scc"); SB();
  asm volatile("v_add_f32 %0, %0, %1" : "+v"(pr[1][0]) : "v"(pr[3][0]) : ); SB();
  asm volatile("v_mfma_f32_16x16x32_bf16 %0, v[180:183], a[4:7], %0" : "+v"(g[0][1][1]) :  : ); SB();
  asm volatile("buffer_load_dwordx4 %0, %1, %2 offen sc1 lds" :  : "v"(vF), "s"(rWS), "s"(W_KV + 4096) : "memory"); SB();
  asm volatile("v_add_f32 %0, %0, %1" : "+v"(pr[1][1]) : "v"(pr[3][1]) : ); SB();
  asm volatile("v_mfma_f32_16x16x32_bf16 %0, v[180:183], a[8:11], %0" : "+v"(g[0][2][1]) :  : ); SB();
  asm volatile("v_add_f32 %0, %0, %1" : "+v"(pr[1][2]) : "v"(pr[3][2]) : ); SB();
  asm volatile("v_mfma_f32_16x16x32_bf16 %0, v[180:183], a[12:15], %0" : "+v"(g[0][3][1]) :  : ); SB();
  asm volatile("v_add_f32 %0, %0, %1" : "+v"(pr[1][3]) : "v"(pr[3][3]) : ); SB();
  asm volatile("s_waitcnt vmcnt(16)" :  :  : "memory"); SB();
  asm volatile("v_mfma_f32_16x16x32_bf16 %0, v[184:187], a[16:19], %0" : "+v"(g[0][0][0]) :  : ); SB();
  asm volatile("buffer_load_dwordx4 v[248:251], %0, %1, %2 offen offset:0 sc1" :  : "v"(vB), "s"(rWS), "s"(69632u) : "memory"); SB();
  asm volatile("v_mul_f32 %0, %0, %1" : "+v"(pr[0][0]) : "v"(a1[0][2][0]) : ); SB();
  asm volatile("v_mfma_f32_16x16x32_bf16 %0, v[184:187], a[20:23], %0" : "+v"(g[0][1][0]) :  : ); SB();
  asm volatile("v_mul_f32 %0, %0, %1" : "+v"(pr[0][1]) : "v"(a1[0][2][1]) : ); SB();
  asm volatile("v_mfma_f32_16x16x32_bf16 %0, v[184:187], a[24:27], %0" : "+v"(g[0][2][0]) :  : ); SB();
  asm volatile("v_mul_f32 %0, %0, %1" : "+v"(pr[0][2]) : "v"(a1[0][2][2]) : ); SB();
  asm volatile("v_mfma_f32_16x16x32_bf16 %0, v[184:187], a[28:31], %0" : "+v"(g[0][3][0]) :  : ); SB();
  asm volatile("buffer_load_dwordx4 v[252:255], %0, %1, %2 offen offset:1024 sc1" :  : "v"(vB), "s"(rWS), "s"(69632u) : "memory"); SB();
  asm volatile("v_mul_f32 %0, %0, %1" : "+v"(pr[0][3]) : "v"(a1[0][2][3]) : ); SB();
  asm volatile("v_mfma_f32_16x16x32_bf16 %0, v[188:191], a[16:19], %0" : "+v"(g[0][0][1]) :  : ); SB();
  asm volatile("v_mul_f32 %0, %0, %1" : "+v"(pr[1][0]) : "v"(a1[1][0][0]) : ); SB();
  asm volatile("v_mfma_f32_16x16x32_bf16 %0, v[188:191], a[20:23], %0" : "+v"(g[0][1][1]) :  : ); SB();
  asm volatile("v_mul_f32 %0, %0, %1" : "+v"(pr[1][1]) : "v"(a1[1][0][1]) : ); SB();
  asm volatile("v_mfma_f32_16x16x32_bf16 %0, v[188:191], a[24:27], %0" : "+v"(g[0][2][1]) :  : ); SB();
  asm volatile("v_mul_f32 %0, %0, %1" : "+v"(pr[1][2]) : "v"(a1[1][0][2]) : ); SB();
  asm volatile("v_mfma_f32_16x16x32_bf16 %0, v[188:191], a[28:31], %0" : "+v"(g[0][3][1]) :  : ); SB();
  asm volatile("buffer_load_dwordx4 a[160:163], %0, %1, %2 offen offset:0 sc1" :  : "v"(vO), "s"(rWS), "s"(0u) : "memory"); SB();
  asm volatile("s_waitcnt vmcnt(17)" :  :  : "memory"); SB();
  asm volatile("v_mfma_f32_16x16x32_bf16 %0, v[192:195], a[32:35], %0" : "+v"(g[0][0][0]) :  : ); SB();
  asm volatile("v_mul_f32 %0, %0, %1" : "+v"(pr[1][3]) : "v"(a1[1][0][3]) : ); SB();
  asm volatile("v_mfma_f32_16x16x32_bf16 %0, v[192:195], a[36:39], %0" : "+v"(g[0][1][0]) :  : ); SB();
  asm volatile("v_cvt_pk_bf16_f32 %0, %1, %2" : "=v"(ho[1][0]) : "v"(pr[0][0]), "v"(pr[0][1]) : ); SB();
  asm volatile("v_mfma_f32_16x16x32_bf16 %0, v[192:195], a[40:43], %0" : "+v"(g[0][2][0]) :  : ); SB();
  asm volatile("v_cvt_pk_bf16_f32 %0, %1, %2" : "=v"(ho[1][1]) : "v"(pr[0][2]), "v"(pr[0][3]) : ); SB();
  asm volatile("v_mfma_f32_16x16x32_bf16 %0, v[192:195], a[44:47], %0" : "+v"(g[0][3][0]) :  : ); SB();
  asm volatile("buffer_load_dwordx4 a[164:167], %0, %1, %2 offen offset:1024 sc1" :  : "v"(vO), "s"(rWS), "s"(0u) : "memory"); SB();
  asm volatile("v_cvt_pk_bf16_f32 %0, %1, %2" : "=v"(ho[1][2]) : "v"(pr[1][0]), "v"(pr[1][1]) : ); SB();
  asm volatile("v_mfma_f32_16x16x32_bf16 %0, v[196:199], a[32:35], %0" : "+v"(g[0][0][1]) :  : ); SB();
  asm volatile("v_cvt_pk_bf16_f32 %0, %1, %2" : "=v"(ho[1][3]) : "v"(pr[1][2]), "v"(pr[1][3]) : ); SB();
  asm volatile("v_mfma_f32_16x16x32_bf16 %0, v[196:199], a[36:39], %0" : "+v"(g[0][1][1]) :  : ); SB();
  asm volatile("v_mfma_f32_16x16x32_bf16 %0, v[196:199], a[40:43], %0" : "+v"(g[0][2][1]) :  : ); SB();
  asm volatile("buffer_store_dwordx4 %1, %0, %2, %3 offen offset:1024 sc1\n\ts_nop 1" :  : "v"(vH_), "v"(ho[1]), "s"(rWS), "s"(0u) : "memory"); SB();
  asm volatile("v_mfma_f32_16x16x32_bf16 %0, v[196:199], a[44:47], %0" : "+v"(g[0][3][1]) :  : ); SB();
  asm volatile("s_waitcnt vmcnt(17)" :  :  : "memory"); SB();
  asm volatile("v_mfma_f32_16x16x32_bf16 %0, v[200:203], a[48:51], %0" : "+v"(g[0][0][0]) :  : ); SB();
  asm volatile("v_mfma_f32_16x16x32_bf16 %0, v[200:203], a[52:55], %0" : "+v"(g[0][1][0]) :  : ); SB();
  asm volatile("v_mfma_f32_16x16x32_bf16 %0, v[200:203], a[56:59], %0" : "+v"(g[0][2][0]) :  : ); SB();
  asm volatile("buffer_load_dwordx4 a[168:171], %0, %1, %2 offen offset:2048 sc1" :  : "v"(vO), "s"(rWS), "s"(0u) : "memory"); SB();
  asm volatile("v_mfma_f32_16x16x32_bf16 %0, v[200:203], a[60:63], %0" : "+v"(g[0][3][0]) :  : ); SB();
  asm volatile("v_mfma_f32_16x16x32_bf16 %0, v[204:207], a[48:51], %0" : "+v"(g[0][0][1]) :  : ); SB();
  asm volatile("v_mfma_f32_16x16x32_bf16 %0, v[204:207], a[52:55], %0" : "+v"(g[0][1][1]) :  : ); SB();
  asm volatile("buffer_load_dwordx4 a[172:175], %0, %1, %2 offen offset:3072 sc1" :  : "v"(vO), "s"(rWS), "s"(0u) : "memory"); SB();
  asm volatile("v_mfma_f32_16x16x32_bf16 %0, v[204:207], a[56:59], %0" : "+v"(g[0][2][1]) :  : ); SB();
  asm volatile("v_mfma_f32_16x16x32_bf16 %0, v[204:207], a[60:63], %0" : "+v"(g[0][3][1]) :  : ); SB();
  asm volatile("s_waitcnt vmcnt(17)" :  :  : "memory"); SB();
  asm volatile("v_mfma_f32_16x16x32_bf16 %0, v[208:211], a[64:67], %0" : "+v"(g[0][0][0]) :  : ); SB();
  asm volatile("ds_read_b128 %0, %1 offset:0" : "=&v"(yq[0]) : "v"(ydr) : "memory"); SB();
  asm volatile("v_mfma_f32_16x16x32_bf16 %0, v[208:211], a[68:71], %0" : "+v"(g[0][1][0]) :  : ); SB();
  asm volatile("ds_read_b128 %0, %1 offset:256" : "=&v"(yq[1]) : "v"(ydr) : "memory"); SB();
  asm volatile("v_mfma_f32_16x16x32_bf16 %0, v[208:211], a[72:75], %0" : "+v"(g[0][2][0]) :  : ); SB();
  asm volatile("ds_read_b128 %0, %1 offset:8192" : "=&v"(yq[2]) : "v"(ydr) : "memory"); SB();
  asm volatile("v_mfma_f32_16x16x32_bf16 %0, v[208:211], a[76:79], %0" : "+v"(g[0][3][0]) :  : ); SB();
  asm volatile("ds_read_b128 %0, %1 offset:8448" : "=&v"(yq[3]) : "v"(ydr) : "memory"); SB();
  asm volatile("v_mfma_f32_16x16x32_bf16 %0, v[212:215], a[64:67], %0" : "+v"(g[0][0][1]) :  : ); SB();
  asm volatile("ds_read_b128 %0, %1 offset:16384" : "=&v"(yq[4]) : "v"(ydr) : "memory"); SB();
  asm volatile("v_mfma_f32_16x16x32_bf16 %0, v[212:215], a[68:71], %0" : "+v"(g[0][1][1]) :  : ); SB();
  asm volatile("ds_read_b128 %0, %1 offset:16640" : "=&v"(yq[5]) : "v"(ydr) : "memory"); SB();
  asm volatile("v_mfma_f32_16x16x32_bf16 %0, v[212:215], a[72:75], %0" : "+v"(g[0][2][1]) :  : ); SB();
  asm volatile("ds_read_b128 %0, %1 offset:24576" : "=&v"(yq[6]) : "v"(ydr) : "memory"); SB();
  asm volatile("v_mfma_f32_16x16x32_bf16 %0, v[212:215], a[76:79], %0" : "+v"(g[0][3][1]) :  : ); SB();
  asm volatile("ds_read_b128 %0, %1 offset:24832" : "=&v"(yq[7]) : "v"(ydr) : "memory"); SB();
  asm volatile("s_waitcnt vmcnt(15)" :  :  : "memory"); SB();
  asm volatile("v_mfma_f32_16x16x32_bf16 %0, v[216:219], a[80:83], %0" : "+v"(g[0][0][0]) :  : ); SB();
  asm volatile("ds_read_b128 %0, %1 offset:25856" : "=&v"(ykv) : "v"(ykr) : "memory"); SB();
  asm volatile("v_mfma_f32_16x16x32_bf16 %0, v[216:219], a[84:87], %0" : "+v"(g[0][1][0]) :  : ); SB();
  asm volatile("buffer_load_dwordx4 a[176:179], %0, %1, %2 offen offset:0 sc1" :  : "v"(vO), "s"(rWS), "s"(4096u) : "memory"); SB();
  asm volatile("v_mfma_f32_16x16x32_bf16 %0, v[216:219], a[88:91], %0" : "+v"(g[0][2][0]) :  : ); SB();
  asm volatile("buffer_load_dwordx4 a[180:183], %0, %1, %2 offen offset:1024 sc1" :  : "v"(vO), "s"(rWS), "s"(4096u) : "memory"); SB();
  asm volatile("v_mfma_f32_16x16x32_bf16 %0, v[216:219], a[92:95], %0" : "+v"(g[0][3][0]) :  : ); SB();
  asm volatile("v_mfma_f32_16x16x32_bf16 %0, v[220:223], a[80:83], %0" : "+v"(g[0][0][1]) :  : ); SB();
  asm volatile("v_mfma_f32_16x16x32_bf16 %0, v[220:223], a[84:87], %0" : "+v"(g[0][1][1]) :  : ); SB();
  asm volatile("buffer_load_dwordx4 a[184:187], %0, %1, %2 offen offset:2048 sc1" :  : "v"(vO), "s"(rWS), "s"(4096u) : "memory"); SB();
  asm volatile("v_mfma_f32_16x16x32_bf16 %0, v[220:223], a[88:91], %0" : "+v"(g[0][2][1]) :  : ); SB();
  asm volatile("v_mfma_f32_16x16x32_bf16 %0, v[220:223], a[92:95], %0" : "+v"(g[0][3][1]) :  : ); SB();
  asm volatile("buffer_load_dwordx4 a[188:191], %0, %1, %2 offen offset:3072 sc1" :  : "v"(vO), "s"(rWS), "s"(4096u) : "memory"); SB();
  asm volatile("buffer_load_dwordx4 a[192:195], %0, %1, %2 offen offset:0 sc1" :  : "v"(vO), "s"(rWS), "s"(8192u) : "memory"); SB();
  asm volatile("s_waitcnt vmcnt(12)\n\ts_barrier" :  :  : "memory"); SB();
  asm volatile("s_waitcnt vmcnt(10)" :  :  : "memory"); SB();
  asm volatile("v_mfma_f32_16x16x32_bf16 %0, v[224:227], a[100:103], %0" : "+v"(g[0][1][0]) :  : ); SB();
  asm volatile("buffer_load_dwordx4 a[196:199], %0, %1, %2 offen offset:1024 sc1" :  : "v"(vO), "s"(rWS), "s"(8192u) : "memory"); SB();
  asm volatile("v_mfma_f32_16x16x32_bf16 %0, v[228:231], a[100:103], %0" : "+v"(g[0][1][1]) :  : ); SB();
  asm volatile("ds_read2_b32 a[64:65], %0 offset0:176 offset1:177" :  : "v"(aa[0]) : "memory"); SB();
  asm volatile("v_mfma_f32_16x16x32_bf16 %0, v[232:235], a[116:119], %0" : "+v"(g[0][1][0]) :  : ); SB();
  asm volatile("ds_read2_b32 a[66:67], %0 offset0:178 offset1:179" :  : "v"(aa[0]) : "memory"); SB();
  asm volatile("v_mfma_f32_16x16x32_bf16 %0, v[236:239], a[116:119], %0" : "+v"(g[0][1][1]) :  : ); SB();
  asm volatile("ds_read_b128 a[60:63], %0 offset:3776" :  : "v"(bb) : "memory"); SB();
  asm volatile("v_mfma_f32_16x16x32_bf16 %0, v[240:243], a[132:135], %0" : "+v"(g[0][1][0]) :  : ); SB();
  asm volatile("buffer_load_dwordx4 a[200:203], %0, %1, %2 offen offset:0 sc1" :  : "v"(vO2), "s"(rWS), "s"(0u) : "memory"); SB();
  asm volatile("v_mfma_f32_16x16x32_bf16 %0, v[244:247], a[132:135], %0" : "+v"(g[0][1][1]) :  : ); SB();
  asm volatile("ds_read_b128 a[56:59], %0 offset:3264" :  : "v"(bb) : "memory"); SB();
  asm volatile("v_mfma_f32_16x16x32_bf16 %0, v[248:251], a[148:151], %0" : "+v"(g[0][1][0]) :  : ); SB();
  asm volatile("ds_read_b128 a[52:55], %0 offset:2752" :  : "v"(bb) : "memory"); SB();
  asm volatile("v_mfma_f32_16x16x32_bf16 %0, v[252:255], a[148:151], %0" : "+v"(g[0][1][1]) :  : ); SB();
  asm volatile("buffer_load_dwordx4 a[204:207], %0, %1, %2 offen offset:1024 sc1" :  : "v"(vO2), "s"(rWS), "s"(0u) : "memory"); SB();
  asm volatile("v_mfma_f32_16x16x32_bf16 %0, v[224:227], a[104:107], %0" : "+v"(g[0][2][0]) :  : ); SB();
  asm volatile("ds_read_b128 a[48:51], %0 offset:2240" :  : "v"(bb) : "memory"); SB();
  asm volatile("v_mfma_f32_16x16x32_bf16 %0, v[228:231], a[104:107], %0" : "+v"(g[0][2][1]) :  : ); SB();
  asm volatile("ds_read2_b32 a[68:69], %0 offset0:48 offset1:49" :  : "v"(aa[0]) : "memory"); SB();
  asm volatile("v_mfma_f32_16x16x32_bf16 %0, v[232:235], a[120:123], %0" : "+v"(g[0][2][0]) :  : ); SB();
  asm volatile("ds_write_b128 %0, %1 offset:0" :  : "v"(pw2), "v"(g[0][1][0]) : "memory"); SB();
  asm volatile("v_mfma_f32_16x16x32_bf16 %0, v[236:239], a[120:123], %0" : "+v"(g[0][2][1]) :  : ); SB();
  asm volatile("ds_read2_b32 a[70:71], %0 offset0:50 offset1:51" :  : "v"(aa[0]) : "memory"); SB();
  asm volatile("v_mfma_f32_16x16x32_bf16 %0, v[240:243], a[136:139], %0" : "+v"(g[0][2][0]) :  : ); SB();
  asm volatile("ds_read_b128 a[44:47], %0 offset:1728" :  : "v"(bb) : "memory"); SB();
  asm volatile("v_mfma_f32_16x16x32_bf16 %0, v[244:247], a[136:139], %0" : "+v"(g[0][2][1]) :  : ); SB();
  asm volatile("ds_write_b128 %0, %1 offset:1024" :  : "v"(pw2), "v"(g[0][1][1]) : "memory"); SB();
  asm volatile("v_mfma_f32_16x16x32_bf16 %0, v[248:251], a[152:155], %0" : "+v"(g[0][2][0]) :  : ); SB();
  asm volatile("ds_read2_b32 a[72:73], %0 offset0:176 offset1:177" :  : "v"(aa[1]) : "memory"); SB();
  asm volatile("v_mfma_f32_16x16x32_bf16 %0, v[252:255], a[152:155], %0" : "+v"(g[0][2][1]) :  : ); SB();
  asm volatile("ds_read2_b32 a[74:75], %0 offset0:178 offset1:179" :  : "v"(aa[1]) : "memory"); SB();
  asm volatile("v_mfma_f32_16x16x32_bf16 %0, v[224:227], a[108:111], %0" : "+v"(g[0][3][0]) :  : ); SB();
  asm volatile("ds_read2_b32 a[76:77], %0 offset0:48 offset1:49" :  : "v"(aa[1]) : "memory"); SB();
  asm volatile("v_mfma_f32_16x16x32_bf16 %0, v[228:231], a[108:111], %0" : "+v"(g[0][3][1]) :  : ); SB();
  asm volatile("ds_read2_b32 a[78:79], %0 offset0:50 offset1:51" :  : "v"(aa[1]) : "memory"); SB();
  asm volatile("v_mfma_f32_16x16x32_bf16 %0, v[232:235], a[124:127], %0" : "+v"(g[0][3][0]) :  : ); SB();
  asm volatile("ds_write_b128 %0, %1 offset:2048" :  : "v"(pw2), "v"(g[0][2][0]) : "memory"); SB();
  asm volatile("v_mfma_f32_16x16x32_bf16 %0, v[236:239], a[124:127], %0" : "+v"(g[0][3][1]) :  : ); SB();
  asm volatile("ds_read_b128 a[40:43], %0 offset:1216" :  : "v"(bb) : "memory"); SB();
  asm volatile("v_mfma_f32_16x16x32_bf16 %0, v[240:243], a[140:143], %0" : "+v"(g[0][3][0]) :  : ); SB();
  asm volatile("ds_read2_b32 a[80:81], %0 offset0:176 offset1:177" :  : "v"(aa[2]) : "memory"); SB();
  asm volatile("v_mfma_f32_16x16x32_bf16 %0, v[244:247], a[140:143], %0" : "+v"(g[0][3][1]) :  : ); SB();
  asm volatile("ds_write_b128 %0, %1 offset:3072" :  : "v"(pw2), "v"(g[0][2][1]) : "memory"); SB();
  asm volatile("v_mfma_f32_16x16x32_bf16 %0, v[248:251], a[156:159], %0" : "+v"(g[0][3][0]) :  : ); SB();
  asm volatile("s_waitcnt vmcnt(10)\n\ts_mov_b64 exec, 1\n\tbuffer_store_dwordx2 %0, off, %1, %2 sc1\n\ts_mov_b64 exec, -1" :  : "v"(((u32x2){tg0, tg1})), "s"(rWS), "s"(sFH) : "memory"); SB();
  asm volatile("buffer_load_dwordx4 a[208:211], %0, %1, %2 offen offset:2048 sc1" :  : "v"(vO2), "s"(rWS), "s"(0u) : "memory"); SB();
  asm volatile("v_mfma_f32_16x16x32_bf16 %0, v[252:255], a[156:159], %0" : "+v"(g[0][3][1]) :  : ); SB();
  asm volatile("ds_read2_b32 a[82:83], %0 offset0:178 offset1:179" :  : "v"(aa[2]) : "memory"); SB();
  asm volatile("v_mfma_f32_16x16x32_bf16 %0, v[224:227], a[96:99], %0" : "+v"(g[0][0][0]) :  : ); SB();
  asm volatile("ds_read2_b32 a[84:85], %0 offset0:48 offset1:49" :  : "v"(aa[2]) : "memory"); SB();
  asm volatile("v_mfma_f32_16x16x32_bf16 %0, v[228:231], a[96:99], %0" : "+v"(g[0][0][1]) :  : ); SB();
  asm volatile("buffer_load_dwordx4 a[212:215], %0, %1, %2 offen offset:3072 sc1" :  : "v"(vO2), "s"(rWS), "s"(0u) : "memory"); SB();
  asm volatile("v_mfma_f32_16x16x32_bf16 %0, v[232:235], a[112:115], %0" : "+v"(g[0][0][0]) :  : ); SB();
  asm volatile("ds_write_b128 %0, %1 offset:4096" :  : "v"(pw2), "v"(g[0][3][0]) : "memory"); SB();
  asm volatile("v_mfma_f32_16x16x32_bf16 %0, v[236:239], a[112:115], %0" : "+v"(g[0][0][1]) :  : ); SB();
  asm volatile("ds_read2_b32 a[86:87], %0 offset0:50 offset1:51" :  : "v"(aa[2]) : "memory"); SB();
  asm volatile("v_mfma_f32_16x16x32_bf16 %0, v[240:243], a[128:131], %0" : "+v"(g[0][0][0]) :  : ); SB();
  asm volatile("buffer_load_dwordx4 a[216:219], %0, %1, %2 offen offset:0 sc1" :  : "v"(vO2), "s"(rWS), "s"(4096u) : "memory"); SB();
  asm volatile("v_mfma_f32_16x16x32_bf16 %0, v[244:247], a[128:131], %0" : "+v"(g[0][0][1]) :  : ); SB();
  asm volatile("ds_write_b128 %0, %1 offset:5120" :  : "v"(pw2), "v"(g[0][3][1]) : "memory"); SB();
  asm volatile("v_mfma_f32_16x16x32_bf16 %0, v[248:251], a[144:147], %0" : "+v"(g[0][0][0]) :  : ); SB();
  asm volatile("buffer_load_dwordx4 a[220:223], %0, %1, %2 offen offset:1024 sc1" :  : "v"(vO2), "s"(rWS), "s"(4096u) : "memory"); SB();
  asm volatile("v_mfma_f32_16x16x32_bf16 %0, v[252:255], a[144:147], %0" : "+v"(g[0][0][1]) :  : ); SB();
  asm volatile("ds_read_b128 a[36:39], %0 offset:704" :  : "v"(bb) : "memory"); SB();
  if ((rb | cb | w) == 0) asm volatile("buffer_store_dwordx2 %0, off, %1, 0 sc1" :: "v"(((u32x2){tg0, tg1})), "s"(rWS) : "memory");
  SB();
  ST(17);
  unsigned pw3, p3[3], yw, ys[2], vYr, ya, po;
  asm volatile("s_waitcnt lgkmcnt(4)\n\tv_mfma_f32_16x16x32_bf16 %0, a[64:67], a[60:63], 0" : "=&v"(ac[7]) :  : ); SB();
  asm volatile("ds_read2_b32 a[88:89], %0 offset0:176 offset1:177" :  : "v"(aa[3]) : "memory"); SB();
  asm volatile("" : "+v"(yq[0]), "+v"(yq[1]), "+v"(yq[2]), "+v"(yq[3]), "+v"(yq[4]), "+v"(yq[5]), "+v"(yq[6]), "+v"(yq[7]), "+v"(ykv) :  : "memory"); SB();
  asm volatile("v_mfma_f32_16x16x32_bf16 %0, a[64:67], a[56:59], 0" : "=&v"(ac[6]) :  : ); SB();
  asm volatile("ds_read2_b32 a[90:91], %0 offset0:178 offset1:179" :  : "v"(aa[3]) : "memory"); SB();
  asm volatile("buffer_load_dwordx4 a[224:227], %0, %1, %2 offen offset:2048 sc1" :  : "v"(vO2), "s"(rWS), "s"(4096u) : "memory"); SB();
  asm volatile("v_mfma_f32_16x16x32_bf16 %0, a[64:67], a[52:55], 0" : "=&v"(ac[5]) :  : ); SB();
  asm volatile("ds_read2_b32 a[92:93], %0 offset0:48 offset1:49" :  : "v"(aa[3]) : "memory"); SB();
  asm volatile("v_add_f32 %0, %0, %1" : "+v"(yq[0][0]) : "v"(yq[2][0]) : ); SB();
  asm volatile("v_mfma_f32_16x16x32_bf16 %0, a[64:67], a[48:51], 0" : "=&v"(ac[4]) :  : ); SB();
  asm volatile("ds_read2_b32 a[94:95], %0 offset0:50 offset1:51" :  : "v"(aa[3]) : "memory"); SB();
  asm volatile("buffer_load_dwordx4 a[228:231], %0, %1, %2 offen offset:3072 sc1" :  : "v"(vO2), "s"(rWS), "s"(4096u) : "memory"); SB();
  asm volatile("v_mfma_f32_16x16x32_bf16 %0, a[68:71], a[56:59], %0" : "+v"(ac[7]) :  : ); SB();
  asm volatile("ds_read_b128 a[32:35], %0 offset:192" :  : "v"(bb) : "memory"); SB();
  asm volatile("s_waitcnt lgkmcnt(6)\n\ts_barrier" :  :  : "memory"); SB();
  asm volatile("v_mfma_f32_16x16x32_bf16 %0, a[68:71], a[52:55], %0" : "+v"(ac[6]) :  : ); SB();
  asm volatile("ds_read2_b32 a[128:129], %0 offset0:160 offset1:161" :  : "v"(aa[0]) : "memory"); SB();
  asm volatile("buffer_load_dwordx4 a[232:235], %0, %1, %2 offen offset:0 sc1" :  : "v"(vO2), "s"(rWS), "s"(8192u) : "memory"); SB();
  asm volatile("v_mfma_f32_16x16x32_bf16 %0, a[68:71], a[48:51], %0" : "+v"(ac[5]) :  : ); SB();
  asm volatile("ds_read2_b32 a[130:131], %0 offset0:162 offset1:163" :  : "v"(aa[0]) : "memory"); SB();
  asm volatile("v_add_f32 %0, %0, %1" : "+v"(yq[0][1]) : "v"(yq[2][1]) : ); SB();
  asm volatile("v_mfma_f32_16x16x32_bf16 %0, a[68:71], a[44:47], %0" : "+v"(ac[4]) :  : ); SB();
  asm volatile("ds_read_b128 %0, %1 offset:0" : "=&v"(pr[0]) : "v"(pe[0]) : "memory"); SB();
  asm volatile("v_add_f32 %0, %0, %1" : "+v"(yq[0][2]) : "v"(yq[2][2]) : ); SB();
  asm volatile("v_mfma_f32_16x16x32_bf16 %0, a[72:75], a[52:55], %0" : "+v"(ac[7]) :  : ); SB();
  asm volatile("ds_read_b128 a[124:127], %0 offset:3712" :  : "v"(bb) : "memory"); SB();
  asm volatile("buffer_load_dwordx4 a[236:239], %0, %1, %2 offen offset:1024 sc1" :  : "v"(vO2), "s"(rWS), "s"(8192u) : "memory"); SB();
  asm volatile("v_mfma_f32_16x16x32_bf16 %0, a[72:75], a[48:51], %0" : "+v"(ac[6]) :  : ); SB();
  asm volatile("ds_read_b128 a[120:123], %0 offset:3200" :  : "v"(bb) : "memory"); SB();
  asm volatile("v_add_f32 %0, %0, %1" : "+v"(yq[0][3]) : "v"(yq[2][3]) : ); SB();
  asm volatile("v_mfma_f32_16x16x32_bf16 %0, a[72:75], a[44:47], %0" : "+v"(ac[5]) :  : ); SB();
  asm volatile("ds_read_b128 a[116:119], %0 offset:2688" :  : "v"(bb) : "memory"); SB();
  asm volatile("v_add_f32 %0, %0, %1" : "+v"(yq[1][0]) : "v"(yq[3][0]) : ); SB();
  asm volatile("v_mfma_f32_16x16x32_bf16 %0, a[64:67], a[44:47], 0" : "=&v"(ac[3]) :  : ); SB();
  asm volatile("ds_read_b128 %0, %1 offset:1024" : "=&v"(pr[1]) : "v"(pe[0]) : "memory"); SB();
  asm volatile("buffer_load_dwordx4 a[240:243], %0, %1, %2 offen offset:2048 sc1" :  : "v"(vO), "s"(rWS), "s"(8192u) : "memory"); SB();
  asm volatile("v_mfma_f32_16x16x32_bf16 %0, a[76:79], a[48:51], %0" : "+v"(ac[7]) :  : ); SB();
  asm volatile("ds_read_b128 a[112:115], %0 offset:2176" :  : "v"(bb) : "memory"); SB();
  asm volatile("v_add_f32 %0, %0, %1" : "+v"(yq[1][1]) : "v"(yq[3][1]) : ); SB();
  asm volatile("v_mfma_f32_16x16x32_bf16 %0, a[76:79], a[44:47], %0" : "+v"(ac[6]) :  : ); SB();
  asm volatile("ds_read2_b32 a[132:133], %0 offset0:32 offset1:33" :  : "v"(aa[0]) : "memory"); SB();
  asm volatile("v_add_f32 %0, %0, %1" : "+v"(yq[1][2]) : "v"(yq[3][2]) : ); SB();
  asm volatile("v_mfma_f32_16x16x32_bf16 %0, a[76:79], a[40:43], %0" : "+v"(ac[5]) :  : ); SB();
  asm volatile("ds_read2_b32 a[134:135], %0 offset0:34 offset1:35" :  : "v"(aa[0]) : "memory"); SB();
  asm volatile("buffer_load_dwordx4 a[244:247], %0, %1, %2 offen offset:3072 sc1" :  : "v"(vO), "s"(rWS), "s"(8192u) : "memory"); SB();
  asm volatile("v_mfma_f32_16x16x32_bf16 %0, a[72:75], a[40:43], %0" : "+v"(ac[4]) :  : ); SB();
  asm volatile("ds_read_b128 %0, %1 offset:2048" : "=&v"(pr[2]) : "v"(pe[1]) : "memory"); SB();
  asm volatile("v_add_f32 %0, %0, %1" : "+v"(yq[1][3]) : "v"(yq[3][3]) : ); SB();
  asm volatile("v_mfma_f32_16x16x32_bf16 %0, a[80:83], a[44:47], %0" : "+v"(ac[7]) :  : ); SB();
  asm volatile("ds_read_b128 a[108:111], %0 offset:1664" :  : "v"(bb) : "memory"); SB();
  asm volatile("v_add_f32 %0, %0, %1" : "+v"(yq[4][0]) : "v"(yq[6][0]) : ); SB();
  asm volatile("v_mfma_f32_16x16x32_bf16 %0, a[80:83], a[40:43], %0" : "+v"(ac[6]) :  : ); SB();
  asm volatile("ds_read2_b32 a[136:137], %0 offset0:160 offset1:161" :  : "v"(aa[1]) : "memory"); SB();
  asm volatile("buffer_load_dwordx4 a[248:251], %0, %1, %2 offen offset:0 sc1" :  : "v"(vO), "s"(rWS), "s"(12288u) : "memory"); SB();
  asm volatile("v_mfma_f32_16x16x32_bf16 %0, a[68:71], a[40:43], %0" : "+v"(ac[3]) :  : ); SB();
  asm volatile("ds_read2_b32 a[138:139], %0 offset0:162 offset1:163" :  : "v"(aa[1]) : "memory"); SB();
  asm volatile("v_add_f32 %0, %0, %1" : "+v"(yq[4][1]) : "v"(yq[6][1]) : ); SB();
  asm volatile("v_mfma_f32_16x16x32_bf16 %0, a[64:67], a[40:43], 0" : "=&v"(ac[2]) :  : ); SB();
  asm volatile("ds_read_b128 %0, %1 offset:3072" : "=&v"(pr[3]) : "v"(pe[1]) : "memory"); SB();
  asm volatile("v_add_f32 %0, %0, %1" : "+v"(yq[4][2]) : "v"(yq[6][2]) : ); SB();
  asm volatile("v_mfma_f32_16x16x32_bf16 %0, a[84:87], a[40:43], %0" : "+v"(ac[7]) :  : ); SB();
  asm volatile("ds_read2_b32 a[140:141], %0 offset0:32 offset1:33" :  : "v"(aa[1]) : "memory"); SB();
  asm volatile("s_waitcnt lgkmcnt(8)\n\tv_mfma_f32_16x16x32_bf16 %0, a[84:87], a[36:39], %0" : "+v"(ac[6]) :  : ); SB();
  asm volatile("ds_read2_b32 a[142:143], %0 offset0:34 offset1:35" :  : "v"(aa[1]) : "memory"); SB();
  asm volatile("v_add_f32 %0, %0, %1" : "+v"(yq[4][3]) : "v"(yq[6][3]) : ); SB();
  asm volatile("v_mfma_f32_16x16x32_bf16 %0, a[80:83], a[36:39], %0" : "+v"(ac[5]) :  : ); SB();
  asm volatile("ds_read_b128 a[104:107], %0 offset:1152" :  : "v"(bb) : "memory"); SB();
  asm volatile("v_add_f32 %0, %0, %1" : "+v"(yq[5][0]) : "v"(yq[7][0]) : ); SB();
  asm volatile("v_mfma_f32_16x16x32_bf16 %0, a[76:79], a[36:39], %0" : "+v"(ac[4]) :  : ); SB();
  asm volatile("ds_read_b128 %0, %1 offset:4096" : "=&v"(pr[4]) : "v"(pe[2]) : "memory"); SB();
  asm volatile("v_add_f32 %0, %0, %1" : "+v"(yq[5][1]) : "v"(yq[7][1]) : ); SB();
  asm volatile("v_mfma_f32_16x16x32_bf16 %0, a[88:91], a[36:39], %0" : "+v"(ac[7]) :  : ); SB();
  asm volatile("ds_read2_b32 a[144:145], %0 offset0:160 offset1:161" :  : "v"(aa[2]) : "memory"); SB();
  asm volatile("v_add_f32 %0, %0, %1" : "+v"(yq[5][2]) : "v"(yq[7][2]) : ); SB();
  asm volatile("v_mfma_f32_16x16x32_bf16 %0, a[72:75], a[36:39], %0" : "+v"(ac[3]) :  : ); SB();
  asm volatile("ds_read2_b32 a[146:147], %0 offset0:162 offset1:163" :  : "v"(aa[2]) : "memory"); SB();
  asm volatile("v_add_f32 %0, %0, %1" : "+v"(yq[5][3]) : "v"(yq[7][3]) : ); SB();
  asm volatile("v_mfma_f32_16x16x32_bf16 %0, a[68:71], a[36:39], %0" : "+v"(ac[2]) :  : ); SB();
  asm volatile("ds_read2_b32 a[148:149], %0 offset0:32 offset1:33" :  : "v"(aa[2]) : "memory"); SB();
  asm volatile("v_add_f32 %0, %0, %1" : "+v"(yq[0][0]) : "v"(yq[4][0]) : ); SB();
  asm volatile("v_mfma_f32_16x16x32_bf16 %0, a[64:67], a[36:39], 0" : "=&v"(ac[1]) :  : ); SB();
  asm volatile("ds_read_b128 %0, %1 offset:5120" : "=&v"(pr[5]) : "v"(pe[2]) : "memory"); SB();
  asm volatile("v_add_f32 %0, %0, %1" : "+v"(yq[0][1]) : "v"(yq[4][1]) : ); SB();
  asm volatile("v_mfma_f32_16x16x32_bf16 %0, a[92:95], a[32:35], %0" : "+v"(ac[7]) :  : ); SB();
  asm volatile("ds_read2_b32 a[150:151], %0 offset0:34 offset1:35" :  : "v"(aa[2]) : "memory"); SB();
  asm volatile("v_add_f32 %0, %0, %1" : "+v"(yq[0][2]) : "v"(yq[4][2]) : ); SB();
  asm volatile("v_mfma_f32_16x16x32_bf16 %0, a[88:91], a[32:35], %0" : "+v"(ac[6]) :  : ); SB();
  asm volatile("ds_read_b128 a[100:103], %0 offset:640" :  : "v"(bb) : "memory"); SB();
  asm volatile("v_add_f32 %0, %0, %1" : "+v"(yq[0][3]) : "v"(yq[4][3]) : ); SB();
  asm volatile("v_mfma_f32_16x16x32_bf16 %0, a[84:87], a[32:35], %0" : "+v"(ac[5]) :  : ); SB();
  asm volatile("ds_read2_b32 a[152:153], %0 offset0:160 offset1:161" :  : "v"(aa[3]) : "memory"); SB();
  asm volatile("v_add_f32 %0, %0, %1" : "+v"(yq[1][0]) : "v"(yq[5][0]) : ); SB();
  asm volatile("v_mfma_f32_16x16x32_bf16 %0, a[80:83], a[32:35], %0" : "+v"(ac[4]) :  : ); SB();
  pw3 = lb + 98304 + w * 15360 + l16; asm volatile("" : "+v"(pw3)); SB();
  asm volatile("v_mfma_f32_16x16x32_bf16 %0, a[76:79], a[32:35], %0" : "+v"(ac[3]) :  : ); SB();
  asm volatile("ds_read2_b32 a[154:155], %0 offset0:162 offset1:163" :  : "v"(aa[3]) : "memory"); SB();
  asm volatile("v_add_f32 %0, %0, %1" : "+v"(yq[1][1]) : "v"(yq[5][1]) : ); SB();
  asm volatile("v_mfma_f32_16x16x32_bf16 %0, a[72:75], a[32:35], %0" : "+v"(ac[2]) :  : ); SB();
  asm volatile("ds_read2_b32 a[156:157], %0 offset0:32 offset1:33" :  : "v"(aa[3]) : "memory"); SB();
  asm volatile("s_waitcnt lgkmcnt(5)" : "+v"(pr[0]), "+v"(pr[1]), "+v"(pr[2]), "+v"(pr[3]), "+v"(pr[4]), "+v"(pr[5]) :  : "memory"); SB();
  asm volatile("v_mfma_f32_16x16x32_bf16 %0, a[68:71], a[32:35], %0" : "+v"(ac[1]) :  : ); SB();
  asm volatile("ds_read2_b32 a[158:159], %0 offset0:34 offset1:35" :  : "v"(aa[3]) : "memory"); SB();
  asm volatile("buffer_load_dwordx4 a[252:255], %0, %1, %2 offen offset:1024 sc1" :  : "v"(vO), "s"(rWS), "s"(12288u) : "memory"); SB();
  asm volatile("v_mfma_f32_16x16x32_bf16 %0, a[64:67], a[32:35], 0" : "=&v"(ac[0]) :  : ); SB();
  {
    const f32x4 h0 = ((g[0][0][0] + pr[0]) + (pr[2] + pr[4])) * a1[1][1], h1 = ((g[0][0][1] + pr[1]) + (pr[3] + pr[5])) * a1[1][2];
    ho[2] = (u32x4){pack2(h0[0], h0[1]), pack2(h0[2], h0[3]), pack2(h1[0], h1[1]), nohole(pack2(h1[2], h1[3]))};
    SB();
  }
  asm volatile("v_mfma_f32_16x16x32_bf16 %0, a[128:131], a[124:127], %0" : "+v"(ac[7]) :  : ); SB();
  asm volatile("ds_read_b128 a[96:99], %0 offset:128" :  : "v"(bb) : "memory"); SB();
  asm volatile("buffer_load_dwordx4 a[0:3], %0, %1, %2 offen offset:2048 sc1" :  : "v"(vO), "s"(rWS), "s"(12288u) : "memory"); SB();
  asm volatile("v_mfma_f32_16x16x32_bf16 %0, a[128:131], a[120:123], %0" : "+v"(ac[6]) :  : ); SB();
  asm volatile("ds_read2_b32 a[64:65], %0 offset0:144 offset1:145" :  : "v"(aa[0]) : "memory"); SB();
  asm volatile("buffer_load_dwordx4 a[4:7], %0, %1, %2 offen offset:2048 sc1" :  : "v"(vO2), "s"(rWS), "s"(8192u) : "memory"); SB();
  asm volatile("v_mfma_f32_16x16x32_bf16 %0, a[128:131], a[116:119], %0" : "+v"(ac[5]) :  : ); SB();
  asm volatile("ds_read2_b32 a[66:67], %0 offset0:146 offset1:147" :  : "v"(aa[0]) : "memory"); SB();
  asm volatile("buffer_load_dwordx4 a[8:11], %0, %1, %2 offen offset:3072 sc1" :  : "v"(vO2), "s"(rWS), "s"(8192u) : "memory"); SB();
  asm volatile("v_mfma_f32_16x16x32_bf16 %0, a[128:131], a[112:115], %0" : "+v"(ac[4]) :  : ); SB();
  asm volatile("buffer_store_dwordx4 %1, %0, %2, %3 offen offset:2048 sc1\n\ts_nop 1" :  : "v"(vH_), "v"(ho[2]), "s"(rWS), "s"(0u) : "memory"); SB();
  asm volatile("v_mfma_f32_16x16x32_bf16 %0, a[132:135], a[120:123], %0" : "+v"(ac[7]) :  : ); SB();
  asm volatile("ds_read_b128 a[60:63], %0 offset:3648" :  : "v"(bb) : "memory"); SB();
  asm volatile("v_add_f32 %0, %0, %1" : "+v"(yq[1][2]) : "v"(yq[5][2]) : ); SB();
  asm volatile("v_mfma_f32_16x16x32_bf16 %0, a[132:135], a[116:119], %0" : "+v"(ac[6]) :  : ); SB();
  asm volatile("ds_read_b128 a[56:59], %0 offset:3136" :  : "v"(bb) : "memory"); SB();
  asm volatile("v_add_f32 %0, %0, %1" : "+v"(yq[1][3]) : "v"(yq[5][3]) : ); SB();
  asm volatile("v_mfma_f32_16x16x32_bf16 %0, a[132:135], a[112:115], %0" : "+v"(ac[5]) :  : ); SB();
  asm volatile("ds_read_b128 a[52:55], %0 offset:2624" :  : "v"(bb) : "memory"); SB();
  asm volatile("v_lshlrev_b32 %0, 16, %1" : "=v"(yq[2][0]) : "v"(ykv[0]) : ); SB();
  asm volatile("v_mfma_f32_16x16x32_bf16 %0, a[132:135], a[108:111], %0" : "+v"(ac[4]) :  : ); SB();
  p3[0] = lb + 98304 + (w ^ 1) * 15360 + l16; asm volatile("" : "+v"(p3[0])); SB();
  asm volatile("v_mfma_f32_16x16x32_bf16 %0, a[136:139], a[116:119], %0" : "+v"(ac[7]) :  : ); SB();
  asm volatile("ds_read_b128 a[48:51], %0 offset:2112" :  : "v"(bb) : "memory"); SB();
  asm volatile("buffer_load_dwordx4 a[12:15], %0, %1, %2 offen offset:0 sc1" :  : "v"(vO2), "s"(rWS), "s"(12288u) : "memory"); SB();
  asm volatile("v_mfma_f32_16x16x32_bf16 %0, a[136:139], a[112:115], %0" : "+v"(ac[6]) :  : ); SB();
  asm volatile("ds_read2_b32 a[68:69], %0 offset0:16 offset1:17" :  : "v"(aa[0]) : "memory"); SB();
  asm volatile("v_and_b32 %0, 0xffff0000, %1" : "=v"(yq[2][1]) : "v"(ykv[0]) : ); SB();
  asm volatile("v_mfma_f32_16x16x32_bf16 %0, a[136:139], a[108:111], %0" : "+v"(ac[5]) :  : ); SB();
  asm volatile("ds_read2_b32 a[70:71], %0 offset0:18 offset1:19" :  : "v"(aa[0]) : "memory"); SB();
  asm volatile("v_lshlrev_b32 %0, 16, %1" : "=v"(yq[2][2]) : "v"(ykv[1]) : ); SB();
  asm volatile("v_mfma_f32_16x16x32_bf16 %0, a[128:131], a[108:111], %0" : "+v"(ac[3]) :  : ); SB();
  p3[1] = lb + 98304 + (w ^ 2) * 15360 + l16; asm volatile("" : "+v"(p3[1])); SB();
  asm volatile("v_mfma_f32_16x16x32_bf16 %0, a[140:143], a[112:115], %0" : "+v"(ac[7]) :  : ); SB();
  asm volatile("ds_read_b128 a[44:47], %0 offset:1600" :  : "v"(bb) : "memory"); SB();
  asm volatile("buffer_load_dwordx4 a[16:19], %0, %1, %2 offen offset:1024 sc1" :  : "v"(vO2), "s"(rWS), "s"(12288u) : "memory"); SB();
  asm volatile("v_mfma_f32_16x16x32_bf16 %0, a[140:143], a[108:111], %0" : "+v"(ac[6]) :  : ); SB();
  asm volatile("ds_read2_b32 a[72:73], %0 offset0:144 offset1:145" :  : "v"(aa[1]) : "memory"); SB();
  asm volatile("v_and_b32 %0, 0xffff0000, %1" : "=v"(yq[2][3]) : "v"(ykv[1]) : ); SB();
  asm volatile("v_mfma_f32_16x16x32_bf16 %0, a[140:143], a[104:107], %0" : "+v"(ac[5]) :  : ); SB();
  asm volatile("ds_read2_b32 a[74:75], %0 offset0:146 offset1:147" :  : "v"(aa[1]) : "memory"); SB();
  asm volatile("buffer_load_dwordx4 a[20:23], %0, %1, %2 offen offset:2048 sc1" :  : "v"(vO2), "s"(rWS), "s"(12288u) : "memory"); SB();
  asm volatile("v_mfma_f32_16x16x32_bf16 %0, a[136:139], a[104:107], %0" : "+v"(ac[4]) :  : ); SB();
  p3[2] = lb + 98304 + (w ^ 3) * 15360 + l16; asm volatile("" : "+v"(p3[2])); SB();
  asm volatile("v_mfma_f32_16x16x32_bf16 %0, a[144:147], a[108:111], %0" : "+v"(ac[7]) :  : ); SB();
  asm volatile("ds_read2_b32 a[76:77], %0 offset0:16 offset1:17" :  : "v"(aa[1]) : "memory"); SB();
  asm volatile("v_lshlrev_b32 %0, 16, %1" : "=v"(yq[3][0]) : "v"(ykv[2]) : ); SB();
  asm volatile("v_mfma_f32_16x16x32_bf16 %0, a[144:147], a[104:107], %0" : "+v"(ac[6]) :  : ); SB();
  asm volatile("ds_read2_b32 a[78:79], %0 offset0:18 offset1:19" :  : "v"(aa[1]) : "memory"); SB();
  asm volatile("v_and_b32 %0, 0xffff0000, %1" : "=v"(yq[3][1]) : "v"(ykv[2]) : ); SB();
  asm volatile("v_mfma_f32_16x16x32_bf16 %0, a[132:135], a[104:107], %0" : "+v"(ac[3]) :  : ); SB();
  asm volatile("ds_read_b128 a[40:43], %0 offset:1088" :  : "v"(bb) : "memory"); SB();
  asm volatile("buffer_load_dwordx2 %0, %1, %2, %3 offen sc1" : "=&v"(fh) : "v"(vFH), "s"(rWS), "s"(0u) : "memory"); SB();
  asm volatile("v_mfma_f32_16x16x32_bf16 %0, a[128:131], a[104:107], %0" : "+v"(ac[2]) :  : ); SB();
  yw = (lane ^ ((lane >> 3) & 6)) * 16; asm volatile("" : "+v"(yw)); SB();
  asm volatile("s_waitcnt lgkmcnt(6)\n\tv_mfma_f32_16x16x32_bf16 %0, a[148:151], a[104:107], %0" : "+v"(ac[7]) :  : ); SB();
  asm volatile("ds_read2_b32 a[80:81], %0 offset0:144 offset1:145" :  : "v"(aa[2]) : "memory"); SB();
  asm volatile("v_lshlrev_b32 %0, 16, %1" : "=v"(yq[3][2]) : "v"(ykv[3]) : ); SB();
  asm volatile("v_mfma_f32_16x16x32_bf16 %0, a[148:151], a[100:103], %0" : "+v"(ac[6]) :  : ); SB();
  asm volatile("ds_read2_b32 a[82:83], %0 offset0:146 offset1:147" :  : "v"(aa[2]) : "memory"); SB();
  asm volatile("v_and_b32 %0, 0xffff0000, %1" : "=v"(yq[3][3]) : "v"(ykv[3]) : ); SB();
  asm volatile("v_mfma_f32_16x16x32_bf16 %0, a[144:147], a[100:103], %0" : "+v"(ac[5]) :  : ); SB();
  asm volatile("ds_read2_b32 a[84:85], %0 offset0:16 offset1:17" :  : "v"(aa[2]) : "memory"); SB();
  asm volatile("v_fmac_f32 %0, %1, %2" : "+v"(yq[0][0]) : "v"(yq[2][0]), "v"(dD[2]) : ); SB();
  asm volatile("v_mfma_f32_16x16x32_bf16 %0, a[140:143], a[100:103], %0" : "+v"(ac[4]) :  : ); SB();
  yw = yw + lb + 32768 + w * 3072; asm volatile("" : "+v"(yw)); SB();
  asm volatile("v_mfma_f32_16x16x32_bf16 %0, a[152:155], a[100:103], %0" : "+v"(ac[7]) :  : ); SB();
  asm volatile("ds_read2_b32 a[86:87], %0 offset0:18 offset1:19" :  : "v"(aa[2]) : "memory"); SB();
  asm volatile("v_fmac_f32 %0, %1, %2" : "+v"(yq[0][1]) : "v"(yq[2][1]), "v"(dD[2]) : ); SB();
  asm volatile("v_mfma_f32_16x16x32_bf16 %0, a[136:139], a[100:103], %0" : "+v"(ac[3]) :  : ); SB();
  asm volatile("ds_read_b128 a[36:39], %0 offset:576" :  : "v"(bb) : "memory"); SB();
  asm volatile("v_fmac_f32 %0, %1, %2" : "+v"(yq[0][2]) : "v"(yq[2][2]), "v"(dD[2]) : ); SB();
  asm volatile("v_mfma_f32_16x16x32_bf16 %0, a[132:135], a[100:103], %0" : "+v"(ac[2]) :  : ); SB();
  asm volatile("ds_read2_b32 a[88:89], %0 offset0:144 offset1:145" :  : "v"(aa[3]) : "memory"); SB();
  asm volatile("v_fmac_f32 %0, %1, %2" : "+v"(yq[0][3]) : "v"(yq[2][3]), "v"(dD[2]) : ); SB();
  asm volatile("v_mfma_f32_16x16x32_bf16 %0, a[128:131], a[100:103], %0" : "+v"(ac[1]) :  : ); SB();
  ya = lb + 32768 + (4 * g4 + q4) * 128 + 8 * p4; asm volatile("" : "+v"(ya)); SB();
  asm volatile("v_mfma_f32_16x16x32_bf16 %0, a[156:159], a[96:99], %0" : "+v"(ac[7]) :  : ); SB();
  asm volatile("ds_read2_b32 a[90:91], %0 offset0:146 offset1:147" :  : "v"(aa[3]) : "memory"); SB();
  asm volatile("v_fmac_f32 %0, %1, %2" : "+v"(yq[1][0]) : "v"(yq[3][0]), "v"(dD[2]) : ); SB();
  asm volatile("v_mfma_f32_16x16x32_bf16 %0, a[152:155], a[96:99], %0" : "+v"(ac[6]) :  : ); SB();
  asm volatile("ds_read2_b32 a[92:93], %0 offset0:16 offset1:17" :  : "v"(aa[3]) : "memory"); SB();
  asm volatile("v_fmac_f32 %0, %1, %2" : "+v"(yq[1][1]) : "v"(yq[3][1]), "v"(dD[2]) : ); SB();
  asm volatile("v_mfma_f32_16x16x32_bf16 %0, a[148:151], a[96:99], %0" : "+v"(ac[5]) :  : ); SB();
  asm volatile("ds_read2_b32 a[94:95], %0 offset0:18 offset1:19" :  : "v"(aa[3]) : "memory"); SB();
  asm volatile("v_fmac_f32 %0, %1, %2" : "+v"(yq[1][2]) : "v"(yq[3][2]), "v"(dD[2]) : ); SB();
  asm volatile("v_mfma_f32_16x16x32_bf16 %0, a[144:147], a[96:99], %0" : "+v"(ac[4]) :  : ); SB();
  po = (2 * g4 + (q4 >> 1)) & 3; asm volatile("" : "+v"(po)); SB();
  asm volatile("v_mfma_f32_16x16x32_bf16 %0, a[140:143], a[96:99], %0" : "+v"(ac[3]) :  : ); SB();
  asm volatile("ds_read_b128 a[32:35], %0 offset:64" :  : "v"(bb) : "memory"); SB();
  asm volatile("v_fmac_f32 %0, %1, %2" : "+v"(yq[1][3]) : "v"(yq[3][3]), "v"(dD[2]) : ); SB();
  asm volatile("v_mfma_f32_16x16x32_bf16 %0, a[136:139], a[96:99], %0" : "+v"(ac[2]) :  : ); SB();
  ya = ya + ((w ^ po) << 5); asm volatile("" : "+v"(ya)); SB();
  asm volatile("v_mfma_f32_16x16x32_bf16 %0, a[132:135], a[96:99], %0" : "+v"(ac[1]) :  : ); SB();
  po = vX + 24 * (g4 & 1); asm volatile("" : "+v"(po)); SB();
  asm volatile("v_mfma_f32_16x16x32_bf16 %0, a[128:131], a[96:99], %0" : "+v"(ac[0]) :  : ); SB();
  asm volatile("v_cvt_pk_bf16_f32 %0, %1, %2" : "=v"(y2[0]) : "v"(yq[0][0]), "v"(yq[0][1]) : ); SB();
  asm volatile("v_mfma_f32_16x16x32_bf16 %0, a[64:67], a[60:63], %0" : "+v"(ac[7]) :  : ); SB();
  asm volatile("ds_read2_b32 a[128:129], %0 offset0:128 offset1:129" :  : "v"(aa[0]) : "memory"); SB();
  asm volatile("v_cvt_pk_bf16_f32 %0, %1, %2" : "=v"(y2[1]) : "v"(yq[0][2]), "v"(yq[0][3]) : ); SB();
  asm volatile("v_mfma_f32_16x16x32_bf16 %0, a[64:67], a[56:59], %0" : "+v"(ac[6]) :  : ); SB();
  asm volatile("ds_read2_b32 a[130:131], %0 offset0:130 offset1:131" :  : "v"(aa[0]) : "memory"); SB();
  asm volatile("v_cvt_pk_bf16_f32 %0, %1, %2" : "=v"(y2[2]) : "v"(yq[1][0]), "v"(yq[1][1]) : ); SB();
  asm volatile("v_mfma_f32_16x16x32_bf16 %0, a[64:67], a[52:55], %0" : "+v"(ac[5]) :  : ); SB();
  asm volatile("ds_read_b128 a[124:127], %0 offset:3584" :  : "v"(bb) : "memory"); SB();
  asm volatile("v_cvt_pk_bf16_f32 %0, %1, %2" : "=v"(y2[3]) : "v"(yq[1][2]), "v"(yq[1][3]) : ); SB();
  asm volatile("v_mfma_f32_16x16x32_bf16 %0, a[64:67], a[48:51], %0" : "+v"(ac[4]) :  : ); SB();
  asm volatile("v_min_u32 %0, -2, %0" : "+v"(y2[3]) :  : ); SB();
  asm volatile("v_mfma_f32_16x16x32_bf16 %0, a[68:71], a[56:59], %0" : "+v"(ac[7]) :  : ); SB();
  asm volatile("ds_read_b128 a[120:123], %0 offset:3072" :  : "v"(bb) : "memory"); SB();
  asm volatile("v_xnor_b32 %0, %0, %1" : "+v"(y2[3]) : "v"(ykv[3]) : ); SB();
  asm volatile("v_mfma_f32_16x16x32_bf16 %0, a[68:71], a[52:55], %0" : "+v"(ac[6]) :  : ); SB();
  asm volatile("ds_read_b128 a[96:99], %0 offset:0" :  : "v"(bb) : "memory"); SB();
  asm volatile("v_mfma_f32_16x16x32_bf16 %0, a[68:71], a[48:51], %0" : "+v"(ac[5]) :  : ); SB();
  asm volatile("ds_read_b128 a[100:103], %0 offset:512" :  : "v"(bb) : "memory"); SB();
  asm volatile("buffer_store_dwordx4 %1, %0, %2, %3 offen offset:0 sc1\n\ts_nop 1" :  : "v"(vF), "v"(y2), "s"(rWS), "s"(uso) : "memory"); SB();
  asm volatile("s_waitcnt lgkmcnt(6)\n\tv_mfma_f32_16x16x32_bf16 %0, a[68:71], a[44:47], %0" : "+v"(ac[4]) :  : ); SB();
  asm volatile("v_mfma_f32_16x16x32_bf16 %0, a[72:75], a[52:55], %0" : "+v"(ac[7]) :  : ); SB();
  asm volatile("ds_read2_b32 a[132:133], %0 offset0:0 offset1:1" :  : "v"(aa[0]) : "memory"); SB();
  asm volatile("v_mfma_f32_16x16x32_bf16 %0, a[72:75], a[48:51], %0" : "+v"(ac[6]) :  : ); SB();
  asm volatile("ds_read2_b32 a[134:135], %0 offset0:2 offset1:3" :  : "v"(aa[0]) : "memory"); SB();
  asm volatile("v_mfma_f32_16x16x32_bf16 %0, a[72:75], a[44:47], %0" : "+v"(ac[5]) :  : ); SB();
  asm volatile("ds_read_b128 a[116:119], %0 offset:2560" :  : "v"(bb) : "memory"); SB();
  asm volatile("v_mfma_f32_16x16x32_bf16 %0, a[64:67], a[44:47], %0" : "+v"(ac[3]) :  : ); SB();
  asm volatile("v_mfma_f32_16x16x32_bf16 %0, a[76:79], a[48:51], %0" : "+v"(ac[7]) :  : ); SB();
  asm volatile("ds_read_b128 a[104:107], %0 offset:1024" :  : "v"(bb) : "memory"); SB();
  asm volatile("v_mfma_f32_16x16x32_bf16 %0, a[76:79], a[44:47], %0" : "+v"(ac[6]) :  : ); SB();
  asm volatile("ds_read2_b32 a[136:137], %0 offset0:128 offset1:129" :  : "v"(aa[1]) : "memory"); SB();
  asm volatile("v_mfma_f32_16x16x32_bf16 %0, a[76:79], a[40:43], %0" : "+v"(ac[5]) :  : ); SB();
  asm volatile("ds_read2_b32 a[138:139], %0 offset0:130 offset1:131" :  : "v"(aa[1]) : "memory"); SB();
  asm volatile("v_mfma_f32_16x16x32_bf16 %0, a[72:75], a[40:43], %0" : "+v"(ac[4]) :  : ); SB();
  asm volatile("v_mfma_f32_16x16x32_bf16 %0, a[80:83], a[44:47], %0" : "+v"(ac[7]) :  : ); SB();
  asm volatile("ds_read_b128 a[108:111], %0 offset:1536" :  : "v"(bb) : "memory"); SB();
  asm volatile("v_mfma_f32_16x16x32_bf16 %0, a[80:83], a[40:43], %0" : "+v"(ac[6]) :  : ); SB();
  asm volatile("ds_read_b128 a[112:115], %0 offset:2048" :  : "v"(bb) : "memory"); SB();
  asm volatile("v_mfma_f32_16x16x32_bf16 %0, a[68:71], a[40:43], %0" : "+v"(ac[3]) :  : ); SB();
  asm volatile("ds_read2_b32 a[140:141], %0 offset0:0 offset1:1" :  : "v"(aa[1]) : "memory"); SB();
  asm volatile("v_mfma_f32_16x16x32_bf16 %0, a[64:67], a[40:43], %0" : "+v"(ac[2]) :  : ); SB();
  asm volatile("v_mfma_f32_16x16x32_bf16 %0, a[84:87], a[40:43], %0" : "+v"(ac[7]) :  : ); SB();
  asm volatile("ds_read2_b32 a[142:143], %0 offset0:2 offset1:3" :  : "v"(aa[1]) : "memory"); SB();
  asm volatile("v_mfma_f32_16x16x32_bf16 %0, a[84:87], a[36:39], %0" : "+v"(ac[6]) :  : ); SB();
  asm volatile("ds_read2_b32 a[144:145], %0 offset0:128 offset1:129" :  : "v"(aa[2]) : "memory"); SB();
  asm volatile("v_mfma_f32_16x16x32_bf16 %0, a[80:83], a[36:39], %0" : "+v"(ac[5]) :  : ); SB();
  asm volatile("ds_read2_b32 a[146:147], %0 offset0:130 offset1:131" :  : "v"(aa[2]) : "memory"); SB();
  asm volatile("v_mfma_f32_16x16x32_bf16 %0, a[76:79], a[36:39], %0" : "+v"(ac[4]) :  : ); SB();
  asm volatile("v_mfma_f32_16x16x32_bf16 %0, a[88:91], a[36:39], %0" : "+v"(ac[7]) :  : ); SB();
  asm volatile("ds_read2_b32 a[148:149], %0 offset0:0 offset1:1" :  : "v"(aa[2]) : "memory"); SB();
  asm volatile("v_mfma_f32_16x16x32_bf16 %0, a[72:75], a[36:39], %0" : "+v"(ac[3]) :  : ); SB();
  asm volatile("ds_read2_b32 a[150:151], %0 offset0:2 offset1:3" :  : "v"(aa[2]) : "memory"); SB();
  asm volatile("v_mfma_f32_16x16x32_bf16 %0, a[68:71], a[36:39], %0" : "+v"(ac[2]) :  : ); SB();
  asm volatile("ds_read2_b32 a[152:153], %0 offset0:128 offset1:129" :  : "v"(aa[3]) : "memory"); SB();
  asm volatile("v_mfma_f32_16x16x32_bf16 %0, a[64:67], a[36:39], %0" : "+v"(ac[1]) :  : ); SB();
  asm volatile("v_mfma_f32_16x16x32_bf16 %0, a[92:95], a[32:35], %0" : "+v"(ac[7]) :  : ); SB();
  asm volatile("ds_read2_b32 a[154:155], %0 offset0:130 offset1:131" :  : "v"(aa[3]) : "memory"); SB();
  asm volatile("v_mfma_f32_16x16x32_bf16 %0, a[88:91], a[32:35], %0" : "+v"(ac[6]) :  : ); SB();
  asm volatile("ds_read2_b32 a[156:157], %0 offset0:0 offset1:1" :  : "v"(aa[3]) : "memory"); SB();
  asm volatile("v_mfma_f32_16x16x32_bf16 %0, a[84:87], a[32:35], %0" : "+v"(ac[5]) :  : ); SB();
  asm volatile("ds_read2_b32 a[158:159], %0 offset0:2 offset1:3" :  : "v"(aa[3]) : "memory"); SB();
  asm volatile("v_mfma_f32_16x16x32_bf16 %0, a[80:83], a[32:35], %0" : "+v"(ac[4]) :  : ); SB();
  asm volatile("v_mfma_f32_16x16x32_bf16 %0, a[76:79], a[32:35], %0" : "+v"(ac[3]) :  : ); SB();
  asm volatile("v_mfma_f32_16x16x32_bf16 %0, a[72:75], a[32:35], %0" : "+v"(ac[2]) :  : ); SB();
  asm volatile("v_mfma_f32_16x16x32_bf16 %0, a[68:71], a[32:35], %0" : "+v"(ac[1]) :  : ); SB();
  asm volatile("v_mfma_f32_16x16x32_bf16 %0, a[64:67], a[32:35], %0" : "+v"(ac[0]) :  : ); SB();
  asm volatile("s_waitcnt lgkmcnt(4)\n\tv_mfma_f32_16x16x32_bf16 %0, a[128:131], a[124:127], %0" : "+v"(ac[7]) :  : ); SB();
  asm volatile("v_mfma_f32_16x16x32_bf16 %0, a[128:131], a[120:123], %0" : "+v"(ac[6]) :  : ); SB();
  asm volatile("v_mfma_f32_16x16x32_bf16 %0, a[128:131], a[96:99], %0" : "+v"(ac[0]) :  : ); SB();
  asm volatile("v_mfma_f32_16x16x32_bf16 %0, a[128:131], a[100:103], %0" : "+v"(ac[1]) :  : ); SB();
  asm volatile("v_mfma_f32_16x16x32_bf16 %0, a[132:135], a[120:123], %0" : "+v"(ac[7]) :  : ); SB();
  asm volatile("v_mfma_f32_16x16x32_bf16 %0, a[132:135], a[116:119], %0" : "+v"(ac[6]) :  : ); SB();
  asm volatile("v_mfma_f32_16x16x32_bf16 %0, a[128:131], a[104:107], %0" : "+v"(ac[2]) :  : ); SB();
  asm volatile("ds_write_b128 %0, %1 offset:0" :  : "v"(dd), "v"(ac[0]) : "memory"); SB();
  asm volatile("v_mfma_f32_16x16x32_bf16 %0, a[132:135], a[96:99], %0" : "+v"(ac[1]) :  : ); SB();
  asm volatile("s_waitcnt vmcnt(1)\n\t.Lc_%=:\n\tv_cmp_ne_u64 vcc, %2, %1\n\ts_cbranch_vccnz .Lr_%=\n\t.Ld_%=:\n\t.subsection 1\n\t.Lr_%=:\n\tv_add_u32 %0, -1, %0\n\ts_nop 1\n\tv_readfirstlane_b32 vcc_hi, %0\n\ts_nop 3\n\ts_cmp_lt_i32 vcc_hi, 1\n\ts_cbranch_scc1 .Ld_%=\n\ts_sleep 2\n\ts_mov_b32 vcc_lo, 0\n\tbuffer_load_dwordx2 %1, %3, %4, vcc_lo offen sc1\n\ts_waitcnt vmcnt(0)\n\ts_branch .Lc_%=\n\t.subsection 0" : "+v"(tmo), "+v"(fh) : "s"(tg), "v"(vFH), "s"(rWS) : "memory", "vcc", "scc"); SB();
  asm volatile("buffer_load_dwordx4 v[164:167], %0, %1, %2 offen offset:0 sc1" :  : "v"(vHr), "s"(rWS), "s"(hso[2]) : "memory"); SB();
  asm volatile("v_mfma_f32_16x16x32_bf16 %0, a[136:139], a[116:119], %0" : "+v"(ac[7]) :  : ); SB();
  asm volatile("buffer_load_dwordx4 v[168:171], %0, %1, %2 offen offset:0 sc1" :  : "v"(vHr), "s"(rWS), "s"(hso[4]) : "memory"); SB();
  asm volatile("v_mfma_f32_16x16x32_bf16 %0, a[128:131], a[108:111], %0" : "+v"(ac[3]) :  : ); SB();
  asm volatile("buffer_load_dwordx4 v[172:175], %0, %1, %2 offen offset:0 sc1" :  : "v"(vHr), "s"(rWS), "s"(hso[6]) : "memory"); SB();
  asm volatile("v_mfma_f32_16x16x32_bf16 %0, a[132:135], a[100:103], %0" : "+v"(ac[2]) :  : ); SB();
  asm volatile("v_mfma_f32_16x16x32_bf16 %0, a[128:131], a[112:115], %0" : "+v"(ac[4]) :  : ); SB();
  asm volatile("buffer_load_dwordx4 v[160:163], %0, %1, %2 offen offset:0 sc1" :  : "v"(vHr), "s"(rWS), "s"(hso[0]) : "memory"); SB();
  asm volatile("v_mfma_f32_16x16x32_bf16 %0, a[140:143], a[112:115], %0" : "+v"(ac[7]) :  : ); SB();
  asm volatile("ds_write_b128 %0, %1 offset:1024" :  : "v"(dd), "v"(ac[1]) : "memory"); SB();
  asm volatile("v_mfma_f32_16x16x32_bf16 %0, a[132:135], a[104:107], %0" : "+v"(ac[3]) :  : ); SB();
  asm volatile("buffer_load_dwordx4 v[180:183], %0, %1, %2 offen offset:1024 sc1" :  : "v"(vHr), "s"(rWS), "s"(hso[2]) : "memory"); SB();
  asm volatile("v_mfma_f32_16x16x32_bf16 %0, a[136:139], a[96:99], %0" : "+v"(ac[2]) :  : ); SB();
  asm volatile("v_mfma_f32_16x16x32_bf16 %0, a[132:135], a[108:111], %0" : "+v"(ac[4]) :  : ); SB();
  asm volatile("v_mfma_f32_16x16x32_bf16 %0, a[128:131], a[116:119], %0" : "+v"(ac[5]) :  : ); SB();
  asm volatile("v_mfma_f32_16x16x32_bf16 %0, a[136:139], a[100:103], %0" : "+v"(ac[3]) :  : ); SB();
  asm volatile("buffer_load_dwordx4 v[184:187], %0, %1, %2 offen offset:1024 sc1" :  : "v"(vHr), "s"(rWS), "s"(hso[4]) : "memory"); SB();
  asm volatile("v_mfma_f32_16x16x32_bf16 %0, a[136:139], a[112:115], %0" : "+v"(ac[6]) :  : ); SB();
  asm volatile("ds_write_b128 %0, %1 offset:2048" :  : "v"(dd), "v"(ac[2]) : "memory"); SB();
  asm volatile("v_mfma_f32_16x16x32_bf16 %0, a[136:139], a[104:107], %0" : "+v"(ac[4]) :  : ); SB();
  asm volatile("buffer_load_dwordx4 v[188:191], %0, %1, %2 offen offset:1024 sc1" :  : "v"(vHr), "s"(rWS), "s"(hso[6]) : "memory"); SB();
  asm volatile("v_mfma_f32_16x16x32_bf16 %0, a[132:135], a[112:115], %0" : "+v"(ac[5]) :  : ); SB();
  asm volatile("v_mfma_f32_16x16x32_bf16 %0, a[140:143], a[96:99], %0" : "+v"(ac[3]) :  : ); SB();
  asm volatile("v_mfma_f32_16x16x32_bf16 %0, a[140:143], a[108:111], %0" : "+v"(ac[6]) :  : ); SB();
  asm volatile("v_mfma_f32_16x16x32_bf16 %0, a[140:143], a[100:103], %0" : "+v"(ac[4]) :  : ); SB();
  asm volatile("buffer_load_dwordx4 v[176:179], %0, %1, %2 offen offset:1024 sc1" :  : "v"(vHr), "s"(rWS), "s"(hso[0]) : "memory"); SB();
  asm volatile("v_mfma_f32_16x16x32_bf16 %0, a[136:139], a[108:111], %0" : "+v"(ac[5]) :  : ); SB();
  asm volatile("v_mfma_f32_16x16x32_bf16 %0, a[144:147], a[108:111], %0" : "+v"(ac[7]) :  : ); SB();
  asm volatile("ds_write_b128 %0, %1 offset:3072" :  : "v"(dd), "v"(ac[3]) : "memory"); SB();
  asm volatile("v_mfma_f32_16x16x32_bf16 %0, a[144:147], a[104:107], %0" : "+v"(ac[6]) :  : ); SB();
  asm volatile("buffer_load_dwordx4 v[196:199], %0, %1, %2 offen offset:0 sc1" :  : "v"(vHr), "s"(rWS), "s"(hso[3]) : "memory"); SB();
  asm volatile("v_mfma_f32_16x16x32_bf16 %0, a[144:147], a[96:99], %0" : "+v"(ac[4]) :  : ); SB();
  asm volatile("v_mfma_f32_16x16x32_bf16 %0, a[140:143], a[104:107], %0" : "+v"(ac[5]) :  : ); SB();
  asm volatile("v_mfma_f32_16x16x32_bf16 %0, a[148:151], a[104:107], %0" : "+v"(ac[7]) :  : ); SB();
  asm volatile("buffer_load_dwordx4 v[200:203], %0, %1, %2 offen offset:0 sc1" :  : "v"(vHr), "s"(rWS), "s"(hso[5]) : "memory"); SB();
  asm volatile("v_mfma_f32_16x16x32_bf16 %0, a[148:151], a[100:103], %0" : "+v"(ac[6]) :  : ); SB();
  asm volatile("v_mfma_f32_16x16x32_bf16 %0, a[144:147], a[100:103], %0" : "+v"(ac[5]) :  : ); SB();
  asm volatile("ds_write_b128 %0, %1 offset:4096" :  : "v"(dd), "v"(ac[4]) : "memory"); SB();
  asm volatile("s_waitcnt lgkmcnt(2)\n\tv_mfma_f32_16x16x32_bf16 %0, a[152:155], a[100:103], %0" : "+v"(ac[7]) :  : ); SB();
  asm volatile("buffer_load_dwordx4 v[204:207], %0, %1, %2 offen offset:0 sc1" :  : "v"(vHr), "s"(rWS), "s"(hso[7]) : "memory"); SB();
  asm volatile("v_mfma_f32_16x16x32_bf16 %0, a[152:155], a[96:99], %0" : "+v"(ac[6]) :  : ); SB();
  asm volatile("v_mfma_f32_16x16x32_bf16 %0, a[148:151], a[96:99], %0" : "+v"(ac[5]) :  : ); SB();
  asm volatile("buffer_load_dwordx4 v[192:195], %0, %1, %2 offen offset:0 sc1" :  : "v"(vHr), "s"(rWS), "s"(hso[1]) : "memory"); SB();
  asm volatile("v_mfma_f32_16x16x32_bf16 %0, a[156:159], a[96:99], %0" : "+v"(ac[7]) :  : ); SB();
  asm volatile("s_nop 5" : "+v"(ac[5]), "+v"(ac[6]), "+v"(ac[7]) :  : ); SB();
  asm volatile("ds_write_b128 %0, %1 offset:6144" :  : "v"(dd), "v"(ac[6]) : "memory"); SB();
  asm volatile("ds_write_b128 %0, %1 offset:5120" :  : "v"(dd), "v"(ac[5]) : "memory"); SB();
  asm volatile("ds_write_b128 %0, %1 offset:7168" :  : "v"(dd), "v"(ac[7]) : "memory"); SB();
  ST(18);
  f32x4 yp[2][4], o3[4][5];
  u32x4 yk[2], yo[2];
  unsigned vtmp;
  asm volatile("ds_read_b128 %0, %1 offset:0" : "=&v"(yk[0]) : "v"(ykr) : "memory"); SB();
  asm volatile("ds_read_b128 %0, %1 offset:12928" : "=&v"(yk[1]) : "v"(ykr) : "memory"); SB();
  asm volatile("s_waitcnt lgkmcnt(0)\n\ts_barrier" :  :  : "memory"); SB();
  asm volatile("ds_read_b128 %0, %1 offset:0" : "=&v"(yp[0][0]) : "v"(ydr) : "memory"); SB();
  asm volatile("ds_read_b128 %0, %1 offset:256" : "=&v"(yp[0][1]) : "v"(ydr) : "memory"); SB();
  asm volatile("ds_read_b128 %0, %1 offset:8192" : "=&v"(yp[0][2]) : "v"(ydr) : "memory"); SB();
  asm volatile("ds_read_b128 %0, %1 offset:8448" : "=&v"(yp[0][3]) : "v"(ydr) : "memory"); SB();
  asm volatile("ds_read_b128 %0, %1 offset:16384" : "=&v"(yp[1][0]) : "v"(ydr) : "memory"); SB();
  asm volatile("ds_read_b128 %0, %1 offset:16640" : "=&v"(yp[1][1]) : "v"(ydr) : "memory"); SB();
  asm volatile("ds_read_b128 %0, %1 offset:24576" : "=&v"(yp[1][2]) : "v"(ydr) : "memory"); SB();
  asm volatile("ds_read_b128 %0, %1 offset:24832" : "=&v"(yp[1][3]) : "v"(ydr) : "memory"); SB();
  asm volatile("buffer_load_dwordx4 v[212:215], %0, %1, %2 offen offset:1024 sc1" :  : "v"(vHr), "s"(rWS), "s"(hso[3]) : "memory"); SB();
  asm volatile("buffer_load_dwordx4 v[216:219], %0, %1, %2 offen offset:1024 sc1" :  : "v"(vHr), "s"(rWS), "s"(hso[5]) : "memory"); SB();
  asm volatile("buffer_load_dwordx4 v[220:223], %0, %1, %2 offen offset:1024 sc1" :  : "v"(vHr), "s"(rWS), "s"(hso[7]) : "memory"); SB();
  asm volatile("" : "+v"(yk[0]), "+v"(yk[1]) :  : ); SB();
  f32x2 yd[2][4];
#pragma unroll
  for (int n = 0; n < 8; ++n) { const float Dv = dD[n >> 2]; yd[n >> 2][n & 3] = unpk(yk[n >> 2][n & 3]) * (f32x2){Dv, Dv}; asm volatile("" : "+v"(yd[n >> 2][n & 3])); }
  SB();
  asm volatile("s_waitcnt lgkmcnt(0)" : "+v"(yp[0][0]), "+v"(yp[0][1]), "+v"(yp[0][2]), "+v"(yp[0][3]) :  : "memory"); SB();
  {
    const f32x4 s0 = yp[0][0] + yp[0][2], s1 = yp[0][1] + yp[0][3];
    yo[0] = (u32x4){pk2((f32x2){s0[0], s0[1]} + yd[0][0]), pk2((f32x2){s0[2], s0[3]} + yd[0][1]), pk2((f32x2){s1[0], s1[1]} + yd[0][2]), ~(nohole(pk2((f32x2){s1[2], s1[3]} + yd[0][3])) ^ yk[0][3])};
    SB();
  }
  asm volatile("buffer_store_dwordx4 %1, %0, %2, %3 offen offset:0 sc1\n\ts_nop 1" :  : "v"(vF), "v"(yo[0]), "s"(rWS), "s"(W_KV + 0) : "memory"); SB();
  asm volatile("" : "+v"(yp[1][0]), "+v"(yp[1][1]), "+v"(yp[1][2]), "+v"(yp[1][3]) :  : "memory"); SB();
  {
    const f32x4 s0 = yp[1][0] + yp[1][2], s1 = yp[1][1] + yp[1][3];
    yo[1] = (u32x4){pk2((f32x2){s0[0], s0[1]} + yd[1][0]), pk2((f32x2){s0[2], s0[3]} + yd[1][1]), pk2((f32x2){s1[0], s1[1]} + yd[1][2]), ~(nohole(pk2((f32x2){s1[2], s1[3]} + yd[1][3])) ^ yk[1][3])};
    SB();
  }
  asm volatile("buffer_store_dwordx4 %1, %0, %2, %3 offen offset:0 sc1\n\ts_nop 1" :  : "v"(vF), "v"(yo[1]), "s"(rWS), "s"(W_KV + 4096) : "memory"); SB();
  ST(19);
  ST(20);
  f32x2 xf[5][2], sv[5][2];
  float rd, mr;
  asm volatile("s_waitcnt vmcnt(13)" :  :  : "memory"); SB();
  asm volatile("v_mfma_f32_16x16x32_bf16 %0, a[160:163], v[164:167], 0" : "=&v"(o3[1][0]) :  : ); SB();
  asm volatile("buffer_load_dwordx4 v[208:211], %0, %1, %2 offen offset:1024 sc1" :  : "v"(vHr), "s"(rWS), "s"(hso[1]) : "memory"); SB();
  asm volatile("v_mfma_f32_16x16x32_bf16 %0, a[164:167], v[164:167], 0" : "=&v"(o3[1][1]) :  : ); SB();
  asm volatile("buffer_load_dwordx4 v[228:231], %0, %1, %2 offen offset:2048 sc1" :  : "v"(vHr), "s"(rWS), "s"(hso[2]) : "memory"); SB();
  asm volatile("v_mfma_f32_16x16x32_bf16 %0, a[168:171], v[164:167], 0" : "=&v"(o3[1][2]) :  : ); SB();
  asm volatile("v_mfma_f32_16x16x32_bf16 %0, a[172:175], v[164:167], 0" : "=&v"(o3[1][3]) :  : ); SB();
  asm volatile("buffer_load_dwordx4 v[232:235], %0, %1, %2 offen offset:2048 sc1" :  : "v"(vHr), "s"(rWS), "s"(hso[4]) : "memory"); SB();
  asm volatile("v_mfma_f32_16x16x32_bf16 %0, a[176:179], v[164:167], 0" : "=&v"(o3[1][4]) :  : ); SB();
  asm volatile("v_mfma_f32_16x16x32_bf16 %0, a[160:163], v[168:171], 0" : "=&v"(o3[2][0]) :  : ); SB();
  asm volatile("v_mfma_f32_16x16x32_bf16 %0, a[164:167], v[168:171], 0" : "=&v"(o3[2][1]) :  : ); SB();
  asm volatile("buffer_load_dwordx4 v[236:239], %0, %1, %2 offen offset:2048 sc1" :  : "v"(vHr), "s"(rWS), "s"(hso[6]) : "memory"); SB();
  asm volatile("v_mfma_f32_16x16x32_bf16 %0, a[168:171], v[168:171], 0" : "=&v"(o3[2][2]) :  : ); SB();
  asm volatile("v_mfma_f32_16x16x32_bf16 %0, a[172:175], v[168:171], 0" : "=&v"(o3[2][3]) :  : ); SB();
  asm volatile("v_mfma_f32_16x16x32_bf16 %0, a[176:179], v[168:171], 0" : "=&v"(o3[2][4]) :  : ); SB();
  asm volatile("v_mfma_f32_16x16x32_bf16 %0, a[160:163], v[172:175], 0" : "=&v"(o3[3][0]) :  : ); SB();
  asm volatile("buffer_load_dwordx4 v[224:227], %0, %1, %2 offen offset:2048 sc1" :  : "v"(vHr), "s"(rWS), "s"(hso[0]) : "memory"); SB();
  asm volatile("v_mfma_f32_16x16x32_bf16 %0, a[164:167], v[172:175], 0" : "=&v"(o3[3][1]) :  : ); SB();
  asm volatile("v_mfma_f32_16x16x32_bf16 %0, a[168:171], v[172:175], 0" : "=&v"(o3[3][2]) :  : ); SB();
  asm volatile("v_mfma_f32_16x16x32_bf16 %0, a[172:175], v[172:175], 0" : "=&v"(o3[3][3]) :  : ); SB();
  asm volatile("buffer_load_dwordx4 v[244:247], %0, %1, %2 offen offset:2048 sc1" :  : "v"(vHr), "s"(rWS), "s"(hso[3]) : "memory"); SB();
  asm volatile("v_mfma_f32_16x16x32_bf16 %0, a[176:179], v[172:175], 0" : "=&v"(o3[3][4]) :  : ); SB();
  asm volatile("s_waitcnt vmcnt(15)" :  :  : "memory"); SB();
  asm volatile("v_mfma_f32_16x16x32_bf16 %0, a[180:183], v[180:183], %0" : "+v"(o3[1][0]) :  : ); SB();
  asm volatile("v_rcp_f32 %0, %1" : "=v"(rd) : "v"(xd[0]) : ); SB();
  asm volatile("v_mfma_f32_16x16x32_bf16 %0, a[184:187], v[180:183], %0" : "+v"(o3[1][1]) :  : ); SB();
  asm volatile("v_mul_f32 %0, -%1, %2" : "=v"(mr) : "v"(xm[0]), "v"(rd) : ); SB();
  asm volatile("v_mfma_f32_16x16x32_bf16 %0, a[188:191], v[180:183], %0" : "+v"(o3[1][2]) :  : ); SB();
  asm volatile("buffer_load_dwordx4 v[248:251], %0, %1, %2 offen offset:2048 sc1" :  : "v"(vHr), "s"(rWS), "s"(hso[5]) : "memory"); SB();
  asm volatile("v_lshlrev_b32 %0, 16, %1" : "=v"(xf[0][0][0]) : "v"(xv[0][0]) : ); SB();
  asm volatile("v_mfma_f32_16x16x32_bf16 %0, a[192:195], v[180:183], %0" : "+v"(o3[1][3]) :  : ); SB();
  asm volatile("v_and_b32 %0, 0xffff0000, %1" : "=v"(xf[0][0][1]) : "v"(xv[0][0]) : ); SB();
  asm volatile("v_mfma_f32_16x16x32_bf16 %0, a[196:199], v[180:183], %0" : "+v"(o3[1][4]) :  : ); SB();
  asm volatile("v_lshlrev_b32 %0, 16, %1" : "=v"(xf[0][1][0]) : "v"(xv[0][1]) : ); SB();
  asm volatile("v_mfma_f32_16x16x32_bf16 %0, a[180:183], v[184:187], %0" : "+v"(o3[2][0]) :  : ); SB();
  asm volatile("buffer_load_dwordx4 v[252:255], %0, %1, %2 offen offset:2048 sc1" :  : "v"(vHr), "s"(rWS), "s"(hso[7]) : "memory"); SB();
  asm volatile("v_and_b32 %0, 0xffff0000, %1" : "=v"(xf[0][1][1]) : "v"(xv[0][1]) : ); SB();
  asm volatile("v_mfma_f32_16x16x32_bf16 %0, a[184:187], v[184:187], %0" : "+v"(o3[2][1]) :  : ); SB();
  asm volatile("v_lshlrev_b32 %0, 16, %1" : "=v"(xf[1][0][0]) : "v"(xv[1][0]) : ); SB();
  asm volatile("v_mfma_f32_16x16x32_bf16 %0, a[188:191], v[184:187], %0" : "+v"(o3[2][2]) :  : ); SB();
  asm volatile("v_and_b32 %0, 0xffff0000, %1" : "=v"(xf[1][0][1]) : "v"(xv[1][0]) : ); SB();
  asm volatile("v_mfma_f32_16x16x32_bf16 %0, a[192:195], v[184:187], %0" : "+v"(o3[2][3]) :  : ); SB();
  asm volatile("v_lshlrev_b32 %0, 16, %1" : "=v"(xf[1][1][0]) : "v"(xv[1][1]) : ); SB();
  asm volatile("v_mfma_f32_16x16x32_bf16 %0, a[196:199], v[184:187], %0" : "+v"(o3[2][4]) :  : ); SB();
  asm volatile("buffer_load_dwordx4 v[240:243], %0, %1, %2 offen offset:2048 sc1" :  : "v"(vHr), "s"(rWS), "s"(hso[1]) : "memory"); SB();
  asm volatile("v_and_b32 %0, 0xffff0000, %1" : "=v"(xf[1][1][1]) : "v"(xv[1][1]) : ); SB();
  asm volatile("v_mfma_f32_16x16x32_bf16 %0, a[180:183], v[188:191], %0" : "+v"(o3[3][0]) :  : ); SB();
  asm volatile("v_lshlrev_b32 %0, 16, %1" : "=v"(xf[2][0][0]) : "v"(xv[2][0]) : ); SB();
  asm volatile("v_mfma_f32_16x16x32_bf16 %0, a[184:187], v[188:191], %0" : "+v"(o3[3][1]) :  : ); SB();
  asm volatile("v_and_b32 %0, 0xffff0000, %1" : "=v"(xf[2][0][1]) : "v"(xv[2][0]) : ); SB();
  asm volatile("v_mfma_f32_16x16x32_bf16 %0, a[188:191], v[188:191], %0" : "+v"(o3[3][2]) :  : ); SB();
  asm volatile("v_lshlrev_b32 %0, 16, %1" : "=v"(xf[2][1][0]) : "v"(xv[2][1]) : ); SB();
  asm volatile("v_mfma_f32_16x16x32_bf16 %0, a[192:195], v[188:191], %0" : "+v"(o3[3][3]) :  : ); SB();
  asm volatile("v_and_b32 %0, 0xffff0000, %1" : "=v"(xf[2][1][1]) : "v"(xv[2][1]) : ); SB();
  asm volatile("v_mfma_f32_16x16x32_bf16 %0, a[196:199], v[188:191], %0" : "+v"(o3[3][4]) :  : ); SB();
  asm volatile("v_lshlrev_b32 %0, 16, %1" : "=v"(xf[3][0][0]) : "v"(xv[3][0]) : ); SB();
  asm volatile("s_waitcnt vmcnt(14)" :  :  : "memory"); SB();
  asm volatile("v_mfma_f32_16x16x32_bf16 %0, a[200:203], v[196:199], %0" : "+v"(o3[1][0]) :  : ); SB();
  asm volatile("v_and_b32 %0, 0xffff0000, %1" : "=v"(xf[3][0][1]) : "v"(xv[3][0]) : ); SB();
  asm volatile("v_mfma_f32_16x16x32_bf16 %0, a[204:207], v[196:199], %0" : "+v"(o3[1][1]) :  : ); SB();
  asm volatile("v_lshlrev_b32 %0, 16, %1" : "=v"(xf[3][1][0]) : "v"(xv[3][1]) : ); SB();
  asm volatile("v_mfma_f32_16x16x32_bf16 %0, a[208:211], v[196:199], %0" : "+v"(o3[1][2]) :  : ); SB();
  asm volatile("v_and_b32 %0, 0xffff0000, %1" : "=v"(xf[3][1][1]) : "v"(xv[3][1]) : ); SB();
  asm volatile("v_mfma_f32_16x16x32_bf16 %0, a[212:215], v[196:199], %0" : "+v"(o3[1][3]) :  : ); SB();
  asm volatile("v_lshlrev_b32 %0, 16, %1" : "=v"(xf[4][0][0]) : "v"(xv[4][0]) : ); SB();
  asm volatile("v_mfma_f32_16x16x32_bf16 %0, a[216:219], v[196:199], %0" : "+v"(o3[1][4]) :  : ); SB();
  asm volatile("v_and_b32 %0, 0xffff0000, %1" : "=v"(xf[4][0][1]) : "v"(xv[4][0]) : ); SB();
  asm volatile("v_mfma_f32_16x16x32_bf16 %0, a[200:203], v[200:203], %0" : "+v"(o3[2][0]) :  : ); SB();
  asm volatile("v_lshlrev_b32 %0, 16, %1" : "=v"(xf[4][1][0]) : "v"(xv[4][1]) : ); SB();
  asm volatile("v_mfma_f32_16x16x32_bf16 %0, a[204:207], v[200:203], %0" : "+v"(o3[2][1]) :  : ); SB();
  asm volatile("v_and_b32 %0, 0xffff0000, %1" : "=v"(xf[4][1][1]) : "v"(xv[4][1]) : ); SB();
  asm volatile("v_mfma_f32_16x16x32_bf16 %0, a[208:211], v[200:203], %0" : "+v"(o3[2][2]) :  : ); SB();
  asm volatile("v_fma_f32 %0, %1, %2, %3" : "=v"(sv[0][0][0]) : "v"(xf[0][0][0]), "v"(rd), "v"(mr) : ); SB();
  asm volatile("v_mfma_f32_16x16x32_bf16 %0, a[212:215], v[200:203], %0" : "+v"(o3[2][3]) :  : ); SB();
  asm volatile("v_fma_f32 %0, %1, %2, %3" : "=v"(sv[0][0][1]) : "v"(xf[0][0][1]), "v"(rd), "v"(mr) : ); SB();
  asm volatile("v_mfma_f32_16x16x32_bf16 %0, a[216:219], v[200:203], %0" : "+v"(o3[2][4]) :  : ); SB();
  asm volatile("v_fma_f32 %0, %1, %2, %3" : "=v"(sv[0][1][0]) : "v"(xf[0][1][0]), "v"(rd), "v"(mr) : ); SB();
  asm volatile("v_mfma_f32_16x16x32_bf16 %0, a[200:203], v[204:207], %0" : "+v"(o3[3][0]) :  : ); SB();
  asm volatile("v_fma_f32 %0, %1, %2, %3" : "=v"(sv[0][1][1]) : "v"(xf[0][1][1]), "v"(rd), "v"(mr) : ); SB();
  asm volatile("v_mfma_f32_16x16x32_bf16 %0, a[204:207], v[204:207], %0" : "+v"(o3[3][1]) :  : ); SB();
  asm volatile("v_fma_f32 %0, %1, %2, %3" : "=v"(sv[1][0][0]) : "v"(xf[1][0][0]), "v"(rd), "v"(mr) : ); SB();
  asm volatile("v_mfma_f32_16x16x32_bf16 %0, a[208:211], v[204:207], %0" : "+v"(o3[3][2]) :  : ); SB();
  asm volatile("v_fma_f32 %0, %1, %2, %3" : "=v"(sv[1][0][1]) : "v"(xf[1][0][1]), "v"(rd), "v"(mr) : ); SB();
  asm volatile("v_mfma_f32_16x16x32_bf16 %0, a[212:215], v[204:207], %0" : "+v"(o3[3][3]) :  : ); SB();
  asm volatile("v_fma_f32 %0, %1, %2, %3" : "=v"(sv[1][1][0]) : "v"(xf[1][1][0]), "v"(rd), "v"(mr) : ); SB();
  asm volatile("v_mfma_f32_16x16x32_bf16 %0, a[216:219], v[204:207], %0" : "+v"(o3[3][4]) :  : ); SB();
  asm volatile("v_fma_f32 %0, %1, %2, %3" : "=v"(sv[1][1][1]) : "v"(xf[1][1][1]), "v"(rd), "v"(mr) : ); SB();
  asm volatile("s_waitcnt vmcnt(8)" :  :  : "memory"); SB();
  asm volatile("v_mfma_f32_16x16x32_bf16 %0, a[220:223], v[212:215], %0" : "+v"(o3[1][0]) :  : ); SB();
  asm volatile("v_fma_f32 %0, %1, %2, %3" : "=v"(sv[2][0][0]) : "v"(xf[2][0][0]), "v"(rd), "v"(mr) : ); SB();
  asm volatile("v_mfma_f32_16x16x32_bf16 %0, a[224:227], v[212:215], %0" : "+v"(o3[1][1]) :  : ); SB();
  asm volatile("v_fma_f32 %0, %1, %2, %3" : "=v"(sv[2][0][1]) : "v"(xf[2][0][1]), "v"(rd), "v"(mr) : ); SB();
  asm volatile("v_mfma_f32_16x16x32_bf16 %0, a[228:231], v[212:215], %0" : "+v"(o3[1][2]) :  : ); SB();
  asm volatile("v_fma_f32 %0, %1, %2, %3" : "=v"(sv[2][1][0]) : "v"(xf[2][1][0]), "v"(rd), "v"(mr) : ); SB();
  asm volatile("v_mfma_f32_16x16x32_bf16 %0, a[232:235], v[212:215], %0" : "+v"(o3[1][3]) :  : ); SB();
  asm volatile("v_fma_f32 %0, %1, %2, %3" : "=v"(sv[2][1][1]) : "v"(xf[2][1][1]), "v"(rd), "v"(mr) : ); SB();
  asm volatile("v_mfma_f32_16x16x32_bf16 %0, a[236:239], v[212:215], %0" : "+v"(o3[1][4]) :  : ); SB();
  asm volatile("v_fma_f32 %0, %1, %2, %3" : "=v"(sv[3][0][0]) : "v"(xf[3][0][0]), "v"(rd), "v"(mr) : ); SB();
  asm volatile("v_mfma_f32_16x16x32_bf16 %0, a[220:223], v[216:219], %0" : "+v"(o3[2][0]) :  : ); SB();
  asm volatile("v_fma_f32 %0, %1, %2, %3" : "=v"(sv[3][0][1]) : "v"(xf[3][0][1]), "v"(rd), "v"(mr) : ); SB();
  asm volatile("v_mfma_f32_16x16x32_bf16 %0, a[224:227], v[216:219], %0" : "+v"(o3[2][1]) :  : ); SB();
  asm volatile("v_fma_f32 %0, %1, %2, %3" : "=v"(sv[3][1][0]) : "v"(xf[3][1][0]), "v"(rd), "v"(mr) : ); SB();
  asm volatile("v_mfma_f32_16x16x32_bf16 %0, a[228:231], v[216:219], %0" : "+v"(o3[2][2]) :  : ); SB();
  asm volatile("v_fma_f32 %0, %1, %2, %3" : "=v"(sv[3][1][1]) : "v"(xf[3][1][1]), "v"(rd), "v"(mr) : ); SB();
  asm volatile("v_mfma_f32_16x16x32_bf16 %0, a[232:235], v[216:219], %0" : "+v"(o3[2][3]) :  : ); SB();
  asm volatile("v_fma_f32 %0, %1, %2, %3" : "=v"(sv[4][0][0]) : "v"(xf[4][0][0]), "v"(rd), "v"(mr) : ); SB();
  asm volatile("v_mfma_f32_16x16x32_bf16 %0, a[236:239], v[216:219], %0" : "+v"(o3[2][4]) :  : ); SB();
  asm volatile("v_fma_f32 %0, %1, %2, %3" : "=v"(sv[4][0][1]) : "v"(xf[4][0][1]), "v"(rd), "v"(mr) : ); SB();
  asm volatile("v_mfma_f32_16x16x32_bf16 %0, a[220:223], v[220:223], %0" : "+v"(o3[3][0]) :  : ); SB();
  asm volatile("v_fma_f32 %0, %1, %2, %3" : "=v"(sv[4][1][0]) : "v"(xf[4][1][0]), "v"(rd), "v"(mr) : ); SB();
  asm volatile("v_mfma_f32_16x16x32_bf16 %0, a[224:227], v[220:223], %0" : "+v"(o3[3][1]) :  : ); SB();
  asm volatile("v_fma_f32 %0, %1, %2, %3" : "=v"(sv[4][1][1]) : "v"(xf[4][1][1]), "v"(rd), "v"(mr) : ); SB();
  asm volatile("v_mfma_f32_16x16x32_bf16 %0, a[228:231], v[220:223], %0" : "+v"(o3[3][2]) :  : ); SB();
  asm volatile("v_mfma_f32_16x16x32_bf16 %0, a[232:235], v[220:223], %0" : "+v"(o3[3][3]) :  : ); SB();
  asm volatile("v_mfma_f32_16x16x32_bf16 %0, a[236:239], v[220:223], %0" : "+v"(o3[3][4]) :  : ); SB();
  ST(30);
  asm volatile("s_waitcnt vmcnt(0)\n\t.Lc_%=:\n\tv_max3_u32 %1, v231, v235, v239\n\tv_max3_u32 %1, %1, v227, v247\n\tv_max3_u32 %1, %1, v251, v255\n\tv_max3_u32 %1, %1, v243, v243\n\tv_cmp_eq_u32 vcc, -1, %1\n\ts_cbranch_vccnz .Lr_%=\n\t.Ld_%=:\n\t.subsection 1\n\t.Lr_%=:\n\tv_add_u32 %0, -1, %0\n\ts_nop 1\n\tv_readfirstlane_b32 vcc_hi, %0\n\ts_nop 3\n\ts_cmp_lt_i32 vcc_hi, 1\n\ts_cbranch_scc1 .Ld_%=\n\ts_sleep 2\n\tbuffer_load_dwordx4 v[228:231], %2, %3, %6 offen offset:2048 sc1\n\tbuffer_load_dwordx4 v[232:235], %2, %3, %8 offen offset:2048 sc1\n\tbuffer_load_dwordx4 v[236:239], %2, %3, %10 offen offset:2048 sc1\n\tbuffer_load_dwordx4 v[224:227], %2, %3, %4 offen offset:2048 sc1\n\tbuffer_load_dwordx4 v[244:247], %2, %3, %7 offen offset:2048 sc1\n\tbuffer_load_dwordx4 v[248:251], %2, %3, %9 offen offset:2048 sc1\n\tbuffer_load_dwordx4 v[252:255], %2, %3, %11 offen offset:2048 sc1\n\tbuffer_load_dwordx4 v[240:243], %2, %3, %5 offen offset:2048 sc1\n\ts_waitcnt vmcnt(0)\n\ts_branch .Lc_%=\n\t.subsection 0" : "+v"(tmo), "=&v"(vtmp) : "v"(vHr), "s"(rWS), "s"(hso[0]), "s"(hso[1]), "s"(hso[2]), "s"(hso[3]), "s"(hso[4]), "s"(hso[5]), "s"(hso[6]), "s"(hso[7]) : "memory", "vcc", "scc"); SB();
  asm volatile("v_mfma_f32_16x16x32_bf16 %0, a[240:243], v[228:231], %0" : "+v"(o3[1][0]) :  : ); SB();
  asm volatile("v_mfma_f32_16x16x32_bf16 %0, a[244:247], v[228:231], %0" : "+v"(o3[1][1]) :  : ); SB();
  asm volatile("v_mfma_f32_16x16x32_bf16 %0, a[248:251], v[228:231], %0" : "+v"(o3[1][2]) :  : ); SB();
  asm volatile("v_mfma_f32_16x16x32_bf16 %0, a[252:255], v[228:231], %0" : "+v"(o3[1][3]) :  : ); SB();
  asm volatile("v_mfma_f32_16x16x32_bf16 %0, a[0:3], v[228:231], %0" : "+v"(o3[1][4]) :  : ); SB();
  asm volatile("v_mfma_f32_16x16x32_bf16 %0, a[4:7], v[244:247], %0" : "+v"(o3[1][0]) :  : ); SB();
  asm volatile("v_mfma_f32_16x16x32_bf16 %0, a[8:11], v[244:247], %0" : "+v"(o3[1][1]) :  : ); SB();
  asm volatile("v_mfma_f32_16x16x32_bf16 %0, a[12:15], v[244:247], %0" : "+v"(o3[1][2]) :  : ); SB();
  asm volatile("v_mfma_f32_16x16x32_bf16 %0, a[16:19], v[244:247], %0" : "+v"(o3[1][3]) :  : ); SB();
  asm volatile("v_mfma_f32_16x16x32_bf16 %0, a[20:23], v[244:247], %0" : "+v"(o3[1][4]) :  : ); SB();
  asm volatile("v_mfma_f32_16x16x32_bf16 %0, a[240:243], v[232:235], %0" : "+v"(o3[2][0]) :  : ); SB();
  asm volatile("ds_write_b128 %0, %1 offset:0" :  : "v"(pw3), "v"(o3[1][0]) : "memory"); SB();
  asm volatile("v_mfma_f32_16x16x32_bf16 %0, a[244:247], v[232:235], %0" : "+v"(o3[2][1]) :  : ); SB();
  asm volatile("v_mfma_f32_16x16x32_bf16 %0, a[248:251], v[232:235], %0" : "+v"(o3[2][2]) :  : ); SB();
  asm volatile("v_mfma_f32_16x16x32_bf16 %0, a[252:255], v[232:235], %0" : "+v"(o3[2][3]) :  : ); SB();
  asm volatile("ds_write_b128 %0, %1 offset:1024" :  : "v"(pw3), "v"(o3[1][1]) : "memory"); SB();
  asm volatile("v_mfma_f32_16x16x32_bf16 %0, a[0:3], v[232:235], %0" : "+v"(o3[2][4]) :  : ); SB();
  asm volatile("v_mfma_f32_16x16x32_bf16 %0, a[4:7], v[248:251], %0" : "+v"(o3[2][0]) :  : ); SB();
  asm volatile("buffer_load_dwordx4 v[164:167], %0, %1, %2 offen offset:0 sc1" :  : "v"(vKV), "s"(rWS), "s"(0u) : "memory"); SB();
  asm volatile("v_mfma_f32_16x16x32_bf16 %0, a[8:11], v[248:251], %0" : "+v"(o3[2][1]) :  : ); SB();
  asm volatile("buffer_load_dwordx4 v[168:171], %0, %1, %2 offen offset:0 sc1" :  : "v"(vKV), "s"(rWS), "s"(w3k[0]) : "memory"); SB();
  asm volatile("v_mfma_f32_16x16x32_bf16 %0, a[12:15], v[248:251], %0" : "+v"(o3[2][2]) :  : ); SB();
  asm volatile("buffer_load_dwordx4 v[172:175], %0, %1, %2 offen offset:0 sc1" :  : "v"(vKV), "s"(rWS), "s"(w3k[1]) : "memory"); SB();
  asm volatile("v_mfma_f32_16x16x32_bf16 %0, a[16:19], v[248:251], %0" : "+v"(o3[2][3]) :  : ); SB();
  asm volatile("ds_write_b128 %0, %1 offset:2048" :  : "v"(pw3), "v"(o3[1][2]) : "memory"); SB();
  asm volatile("v_mfma_f32_16x16x32_bf16 %0, a[20:23], v[248:251], %0" : "+v"(o3[2][4]) :  : ); SB();
  asm volatile("v_mfma_f32_16x16x32_bf16 %0, a[240:243], v[236:239], %0" : "+v"(o3[3][0]) :  : ); SB();
  asm volatile("v_mfma_f32_16x16x32_bf16 %0, a[244:247], v[236:239], %0" : "+v"(o3[3][1]) :  : ); SB();
  asm volatile("ds_write_b128 %0, %1 offset:3072" :  : "v"(pw3), "v"(o3[1][3]) : "memory"); SB();
  asm volatile("v_mfma_f32_16x16x32_bf16 %0, a[248:251], v[236:239], %0" : "+v"(o3[3][2]) :  : ); SB();
  asm volatile("v_mfma_f32_16x16x32_bf16 %0, a[252:255], v[236:239], %0" : "+v"(o3[3][3]) :  : ); SB();
  asm volatile("v_mfma_f32_16x16x32_bf16 %0, a[0:3], v[236:239], %0" : "+v"(o3[3][4]) :  : ); SB();
  asm volatile("ds_write_b128 %0, %1 offset:4096" :  : "v"(pw3), "v"(o3[1][4]) : "memory"); SB();
  asm volatile("v_mfma_f32_16x16x32_bf16 %0, a[4:7], v[252:255], %0" : "+v"(o3[3][0]) :  : ); SB();
  asm volatile("v_mfma_f32_16x16x32_bf16 %0, a[8:11], v[252:255], %0" : "+v"(o3[3][1]) :  : ); SB();
  asm volatile("v_mfma_f32_16x16x32_bf16 %0, a[12:15], v[252:255], %0" : "+v"(o3[3][2]) :  : ); SB();
  asm volatile("ds_write_b128 %0, %1 offset:5120" :  : "v"(pw3), "v"(o3[2][0]) : "memory"); SB();
  asm volatile("v_mfma_f32_16x16x32_bf16 %0, a[16:19], v[252:255], %0" : "+v"(o3[3][3]) :  : ); SB();
  asm volatile("v_mfma_f32_16x16x32_bf16 %0, a[20:23], v[252:255], %0" : "+v"(o3[3][4]) :  : ); SB();
  asm volatile("v_mfma_f32_16x16x32_bf16 %0, a[160:163], v[160:163], 0" : "=&v"(o3[0][0]) :  : ); SB();
  asm volatile("ds_write_b128 %0, %1 offset:6144" :  : "v"(pw3), "v"(o3[2][1]) : "memory"); SB();
  asm volatile("v_mfma_f32_16x16x32_bf16 %0, a[164:167], v[160:163], 0" : "=&v"(o3[0][1]) :  : ); SB();
  asm volatile("v_mfma_f32_16x16x32_bf16 %0, a[168:171], v[160:163], 0" : "=&v"(o3[0][2]) :  : ); SB();
  asm volatile("v_mfma_f32_16x16x32_bf16 %0, a[172:175], v[160:163], 0" : "=&v"(o3[0][3]) :  : ); SB();
  asm volatile("ds_write_b128 %0, %1 offset:7168" :  : "v"(pw3), "v"(o3[2][2]) : "memory"); SB();
  asm volatile("v_mfma_f32_16x16x32_bf16 %0, a[176:179], v[160:163], 0" : "=&v"(o3[0][4]) :  : ); SB();
  asm volatile("v_mfma_f32_16x16x32_bf16 %0, a[180:183], v[176:179], %0" : "+v"(o3[0][0]) :  : ); SB();
  asm volatile("v_mfma_f32_16x16x32_bf16 %0, a[184:187], v[176:179], %0" : "+v"(o3[0][1]) :  : ); SB();
  asm volatile("ds_write_b128 %0, %1 offset:8192" :  : "v"(pw3), "v"(o3[2][3]) : "memory"); SB();
  asm volatile("v_mfma_f32_16x16x32_bf16 %0, a[188:191], v[176:179], %0" : "+v"(o3[0][2]) :  : ); SB();
  asm volatile("v_mfma_f32_16x16x32_bf16 %0, a[192:195], v[176:179], %0" : "+v"(o3[0][3]) :  : ); SB();
  asm volatile("v_mfma_f32_16x16x32_bf16 %0, a[196:199], v[176:179], %0" : "+v"(o3[0][4]) :  : ); SB();
  asm volatile("ds_write_b128 %0, %1 offset:9216" :  : "v"(pw3), "v"(o3[2][4]) : "memory"); SB();
  asm volatile("v_mfma_f32_16x16x32_bf16 %0, a[200:203], v[192:195], %0" : "+v"(o3[0][0]) :  : ); SB();
  asm volatile("v_mfma_f32_16x16x32_bf16 %0, a[204:207], v[192:195], %0" : "+v"(o3[0][1]) :  : ); SB();
  asm volatile("v_mfma_f32_16x16x32_bf16 %0, a[208:211], v[192:195], %0" : "+v"(o3[0][2]) :  : ); SB();
  asm volatile("ds_write_b128 %0, %1 offset:10240" :  : "v"(pw3), "v"(o3[3][0]) : "memory"); SB();
  asm volatile("v_mfma_f32_16x16x32_bf16 %0, a[212:215], v[192:195], %0" : "+v"(o3[0][3]) :  : ); SB();
  asm volatile("v_mfma_f32_16x16x32_bf16 %0, a[216:219], v[192:195], %0" : "+v"(o3[0][4]) :  : ); SB();
  asm volatile("v_mfma_f32_16x16x32_bf16 %0, a[220:223], v[208:211], %0" : "+v"(o3[0][0]) :  : ); SB();
  asm volatile("ds_write_b128 %0, %1 offset:11264" :  : "v"(pw3), "v"(o3[3][1]) : "memory"); SB();
  asm volatile("v_mfma_f32_16x16x32_bf16 %0, a[224:227], v[208:211], %0" : "+v"(o3[0][1]) :  : ); SB();
  asm volatile("v_mfma_f32_16x16x32_bf16 %0, a[228:231], v[208:211], %0" : "+v"(o3[0][2]) :  : ); SB();
  asm volatile("v_mfma_f32_16x16x32_bf16 %0, a[232:235], v[208:211], %0" : "+v"(o3[0][3]) :  : ); SB();
  asm volatile("ds_write_b128 %0, %1 offset:12288" :  : "v"(pw3), "v"(o3[3][2]) : "memory"); SB();
  asm volatile("v_mfma_f32_16x16x32_bf16 %0, a[236:239], v[208:211], %0" : "+v"(o3[0][4]) :  : ); SB();
  asm volatile("v_mfma_f32_16x16x32_bf16 %0, a[240:243], v[224:227], %0" : "+v"(o3[0][0]) :  : ); SB();
  asm volatile("v_mfma_f32_16x16x32_bf16 %0, a[244:247], v[224:227], %0" : "+v"(o3[0][1]) :  : ); SB();
  asm volatile("ds_write_b128 %0, %1 offset:13312" :  : "v"(pw3), "v"(o3[3][3]) : "memory"); SB();
  asm volatile("v_mfma_f32_16x16x32_bf16 %0, a[248:251], v[224:227], %0" : "+v"(o3[0][2]) :  : ); SB();
  asm volatile("v_mfma_f32_16x16x32_bf16 %0, a[252:255], v[224:227], %0" : "+v"(o3[0][3]) :  : ); SB();
  asm volatile("v_mfma_f32_16x16x32_bf16 %0, a[0:3], v[224:227], %0" : "+v"(o3[0][4]) :  : ); SB();
  asm volatile("ds_write_b128 %0, %1 offset:14336" :  : "v"(pw3), "v"(o3[3][4]) : "memory"); SB();
  asm volatile("v_mfma_f32_16x16x32_bf16 %0, a[4:7], v[240:243], %0" : "+v"(o3[0][0]) :  : ); SB();
  asm volatile("v_mfma_f32_16x16x32_bf16 %0, a[8:11], v[240:243], %0" : "+v"(o3[0][1]) :  : ); SB();
  asm volatile("v_mfma_f32_16x16x32_bf16 %0, a[12:15], v[240:243], %0" : "+v"(o3[0][2]) :  : ); SB();
  asm volatile("v_mfma_f32_16x16x32_bf16 %0, a[16:19], v[240:243], %0" : "+v"(o3[0][3]) :  : ); SB();
  asm volatile("v_mfma_f32_16x16x32_bf16 %0, a[20:23], v[240:243], %0" : "+v"(o3[0][4]) :  : ); SB();
  asm volatile("s_waitcnt lgkmcnt(0)\n\ts_barrier" :  :  : "memory"); SB();
  asm volatile("ds_read_b128 %0, %1 offset:0" : "=&v"(o3[1][0]) : "v"(p3[0]) : "memory"); SB();
  asm volatile("ds_read_b128 %0, %1 offset:5120" : "=&v"(o3[2][0]) : "v"(p3[1]) : "memory"); SB();
  asm volatile("ds_read_b128 %0, %1 offset:10240" : "=&v"(o3[3][0]) : "v"(p3[2]) : "memory"); SB();
  asm volatile("ds_read_b128 %0, %1 offset:1024" : "=&v"(o3[1][1]) : "v"(p3[0]) : "memory"); SB();
  asm volatile("ds_read_b128 %0, %1 offset:6144" : "=&v"(o3[2][1]) : "v"(p3[1]) : "memory"); SB();
  asm volatile("ds_read_b128 %0, %1 offset:11264" : "=&v"(o3[3][1]) : "v"(p3[2]) : "memory"); SB();
  asm volatile("ds_read_b128 %0, %1 offset:2048" : "=&v"(o3[1][2]) : "v"(p3[0]) : "memory"); SB();
  asm volatile("ds_read_b128 %0, %1 offset:7168" : "=&v"(o3[2][2]) : "v"(p3[1]) : "memory"); SB();
  asm volatile("ds_read_b128 %0, %1 offset:12288" : "=&v"(o3[3][2]) : "v"(p3[2]) : "memory"); SB();
  asm volatile("ds_read_b128 %0, %1 offset:3072" : "=&v"(o3[1][3]) : "v"(p3[0]) : "memory"); SB();
  asm volatile("ds_read_b128 %0, %1 offset:8192" : "=&v"(o3[2][3]) : "v"(p3[1]) : "memory"); SB();
  asm volatile("ds_read_b128 %0, %1 offset:13312" : "=&v"(o3[3][3]) : "v"(p3[2]) : "memory"); SB();
  asm volatile("ds_read_b128 %0, %1 offset:4096" : "=&v"(o3[1][4]) : "v"(p3[0]) : "memory"); SB();
  asm volatile("ds_read_b128 %0, %1 offset:9216" : "=&v"(o3[2][4]) : "v"(p3[1]) : "memory"); SB();
  asm volatile("ds_read_b128 %0, %1 offset:14336" : "=&v"(o3[3][4]) : "v"(p3[2]) : "memory"); SB();
  ST(21);
  u32x2 yr[5];
  asm volatile("s_waitcnt lgkmcnt(9)" : "+v"(o3[0][0]), "+v"(o3[0][1]), "+v"(o3[1][0]), "+v"(o3[1][1]), "+v"(o3[2][0]), "+v"(o3[2][1]), "+v"(o3[3][0]), "+v"(o3[3][1]) :  : "memory"); SB();
#pragma unroll
  for (int j = 0; j <= 1; ++j) {
    const f32x4 o4 = (o3[0][j] + o3[1][j]) + (o3[2][j] + o3[3][j]);
#pragma unroll
    for (int h = 0; h < 2; ++h) {
      const f32x2 o = {o4[2 * h], o4[2 * h + 1]};
      const f32x2 a = o * (f32x2){-1.4426950f, -1.4426950f};
      const f32x2 e = (f32x2){__builtin_amdgcn_exp2f(a[0]), __builtin_amdgcn_exp2f(a[1])} + (f32x2){1.0f, 1.0f};
      const f32x2 r = {__builtin_amdgcn_rcpf(e[0]), __builtin_amdgcn_rcpf(e[1])};
      sv[j][h] = (sv[j][h] * o) * r;
    }
  }
  asm volatile("" : "+v"(sv[0][0]), "+v"(sv[0][1]), "+v"(sv[1][0]), "+v"(sv[1][1]));
  SB();
  asm volatile("s_waitcnt lgkmcnt(0)" : "+v"(o3[0][2]), "+v"(o3[0][3]), "+v"(o3[1][2]), "+v"(o3[1][3]), "+v"(o3[2][2]), "+v"(o3[2][3]), "+v"(o3[3][2]), "+v"(o3[3][3]) :  : "memory"); SB();
#pragma unroll
  for (int j = 2; j <= 3; ++j) {
    const f32x4 o4 = (o3[0][j] + o3[1][j]) + (o3[2][j] + o3[3][j]);
#pragma unroll
    for (int h = 0; h < 2; ++h) {
      const f32x2 o = {o4[2 * h], o4[2 * h + 1]};
      const f32x2 a = o * (f32x2){-1.4426950f, -1.4426950f};
      const f32x2 e = (f32x2){__builtin_amdgcn_exp2f(a[0]), __builtin_amdgcn_exp2f(a[1])} + (f32x2){1.0f, 1.0f};
      const f32x2 r = {__builtin_amdgcn_rcpf(e[0]), __builtin_amdgcn_rcpf(e[1])};
      sv[j][h] = (sv[j][h] * o) * r;
    }
  }
  asm volatile("" : "+v"(sv[2][0]), "+v"(sv[2][1]), "+v"(sv[3][0]), "+v"(sv[3][1]));
  SB();
  ST(22);
  asm volatile("s_waitcnt vmcnt(0)\n\t.Lc_%=:\n\tv_xnor_b32 v167, v167, %6\n\tv_xnor_b32 v171, v171, %7\n\tv_xnor_b32 v175, v175, %8\n\tv_max3_u32 %1, v167, v171, v175\n\tv_cmp_eq_u32 vcc, -1, %1\n\ts_cbranch_vccnz .Lr_%=\n\t.Ld_%=:\n\t.subsection 1\n\t.Lr_%=:\n\tv_add_u32 %0, -1, %0\n\ts_nop 1\n\tv_readfirstlane_b32 vcc_hi, %0\n\ts_nop 3\n\ts_cmp_lt_i32 vcc_hi, 1\n\ts_cbranch_scc1 .Ld_%=\n\ts_sleep 2\n\tbuffer_load_dwordx4 v[164:167], %2, %3, 0 offen sc1\n\tbuffer_load_dwordx4 v[168:171], %2, %3, %4 offen sc1\n\tbuffer_load_dwordx4 v[172:175], %2, %3, %5 offen sc1\n\ts_waitcnt vmcnt(0)\n\ts_branch .Lc_%=\n\t.subsection 0" : "+v"(tmo), "=&v"(vtmp) : "v"(vKV), "s"(rWS), "s"(w3k[0]), "s"(w3k[1]), "v"(kz[0]), "v"(kz[1]), "v"(kz[2]) : "memory", "vcc", "scc"); SB();
  ST(31);
  asm volatile("ds_write_b128 %0, v[164:167] offset:0" :  : "v"(yw) : "memory"); SB();
  asm volatile("ds_write_b128 %0, v[168:171] offset:1024" :  : "v"(yw) : "memory"); SB();
  asm volatile("ds_write_b128 %0, v[172:175] offset:2048" :  : "v"(yw) : "memory"); SB();
  asm volatile("s_waitcnt lgkmcnt(0)\n\ts_barrier" :  :  : "memory"); SB();
  asm volatile("ds_read_b64_tr_b16 %0, %1 offset:0" : "=&v"(yr[0]) : "v"(ya) : "memory"); SB();
  asm volatile("ds_read_b64_tr_b16 %0, %1 offset:2048" : "=&v"(yr[1]) : "v"(ya) : "memory"); SB();
  asm volatile("ds_read_b64_tr_b16 %0, %1 offset:4096" : "=&v"(yr[2]) : "v"(ya) : "memory"); SB();
  asm volatile("ds_read_b64_tr_b16 %0, %1 offset:6144" : "=&v"(yr[3]) : "v"(ya) : "memory"); SB();
  asm volatile("ds_read_b64_tr_b16 %0, %1 offset:8192" : "=&v"(yr[4]) : "v"(ya) : "memory"); SB();
  asm volatile("" : "+v"(o3[0][4]), "+v"(o3[1][4]), "+v"(o3[2][4]), "+v"(o3[3][4]) :  : "memory"); SB();
#pragma unroll
  for (int j = 4; j <= 4; ++j) {
    const f32x4 o4 = (o3[0][j] + o3[1][j]) + (o3[2][j] + o3[3][j]);
#pragma unroll
    for (int h = 0; h < 2; ++h) {
      const f32x2 o = {o4[2 * h], o4[2 * h + 1]};
      const f32x2 a = o * (f32x2){-1.4426950f, -1.4426950f};
      const f32x2 e = (f32x2){__builtin_amdgcn_exp2f(a[0]), __builtin_amdgcn_exp2f(a[1])} + (f32x2){1.0f, 1.0f};
      const f32x2 r = {__builtin_amdgcn_rcpf(e[0]), __builtin_amdgcn_rcpf(e[1])};
      sv[j][h] = (sv[j][h] * o) * r;
    }
  }
  asm volatile("" : "+v"(sv[4][0]), "+v"(sv[4][1]));
  SB();
  asm volatile("s_waitcnt lgkmcnt(0)" : "+v"(yr[0]), "+v"(yr[1]), "+v"(yr[2]), "+v"(yr[3]), "+v"(yr[4]) :  : "memory"); SB();
  {
    unsigned pk[5][2];
#pragma unroll
    for (int n = 0; n < 10; ++n) pk[n >> 1][n & 1] = pk2(__builtin_elementwise_fma(sv[n >> 1][n & 1], unpk(yr[n >> 1][n & 1]), xf[n >> 1][n & 1]));
#pragma unroll
    for (int q = 0; q < 2; ++q) {
      const auto s0 = __builtin_amdgcn_permlane16_swap(pk[2 * q][0], pk[2 * q + 1][0], false, false);
      const auto s1 = __builtin_amdgcn_permlane16_swap(pk[2 * q][1], pk[2 * q + 1][1], false, false);
      asm volatile("s_nop 1\n\tglobal_store_dwordx4 %0, %1, %2 offset:%3 " :: "v"(po), "v"((u32x4){s0[0], s1[0], s0[1], s1[1]}), "s"(out), "i"(64 * q) : "memory");
    }
    asm volatile("global_store_dwordx2 %0, %1, %2 offset:128 " :: "v"(vX), "v"((u32x2){pk[4][0], pk[4][1]}), "s"(out) : "memory");
  }
  ST(23);
  ST_OUT();
  }}}
}

extern "C" __global__ void k_end() { asm volatile(".subsection 2\n.Lcode_end:\n\ts_endpgm\n\t.subsection 0"); }

extern "C" void kernel_launch(void* const* d_in, const int* in_sizes, int n_in,
                              void* d_out, int out_size, void* d_ws, size_t ws_size,
                              hipStream_t stream) {
  (void)hipFuncSetAttribute((const void*)k_all, hipFuncAttributeMaxDynamicSharedMemorySize, 163840);
  k_all<<<256, 256, 163840, stream>>>((const char*)d_in[0], (const char*)d_in[1], (const char*)d_in[2], (const char*)d_in[3], (const char*)d_in[4],
                                      (const char*)d_in[5], (const char*)d_in[7], (const char*)d_in[8], (const char*)d_in[9], (const char*)d_in[10],
                                      (char*)d_out, (char*)d_ws);
}
```

```cpp
#include <hip/hip_runtime.h>
#include <stdint.h>

typedef unsigned short u16;
typedef __attribute__((ext_vector_type(4))) float f32x4;
typedef __attribute__((ext_vector_type(2))) float f32x2;
typedef __attribute__((ext_vector_type(4))) unsigned int u32x4;
typedef __attribute__((ext_vector_type(2))) unsigned int u32x2;
typedef __attribute__((ext_vector_type(2))) __bf16 bf16x2n;
typedef __attribute__((ext_vector_type(8))) unsigned long long u64x8;
typedef __attribute__((ext_vector_type(4))) unsigned long long u64x4;
#define LDS_AS __attribute__((address_space(3)))
#define GAS __attribute__((address_space(1)))
#define SB() __builtin_amdgcn_sched_barrier(0)
#define DI __device__ __forceinline__

#define SGA
#define ST_DECL do {} while (0)
#define ST(i) do {} while (0)
#define ST_OUT() do {} while (0)

constexpr int S_ = 2048, D_ = 640;
constexpr unsigned HOLE = 0xFFFFFFFFu;
constexpr int W_ST = 4096, W_FZ = 768 << 10, W_FK = W_FZ + 8192, W_FH = W_FK + 8192, W_KV = 1 << 20, W_Z2 = 4 << 20, W_HH = 11 << 20, W_WO = 14 << 20;

#define wm (w >> 1)
#define wn (w & 1)
#define g4 (lane >> 4)
#define l15 (lane & 15)
#define q4 ((lane >> 2) & 3)
#define p4 (lane & 3)
#define l16 (lane * 16)
#define nch (3 - (rb >> 4))
#define ch0 (cb * 80 + rb * 2 + min(rb, 16u))
#define c2o ((nch - 2) * 2)
#define c2s (8192u + ((rb >> 4) << 30))
#define w3s ((w + 1) >> 2 << 30)
#define vZ (W_Z2 + ((rb * 2 + wm) * 64 + cb * 6 + wn * 3) * 1024 + l16)
#define vH (W_HH + ((rb * 4 + w) * 24 + cb * 3) * 1024 + l16)

DI unsigned pack2(float lo, float hi) {
  f32x2 v = {lo, hi};
  return __builtin_bit_cast(unsigned, __builtin_convertvector(v, bf16x2n));
}
DI float lo16(unsigned w) { return __uint_as_float(w << 16); }
DI float hi16(unsigned w) { return __uint_as_float(w & 0xFFFF0000u); }
DI float rbf(float f) { return lo16(pack2(f, f)); }
DI f32x2 unpk(unsigned w) { f32x2 r = {lo16(w), hi16(w)}; return r; }
DI unsigned pk2(f32x2 v) { return pack2(v[0], v[1]); }
DI unsigned nohole(unsigned a) { return a < HOLE - 1 ? a : HOLE - 1; }
template <int CTRL> DI float dppf(float v) {
  return __builtin_bit_cast(float, __builtin_amdgcn_update_dpp(0, __builtin_bit_cast(int, v), CTRL, 0xF, 0xF, true));
}
DI float red16(float v) {
  const auto a = __builtin_amdgcn_permlane16_swap(__float_as_uint(v), __float_as_uint(v), false, false);
  return __uint_as_float(a[0]) + __uint_as_float(a[1]);
}
DI float red32(float v) {
  const auto c = __builtin_amdgcn_permlane32_swap(__float_as_uint(v), __float_as_uint(v), false, false);
  return __uint_as_float(c[0]) + __uint_as_float(c[1]);
}
DI float sel32(float a, float b) {
  const auto c = __builtin_amdgcn_permlane32_swap(__float_as_uint(a), __float_as_uint(b), false, false);
  return __uint_as_float(c[0]) + __uint_as_float(c[1]);
}
DI unsigned m10(unsigned a) { return a >= 10 ? a - 10 : a; }
DI unsigned lds_addr(const void* p) { return (unsigned)(uintptr_t)(LDS_AS const char*)p; }

__global__ __launch_bounds__(256, 1) __attribute__((amdgpu_num_vgpr(160))) SGA void k_all(
    const char*, const char*, const char*, const char*, const char*, const char*, const char*, const char*, const char*, const char*, char*, char*) {
  extern __shared__ __attribute__((aligned(16))) char smem[];
  ST_DECL;
  ST(0);
  u64x8 ka0; u64x4 ka1;
  asm volatile("s_load_dwordx16 %0, %2, 0x0\n\ts_load_dwordx8 %1, %2, 0x40" : "=&s"(ka0), "=&s"(ka1) : "s"(__builtin_amdgcn_kernarg_segment_ptr()) : "memory"); SB();
  const unsigned tid = threadIdx.x, lane0 = tid & 63, w0 = __builtin_amdgcn_readfirstlane(tid >> 6);
  const unsigned b = blockIdx.x, xcd = b & 7, idx = b >> 3;
  const unsigned rb0 = 8 * (xcd >> 1) + (idx >> 2), cb0 = 4 * (xcd & 1) + (idx & 3);
  const unsigned lb = lds_addr(smem);
  const unsigned lane = lane0, w = w0, rb = rb0, cb = cb0;
  {
    unsigned long long pc; unsigned clen;
    asm volatile("s_getpc_b64 %0\n.Lpc_here_%=:\n\ts_mov_b32 %1, .Lcode_end-.Lpc_here_%=" : "=s"(pc), "=s"(clen));
    const unsigned o = min((((lane & 3) * 4 + w) * 32 + idx) * 128, clen - 4u) & ~3u;
    asm volatile("s_mov_b64 exec, 15\n\tglobal_load_dword v254, %0, %1\n\ts_mov_b64 exec, -1" :: "v"(o), "s"(pc) : "memory");
    SB();
  }
  unsigned voA = (rb * 64 + w * 16) * 1280 + __umul24(lane >> 3, 1280) + (((lane & 7) ^ (lane >> 3)) << 4), voB[3];
#pragma unroll
  for (int i = 0; i < 3; ++i) { const unsigned r = __umul24(i * 64 + lane, 171) >> 11; voB[i] = w * 49152 + cb * 192 + __umul24(r, 2880) + ((lane ^ ((r >> 3) << 1)) << 4); }
  unsigned mX = lb + w * 2048, mW = lb + w * 3072, mA;
  const unsigned rot = (((rb + 2 * cb) & 7) * 10) >> 3;
  unsigned kx[10], kw_[10], voA2 = voA + 9216;
  auto kofs = [&](int s) {
    asm volatile("s_add_u32 %0, %2, %3\n\ts_cmp_ge_u32 %0, 10\n\ts_cselect_b32 %1, 10, 0\n\ts_sub_u32 %0, %0, %1\n\ts_mul_i32 %1, %0, 0x30000\n\ts_lshl_b32 %0, %0, 7"
                 : "=&s"(kx[s]), "=&s"(kw_[s]) : "s"(rot), "i"(s) : "scc");
  };
  kofs(0); kofs(1);
  asm volatile("" : "+v"(voA), "+v"(voA2), "+v"(voB[0]), "+v"(voB[1]), "+v"(voB[2]));
  asm volatile("" : "+s"(mX), "+s"(mW)); SB();
  asm volatile("s_waitcnt lgkmcnt(0)" : "+s"(ka0), "+s"(ka1) :: "memory"); SB();
  const char *x = (const char*)ka0[0], *kin = (const char*)ka0[1], *vin = (const char*)ka0[2], *shift_k = (const char*)ka0[3], *ssm_k = (const char*)ka0[4];
  const char *ssm_D = (const char*)ka0[5], *w_in = (const char*)ka0[6], *w_sp = (const char*)ka0[7], *b_sp = (const char*)ka1[0], *w_out = (const char*)ka1[1];
  char *out = (char*)ka1[2], *ws = (char*)ka1[3];
  const auto rX = __builtin_amdgcn_make_buffer_rsrc((void*)x, 0, S_ * D_ * 2, 0x00020000);
  const auto rW = __builtin_amdgcn_make_buffer_rsrc((void*)(w_in + 1536), 0, 640 * 3072 - 1536, 0x00020000);
  asm volatile("; named registers" :  :  : "v255", "a255"); SB();
  asm volatile("s_add_u32 m0, %0, 81920" :  : "s"(mX) : "memory", "scc"); SB();
  asm volatile("s_nop 0\n\tbuffer_load_dwordx4 %0, %1, %2 offen lds" :  : "v"(voA), "s"(rX), "s"(kx[0]) : "memory"); SB();
  asm volatile("buffer_load_dwordx4 %0, %1, %2 offen offset:1024 lds" :  : "v"(voA2), "s"(rX), "s"(kx[0]) : "memory"); SB();
  asm volatile("s_add_u32 m0, %0, 0" :  : "s"(mW) : "memory", "scc"); SB();
  asm volatile("s_nop 0\n\tbuffer_load_dwordx4 %0, %1, %2 offen lds" :  : "v"(voB[0]), "s"(rW), "s"(kw_[0]) : "memory"); SB();
  asm volatile("buffer_load_dwordx4 %0, %1, %2 offen offset:1024 lds" :  : "v"(voB[1]), "s"(rW), "s"(kw_[0]) : "memory"); SB();
  asm volatile("buffer_load_dwordx4 %0, %1, %2 offen offset:2048 lds" :  : "v"(voB[2]), "s"(rW), "s"(kw_[0]) : "memory"); SB();
  asm volatile("s_add_u32 m0, %0, 90112" :  : "s"(mX) : "memory", "scc"); SB();
  asm volatile("s_nop 0\n\tbuffer_load_dwordx4 %0, %1, %2 offen lds" :  : "v"(voA), "s"(rX), "s"(kx[1]) : "memory"); SB();
  asm volatile("buffer_load_dwordx4 %0, %1, %2 offen offset:1024 lds" :  : "v"(voA2), "s"(rX), "s"(kx[1]) : "memory"); SB();
  kofs(2); kofs(3); kofs(4); kofs(5); kofs(6); kofs(7); kofs(8); kofs(9);
  const auto rWS = __builtin_amdgcn_make_buffer_rsrc((void*)ws, 0, 32 << 20, 0x00020000);
  const auto rW1 = __builtin_amdgcn_make_buffer_rsrc((void*)w_in, 0, 640 * 3072, 0x00020000);
  mA = lb + w * 8192; asm volatile("" : "+s"(mA));
  { unsigned lane = lane0, w = w0, rb = rb0, cb = cb0;
  asm volatile("" : "+v"(lane)); asm volatile("" : "+s"(w), "+s"(rb), "+s"(cb)); SB();

  unsigned ao[8];
  u32x2 kp[5], kc[5], vv[5], sw[5], wt[2], ko[5];
  u32x4 kq[3];
  f32x2 kt[2], kf[2];
  unsigned ku[5];
  unsigned aX[2][2], aW[2][2][3];
#pragma unroll
  for (int n = 0; n < 4; ++n) aX[n >> 1][n & 1] = lb + 98304 + (wm * 32 + ((n & 1) ^ wn) * 16 + l15) * 128 + (((((n >> 1) ^ wm) * 4 + g4) ^ (l15 & 7)) << 4);
#pragma unroll
  for (int n = 0; n < 6; ++n) {
    aW[0][n / 3][n % 3] = lb + ((n / 3 ^ wm) * 32 + 8 * g4 + q4) * 192 + (((wn * 3 + n % 3) ^ (g4 & 1)) << 5) + 8 * p4;
    aW[1][n / 3][n % 3] = aW[0][n / 3][n % 3] + 49152;
    asm volatile("" : "+v"(aW[0][n / 3][n % 3]), "+v"(aW[1][n / 3][n % 3]));
  }
  f32x4 co[3];
  u32x4 ones = {0x3F803F80u, 0x3F803F80u, 0x3F803F80u, 0x3F803F80u};
  f32x4 a2[2][3], a1[2][3], rs, gr, c2[3], c1[3];
  u32x2 zt[3][2];
  asm volatile("" : "+v"(ones), "+v"(aX[0][0]), "+v"(aX[0][1]), "+v"(aX[1][0]), "+v"(aX[1][1]));
  int tmo = (1 << 18);
  u32x2 fk, fz, fh, fi;
  unsigned tg0, tg1;
  float xm[2], xd[2], k1[2], k2[2];
  f32x4 xs[2];
  float xt[4];
  u32x2 xv[5];
  unsigned mU, uso, w3k[2], sFK, sFZ;
  float dD[3], bs;
  u32x4 im[6], zero4;
  SB();
  asm volatile("s_add_u32 m0, %0, 12288" :  : "s"(mW) : "memory", "scc"); SB();
  asm volatile("s_nop 0\n\tbuffer_load_dwordx4 %0, %1, %2 offen lds" :  : "v"(voB[0]), "s"(rW), "s"(kw_[1]) : "memory"); SB();
  asm volatile("buffer_load_dwordx4 %0, %1, %2 offen offset:1024 lds" :  : "v"(voB[1]), "s"(rW), "s"(kw_[1]) : "memory"); SB();
  asm volatile("buffer_load_dwordx4 %0, %1, %2 offen offset:2048 lds" :  : "v"(voB[2]), "s"(rW), "s"(kw_[1]) : "memory"); SB();
  const auto rK = __builtin_amdgcn_make_buffer_rsrc((void*)kin, 0, S_ * D_ * 2, 0x00020000);
  const auto rV = __builtin_amdgcn_make_buffer_rsrc((void*)vin, 0, S_ * D_ * 2, 0x00020000);
  const auto rS = __builtin_amdgcn_make_buffer_rsrc((void*)(shift_k - 3328), 0, D_ * 8 + 3328, 0x00020000);
  const auto rO = __builtin_amdgcn_make_buffer_rsrc((void*)(w_out - 3072), 0, 768 * 1280 + 3072, 0x00020000);
  unsigned voK[4], voV[3], voH = cb * 640 + l16;
#pragma unroll
  for (int n = 0; n < 4; ++n) { const unsigned o = n * 64 + lane, r = (o * 205) >> 11; voK[n] = (rb * 64 + w * 16 - 4) * 1280 + cb * 160 + r * 1120 + lane * 16; }
#pragma unroll
  for (int n = 0; n < 3; ++n) voV[n] = voK[n] + 5120;
  const unsigned wof = (rb >> 3) * 30 + min((rb & 7) * 4 + w, 29u), wok = (wof * 205) >> 10;
  unsigned voO = (lane >> 1) * 1280 + (lane & 1) * 16 + wok * 40960 + (wof - 5 * wok) * 32 + cb * 160, vWO = W_WO + (cb * 120 + wof) * 1024 + l16;
  asm volatile("" : "+v"(vWO), "+v"(voK[0]), "+v"(voK[1]), "+v"(voK[2]), "+v"(voK[3]), "+v"(voV[0]), "+v"(voV[1]), "+v"(voV[2]), "+v"(voH), "+v"(voO));
  SB();
  const auto rF = __builtin_amdgcn_make_buffer_rsrc((void*)ssm_k, 0, S_ * D_ * 2, 0x00020000);
  unsigned vF = ch0 * 4096 + w * 1024 + l16, c2s_ = c2s;
  unsigned hpo[3], vD = ch0 * 2, vD2 = vD + c2o * 2 - 4, vBs = (rb * 64 + w * 16 + l15) * 2;
  hpo[0] = w ? ch0 * 4096 + w * 1024 - 4 : 0x40000000u; hpo[1] = hpo[0] + 4096; hpo[2] = hpo[0] + c2s;
  asm volatile("" : "+s"(hpo[0]), "+s"(hpo[1]), "+s"(hpo[2]), "+v"(vD), "+v"(vD2), "+v"(vBs), "+v"(vF), "+s"(c2s_));
  SB();
  unsigned ka = lb + 98304 + w * 8192 + (4 * p4 + q4) * 160 + 8 * g4, oa = lb + 98304 + 7168 + w * 8192 + (4 * g4 + q4) * 32 + 8 * p4;
  unsigned sa = lb + 98304 + 3328 + w * 8192 + (4 * g4 + p4) * 8;
  unsigned kw = lb + 12288 + (4 * g4 + p4) * 144 + w * 32 + q4 * 8, kr = lb + 12288 + (w * 24 + (lane >> 3)) * 144 + (lane & 7) * 16;
  unsigned vKV = W_KV + (cb * 80 + w * 24 + (lane >> 3)) * 4096 + rb * 128 + (lane & 7) * 16;
  unsigned vS = W_ST + (rb * 2 + wm) * 8192 + wn * 512 + (lane >> 5) * 512 + l16;
  asm volatile("" : "+v"(ka), "+v"(oa), "+v"(sa), "+v"(kw), "+v"(kr), "+v"(vKV), "+v"(vS));
  SB();
  ST(1);
  SB();
  asm volatile("s_add_u32 m0, %0, 98304" :  : "s"(mX) : "memory", "scc"); SB();
  asm volatile("s_nop 0\n\tbuffer_load_dwordx4 %0, %1, %2 offen lds" :  : "v"(voA), "s"(rX), "s"(kx[2]) : "memory"); SB();
  asm volatile("buffer_load_dwordx4 %0, %1, %2 offen offset:1024 lds" :  : "v"(voA2), "s"(rX), "s"(kx[2]) : "memory"); SB();
  asm volatile("buffer_load_dwordx2 v[252:253], off, %0, 0 sc1" :  : "s"(rWS) : "memory"); SB();
  asm volatile("s_add_u32 m0, %0, 106496" :  : "s"(mX) : "memory", "scc"); SB();
  asm volatile("s_nop 0\n\tbuffer_load_dwordx4 %0, %1, %2 offen lds" :  : "v"(voA), "s"(rX), "s"(kx[3]) : "memory"); SB();
  asm volatile("buffer_load_dwordx4 %0, %1, %2 offen offset:1024 lds" :  : "v"(voA2), "s"(rX), "s"(kx[3]) : "memory"); SB();
  asm volatile("s_add_u32 m0, %0, 24576" :  : "s"(mW) : "memory", "scc"); SB();
  asm volatile("s_nop 0\n\tbuffer_load_dwordx4 %0, %1, %2 offen lds" :  : "v"(voB[0]), "s"(rW), "s"(kw_[2]) : "memory"); SB();
  asm volatile("s_waitcnt vmcnt(11) lgkmcnt(0)\n\ts_barrier" :  :  : "memory"); SB();
  ST(4);
  asm volatile("ds_read_b128 a[0:3], %0 offset:0" :  : "v"((aX[0][0] - 16384)) : "memory"); SB();
  asm volatile("ds_read_b128 a[4:7], %0 offset:0" :  : "v"((aX[0][1] - 16384)) : "memory"); SB();
  asm volatile("ds_read_b64_tr_b16 v[160:161], %0 offset:0" :  : "v"(aW[0][0][0]) : "memory"); SB();
  asm volatile("ds_read_b64_tr_b16 v[162:163], %0 offset:768" :  : "v"(aW[0][0][0]) : "memory"); SB();
  asm volatile("ds_read_b64_tr_b16 v[164:165], %0 offset:0" :  : "v"(aW[0][0][1]) : "memory"); SB();
  asm volatile("ds_read_b64_tr_b16 v[166:167], %0 offset:768" :  : "v"(aW[0][0][1]) : "memory"); SB();
  asm volatile("ds_read_b64_tr_b16 v[168:169], %0 offset:0" :  : "v"(aW[0][0][2]) : "memory"); SB();
  asm volatile("ds_read_b64_tr_b16 v[170:171], %0 offset:768" :  : "v"(aW[0][0][2]) : "memory"); SB();
  asm volatile("ds_read_b128 a[8:11], %0 offset:0" :  : "v"((aX[1][0] - 16384)) : "memory"); SB();
  asm volatile("ds_read_b128 a[12:15], %0 offset:0" :  : "v"((aX[1][1] - 16384)) : "memory"); SB();
  asm volatile("s_waitcnt lgkmcnt(6)\n\tv_mfma_f32_16x16x32_bf16 %0, v[160:163], a[0:3], 0" : "=&v"(a2[0][0]) :  : ); SB();
  asm volatile("ds_read_b64_tr_b16 v[172:173], %0 offset:0" :  : "v"(aW[0][1][0]) : "memory"); SB();
  asm volatile("buffer_load_dwordx4 %0, %1, %2 offen offset:1024 lds" :  : "v"(voB[1]), "s"(rW), "s"(kw_[2]) : "memory"); SB();
  asm volatile("v_mfma_f32_16x16x32_bf16 %0, v[160:163], a[4:7], 0" : "=&v"(a2[1][0]) :  : ); SB();
  asm volatile("ds_read_b64_tr_b16 v[174:175], %0 offset:768" :  : "v"(aW[0][1][0]) : "memory"); SB();
  asm volatile("buffer_load_dwordx4 %0, %1, %2 offen offset:2048 lds" :  : "v"(voB[2]), "s"(rW), "s"(kw_[2]) : "memory"); SB();
  asm volatile("v_mfma_f32_16x16x32_bf16 %0, v[160:163], %1, 0" : "=&v"(c2[0]) : "v"(ones) : ); SB();
  asm volatile("ds_read_b64_tr_b16 v[176:177], %0 offset:0" :  : "v"(aW[0][1][1]) : "memory"); SB();
  asm volatile("s_waitcnt lgkmcnt(5)\n\tv_mfma_f32_16x16x32_bf16 %0, v[164:167], a[0:3], 0" : "=&v"(a2[0][1]) :  : ); SB();
  asm volatile("ds_read_b64_tr_b16 v[178:179], %0 offset:768" :  : "v"(aW[0][1][1]) : "memory"); SB();
  asm volatile("v_mfma_f32_16x16x32_bf16 %0, v[164:167], a[4:7], 0" : "=&v"(a2[1][1]) :  : ); SB();
  asm volatile("ds_read_b64_tr_b16 v[180:181], %0 offset:0" :  : "v"(aW[0][1][2]) : "memory"); SB();
  asm volatile("s_add_u32 m0, %0, 114688" :  : "s"(mX) : "memory", "scc"); SB();
  asm volatile("v_mfma_f32_16x16x32_bf16 %0, v[164:167], %1, 0" : "=&v"(c2[1]) : "v"(ones) : ); SB();
  asm volatile("ds_read_b64_tr_b16 v[182:183], %0 offset:768" :  : "v"(aW[0][1][2]) : "memory"); SB();
  asm volatile("buffer_load_dwordx4 %0, %1, %2 offen lds" :  : "v"(voA), "s"(rX), "s"(kx[4]) : "memory"); SB();
  asm volatile("v_mfma_f32_16x16x32_bf16 %0, v[168:171], a[0:3], 0" : "=&v"(a2[0][2]) :  : ); SB();
  asm volatile("s_waitcnt vmcnt(9)\n\ts_barrier" :  :  : "memory"); SB();
  asm volatile("ds_read_b64_tr_b16 v[184:185], %0 offset:12288" :  : "v"(aW[0][0][0]) : "memory"); SB();
  asm volatile("buffer_load_dwordx4 %0, %1, %2 offen offset:1024 lds" :  : "v"(voA2), "s"(rX), "s"(kx[4]) : "memory"); SB();
  asm volatile("v_mfma_f32_16x16x32_bf16 %0, v[168:171], a[4:7], 0" : "=&v"(a2[1][2]) :  : ); SB();
  asm volatile("ds_read_b64_tr_b16 v[186:187], %0 offset:13056" :  : "v"(aW[0][0][0]) : "memory"); SB();
  asm volatile("s_add_u32 m0, %0, 122880" :  : "s"(mX) : "memory", "scc"); SB();
  asm volatile("v_mfma_f32_16x16x32_bf16 %0, v[168:171], %1, 0" : "=&v"(c2[2]) : "v"(ones) : ); SB();
  asm volatile("ds_read_b64_tr_b16 v[188:189], %0 offset:12288" :  : "v"(aW[0][0][1]) : "memory"); SB();
  asm volatile("v_mfma_f32_16x16x32_bf16 %0, %1, a[0:3], 0" : "=&v"(rs) : "v"(ones) : ); SB();
  asm volatile("ds_read_b128 a[16:19], %0 offset:8192" :  : "v"((aX[0][0] - 16384)) : "memory"); SB();
  asm volatile("ds_read_b128 a[20:23], %0 offset:8192" :  : "v"((aX[0][1] - 16384)) : "memory"); SB();
  asm volatile("ds_read_b64_tr_b16 v[190:191], %0 offset:13056" :  : "v"(aW[0][0][1]) : "memory"); SB();
  asm volatile("v_mfma_f32_16x16x32_bf16 %0, a[0:3], a[0:3], 0" : "=&v"(gr) :  : ); SB();
  asm volatile("ds_read_b64_tr_b16 v[192:193], %0 offset:12288" :  : "v"(aW[0][0][2]) : "memory"); SB();
  asm volatile("s_waitcnt lgkmcnt(7)\n\tv_mfma_f32_16x16x32_bf16 %0, v[172:175], a[8:11], %0" : "+v"(a2[0][0]) :  : ); SB();
  asm volatile("ds_read_b64_tr_b16 v[194:195], %0 offset:13056" :  : "v"(aW[0][0][2]) : "memory"); SB();
  asm volatile("v_mfma_f32_16x16x32_bf16 %0, v[172:175], a[12:15], %0" : "+v"(a2[1][0]) :  : ); SB();
  asm volatile("ds_read_b128 a[24:27], %0 offset:8192" :  : "v"((aX[1][0] - 16384)) : "memory"); SB();
  asm volatile("v_mfma_f32_16x16x32_bf16 %0, v[176:179], a[8:11], %0" : "+v"(a2[0][1]) :  : ); SB();
  asm volatile("ds_read_b128 a[28:31], %0 offset:8192" :  : "v"((aX[1][1] - 16384)) : "memory"); SB();
  asm volatile("v_mfma_f32_16x16x32_bf16 %0, v[176:179], a[12:15], %0" : "+v"(a2[1][1]) :  : ); SB();
  asm volatile("buffer_load_dwordx4 %0, %1, %2 offen lds" :  : "v"(voA), "s"(rX), "s"(kx[5]) : "memory"); SB();
  asm volatile("v_mfma_f32_16x16x32_bf16 %0, v[180:183], a[8:11], %0" : "+v"(a2[0][2]) :  : ); SB();
  asm volatile("ds_read_b64_tr_b16 v[160:161], %0 offset:12288" :  : "v"(aW[0][1][0]) : "memory"); SB();
  asm volatile("v_mfma_f32_16x16x32_bf16 %0, v[180:183], a[12:15], %0" : "+v"(a2[1][2]) :  : ); SB();
  asm volatile("ds_read_b64_tr_b16 v[162:163], %0 offset:13056" :  : "v"(aW[0][1][0]) : "memory"); SB();
  asm volatile("v_mfma_f32_16x16x32_bf16 %0, %1, a[8:11], %0" : "+v"(rs) : "v"(ones) : ); SB();
  asm volatile("ds_read_b64_tr_b16 v[164:165], %0 offset:12288" :  : "v"(aW[0][1][1]) : "memory"); SB();
  asm volatile("v_mfma_f32_16x16x32_bf16 %0, a[8:11], a[8:11], %0" : "+v"(gr) :  : ); SB();
  asm volatile("ds_read_b64_tr_b16 v[166:167], %0 offset:13056" :  : "v"(aW[0][1][1]) : "memory"); SB();
  asm volatile("s_waitcnt lgkmcnt(9)\n\tv_mfma_f32_16x16x32_bf16 %0, v[184:187], a[16:19], %0" : "+v"(a2[0][0]) :  : ); SB();
  asm volatile("ds_read_b64_tr_b16 v[168:169], %0 offset:12288" :  : "v"(aW[0][1][2]) : "memory"); SB();
  asm volatile("v_mfma_f32_16x16x32_bf16 %0, v[184:187], a[20:23], %0" : "+v"(a2[1][0]) :  : ); SB();
  asm volatile("ds_read_b64_tr_b16 v[170:171], %0 offset:13056" :  : "v"(aW[0][1][2]) : "memory"); SB();
  asm volatile("v_mfma_f32_16x16x32_bf16 %0, v[184:187], %1, %0" : "+v"(c2[0]) : "v"(ones) : ); SB();
  asm volatile("buffer_load_dwordx4 %0, %1, %2 offen offset:1024 lds" :  : "v"(voA2), "s"(rX), "s"(kx[5]) : "memory"); SB();
  asm volatile("s_waitcnt lgkmcnt(7)\n\tv_mfma_f32_16x16x32_bf16 %0, v[188:191], a[16:19], %0" : "+v"(a2[0][1]) :  : ); SB();
  asm volatile("s_add_u32 m0, %0, 36864" :  : "s"(mW) : "memory", "scc"); SB();
  asm volatile("v_mfma_f32_16x16x32_bf16 %0, v[188:191], a[20:23], %0" : "+v"(a2[1][1]) :  : ); SB();
  asm volatile("buffer_load_dwordx4 %0, %1, %2 offen lds" :  : "v"(voB[0]), "s"(rW), "s"(kw_[3]) : "memory"); SB();
  asm volatile("buffer_load_dwordx4 %0, %1, %2 offen offset:1024 lds" :  : "v"(voB[1]), "s"(rW), "s"(kw_[3]) : "memory"); SB();
  asm volatile("buffer_load_dwordx4 %0, %1, %2 offen offset:2048 lds" :  : "v"(voB[2]), "s"(rW), "s"(kw_[3]) : "memory"); SB();
  asm volatile("s_add_u32 m0, %0, 131072" :  : "s"(mX) : "memory", "scc"); SB();
  asm volatile("s_nop 0\n\tbuffer_load_dwordx4 %0, %1, %2 offen lds" :  : "v"(voA), "s"(rX), "s"(kx[6]) : "memory"); SB();
  asm volatile("buffer_load_dwordx4 %0, %1, %2 offen offset:1024 lds" :  : "v"(voA2), "s"(rX), "s"(kx[6]) : "memory"); SB();
  asm volatile("s_add_u32 m0, %0, 139264" :  : "s"(mX) : "memory", "scc"); SB();
  asm volatile("s_nop 0\n\tbuffer_load_dwordx4 %0, %1, %2 offen lds" :  : "v"(voA), "s"(rX), "s"(kx[7]) : "memory"); SB();
  asm volatile("s_waitcnt vmcnt(10)\n\ts_barrier" :  :  : "memory"); SB();
  asm volatile("ds_read_b64_tr_b16 v[172:173], %0 offset:24576" :  : "v"(aW[0][0][0]) : "memory"); SB();
  asm volatile("v_mfma_f32_16x16x32_bf16 %0, v[188:191], %1, %0" : "+v"(c2[1]) : "v"(ones) : ); SB();
  asm volatile("ds_read_b64_tr_b16 v[174:175], %0 offset:25344" :  : "v"(aW[0][0][0]) : "memory"); SB();
  asm volatile("v_mfma_f32_16x16x32_bf16 %0, v[192:195], a[16:19], %0" : "+v"(a2[0][2]) :  : ); SB();
  asm volatile("ds_read_b64_tr_b16 v[176:177], %0 offset:24576" :  : "v"(aW[0][0][1]) : "memory"); SB();
  asm volatile("v_mfma_f32_16x16x32_bf16 %0, v[192:195], a[20:23], %0" : "+v"(a2[1][2]) :  : ); SB();
  asm volatile("ds_read_b64_tr_b16 v[178:179], %0 offset:25344" :  : "v"(aW[0][0][1]) : "memory"); SB();
  asm volatile("v_mfma_f32_16x16x32_bf16 %0, v[192:195], %1, %0" : "+v"(c2[2]) : "v"(ones) : ); SB();
  asm volatile("ds_read_b64_tr_b16 v[180:181], %0 offset:24576" :  : "v"(aW[0][0][2]) : "memory"); SB();
  asm volatile("v_mfma_f32_16x16x32_bf16 %0, %1, a[16:19], %0" : "+v"(rs) : "v"(ones) : ); SB();
  asm volatile("ds_read_b128 a[32:35], %0 offset:0" :  : "v"(aX[0][0]) : "memory"); SB();
  asm volatile("ds_read_b128 a[36:39], %0 offset:0" :  : "v"(aX[0][1]) : "memory"); SB();
  asm volatile("ds_read_b64_tr_b16 v[182:183], %0 offset:25344" :  : "v"(aW[0][0][2]) : "memory"); SB();
  asm volatile("buffer_load_dwordx4 %0, %1, %2 offen offset:1024 lds" :  : "v"(voA2), "s"(rX), "s"(kx[7]) : "memory"); SB();
  asm volatile("v_mfma_f32_16x16x32_bf16 %0, a[16:19], a[16:19], %0" : "+v"(gr) :  : ); SB();
  asm volatile("ds_read_b128 a[40:43], %0 offset:0" :  : "v"(aX[1][0]) : "memory"); SB();
  asm volatile("s_waitcnt lgkmcnt(9)\n\tv_mfma_f32_16x16x32_bf16 %0, v[160:163], a[24:27], %0" : "+v"(a2[0][0]) :  : ); SB();
  asm volatile("ds_read_b128 a[44:47], %0 offset:0" :  : "v"(aX[1][1]) : "memory"); SB();
  asm volatile("s_add_u32 m0, %0, 49152" :  : "s"(mW) : "memory", "scc"); SB();
  asm volatile("v_mfma_f32_16x16x32_bf16 %0, v[160:163], a[28:31], %0" : "+v"(a2[1][0]) :  : ); SB();
  asm volatile("ds_read_b128 a[48:51], %0 offset:8192" :  : "v"(aX[0][0]) : "memory"); SB();
  asm volatile("v_mfma_f32_16x16x32_bf16 %0, v[164:167], a[24:27], %0" : "+v"(a2[0][1]) :  : ); SB();
  asm volatile("ds_read_b128 a[52:55], %0 offset:8192" :  : "v"(aX[0][1]) : "memory"); SB();
  asm volatile("v_mfma_f32_16x16x32_bf16 %0, v[164:167], a[28:31], %0" : "+v"(a2[1][1]) :  : ); SB();
  asm volatile("ds_read_b128 a[56:59], %0 offset:8192" :  : "v"(aX[1][0]) : "memory"); SB();
  asm volatile("v_mfma_f32_16x16x32_bf16 %0, v[168:171], a[24:27], %0" : "+v"(a2[0][2]) :  : ); SB();
  asm volatile("ds_read_b128 a[60:63], %0 offset:8192" :  : "v"(aX[1][1]) : "memory"); SB();
  asm volatile("v_mfma_f32_16x16x32_bf16 %0, v[168:171], a[28:31], %0" : "+v"(a2[1][2]) :  : ); SB();
  asm volatile("buffer_load_dwordx4 %0, %1, %2 offen lds" :  : "v"(voB[0]), "s"(rW), "s"(kw_[4]) : "memory"); SB();
  asm volatile("v_mfma_f32_16x16x32_bf16 %0, %1, a[24:27], %0" : "+v"(rs) : "v"(ones) : ); SB();
  asm volatile("ds_read_b64_tr_b16 v[184:185], %0 offset:24576" :  : "v"(aW[0][1][0]) : "memory"); SB();
  asm volatile("v_mfma_f32_16x16x32_bf16 %0, a[24:27], a[24:27], %0" : "+v"(gr) :  : ); SB();
  asm volatile("ds_read_b64_tr_b16 v[186:187], %0 offset:25344" :  : "v"(aW[0][1][0]) : "memory"); SB();
  asm volatile("s_waitcnt lgkmcnt(8)\n\tv_mfma_f32_16x16x32_bf16 %0, v[172:175], a[32:35], %0" : "+v"(a2[0][0]) :  : ); SB();
  asm volatile("ds_read_b64_tr_b16 v[188:189], %0 offset:24576" :  : "v"(aW[0][1][1]) : "memory"); SB();
  asm volatile("v_mfma_f32_16x16x32_bf16 %0, v[172:175], a[36:39], %0" : "+v"(a2[1][0]) :  : ); SB();
  asm volatile("ds_read_b64_tr_b16 v[190:191], %0 offset:25344" :  : "v"(aW[0][1][1]) : "memory"); SB();
  asm volatile("v_mfma_f32_16x16x32_bf16 %0, v[172:175], %1, %0" : "+v"(c2[0]) : "v"(ones) : ); SB();
  asm volatile("ds_read_b64_tr_b16 v[192:193], %0 offset:24576" :  : "v"(aW[0][1][2]) : "memory"); SB();
  asm volatile("v_mfma_f32_16x16x32_bf16 %0, v[176:179], a[32:35], %0" : "+v"(a2[0][1]) :  : ); SB();
  asm volatile("ds_read_b64_tr_b16 v[194:195], %0 offset:25344" :  : "v"(aW[0][1][2]) : "memory"); SB();
  asm volatile("v_mfma_f32_16x16x32_bf16 %0, v[176:179], a[36:39], %0" : "+v"(a2[1][1]) :  : ); SB();
  asm volatile("buffer_load_dwordx4 %0, %1, %2 offen offset:1024 lds" :  : "v"(voB[1]), "s"(rW), "s"(kw_[4]) : "memory"); SB();
  asm volatile("v_mfma_f32_16x16x32_bf16 %0, v[176:179], %1, %0" : "+v"(c2[1]) : "v"(ones) : ); SB();
  asm volatile("buffer_load_dwordx4 %0, %1, %2 offen offset:2048 lds" :  : "v"(voB[2]), "s"(rW), "s"(kw_[4]) : "memory"); SB();
  asm volatile("v_mfma_f32_16x16x32_bf16 %0, v[180:183], a[32:35], %0" : "+v"(a2[0][2]) :  : ); SB();
  asm volatile("v_mfma_f32_16x16x32_bf16 %0, v[180:183], a[36:39], %0" : "+v"(a2[1][2]) :  : ); SB();
  asm volatile("v_mfma_f32_16x16x32_bf16 %0, v[180:183], %1, %0" : "+v"(c2[2]) : "v"(ones) : ); SB();
  const auto rA = __builtin_amdgcn_make_buffer_rsrc((void*)(w_sp - 3072), 0, S_ * S_ * 2 + 3072, 0x00020000);
  unsigned ca = lb + 98304 + 6912 + w * 8192 + g4 * 16, cr = lb + 98304 + 6912 + (w ^ 2) * 8192 + g4 * 16;
  unsigned voS = (rb * 64 + (lane >> 3)) * 4096 + w * 1024 + (cb & 5) * 128 + (((lane & 7) ^ (lane >> 3)) << 4), aA[2];
#pragma unroll
  for (int k = 0; k < 2; ++k) aA[k] = lb + 98304 + w * 8192 + l15 * 128 + (((k * 4 + g4) ^ (l15 & 7)) << 4);
  unsigned xw = lb + 98304 + 6656 + w * 8192 + l15 * 16, xr_ = lb + 98304 + 6656 + (w ^ 1) * 8192 + l15 * 16;
  unsigned vo = W_ST + ((rb * 2 + wm) * 8 + cb) * 1024 + wn * 128 + ((lane >> 5) ^ wn) * 512 + (l15 >> 3) * 256 + (l15 & 7) * 16;
  asm volatile("" : "+v"(ca), "+v"(cr), "+v"(voS), "+v"(aA[0]), "+v"(aA[1]), "+v"(xw), "+v"(xr_), "+v"(vo));
  SB();
  asm volatile("s_add_u32 m0, %0, 147456" :  : "s"(mX) : "memory", "scc"); SB();
  asm volatile("s_nop 0\n\tbuffer_load_dwordx4 %0, %1, %2 offen lds" :  : "v"(voA), "s"(rX), "s"(kx[8]) : "memory"); SB();
  asm volatile("buffer_load_dwordx4 %0, %1, %2 offen offset:1024 lds" :  : "v"(voA2), "s"(rX), "s"(kx[8]) : "memory"); SB();
  asm volatile("s_waitcnt vmcnt(9)\n\ts_barrier" :  :  : "memory"); SB();
  asm volatile("ds_read_b64_tr_b16 v[160:161], %0 offset:36864" :  : "v"(aW[0][0][0]) : "memory"); SB();
  asm volatile("s_add_u32 m0, %0, 155648" :  : "s"(mX) : "memory", "scc"); SB();
  asm volatile("v_mfma_f32_16x16x32_bf16 %0, %1, a[32:35], %0" : "+v"(rs) : "v"(ones) : ); SB();
  asm volatile("ds_read_b64_tr_b16 v[162:163], %0 offset:37632" :  : "v"(aW[0][0][0]) : "memory"); SB();
  asm volatile("buffer_load_dwordx4 %0, %1, %2 offen lds" :  : "v"(voA), "s"(rX), "s"(kx[9]) : "memory"); SB();
  asm volatile("v_mfma_f32_16x16x32_bf16 %0, a[32:35], a[32:35], %0" : "+v"(gr) :  : ); SB();
  asm volatile("ds_read_b64_tr_b16 v[164:165], %0 offset:36864" :  : "v"(aW[0][0][1]) : "memory"); SB();
  asm volatile("s_waitcnt lgkmcnt(3)\n\tv_mfma_f32_16x16x32_bf16 %0, %1, a[40:43], %0" : "+v"(rs) : "v"(ones) : ); SB();
  asm volatile("ds_read_b64_tr_b16 v[166:167], %0 offset:37632" :  : "v"(aW[0][0][1]) : "memory"); SB();
  asm volatile("buffer_load_dwordx4 %0, %1, %2 offen offset:1024 lds" :  : "v"(voA2), "s"(rX), "s"(kx[9]) : "memory"); SB();
  asm volatile("v_mfma_f32_16x16x32_bf16 %0, a[40:43], a[40:43], %0" : "+v"(gr) :  : ); SB();
  asm volatile("ds_read_b64_tr_b16 v[168:169], %0 offset:36864" :  : "v"(aW[0][0][2]) : "memory"); SB();
  asm volatile("v_mfma_f32_16x16x32_bf16 %0, v[184:187], a[40:43], %0" : "+v"(a2[0][0]) :  : ); SB();
  asm volatile("ds_read_b64_tr_b16 v[170:171], %0 offset:37632" :  : "v"(aW[0][0][2]) : "memory"); SB();
  asm volatile("v_mfma_f32_16x16x32_bf16 %0, v[184:187], a[44:47], %0" : "+v"(a2[1][0]) :  : ); SB();
  asm volatile("ds_read_b128 a[64:67], %0 offset:16384" :  : "v"(aX[0][0]) : "memory"); SB();
  asm volatile("v_mfma_f32_16x16x32_bf16 %0, v[188:191], a[40:43], %0" : "+v"(a2[0][1]) :  : ); SB();
  asm volatile("s_add_u32 m0, %0, 61440" :  : "s"(mW) : "memory", "scc"); SB();
  asm volatile("ds_read_b128 a[68:71], %0 offset:16384" :  : "v"(aX[0][1]) : "memory"); SB();
  asm volatile("v_mfma_f32_16x16x32_bf16 %0, v[188:191], a[44:47], %0" : "+v"(a2[1][1]) :  : ); SB();
  asm volatile("buffer_load_dwordx4 %0, %1, %2 offen lds" :  : "v"(voB[0]), "s"(rW), "s"(kw_[5]) : "memory"); SB();
  asm volatile("v_mfma_f32_16x16x32_bf16 %0, v[192:195], a[40:43], %0" : "+v"(a2[0][2]) :  : ); SB();
  asm volatile("ds_read_b128 a[72:75], %0 offset:16384" :  : "v"(aX[1][0]) : "memory"); SB();
  asm volatile("v_mfma_f32_16x16x32_bf16 %0, v[192:195], a[44:47], %0" : "+v"(a2[1][2]) :  : ); SB();
  asm volatile("ds_read_b128 a[76:79], %0 offset:16384" :  : "v"(aX[1][1]) : "memory"); SB();
  asm volatile("v_mfma_f32_16x16x32_bf16 %0, %1, a[48:51], %0" : "+v"(rs) : "v"(ones) : ); SB();
  asm volatile("ds_read_b128 a[80:83], %0 offset:24576" :  : "v"(aX[0][0]) : "memory"); SB();
  asm volatile("v_mfma_f32_16x16x32_bf16 %0, a[48:51], a[48:51], %0" : "+v"(gr) :  : ); SB();
  asm volatile("ds_read_b128 a[84:87], %0 offset:24576" :  : "v"(aX[0][1]) : "memory"); SB();
  asm volatile("v_mfma_f32_16x16x32_bf16 %0, %1, a[56:59], %0" : "+v"(rs) : "v"(ones) : ); SB();
  asm volatile("ds_read_b64_tr_b16 v[172:173], %0 offset:36864" :  : "v"(aW[0][1][0]) : "memory"); SB();
  asm volatile("v_mfma_f32_16x16x32_bf16 %0, a[56:59], a[56:59], %0" : "+v"(gr) :  : ); SB();
  asm volatile("ds_read_b64_tr_b16 v[174:175], %0 offset:37632" :  : "v"(aW[0][1][0]) : "memory"); SB();
  asm volatile("s_waitcnt lgkmcnt(8)\n\tv_mfma_f32_16x16x32_bf16 %0, v[160:163], a[48:51], %0" : "+v"(a2[0][0]) :  : ); SB();
  asm volatile("ds_read_b64_tr_b16 v[176:177], %0 offset:36864" :  : "v"(aW[0][1][1]) : "memory"); SB();
  asm volatile("v_mfma_f32_16x16x32_bf16 %0, v[160:163], a[52:55], %0" : "+v"(a2[1][0]) :  : ); SB();
  asm volatile("ds_read_b64_tr_b16 v[178:179], %0 offset:37632" :  : "v"(aW[0][1][1]) : "memory"); SB();
  asm volatile("v_mfma_f32_16x16x32_bf16 %0, v[160:163], %1, %0" : "+v"(c2[0]) : "v"(ones) : ); SB();
  asm volatile("ds_read_b64_tr_b16 v[180:181], %0 offset:36864" :  : "v"(aW[0][1][2]) : "memory"); SB();
  asm volatile("v_mfma_f32_16x16x32_bf16 %0, v[164:167], a[48:51], %0" : "+v"(a2[0][1]) :  : ); SB();
  asm volatile("ds_read_b64_tr_b16 v[182:183], %0 offset:37632" :  : "v"(aW[0][1][2]) : "memory"); SB();
  asm volatile("v_mfma_f32_16x16x32_bf16 %0, v[164:167], a[52:55], %0" : "+v"(a2[1][1]) :  : ); SB();
  asm volatile("ds_read_b128 a[88:91], %0 offset:24576" :  : "v"(aX[1][0]) : "memory"); SB();
  asm volatile("v_mfma_f32_16x16x32_bf16 %0, v[164:167], %1, %0" : "+v"(c2[1]) : "v"(ones) : ); SB();
  asm volatile("ds_read_b128 a[92:95], %0 offset:24576" :  : "v"(aX[1][1]) : "memory"); SB();
  asm volatile("v_mfma_f32_16x16x32_bf16 %0, v[168:171], a[48:51], %0" : "+v"(a2[0][2]) :  : ); SB();
  asm volatile("buffer_load_dwordx4 %0, %1, %2 offen offset:1024 lds" :  : "v"(voB[1]), "s"(rW), "s"(kw_[5]) : "memory"); SB();
  asm volatile("v_mfma_f32_16x16x32_bf16 %0, v[168:171], a[52:55], %0" : "+v"(a2[1][2]) :  : ); SB();
  asm volatile("buffer_load_dwordx4 %0, %1, %2 offen offset:2048 lds" :  : "v"(voB[2]), "s"(rW), "s"(kw_[5]) : "memory"); SB();
  asm volatile("v_mfma_f32_16x16x32_bf16 %0, v[168:171], %1, %0" : "+v"(c2[2]) : "v"(ones) : ); SB();
#pragma unroll
  for (int n = 0; n < 8; ++n) { ao[n] = (n ^ (2 * w)) * 32768 + (3 - (n & 3)) * 1024; asm volatile("" : "+s"(ao[n])); }
  w3k[0] = w3s + 32768; w3k[1] = w3s + 65536; sFZ = W_FZ + ((cb * 32 + rb) * 4 + w) * 8; sFK = sFZ + 8192; mU = lb + w * 1024; uso = W_KV + c2s;
  asm volatile("" : "+s"(w3k[0]), "+s"(w3k[1]), "+s"(sFZ), "+s"(sFK), "+s"(mU), "+s"(uso));
  SB();
  asm volatile("s_add_u32 m0, %0, 73728" :  : "s"(mW) : "memory", "scc"); SB();
  asm volatile("s_nop 0\n\tbuffer_load_dwordx4 %0, %1, %2 offen lds" :  : "v"(voB[0]), "s"(rW), "s"(kw_[6]) : "memory"); SB();
  asm volatile("buffer_load_dwordx4 %0, %1, %2 offen offset:1024 lds" :  : "v"(voB[1]), "s"(rW), "s"(kw_[6]) : "memory"); SB();
  asm volatile("v_readfirstlane_b32 %0, v252\n\tv_readfirstlane_b32 %1, v253\n\ts_nop 0\n\ts_add_u32 %0, %0, 1\n\ts_addc_u32 %1, %1, 0" : "=&s"(tg0), "=&s"(tg1) :  : "memory", "scc"); SB();
  asm volatile("buffer_load_dwordx4 %0, %1, %2 offen offset:2048 lds" :  : "v"(voB[2]), "s"(rW), "s"(kw_[6]) : "memory"); SB();
  asm volatile("s_waitcnt vmcnt(10)\n\ts_barrier" :  :  : "memory"); SB();
  asm volatile("ds_read_b64_tr_b16 v[184:185], %0 offset:0" :  : "v"(aW[1][0][0]) : "memory"); SB();
  asm volatile("s_waitcnt lgkmcnt(1)\n\tv_mfma_f32_16x16x32_bf16 %0, %1, a[64:67], %0" : "+v"(rs) : "v"(ones) : ); SB();
  asm volatile("ds_read_b64_tr_b16 v[186:187], %0 offset:768" :  : "v"(aW[1][0][0]) : "memory"); SB();
  asm volatile("s_add_u32 m0, %0, 86016" :  : "s"(mW) : "memory", "scc"); SB();
  asm volatile("v_mfma_f32_16x16x32_bf16 %0, a[64:67], a[64:67], %0" : "+v"(gr) :  : ); SB();
  asm volatile("ds_read_b64_tr_b16 v[188:189], %0 offset:0" :  : "v"(aW[1][0][1]) : "memory"); SB();
  asm volatile("v_mfma_f32_16x16x32_bf16 %0, %1, a[72:75], %0" : "+v"(rs) : "v"(ones) : ); SB();
  asm volatile("ds_read_b64_tr_b16 v[190:191], %0 offset:768" :  : "v"(aW[1][0][1]) : "memory"); SB();
  asm volatile("v_mfma_f32_16x16x32_bf16 %0, a[72:75], a[72:75], %0" : "+v"(gr) :  : ); SB();
  asm volatile("ds_read_b64_tr_b16 v[192:193], %0 offset:0" :  : "v"(aW[1][0][2]) : "memory"); SB();
  asm volatile("v_mfma_f32_16x16x32_bf16 %0, v[172:175], a[56:59], %0" : "+v"(a2[0][0]) :  : ); SB();
  asm volatile("ds_read_b64_tr_b16 v[194:195], %0 offset:768" :  : "v"(aW[1][0][2]) : "memory"); SB();
  asm volatile("v_mfma_f32_16x16x32_bf16 %0, v[172:175], a[60:63], %0" : "+v"(a2[1][0]) :  : ); SB();
  asm volatile("ds_read_b128 a[96:99], %0 offset:32768" :  : "v"(aX[0][0]) : "memory"); SB();
  asm volatile("v_mfma_f32_16x16x32_bf16 %0, v[176:179], a[56:59], %0" : "+v"(a2[0][1]) :  : ); SB();
  asm volatile("ds_read_b128 a[100:103], %0 offset:32768" :  : "v"(aX[0][1]) : "memory"); SB();
  asm volatile("v_mfma_f32_16x16x32_bf16 %0, v[176:179], a[60:63], %0" : "+v"(a2[1][1]) :  : ); SB();
  asm volatile("ds_read_b128 a[104:107], %0 offset:32768" :  : "v"(aX[1][0]) : "memory"); SB();
  asm volatile("v_mfma_f32_16x16x32_bf16 %0, v[180:183], a[56:59], %0" : "+v"(a2[0][2]) :  : ); SB();
  asm volatile("ds_read_b128 a[108:111], %0 offset:32768" :  : "v"(aX[1][1]) : "memory"); SB();
  asm volatile("v_mfma_f32_16x16x32_bf16 %0, v[180:183], a[60:63], %0" : "+v"(a2[1][2]) :  : ); SB();
  asm volatile("ds_read_b128 a[112:115], %0 offset:40960" :  : "v"(aX[0][0]) : "memory"); SB();
  asm volatile("v_mfma_f32_16x16x32_bf16 %0, %1, a[80:83], %0" : "+v"(rs) : "v"(ones) : ); SB();
  asm volatile("ds_read_b128 a[116:119], %0 offset:40960" :  : "v"(aX[0][1]) : "memory"); SB();
  asm volatile("v_mfma_f32_16x16x32_bf16 %0, a[80:83], a[80:83], %0" : "+v"(gr) :  : ); SB();
  asm volatile("ds_read_b128 a[120:123], %0 offset:40960" :  : "v"(aX[1][0]) : "memory"); SB();
  asm volatile("v_mfma_f32_16x16x32_bf16 %0, %1, a[88:91], %0" : "+v"(rs) : "v"(ones) : ); SB();
  asm volatile("ds_read_b64_tr_b16 v[160:161], %0 offset:0" :  : "v"(aW[1][1][0]) : "memory"); SB();
  asm volatile("v_mfma_f32_16x16x32_bf16 %0, a[88:91], a[88:91], %0" : "+v"(gr) :  : ); SB();
  asm volatile("ds_read_b64_tr_b16 v[162:163], %0 offset:768" :  : "v"(aW[1][1][0]) : "memory"); SB();
  asm volatile("s_waitcnt lgkmcnt(9)\n\tv_mfma_f32_16x16x32_bf16 %0, v[184:187], a[64:67], %0" : "+v"(a2[0][0]) :  : ); SB();
  asm volatile("ds_read_b64_tr_b16 v[164:165], %0 offset:0" :  : "v"(aW[1][1][1]) : "memory"); SB();
  asm volatile("buffer_load_dwordx4 %0, %1, %2 offen lds" :  : "v"(voB[0]), "s"(rW), "s"(kw_[7]) : "memory"); SB();
  asm volatile("v_mfma_f32_16x16x32_bf16 %0, v[184:187], a[68:71], %0" : "+v"(a2[1][0]) :  : ); SB();
  asm volatile("ds_read_b64_tr_b16 v[166:167], %0 offset:768" :  : "v"(aW[1][1][1]) : "memory"); SB();
  asm volatile("v_mfma_f32_16x16x32_bf16 %0, v[184:187], %1, %0" : "+v"(c2[0]) : "v"(ones) : ); SB();
  asm volatile("ds_read_b64_tr_b16 v[168:169], %0 offset:0" :  : "v"(aW[1][1][2]) : "memory"); SB();
  asm volatile("v_mfma_f32_16x16x32_bf16 %0, v[188:191], a[64:67], %0" : "+v"(a2[0][1]) :  : ); SB();
  asm volatile("ds_read_b64_tr_b16 v[170:171], %0 offset:768" :  : "v"(aW[1][1][2]) : "memory"); SB();
  asm volatile("v_mfma_f32_16x16x32_bf16 %0, v[188:191], a[68:71], %0" : "+v"(a2[1][1]) :  : ); SB();
  asm volatile("buffer_load_dwordx4 %0, %1, %2 offen offset:1024 lds" :  : "v"(voB[1]), "s"(rW), "s"(kw_[7]) : "memory"); SB();
  asm volatile("v_mfma_f32_16x16x32_bf16 %0, v[188:191], %1, %0" : "+v"(c2[1]) : "v"(ones) : ); SB();
  asm volatile("ds_read_b128 a[124:127], %0 offset:40960" :  : "v"(aX[1][1]) : "memory"); SB();
  asm volatile("v_mfma_f32_16x16x32_bf16 %0, v[192:195], a[64:67], %0" : "+v"(a2[0][2]) :  : ); SB();
  asm volatile("buffer_load_dwordx4 %0, %1, %2 offen offset:2048 lds" :  : "v"(voB[2]), "s"(rW), "s"(kw_[7]) : "memory"); SB();
  asm volatile("v_mfma_f32_16x16x32_bf16 %0, v[192:195], a[68:71], %0" : "+v"(a2[1][2]) :  : ); SB();
  asm volatile("v_mfma_f32_16x16x32_bf16 %0, v[192:195], %1, %0" : "+v"(c2[2]) : "v"(ones) : ); SB();
  asm volatile("s_add_u32 m0, %0, 0" :  : "s"(mW) : "memory", "scc"); SB();
  asm volatile("s_nop 0\n\tbuffer_load_dwordx4 %0, %1, %2 offen lds" :  : "v"(voB[0]), "s"(rW), "s"(kw_[8]) : "memory"); SB();
  asm volatile("buffer_load_dwordx4 %0, %1, %2 offen offset:1024 lds" :  : "v"(voB[1]), "s"(rW), "s"(kw_[8]) : "memory"); SB();
  asm volatile("buffer_load_dwordx4 %0, %1, %2 offen offset:2048 lds" :  : "v"(voB[2]), "s"(rW), "s"(kw_[8]) : "memory"); SB();
  asm volatile("s_add_u32 m0, %0, 12288" :  : "s"(mW) : "memory", "scc"); SB();
  asm volatile("s_nop 0\n\tbuffer_load_dwordx4 %0, %1, %2 offen lds" :  : "v"(voB[0]), "s"(rW), "s"(kw_[9]) : "memory"); SB();
  asm volatile("s_waitcnt vmcnt(10)\n\ts_barrier" :  :  : "memory"); SB();
  asm volatile("ds_read_b64_tr_b16 v[172:173], %0 offset:12288" :  : "v"(aW[1][0][0]) : "memory"); SB();
  asm volatile("s_waitcnt lgkmcnt(1)\n\tv_mfma_f32_16x16x32_bf16 %0, %1, a[96:99], %0" : "+v"(rs) : "v"(ones) : ); SB();
  asm volatile("ds_read_b64_tr_b16 v[174:175], %0 offset:13056" :  : "v"(aW[1][0][0]) : "memory"); SB();
  asm volatile("v_mfma_f32_16x16x32_bf16 %0, a[96:99], a[96:99], %0" : "+v"(gr) :  : ); SB();
  asm volatile("ds_read_b64_tr_b16 v[176:177], %0 offset:12288" :  : "v"(aW[1][0][1]) : "memory"); SB();
  asm volatile("v_mfma_f32_16x16x32_bf16 %0, %1, a[104:107], %0" : "+v"(rs) : "v"(ones) : ); SB();
  asm volatile("ds_read_b64_tr_b16 v[178:179], %0 offset:13056" :  : "v"(aW[1][0][1]) : "memory"); SB();
  asm volatile("v_mfma_f32_16x16x32_bf16 %0, a[104:107], a[104:107], %0" : "+v"(gr) :  : ); SB();
  asm volatile("ds_read_b64_tr_b16 v[180:181], %0 offset:12288" :  : "v"(aW[1][0][2]) : "memory"); SB();
  asm volatile("v_mfma_f32_16x16x32_bf16 %0, v[160:163], a[72:75], %0" : "+v"(a2[0][0]) :  : ); SB();
  asm volatile("ds_read_b64_tr_b16 v[182:183], %0 offset:13056" :  : "v"(aW[1][0][2]) : "memory"); SB();
  asm volatile("v_mfma_f32_16x16x32_bf16 %0, v[160:163], a[76:79], %0" : "+v"(a2[1][0]) :  : ); SB();
  asm volatile("ds_read_b128 a[128:131], %0 offset:49152" :  : "v"(aX[0][0]) : "memory"); SB();
  asm volatile("v_mfma_f32_16x16x32_bf16 %0, v[164:167], a[72:75], %0" : "+v"(a2[0][1]) :  : ); SB();
  asm volatile("ds_read_b128 a[132:135], %0 offset:49152" :  : "v"(aX[0][1]) : "memory"); SB();
  asm volatile("v_mfma_f32_16x16x32_bf16 %0, v[164:167], a[76:79], %0" : "+v"(a2[1][1]) :  : ); SB();
  asm volatile("ds_read_b128 a[136:139], %0 offset:49152" :  : "v"(aX[1][0]) : "memory"); SB();
  asm volatile("v_mfma_f32_16x16x32_bf16 %0, v[168:171], a[72:75], %0" : "+v"(a2[0][2]) :  : ); SB();
  asm volatile("ds_read_b128 a[140:143], %0 offset:49152" :  : "v"(aX[1][1]) : "memory"); SB();
  asm volatile("v_mfma_f32_16x16x32_bf16 %0, v[168:171], a[76:79], %0" : "+v"(a2[1][2]) :  : ); SB();
  asm volatile("buffer_load_dwordx4 %0, %1, %2 offen offset:1024 lds" :  : "v"(voB[1]), "s"(rW), "s"(kw_[9]) : "memory"); SB();
  asm volatile("v_mfma_f32_16x16x32_bf16 %0, %1, a[112:115], %0" : "+v"(rs) : "v"(ones) : ); SB();
  asm volatile("ds_read_b128 a[144:147], %0 offset:57344" :  : "v"(aX[0][0]) : "memory"); SB();
  asm volatile("v_mfma_f32_16x16x32_bf16 %0, a[112:115], a[112:115], %0" : "+v"(gr) :  : ); SB();
  asm volatile("ds_read_b128 a[148:151], %0 offset:57344" :  : "v"(aX[0][1]) : "memory"); SB();
  asm volatile("v_mfma_f32_16x16x32_bf16 %0, %1, a[120:123], %0" : "+v"(rs) : "v"(ones) : ); SB();
  asm volatile("ds_read_b64_tr_b16 v[184:185], %0 offset:12288" :  : "v"(aW[1][1][0]) : "memory"); SB();
  asm volatile("v_mfma_f32_16x16x32_bf16 %0, a[120:123], a[120:123], %0" : "+v"(gr) :  : ); SB();
  asm volatile("ds_read_b64_tr_b16 v[186:187], %0 offset:13056" :  : "v"(aW[1][1][0]) : "memory"); SB();
  asm volatile("buffer_load_dwordx4 %0, %1, %2 offen offset:2048 lds" :  : "v"(voB[2]), "s"(rW), "s"(kw_[9]) : "memory"); SB();
  asm volatile("s_waitcnt lgkmcnt(8)\n\tv_mfma_f32_16x16x32_bf16 %0, v[172:175], a[80:83], %0" : "+v"(a2[0][0]) :  : ); SB();
  asm volatile("ds_read_b64_tr_b16 v[188:189], %0 offset:12288" :  : "v"(aW[1][1][1]) : "memory"); SB();
  asm volatile("v_mfma_f32_16x16x32_bf16 %0, v[172:175], a[84:87], %0" : "+v"(a2[1][0]) :  : ); SB();
  asm volatile("ds_read_b64_tr_b16 v[190:191], %0 offset:13056" :  : "v"(aW[1][1][1]) : "memory"); SB();
  asm volatile("v_mfma_f32_16x16x32_bf16 %0, v[172:175], %1, %0" : "+v"(c2[0]) : "v"(ones) : ); SB();
  asm volatile("ds_read_b64_tr_b16 v[192:193], %0 offset:12288" :  : "v"(aW[1][1][2]) : "memory"); SB();
  asm volatile("v_mfma_f32_16x16x32_bf16 %0, v[176:179], a[80:83], %0" : "+v"(a2[0][1]) :  : ); SB();
  asm volatile("ds_read_b64_tr_b16 v[194:195], %0 offset:13056" :  : "v"(aW[1][1][2]) : "memory"); SB();
  asm volatile("v_mfma_f32_16x16x32_bf16 %0, v[176:179], a[84:87], %0" : "+v"(a2[1][1]) :  : ); SB();
  asm volatile("ds_read_b128 a[152:155], %0 offset:57344" :  : "v"(aX[1][0]) : "memory"); SB();
  asm volatile("v_mfma_f32_16x16x32_bf16 %0, v[176:179], %1, %0" : "+v"(c2[1]) : "v"(ones) : ); SB();
  asm volatile("ds_read_b128 a[156:159], %0 offset:57344" :  : "v"(aX[1][1]) : "memory"); SB();
  asm volatile("v_mfma_f32_16x16x32_bf16 %0, v[180:183], a[80:83], %0" : "+v"(a2[0][2]) :  : ); SB();
  asm volatile("buffer_load_dword v242, off, %0, %1" :  : "s"(rF), "s"(hpo[0]) : "memory"); SB();
  asm volatile("s_add_u32 m0, %0, 98304" :  : "s"(mA) : "memory", "scc"); SB();
  asm volatile("v_mfma_f32_16x16x32_bf16 %0, v[180:183], a[84:87], %0" : "+v"(a2[1][2]) :  : ); SB();
  asm volatile("buffer_load_dwordx4 %0, %1, %2 offen lds" :  : "v"(voK[0]), "s"(rK), "s"(0u) : "memory"); SB();
  asm volatile("v_mfma_f32_16x16x32_bf16 %0, v[180:183], %1, %0" : "+v"(c2[2]) : "v"(ones) : ); SB();
  asm volatile("s_waitcnt vmcnt(11)\n\ts_barrier" :  :  : "memory"); SB();
  asm volatile("ds_read_b64_tr_b16 v[160:161], %0 offset:24576" :  : "v"(aW[1][0][0]) : "memory"); SB();
  asm volatile("s_waitcnt lgkmcnt(5)\n\tv_mfma_f32_16x16x32_bf16 %0, %1, a[128:131], %0" : "+v"(rs) : "v"(ones) : ); SB();
  asm volatile("ds_read_b64_tr_b16 v[162:163], %0 offset:25344" :  : "v"(aW[1][0][0]) : "memory"); SB();
  asm volatile("v_mfma_f32_16x16x32_bf16 %0, a[128:131], a[128:131], %0" : "+v"(gr) :  : ); SB();
  asm volatile("ds_read_b64_tr_b16 v[164:165], %0 offset:24576" :  : "v"(aW[1][0][1]) : "memory"); SB();
  asm volatile("v_mfma_f32_16x16x32_bf16 %0, %1, a[136:139], %0" : "+v"(rs) : "v"(ones) : ); SB();
  asm volatile("ds_read_b64_tr_b16 v[166:167], %0 offset:25344" :  : "v"(aW[1][0][1]) : "memory"); SB();
  asm volatile("v_mfma_f32_16x16x32_bf16 %0, a[136:139], a[136:139], %0" : "+v"(gr) :  : ); SB();
  asm volatile("ds_read_b64_tr_b16 v[168:169], %0 offset:24576" :  : "v"(aW[1][0][2]) : "memory"); SB();
  asm volatile("v_mfma_f32_16x16x32_bf16 %0, v[184:187], a[88:91], %0" : "+v"(a2[0][0]) :  : ); SB();
  asm volatile("ds_read_b64_tr_b16 v[170:171], %0 offset:25344" :  : "v"(aW[1][0][2]) : "memory"); SB();
  asm volatile("v_mfma_f32_16x16x32_bf16 %0, v[184:187], a[92:95], %0" : "+v"(a2[1][0]) :  : ); SB();
  asm volatile("global_load_ushort v245, %0, %1 offset:0" :  : "v"(vD), "s"(ssm_D) : "memory"); SB();
  asm volatile("v_mfma_f32_16x16x32_bf16 %0, v[188:191], a[88:91], %0" : "+v"(a2[0][1]) :  : ); SB();
  asm volatile("buffer_load_dwordx4 %0, %1, %2 offen offset:1024 lds" :  : "v"(voK[1]), "s"(rK), "s"(0u) : "memory"); SB();
  asm volatile("v_mfma_f32_16x16x32_bf16 %0, v[188:191], a[92:95], %0" : "+v"(a2[1][1]) :  : ); SB();
  asm volatile("s_waitcnt lgkmcnt(6)\n\tv_mfma_f32_16x16x32_bf16 %0, v[192:195], a[88:91], %0" : "+v"(a2[0][2]) :  : ); SB();
  asm volatile("ds_read_b64_tr_b16 v[172:173], %0 offset:24576" :  : "v"(aW[1][1][0]) : "memory"); SB();
  asm volatile("v_mfma_f32_16x16x32_bf16 %0, v[192:195], a[92:95], %0" : "+v"(a2[1][2]) :  : ); SB();
  asm volatile("ds_read_b64_tr_b16 v[174:175], %0 offset:25344" :  : "v"(aW[1][1][0]) : "memory"); SB();
  asm volatile("v_mfma_f32_16x16x32_bf16 %0, %1, a[144:147], %0" : "+v"(rs) : "v"(ones) : ); SB();
  asm volatile("ds_read_b64_tr_b16 v[176:177], %0 offset:24576" :  : "v"(aW[1][1][1]) : "memory"); SB();
  asm volatile("v_mfma_f32_16x16x32_bf16 %0, a[144:147], a[144:147], %0" : "+v"(gr) :  : ); SB();
  asm volatile("ds_read_b64_tr_b16 v[178:179], %0 offset:25344" :  : "v"(aW[1][1][1]) : "memory"); SB();
  asm volatile("v_mfma_f32_16x16x32_bf16 %0, %1, a[152:155], %0" : "+v"(rs) : "v"(ones) : ); SB();
  asm volatile("ds_read_b64_tr_b16 v[180:181], %0 offset:24576" :  : "v"(aW[1][1][2]) : "memory"); SB();
  asm volatile("v_mfma_f32_16x16x32_bf16 %0, a[152:155], a[152:155], %0" : "+v"(gr) :  : ); SB();
  asm volatile("ds_read_b64_tr_b16 v[182:183], %0 offset:25344" :  : "v"(aW[1][1][2]) : "memory"); SB();
  asm volatile("s_waitcnt lgkmcnt(6)\n\tv_mfma_f32_16x16x32_bf16 %0, v[160:163], a[96:99], %0" : "+v"(a2[0][0]) :  : ); SB();
  asm volatile("buffer_load_dword v243, off, %0, %1" :  : "s"(rF), "s"(hpo[1]) : "memory"); SB();
  asm volatile("buffer_load_dwordx4 %0, %1, %2 offen offset:2048 lds" :  : "v"(voK[2]), "s"(rK), "s"(0u) : "memory"); SB();
  asm volatile("s_mov_b32 exec_lo, 0xff\n\ts_mov_b32 exec_hi, 0\n\tbuffer_load_dwordx4 %0, %1, %2 offen offset:3072 lds\n\ts_mov_b64 exec, -1" :  : "v"(voK[3]), "s"(rK), "s"(0u) : "memory"); SB();
  asm volatile("s_waitcnt vmcnt(13)\n\ts_barrier" :  :  : "memory"); SB();
  asm volatile("ds_read_b64_tr_b16 v[184:185], %0 offset:36864" :  : "v"(aW[1][0][0]) : "memory"); SB();
  asm volatile("v_mfma_f32_16x16x32_bf16 %0, v[160:163], a[100:103], %0" : "+v"(a2[1][0]) :  : ); SB();
  asm volatile("ds_read_b64_tr_b16 v[186:187], %0 offset:37632" :  : "v"(aW[1][0][0]) : "memory"); SB();
  asm volatile("v_mfma_f32_16x16x32_bf16 %0, v[160:163], %1, %0" : "+v"(c2[0]) : "v"(ones) : ); SB();
  asm volatile("ds_read_b64_tr_b16 v[188:189], %0 offset:36864" :  : "v"(aW[1][0][1]) : "memory"); SB();
  asm volatile("v_mfma_f32_16x16x32_bf16 %0, v[164:167], a[96:99], %0" : "+v"(a2[0][1]) :  : ); SB();
  asm volatile("ds_read_b64_tr_b16 v[190:191], %0 offset:37632" :  : "v"(aW[1][0][1]) : "memory"); SB();
  asm volatile("v_mfma_f32_16x16x32_bf16 %0, v[164:167], a[100:103], %0" : "+v"(a2[1][1]) :  : ); SB();
  asm volatile("ds_read_b64_tr_b16 v[192:193], %0 offset:36864" :  : "v"(aW[1][0][2]) : "memory"); SB();
  asm volatile("v_mfma_f32_16x16x32_bf16 %0, v[164:167], %1, %0" : "+v"(c2[1]) : "v"(ones) : ); SB();
  asm volatile("ds_read_b64_tr_b16 v[194:195], %0 offset:37632" :  : "v"(aW[1][0][2]) : "memory"); SB();
  asm volatile("v_mfma_f32_16x16x32_bf16 %0, v[168:171], a[96:99], %0" : "+v"(a2[0][2]) :  : ); SB();
  asm volatile("global_load_ushort v246, %0, %1 offset:2" :  : "v"(vD), "s"(ssm_D) : "memory"); SB();
  asm volatile("v_mfma_f32_16x16x32_bf16 %0, v[168:171], a[100:103], %0" : "+v"(a2[1][2]) :  : ); SB();
  asm volatile("s_mov_b32 exec_lo, -1\n\ts_mov_b32 exec_hi, 0xff\n\tbuffer_load_dwordx4 %0, %1, %2 offen offset:3328 lds\n\ts_mov_b64 exec, -1" :  : "v"(voH), "s"(rS), "s"(0u) : "memory"); SB();
  asm volatile("v_mfma_f32_16x16x32_bf16 %0, v[168:171], %1, %0" : "+v"(c2[2]) : "v"(ones) : ); SB();
  asm volatile("s_waitcnt lgkmcnt(6)\n\tv_mfma_f32_16x16x32_bf16 %0, v[172:175], a[104:107], %0" : "+v"(a2[0][0]) :  : ); SB();
  asm volatile("ds_read_b64_tr_b16 v[160:161], %0 offset:36864" :  : "v"(aW[1][1][0]) : "memory"); SB();
  asm volatile("v_cndmask_b32 %0, %1, %2, %3" : "=v"(xt[0]) : "v"(gr[0]), "v"(gr[1]), "s"(0xAAAAAAAAAAAAAAAAull) : ); SB();
  asm volatile("v_mfma_f32_16x16x32_bf16 %0, v[172:175], a[108:111], %0" : "+v"(a2[1][0]) :  : ); SB();
  asm volatile("s_mov_b32 exec_lo, 0x10001\n\ts_mov_b32 exec_hi, 0x10001\n\tds_write_b128 %0, %1 offset:0\n\ts_mov_b64 exec, -1" :  : "v"(ca), "v"(c2[0]) : "memory"); SB();
  asm volatile("ds_read_b64_tr_b16 v[162:163], %0 offset:37632" :  : "v"(aW[1][1][0]) : "memory"); SB();
  asm volatile("s_add_u32 m0, %0, 102400" :  : "s"(mA) : "memory", "scc"); SB();
  asm volatile("v_mfma_f32_16x16x32_bf16 %0, v[176:179], a[104:107], %0" : "+v"(a2[0][1]) :  : ); SB();
  asm volatile("ds_read_b64_tr_b16 v[164:165], %0 offset:36864" :  : "v"(aW[1][1][1]) : "memory"); SB();
  asm volatile("buffer_load_dwordx4 %0, %1, %2 offen lds" :  : "v"(voV[0]), "s"(rV), "s"(0u) : "memory"); SB();
  asm volatile("v_mfma_f32_16x16x32_bf16 %0, v[176:179], a[108:111], %0" : "+v"(a2[1][1]) :  : ); SB();
  asm volatile("s_mov_b32 exec_lo, 0x10001\n\ts_mov_b32 exec_hi, 0x10001\n\tds_write_b128 %0, %1 offset:64\n\ts_mov_b64 exec, -1" :  : "v"(ca), "v"(c2[1]) : "memory"); SB();
  asm volatile("ds_read_b64_tr_b16 v[166:167], %0 offset:37632" :  : "v"(aW[1][1][1]) : "memory"); SB();
  asm volatile("buffer_load_dword v244, off, %0, %1" :  : "s"(rF), "s"(hpo[2]) : "memory"); SB();
  asm volatile("v_mfma_f32_16x16x32_bf16 %0, v[180:183], a[104:107], %0" : "+v"(a2[0][2]) :  : ); SB();
  asm volatile("ds_read_b64_tr_b16 v[168:169], %0 offset:36864" :  : "v"(aW[1][1][2]) : "memory"); SB();
  asm volatile("buffer_load_dwordx4 %0, %1, %2 offen offset:1024 lds" :  : "v"(voV[1]), "s"(rV), "s"(0u) : "memory"); SB();
  asm volatile("v_mfma_f32_16x16x32_bf16 %0, v[180:183], a[108:111], %0" : "+v"(a2[1][2]) :  : ); SB();
  asm volatile("s_mov_b32 exec_lo, 0x10001\n\ts_mov_b32 exec_hi, 0x10001\n\tds_write_b128 %0, %1 offset:128\n\ts_mov_b64 exec, -1" :  : "v"(ca), "v"(c2[2]) : "memory"); SB();
  asm volatile("ds_read_b64_tr_b16 v[170:171], %0 offset:37632" :  : "v"(aW[1][1][2]) : "memory"); SB();
  asm volatile("s_waitcnt lgkmcnt(5)\n\tv_mfma_f32_16x16x32_bf16 %0, v[184:187], a[112:115], %0" : "+v"(a2[0][0]) :  : ); SB();
  asm volatile("s_mov_b32 exec_lo, -1\n\ts_mov_b32 exec_hi, 0\n\tbuffer_load_dwordx4 %0, %1, %2 offen offset:2048 lds\n\ts_mov_b64 exec, -1" :  : "v"(voV[2]), "s"(rV), "s"(0u) : "memory"); SB();
  asm volatile("v_cndmask_b32 %0, %1, %2, %3" : "=v"(xt[1]) : "v"(gr[2]), "v"(gr[3]), "s"(0xAAAAAAAAAAAAAAAAull) : ); SB();
  asm volatile("v_mfma_f32_16x16x32_bf16 %0, v[184:187], a[116:119], %0" : "+v"(a2[1][0]) :  : ); SB();
  asm volatile("buffer_load_dwordx4 %0, %1, %2 offen offset:3072 lds" :  : "v"(voO), "s"(rO), "s"(0u) : "memory"); SB();
  asm volatile("v_mul_f32 %0, 0x3acccccd, %1" : "=v"(xs[0][0]) : "v"(rs[0]) : ); SB();
  asm volatile("v_mfma_f32_16x16x32_bf16 %0, v[184:187], %1, %0" : "+v"(c2[0]) : "v"(ones) : ); SB();
  asm volatile("global_load_ushort v247, %0, %1 offset:4" :  : "v"(vD2), "s"(ssm_D) : "memory"); SB();
  asm volatile("v_cndmask_b32 %0, %0, %1, %2" : "+v"(xt[0]) : "v"(xt[1]), "s"(0xCCCCCCCCCCCCCCCCull) : ); SB();
  asm volatile("v_mfma_f32_16x16x32_bf16 %0, v[188:191], a[112:115], %0" : "+v"(a2[0][1]) :  : ); SB();
  asm volatile("s_waitcnt vmcnt(18)\n\ts_barrier" :  :  : "memory"); SB();
  asm volatile("ds_read_b64_tr_b16 v[172:173], %0 offset:0" :  : "v"(aW[0][0][0]) : "memory"); SB();
  asm volatile("v_mul_f32 %0, 0x3a83126f, %1" : "=v"(xt[1]) : "v"(rs[0]) : ); SB();
  asm volatile("v_mfma_f32_16x16x32_bf16 %0, v[188:191], a[116:119], %0" : "+v"(a2[1][1]) :  : ); SB();
  asm volatile("ds_read_b64_tr_b16 v[174:175], %0 offset:768" :  : "v"(aW[0][0][0]) : "memory"); SB();
  asm volatile("v_fma_f32 %0, -%1, %2, %3" : "=v"(xs[0][1]) : "v"(rs[0]), "v"(xs[0][0]), "v"(xt[0]) : ); SB();
  asm volatile("v_mfma_f32_16x16x32_bf16 %0, v[188:191], %1, %0" : "+v"(c2[1]) : "v"(ones) : ); SB();
  asm volatile("ds_read_b64_tr_b16 v[176:177], %0 offset:0" :  : "v"(aW[0][0][1]) : "memory"); SB();
  asm volatile("v_mul_f32 %0, %0, %1" : "+v"(xt[1]) : "v"(xs[0][0]) : ); SB();
  asm volatile("v_mfma_f32_16x16x32_bf16 %0, v[192:195], a[112:115], %0" : "+v"(a2[0][2]) :  : ); SB();
  asm volatile("ds_read_b64_tr_b16 v[178:179], %0 offset:768" :  : "v"(aW[0][0][1]) : "memory"); SB();
  asm volatile("v_mfma_f32_16x16x32_bf16 %0, v[192:195], a[116:119], %0" : "+v"(a2[1][2]) :  : ); SB();
  asm volatile("ds_read_b64_tr_b16 v[180:181], %0 offset:0" :  : "v"(aW[0][0][2]) : "memory"); SB();
  asm volatile("v_mfma_f32_16x16x32_bf16 %0, v[192:195], %1, %0" : "+v"(c2[2]) : "v"(ones) : ); SB();
  asm volatile("ds_read_b64_tr_b16 v[182:183], %0 offset:768" :  : "v"(aW[0][0][2]) : "memory"); SB();
  asm volatile("v_mfma_f32_16x16x32_bf16 %0, v[160:163], a[120:123], %0" : "+v"(a2[0][0]) :  : ); SB();
  if (__builtin_expect((__builtin_amdgcn_ballot_w64(!(xs[0][1] >= xt[1])) & 0xF0000F0000F0000Full) != 0, 0)) {
    const f32x2 mm = {xs[0][0], xs[0][0]};
    f32x2 q2 = {0.f, 0.f};
    const u32x4* xp = (const u32x4*)(x + (size_t)(rb * 64 + w * 16 + l15) * 1280 + g4 * 320);
#pragma unroll 1
    for (int c = 0; c < 20; ++c) {
      u32x4 xq;
      asm volatile("global_load_dwordx4 %0, %1, off\n\ts_waitcnt vmcnt(0)" : "=&v"(xq) : "v"(xp + c) : "memory");
#pragma unroll
      for (int e = 0; e < 4; ++e) { const f32x2 d = unpk(xq[e]) - mm; q2 = __builtin_elementwise_fma(d, d, q2); }
    }
    xs[0][1] = red32(red16(q2[0] + q2[1]));
  }
  SB();
  asm volatile("v_mfma_f32_16x16x32_bf16 %0, v[160:163], a[124:127], %0" : "+v"(a2[1][0]) :  : ); SB();
  asm volatile("global_load_ushort v248, %0, %1" :  : "v"(vBs), "s"(b_sp) : "memory"); SB();
  asm volatile("v_max_f32 %0, 0, %0" : "+v"(xs[0][1]) :  : ); SB();
  asm volatile("v_mfma_f32_16x16x32_bf16 %0, v[160:163], %1, %0" : "+v"(c2[0]) : "v"(ones) : ); SB();
  asm volatile("buffer_load_dwordx4 v[228:231], %0, %1, %2 offen" :  : "v"(vF), "s"(rF), "s"(0u) : "memory"); SB();
  asm volatile("s_waitcnt lgkmcnt(5)\n\tv_mfma_f32_16x16x32_bf16 %0, v[164:167], a[120:123], %0" : "+v"(a2[0][1]) :  : ); SB();
  asm volatile("ds_read_b64_tr_b16 v[184:185], %0 offset:0" :  : "v"(aW[0][1][0]) : "memory"); SB();
  asm volatile("v_mul_f32 %0, 0x3acccccd, %0" : "+v"(xs[0][1]) :  : ); SB();
  asm volatile("v_mfma_f32_16x16x32_bf16 %0, v[164:167], a[124:127], %0" : "+v"(a2[1][1]) :  : ); SB();
  asm volatile("ds_read_b64_tr_b16 v[186:187], %0 offset:768" :  : "v"(aW[0][1][0]) : "memory"); SB();
  asm volatile("v_cvt_pk_bf16_f32 %0, %0, %0" : "+v"(xs[0][1]) :  : ); SB();
  asm volatile("v_mfma_f32_16x16x32_bf16 %0, v[164:167], %1, %0" : "+v"(c2[1]) : "v"(ones) : ); SB();
  asm volatile("ds_read_b64_tr_b16 v[188:189], %0 offset:0" :  : "v"(aW[0][1][1]) : "memory"); SB();
  asm volatile("v_lshlrev_b32 %0, 16, %0" : "+v"(xs[0][1]) :  : ); SB();
  asm volatile("v_mfma_f32_16x16x32_bf16 %0, v[168:171], a[120:123], %0" : "+v"(a2[0][2]) :  : ); SB();
  asm volatile("ds_read_b64_tr_b16 v[190:191], %0 offset:768" :  : "v"(aW[0][1][1]) : "memory"); SB();
  asm volatile("v_add_f32 %0, 0x3727c5ac, %0" : "+v"(xs[0][1]) :  : ); SB();
  asm volatile("v_mfma_f32_16x16x32_bf16 %0, v[168:171], a[124:127], %0" : "+v"(a2[1][2]) :  : ); SB();
  asm volatile("ds_read_b64_tr_b16 v[192:193], %0 offset:0" :  : "v"(aW[0][1][2]) : "memory"); SB();
  asm volatile("v_cvt_pk_bf16_f32 %0, %0, %0" : "+v"(xs[0][1]) :  : ); SB();
  asm volatile("v_mfma_f32_16x16x32_bf16 %0, v[168:171], %1, %0" : "+v"(c2[2]) : "v"(ones) : ); SB();
  asm volatile("ds_read_b64_tr_b16 v[194:195], %0 offset:768" :  : "v"(aW[0][1][2]) : "memory"); SB();
  asm volatile("s_waitcnt lgkmcnt(6)\n\tv_mfma_f32_16x16x32_bf16 %0, v[172:175], a[128:131], %0" : "+v"(a2[0][0]) :  : ); SB();
  asm volatile("buffer_load_dwordx4 v[232:235], %0, %1, %2 offen" :  : "v"(vF), "s"(rF), "s"(4096u) : "memory"); SB();
  asm volatile("v_lshlrev_b32 %0, 16, %0" : "+v"(xs[0][1]) :  : ); SB();
  asm volatile("v_mfma_f32_16x16x32_bf16 %0, v[172:175], a[132:135], %0" : "+v"(a2[1][0]) :  : ); SB();
  asm volatile("buffer_load_dwordx4 v[236:239], %0, %1, %2 offen" :  : "v"(vF), "s"(rF), "s"(c2s_) : "memory"); SB();
  asm volatile("v_mfma_f32_16x16x32_bf16 %0, v[172:175], %1, %0" : "+v"(c2[0]) : "v"(ones) : ); SB();
  asm volatile("v_sqrt_f32 %0, %0" : "+v"(xs[0][1]) :  : ); SB();
  asm volatile("v_mfma_f32_16x16x32_bf16 %0, v[176:179], a[128:131], %0" : "+v"(a2[0][1]) :  : ); SB();
  asm volatile("v_cvt_pk_bf16_f32 %0, %0, %0" : "+v"(xs[0][0]) :  : ); SB();
  asm volatile("v_lshlrev_b32 %0, 16, %0" : "+v"(xs[0][0]) :  : ); SB();
  asm volatile("v_cvt_pk_bf16_f32 %0, %0, %0" : "+v"(xs[0][1]) :  : ); SB();
  asm volatile("v_lshlrev_b32 %0, 16, %0" : "+v"(xs[0][1]) :  : ); SB();
  asm volatile("v_rcp_f32 %0, %1" : "=v"(xt[0]) : "v"(xs[0][1]) : ); SB();
  asm volatile("s_nop 0\n\tv_mul_f32 %0, 0xc0135761, %1" : "=v"(xs[0][2]) : "v"(xt[0]) : ); SB();
  asm volatile("v_mul_f32 %0, %1, %1" : "=v"(xt[1]) : "v"(xt[0]) : ); SB();
  asm volatile("v_mul_f32 %0, 0xbdd2d3e8, %0" : "+v"(xt[0]) :  : ); SB();
  asm volatile("v_mul_f32 %0, %1, %2" : "=v"(xs[0][3]) : "v"(xt[0]), "v"(xt[1]) : ); SB();
  asm volatile("s_mov_b32 exec_lo, 0xf0000f\n\ts_mov_b32 exec_hi, 0xf0000f00\n\tds_write_b128 %0, %1\n\ts_mov_b64 exec, -1" :  : "v"(xw), "v"(xs[0]) : "memory"); SB();
  asm volatile("s_waitcnt vmcnt(19) lgkmcnt(0)\n\ts_barrier" :  :  : "memory"); SB();
  asm volatile("ds_read_b64_tr_b16 v[160:161], %0 offset:12288" :  : "v"(aW[0][0][0]) : "memory"); SB();
  asm volatile("v_mfma_f32_16x16x32_bf16 %0, v[176:179], a[132:135], %0" : "+v"(a2[1][1]) :  : ); SB();
  asm volatile("ds_read_b64_tr_b16 v[162:163], %0 offset:13056" :  : "v"(aW[0][0][0]) : "memory"); SB();
  asm volatile("v_mfma_f32_16x16x32_bf16 %0, v[176:179], %1, %0" : "+v"(c2[1]) : "v"(ones) : ); SB();
  asm volatile("ds_read_b64_tr_b16 v[164:165], %0 offset:12288" :  : "v"(aW[0][0][1]) : "memory"); SB();
  asm volatile("v_mfma_f32_16x16x32_bf16 %0, v[180:183], a[128:131], %0" : "+v"(a2[0][2]) :  : ); SB();
  asm volatile("ds_read_b64_tr_b16 v[166:167], %0 offset:13056" :  : "v"(aW[0][0][1]) : "memory"); SB();
  asm volatile("v_mfma_f32_16x16x32_bf16 %0, v[180:183], a[132:135], %0" : "+v"(a2[1][2]) :  : ); SB();
  asm volatile("ds_read_b64_tr_b16 v[168:169], %0 offset:12288" :  : "v"(aW[0][0][2]) : "memory"); SB();
  asm volatile("v_mfma_f32_16x16x32_bf16 %0, v[180:183], %1, %0" : "+v"(c2[2]) : "v"(ones) : ); SB();
  asm volatile("ds_read_b64_tr_b16 v[170:171], %0 offset:13056" :  : "v"(aW[0][0][2]) : "memory"); SB();
  asm volatile("v_mfma_f32_16x16x32_bf16 %0, v[184:187], a[136:139], %0" : "+v"(a2[0][0]) :  : ); SB();
  asm volatile("ds_read_b128 %0, %1 offset:0" : "=&v"(xs[0]) : "v"(xw) : "memory"); SB();
  asm volatile("v_mfma_f32_16x16x32_bf16 %0, v[184:187], a[140:143], %0" : "+v"(a2[1][0]) :  : ); SB();
  asm volatile("ds_read_b128 %0, %1 offset:0" : "=&v"(xs[1]) : "v"(xr_) : "memory"); SB();
  asm volatile("v_mfma_f32_16x16x32_bf16 %0, v[184:187], %1, %0" : "+v"(c2[0]) : "v"(ones) : ); SB();
  asm volatile("ds_read_b128 %0, %1 offset:0" : "=&v"(co[0]) : "v"(cr) : "memory"); SB();
  asm volatile("v_mfma_f32_16x16x32_bf16 %0, v[188:191], a[136:139], %0" : "+v"(a2[0][1]) :  : ); SB();
  asm volatile("ds_read_b64_tr_b16 v[172:173], %0 offset:12288" :  : "v"(aW[0][1][0]) : "memory"); SB();
  asm volatile("v_mfma_f32_16x16x32_bf16 %0, v[188:191], a[140:143], %0" : "+v"(a2[1][1]) :  : ); SB();
  asm volatile("ds_read_b64_tr_b16 v[174:175], %0 offset:13056" :  : "v"(aW[0][1][0]) : "memory"); SB();
  asm volatile("v_mfma_f32_16x16x32_bf16 %0, v[188:191], %1, %0" : "+v"(c2[1]) : "v"(ones) : ); SB();
  asm volatile("ds_read_b64_tr_b16 v[176:177], %0 offset:12288" :  : "v"(aW[0][1][1]) : "memory"); SB();
  asm volatile("v_mfma_f32_16x16x32_bf16 %0, v[192:195], a[136:139], %0" : "+v"(a2[0][2]) :  : ); SB();
  asm volatile("ds_read_b64_tr_b16 v[178:179], %0 offset:13056" :  : "v"(aW[0][1][1]) : "memory"); SB();
  asm volatile("v_mfma_f32_16x16x32_bf16 %0, v[192:195], a[140:143], %0" : "+v"(a2[1][2]) :  : ); SB();
  asm volatile("ds_read_b64_tr_b16 v[180:181], %0 offset:12288" :  : "v"(aW[0][1][2]) : "memory"); SB();
  asm volatile("v_mfma_f32_16x16x32_bf16 %0, v[192:195], %1, %0" : "+v"(c2[2]) : "v"(ones) : ); SB();
  asm volatile("ds_read_b64_tr_b16 v[182:183], %0 offset:13056" :  : "v"(aW[0][1][2]) : "memory"); SB();
  asm volatile("s_waitcnt lgkmcnt(9)\n\tv_mfma_f32_16x16x32_bf16 %0, v[160:163], a[144:147], %0" : "+v"(a2[0][0]) :  : ); SB();
  asm volatile("ds_read_b128 %0, %1 offset:64" : "=&v"(co[1]) : "v"(cr) : "memory"); SB();
  asm volatile("v_mfma_f32_16x16x32_bf16 %0, v[160:163], a[148:151], %0" : "+v"(a2[1][0]) :  : ); SB();
  asm volatile("ds_read_b128 %0, %1 offset:128" : "=&v"(co[2]) : "v"(cr) : "memory"); SB();
  asm volatile("v_mfma_f32_16x16x32_bf16 %0, v[160:163], %1, %0" : "+v"(c2[0]) : "v"(ones) : ); SB();
  asm volatile("v_mfma_f32_16x16x32_bf16 %0, v[164:167], a[144:147], %0" : "+v"(a2[0][1]) :  : ); SB();
  asm volatile("v_mfma_f32_16x16x32_bf16 %0, v[164:167], a[148:151], %0" : "+v"(a2[1][1]) :  : ); SB();
  asm volatile("v_mfma_f32_16x16x32_bf16 %0, v[164:167], %1, %0" : "+v"(c2[1]) : "v"(ones) : ); SB();
  asm volatile("v_mfma_f32_16x16x32_bf16 %0, v[168:171], a[144:147], %0" : "+v"(a2[0][2]) :  : ); SB();
  asm volatile("v_mfma_f32_16x16x32_bf16 %0, v[168:171], a[148:151], %0" : "+v"(a2[1][2]) :  : ); SB();
  asm volatile("v_mfma_f32_16x16x32_bf16 %0, v[168:171], %1, %0" : "+v"(c2[2]) : "v"(ones) : ); SB();
  asm volatile("s_waitcnt lgkmcnt(3)\n\tv_mfma_f32_16x16x32_bf16 %0, v[172:175], a[152:155], %0" : "+v"(a2[0][0]) :  : ); SB();
  asm volatile("v_mfma_f32_16x16x32_bf16 %0, v[172:175], a[156:159], %0" : "+v"(a2[1][0]) :  : ); SB();
  asm volatile("v_mfma_f32_16x16x32_bf16 %0, v[172:175], %1, %0" : "+v"(c2[0]) : "v"(ones) : ); SB();
  asm volatile("s_add_u32 m0, %0, 24576" :  : "s"(mW) : "memory", "scc"); SB();
  asm volatile("v_mfma_f32_16x16x32_bf16 %0, v[176:179], a[152:155], %0" : "+v"(a2[0][1]) :  : ); SB();
  asm volatile("buffer_load_dwordx4 %0, %1, %2 offen lds" :  : "v"(voB[0]), "s"(rW1), "s"(kw_[0]) : "memory"); SB();
  asm volatile("v_mfma_f32_16x16x32_bf16 %0, v[176:179], a[156:159], %0" : "+v"(a2[1][1]) :  : ); SB();
  asm volatile("v_mfma_f32_16x16x32_bf16 %0, v[176:179], %1, %0" : "+v"(c2[1]) : "v"(ones) : ); SB();
  asm volatile("s_waitcnt lgkmcnt(0)\n\tv_mfma_f32_16x16x32_bf16 %0, v[180:183], a[152:155], %0" : "+v"(a2[0][2]) :  : ); SB();
  asm volatile("v_mfma_f32_16x16x32_bf16 %0, v[180:183], a[156:159], %0" : "+v"(a2[1][2]) :  : ); SB();
  asm volatile("v_mfma_f32_16x16x32_bf16 %0, v[180:183], %1, %0" : "+v"(c2[2]) : "v"(ones) : ); SB();
  ST(3);
  ST(5);
  const unsigned long long tg = ((unsigned long long)tg1 << 32) | tg0;
  asm volatile("" : "+v"(xs[0]), "+v"(xs[1]), "+v"(co[0]), "+v"(co[1]), "+v"(co[2]) :  : "memory"); SB();
  asm volatile("s_nop 3" : "+v"(a2[0][0]), "+v"(a2[0][1]), "+v"(a2[0][2]), "+v"(a2[1][0]), "+v"(a2[1][1]), "+v"(a2[1][2]), "+v"(c2[0]), "+v"(c2[1]), "+v"(c2[2]) :  : ); SB();
  for (int i = 0; i < 2; ++i) { xm[i] = xs[i][0]; xd[i] = xs[i][1]; k1[i] = xs[i][2]; k2[i] = xs[i][3]; }
  c2[0] += co[0]; c2[1] += co[1]; c2[2] += co[2];
  f32x2 ge2[2];
  auto act = [&](f32x2& t, f32x2& e, const f32x4& a, const f32x4& cs, int i, int h) {
    t = __builtin_elementwise_fma((f32x2){-xm[i], -xm[i]}, (f32x2){cs[2 * h], cs[2 * h + 1]}, (f32x2){a[2 * h], a[2 * h + 1]});
    const f32x2 u = t * __builtin_elementwise_fma(t * t, (f32x2){k2[i], k2[i]}, (f32x2){k1[i], k1[i]});
    e = (f32x2){__builtin_amdgcn_exp2f(u[0]), __builtin_amdgcn_exp2f(u[1])};
  };
  auto act2 = [&](f32x2& t, f32x2 e, int i) {
    e = __builtin_elementwise_fma(e, (f32x2){xd[i], xd[i]}, (f32x2){xd[i], xd[i]});
    t *= (f32x2){__builtin_amdgcn_rcpf(e[0]), __builtin_amdgcn_rcpf(e[1])};
  };
  f32x2 gv[2][3][2];
  float sv[2][2], pv[2];
  act(gv[0][0][0], ge2[0], a2[0][0], c2[0], 0, 0); act(gv[0][0][1], ge2[1], a2[0][0], c2[0], 0, 1);
  asm volatile("" : "+v"(gv[0][0][0]), "+v"(gv[0][0][1]), "+v"(ge2[0]), "+v"(ge2[1]));
  SB();
  asm volatile("buffer_load_dwordx4 %0, %1, %2 offen offset:1024 lds" :  : "v"(voB[1]), "s"(rW1), "s"(kw_[0]) : "memory"); SB();
  asm volatile("buffer_load_dwordx4 %0, %1, %2 offen offset:2048 lds" :  : "v"(voB[2]), "s"(rW1), "s"(kw_[0]) : "memory"); SB();
  act2(gv[0][0][0], ge2[0], 0); act2(gv[0][0][1], ge2[1], 0);
  asm volatile("" : "+v"(gv[0][0][0]), "+v"(gv[0][0][1]));
  SB();
  asm volatile("s_add_u32 m0, %0, 36864" :  : "s"(mW) : "memory", "scc"); SB();
  asm volatile("s_nop 0\n\tbuffer_load_dwordx4 %0, %1, %2 offen lds" :  : "v"(voB[0]), "s"(rW1), "s"(kw_[1]) : "memory"); SB();
  act(gv[0][1][0], ge2[0], a2[0][1], c2[1], 0, 0); act(gv[0][1][1], ge2[1], a2[0][1], c2[1], 0, 1);
  asm volatile("" : "+v"(gv[0][1][0]), "+v"(gv[0][1][1]), "+v"(ge2[0]), "+v"(ge2[1]));
  SB();
  asm volatile("buffer_load_dwordx4 %0, %1, %2 offen offset:1024 lds" :  : "v"(voB[1]), "s"(rW1), "s"(kw_[1]) : "memory"); SB();
  asm volatile("buffer_load_dwordx4 %0, %1, %2 offen offset:2048 lds" :  : "v"(voB[2]), "s"(rW1), "s"(kw_[1]) : "memory"); SB();
  act2(gv[0][1][0], ge2[0], 0); act2(gv[0][1][1], ge2[1], 0);
  asm volatile("" : "+v"(gv[0][1][0]), "+v"(gv[0][1][1]));
  SB();
  asm volatile("s_add_u32 m0, %0, 49152" :  : "s"(mW) : "memory", "scc"); SB();
  asm volatile("s_nop 0\n\tbuffer_load_dwordx4 %0, %1, %2 offen lds" :  : "v"(voB[0]), "s"(rW1), "s"(kw_[2]) : "memory"); SB();
  act(gv[0][2][0], ge2[0], a2[0][2], c2[2], 0, 0); act(gv[0][2][1], ge2[1], a2[0][2], c2[2], 0, 1);
  asm volatile("" : "+v"(gv[0][2][0]), "+v"(gv[0][2][1]), "+v"(ge2[0]), "+v"(ge2[1]));
  SB();
  act2(gv[0][2][0], ge2[0], 0); act2(gv[0][2][1], ge2[1], 0);
  asm volatile("" : "+v"(gv[0][2][0]), "+v"(gv[0][2][1]));
  SB();
  asm volatile("buffer_load_dwordx4 %0, %1, %2 offen offset:1024 lds" :  : "v"(voB[1]), "s"(rW1), "s"(kw_[2]) : "memory"); SB();
  act(gv[1][0][0], ge2[0], a2[1][0], c2[0], 1, 0); act(gv[1][0][1], ge2[1], a2[1][0], c2[0], 1, 1);
  asm volatile("" : "+v"(gv[1][0][0]), "+v"(gv[1][0][1]), "+v"(ge2[0]), "+v"(ge2[1]));
  SB();
  asm volatile("buffer_load_dwordx4 %0, %1, %2 offen offset:2048 lds" :  : "v"(voB[2]), "s"(rW1), "s"(kw_[2]) : "memory"); SB();
  act2(gv[1][0][0], ge2[0], 1); act2(gv[1][0][1], ge2[1], 1);
  asm volatile("" : "+v"(gv[1][0][0]), "+v"(gv[1][0][1]));
  SB();
  act(gv[1][1][0], ge2[0], a2[1][1], c2[1], 1, 0); act(gv[1][1][1], ge2[1], a2[1][1], c2[1], 1, 1);
  asm volatile("" : "+v"(gv[1][1][0]), "+v"(gv[1][1][1]), "+v"(ge2[0]), "+v"(ge2[1]));
  SB();
  asm volatile("s_add_u32 m0, %0, 61440" :  : "s"(mW) : "memory", "scc"); SB();
  asm volatile("s_nop 0\n\tbuffer_load_dwordx4 %0, %1, %2 offen lds" :  : "v"(voB[0]), "s"(rW1), "s"(kw_[3]) : "memory"); SB();
  asm volatile("buffer_load_dwordx4 %0, %1, %2 offen offset:1024 lds" :  : "v"(voB[1]), "s"(rW1), "s"(kw_[3]) : "memory"); SB();
  act2(gv[1][1][0], ge2[0], 1); act2(gv[1][1][1], ge2[1], 1);
  asm volatile("" : "+v"(gv[1][1][0]), "+v"(gv[1][1][1]));
  SB();
  asm volatile("buffer_load_dwordx4 %0, %1, %2 offen offset:2048 lds" :  : "v"(voB[2]), "s"(rW1), "s"(kw_[3]) : "memory"); SB();
  act(gv[1][2][0], ge2[0], a2[1][2], c2[2], 1, 0); act(gv[1][2][1], ge2[1], a2[1][2], c2[2], 1, 1);
  asm volatile("" : "+v"(gv[1][2][0]), "+v"(gv[1][2][1]), "+v"(ge2[0]), "+v"(ge2[1]));
  SB();
  act2(gv[1][2][0], ge2[0], 1); act2(gv[1][2][1], ge2[1], 1);
  asm volatile("" : "+v"(gv[1][2][0]), "+v"(gv[1][2][1]));
  SB();
#pragma unroll
  for (int i = 0; i < 2; ++i) {
    const unsigned p0 = __float_as_uint(gv[i][0][0][0]);
    const auto pa = __builtin_amdgcn_permlane16_swap(p0, p0, false, false);
    const auto pb = __builtin_amdgcn_permlane32_swap(pa[0], pa[0], false, false);
    pv[i] = __uint_as_float(pb[0]);
  }
  asm volatile("" : "+v"(pv[0]), "+v"(pv[1]));
  f32x2 s1[2], s2[2];
  SB();
  asm volatile("s_add_u32 m0, %0, 73728" :  : "s"(mW) : "memory", "scc"); SB();
  asm volatile("s_nop 0\n\tbuffer_load_dwordx4 %0, %1, %2 offen lds" :  : "v"(voB[0]), "s"(rW1), "s"(kw_[4]) : "memory"); SB();
#pragma unroll
  for (int m = 0; m < 4; ++m) {
    const int i = m & 1, n = 0 + (m >> 1);
    const f32x2 d = gv[i][n >> 1][n & 1] - (f32x2){pv[i], pv[i]};
    s1[i] = n ? s1[i] + d : d; s2[i] = n ? __builtin_elementwise_fma(d, d, s2[i]) : d * d;
  }
  asm volatile("" : "+v"(s1[0]), "+v"(s1[1]), "+v"(s2[0]), "+v"(s2[1]));
  SB();
  asm volatile("buffer_load_dwordx4 %0, %1, %2 offen offset:1024 lds" :  : "v"(voB[1]), "s"(rW1), "s"(kw_[4]) : "memory"); SB();
#pragma unroll
  for (int m = 0; m < 4; ++m) {
    const int i = m & 1, n = 2 + (m >> 1);
    const f32x2 d = gv[i][n >> 1][n & 1] - (f32x2){pv[i], pv[i]};
    s1[i] = n ? s1[i] + d : d; s2[i] = n ? __builtin_elementwise_fma(d, d, s2[i]) : d * d;
  }
  asm volatile("" : "+v"(s1[0]), "+v"(s1[1]), "+v"(s2[0]), "+v"(s2[1]));
  SB();
  asm volatile("buffer_load_dwordx4 %0, %1, %2 offen offset:2048 lds" :  : "v"(voB[2]), "s"(rW1), "s"(kw_[4]) : "memory"); SB();
#pragma unroll
  for (int m = 0; m < 4; ++m) {
    const int i = m & 1, n = 4 + (m >> 1);
    const f32x2 d = gv[i][n >> 1][n & 1] - (f32x2){pv[i], pv[i]};
    s1[i] = n ? s1[i] + d : d; s2[i] = n ? __builtin_elementwise_fma(d, d, s2[i]) : d * d;
  }
  asm volatile("" : "+v"(s1[0]), "+v"(s1[1]), "+v"(s2[0]), "+v"(s2[1]));
  SB();
  asm volatile("s_add_u32 m0, %0, 86016" :  : "s"(mW) : "memory", "scc"); SB();
  asm volatile("s_nop 0\n\tbuffer_load_dwordx4 %0, %1, %2 offen lds" :  : "v"(voB[0]), "s"(rW1), "s"(kw_[5]) : "memory"); SB();
  for (int i = 0; i < 2; ++i) { sv[i][0] = s1[i][0] + s1[i][1]; sv[i][1] = s2[i][0] + s2[i][1]; }
  {
    const float t1 = red16(sel32(sv[0][0], sv[1][0])), t2 = red16(sel32(sv[0][1], sv[1][1]));
    const float dl = t1 * (1.0f / 48.0f);
    const u32x4 so = {__float_as_uint((lane < 32 ? pv[0] : pv[1]) + dl), __float_as_uint(fmaxf(t2 - t1 * dl, 0.f)), tg0, tg1};
    SB();
  asm volatile("s_mov_b32 exec_lo, 0xffff\n\ts_mov_b32 exec_hi, 0xffff\n\tbuffer_store_dwordx4 %1, %0, %2, %3 offen offset:0 sc1\n\ts_mov_b64 exec, -1\n\ts_nop 0" :  : "v"(vo), "v"(so), "s"(rWS), "s"(0u) : "memory"); SB();
  }
  ST(6);
  unsigned zp[2][3][2];
#pragma unroll
  for (int n = 0; n < 12; ++n) { zp[n / 6][(n >> 1) % 3][n & 1] = pk2(gv[n / 6][(n >> 1) % 3][n & 1]); asm volatile("" : "+v"(zp[n / 6][(n >> 1) % 3][n & 1])); }
  SB();
  asm volatile("s_waitcnt vmcnt(22)\n\ts_barrier" :  :  : "memory"); SB();
  ST(2);
  asm volatile("ds_read_b64_tr_b16 %0, %1 offset:0" : "=&v"(wt[0]) : "v"(oa) : "memory"); SB();
  asm volatile("ds_read_b64_tr_b16 %0, %1 offset:512" : "=&v"(wt[1]) : "v"(oa) : "memory"); SB();
  asm volatile("ds_read_b64_tr_b16 %0, %1 offset:0" : "=&v"(kp[0]) : "v"(ka) : "memory"); SB();
  asm volatile("ds_read_b64_tr_b16 %0, %1 offset:640" : "=&v"(kc[0]) : "v"(ka) : "memory"); SB();
  asm volatile("ds_read_b64_tr_b16 %0, %1 offset:4096" : "=&v"(vv[0]) : "v"(ka) : "memory"); SB();
  asm volatile("ds_read_b64 %0, %1 offset:0" : "=&v"(sw[0]) : "v"(sa) : "memory"); SB();
  asm volatile("buffer_load_dwordx4 %0, %1, %2 offen offset:1024 lds" :  : "v"(voB[1]), "s"(rW1), "s"(kw_[5]) : "memory"); SB();
  asm volatile("ds_read_b64_tr_b16 %0, %1 offset:32" : "=&v"(kp[1]) : "v"(ka) : "memory"); SB();
  asm volatile("ds_read_b64_tr_b16 %0, %1 offset:672" : "=&v"(kc[1]) : "v"(ka) : "memory"); SB();
  asm volatile("ds_read_b64_tr_b16 %0, %1 offset:4128" : "=&v"(vv[1]) : "v"(ka) : "memory"); SB();
  asm volatile("ds_read_b64 %0, %1 offset:128" : "=&v"(sw[1]) : "v"(sa) : "memory"); SB();
  asm volatile("buffer_load_dwordx4 %0, %1, %2 offen offset:2048 lds" :  : "v"(voB[2]), "s"(rW1), "s"(kw_[5]) : "memory"); SB();
  asm volatile("ds_read_b64_tr_b16 %0, %1 offset:64" : "=&v"(kp[2]) : "v"(ka) : "memory"); SB();
  asm volatile("ds_read_b64_tr_b16 %0, %1 offset:704" : "=&v"(kc[2]) : "v"(ka) : "memory"); SB();
  asm volatile("ds_read_b64_tr_b16 %0, %1 offset:4160" : "=&v"(vv[2]) : "v"(ka) : "memory"); SB();
  asm volatile("ds_read_b64 %0, %1 offset:256" : "=&v"(sw[2]) : "v"(sa) : "memory"); SB();
  asm volatile("s_add_u32 m0, %0, 0" :  : "s"(mW) : "memory", "scc"); SB();
  asm volatile("s_nop 0\n\tbuffer_load_dwordx4 %0, %1, %2 offen lds" :  : "v"(voB[0]), "s"(rW1), "s"(kw_[6]) : "memory"); SB();
  asm volatile("ds_read_b64_tr_b16 %0, %1 offset:96" : "=&v"(kp[3]) : "v"(ka) : "memory"); SB();
  asm volatile("ds_read_b64_tr_b16 %0, %1 offset:736" : "=&v"(kc[3]) : "v"(ka) : "memory"); SB();
  asm volatile("ds_read_b64_tr_b16 %0, %1 offset:4192" : "=&v"(vv[3]) : "v"(ka) : "memory"); SB();
  asm volatile("ds_read_b64 %0, %1 offset:384" : "=&v"(sw[3]) : "v"(sa) : "memory"); SB();
  asm volatile("buffer_load_dwordx4 %0, %1, %2 offen offset:1024 lds" :  : "v"(voB[1]), "s"(rW1), "s"(kw_[6]) : "memory"); SB();
  asm volatile("ds_read_b64_tr_b16 %0, %1 offset:128" : "=&v"(kp[4]) : "v"(ka) : "memory"); SB();
  asm volatile("ds_read_b64_tr_b16 %0, %1 offset:768" : "=&v"(kc[4]) : "v"(ka) : "memory"); SB();
  asm volatile("ds_read_b64_tr_b16 %0, %1 offset:4224" : "=&v"(vv[4]) : "v"(ka) : "memory"); SB();
  asm volatile("ds_read_b64 %0, %1 offset:512" : "=&v"(sw[4]) : "v"(sa) : "memory"); SB();
  asm volatile("s_waitcnt lgkmcnt(8)" : "+v"(kp[0]), "+v"(kc[0]), "+v"(vv[0]), "+v"(sw[0]), "+v"(wt[0]), "+v"(wt[1]) :  : "memory"); SB();
  asm volatile("v_alignbit_b32 %4, %14, %14, 16\n\tv_alignbit_b32 %5, %10, %9, 16\n\tv_alignbit_b32 %6, %11, %10, 16\n\tv_alignbit_b32 %7, %13, %13, 16\n\tv_alignbit_b32 %8, %12, %11, 16\n\tv_dot2_f32_bf16 %0, %5, %4, 0\n\tv_dot2_f32_bf16 %1, %10, %4, 0\n\tv_dot2_f32_bf16 %2, %6, %4, 0\n\tv_dot2_f32_bf16 %3, %11, %4, 0\n\tv_dot2_f32_bf16 %0, %6, %7, %0\n\tv_dot2_f32_bf16 %1, %11, %7, %1\n\tv_dot2_f32_bf16 %2, %8, %7, %2\n\tv_dot2_f32_bf16 %3, %12, %7, %3" : "=&v"(kt[0][0]), "=&v"(kt[0][1]), "=&v"(kt[1][0]), "=&v"(kt[1][1]), "=&v"(ku[0]), "=&v"(ku[1]), "=&v"(ku[2]), "=&v"(ku[3]), "=&v"(ku[4]) : "v"(kp[0][0]), "v"(kp[0][1]), "v"(kc[0][0]), "v"(kc[0][1]), "v"(sw[0][0]), "v"(sw[0][1]) : ); SB();
  asm volatile("buffer_load_dwordx4 %0, %1, %2 offen offset:2048 lds" :  : "v"(voB[2]), "s"(rW1), "s"(kw_[6]) : "memory"); SB();
  asm volatile("v_lshlrev_b32 %0, 16, %4\n\tv_and_b32 %1, 0xffff0000, %4\n\tv_lshlrev_b32 %2, 16, %5\n\tv_and_b32 %3, 0xffff0000, %5" : "=&v"(kf[0][0]), "=&v"(kf[0][1]), "=&v"(kf[1][0]), "=&v"(kf[1][1]) : "v"(vv[0][0]), "v"(vv[0][1]) : ); SB();
  kt[0] *= kf[0]; kt[1] *= kf[1]; SB();
  ko[0] = (u32x2){pk2(kt[0]), pk2(kt[1])};
  asm volatile("" : "+v"(ko[0]));
  SB();
  asm volatile("s_add_u32 m0, %0, 131072" :  : "s"(mA) : "memory", "scc"); SB();
  asm volatile("s_nop 0\n\tbuffer_load_dwordx4 %0, %1, %2 offen lds" :  : "v"(voS), "s"(rA), "s"(ao[0]) : "memory"); SB();
  asm volatile("ds_write_b64 %0, %1 offset:0" :  : "v"(kw), "v"(ko[0]) : "memory"); SB();
  asm volatile("" : "+v"(kp[1]), "+v"(kc[1]), "+v"(vv[1]), "+v"(sw[1]) :  : "memory"); SB();
  asm volatile("v_alignbit_b32 %4, %14, %14, 16\n\tv_alignbit_b32 %5, %10, %9, 16\n\tv_alignbit_b32 %6, %11, %10, 16\n\tv_alignbit_b32 %7, %13, %13, 16\n\tv_alignbit_b32 %8, %12, %11, 16\n\tv_dot2_f32_bf16 %0, %5, %4, 0\n\tv_dot2_f32_bf16 %1, %10, %4, 0\n\tv_dot2_f32_bf16 %2, %6, %4, 0\n\tv_dot2_f32_bf16 %3, %11, %4, 0\n\tv_dot2_f32_bf16 %0, %6, %7, %0\n\tv_dot2_f32_bf16 %1, %11, %7, %1\n\tv_dot2_f32_bf16 %2, %8, %7, %2\n\tv_dot2_f32_bf16 %3, %12, %7, %3" : "=&v"(kt[0][0]), "=&v"(kt[0][1]), "=&v"(kt[1][0]), "=&v"(kt[1][1]), "=&v"(ku[0]), "=&v"(ku[1]), "=&v"(ku[2]), "=&v"(ku[3]), "=&v"(ku[4]) : "v"(kp[1][0]), "v"(kp[1][1]), "v"(kc[1][0]), "v"(kc[1][1]), "v"(sw[1][0]), "v"(sw[1][1]) : ); SB();
  asm volatile("buffer_load_dwordx4 %0, %1, %2 offen offset:1024 lds" :  : "v"(voS), "s"(rA), "s"(ao[1]) : "memory"); SB();
  asm volatile("v_lshlrev_b32 %0, 16, %4\n\tv_and_b32 %1, 0xffff0000, %4\n\tv_lshlrev_b32 %2, 16, %5\n\tv_and_b32 %3, 0xffff0000, %5" : "=&v"(kf[0][0]), "=&v"(kf[0][1]), "=&v"(kf[1][0]), "=&v"(kf[1][1]) : "v"(vv[1][0]), "v"(vv[1][1]) : ); SB();
  kt[0] *= kf[0]; kt[1] *= kf[1]; SB();
  ko[1] = (u32x2){pk2(kt[0]), pk2(kt[1])};
  asm volatile("" : "+v"(ko[1]));
  SB();
  asm volatile("buffer_load_dwordx4 %0, %1, %2 offen offset:2048 lds" :  : "v"(voS), "s"(rA), "s"(ao[2]) : "memory"); SB();
  asm volatile("ds_write_b64 %0, %1 offset:2304" :  : "v"(kw), "v"(ko[1]) : "memory"); SB();
  asm volatile("" : "+v"(kp[2]), "+v"(kc[2]), "+v"(vv[2]), "+v"(sw[2]) :  : "memory"); SB();
  asm volatile("v_alignbit_b32 %4, %14, %14, 16\n\tv_alignbit_b32 %5, %10, %9, 16\n\tv_alignbit_b32 %6, %11, %10, 16\n\tv_alignbit_b32 %7, %13, %13, 16\n\tv_alignbit_b32 %8, %12, %11, 16\n\tv_dot2_f32_bf16 %0, %5, %4, 0\n\tv_dot2_f32_bf16 %1, %10, %4, 0\n\tv_dot2_f32_bf16 %2, %6, %4, 0\n\tv_dot2_f32_bf16 %3, %11, %4, 0\n\tv_dot2_f32_bf16 %0, %6, %7, %0\n\tv_dot2_f32_bf16 %1, %11, %7, %1\n\tv_dot2_f32_bf16 %2, %8, %7, %2\n\tv_dot2_f32_bf16 %3, %12, %7, %3" : "=&v"(kt[0][0]), "=&v"(kt[0][1]), "=&v"(kt[1][0]), "=&v"(kt[1][1]), "=&v"(ku[0]), "=&v"(ku[1]), "=&v"(ku[2]), "=&v"(ku[3]), "=&v"(ku[4]) : "v"(kp[2][0]), "v"(kp[2][1]), "v"(kc[2][0]), "v"(kc[2][1]), "v"(sw[2][0]), "v"(sw[2][1]) : ); SB();
  asm volatile("buffer_load_dwordx4 %0, %1, %2 offen offset:3072 lds" :  : "v"(voS), "s"(rA), "s"(ao[3]) : "memory"); SB();
  asm volatile("v_lshlrev_b32 %0, 16, %4\n\tv_and_b32 %1, 0xffff0000, %4\n\tv_lshlrev_b32 %2, 16, %5\n\tv_and_b32 %3, 0xffff0000, %5" : "=&v"(kf[0][0]), "=&v"(kf[0][1]), "=&v"(kf[1][0]), "=&v"(kf[1][1]) : "v"(vv[2][0]), "v"(vv[2][1]) : ); SB();
  kt[0] *= kf[0]; kt[1] *= kf[1]; SB();
  ko[2] = (u32x2){pk2(kt[0]), pk2(kt[1])};
  asm volatile("" : "+v"(ko[2]));
  SB();
  asm volatile("ds_write_b64 %0, %1 offset:4608" :  : "v"(kw), "v"(ko[2]) : "memory"); SB();
  asm volatile("s_waitcnt lgkmcnt(2)" : "+v"(kp[3]), "+v"(kc[3]), "+v"(vv[3]), "+v"(sw[3]) :  : "memory"); SB();
  asm volatile("v_alignbit_b32 %4, %14, %14, 16\n\tv_alignbit_b32 %5, %10, %9, 16\n\tv_alignbit_b32 %6, %11, %10, 16\n\tv_alignbit_b32 %7, %13, %13, 16\n\tv_alignbit_b32 %8, %12, %11, 16\n\tv_dot2_f32_bf16 %0, %5, %4, 0\n\tv_dot2_f32_bf16 %1, %10, %4, 0\n\tv_dot2_f32_bf16 %2, %6, %4, 0\n\tv_dot2_f32_bf16 %3, %11, %4, 0\n\tv_dot2_f32_bf16 %0, %6, %7, %0\n\tv_dot2_f32_bf16 %1, %11, %7, %1\n\tv_dot2_f32_bf16 %2, %8, %7, %2\n\tv_dot2_f32_bf16 %3, %12, %7, %3" : "=&v"(kt[0][0]), "=&v"(kt[0][1]), "=&v"(kt[1][0]), "=&v"(kt[1][1]), "=&v"(ku[0]), "=&v"(ku[1]), "=&v"(ku[2]), "=&v"(ku[3]), "=&v"(ku[4]) : "v"(kp[3][0]), "v"(kp[3][1]), "v"(kc[3][0]), "v"(kc[3][1]), "v"(sw[3][0]), "v"(sw[3][1]) : ); SB();
  asm volatile("s_add_u32 m0, %0, 135168" :  : "s"(mA) : "memory", "scc"); SB();
  asm volatile("s_nop 0\n\tbuffer_load_dwordx4 %0, %1, %2 offen lds" :  : "v"(voS), "s"(rA), "s"(ao[4]) : "memory"); SB();
  asm volatile("buffer_load_dwordx4 %0, %1, %2 offen offset:1024 lds" :  : "v"(voS), "s"(rA), "s"(ao[5]) : "memory"); SB();
  asm volatile("v_lshlrev_b32 %0, 16, %4\n\tv_and_b32 %1, 0xffff0000, %4\n\tv_lshlrev_b32 %2, 16, %5\n\tv_and_b32 %3, 0xffff0000, %5" : "=&v"(kf[0][0]), "=&v"(kf[0][1]), "=&v"(kf[1][0]), "=&v"(kf[1][1]) : "v"(vv[3][0]), "v"(vv[3][1]) : ); SB();
  kt[0] *= kf[0]; kt[1] *= kf[1]; SB();
  ko[3] = (u32x2){pk2(kt[0]), pk2(kt[1])};
  asm volatile("" : "+v"(ko[3]));
  SB();
  asm volatile("buffer_load_dwordx4 %0, %1, %2 offen offset:2048 lds" :  : "v"(voS), "s"(rA), "s"(ao[6]) : "memory"); SB();
  asm volatile("ds_write_b64 %0, %1 offset:6912" :  : "v"(kw), "v"(ko[3]) : "memory"); SB();
  asm volatile("" : "+v"(kp[4]), "+v"(kc[4]), "+v"(vv[4]), "+v"(sw[4]) :  : "memory"); SB();
  asm volatile("v_alignbit_b32 %4, %14, %14, 16\n\tv_alignbit_b32 %5, %10, %9, 16\n\tv_alignbit_b32 %6, %11, %10, 16\n\tv_alignbit_b32 %7, %13, %13, 16\n\tv_alignbit_b32 %8, %12, %11, 16\n\tv_dot2_f32_bf16 %0, %5, %4, 0\n\tv_dot2_f32_bf16 %1, %10, %4, 0\n\tv_dot2_f32_bf16 %2, %6, %4, 0\n\tv_dot2_f32_bf16 %3, %11, %4, 0\n\tv_dot2_f32_bf16 %0, %6, %7, %0\n\tv_dot2_f32_bf16 %1, %11, %7, %1\n\tv_dot2_f32_bf16 %2, %8, %7, %2\n\tv_dot2_f32_bf16 %3, %12, %7, %3" : "=&v"(kt[0][0]), "=&v"(kt[0][1]), "=&v"(kt[1][0]), "=&v"(kt[1][1]), "=&v"(ku[0]), "=&v"(ku[1]), "=&v"(ku[2]), "=&v"(ku[3]), "=&v"(ku[4]) : "v"(kp[4][0]), "v"(kp[4][1]), "v"(kc[4][0]), "v"(kc[4][1]), "v"(sw[4][0]), "v"(sw[4][1]) : ); SB();
  asm volatile("v_lshlrev_b32 %0, 16, %4\n\tv_and_b32 %1, 0xffff0000, %4\n\tv_lshlrev_b32 %2, 16, %5\n\tv_and_b32 %3, 0xffff0000, %5" : "=&v"(kf[0][0]), "=&v"(kf[0][1]), "=&v"(kf[1][0]), "=&v"(kf[1][1]) : "v"(vv[4][0]), "v"(vv[4][1]) : ); SB();
  kt[0] *= kf[0]; kt[1] *= kf[1]; SB();
  ko[4] = (u32x2){pk2(kt[0]), pk2(kt[1])};
  asm volatile("" : "+v"(ko[4]));
  SB();
  asm volatile("ds_write_b64 %0, %1 offset:9216" :  : "v"(kw), "v"(ko[4]) : "memory"); SB();
  ST(16);
  asm volatile("s_waitcnt vmcnt(23) lgkmcnt(0)\n\ts_barrier" :  :  : "memory"); SB();
  asm volatile("ds_read_b128 %0, %1 offset:0" : "=&v"(kq[0]) : "v"(kr) : "memory"); SB();
  asm volatile("ds_read_b128 %0, %1 offset:1152" : "=&v"(kq[1]) : "v"(kr) : "memory"); SB();
  asm volatile("ds_read_b128 %0, %1 offset:2304" : "=&v"(kq[2]) : "v"(kr) : "memory"); SB();
  asm volatile("ds_read_b64_tr_b16 v[184:185], %0 offset:24576" :  : "v"(aW[0][0][0]) : "memory"); SB();
  asm volatile("ds_read_b64_tr_b16 v[186:187], %0 offset:25344" :  : "v"(aW[0][0][0]) : "memory"); SB();
  asm volatile("ds_read_b64_tr_b16 v[188:189], %0 offset:24576" :  : "v"(aW[0][0][1]) : "memory"); SB();
  asm volatile("ds_read_b64_tr_b16 v[190:191], %0 offset:25344" :  : "v"(aW[0][0][1]) : "memory"); SB();
  asm volatile("ds_read_b64_tr_b16 v[192:193], %0 offset:24576" :  : "v"(aW[0][0][2]) : "memory"); SB();
  asm volatile("ds_read_b64_tr_b16 v[194:195], %0 offset:25344" :  : "v"(aW[0][0][2]) : "memory"); SB();
  ST(7);
  asm volatile("s_waitcnt lgkmcnt(6)" : "+v"(kq[0]), "+v"(kq[1]), "+v"(kq[2]) :  : "memory"); SB();
  unsigned kz[3] = {kq[0][3], kq[1][3] | (w + 1) >> 2, kq[2][3] | (w + 1) >> 2};
  asm volatile("" : "+v"(kz[0]), "+v"(kz[1]), "+v"(kz[2])); SB();
  asm volatile("buffer_load_dwordx4 v[196:199], %0, %1, %2 offen offset:0 sc1" :  : "v"(vS), "s"(rWS), "s"(0u) : "memory"); SB();
  asm volatile("buffer_load_dwordx4 v[200:203], %0, %1, %2 offen offset:2048 sc1" :  : "v"(vS), "s"(rWS), "s"(0u) : "memory"); SB();
  asm volatile("s_waitcnt lgkmcnt(3)\n\tv_mfma_f32_16x16x32_bf16 %0, v[184:187], a[0:3], 0" : "=&v"(a1[0][0]) :  : ); SB();
  asm volatile("ds_read_b64_tr_b16 v[160:161], %0 offset:24576" :  : "v"(aW[0][1][0]) : "memory"); SB();
  asm volatile("v_mfma_f32_16x16x32_bf16 %0, v[184:187], a[4:7], 0" : "=&v"(a1[1][0]) :  : ); SB();
  asm volatile("ds_read_b64_tr_b16 v[162:163], %0 offset:25344" :  : "v"(aW[0][1][0]) : "memory"); SB();
  asm volatile("v_mfma_f32_16x16x32_bf16 %0, v[184:187], %1, 0" : "=&v"(c1[0]) : "v"(ones) : ); SB();
  asm volatile("ds_read_b64_tr_b16 v[164:165], %0 offset:24576" :  : "v"(aW[0][1][1]) : "memory"); SB();
  asm volatile("s_waitcnt lgkmcnt(3)\n\tv_mfma_f32_16x16x32_bf16 %0, v[188:191], a[0:3], 0" : "=&v"(a1[0][1]) :  : ); SB();
  asm volatile("ds_read_b64_tr_b16 v[166:167], %0 offset:25344" :  : "v"(aW[0][1][1]) : "memory"); SB();
  asm volatile("v_mfma_f32_16x16x32_bf16 %0, v[188:191], a[4:7], 0" : "=&v"(a1[1][1]) :  : ); SB();
  asm volatile("ds_read_b64_tr_b16 v[168:169], %0 offset:24576" :  : "v"(aW[0][1][2]) : "memory"); SB();
  asm volatile("v_mfma_f32_16x16x32_bf16 %0, v[188:191], %1, 0" : "=&v"(c1[1]) : "v"(ones) : ); SB();
  asm volatile("ds_read_b64_tr_b16 v[170:171], %0 offset:25344" :  : "v"(aW[0][1][2]) : "memory"); SB();
  asm volatile("v_mfma_f32_16x16x32_bf16 %0, v[192:195], a[0:3], 0" : "=&v"(a1[0][2]) :  : ); SB();
  asm volatile("ds_read_b64_tr_b16 v[172:173], %0 offset:36864" :  : "v"(aW[0][0][0]) : "memory"); SB();
  asm volatile("v_mfma_f32_16x16x32_bf16 %0, v[192:195], a[4:7], 0" : "=&v"(a1[1][2]) :  : ); SB();
  asm volatile("ds_read_b64_tr_b16 v[174:175], %0 offset:37632" :  : "v"(aW[0][0][0]) : "memory"); SB();
  asm volatile("v_mfma_f32_16x16x32_bf16 %0, v[192:195], %1, 0" : "=&v"(c1[2]) : "v"(ones) : ); SB();
  asm volatile("ds_read_b64_tr_b16 v[176:177], %0 offset:36864" :  : "v"(aW[0][0][1]) : "memory"); SB();
  asm volatile("s_waitcnt lgkmcnt(7)\n\tv_mfma_f32_16x16x32_bf16 %0, v[160:163], a[8:11], %0" : "+v"(a1[0][0]) :  : ); SB();
  asm volatile("ds_read_b64_tr_b16 v[178:179], %0 offset:37632" :  : "v"(aW[0][0][1]) : "memory"); SB();
  asm volatile("buffer_load_dwordx4 v[204:207], %0, %1, %2 offen offset:0 sc1" :  : "v"(vS), "s"(rWS), "s"(4096u) : "memory"); SB();
  asm volatile("v_mfma_f32_16x16x32_bf16 %0, v[160:163], a[12:15], %0" : "+v"(a1[1][0]) :  : ); SB();
  asm volatile("ds_read_b64_tr_b16 v[180:181], %0 offset:36864" :  : "v"(aW[0][0][2]) : "memory"); SB();
  asm volatile("s_waitcnt lgkmcnt(7)\n\tv_mfma_f32_16x16x32_bf16 %0, v[164:167], a[8:11], %0" : "+v"(a1[0][1]) :  : ); SB();
  asm volatile("ds_read_b64_tr_b16 v[182:183], %0 offset:37632" :  : "v"(aW[0][0][2]) : "memory"); SB();
  asm volatile("v_mfma_f32_16x16x32_bf16 %0, v[164:167], a[12:15], %0" : "+v"(a1[1][1]) :  : ); SB();
  asm volatile("ds_read_b64_tr_b16 v[184:185], %0 offset:36864" :  : "v"(aW[0][1][0]) : "memory"); SB();
  asm volatile("s_waitcnt lgkmcnt(7)\n\tv_mfma_f32_16x16x32_bf16 %0, v[168:171], a[8:11], %0" : "+v"(a1[0][2]) :  : ); SB();
  asm volatile("ds_read_b64_tr_b16 v[186:187], %0 offset:37632" :  : "v"(aW[0][1][0]) : "memory"); SB();
  asm volatile("v_mfma_f32_16x16x32_bf16 %0, v[168:171], a[12:15], %0" : "+v"(a1[1][2]) :  : ); SB();
  asm volatile("ds_read_b64_tr_b16 v[188:189], %0 offset:36864" :  : "v"(aW[0][1][1]) : "memory"); SB();
  asm volatile("buffer_load_dwordx4 v[208:211], %0, %1, %2 offen offset:2048 sc1" :  : "v"(vS), "s"(rWS), "s"(4096u) : "memory"); SB();
  asm volatile("s_waitcnt lgkmcnt(7)\n\tv_mfma_f32_16x16x32_bf16 %0, v[172:175], a[16:19], %0" : "+v"(a1[0][0]) :  : ); SB();
  asm volatile("ds_read_b64_tr_b16 v[190:191], %0 offset:37632" :  : "v"(aW[0][1][1]) : "memory"); SB();
  asm volatile("v_mfma_f32_16x16x32_bf16 %0, v[172:175], a[20:23], %0" : "+v"(a1[1][0]) :  : ); SB();
  asm volatile("ds_read_b64_tr_b16 v[192:193], %0 offset:36864" :  : "v"(aW[0][1][2]) : "memory"); SB();
  asm volatile("v_mfma_f32_16x16x32_bf16 %0, v[172:175], %1, %0" : "+v"(c1[0]) : "v"(ones) : ); SB();
  asm volatile("ds_read_b64_tr_b16 v[194:195], %0 offset:37632" :  : "v"(aW[0][1][2]) : "memory"); SB();
  asm volatile("buffer_store_dwordx4 %1, %0, %2, %3 offen offset:0 sc1\n\ts_nop 1" :  : "v"(vKV), "v"(kq[0]), "s"(rWS), "s"(0u) : "memory"); SB();
  asm volatile("s_waitcnt lgkmcnt(8)\n\tv_mfma_f32_16x16x32_bf16 %0, v[176:179], a[16:19], %0" : "+v"(a1[0][1]) :  : ); SB();
  asm volatile("s_waitcnt vmcnt(22)\n\ts_barrier" :  :  : "memory"); SB();
  asm volatile("ds_read_b64_tr_b16 v[160:161], %0 offset:0" :  : "v"(aW[1][0][0]) : "memory"); SB();
  asm volatile("v_mfma_f32_16x16x32_bf16 %0, v[176:179], a[20:23], %0" : "+v"(a1[1][1]) :  : ); SB();
  asm volatile("ds_read_b64_tr_b16 v[162:163], %0 offset:768" :  : "v"(aW[1][0][0]) : "memory"); SB();
  asm volatile("buffer_store_dwordx4 %1, %0, %2, %3 offen offset:0 sc1\n\ts_nop 1" :  : "v"(vKV), "v"(kq[1]), "s"(rWS), "s"(w3k[0]) : "memory"); SB();
  asm volatile("v_mfma_f32_16x16x32_bf16 %0, v[176:179], %1, %0" : "+v"(c1[1]) : "v"(ones) : ); SB();
  asm volatile("ds_read_b64_tr_b16 v[164:165], %0 offset:0" :  : "v"(aW[1][0][1]) : "memory"); SB();
  asm volatile("s_waitcnt lgkmcnt(6)\n\tv_mfma_f32_16x16x32_bf16 %0, v[180:183], a[16:19], %0" : "+v"(a1[0][2]) :  : ); SB();
  asm volatile("ds_read_b64_tr_b16 v[166:167], %0 offset:768" :  : "v"(aW[1][0][1]) : "memory"); SB();
  asm volatile("v_mfma_f32_16x16x32_bf16 %0, v[180:183], a[20:23], %0" : "+v"(a1[1][2]) :  : ); SB();
  asm volatile("ds_read_b64_tr_b16 v[168:169], %0 offset:0" :  : "v"(aW[1][0][2]) : "memory"); SB();
  asm volatile("v_mfma_f32_16x16x32_bf16 %0, v[180:183], %1, %0" : "+v"(c1[2]) : "v"(ones) : ); SB();
  asm volatile("ds_read_b64_tr_b16 v[170:171], %0 offset:768" :  : "v"(aW[1][0][2]) : "memory"); SB();
  asm volatile("v_mfma_f32_16x16x32_bf16 %0, v[184:187], a[24:27], %0" : "+v"(a1[0][0]) :  : ); SB();
  asm volatile("ds_read_b64_tr_b16 v[172:173], %0 offset:0" :  : "v"(aW[1][1][0]) : "memory"); SB();
  asm volatile("v_mfma_f32_16x16x32_bf16 %0, v[184:187], a[28:31], %0" : "+v"(a1[1][0]) :  : ); SB();
  asm volatile("ds_read_b64_tr_b16 v[174:175], %0 offset:768" :  : "v"(aW[1][1][0]) : "memory"); SB();
  asm volatile("s_waitcnt lgkmcnt(8)\n\tv_mfma_f32_16x16x32_bf16 %0, v[188:191], a[24:27], %0" : "+v"(a1[0][1]) :  : ); SB();
  asm volatile("ds_read_b64_tr_b16 v[176:177], %0 offset:0" :  : "v"(aW[1][1][1]) : "memory"); SB();
  asm volatile("v_mfma_f32_16x16x32_bf16 %0, v[188:191], a[28:31], %0" : "+v"(a1[1][1]) :  : ); SB();
  asm volatile("ds_read_b64_tr_b16 v[178:179], %0 offset:768" :  : "v"(aW[1][1][1]) : "memory"); SB();
  asm volatile("buffer_store_dwordx4 %1, %0, %2, %3 offen offset:0 sc1\n\ts_nop 1" :  : "v"(vKV), "v"(kq[2]), "s"(rWS), "s"(w3k[1]) : "memory"); SB();
  asm volatile("v_mfma_f32_16x16x32_bf16 %0, v[192:195], a[24:27], %0" : "+v"(a1[0][2]) :  : ); SB();
  asm volatile("ds_read_b64_tr_b16 v[180:181], %0 offset:0" :  : "v"(aW[1][1][2]) : "memory"); SB();
  asm volatile("v_mfma_f32_16x16x32_bf16 %0, v[192:195], a[28:31], %0" : "+v"(a1[1][2]) :  : ); SB();
  asm volatile("ds_read_b64_tr_b16 v[182:183], %0 offset:768" :  : "v"(aW[1][1][2]) : "memory"); SB();
  asm volatile("s_waitcnt lgkmcnt(9)\n\tv_mfma_f32_16x16x32_bf16 %0, v[160:163], a[32:35], %0" : "+v"(a1[0][0]) :  : ); SB();
  asm volatile("ds_read_b64_tr_b16 v[184:185], %0 offset:12288" :  : "v"(aW[1][0][0]) : "memory"); SB();
  asm volatile("buffer_store_dwordx4 %1, %0, %2, %3 offen offset:0 sc1\n\ts_nop 1" :  : "v"(vWO), "v"(((u32x4){wt[0][0], wt[0][1], wt[1][0], wt[1][1]})), "s"(rWS), "s"(0u) : "memory"); SB();
  asm volatile("v_mfma_f32_16x16x32_bf16 %0, v[160:163], a[36:39], %0" : "+v"(a1[1][0]) :  : ); SB();
  asm volatile("ds_read_b64_tr_b16 v[186:187], %0 offset:13056" :  : "v"(aW[1][0][0]) : "memory"); SB();
  asm volatile("s_waitcnt vmcnt(13)" :  :  : "memory"); SB();
  asm volatile("v_mfma_f32_16x16x32_bf16 %0, v[160:163], %1, %0" : "+v"(c1[0]) : "v"(ones) : ); SB();
  asm volatile("ds_read_b64_tr_b16 v[188:189], %0 offset:12288" :  : "v"(aW[1][0][1]) : "memory"); SB();
  asm volatile("s_waitcnt lgkmcnt(8)\n\tv_mfma_f32_16x16x32_bf16 %0, v[164:167], a[32:35], %0" : "+v"(a1[0][1]) :  : ); SB();
  asm volatile("ds_read_b64_tr_b16 v[190:191], %0 offset:13056" :  : "v"(aW[1][0][1]) : "memory"); SB();
  asm volatile("ds_read_b128 a[160:163], %0 offset:32768" :  : "v"(aA[0]) : "memory"); SB();
  asm volatile("v_mfma_f32_16x16x32_bf16 %0, v[164:167], a[36:39], %0" : "+v"(a1[1][1]) :  : ); SB();
  asm volatile("ds_read_b64_tr_b16 v[192:193], %0 offset:12288" :  : "v"(aW[1][0][2]) : "memory"); SB();
  asm volatile("ds_read_b128 a[176:179], %0 offset:32768" :  : "v"(aA[1]) : "memory"); SB();
  asm volatile("v_mfma_f32_16x16x32_bf16 %0, v[164:167], %1, %0" : "+v"(c1[1]) : "v"(ones) : ); SB();
  asm volatile("ds_read_b64_tr_b16 v[194:195], %0 offset:13056" :  : "v"(aW[1][0][2]) : "memory"); SB();
  asm volatile("s_waitcnt vmcnt(11)" :  :  : "memory"); SB();
  asm volatile("v_mfma_f32_16x16x32_bf16 %0, v[168:171], a[32:35], %0" : "+v"(a1[0][2]) :  : ); SB();
  asm volatile("ds_read_b128 a[164:167], %0 offset:34816" :  : "v"(aA[0]) : "memory"); SB();
  asm volatile("v_mfma_f32_16x16x32_bf16 %0, v[168:171], a[36:39], %0" : "+v"(a1[1][2]) :  : ); SB();
  asm volatile("ds_read_b128 a[180:183], %0 offset:34816" :  : "v"(aA[1]) : "memory"); SB();
  asm volatile("s_waitcnt vmcnt(9)" :  :  : "memory"); SB();
  asm volatile("v_mfma_f32_16x16x32_bf16 %0, v[168:171], %1, %0" : "+v"(c1[2]) : "v"(ones) : ); SB();
  asm volatile("ds_read_b128 a[168:171], %0 offset:36864" :  : "v"(aA[0]) : "memory"); SB();
  asm volatile("s_waitcnt lgkmcnt(10)\n\tv_mfma_f32_16x16x32_bf16 %0, v[172:175], a[40:43], %0" : "+v"(a1[0][0]) :  : ); SB();
  asm volatile("ds_read_b64_tr_b16 v[160:161], %0 offset:12288" :  : "v"(aW[1][1][0]) : "memory"); SB();
  asm volatile("ds_read_b128 a[184:187], %0 offset:36864" :  : "v"(aA[1]) : "memory"); SB();
  asm volatile("v_mfma_f32_16x16x32_bf16 %0, v[172:175], a[44:47], %0" : "+v"(a1[1][0]) :  : ); SB();
  asm volatile("ds_read_b64_tr_b16 v[162:163], %0 offset:13056" :  : "v"(aW[1][1][0]) : "memory"); SB();
  asm volatile("s_add_u32 m0, %0, 12288" :  : "s"(mW) : "memory", "scc"); SB();
  asm volatile("v_mfma_f32_16x16x32_bf16 %0, v[176:179], a[40:43], %0" : "+v"(a1[0][1]) :  : ); SB();
  asm volatile("ds_read_b64_tr_b16 v[164:165], %0 offset:12288" :  : "v"(aW[1][1][1]) : "memory"); SB();
  asm volatile("buffer_load_dwordx4 %0, %1, %2 offen lds" :  : "v"(voB[0]), "s"(rW1), "s"(kw_[7]) : "memory"); SB();
  asm volatile("v_mfma_f32_16x16x32_bf16 %0, v[176:179], a[44:47], %0" : "+v"(a1[1][1]) :  : ); SB();
  asm volatile("ds_read_b64_tr_b16 v[166:167], %0 offset:13056" :  : "v"(aW[1][1][1]) : "memory"); SB();
  asm volatile("v_mfma_f32_16x16x32_bf16 %0, v[180:183], a[40:43], %0" : "+v"(a1[0][2]) :  : ); SB();
  asm volatile("ds_read_b64_tr_b16 v[168:169], %0 offset:12288" :  : "v"(aW[1][1][2]) : "memory"); SB();
  asm volatile("v_mfma_f32_16x16x32_bf16 %0, v[180:183], a[44:47], %0" : "+v"(a1[1][2]) :  : ); SB();
  asm volatile("ds_read_b64_tr_b16 v[170:171], %0 offset:13056" :  : "v"(aW[1][1][2]) : "memory"); SB();
  ST(8);
  { unsigned lane = lane0, w = w0, rb = rb0, cb = cb0;
  asm volatile("" : "+v"(lane)); asm volatile("" : "+s"(w), "+s"(rb), "+s"(cb)); SB();

  unsigned zwa[2] = {lb + 98304 + 4096 + w * 8192 + (wn * 16 + l15) * 104 + g4 * 8, lb + 98304 + 4096 + w * 8192 + ((wn ^ 1) * 16 + l15) * 104 + g4 * 8};
  unsigned zra = lb + 98304 + 4096 + w * 8192 + (8 * g4 + q4) * 104 + 8 * p4, vZ_ = vZ;
  unsigned nw = lb + 98304 + 7424 + wm * 16384 + wn * 64 + (g4 & 1) * 32 + (lane & 7) * 4, nr = lb + 98304 + 7424 + wm * 16384 + g4 * 32;
  asm volatile("" : "+v"(zwa[0]), "+v"(zwa[1]), "+v"(zra), "+v"(vZ_), "+v"(nw), "+v"(nr));
  SB();
  asm volatile("buffer_load_dwordx4 %0, %1, %2 offen offset:1024 lds" :  : "v"(voB[1]), "s"(rW1), "s"(kw_[7]) : "memory"); SB();
  asm volatile("buffer_load_dwordx4 %0, %1, %2 offen offset:2048 lds" :  : "v"(voB[2]), "s"(rW1), "s"(kw_[7]) : "memory"); SB();
  asm volatile("s_add_u32 m0, %0, 24576" :  : "s"(mW) : "memory", "scc"); SB();
  asm volatile("s_nop 0\n\tbuffer_load_dwordx4 %0, %1, %2 offen lds" :  : "v"(voB[0]), "s"(rW1), "s"(kw_[8]) : "memory"); SB();
  asm volatile("buffer_load_dwordx4 %0, %1, %2 offen offset:1024 lds" :  : "v"(voB[1]), "s"(rW1), "s"(kw_[8]) : "memory"); SB();
  asm volatile("buffer_load_dwordx4 %0, %1, %2 offen offset:2048 lds" :  : "v"(voB[2]), "s"(rW1), "s"(kw_[8]) : "memory"); SB();
  asm volatile("s_waitcnt vmcnt(10)\n\t.Lc_%=:\n\tv_cmpx_eq_u64 vcc, %1, v[198:199]\n\tv_cmpx_eq_u64 vcc, %1, v[202:203]\n\tv_cmpx_eq_u64 vcc, %1, v[206:207]\n\tv_cmpx_eq_u64 vcc, %1, v[210:211]\n\ts_cmp_eq_u64 vcc, -1\n\ts_mov_b64 exec, -1\n\ts_cbranch_scc0 .Lr_%=\n\t.Ld_%=:\n\t.subsection 1\n\t.Lr_%=:\n\tv_add_u32 %0, -1, %0\n\ts_nop 1\n\tv_readfirstlane_b32 vcc_hi, %0\n\ts_nop 3\n\ts_cmp_lt_i32 vcc_hi, 1\n\ts_cbranch_scc1 .Ld_%=\n\ts_sleep 2\n\ts_movk_i32 vcc_lo, 0x1000\n\tbuffer_load_dwordx4 v[196:199], %2, %3, 0 offen offset:0 sc1\n\tbuffer_load_dwordx4 v[200:203], %2, %3, 0 offen offset:2048 sc1\n\tbuffer_load_dwordx4 v[204:207], %2, %3, vcc_lo offen offset:0 sc1\n\tbuffer_load_dwordx4 v[208:211], %2, %3, vcc_lo offen offset:2048 sc1\n\ts_waitcnt vmcnt(0)\n\ts_branch .Lc_%=\n\t.subsection 0" : "+v"(tmo) : "s"(tg), "v"(vS), "s"(rWS) : "memory", "vcc", "scc"); SB();
  ST(9);
  {
    f32x2 s4[2], dm[2];
  asm volatile("v_pk_add_f32 %0, v[196:197], v[200:201]\n\tv_pk_add_f32 %1, v[204:205], v[208:209]" : "=&v"(s4[0]), "=&v"(s4[1]) :  : ); SB();
    const f32x2 sm = s4[0] + s4[1];
    const float mean = red32(sm[0] + dppf<0x128>(sm[0])) * (1.0f / 16.0f);
  asm volatile("v_sub_f32 %0, v196, %4\n\tv_sub_f32 %1, v200, %4\n\tv_sub_f32 %2, v204, %4\n\tv_sub_f32 %3, v208, %4" : "=&v"(dm[0][0]), "=&v"(dm[0][1]), "=&v"(dm[1][0]), "=&v"(dm[1][1]) : "v"(mean) : ); SB();
    const f32x2 dq = __builtin_elementwise_fma(dm[0], dm[0], dm[1] * dm[1]);
    float qs = __builtin_fmaf(dq[0] + dq[1], 48.0f, sm[1]);
    qs = red32(qs + dppf<0x128>(qs));
    float r = __builtin_amdgcn_rcpf(rbf(__builtin_amdgcn_sqrtf(rbf(rbf(qs * (1.0f / 768.0f)) + 1e-5f))));
    float mr = -rbf(mean) * r;
    f32x4 rm[4];
    asm volatile("" : "+v"(r), "+v"(mr)); SB();
  asm volatile("s_mov_b64 exec, 0xff00ff\n\tds_write2_b32 %0, %1, %2 offset1:32\n\ts_mov_b64 exec, -1" :  : "v"(nw), "v"(r), "v"(mr) : "memory"); SB();
  asm volatile("ds_write_b64 %0, %1 offset:0" :  : "v"(zwa[0]), "v"(((u32x2){zp[0][0][0], zp[0][0][1]})) : "memory"); SB();
  asm volatile("ds_write_b64 %0, %1 offset:32" :  : "v"(zwa[0]), "v"(((u32x2){zp[0][1][0], zp[0][1][1]})) : "memory"); SB();
  asm volatile("ds_write_b64 %0, %1 offset:64" :  : "v"(zwa[0]), "v"(((u32x2){zp[0][2][0], zp[0][2][1]})) : "memory"); SB();
  asm volatile("s_add_u32 m0, %0, 135168" :  : "s"(mA) : "memory", "scc"); SB();
  asm volatile("s_nop 0\n\tbuffer_load_dwordx4 %0, %1, %2 offen offset:3072 lds" :  : "v"(voS), "s"(rA), "s"(ao[7]) : "memory"); SB();
  asm volatile("v_xor_b32 %0, 128, %0" : "+v"(voS) :  : "memory"); SB();
  asm volatile("s_add_u32 m0, %0, 98304" :  : "s"(mA) : "memory", "scc"); SB();
  asm volatile("s_nop 0\n\tbuffer_load_dwordx4 %0, %1, %2 offen lds" :  : "v"(voS), "s"(rA), "s"(ao[0]) : "memory"); SB();
  asm volatile("ds_write_b64 %0, %1 offset:0" :  : "v"(zwa[1]), "v"(((u32x2){zp[1][0][0], zp[1][0][1]})) : "memory"); SB();
  asm volatile("ds_write_b64 %0, %1 offset:32" :  : "v"(zwa[1]), "v"(((u32x2){zp[1][1][0], zp[1][1][1]})) : "memory"); SB();
  asm volatile("ds_write_b64 %0, %1 offset:64" :  : "v"(zwa[1]), "v"(((u32x2){zp[1][2][0], zp[1][2][1]})) : "memory"); SB();
  asm volatile("buffer_load_dwordx4 %0, %1, %2 offen offset:1024 lds" :  : "v"(voS), "s"(rA), "s"(ao[1]) : "memory"); SB();
  asm volatile("ds_read_b64_tr_b16 %0, %1 offset:0" : "=&v"(zt[0][0]) : "v"(zra) : "memory"); SB();
  asm volatile("ds_read_b64_tr_b16 %0, %1 offset:416" : "=&v"(zt[0][1]) : "v"(zra) : "memory"); SB();
  asm volatile("ds_read_b64_tr_b16 %0, %1 offset:32" : "=&v"(zt[1][0]) : "v"(zra) : "memory"); SB();
  asm volatile("ds_read_b64_tr_b16 %0, %1 offset:448" : "=&v"(zt[1][1]) : "v"(zra) : "memory"); SB();
  asm volatile("ds_read_b64_tr_b16 %0, %1 offset:64" : "=&v"(zt[2][0]) : "v"(zra) : "memory"); SB();
  asm volatile("ds_read_b64_tr_b16 %0, %1 offset:480" : "=&v"(zt[2][1]) : "v"(zra) : "memory"); SB();
  asm volatile("s_waitcnt lgkmcnt(6)\n\ts_barrier" :  :  : "memory"); SB();
  asm volatile("ds_read_b128 %0, %1 offset:0" : "=&v"(rm[0]) : "v"(nr) : "memory"); SB();
  asm volatile("ds_read_b128 %0, %1 offset:16" : "=&v"(rm[1]) : "v"(nr) : "memory"); SB();
  asm volatile("ds_read_b128 %0, %1 offset:128" : "=&v"(rm[2]) : "v"(nr) : "memory"); SB();
  asm volatile("ds_read_b128 %0, %1 offset:144" : "=&v"(rm[3]) : "v"(nr) : "memory"); SB();
  asm volatile("s_add_u32 m0, %0, 36864" :  : "s"(mW) : "memory", "scc"); SB();
  asm volatile("s_nop 0\n\tbuffer_load_dwordx4 %0, %1, %2 offen lds" :  : "v"(voB[0]), "s"(rW1), "s"(kw_[9]) : "memory"); SB();
  asm volatile("buffer_load_dwordx4 %0, %1, %2 offen offset:1024 lds" :  : "v"(voB[1]), "s"(rW1), "s"(kw_[9]) : "memory"); SB();
  asm volatile("s_waitcnt lgkmcnt(4)" : "+v"(zt[0][0]), "+v"(zt[0][1]), "+v"(zt[1][0]), "+v"(zt[1][1]), "+v"(zt[2][0]), "+v"(zt[2][1]) :  : "memory"); SB();
  asm volatile("s_waitcnt lgkmcnt(0)" : "+v"(rm[0]), "+v"(rm[1]), "+v"(rm[2]), "+v"(rm[3]) :  : "memory"); SB();
    const f32x2 re[4] = {{rm[0][0], rm[0][1]}, {rm[0][2], rm[0][3]}, {rm[1][0], rm[1][1]}, {rm[1][2], rm[1][3]}};
    const f32x2 me[4] = {{rm[2][0], rm[2][1]}, {rm[2][2], rm[2][3]}, {rm[3][0], rm[3][1]}, {rm[3][2], rm[3][3]}};
    u32x4 zo[3];
#pragma unroll
    for (int e = 0; e < 4; ++e) zo[0][e] = pk2(__builtin_elementwise_fma(unpk(zt[0][e >> 1][e & 1]), re[e], me[e]));
    SB();
  asm volatile("buffer_store_dwordx4 %1, %0, %2, %3 offen offset:0 sc1\n\ts_nop 1" :  : "v"(vZ_), "v"(zo[0]), "s"(rWS), "s"(0u) : "memory"); SB();
#pragma unroll
    for (int e = 0; e < 4; ++e) zo[1][e] = pk2(__builtin_elementwise_fma(unpk(zt[1][e >> 1][e & 1]), re[e], me[e]));
    SB();
  asm volatile("buffer_store_dwordx4 %1, %0, %2, %3 offen offset:1024 sc1\n\ts_nop 1" :  : "v"(vZ_), "v"(zo[1]), "s"(rWS), "s"(0u) : "memory"); SB();
#pragma unroll
    for (int e = 0; e < 4; ++e) zo[2][e] = pk2(__builtin_elementwise_fma(unpk(zt[2][e >> 1][e & 1]), re[e], me[e]));
    SB();
  asm volatile("buffer_store_dwordx4 %1, %0, %2, %3 offen offset:2048 sc1\n\ts_nop 1" :  : "v"(vZ_), "v"(zo[2]), "s"(rWS), "s"(0u) : "memory"); SB();
  }
  ST(10);
  asm volatile("v_mfma_f32_16x16x32_bf16 %0, v[184:187], a[48:51], %0" : "+v"(a1[0][0]) :  : ); SB();
  asm volatile("s_barrier" :  :  : "memory"); SB();
  asm volatile("ds_read_b64_tr_b16 v[172:173], %0 offset:24576" :  : "v"(aW[1][0][0]) : "memory"); SB();
  asm volatile("s_waitcnt vmcnt(14)\n\ts_mov_b64 exec, 1\n\tbuffer_store_dwordx2 %0, off, %1, %2 sc1\n\ts_mov_b64 exec, -1" :  : "v"(((u32x2){tg0, tg1})), "s"(rWS), "s"(sFK) : "memory"); SB();
  asm volatile("v_mfma_f32_16x16x32_bf16 %0, v[184:187], a[52:55], %0" : "+v"(a1[1][0]) :  : ); SB();
  asm volatile("ds_read_b64_tr_b16 v[174:175], %0 offset:25344" :  : "v"(aW[1][0][0]) : "memory"); SB();
  asm volatile("v_mfma_f32_16x16x32_bf16 %0, v[184:187], %1, %0" : "+v"(c1[0]) : "v"(ones) : ); SB();
  asm volatile("ds_read_b64_tr_b16 v[176:177], %0 offset:24576" :  : "v"(aW[1][0][1]) : "memory"); SB();
  asm volatile("v_mfma_f32_16x16x32_bf16 %0, v[188:191], a[48:51], %0" : "+v"(a1[0][1]) :  : ); SB();
  asm volatile("ds_read_b64_tr_b16 v[178:179], %0 offset:25344" :  : "v"(aW[1][0][1]) : "memory"); SB();
  asm volatile("v_mfma_f32_16x16x32_bf16 %0, v[188:191], a[52:55], %0" : "+v"(a1[1][1]) :  : ); SB();
  asm volatile("ds_read_b64_tr_b16 v[180:181], %0 offset:24576" :  : "v"(aW[1][0][2]) : "memory"); SB();
  asm volatile("v_mfma_f32_16x16x32_bf16 %0, v[188:191], %1, %0" : "+v"(c1[1]) : "v"(ones) : ); SB();
  asm volatile("ds_read_b64_tr_b16 v[182:183], %0 offset:25344" :  : "v"(aW[1][0][2]) : "memory"); SB();
  asm volatile("v_mfma_f32_16x16x32_bf16 %0, v[192:195], a[48:51], %0" : "+v"(a1[0][2]) :  : ); SB();
  ca = lb + 57344 + w * 192 + g4 * 16; asm volatile("" : "+v"(ca)); SB();
  asm volatile("v_mfma_f32_16x16x32_bf16 %0, v[192:195], a[52:55], %0" : "+v"(a1[1][2]) :  : ); SB();
  cr = lb + 57344 + (w ^ 2) * 192 + g4 * 16; asm volatile("" : "+v"(cr)); SB();
  asm volatile("v_mfma_f32_16x16x32_bf16 %0, v[192:195], %1, %0" : "+v"(c1[2]) : "v"(ones) : ); SB();
  asm volatile("buffer_load_dwordx4 %0, %1, %2 offen offset:2048 lds" :  : "v"(voB[2]), "s"(rW1), "s"(kw_[9]) : "memory"); SB();
  asm volatile("v_mfma_f32_16x16x32_bf16 %0, v[160:163], a[56:59], %0" : "+v"(a1[0][0]) :  : ); SB();
  asm volatile("ds_read_b64_tr_b16 v[184:185], %0 offset:24576" :  : "v"(aW[1][1][0]) : "memory"); SB();
  asm volatile("s_add_u32 m0, %0, 98304" :  : "s"(mA) : "memory", "scc"); SB();
  asm volatile("v_mfma_f32_16x16x32_bf16 %0, v[160:163], a[60:63], %0" : "+v"(a1[1][0]) :  : ); SB();
  asm volatile("ds_read_b64_tr_b16 v[186:187], %0 offset:25344" :  : "v"(aW[1][1][0]) : "memory"); SB();
  asm volatile("v_lshlrev_b32 %0, 16, v245" : "=v"(dD[0]) :  : ); SB();
  asm volatile("v_mfma_f32_16x16x32_bf16 %0, v[164:167], a[56:59], %0" : "+v"(a1[0][1]) :  : ); SB();
  asm volatile("ds_read_b64_tr_b16 v[188:189], %0 offset:24576" :  : "v"(aW[1][1][1]) : "memory"); SB();
  asm volatile("v_lshlrev_b32 %0, 16, v246" : "=v"(dD[1]) :  : ); SB();
  asm volatile("v_mfma_f32_16x16x32_bf16 %0, v[164:167], a[60:63], %0" : "+v"(a1[1][1]) :  : ); SB();
  asm volatile("ds_read_b64_tr_b16 v[190:191], %0 offset:25344" :  : "v"(aW[1][1][1]) : "memory"); SB();
  asm volatile("v_lshlrev_b32 %0, 16, v247" : "=v"(dD[2]) :  : ); SB();
  asm volatile("v_mfma_f32_16x16x32_bf16 %0, v[168:171], a[56:59], %0" : "+v"(a1[0][2]) :  : ); SB();
  asm volatile("ds_read_b64_tr_b16 v[192:193], %0 offset:24576" :  : "v"(aW[1][1][2]) : "memory"); SB();
  asm volatile("v_lshlrev_b32 %0, 16, v248" : "=v"(bs) :  : ); SB();
  asm volatile("v_mfma_f32_16x16x32_bf16 %0, v[168:171], a[60:63], %0" : "+v"(a1[1][2]) :  : ); SB();
  asm volatile("ds_read_b64_tr_b16 v[194:195], %0 offset:25344" :  : "v"(aW[1][1][2]) : "memory"); SB();
  asm volatile("s_waitcnt lgkmcnt(6)\n\tv_mfma_f32_16x16x32_bf16 %0, v[172:175], a[64:67], %0" : "+v"(a1[0][0]) :  : ); SB();
  asm volatile("ds_read_b64_tr_b16 v[160:161], %0 offset:36864" :  : "v"(aW[1][0][0]) : "memory"); SB();
  asm volatile("v_mov_b32_dpp v242, v231 wave_shr:1 row_mask:0xf bank_mask:0xf" :  :  : ); SB();
  asm volatile("v_mfma_f32_16x16x32_bf16 %0, v[172:175], a[68:71], %0" : "+v"(a1[1][0]) :  : ); SB();
  asm volatile("ds_read_b64_tr_b16 v[162:163], %0 offset:37632" :  : "v"(aW[1][0][0]) : "memory"); SB();
  asm volatile("v_alignbit_b32 %0, v231, v231, 16" : "=v"(im[0][0]) :  : ); SB();
  asm volatile("v_mfma_f32_16x16x32_bf16 %0, v[172:175], %1, %0" : "+v"(c1[0]) : "v"(ones) : ); SB();
  asm volatile("ds_read_b64_tr_b16 v[164:165], %0 offset:36864" :  : "v"(aW[1][0][1]) : "memory"); SB();
  asm volatile("v_alignbit_b32 %0, v230, v230, 16" : "=v"(im[0][1]) :  : ); SB();
  asm volatile("v_mfma_f32_16x16x32_bf16 %0, v[176:179], a[64:67], %0" : "+v"(a1[0][1]) :  : ); SB();
  asm volatile("ds_read_b64_tr_b16 v[166:167], %0 offset:37632" :  : "v"(aW[1][0][1]) : "memory"); SB();
  asm volatile("v_alignbit_b32 %0, v229, v229, 16" : "=v"(im[0][2]) :  : ); SB();
  asm volatile("v_mfma_f32_16x16x32_bf16 %0, v[176:179], a[68:71], %0" : "+v"(a1[1][1]) :  : ); SB();
  asm volatile("ds_read_b64_tr_b16 v[168:169], %0 offset:36864" :  : "v"(aW[1][0][2]) : "memory"); SB();
  asm volatile("v_alignbit_b32 %0, v228, v228, 16" : "=v"(im[0][3]) :  : ); SB();
  asm volatile("v_mfma_f32_16x16x32_bf16 %0, v[176:179], %1, %0" : "+v"(c1[1]) : "v"(ones) : ); SB();
  asm volatile("ds_read_b64_tr_b16 v[170:171], %0 offset:37632" :  : "v"(aW[1][0][2]) : "memory"); SB();
  asm volatile("v_bfi_b32 %0, %1, v231, v230" : "=v"(im[1][0]) : "s"(0xFFFFu) : ); SB();
  asm volatile("v_mfma_f32_16x16x32_bf16 %0, v[180:183], a[64:67], %0" : "+v"(a1[0][2]) :  : ); SB();
  asm volatile("buffer_load_dwordx4 %0, %1, %2 offen offset:2048 lds" :  : "v"(voS), "s"(rA), "s"(ao[2]) : "memory"); SB();
  asm volatile("v_bfi_b32 %0, %1, v230, v229" : "=v"(im[1][1]) : "s"(0xFFFFu) : ); SB();
  asm volatile("v_mfma_f32_16x16x32_bf16 %0, v[180:183], a[68:71], %0" : "+v"(a1[1][2]) :  : ); SB();
  asm volatile("s_waitcnt vmcnt(10)" :  :  : "memory"); SB();
  asm volatile("ds_read_b128 a[172:175], %0 offset:38912" :  : "v"(aA[0]) : "memory"); SB();
  asm volatile("v_mfma_f32_16x16x32_bf16 %0, v[180:183], %1, %0" : "+v"(c1[2]) : "v"(ones) : ); SB();
  asm volatile("ds_read_b128 a[188:191], %0 offset:38912" :  : "v"(aA[1]) : "memory"); SB();
  asm volatile("s_waitcnt lgkmcnt(8)\n\tv_mfma_f32_16x16x32_bf16 %0, v[184:187], a[72:75], %0" : "+v"(a1[0][0]) :  : ); SB();
  asm volatile("ds_read_b64_tr_b16 v[172:173], %0 offset:36864" :  : "v"(aW[1][1][0]) : "memory"); SB();
  asm volatile("v_bfi_b32 %0, %1, v229, v228" : "=v"(im[1][2]) : "s"(0xFFFFu) : ); SB();
  asm volatile("v_mfma_f32_16x16x32_bf16 %0, v[184:187], a[76:79], %0" : "+v"(a1[1][0]) :  : ); SB();
  asm volatile("ds_read_b64_tr_b16 v[174:175], %0 offset:37632" :  : "v"(aW[1][1][0]) : "memory"); SB();
  asm volatile("buffer_load_dwordx4 %0, %1, %2 offen offset:3072 lds" :  : "v"(voS), "s"(rA), "s"(ao[3]) : "memory"); SB();
  asm volatile("v_mfma_f32_16x16x32_bf16 %0, v[188:191], a[72:75], %0" : "+v"(a1[0][1]) :  : ); SB();
  asm volatile("ds_read_b64_tr_b16 v[176:177], %0 offset:36864" :  : "v"(aW[1][1][1]) : "memory"); SB();
  asm volatile("s_add_u32 m0, %0, 102400" :  : "s"(mA) : "memory", "scc"); SB();
  asm volatile("v_mfma_f32_16x16x32_bf16 %0, v[188:191], a[76:79], %0" : "+v"(a1[1][1]) :  : ); SB();
  asm volatile("ds_read_b64_tr_b16 v[178:179], %0 offset:37632" :  : "v"(aW[1][1][1]) : "memory"); SB();
  asm volatile("v_bfi_b32 %0, %1, v228, v242" : "=v"(im[1][3]) : "s"(0xFFFFu) : ); SB();
  asm volatile("v_mfma_f32_16x16x32_bf16 %0, v[192:195], a[72:75], %0" : "+v"(a1[0][2]) :  : ); SB();
  asm volatile("ds_read_b64_tr_b16 v[180:181], %0 offset:36864" :  : "v"(aW[1][1][2]) : "memory"); SB();
  asm volatile("buffer_load_dwordx4 %0, %1, %2 offen lds" :  : "v"(voS), "s"(rA), "s"(ao[4]) : "memory"); SB();
  asm volatile("v_mfma_f32_16x16x32_bf16 %0, v[192:195], a[76:79], %0" : "+v"(a1[1][2]) :  : ); SB();
  asm volatile("ds_read_b64_tr_b16 v[182:183], %0 offset:37632" :  : "v"(aW[1][1][2]) : "memory"); SB();
  asm volatile("s_waitcnt lgkmcnt(8)\n\tv_mfma_f32_16x16x32_bf16 %0, v[160:163], a[80:83], %0" : "+v"(a1[0][0]) :  : ); SB();
  unsigned vFZ = W_FZ + cb * 1024 + w * 256 + (lane & 31) * 8;
  unsigned vB = W_Z2 + (w * 1024 + (cb & 5) * 128 + cb * 6) * 1024 + l16;
  unsigned ha = lb + 59520 + 4608 + (2040 - 8 * (w * 64 + lane)) * 2;
  unsigned za = lb + 59520 + min(w, 2u) * 12928 + (lane < 32 ? lane * 16 : 4608 + ((lane >> 1) & 1) * 4160 + 4096 + (lane & 1) * 16);
  unsigned ew[2] = {lb + 32768 + w * 6144 + wn * 3072 + l16, lb + 32768 + (w ^ 1) * 6144 + wn * 3072 + l16};
  unsigned aa[4], bb;
  {
    const unsigned c = (l15 + 1) & 1;
    const unsigned o = lb + 59520 + 2 * 12928 - w * 128 - 192;
#pragma unroll
    for (int k = 0; k < 4; ++k) aa[k] = o + 4608 + c * 4160 + (2031 - l15 + 8 * g4 - c) * 2 - 512 - 1024 * k;
    bb = o + 512 + 32 * l15 - 32 + 16 * g4;
  }
  unsigned dd = lb + 0 + w * 8192 + g4 * 256 + ((l15 ^ ((g4 >> 1) << 3)) << 4);
  asm volatile("" : "+v"(aa[0]), "+v"(aa[1]), "+v"(aa[2]), "+v"(aa[3]), "+v"(bb), "+v"(dd), "+v"(vFZ), "+v"(vB), "+v"(ha), "+v"(za), "+v"(ew[0]), "+v"(ew[1]));
  SB();
  asm volatile("s_waitcnt vmcnt(10)" :  :  : "memory"); SB();
  asm volatile("ds_read_b128 a[192:195], %0 offset:0" :  : "v"(aA[0]) : "memory"); SB();
  asm volatile("ds_read_b128 a[208:211], %0 offset:0" :  : "v"(aA[1]) : "memory"); SB();
  asm volatile("buffer_load_dwordx4 %0, %1, %2 offen offset:1024 lds" :  : "v"(voS), "s"(rA), "s"(ao[5]) : "memory"); SB();
  asm volatile("buffer_load_dwordx4 %0, %1, %2 offen offset:2048 lds" :  : "v"(voS), "s"(rA), "s"(ao[6]) : "memory"); SB();
  asm volatile("s_barrier" :  :  : "memory"); SB();
  asm volatile("ds_read_b64_tr_b16 v[184:185], %0 offset:0" :  : "v"(aW[0][0][0]) : "memory"); SB();
  asm volatile("buffer_load_dwordx4 %0, %1, %2 offen offset:3072 lds" :  : "v"(voS), "s"(rA), "s"(ao[7]) : "memory"); SB();
  asm volatile("v_mfma_f32_16x16x32_bf16 %0, v[160:163], a[84:87], %0" : "+v"(a1[1][0]) :  : ); SB();
  asm volatile("ds_read_b64_tr_b16 v[186:187], %0 offset:768" :  : "v"(aW[0][0][0]) : "memory"); SB();
  asm volatile("v_mov_b32_dpp v243, v235 wave_shr:1 row_mask:0xf bank_mask:0xf" :  :  : ); SB();
  asm volatile("v_mfma_f32_16x16x32_bf16 %0, v[160:163], %1, %0" : "+v"(c1[0]) : "v"(ones) : ); SB();
  asm volatile("ds_read_b64_tr_b16 v[188:189], %0 offset:0" :  : "v"(aW[0][0][1]) : "memory"); SB();
  asm volatile("v_alignbit_b32 %0, v235, v235, 16" : "=v"(im[2][0]) :  : ); SB();
  asm volatile("v_mfma_f32_16x16x32_bf16 %0, v[164:167], a[80:83], %0" : "+v"(a1[0][1]) :  : ); SB();
  asm volatile("ds_read_b64_tr_b16 v[190:191], %0 offset:768" :  : "v"(aW[0][0][1]) : "memory"); SB();
  asm volatile("v_alignbit_b32 %0, v234, v234, 16" : "=v"(im[2][1]) :  : ); SB();
  asm volatile("v_mfma_f32_16x16x32_bf16 %0, v[164:167], a[84:87], %0" : "+v"(a1[1][1]) :  : ); SB();
  asm volatile("ds_read_b64_tr_b16 v[192:193], %0 offset:0" :  : "v"(aW[0][0][2]) : "memory"); SB();
  asm volatile("v_alignbit_b32 %0, v233, v233, 16" : "=v"(im[2][2]) :  : ); SB();
  asm volatile("v_mfma_f32_16x16x32_bf16 %0, v[164:167], %1, %0" : "+v"(c1[1]) : "v"(ones) : ); SB();
  asm volatile("ds_read_b64_tr_b16 v[194:195], %0 offset:768" :  : "v"(aW[0][0][2]) : "memory"); SB();
  asm volatile("v_alignbit_b32 %0, v232, v232, 16" : "=v"(im[2][3]) :  : ); SB();
  asm volatile("v_mfma_f32_16x16x32_bf16 %0, v[168:171], a[80:83], %0" : "+v"(a1[0][2]) :  : ); SB();
  asm volatile("v_xor_b32 %0, 384, %0" : "+v"(voS) :  : "memory"); SB();
  asm volatile("s_add_u32 m0, %0, 131072" :  : "s"(mA) : "memory", "scc"); SB();
  asm volatile("v_mfma_f32_16x16x32_bf16 %0, v[168:171], a[84:87], %0" : "+v"(a1[1][2]) :  : ); SB();
  asm volatile("buffer_load_dwordx4 %0, %1, %2 offen lds" :  : "v"(voS), "s"(rA), "s"(ao[0]) : "memory"); SB();
  asm volatile("v_bfi_b32 %0, %1, v235, v234" : "=v"(im[3][0]) : "s"(0xFFFFu) : ); SB();
  asm volatile("v_mfma_f32_16x16x32_bf16 %0, v[168:171], %1, %0" : "+v"(c1[2]) : "v"(ones) : ); SB();
  asm volatile("buffer_load_dwordx4 %0, %1, %2 offen offset:1024 lds" :  : "v"(voS), "s"(rA), "s"(ao[1]) : "memory"); SB();
  asm volatile("s_waitcnt lgkmcnt(5)\n\tv_mfma_f32_16x16x32_bf16 %0, v[172:175], a[88:91], %0" : "+v"(a1[0][0]) :  : ); SB();
  asm volatile("ds_read_b64_tr_b16 v[160:161], %0 offset:0" :  : "v"(aW[0][1][0]) : "memory"); SB();
  asm volatile("v_bfi_b32 %0, %1, v234, v233" : "=v"(im[3][1]) : "s"(0xFFFFu) : ); SB();
  asm volatile("v_mfma_f32_16x16x32_bf16 %0, v[172:175], a[92:95], %0" : "+v"(a1[1][0]) :  : ); SB();
  asm volatile("ds_read_b64_tr_b16 v[162:163], %0 offset:768" :  : "v"(aW[0][1][0]) : "memory"); SB();
  asm volatile("v_bfi_b32 %0, %1, v233, v232" : "=v"(im[3][2]) : "s"(0xFFFFu) : ); SB();
  asm volatile("v_mfma_f32_16x16x32_bf16 %0, v[176:179], a[88:91], %0" : "+v"(a1[0][1]) :  : ); SB();
  asm volatile("ds_read_b64_tr_b16 v[164:165], %0 offset:0" :  : "v"(aW[0][1][1]) : "memory"); SB();
  asm volatile("v_bfi_b32 %0, %1, v232, v243" : "=v"(im[3][3]) : "s"(0xFFFFu) : ); SB();
  asm volatile("v_mfma_f32_16x16x32_bf16 %0, v[176:179], a[92:95], %0" : "+v"(a1[1][1]) :  : ); SB();
  asm volatile("ds_read_b64_tr_b16 v[166:167], %0 offset:768" :  : "v"(aW[0][1][1]) : "memory"); SB();
  asm volatile("v_mov_b32_dpp v244, v239 wave_shr:1 row_mask:0xf bank_mask:0xf" :  :  : ); SB();
  asm volatile("v_mfma_f32_16x16x32_bf16 %0, v[180:183], a[88:91], %0" : "+v"(a1[0][2]) :  : ); SB();
  asm volatile("ds_read_b64_tr_b16 v[168:169], %0 offset:0" :  : "v"(aW[0][1][2]) : "memory"); SB();
  asm volatile("v_alignbit_b32 %0, v239, v239, 16" : "=v"(im[4][0]) :  : ); SB();
  asm volatile("v_mfma_f32_16x16x32_bf16 %0, v[180:183], a[92:95], %0" : "+v"(a1[1][2]) :  : ); SB();
  asm volatile("ds_read_b64_tr_b16 v[170:171], %0 offset:768" :  : "v"(aW[0][1][2]) : "memory"); SB();
  asm volatile("s_waitcnt lgkmcnt(6)\n\tv_mfma_f32_16x16x32_bf16 %0, v[184:187], a[96:99], %0" : "+v"(a1[0][0]) :  : ); SB();
  asm volatile("ds_read_b64_tr_b16 v[172:173], %0 offset:12288" :  : "v"(aW[0][0][0]) : "memory"); SB();
  asm volatile("v_alignbit_b32 %0, v238, v238, 16" : "=v"(im[4][1]) :  : ); SB();
  asm volatile("v_mfma_f32_16x16x32_bf16 %0, v[184:187], a[100:103], %0" : "+v"(a1[1][0]) :  : ); SB();
  asm volatile("ds_read_b64_tr_b16 v[174:175], %0 offset:13056" :  : "v"(aW[0][0][0]) : "memory"); SB();
  asm volatile("v_alignbit_b32 %0, v237, v237, 16" : "=v"(im[4][2]) :  : ); SB();
  asm volatile("v_mfma_f32_16x16x32_bf16 %0, v[184:187], %1, %0" : "+v"(c1[0]) : "v"(ones) : ); SB();
  asm volatile("ds_read_b64_tr_b16 v[176:177], %0 offset:12288" :  : "v"(aW[0][0][1]) : "memory"); SB();
  asm volatile("v_alignbit_b32 %0, v236, v236, 16" : "=v"(im[4][3]) :  : ); SB();
  asm volatile("v_mfma_f32_16x16x32_bf16 %0, v[188:191], a[96:99], %0" : "+v"(a1[0][1]) :  : ); SB();
  asm volatile("ds_read_b64_tr_b16 v[178:179], %0 offset:13056" :  : "v"(aW[0][0][1]) : "memory"); SB();
  asm volatile("v_bfi_b32 %0, %1, v239, v238" : "=v"(im[5][0]) : "s"(0xFFFFu) : ); SB();
  asm volatile("v_mfma_f32_16x16x32_bf16 %0, v[188:191], a[100:103], %0" : "+v"(a1[1][1]) :  : ); SB();
  asm volatile("ds_read_b64_tr_b16 v[180:181], %0 offset:12288" :  : "v"(aW[0][0][2]) : "memory"); SB();
  asm volatile("v_bfi_b32 %0, %1, v238, v237" : "=v"(im[5][1]) : "s"(0xFFFFu) : ); SB();
  asm volatile("v_mfma_f32_16x16x32_bf16 %0, v[188:191], %1, %0" : "+v"(c1[1]) : "v"(ones) : ); SB();
  asm volatile("ds_read_b64_tr_b16 v[182:183], %0 offset:13056" :  : "v"(aW[0][0][2]) : "memory"); SB();
  asm volatile("v_bfi_b32 %0, %1, v237, v236" : "=v"(im[5][2]) : "s"(0xFFFFu) : ); SB();
  asm volatile("v_mfma_f32_16x16x32_bf16 %0, v[192:195], a[96:99], %0" : "+v"(a1[0][2]) :  : ); SB();
  asm volatile("buffer_load_dwordx4 %0, %1, %2 offen offset:2048 lds" :  : "v"(voS), "s"(rA), "s"(ao[2]) : "memory"); SB();
  asm volatile("v_bfi_b32 %0, %1, v236, v244" : "=v"(im[5][3]) : "s"(0xFFFFu) : ); SB();
  asm volatile("v_mfma_f32_16x16x32_bf16 %0, v[192:195], a[100:103], %0" : "+v"(a1[1][2]) :  : ); SB();
  asm volatile("buffer_load_dwordx4 %0, %1, %2 offen offset:3072 lds" :  : "v"(voS), "s"(rA), "s"(ao[3]) : "memory"); SB();
  asm volatile("s_waitcnt vmcnt(12)\n\ts_mov_b64 exec, 1\n\tbuffer_store_dwordx2 %0, off, %1, %2 sc1\n\ts_mov_b64 exec, -1" :  : "v"(((u32x2){tg0, tg1})), "s"(rWS), "s"(sFZ) : "memory"); SB();
  asm volatile("v_mfma_f32_16x16x32_bf16 %0, v[192:195], %1, %0" : "+v"(c1[2]) : "v"(ones) : ); SB();
  asm volatile("v_mov_b32 %0, 0" : "=v"(zero4[0]) :  : ); SB();
  asm volatile("v_mov_b32 %0, 0" : "=v"(zero4[1]) :  : ); SB();
  asm volatile("v_mov_b32 %0, 0" : "=v"(zero4[2]) :  : ); SB();
  asm volatile("v_mov_b32 %0, 0" : "=v"(zero4[3]) :  : ); SB();
  asm volatile("s_waitcnt lgkmcnt(3)\n\tv_mfma_f32_16x16x32_bf16 %0, v[160:163], a[104:107], %0" : "+v"(a1[0][0]) :  : ); SB();
  asm volatile("ds_read_b64_tr_b16 v[184:185], %0 offset:12288" :  : "v"(aW[0][1][0]) : "memory"); SB();
  asm volatile("v_mfma_f32_16x16x32_bf16 %0, v[160:163], a[108:111], %0" : "+v"(a1[1][0]) :  : ); SB();
  asm volatile("s_mov_b32 exec_lo, 0x10001\n\ts_mov_b32 exec_hi, 0x10001\n\tds_write_b128 %0, %1 offset:0\n\ts_mov_b64 exec, -1" :  : "v"(ca), "v"(c1[0]) : "memory"); SB();
  asm volatile("ds_read_b64_tr_b16 v[186:187], %0 offset:13056" :  : "v"(aW[0][1][0]) : "memory"); SB();
  asm volatile("s_add_u32 m0, %0, 135168" :  : "s"(mA) : "memory", "scc"); SB();
  asm volatile("v_mfma_f32_16x16x32_bf16 %0, v[164:167], a[104:107], %0" : "+v"(a1[0][1]) :  : ); SB();
  asm volatile("ds_read_b64_tr_b16 v[188:189], %0 offset:12288" :  : "v"(aW[0][1][1]) : "memory"); SB();
  asm volatile("v_mfma_f32_16x16x32_bf16 %0, v[164:167], a[108:111], %0" : "+v"(a1[1][1]) :  : ); SB();
  asm volatile("s_mov_b32 exec_lo, 0x10001\n\ts_mov_b32 exec_hi, 0x10001\n\tds_write_b128 %0, %1 offset:64\n\ts_mov_b64 exec, -1" :  : "v"(ca), "v"(c1[1]) : "memory"); SB();
  asm volatile("ds_read_b64_tr_b16 v[190:191], %0 offset:13056" :  : "v"(aW[0][1][1]) : "memory"); SB();
  asm volatile("buffer_load_dwordx4 %0, %1, %2 offen lds" :  : "v"(voS), "s"(rA), "s"(ao[4]) : "memory"); SB();
  asm volatile("v_mfma_f32_16x16x32_bf16 %0, v[168:171], a[104:107], %0" : "+v"(a1[0][2]) :  : ); SB();
  asm volatile("ds_read_b64_tr_b16 v[192:193], %0 offset:12288" :  : "v"(aW[0][1][2]) : "memory"); SB();
  asm volatile("v_mfma_f32_16x16x32_bf16 %0, v[168:171], a[108:111], %0" : "+v"(a1[1][2]) :  : ); SB();
  asm volatile("s_mov_b32 exec_lo, 0x10001\n\ts_mov_b32 exec_hi, 0x10001\n\tds_write_b128 %0, %1 offset:128\n\ts_mov_b64 exec, -1" :  : "v"(ca), "v"(c1[2]) : "memory"); SB();
  asm volatile("ds_read_b64_tr_b16 v[194:195], %0 offset:13056" :  : "v"(aW[0][1][2]) : "memory"); SB();
  asm volatile("buffer_load_dwordx4 %0, %1, %2 offen offset:1024 lds" :  : "v"(voS), "s"(rA), "s"(ao[5]) : "memory"); SB();
  asm volatile("v_mfma_f32_16x16x32_bf16 %0, v[172:175], a[112:115], %0" : "+v"(a1[0][0]) :  : ); SB();
  asm volatile("s_waitcnt vmcnt(11)" :  :  : "memory"); SB();
  asm volatile("ds_read_b128 a[196:199], %0 offset:2048" :  : "v"(aA[0]) : "memory"); SB();
  asm volatile("v_mfma_f32_16x16x32_bf16 %0, v[172:175], a[116:119], %0" : "+v"(a1[1][0]) :  : ); SB();
  asm volatile("ds_read_b128 a[212:215], %0 offset:2048" :  : "v"(aA[1]) : "memory"); SB();
  asm volatile("v_mfma_f32_16x16x32_bf16 %0, v[172:175], %1, %0" : "+v"(c1[0]) : "v"(ones) : ); SB();
  asm volatile("buffer_load_dwordx4 %0, %1, %2 offen offset:2048 lds" :  : "v"(voS), "s"(rA), "s"(ao[6]) : "memory"); SB();
  asm volatile("s_waitcnt lgkmcnt(7)\n\tv_mfma_f32_16x16x32_bf16 %0, v[176:179], a[112:115], %0" : "+v"(a1[0][1]) :  : ); SB();
  asm volatile("s_waitcnt lgkmcnt(3)\n\ts_barrier" :  :  : "memory"); SB();
  asm volatile("ds_read_b64_tr_b16 v[160:161], %0 offset:24576" :  : "v"(aW[0][0][0]) : "memory"); SB();
  asm volatile("s_waitcnt vmcnt(10)" :  :  : "memory"); SB();
  asm volatile("v_mfma_f32_16x16x32_bf16 %0, v[176:179], a[116:119], %0" : "+v"(a1[1][1]) :  : ); SB();
  asm volatile("ds_read_b64_tr_b16 v[162:163], %0 offset:25344" :  : "v"(aW[0][0][0]) : "memory"); SB();
  asm volatile("ds_read_b128 a[200:203], %0 offset:4096" :  : "v"(aA[0]) : "memory"); SB();
  asm volatile("v_mfma_f32_16x16x32_bf16 %0, v[176:179], %1, %0" : "+v"(c1[1]) : "v"(ones) : ); SB();
  asm volatile("ds_read_b64_tr_b16 v[164:165], %0 offset:24576" :  : "v"(aW[0][0][1]) : "memory"); SB();
  asm volatile("ds_read_b128 a[216:219], %0 offset:4096" :  : "v"(aA[1]) : "memory"); SB();
  asm volatile("v_mfma_f32_16x16x32_bf16 %0, v[180:183], a[112:115], %0" : "+v"(a1[0][2]) :  : ); SB();
  asm volatile("ds_read_b64_tr_b16 v[166:167], %0 offset:25344" :  : "v"(aW[0][0][1]) : "memory"); SB();
  asm volatile("buffer_load_dwordx4 %0, %1, %2 offen offset:3072 lds" :  : "v"(voS), "s"(rA), "s"(ao[7]) : "memory"); SB();
  asm volatile("v_mfma_f32_16x16x32_bf16 %0, v[180:183], a[116:119], %0" : "+v"(a1[1][2]) :  : ); SB();
  asm volatile("ds_read_b64_tr_b16 v[168:169], %0 offset:24576" :  : "v"(aW[0][0][2]) : "memory"); SB();
  asm volatile("v_xor_b32 %0, 128, %0" : "+v"(voS) :  : "memory"); SB();
  asm volatile("v_mfma_f32_16x16x32_bf16 %0, v[180:183], %1, %0" : "+v"(c1[2]) : "v"(ones) : ); SB();
  asm volatile("ds_read_b64_tr_b16 v[170:171], %0 offset:25344" :  : "v"(aW[0][0][2]) : "memory"); SB();
  asm volatile("s_add_u32 m0, %0, 98304" :  : "s"(mA) : "memory", "scc"); SB();
  asm volatile("v_mfma_f32_16x16x32_bf16 %0, v[184:187], a[120:123], %0" : "+v"(a1[0][0]) :  : ); SB();
  asm volatile("buffer_load_dwordx4 %0, %1, %2 offen lds" :  : "v"(voS), "s"(rA), "s"(ao[0]) : "memory"); SB();
  asm volatile("v_mfma_f32_16x16x32_bf16 %0, v[184:187], a[124:127], %0" : "+v"(a1[1][0]) :  : ); SB();
  asm volatile("s_waitcnt vmcnt(10)" :  :  : "memory"); SB();
  asm volatile("ds_read_b128 a[204:207], %0 offset:6144" :  : "v"(aA[0]) : "memory"); SB();
  asm volatile("v_mfma_f32_16x16x32_bf16 %0, v[184:187], %1, %0" : "+v"(c1[0]) : "v"(ones) : ); SB();
  asm volatile("ds_read_b128 a[220:223], %0 offset:6144" :  : "v"(aA[1]) : "memory"); SB();
  asm volatile("v_mfma_f32_16x16x32_bf16 %0, v[188:191], a[120:123], %0" : "+v"(a1[0][1]) :  : ); SB();
  asm volatile("ds_read_b64_tr_b16 v[172:173], %0 offset:24576" :  : "v"(aW[0][1][0]) : "memory"); SB();
  asm volatile("buffer_load_dwordx4 %0, %1, %2 offen offset:1024 lds" :  : "v"(voS), "s"(rA), "s"(ao[1]) : "memory"); SB();
  asm volatile("v_mfma_f32_16x16x32_bf16 %0, v[188:191], a[124:127], %0" : "+v"(a1[1][1]) :  : ); SB();
  asm volatile("ds_read_b64_tr_b16 v[174:175], %0 offset:25344" :  : "v"(aW[0][1][0]) : "memory"); SB();
  asm volatile("s_waitcnt lgkmcnt(8)" :  :  : "memory"); SB();
  asm volatile("v_mfma_f32_16x16x32_bf16 %0, v[188:191], %1, %0" : "+v"(c1[1]) : "v"(ones) : ); SB();
  asm volatile("ds_read_b64_tr_b16 v[176:177], %0 offset:24576" :  : "v"(aW[0][1][1]) : "memory"); SB();
  asm volatile("buffer_load_dwordx4 %0, %1, %2 offen offset:2048 lds" :  : "v"(voS), "s"(rA), "s"(ao[2]) : "memory"); SB();
  asm volatile("v_mfma_f32_16x16x32_bf16 %0, v[192:195], a[120:123], %0" : "+v"(a1[0][2]) :  : ); SB();
  asm volatile("ds_read_b64_tr_b16 v[178:179], %0 offset:25344" :  : "v"(aW[0][1][1]) : "memory"); SB();
  asm volatile("v_mfma_f32_16x16x32_bf16 %0, v[192:195], a[124:127], %0" : "+v"(a1[1][2]) :  : ); SB();
  asm volatile("ds_read_b64_tr_b16 v[180:181], %0 offset:24576" :  : "v"(aW[0][1][2]) : "memory"); SB();
  asm volatile("v_mfma_f32_16x16x32_bf16 %0, v[192:195], %1, %0" : "+v"(c1[2]) : "v"(ones) : ); SB();
  asm volatile("ds_read_b64_tr_b16 v[182:183], %0 offset:25344" :  : "v"(aW[0][1][2]) : "memory"); SB();
  asm volatile("s_waitcnt vmcnt(10)" :  :  : "memory"); SB();
  asm volatile("v_mfma_f32_16x16x32_bf16 %0, v[160:163], a[128:131], %0" : "+v"(a1[0][0]) :  : ); SB();
  asm volatile("ds_read_b128 a[224:227], %0 offset:32768" :  : "v"(aA[0]) : "memory"); SB();
  asm volatile("v_mfma_f32_16x16x32_bf16 %0, v[160:163], a[132:135], %0" : "+v"(a1[1][0]) :  : ); SB();
  asm volatile("ds_read_b128 a[240:243], %0 offset:32768" :  : "v"(aA[1]) : "memory"); SB();
  asm volatile("v_mfma_f32_16x16x32_bf16 %0, v[160:163], %1, %0" : "+v"(c1[0]) : "v"(ones) : ); SB();
  asm volatile("buffer_load_dwordx4 %0, %1, %2 offen offset:3072 lds" :  : "v"(voS), "s"(rA), "s"(ao[3]) : "memory"); SB();
  asm volatile("s_waitcnt lgkmcnt(8)\n\tv_mfma_f32_16x16x32_bf16 %0, v[164:167], a[128:131], %0" : "+v"(a1[0][1]) :  : ); SB();
  asm volatile("ds_read_b64_tr_b16 v[184:185], %0 offset:36864" :  : "v"(aW[0][0][0]) : "memory"); SB();
  asm volatile("s_add_u32 m0, %0, 102400" :  : "s"(mA) : "memory", "scc"); SB();
  asm volatile("v_mfma_f32_16x16x32_bf16 %0, v[164:167], a[132:135], %0" : "+v"(a1[1][1]) :  : ); SB();
  asm volatile("ds_read_b64_tr_b16 v[186:187], %0 offset:37632" :  : "v"(aW[0][0][0]) : "memory"); SB();
  asm volatile("buffer_load_dwordx4 %0, %1, %2 offen lds" :  : "v"(voS), "s"(rA), "s"(ao[4]) : "memory"); SB();
  asm volatile("v_mfma_f32_16x16x32_bf16 %0, v[164:167], %1, %0" : "+v"(c1[1]) : "v"(ones) : ); SB();
  asm volatile("ds_read_b64_tr_b16 v[188:189], %0 offset:36864" :  : "v"(aW[0][0][1]) : "memory"); SB();
  asm volatile("v_mfma_f32_16x16x32_bf16 %0, v[168:171], a[128:131], %0" : "+v"(a1[0][2]) :  : ); SB();
  asm volatile("ds_read_b64_tr_b16 v[190:191], %0 offset:37632" :  : "v"(aW[0][0][1]) : "memory"); SB();
  asm volatile("v_mfma_f32_16x16x32_bf16 %0, v[168:171], a[132:135], %0" : "+v"(a1[1][2]) :  : ); SB();
  asm volatile("ds_read_b64_tr_b16 v[192:193], %0 offset:36864" :  : "v"(aW[0][0][2]) : "memory"); SB();
  asm volatile("v_mfma_f32_16x16x32_bf16 %0, v[168:171], %1, %0" : "+v"(c1[2]) : "v"(ones) : ); SB();
  asm volatile("ds_read_b64_tr_b16 v[194:195], %0 offset:37632" :  : "v"(aW[0][0][2]) : "memory"); SB();
  asm volatile("s_waitcnt lgkmcnt(8)\n\tv_mfma_f32_16x16x32_bf16 %0, v[172:175], a[136:139], %0" : "+v"(a1[0][0]) :  : ); SB();
  asm volatile("ds_read_b128 %0, %1 offset:0" : "=&v"(co[0]) : "v"(cr) : "memory"); SB();
  asm volatile("v_mfma_f32_16x16x32_bf16 %0, v[172:175], a[140:143], %0" : "+v"(a1[1][0]) :  : ); SB();
  asm volatile("ds_read_b128 %0, %1 offset:64" : "=&v"(co[1]) : "v"(cr) : "memory"); SB();
  asm volatile("v_mfma_f32_16x16x32_bf16 %0, v[172:175], %1, %0" : "+v"(c1[0]) : "v"(ones) : ); SB();
  asm volatile("buffer_load_dwordx2 %0, %1, %2, %3 offen sc1" : "=&v"(fk) : "v"(vFZ), "s"(rWS), "s"(8192u) : "memory"); SB();
  asm volatile("v_mfma_f32_16x16x32_bf16 %0, v[176:179], a[136:139], %0" : "+v"(a1[0][1]) :  : ); SB();
  asm volatile("ds_read_b64_tr_b16 v[160:161], %0 offset:36864" :  : "v"(aW[0][1][0]) : "memory"); SB();
  asm volatile("buffer_load_dwordx4 %0, %1, %2 offen offset:1024 lds" :  : "v"(voS), "s"(rA), "s"(ao[5]) : "memory"); SB();
  asm volatile("v_mfma_f32_16x16x32_bf16 %0, v[176:179], a[140:143], %0" : "+v"(a1[1][1]) :  : ); SB();
  asm volatile("ds_read_b64_tr_b16 v[162:163], %0 offset:37632" :  : "v"(aW[0][1][0]) : "memory"); SB();
  asm volatile("v_mfma_f32_16x16x32_bf16 %0, v[176:179], %1, %0" : "+v"(c1[1]) : "v"(ones) : ); SB();
  asm volatile("ds_read_b64_tr_b16 v[164:165], %0 offset:36864" :  : "v"(aW[0][1][1]) : "memory"); SB();
  asm volatile("s_waitcnt vmcnt(12)" :  :  : "memory"); SB();
  asm volatile("v_mfma_f32_16x16x32_bf16 %0, v[180:183], a[136:139], %0" : "+v"(a1[0][2]) :  : ); SB();
  asm volatile("ds_read_b64_tr_b16 v[166:167], %0 offset:37632" :  : "v"(aW[0][1][1]) : "memory"); SB();
  asm volatile("ds_read_b128 a[228:231], %0 offset:34816" :  : "v"(aA[0]) : "memory"); SB();
  asm volatile("v_mfma_f32_16x16x32_bf16 %0, v[180:183], a[140:143], %0" : "+v"(a1[1][2]) :  : ); SB();
  asm volatile("ds_read_b64_tr_b16 v[168:169], %0 offset:36864" :  : "v"(aW[0][1][2]) : "memory"); SB();
  asm volatile("ds_read_b128 a[244:247], %0 offset:34816" :  : "v"(aA[1]) : "memory"); SB();
  asm volatile("v_mfma_f32_16x16x32_bf16 %0, v[180:183], %1, %0" : "+v"(c1[2]) : "v"(ones) : ); SB();
  asm volatile("ds_read_b64_tr_b16 v[170:171], %0 offset:37632" :  : "v"(aW[0][1][2]) : "memory"); SB();
  asm volatile("buffer_load_dwordx4 %0, %1, %2 offen offset:2048 lds" :  : "v"(voS), "s"(rA), "s"(ao[6]) : "memory"); SB();
  asm volatile("s_waitcnt lgkmcnt(9)\n\tv_mfma_f32_16x16x32_bf16 %0, v[184:187], a[144:147], %0" : "+v"(a1[0][0]) :  : ); SB();
  asm volatile("ds_read_b128 %0, %1 offset:128" : "=&v"(co[2]) : "v"(cr) : "memory"); SB();
  asm volatile("v_mfma_f32_16x16x32_bf16 %0, v[184:187], a[148:151], %0" : "+v"(a1[1][0]) :  : ); SB();
  asm volatile("ds_write_b128 %0, %1 offset:0" :  : "v"(ha), "v"(im[0]) : "memory"); SB();
  asm volatile("ds_write_b128 %0, %1 offset:4160" :  : "v"(ha), "v"(im[1]) : "memory"); SB();
  asm volatile("buffer_load_dwordx4 %0, %1, %2 offen offset:3072 lds" :  : "v"(voS), "s"(rA), "s"(ao[7]) : "memory"); SB();
  asm volatile("v_mfma_f32_16x16x32_bf16 %0, v[184:187], %1, %0" : "+v"(c1[0]) : "v"(ones) : ); SB();
  asm volatile("ds_write_b128 %0, %1 offset:12928" :  : "v"(ha), "v"(im[2]) : "memory"); SB();
  asm volatile("ds_write_b128 %0, %1 offset:17088" :  : "v"(ha), "v"(im[3]) : "memory"); SB();
  asm volatile("v_xor_b32 %0, 896, %0" : "+v"(voS) :  : "memory"); SB();
  asm volatile("s_add_u32 m0, %0, 131072" :  : "s"(mA) : "memory", "scc"); SB();
  asm volatile("v_mfma_f32_16x16x32_bf16 %0, v[188:191], a[144:147], %0" : "+v"(a1[0][1]) :  : ); SB();
  asm volatile("ds_write_b128 %0, %1 offset:25856" :  : "v"(ha), "v"(im[4]) : "memory"); SB();
  asm volatile("ds_write_b128 %0, %1 offset:30016" :  : "v"(ha), "v"(im[5]) : "memory"); SB();
  asm volatile("buffer_load_dwordx4 %0, %1, %2 offen lds" :  : "v"(voS), "s"(rA), "s"(ao[0]) : "memory"); SB();
  asm volatile("s_waitcnt vmcnt(12)" :  :  : "memory"); SB();
  asm volatile("v_mfma_f32_16x16x32_bf16 %0, v[188:191], a[148:151], %0" : "+v"(a1[1][1]) :  : ); SB();
  asm volatile("ds_read_b128 a[232:235], %0 offset:36864" :  : "v"(aA[0]) : "memory"); SB();
  asm volatile("v_mfma_f32_16x16x32_bf16 %0, v[188:191], %1, %0" : "+v"(c1[1]) : "v"(ones) : ); SB();
  asm volatile("ds_read_b128 a[248:251], %0 offset:36864" :  : "v"(aA[1]) : "memory"); SB();
  asm volatile("v_mfma_f32_16x16x32_bf16 %0, v[192:195], a[144:147], %0" : "+v"(a1[0][2]) :  : ); SB();
  asm volatile("ds_write_b128 %0, %1 offset:0" :  : "v"(za), "v"(zero4) : "memory"); SB();
  asm volatile("buffer_load_dwordx4 %0, %1, %2 offen offset:1024 lds" :  : "v"(voS), "s"(rA), "s"(ao[1]) : "memory"); SB();
  asm volatile("s_waitcnt lgkmcnt(5)" :  :  : "memory"); SB();
  asm volatile("v_mfma_f32_16x16x32_bf16 %0, v[192:195], a[148:151], %0" : "+v"(a1[1][2]) :  : ); SB();
  asm volatile("buffer_load_dwordx4 %0, %1, %2 offen offset:2048 lds" :  : "v"(voS), "s"(rA), "s"(ao[2]) : "memory"); SB();
  asm volatile("v_mfma_f32_16x16x32_bf16 %0, v[192:195], %1, %0" : "+v"(c1[2]) : "v"(ones) : ); SB();
  asm volatile("v_mfma_f32_16x16x32_bf16 %0, v[160:163], a[152:155], %0" : "+v"(a1[0][0]) :  : ); SB();
  asm volatile("v_mfma_f32_16x16x32_bf16 %0, v[160:163], a[156:159], %0" : "+v"(a1[1][0]) :  : ); SB();
  asm volatile("buffer_load_dwordx4 %0, %1, %2 offen offset:3072 lds" :  : "v"(voS), "s"(rA), "s"(ao[3]) : "memory"); SB();
  asm volatile("s_waitcnt vmcnt(13)" :  :  : "memory"); SB();
  asm volatile("v_mfma_f32_16x16x32_bf16 %0, v[160:163], %1, %0" : "+v"(c1[0]) : "v"(ones) : ); SB();
  asm volatile("ds_read_b128 a[236:239], %0 offset:38912" :  : "v"(aA[0]) : "memory"); SB();
  asm volatile("v_mfma_f32_16x16x32_bf16 %0, v[164:167], a[152:155], %0" : "+v"(a1[0][1]) :  : ); SB();
  asm volatile("ds_read_b128 a[252:255], %0 offset:38912" :  : "v"(aA[1]) : "memory"); SB();
  asm volatile("s_waitcnt lgkmcnt(3)" :  :  : "memory"); SB();
  asm volatile("v_mfma_f32_16x16x32_bf16 %0, v[164:167], a[156:159], %0" : "+v"(a1[1][1]) :  : ); SB();
  asm volatile("v_mfma_f32_16x16x32_bf16 %0, v[164:167], %1, %0" : "+v"(c1[1]) : "v"(ones) : ); SB();
  asm volatile("s_add_u32 m0, %0, 135168" :  : "s"(mA) : "memory", "scc"); SB();
  asm volatile("v_mfma_f32_16x16x32_bf16 %0, v[168:171], a[152:155], %0" : "+v"(a1[0][2]) :  : ); SB();
  asm volatile("buffer_load_dwordx4 %0, %1, %2 offen lds" :  : "v"(voS), "s"(rA), "s"(ao[4]) : "memory"); SB();
  asm volatile("v_mfma_f32_16x16x32_bf16 %0, v[168:171], a[156:159], %0" : "+v"(a1[1][2]) :  : ); SB();
  asm volatile("v_mfma_f32_16x16x32_bf16 %0, v[168:171], %1, %0" : "+v"(c1[2]) : "v"(ones) : ); SB();
  asm volatile("buffer_load_dwordx4 %0, %1, %2 offen offset:1024 lds" :  : "v"(voS), "s"(rA), "s"(ao[5]) : "memory"); SB();
  ST(11);
  f32x4 ac[8];
  u32x2 cA[2][8][2];
  asm volatile("s_waitcnt vmcnt(13)" :  :  : "memory"); SB();
  asm volatile("ds_read_b128 a[0:3], %0 offset:0" :  : "v"(aA[0]) : "memory"); SB();
  asm volatile("ds_read_b128 a[16:19], %0 offset:0" :  : "v"(aA[1]) : "memory"); SB();
  asm volatile("s_waitcnt lgkmcnt(2)" :  :  : "memory"); SB();
  asm volatile("buffer_load_dwordx4 %0, %1, %2 offen offset:2048 lds" :  : "v"(voS), "s"(rA), "s"(ao[6]) : "memory"); SB();
  asm volatile("buffer_load_dwordx4 %0, %1, %2 offen offset:3072 lds" :  : "v"(voS), "s"(rA), "s"(ao[7]) : "memory"); SB();
  asm volatile("buffer_load_dwordx2 %0, %1, %2, %3 offen sc1" : "=&v"(fz) : "v"(vFZ), "s"(rWS), "s"(0u) : "memory"); SB();
  asm volatile("s_waitcnt vmcnt(14)" :  :  : "memory"); SB();
  asm volatile("ds_read_b128 a[4:7], %0 offset:2048" :  : "v"(aA[0]) : "memory"); SB();
  asm volatile("ds_read_b128 a[20:23], %0 offset:2048" :  : "v"(aA[1]) : "memory"); SB();
  asm volatile("s_waitcnt lgkmcnt(2)" :  :  : "memory"); SB();
  asm volatile("v_xor_b32 %0, 128, %0" : "+v"(voS) :  : "memory"); SB();
  asm volatile("s_add_u32 m0, %0, 98304" :  : "s"(mA) : "memory", "scc"); SB();
  asm volatile("s_nop 0\n\tbuffer_load_dwordx4 %0, %1, %2 offen lds" :  : "v"(voS), "s"(rA), "s"(ao[0]) : "memory"); SB();
  asm volatile("buffer_load_dwordx4 %0, %1, %2 offen offset:1024 lds" :  : "v"(voS), "s"(rA), "s"(ao[1]) : "memory"); SB();
  asm volatile("s_waitcnt vmcnt(14)\n\t.Lc_%=:\n\tv_cmp_ne_u64 vcc, %2, %1\n\ts_cbranch_vccnz .Lr_%=\n\t.Ld_%=:\n\t.subsection 1\n\t.Lr_%=:\n\tv_add_u32 %0, -1, %0\n\ts_nop 1\n\tv_readfirstlane_b32 vcc_hi, %0\n\ts_nop 3\n\ts_cmp_lt_i32 vcc_hi, 1\n\ts_cbranch_scc1 .Ld_%=\n\ts_sleep 2\n\ts_mov_b32 vcc_lo, 8192\n\tbuffer_load_dwordx2 %1, %3, %4, vcc_lo offen sc1\n\ts_waitcnt vmcnt(0)\n\ts_branch .Lc_%=\n\t.subsection 0" : "+v"(tmo), "+v"(fk) : "s"(tg), "v"(vFZ), "s"(rWS) : "memory", "vcc", "scc"); SB();
  asm volatile("s_add_u32 m0, %0, 85888" :  : "s"(mU) : "memory", "scc"); SB();
  asm volatile("s_nop 0\n\tbuffer_load_dwordx4 %0, %1, %2 offen sc1 lds" :  : "v"(vF), "s"(rWS), "s"(uso) : "memory"); SB();
  asm volatile("s_waitcnt lgkmcnt(0)" :  :  : "memory"); SB();
  asm volatile("s_waitcnt vmcnt(14)" :  :  : "memory"); SB();
  asm volatile("ds_read_b128 a[8:11], %0 offset:4096" :  : "v"(aA[0]) : "memory"); SB();
  asm volatile("ds_read_b128 a[24:27], %0 offset:4096" :  : "v"(aA[1]) : "memory"); SB();
  asm volatile("s_add_u32 m0, %0, 98304" :  : "s"(mA) : "memory", "scc"); SB();
  asm volatile("s_nop 0\n\tbuffer_load_dwordx4 %0, %1, %2 offen offset:2048 lds" :  : "v"(voS), "s"(rA), "s"(ao[2]) : "memory"); SB();
  asm volatile("buffer_load_dwordx4 %0, %1, %2 offen offset:3072 lds" :  : "v"(voS), "s"(rA), "s"(ao[3]) : "memory"); SB();
  asm volatile("s_waitcnt vmcnt(14)" :  :  : "memory"); SB();
  asm volatile("ds_read_b128 a[12:15], %0 offset:6144" :  : "v"(aA[0]) : "memory"); SB();
  asm volatile("ds_read_b128 a[28:31], %0 offset:6144" :  : "v"(aA[1]) : "memory"); SB();
  asm volatile("s_waitcnt lgkmcnt(2)" :  :  : "memory"); SB();
  asm volatile("s_add_u32 m0, %0, 102400" :  : "s"(mA) : "memory", "scc"); SB();
  asm volatile("s_nop 0\n\tbuffer_load_dwordx4 %0, %1, %2 offen lds" :  : "v"(voS), "s"(rA), "s"(ao[4]) : "memory"); SB();
  asm volatile("buffer_load_dwordx4 %0, %1, %2 offen offset:1024 lds" :  : "v"(voS), "s"(rA), "s"(ao[5]) : "memory"); SB();
  asm volatile("s_waitcnt lgkmcnt(0)" :  :  : "memory"); SB();
  asm volatile("s_waitcnt vmcnt(14)" :  :  : "memory"); SB();
  asm volatile("ds_read_b128 a[32:35], %0 offset:32768" :  : "v"(aA[0]) : "memory"); SB();
  asm volatile("ds_read_b128 a[48:51], %0 offset:32768" :  : "v"(aA[1]) : "memory"); SB();
  asm volatile("buffer_load_dwordx4 %0, %1, %2 offen offset:2048 lds" :  : "v"(voS), "s"(rA), "s"(ao[6]) : "memory"); SB();
  asm volatile("buffer_load_dwordx4 %0, %1, %2 offen offset:3072 lds" :  : "v"(voS), "s"(rA), "s"(ao[7]) : "memory"); SB();
  asm volatile("s_waitcnt vmcnt(14)" :  :  : "memory"); SB();
  asm volatile("ds_read_b128 a[36:39], %0 offset:34816" :  : "v"(aA[0]) : "memory"); SB();
  asm volatile("ds_read_b128 a[52:55], %0 offset:34816" :  : "v"(aA[1]) : "memory"); SB();
  asm volatile("s_waitcnt lgkmcnt(2)" :  :  : "memory"); SB();
  asm volatile("v_xor_b32 %0, 384, %0" : "+v"(voS) :  : "memory"); SB();
  asm volatile("s_add_u32 m0, %0, 131072" :  : "s"(mA) : "memory", "scc"); SB();
  asm volatile("s_nop 0\n\tbuffer_load_dwordx4 %0, %1, %2 offen lds" :  : "v"(voS), "s"(rA), "s"(ao[0]) : "memory"); SB();
  asm volatile("buffer_load_dwordx4 %0, %1, %2 offen offset:1024 lds" :  : "v"(voS), "s"(rA), "s"(ao[1]) : "memory"); SB();
  asm volatile("s_waitcnt vmcnt(14)" :  :  : "memory"); SB();
  asm volatile("ds_read_b128 a[40:43], %0 offset:36864" :  : "v"(aA[0]) : "memory"); SB();
  asm volatile("ds_read_b128 a[56:59], %0 offset:36864" :  : "v"(aA[1]) : "memory"); SB();
  asm volatile("s_waitcnt lgkmcnt(2)" :  :  : "memory"); SB();
  asm volatile("buffer_load_dwordx4 %0, %1, %2 offen offset:2048 lds" :  : "v"(voS), "s"(rA), "s"(ao[2]) : "memory"); SB();
  asm volatile("buffer_load_dwordx4 %0, %1, %2 offen offset:3072 lds" :  : "v"(voS), "s"(rA), "s"(ao[3]) : "memory"); SB();
  asm volatile("s_waitcnt vmcnt(14)" :  :  : "memory"); SB();
  asm volatile("ds_read_b128 a[44:47], %0 offset:38912" :  : "v"(aA[0]) : "memory"); SB();
  asm volatile("ds_read_b128 a[60:63], %0 offset:38912" :  : "v"(aA[1]) : "memory"); SB();
  asm volatile("s_waitcnt vmcnt(13)\n\t.Lc_%=:\n\tv_cmp_ne_u64 vcc, %2, %1\n\ts_cbranch_vccnz .Lr_%=\n\t.Ld_%=:\n\t.subsection 1\n\t.Lr_%=:\n\tv_add_u32 %0, -1, %0\n\ts_nop 1\n\tv_readfirstlane_b32 vcc_hi, %0\n\ts_nop 3\n\ts_cmp_lt_i32 vcc_hi, 1\n\ts_cbranch_scc1 .Ld_%=\n\ts_sleep 2\n\ts_mov_b32 vcc_lo, 0\n\tbuffer_load_dwordx2 %1, %3, %4, vcc_lo offen sc1\n\ts_waitcnt vmcnt(0)\n\ts_branch .Lc_%=\n\t.subsection 0" : "+v"(tmo), "+v"(fz) : "s"(tg), "v"(vFZ), "s"(rWS) : "memory", "vcc", "scc"); SB();
  asm volatile("buffer_load_dwordx4 v[160:163], %0, %1, %2 offen offset:0 sc1" :  : "v"(vB), "s"(rWS), "s"(0u) : "memory"); SB();
  asm volatile("s_waitcnt lgkmcnt(2)" :  :  : "memory"); SB();
  asm volatile("buffer_load_dwordx4 v[164:167], %0, %1, %2 offen offset:1024 sc1" :  : "v"(vB), "s"(rWS), "s"(0u) : "memory"); SB();
  asm volatile("s_waitcnt vmcnt(12) lgkmcnt(0)\n\ts_barrier" :  :  : "memory"); SB();
  ST(14);
  asm volatile("ds_read2_b32 %0, %1 offset0:176 offset1:177" : "=&v"(cA[0][0][0]) : "v"(aa[0]) : "memory"); SB();
  asm volatile("ds_read2_b32 %0, %1 offset0:178 offset1:179" : "=&v"(cA[0][0][1]) : "v"(aa[0]) : "memory"); SB();
  asm volatile("ds_read_b128 a[124:127], %0 offset:3776" :  : "v"(bb) : "memory"); SB();
  asm volatile("ds_read_b128 a[120:123], %0 offset:3264" :  : "v"(bb) : "memory"); SB();
  asm volatile("ds_read_b128 a[116:119], %0 offset:2752" :  : "v"(bb) : "memory"); SB();
  asm volatile("ds_read_b128 a[112:115], %0 offset:2240" :  : "v"(bb) : "memory"); SB();
  asm volatile("ds_read2_b32 %0, %1 offset0:48 offset1:49" : "=&v"(cA[0][1][0]) : "v"(aa[0]) : "memory"); SB();
  asm volatile("ds_read2_b32 %0, %1 offset0:50 offset1:51" : "=&v"(cA[0][1][1]) : "v"(aa[0]) : "memory"); SB();
  asm volatile("ds_read_b128 a[108:111], %0 offset:1728" :  : "v"(bb) : "memory"); SB();
  asm volatile("ds_read2_b32 %0, %1 offset0:176 offset1:177" : "=&v"(cA[0][2][0]) : "v"(aa[1]) : "memory"); SB();
  asm volatile("" : "+v"(a1[0][0]), "+v"(a1[0][1]), "+v"(a1[0][2]), "+v"(a1[1][0]), "+v"(a1[1][1]), "+v"(a1[1][2]), "+v"(c1[0]), "+v"(c1[1]), "+v"(c1[2]), "+v"(co[0]), "+v"(co[1]), "+v"(co[2]) :  : ); SB();
  c1[0] += co[0]; c1[1] += co[1]; c1[2] += co[2];
#pragma unroll
  for (int n = 0; n < 6; ++n) { a1[n / 3][n % 3] = __builtin_elementwise_fma((f32x4){-xm[n / 3], -xm[n / 3], -xm[n / 3], -xm[n / 3]}, c1[n % 3], a1[n / 3][n % 3]); asm volatile("" : "+v"(a1[n / 3][n % 3])); }
  SB();
  asm volatile("ds_read_b128 a[64:67], %0 offset:0" :  : "v"(aA[0]) : "memory"); SB();
  asm volatile("ds_read_b128 a[80:83], %0 offset:0" :  : "v"(aA[1]) : "memory"); SB();
  asm volatile("s_waitcnt vmcnt(10)" :  :  : "memory"); SB();
  asm volatile("ds_read_b128 a[68:71], %0 offset:2048" :  : "v"(aA[0]) : "memory"); SB();
  asm volatile("ds_read_b128 a[84:87], %0 offset:2048" :  : "v"(aA[1]) : "memory"); SB();
  asm volatile("buffer_load_dwordx4 v[168:171], %0, %1, %2 offen offset:0 sc1" :  : "v"(vB), "s"(rWS), "s"(65536u) : "memory"); SB();
  asm volatile("buffer_load_dwordx4 v[172:175], %0, %1, %2 offen offset:1024 sc1" :  : "v"(vB), "s"(rWS), "s"(65536u) : "memory"); SB();
  asm volatile("v_xor_b32 %0, 131072, %0" : "+v"(vB) :  : "memory"); SB();
  if (nch == 3) {
  asm volatile("s_waitcnt lgkmcnt(5)\n\tv_mfma_f32_16x16x32_bf16 %0, %1, a[124:127], 0" : "=&v"(ac[7]) : "v"(((u32x4){cA[0][0][0][0], cA[0][0][0][1], cA[0][0][1][0], cA[0][0][1][1]})) : ); SB();
  asm volatile("ds_read2_b32 %0, %1 offset0:178 offset1:179" : "=&v"(cA[0][2][1]) : "v"(aa[1]) : "memory"); SB();
  asm volatile("v_mfma_f32_16x16x32_bf16 %0, %1, a[120:123], 0" : "=&v"(ac[6]) : "v"(((u32x4){cA[0][0][0][0], cA[0][0][0][1], cA[0][0][1][0], cA[0][0][1][1]})) : ); SB();
  asm volatile("ds_read2_b32 %0, %1 offset0:48 offset1:49" : "=&v"(cA[0][3][0]) : "v"(aa[1]) : "memory"); SB();
  asm volatile("s_waitcnt vmcnt(10)" :  :  : "memory"); SB();
  asm volatile("v_mfma_f32_16x16x32_bf16 %0, %1, a[116:119], 0" : "=&v"(ac[5]) : "v"(((u32x4){cA[0][0][0][0], cA[0][0][0][1], cA[0][0][1][0], cA[0][0][1][1]})) : ); SB();
  asm volatile("ds_read2_b32 %0, %1 offset0:50 offset1:51" : "=&v"(cA[0][3][1]) : "v"(aa[1]) : "memory"); SB();
  asm volatile("ds_read_b128 a[72:75], %0 offset:4096" :  : "v"(aA[0]) : "memory"); SB();
  asm volatile("v_mfma_f32_16x16x32_bf16 %0, %1, a[112:115], 0" : "=&v"(ac[4]) : "v"(((u32x4){cA[0][0][0][0], cA[0][0][0][1], cA[0][0][1][0], cA[0][0][1][1]})) : ); SB();
  asm volatile("ds_read_b128 a[104:107], %0 offset:1216" :  : "v"(bb) : "memory"); SB();
  asm volatile("ds_read_b128 a[88:91], %0 offset:4096" :  : "v"(aA[1]) : "memory"); SB();
  asm volatile("v_mfma_f32_16x16x32_bf16 %0, %1, a[120:123], %0" : "+v"(ac[7]) : "v"(((u32x4){cA[0][1][0][0], cA[0][1][0][1], cA[0][1][1][0], cA[0][1][1][1]})) : ); SB();
  asm volatile("ds_read2_b32 %0, %1 offset0:176 offset1:177" : "=&v"(cA[0][4][0]) : "v"(aa[2]) : "memory"); SB();
  asm volatile("buffer_load_dwordx4 v[176:179], %0, %1, %2 offen offset:0 sc1" :  : "v"(vB), "s"(rWS), "s"(0u) : "memory"); SB();
  asm volatile("v_mfma_f32_16x16x32_bf16 %0, %1, a[116:119], %0" : "+v"(ac[6]) : "v"(((u32x4){cA[0][1][0][0], cA[0][1][0][1], cA[0][1][1][0], cA[0][1][1][1]})) : ); SB();
  asm volatile("ds_read2_b32 %0, %1 offset0:178 offset1:179" : "=&v"(cA[0][4][1]) : "v"(aa[2]) : "memory"); SB();
  asm volatile("v_mfma_f32_16x16x32_bf16 %0, %1, a[112:115], %0" : "+v"(ac[5]) : "v"(((u32x4){cA[0][1][0][0], cA[0][1][0][1], cA[0][1][1][0], cA[0][1][1][1]})) : ); SB();
  asm volatile("ds_read2_b32 %0, %1 offset0:48 offset1:49" : "=&v"(cA[0][5][0]) : "v"(aa[2]) : "memory"); SB();
  asm volatile("buffer_load_dwordx4 v[180:183], %0, %1, %2 offen offset:1024 sc1" :  : "v"(vB), "s"(rWS), "s"(0u) : "memory"); SB();
  asm volatile("v_mfma_f32_16x16x32_bf16 %0, %1, a[108:111], %0" : "+v"(ac[4]) : "v"(((u32x4){cA[0][1][0][0], cA[0][1][0][1], cA[0][1][1][0], cA[0][1][1][1]})) : ); SB();
  asm volatile("ds_read2_b32 %0, %1 offset0:50 offset1:51" : "=&v"(cA[0][5][1]) : "v"(aa[2]) : "memory"); SB();
  asm volatile("s_waitcnt lgkmcnt(9)\n\tv_mfma_f32_16x16x32_bf16 %0, %1, a[116:119], %0" : "+v"(ac[7]) : "v"(((u32x4){cA[0][2][0][0], cA[0][2][0][1], cA[0][2][1][0], cA[0][2][1][1]})) : ); SB();
  asm volatile("ds_read_b128 a[100:103], %0 offset:704" :  : "v"(bb) : "memory"); SB();
  asm volatile("v_mfma_f32_16x16x32_bf16 %0, %1, a[112:115], %0" : "+v"(ac[6]) : "v"(((u32x4){cA[0][2][0][0], cA[0][2][0][1], cA[0][2][1][0], cA[0][2][1][1]})) : ); SB();
  asm volatile("ds_read2_b32 %0, %1 offset0:176 offset1:177" : "=&v"(cA[0][6][0]) : "v"(aa[3]) : "memory"); SB();
  asm volatile("v_mfma_f32_16x16x32_bf16 %0, %1, a[108:111], %0" : "+v"(ac[5]) : "v"(((u32x4){cA[0][2][0][0], cA[0][2][0][1], cA[0][2][1][0], cA[0][2][1][1]})) : ); SB();
  asm volatile("ds_read2_b32 %0, %1 offset0:178 offset1:179" : "=&v"(cA[0][6][1]) : "v"(aa[3]) : "memory"); SB();
  asm volatile("s_waitcnt vmcnt(10)" :  :  : "memory"); SB();
  asm volatile("v_mfma_f32_16x16x32_bf16 %0, %1, a[108:111], 0" : "=&v"(ac[3]) : "v"(((u32x4){cA[0][0][0][0], cA[0][0][0][1], cA[0][0][1][0], cA[0][0][1][1]})) : ); SB();
  asm volatile("ds_read2_b32 %0, %1 offset0:48 offset1:49" : "=&v"(cA[0][7][0]) : "v"(aa[3]) : "memory"); SB();
  asm volatile("ds_read_b128 a[76:79], %0 offset:6144" :  : "v"(aA[0]) : "memory"); SB();
  asm volatile("s_waitcnt lgkmcnt(10)\n\tv_mfma_f32_16x16x32_bf16 %0, %1, a[112:115], %0" : "+v"(ac[7]) : "v"(((u32x4){cA[0][3][0][0], cA[0][3][0][1], cA[0][3][1][0], cA[0][3][1][1]})) : ); SB();
  asm volatile("ds_read2_b32 %0, %1 offset0:50 offset1:51" : "=&v"(cA[0][7][1]) : "v"(aa[3]) : "memory"); SB();
  asm volatile("ds_read_b128 a[92:95], %0 offset:6144" :  : "v"(aA[1]) : "memory"); SB();
  asm volatile("v_mfma_f32_16x16x32_bf16 %0, %1, a[108:111], %0" : "+v"(ac[6]) : "v"(((u32x4){cA[0][3][0][0], cA[0][3][0][1], cA[0][3][1][0], cA[0][3][1][1]})) : ); SB();
  asm volatile("ds_read_b128 a[96:99], %0 offset:192" :  : "v"(bb) : "memory"); SB();
  asm volatile("buffer_load_dwordx4 v[184:187], %0, %1, %2 offen offset:0 sc1" :  : "v"(vB), "s"(rWS), "s"(65536u) : "memory"); SB();
  asm volatile("v_mfma_f32_16x16x32_bf16 %0, %1, a[104:107], %0" : "+v"(ac[5]) : "v"(((u32x4){cA[0][3][0][0], cA[0][3][0][1], cA[0][3][1][0], cA[0][3][1][1]})) : ); SB();
  asm volatile("ds_read2_b32 %0, %1 offset0:160 offset1:161" : "=&v"(cA[1][0][0]) : "v"(aa[0]) : "memory"); SB();
  asm volatile("buffer_load_dwordx4 v[188:191], %0, %1, %2 offen offset:1024 sc1" :  : "v"(vB), "s"(rWS), "s"(65536u) : "memory"); SB();
  asm volatile("v_mfma_f32_16x16x32_bf16 %0, %1, a[104:107], %0" : "+v"(ac[4]) : "v"(((u32x4){cA[0][2][0][0], cA[0][2][0][1], cA[0][2][1][0], cA[0][2][1][1]})) : ); SB();
  asm volatile("v_xor_b32 %0, 393216, %0" : "+v"(vB) :  : "memory"); SB();
  asm volatile("s_waitcnt lgkmcnt(9)\n\tv_mfma_f32_16x16x32_bf16 %0, %1, a[108:111], %0" : "+v"(ac[7]) : "v"(((u32x4){cA[0][4][0][0], cA[0][4][0][1], cA[0][4][1][0], cA[0][4][1][1]})) : ); SB();
  asm volatile("ds_read2_b32 %0, %1 offset0:162 offset1:163" : "=&v"(cA[1][0][1]) : "v"(aa[0]) : "memory"); SB();
  asm volatile("v_mfma_f32_16x16x32_bf16 %0, %1, a[104:107], %0" : "+v"(ac[6]) : "v"(((u32x4){cA[0][4][0][0], cA[0][4][0][1], cA[0][4][1][0], cA[0][4][1][1]})) : ); SB();
  asm volatile("ds_read_b128 a[156:159], %0 offset:3712" :  : "v"(bb) : "memory"); SB();
  asm volatile("buffer_load_dwordx4 v[192:195], %0, %1, %2 offen offset:0 sc1" :  : "v"(vB), "s"(rWS), "s"(0u) : "memory"); SB();
  asm volatile("v_mfma_f32_16x16x32_bf16 %0, %1, a[104:107], %0" : "+v"(ac[3]) : "v"(((u32x4){cA[0][1][0][0], cA[0][1][0][1], cA[0][1][1][0], cA[0][1][1][1]})) : ); SB();
  asm volatile("ds_read_b128 a[152:155], %0 offset:3200" :  : "v"(bb) : "memory"); SB();
  asm volatile("v_mfma_f32_16x16x32_bf16 %0, %1, a[104:107], 0" : "=&v"(ac[2]) : "v"(((u32x4){cA[0][0][0][0], cA[0][0][0][1], cA[0][0][1][0], cA[0][0][1][1]})) : ); SB();
  asm volatile("ds_write_b128 %0, %1 offset:0" :  : "v"(ew[0]), "v"(a1[0][0]) : "memory"); SB();
  asm volatile("v_mfma_f32_16x16x32_bf16 %0, %1, a[104:107], %0" : "+v"(ac[7]) : "v"(((u32x4){cA[0][5][0][0], cA[0][5][0][1], cA[0][5][1][0], cA[0][5][1][1]})) : ); SB();
  asm volatile("ds_read_b128 a[128:131], %0 offset:128" :  : "v"(bb) : "memory"); SB();
  asm volatile("buffer_load_dwordx4 v[196:199], %0, %1, %2 offen offset:1024 sc1" :  : "v"(vB), "s"(rWS), "s"(0u) : "memory"); SB();
  asm volatile("s_waitcnt lgkmcnt(6)\n\tv_mfma_f32_16x16x32_bf16 %0, %1, a[100:103], %0" : "+v"(ac[6]) : "v"(((u32x4){cA[0][5][0][0], cA[0][5][0][1], cA[0][5][1][0], cA[0][5][1][1]})) : ); SB();
  asm volatile("ds_read_b128 a[132:135], %0 offset:640" :  : "v"(bb) : "memory"); SB();
  asm volatile("v_mfma_f32_16x16x32_bf16 %0, %1, a[100:103], %0" : "+v"(ac[5]) : "v"(((u32x4){cA[0][4][0][0], cA[0][4][0][1], cA[0][4][1][0], cA[0][4][1][1]})) : ); SB();
  asm volatile("ds_read2_b32 %0, %1 offset0:32 offset1:33" : "=&v"(cA[1][1][0]) : "v"(aa[0]) : "memory"); SB();
  asm volatile("buffer_load_dwordx4 v[200:203], %0, %1, %2 offen offset:0 sc1" :  : "v"(vB), "s"(rWS), "s"(65536u) : "memory"); SB();
  asm volatile("v_mfma_f32_16x16x32_bf16 %0, %1, a[100:103], %0" : "+v"(ac[4]) : "v"(((u32x4){cA[0][3][0][0], cA[0][3][0][1], cA[0][3][1][0], cA[0][3][1][1]})) : ); SB();
  asm volatile("ds_write_b128 %0, %1 offset:1024" :  : "v"(ew[0]), "v"(a1[0][1]) : "memory"); SB();
  asm volatile("v_mfma_f32_16x16x32_bf16 %0, %1, a[100:103], %0" : "+v"(ac[7]) : "v"(((u32x4){cA[0][6][0][0], cA[0][6][0][1], cA[0][6][1][0], cA[0][6][1][1]})) : ); SB();
  asm volatile("ds_read2_b32 %0, %1 offset0:34 offset1:35" : "=&v"(cA[1][1][1]) : "v"(aa[0]) : "memory"); SB();
  asm volatile("buffer_load_dwordx4 v[204:207], %0, %1, %2 offen offset:1024 sc1" :  : "v"(vB), "s"(rWS), "s"(65536u) : "memory"); SB();
  asm volatile("v_mfma_f32_16x16x32_bf16 %0, %1, a[100:103], %0" : "+v"(ac[3]) : "v"(((u32x4){cA[0][2][0][0], cA[0][2][0][1], cA[0][2][1][0], cA[0][2][1][1]})) : ); SB();
  asm volatile("ds_read_b128 a[148:151], %0 offset:2688" :  : "v"(bb) : "memory"); SB();
  asm volatile("v_mfma_f32_16x16x32_bf16 %0, %1, a[100:103], %0" : "+v"(ac[2]) : "v"(((u32x4){cA[0][1][0][0], cA[0][1][0][1], cA[0][1][1][0], cA[0][1][1][1]})) : ); SB();
  asm volatile("ds_read_b128 a[136:139], %0 offset:1152" :  : "v"(bb) : "memory"); SB();
  asm volatile("v_mfma_f32_16x16x32_bf16 %0, %1, a[100:103], 0" : "=&v"(ac[1]) : "v"(((u32x4){cA[0][0][0][0], cA[0][0][0][1], cA[0][0][1][0], cA[0][0][1][1]})) : ); SB();
  asm volatile("v_xor_b32 %0, 262144, %0" : "+v"(vB) :  : "memory"); SB();
  asm volatile("v_mfma_f32_16x16x32_bf16 %0, %1, a[96:99], %0" : "+v"(ac[7]) : "v"(((u32x4){cA[0][7][0][0], cA[0][7][0][1], cA[0][7][1][0], cA[0][7][1][1]})) : ); SB();
  asm volatile("ds_read2_b32 %0, %1 offset0:160 offset1:161" : "=&v"(cA[1][2][0]) : "v"(aa[1]) : "memory"); SB();
  asm volatile("buffer_load_dwordx4 v[208:211], %0, %1, %2 offen offset:0 sc1" :  : "v"(vB), "s"(rWS), "s"(2048u) : "memory"); SB();
  asm volatile("v_mfma_f32_16x16x32_bf16 %0, %1, a[96:99], %0" : "+v"(ac[6]) : "v"(((u32x4){cA[0][6][0][0], cA[0][6][0][1], cA[0][6][1][0], cA[0][6][1][1]})) : ); SB();
  asm volatile("ds_read2_b32 %0, %1 offset0:162 offset1:163" : "=&v"(cA[1][2][1]) : "v"(aa[1]) : "memory"); SB();
  asm volatile("v_mfma_f32_16x16x32_bf16 %0, %1, a[96:99], %0" : "+v"(ac[5]) : "v"(((u32x4){cA[0][5][0][0], cA[0][5][0][1], cA[0][5][1][0], cA[0][5][1][1]})) : ); SB();
  asm volatile("ds_read_b128 a[140:143], %0 offset:1664" :  : "v"(bb) : "memory"); SB();
  asm volatile("v_mfma_f32_16x16x32_bf16 %0, %1, a[96:99], %0" : "+v"(ac[4]) : "v"(((u32x4){cA[0][4][0][0], cA[0][4][0][1], cA[0][4][1][0], cA[0][4][1][1]})) : ); SB();
  asm volatile("buffer_load_dwordx4 v[212:215], %0, %1, %2 offen offset:1024 sc1" :  : "v"(vB), "s"(rWS), "s"(2048u) : "memory"); SB();
  asm volatile("v_mfma_f32_16x16x32_bf16 %0, %1, a[96:99], %0" : "+v"(ac[3]) : "v"(((u32x4){cA[0][3][0][0], cA[0][3][0][1], cA[0][3][1][0], cA[0][3][1][1]})) : ); SB();
  asm volatile("ds_read_b128 a[144:147], %0 offset:2176" :  : "v"(bb) : "memory"); SB();
  asm volatile("v_mfma_f32_16x16x32_bf16 %0, %1, a[96:99], %0" : "+v"(ac[2]) : "v"(((u32x4){cA[0][2][0][0], cA[0][2][0][1], cA[0][2][1][0], cA[0][2][1][1]})) : ); SB();
  asm volatile("ds_read2_b32 %0, %1 offset0:32 offset1:33" : "=&v"(cA[1][3][0]) : "v"(aa[1]) : "memory"); SB();
  asm volatile("v_mfma_f32_16x16x32_bf16 %0, %1, a[96:99], %0" : "+v"(ac[1]) : "v"(((u32x4){cA[0][1][0][0], cA[0][1][0][1], cA[0][1][1][0], cA[0][1][1][1]})) : ); SB();
  asm volatile("ds_read2_b32 %0, %1 offset0:34 offset1:35" : "=&v"(cA[1][3][1]) : "v"(aa[1]) : "memory"); SB();
  asm volatile("v_mfma_f32_16x16x32_bf16 %0, %1, a[96:99], 0" : "=&v"(ac[0]) : "v"(((u32x4){cA[0][0][0][0], cA[0][0][0][1], cA[0][0][1][0], cA[0][0][1][1]})) : ); SB();
  asm volatile("buffer_load_dwordx4 v[216:219], %0, %1, %2 offen offset:0 sc1" :  : "v"(vB), "s"(rWS), "s"(67584u) : "memory"); SB();
  asm volatile("s_waitcnt lgkmcnt(6)\n\tv_mfma_f32_16x16x32_bf16 %0, %1, a[156:159], %0" : "+v"(ac[7]) : "v"(((u32x4){cA[1][0][0][0], cA[1][0][0][1], cA[1][0][1][0], cA[1][0][1][1]})) : ); SB();
  asm volatile("ds_read2_b32 %0, %1 offset0:160 offset1:161" : "=&v"(cA[1][4][0]) : "v"(aa[2]) : "memory"); SB();
  asm volatile("v_mfma_f32_16x16x32_bf16 %0, %1, a[152:155], %0" : "+v"(ac[6]) : "v"(((u32x4){cA[1][0][0][0], cA[1][0][0][1], cA[1][0][1][0], cA[1][0][1][1]})) : ); SB();
  asm volatile("ds_read2_b32 %0, %1 offset0:162 offset1:163" : "=&v"(cA[1][4][1]) : "v"(aa[2]) : "memory"); SB();
  asm volatile("v_mfma_f32_16x16x32_bf16 %0, %1, a[128:131], %0" : "+v"(ac[0]) : "v"(((u32x4){cA[1][0][0][0], cA[1][0][0][1], cA[1][0][1][0], cA[1][0][1][1]})) : ); SB();
  asm volatile("ds_read2_b32 %0, %1 offset0:32 offset1:33" : "=&v"(cA[1][5][0]) : "v"(aa[2]) : "memory"); SB();
  asm volatile("v_mfma_f32_16x16x32_bf16 %0, %1, a[132:135], %0" : "+v"(ac[1]) : "v"(((u32x4){cA[1][0][0][0], cA[1][0][0][1], cA[1][0][1][0], cA[1][0][1][1]})) : ); SB();
  asm volatile("buffer_load_dwordx4 v[220:223], %0, %1, %2 offen offset:1024 sc1" :  : "v"(vB), "s"(rWS), "s"(67584u) : "memory"); SB();
  asm volatile("v_mfma_f32_16x16x32_bf16 %0, %1, a[152:155], %0" : "+v"(ac[7]) : "v"(((u32x4){cA[1][1][0][0], cA[1][1][0][1], cA[1][1][1][0], cA[1][1][1][1]})) : ); SB();
  asm volatile("ds_read2_b32 %0, %1 offset0:34 offset1:35" : "=&v"(cA[1][5][1]) : "v"(aa[2]) : "memory"); SB();
  asm volatile("v_mfma_f32_16x16x32_bf16 %0, %1, a[148:151], %0" : "+v"(ac[6]) : "v"(((u32x4){cA[1][1][0][0], cA[1][1][0][1], cA[1][1][1][0], cA[1][1][1][1]})) : ); SB();
  asm volatile("ds_read2_b32 %0, %1 offset0:160 offset1:161" : "=&v"(cA[1][6][0]) : "v"(aa[3]) : "memory"); SB();
  asm volatile("v_mfma_f32_16x16x32_bf16 %0, %1, a[136:139], %0" : "+v"(ac[2]) : "v"(((u32x4){cA[1][0][0][0], cA[1][0][0][1], cA[1][0][1][0], cA[1][0][1][1]})) : ); SB();
  asm volatile("ds_read2_b32 %0, %1 offset0:162 offset1:163" : "=&v"(cA[1][6][1]) : "v"(aa[3]) : "memory"); SB();
  asm volatile("v_mfma_f32_16x16x32_bf16 %0, %1, a[128:131], %0" : "+v"(ac[1]) : "v"(((u32x4){cA[1][1][0][0], cA[1][1][0][1], cA[1][1][1][0], cA[1][1][1][1]})) : ); SB();
  asm volatile("ds_write_b128 %0, %1 offset:0" :  : "v"(dd), "v"(ac[0]) : "memory"); SB();
  asm volatile("s_waitcnt lgkmcnt(7)\n\tv_mfma_f32_16x16x32_bf16 %0, %1, a[148:151], %0" : "+v"(ac[7]) : "v"(((u32x4){cA[1][2][0][0], cA[1][2][0][1], cA[1][2][1][0], cA[1][2][1][1]})) : ); SB();
  asm volatile("ds_read2_b32 %0, %1 offset0:32 offset1:33" : "=&v"(cA[1][7][0]) : "v"(aa[3]) : "memory"); SB();
  asm volatile("v_mfma_f32_16x16x32_bf16 %0, %1, a[140:143], %0" : "+v"(ac[3]) : "v"(((u32x4){cA[1][0][0][0], cA[1][0][0][1], cA[1][0][1][0], cA[1][0][1][1]})) : ); SB();
  asm volatile("ds_read2_b32 %0, %1 offset0:34 offset1:35" : "=&v"(cA[1][7][1]) : "v"(aa[3]) : "memory"); SB();
  asm volatile("v_mfma_f32_16x16x32_bf16 %0, %1, a[132:135], %0" : "+v"(ac[2]) : "v"(((u32x4){cA[1][1][0][0], cA[1][1][0][1], cA[1][1][1][0], cA[1][1][1][1]})) : ); SB();
  asm volatile("v_xor_b32 %0, 131072, %0" : "+v"(vB) :  : "memory"); SB();
  asm volatile("v_mfma_f32_16x16x32_bf16 %0, %1, a[144:147], %0" : "+v"(ac[4]) : "v"(((u32x4){cA[1][0][0][0], cA[1][0][0][1], cA[1][0][1][0], cA[1][0][1][1]})) : ); SB();
  asm volatile("ds_write_b128 %0, %1 offset:1024" :  : "v"(dd), "v"(ac[1]) : "memory"); SB();
  asm volatile("v_mfma_f32_16x16x32_bf16 %0, %1, a[144:147], %0" : "+v"(ac[7]) : "v"(((u32x4){cA[1][3][0][0], cA[1][3][0][1], cA[1][3][1][0], cA[1][3][1][1]})) : ); SB();
  asm volatile("buffer_load_dwordx4 v[224:227], %0, %1, %2 offen offset:0 sc1" :  : "v"(vB), "s"(rWS), "s"(2048u) : "memory"); SB();
  asm volatile("v_mfma_f32_16x16x32_bf16 %0, %1, a[136:139], %0" : "+v"(ac[3]) : "v"(((u32x4){cA[1][1][0][0], cA[1][1][0][1], cA[1][1][1][0], cA[1][1][1][1]})) : ); SB();
  asm volatile("buffer_load_dwordx4 v[228:231], %0, %1, %2 offen offset:1024 sc1" :  : "v"(vB), "s"(rWS), "s"(2048u) : "memory"); SB();
  asm volatile("v_mfma_f32_16x16x32_bf16 %0, %1, a[128:131], %0" : "+v"(ac[2]) : "v"(((u32x4){cA[1][2][0][0], cA[1][2][0][1], cA[1][2][1][0], cA[1][2][1][1]})) : ); SB();
  asm volatile("ds_write_b128 %0, %1 offset:2048" :  : "v"(ew[0]), "v"(a1[0][2]) : "memory"); SB();
  asm volatile("v_mfma_f32_16x16x32_bf16 %0, %1, a[140:143], %0" : "+v"(ac[4]) : "v"(((u32x4){cA[1][1][0][0], cA[1][1][0][1], cA[1][1][1][0], cA[1][1][1][1]})) : ); SB();
  asm volatile("buffer_load_dwordx4 v[232:235], %0, %1, %2 offen offset:0 sc1" :  : "v"(vB), "s"(rWS), "s"(67584u) : "memory"); SB();
  asm volatile("v_mfma_f32_16x16x32_bf16 %0, %1, a[148:151], %0" : "+v"(ac[5]) : "v"(((u32x4){cA[1][0][0][0], cA[1][0][0][1], cA[1][0][1][0], cA[1][0][1][1]})) : ); SB();
  asm volatile("ds_write_b128 %0, %1 offset:0" :  : "v"(ew[1]), "v"(a1[1][0]) : "memory"); SB();
  asm volatile("v_mfma_f32_16x16x32_bf16 %0, %1, a[132:135], %0" : "+v"(ac[3]) : "v"(((u32x4){cA[1][2][0][0], cA[1][2][0][1], cA[1][2][1][0], cA[1][2][1][1]})) : ); SB();
  asm volatile("buffer_load_dwordx4 v[236:239], %0, %1, %2 offen offset:1024 sc1" :  : "v"(vB), "s"(rWS), "s"(67584u) : "memory"); SB();
  asm volatile("v_mfma_f32_16x16x32_bf16 %0, %1, a[144:147], %0" : "+v"(ac[6]) : "v"(((u32x4){cA[1][2][0][0], cA[1][2][0][1], cA[1][2][1][0], cA[1][2][1][1]})) : ); SB();
  asm volatile("ds_write_b128 %0, %1 offset:2048" :  : "v"(dd), "v"(ac[2]) : "memory"); SB();
  asm volatile("v_mfma_f32_16x16x32_bf16 %0, %1, a[136:139], %0" : "+v"(ac[4]) : "v"(((u32x4){cA[1][2][0][0], cA[1][2][0][1], cA[1][2][1][0], cA[1][2][1][1]})) : ); SB();
  asm volatile("v_xor_b32 %0, 393216, %0" : "+v"(vB) :  : "memory"); SB();
  asm volatile("v_mfma_f32_16x16x32_bf16 %0, %1, a[144:147], %0" : "+v"(ac[5]) : "v"(((u32x4){cA[1][1][0][0], cA[1][1][0][1], cA[1][1][1][0], cA[1][1][1][1]})) : ); SB();
  asm volatile("ds_write_b128 %0, %1 offset:1024" :  : "v"(ew[1]), "v"(a1[1][1]) : "memory"); SB();
  asm volatile("v_mfma_f32_16x16x32_bf16 %0, %1, a[128:131], %0" : "+v"(ac[3]) : "v"(((u32x4){cA[1][3][0][0], cA[1][3][0][1], cA[1][3][1][0], cA[1][3][1][1]})) : ); SB();
  asm volatile("ds_write_b128 %0, %1 offset:2048" :  : "v"(ew[1]), "v"(a1[1][2]) : "memory"); SB();
  asm volatile("v_mfma_f32_16x16x32_bf16 %0, %1, a[140:143], %0" : "+v"(ac[6]) : "v"(((u32x4){cA[1][3][0][0], cA[1][3][0][1], cA[1][3][1][0], cA[1][3][1][1]})) : ); SB();
  asm volatile("buffer_load_dwordx4 v[240:243], %0, %1, %2 offen offset:0 sc1" :  : "v"(vB), "s"(rWS), "s"(2048u) : "memory"); SB();
  asm volatile("v_mfma_f32_16x16x32_bf16 %0, %1, a[132:135], %0" : "+v"(ac[4]) : "v"(((u32x4){cA[1][3][0][0], cA[1][3][0][1], cA[1][3][1][0], cA[1][3][1][1]})) : ); SB();
  asm volatile("v_mfma_f32_16x16x32_bf16 %0, %1, a[140:143], %0" : "+v"(ac[5]) : "v"(((u32x4){cA[1][2][0][0], cA[1][2][0][1], cA[1][2][1][0], cA[1][2][1][1]})) : ); SB();
  asm volatile("buffer_load_dwordx4 v[244:247], %0, %1, %2 offen offset:1024 sc1" :  : "v"(vB), "s"(rWS), "s"(2048u) : "memory"); SB();
  asm volatile("s_waitcnt lgkmcnt(3)\n\tv_mfma_f32_16x16x32_bf16 %0, %1, a[140:143], %0" : "+v"(ac[7]) : "v"(((u32x4){cA[1][4][0][0], cA[1][4][0][1], cA[1][4][1][0], cA[1][4][1][1]})) : ); SB();
  asm volatile("ds_write_b128 %0, %1 offset:3072" :  : "v"(dd), "v"(ac[3]) : "memory"); SB();
  asm volatile("v_mfma_f32_16x16x32_bf16 %0, %1, a[136:139], %0" : "+v"(ac[6]) : "v"(((u32x4){cA[1][4][0][0], cA[1][4][0][1], cA[1][4][1][0], cA[1][4][1][1]})) : ); SB();
  asm volatile("v_mfma_f32_16x16x32_bf16 %0, %1, a[128:131], %0" : "+v"(ac[4]) : "v"(((u32x4){cA[1][4][0][0], cA[1][4][0][1], cA[1][4][1][0], cA[1][4][1][1]})) : ); SB();
  asm volatile("buffer_load_dwordx4 v[248:251], %0, %1, %2 offen offset:0 sc1" :  : "v"(vB), "s"(rWS), "s"(67584u) : "memory"); SB();
  asm volatile("v_mfma_f32_16x16x32_bf16 %0, %1, a[136:139], %0" : "+v"(ac[5]) : "v"(((u32x4){cA[1][3][0][0], cA[1][3][0][1], cA[1][3][1][0], cA[1][3][1][1]})) : ); SB();
  asm volatile("v_mfma_f32_16x16x32_bf16 %0, %1, a[136:139], %0" : "+v"(ac[7]) : "v"(((u32x4){cA[1][5][0][0], cA[1][5][0][1], cA[1][5][1][0], cA[1][5][1][1]})) : ); SB();
  asm volatile("v_mfma_f32_16x16x32_bf16 %0, %1, a[132:135], %0" : "+v"(ac[6]) : "v"(((u32x4){cA[1][5][0][0], cA[1][5][0][1], cA[1][5][1][0], cA[1][5][1][1]})) : ); SB();
  asm volatile("buffer_load_dwordx4 v[252:255], %0, %1, %2 offen offset:1024 sc1" :  : "v"(vB), "s"(rWS), "s"(67584u) : "memory"); SB();
  asm volatile("v_mfma_f32_16x16x32_bf16 %0, %1, a[132:135], %0" : "+v"(ac[5]) : "v"(((u32x4){cA[1][4][0][0], cA[1][4][0][1], cA[1][4][1][0], cA[1][4][1][1]})) : ); SB();
  asm volatile("ds_write_b128 %0, %1 offset:4096" :  : "v"(dd), "v"(ac[4]) : "memory"); SB();
  asm volatile("v_mfma_f32_16x16x32_bf16 %0, %1, a[132:135], %0" : "+v"(ac[7]) : "v"(((u32x4){cA[1][6][0][0], cA[1][6][0][1], cA[1][6][1][0], cA[1][6][1][1]})) : ); SB();
  asm volatile("v_xor_b32 %0, 131072, %0" : "+v"(vB) :  : "memory"); SB();
  asm volatile("s_add_u32 m0, %0, 135168" :  : "s"(mA) : "memory", "scc"); SB();
  asm volatile("v_mfma_f32_16x16x32_bf16 %0, %1, a[128:131], %0" : "+v"(ac[6]) : "v"(((u32x4){cA[1][6][0][0], cA[1][6][0][1], cA[1][6][1][0], cA[1][6][1][1]})) : ); SB();
  asm volatile("buffer_load_dwordx4 %0, %1, %2 offen lds" :  : "v"(voS), "s"(rA), "s"(ao[4]) : "memory"); SB();
  asm volatile("v_mfma_f32_16x16x32_bf16 %0, %1, a[128:131], %0" : "+v"(ac[5]) : "v"(((u32x4){cA[1][5][0][0], cA[1][5][0][1], cA[1][5][1][0], cA[1][5][1][1]})) : ); SB();
  asm volatile("v_mfma_f32_16x16x32_bf16 %0, %1, a[128:131], %0" : "+v"(ac[7]) : "v"(((u32x4){cA[1][7][0][0], cA[1][7][0][1], cA[1][7][1][0], cA[1][7][1][1]})) : ); SB();
  asm volatile("s_nop 5" : "+v"(ac[5]), "+v"(ac[6]), "+v"(ac[7]) :  : ); SB();
  asm volatile("ds_write_b128 %0, %1 offset:6144" :  : "v"(dd), "v"(ac[6]) : "memory"); SB();
  asm volatile("ds_write_b128 %0, %1 offset:5120" :  : "v"(dd), "v"(ac[5]) : "memory"); SB();
  asm volatile("ds_write_b128 %0, %1 offset:7168" :  : "v"(dd), "v"(ac[7]) : "memory"); SB();
  } else {
  asm volatile("; ELSE_BEGIN"); SB();
  asm volatile("s_waitcnt lgkmcnt(0)" ::: "memory"); SB();
  asm volatile("s_waitcnt vmcnt(10)" :  :  : "memory"); SB();
  asm volatile("s_waitcnt lgkmcnt(0)" ::: "memory"); SB();
  asm volatile("ds_read_b128 a[72:75], %0 offset:4096" :  : "v"(aA[0]) : "memory"); SB();
  asm volatile("s_waitcnt lgkmcnt(0)" ::: "memory"); SB();
  asm volatile("ds_read_b128 a[88:91], %0 offset:4096" :  : "v"(aA[1]) : "memory"); SB();
  asm volatile("s_waitcnt lgkmcnt(0)" ::: "memory"); SB();
  asm volatile("buffer_load_dwordx4 v[176:179], %0, %1, %2 offen offset:0 sc1" :  : "v"(vB), "s"(rWS), "s"(0u) : "memory"); SB();
  asm volatile("s_waitcnt lgkmcnt(0)" ::: "memory"); SB();
  asm volatile("buffer_load_dwordx4 v[180:183], %0, %1, %2 offen offset:1024 sc1" :  : "v"(vB), "s"(rWS), "s"(0u) : "memory"); SB();
  asm volatile("s_waitcnt lgkmcnt(0)" ::: "memory"); SB();
  asm volatile("s_waitcnt vmcnt(10)" :  :  : "memory"); SB();
  asm volatile("s_waitcnt lgkmcnt(0)" ::: "memory"); SB();
  asm volatile("ds_read_b128 a[76:79], %0 offset:6144" :  : "v"(aA[0]) : "memory"); SB();
  asm volatile("s_waitcnt lgkmcnt(0)" ::: "memory"); SB();
  asm volatile("ds_read_b128 a[92:95], %0 offset:6144" :  : "v"(aA[1]) : "memory"); SB();
  asm volatile("s_waitcnt lgkmcnt(0)" ::: "memory"); SB();
  asm volatile("buffer_load_dwordx4 v[184:187], %0, %1, %2 offen offset:0 sc1" :  : "v"(vB), "s"(rWS), "s"(65536u) : "memory"); SB();
  asm volatile("s_waitcnt lgkmcnt(0)" ::: "memory"); SB();
  asm volatile("buffer_load_dwordx4 v[188:191], %0, %1, %2 offen offset:1024 sc1" :  : "v"(vB), "s"(rWS), "s"(65536u) : "memory"); SB();
  asm volatile("s_waitcnt lgkmcnt(0)" ::: "memory"); SB();
  asm volatile("v_xor_b32 %0, 393216, %0" : "+v"(vB) :  : "memory"); SB();
  asm volatile("s_waitcnt lgkmcnt(0)" ::: "memory"); SB();
  asm volatile("buffer_load_dwordx4 v[192:195], %0, %1, %2 offen offset:0 sc1" :  : "v"(vB), "s"(rWS), "s"(0u) : "memory"); SB();
  asm volatile("s_waitcnt lgkmcnt(0)" ::: "memory"); SB();
  asm volatile("ds_write_b128 %0, %1 offset:0" :  : "v"(ew[0]), "v"(a1[0][0]) : "memory"); SB();
  asm volatile("s_waitcnt lgkmcnt(0)" ::: "memory"); SB();
  asm volatile("buffer_load_dwordx4 v[196:199], %0, %1, %2 offen offset:1024 sc1" :  : "v"(vB), "s"(rWS), "s"(0u) : "memory"); SB();
  asm volatile("s_waitcnt lgkmcnt(0)" ::: "memory"); SB();
  asm volatile("buffer_load_dwordx4 v[200:203], %0, %1, %2 offen offset:0 sc1" :  : "v"(vB), "s"(rWS), "s"(65536u) : "memory"); SB();
  asm volatile("s_waitcnt lgkmcnt(0)" ::: "memory"); SB();
  asm volatile("ds_write_b128 %0, %1 offset:1024" :  : "v"(ew[0]), "v"(a1[0][1]) : "memory"); SB();
  asm volatile("s_waitcnt lgkmcnt(0)" ::: "memory"); SB();
  asm volatile("buffer_load_dwordx4 v[204:207], %0, %1, %2 offen offset:1024 sc1" :  : "v"(vB), "s"(rWS), "s"(65536u) : "memory"); SB();
  asm volatile("s_waitcnt lgkmcnt(0)" ::: "memory"); SB();
  asm volatile("v_xor_b32 %0, 262144, %0" : "+v"(vB) :  : "memory"); SB();
  asm volatile("s_waitcnt lgkmcnt(0)" ::: "memory"); SB();
  asm volatile("buffer_load_dwordx4 v[208:211], %0, %1, %2 offen offset:0 sc1" :  : "v"(vB), "s"(rWS), "s"(2048u) : "memory"); SB();
  asm volatile("s_waitcnt lgkmcnt(0)" ::: "memory"); SB();
  asm volatile("buffer_load_dwordx4 v[212:215], %0, %1, %2 offen offset:1024 sc1" :  : "v"(vB), "s"(rWS), "s"(2048u) : "memory"); SB();
  asm volatile("s_waitcnt lgkmcnt(0)" ::: "memory"); SB();
  asm volatile("buffer_load_dwordx4 v[216:219], %0, %1, %2 offen offset:0 sc1" :  : "v"(vB), "s"(rWS), "s"(67584u) : "memory"); SB();
  asm volatile("s_waitcnt lgkmcnt(0)" ::: "memory"); SB();
  asm volatile("buffer_load_dwordx4 v[220:223], %0, %1, %2 offen offset:1024 sc1" :  : "v"(vB), "s"(rWS), "s"(67584u) : "memory"); SB();
  asm volatile("s_waitcnt lgkmcnt(0)" ::: "memory"); SB();
  asm volatile("v_xor_b32 %0, 131072, %0" : "+v"(vB) :  : "memory"); SB();
  asm volatile("s_waitcnt lgkmcnt(0)" ::: "memory"); SB();
  asm volatile("buffer_load_dwordx4 v[224:227], %0, %1, %2 offen offset:0 sc1" :  : "v"(vB), "s"(rWS), "s"(2048u) : "memory"); SB();
  asm volatile("s_waitcnt lgkmcnt(0)" ::: "memory"); SB();
  asm volatile("buffer_load_dwordx4 v[228:231], %0, %1, %2 offen offset:1024 sc1" :  : "v"(vB), "s"(rWS), "s"(2048u) : "memory"); SB();
  asm volatile("s_waitcnt lgkmcnt(0)" ::: "memory"); SB();
  asm volatile("ds_write_b128 %0, %1 offset:2048" :  : "v"(ew[0]), "v"(a1[0][2]) : "memory"); SB();
  asm volatile("s_waitcnt lgkmcnt(0)" ::: "memory"); SB();
  asm volatile("buffer_load_dwordx4 v[232:235], %0, %1, %2 offen offset:0 sc1" :  : "v"(vB), "s"(rWS), "s"(67584u) : "memory"); SB();
  asm volatile("s_waitcnt lgkmcnt(0)" ::: "memory"); SB();
  asm volatile("ds_write_b128 %0, %1 offset:0" :  : "v"(ew[1]), "v"(a1[1][0]) : "memory"); SB();
  asm volatile("s_waitcnt lgkmcnt(0)" ::: "memory"); SB();
  asm volatile("buffer_load_dwordx4 v[236:239], %0, %1, %2 offen offset:1024 sc1" :  : "v"(vB), "s"(rWS), "s"(67584u) : "memory"); SB();
  asm volatile("s_waitcnt lgkmcnt(0)" ::: "memory"); SB();
  asm volatile("v_xor_b32 %0, 393216, %0" : "+v"(vB) :  : "memory"); SB();
  asm volatile("s_waitcnt lgkmcnt(0)" ::: "memory"); SB();
  asm volatile("ds_write_b128 %0, %1 offset:1024" :  : "v"(ew[1]), "v"(a1[1][1]) : "memory"); SB();
  asm volatile("s_waitcnt lgkmcnt(0)" ::: "memory"); SB();
  asm volatile("ds_write_b128 %0, %1 offset:2048" :  : "v"(ew[1]), "v"(a1[1][2]) : "memory"); SB();
  asm volatile("s_waitcnt lgkmcnt(0)" ::: "memory"); SB();
  asm volatile("buffer_load_dwordx4 v[240:243], %0, %1, %2 offen offset:0 sc1" :  : "v"(vB), "s"(rWS), "s"(2048u) : "memory"); SB();
  asm volatile("s_waitcnt lgkmcnt(0)" ::: "memory"); SB();
  asm volatile("buffer_load_dwordx4 v[244:247], %0, %1, %2 offen offset:1024 sc1" :  : "v"(vB), "s"(rWS), "s"(2048u) : "memory"); SB();
  asm volatile("s_waitcnt lgkmcnt(0)" ::: "memory"); SB();
  asm volatile("buffer_load_dwordx4 v[248:251], %0, %1, %2 offen offset:0 sc1" :  : "v"(vB), "s"(rWS), "s"(67584u) : "memory"); SB();
  asm volatile("s_waitcnt lgkmcnt(0)" ::: "memory"); SB();
  asm volatile("buffer_load_dwordx4 v[252:255], %0, %1, %2 offen offset:1024 sc1" :  : "v"(vB), "s"(rWS), "s"(67584u) : "memory"); SB();
  asm volatile("s_waitcnt lgkmcnt(0)" ::: "memory"); SB();
  asm volatile("v_xor_b32 %0, 131072, %0" : "+v"(vB) :  : "memory"); SB();
  asm volatile("s_waitcnt lgkmcnt(0)" ::: "memory"); SB();
  asm volatile("s_add_u32 m0, %0, 135168" :  : "s"(mA) : "memory", "scc"); SB();
  asm volatile("s_waitcnt lgkmcnt(0)" ::: "memory"); SB();
  asm volatile("buffer_load_dwordx4 %0, %1, %2 offen lds" :  : "v"(voS), "s"(rA), "s"(ao[4]) : "memory"); SB();
  asm volatile("s_waitcnt lgkmcnt(0) ; ELSE_END" ::: "memory"); SB();
  }
  ST(15);
  f32x4 g[2][4][2], pr[6], yq[8], bs4 = {bs, bs, bs, bs};
  u32x4 ho[3], ykv, y2;
  float gt[4], ge[4];
  unsigned vH_, pw2, pe[3], vX, vO, vO2, vP, vFH, vFY, vHr, hso[8], ys2, ydr, ykr, er, sFH, sFY;
  int dl;
  asm volatile("" : "+v"(bs4)); SB();
  asm volatile("s_waitcnt vmcnt(23)" :  :  : "memory"); SB();
  asm volatile("v_mfma_f32_16x16x32_bf16 %0, v[160:163], a[160:163], %1" : "=&v"(g[0][0][0]) : "v"(bs4) : ); SB();
  { unsigned lane = lane0, w = w0, rb = rb0, cb = cb0;
  asm volatile("" : "+v"(lane)); asm volatile("" : "+s"(w), "+s"(rb), "+s"(cb)); SB();

  asm volatile("v_mfma_f32_16x16x32_bf16 %0, v[160:163], a[164:167], 0" : "=&v"(g[0][1][0]) :  : ); SB();
  er = lb + 32768 + w * 6144 + l16; asm volatile("" : "+v"(er)); SB();
  asm volatile("v_mfma_f32_16x16x32_bf16 %0, v[160:163], a[168:171], 0" : "=&v"(g[0][2][0]) :  : ); SB();
  asm volatile("s_waitcnt lgkmcnt(0)\n\ts_barrier" :  :  : "memory"); SB();
  asm volatile("ds_read_b128 %0, %1 offset:0" : "=&v"(a1[0][0]) : "v"(er) : "memory"); SB();
  asm volatile("v_mfma_f32_16x16x32_bf16 %0, v[160:163], a[172:175], 0" : "=&v"(g[0][3][0]) :  : ); SB();
  asm volatile("ds_read_b128 a[96:99], %0 offset:32768" :  : "v"(aA[0]) : "memory"); SB();
  asm volatile("v_mfma_f32_16x16x32_bf16 %0, v[164:167], a[160:163], %1" : "=&v"(g[0][0][1]) : "v"(bs4) : ); SB();
  asm volatile("ds_read_b128 a[112:115], %0 offset:32768" :  : "v"(aA[1]) : "memory"); SB();
  asm volatile("v_mfma_f32_16x16x32_bf16 %0, v[164:167], a[164:167], 0" : "=&v"(g[0][1][1]) :  : ); SB();
  asm volatile("ds_read_b128 a[100:103], %0 offset:34816" :  : "v"(aA[0]) : "memory"); SB();
  asm volatile("v_mfma_f32_16x16x32_bf16 %0, v[164:167], a[168:171], 0" : "=&v"(g[0][2][1]) :  : ); SB();
  asm volatile("ds_read_b128 a[116:119], %0 offset:34816" :  : "v"(aA[1]) : "memory"); SB();
  asm volatile("v_mfma_f32_16x16x32_bf16 %0, v[164:167], a[172:175], 0" : "=&v"(g[0][3][1]) :  : ); SB();
  asm volatile("buffer_load_dwordx4 %0, %1, %2 offen offset:1024 lds" :  : "v"(voS), "s"(rA), "s"(ao[5]) : "memory"); SB();
  ST(25);
  asm volatile("s_waitcnt vmcnt(22)" :  :  : "memory"); SB();
  asm volatile("v_mfma_f32_16x16x32_bf16 %0, v[168:171], a[176:179], %0" : "+v"(g[0][0][0]) :  : ); SB();
  asm volatile("buffer_load_dwordx4 v[160:163], %0, %1, %2 offen offset:0 sc1" :  : "v"(vB), "s"(rWS), "s"(0u) : "memory"); SB();
  asm volatile("v_mfma_f32_16x16x32_bf16 %0, v[168:171], a[180:183], %0" : "+v"(g[0][1][0]) :  : ); SB();
  asm volatile("ds_read_b128 %0, %1 offset:1024" : "=&v"(a1[0][1]) : "v"(er) : "memory"); SB();
  asm volatile("v_mfma_f32_16x16x32_bf16 %0, v[168:171], a[184:187], %0" : "+v"(g[0][2][0]) :  : ); SB();
  asm volatile("ds_read_b128 %0, %1 offset:2048" : "=&v"(a1[0][2]) : "v"(er) : "memory"); SB();
  asm volatile("v_mfma_f32_16x16x32_bf16 %0, v[168:171], a[188:191], %0" : "+v"(g[0][3][0]) :  : ); SB();
  asm volatile("buffer_load_dwordx4 v[164:167], %0, %1, %2 offen offset:1024 sc1" :  : "v"(vB), "s"(rWS), "s"(0u) : "memory"); SB();
  asm volatile("v_mfma_f32_16x16x32_bf16 %0, v[172:175], a[176:179], %0" : "+v"(g[0][0][1]) :  : ); SB();
  asm volatile("ds_read_b128 %0, %1 offset:3072" : "=&v"(a1[1][0]) : "v"(er) : "memory"); SB();
  asm volatile("v_mfma_f32_16x16x32_bf16 %0, v[172:175], a[180:183], %0" : "+v"(g[0][1][1]) :  : ); SB();
  asm volatile("ds_read_b128 %0, %1 offset:4096" : "=&v"(a1[1][1]) : "v"(er) : "memory"); SB();
  asm volatile("v_mfma_f32_16x16x32_bf16 %0, v[172:175], a[184:187], %0" : "+v"(g[0][2][1]) :  : ); SB();
  asm volatile("ds_read_b128 %0, %1 offset:5120" : "=&v"(a1[1][2]) : "v"(er) : "memory"); SB();
  asm volatile("v_mfma_f32_16x16x32_bf16 %0, v[172:175], a[188:191], %0" : "+v"(g[0][3][1]) :  : ); SB();
  asm volatile("buffer_load_dwordx4 %0, %1, %2 offen offset:2048 lds" :  : "v"(voS), "s"(rA), "s"(ao[6]) : "memory"); SB();
  asm volatile("s_waitcnt vmcnt(23)" :  :  : "memory"); SB();
  asm volatile("v_mfma_f32_16x16x32_bf16 %0, v[176:179], a[192:195], %0" : "+v"(g[0][0][0]) :  : ); SB();
  ydr = lb + 0 + (w * 2 + (lane >> 5)) * 1024 + (lane & 1) * 512; asm volatile("" : "+v"(ydr)); SB();
  asm volatile("v_mfma_f32_16x16x32_bf16 %0, v[176:179], a[196:199], %0" : "+v"(g[0][1][0]) :  : ); SB();
  ydr = ydr + ((((lane >> 1) & 15) ^ ((lane & 1) << 3)) << 4); asm volatile("" : "+v"(ydr)); SB();
  asm volatile("v_mfma_f32_16x16x32_bf16 %0, v[176:179], a[200:203], %0" : "+v"(g[0][2][0]) :  : ); SB();
  ykr = lb + 59520 + 512 + w * 1024 + l16; asm volatile("" : "+v"(ykr)); SB();
  asm volatile("v_mfma_f32_16x16x32_bf16 %0, v[176:179], a[204:207], %0" : "+v"(g[0][3][0]) :  : ); SB();
  asm volatile("buffer_load_dwordx4 v[168:171], %0, %1, %2 offen offset:0 sc1" :  : "v"(vB), "s"(rWS), "s"(65536u) : "memory"); SB();
  pw2 = er + (122880 - 32768); asm volatile("" : "+v"(pw2)); SB();
  asm volatile("v_mfma_f32_16x16x32_bf16 %0, v[180:183], a[192:195], %0" : "+v"(g[0][0][1]) :  : ); SB();
  pe[0] = lb + 122880 + (w ^ 1) * 6144 + l16; asm volatile("" : "+v"(pe[0])); SB();
  asm volatile("v_mfma_f32_16x16x32_bf16 %0, v[180:183], a[196:199], %0" : "+v"(g[0][1][1]) :  : ); SB();
  pe[1] = lb + 122880 + (w ^ 2) * 6144 + l16; asm volatile("" : "+v"(pe[1])); SB();
  asm volatile("v_mfma_f32_16x16x32_bf16 %0, v[180:183], a[200:203], %0" : "+v"(g[0][2][1]) :  : ); SB();
  asm volatile("buffer_load_dwordx4 v[172:175], %0, %1, %2 offen offset:1024 sc1" :  : "v"(vB), "s"(rWS), "s"(65536u) : "memory"); SB();
  asm volatile("v_mfma_f32_16x16x32_bf16 %0, v[180:183], a[204:207], %0" : "+v"(g[0][3][1]) :  : ); SB();
  pe[2] = lb + 122880 + (w ^ 3) * 6144 + l16; asm volatile("" : "+v"(pe[2])); SB();
  asm volatile("s_waitcnt vmcnt(23)" :  :  : "memory"); SB();
  asm volatile("v_mfma_f32_16x16x32_bf16 %0, v[184:187], a[208:211], %0" : "+v"(g[0][0][0]) :  : ); SB();
  asm volatile("s_waitcnt lgkmcnt(0)" : "+v"(a1[0][0]), "+v"(a1[0][1]), "+v"(a1[0][2]), "+v"(a1[1][0]), "+v"(a1[1][1]), "+v"(a1[1][2]) :  : "memory"); SB();
  asm volatile("v_xor_b32 %0, 917504, %0" : "+v"(vB) :  : "memory"); SB();
  asm volatile("v_mfma_f32_16x16x32_bf16 %0, v[184:187], a[212:215], %0" : "+v"(g[0][1][0]) :  : ); SB();
  dl = (int)(wm * 12928 - wn * 256 + w * 128) - 2 * 12928; asm volatile("" : "+s"(dl)); SB();
  asm volatile("v_mfma_f32_16x16x32_bf16 %0, v[184:187], a[216:219], %0" : "+v"(g[0][2][0]) :  : ); SB();
  asm volatile("buffer_load_dwordx4 v[176:179], %0, %1, %2 offen offset:0 sc1" :  : "v"(vB), "s"(rWS), "s"(0u) : "memory"); SB();
  asm volatile("v_add_u32 %0, %1, %0" : "+v"(aa[0]) : "s"(dl) : ); SB();
  asm volatile("v_mfma_f32_16x16x32_bf16 %0, v[184:187], a[220:223], %0" : "+v"(g[0][3][0]) :  : ); SB();
  asm volatile("v_add_u32 %0, %1, %0" : "+v"(aa[1]) : "s"(dl) : ); SB();
  asm volatile("v_mfma_f32_16x16x32_bf16 %0, v[188:191], a[208:211], %0" : "+v"(g[0][0][1]) :  : ); SB();
  asm volatile("v_add_u32 %0, %1, %0" : "+v"(aa[2]) : "s"(dl) : ); SB();
  asm volatile("v_mfma_f32_16x16x32_bf16 %0, v[188:191], a[212:215], %0" : "+v"(g[0][1][1]) :  : ); SB();
  asm volatile("buffer_load_dwordx4 v[180:183], %0, %1, %2 offen offset:1024 sc1" :  : "v"(vB), "s"(rWS), "s"(0u) : "memory"); SB();
  asm volatile("v_add_u32 %0, %1, %0" : "+v"(aa[3]) : "s"(dl) : ); SB();
  asm volatile("v_mfma_f32_16x16x32_bf16 %0, v[188:191], a[216:219], %0" : "+v"(g[0][2][1]) :  : ); SB();
  asm volatile("v_add_u32 %0, %1, %0" : "+v"(bb) : "s"(dl) : ); SB();
  asm volatile("v_mfma_f32_16x16x32_bf16 %0, v[188:191], a[220:223], %0" : "+v"(g[0][3][1]) :  : ); SB();
  asm volatile("v_mul_f32 %0, %1, %1" : "=v"(gt[0]) : "v"(a1[0][0][0]) : ); SB();
  asm volatile("s_waitcnt vmcnt(23)" :  :  : "memory"); SB();
  asm volatile("v_mfma_f32_16x16x32_bf16 %0, v[192:195], a[224:227], %0" : "+v"(g[0][0][0]) :  : ); SB();
  asm volatile("buffer_load_dwordx4 v[184:187], %0, %1, %2 offen offset:0 sc1" :  : "v"(vB), "s"(rWS), "s"(65536u) : "memory"); SB();
  asm volatile("v_mul_f32 %0, %1, %1" : "=v"(gt[1]) : "v"(a1[0][0][1]) : ); SB();
  asm volatile("v_mfma_f32_16x16x32_bf16 %0, v[192:195], a[228:231], %0" : "+v"(g[0][1][0]) :  : ); SB();
  asm volatile("v_mul_f32 %0, %1, %1" : "=v"(gt[2]) : "v"(a1[0][0][2]) : ); SB();
  asm volatile("v_mfma_f32_16x16x32_bf16 %0, v[192:195], a[232:235], %0" : "+v"(g[0][2][0]) :  : ); SB();
  asm volatile("v_mul_f32 %0, %1, %1" : "=v"(gt[3]) : "v"(a1[0][0][3]) : ); SB();
  asm volatile("v_mfma_f32_16x16x32_bf16 %0, v[192:195], a[236:239], %0" : "+v"(g[0][3][0]) :  : ); SB();
  asm volatile("v_fma_f32 %0, %0, %1, %2" : "+v"(gt[0]) : "v"(k2[0]), "v"(k1[0]) : ); SB();
  asm volatile("v_mfma_f32_16x16x32_bf16 %0, v[196:199], a[224:227], %0" : "+v"(g[0][0][1]) :  : ); SB();
  asm volatile("buffer_load_dwordx4 v[188:191], %0, %1, %2 offen offset:1024 sc1" :  : "v"(vB), "s"(rWS), "s"(65536u) : "memory"); SB();
  asm volatile("v_fma_f32 %0, %0, %1, %2" : "+v"(gt[1]) : "v"(k2[0]), "v"(k1[0]) : ); SB();
  asm volatile("v_mfma_f32_16x16x32_bf16 %0, v[196:199], a[228:231], %0" : "+v"(g[0][1][1]) :  : ); SB();
  asm volatile("v_xor_b32 %0, 131072, %0" : "+v"(vB) :  : "memory"); SB();
  asm volatile("v_fma_f32 %0, %0, %1, %2" : "+v"(gt[2]) : "v"(k2[0]), "v"(k1[0]) : ); SB();
  asm volatile("v_mfma_f32_16x16x32_bf16 %0, v[196:199], a[232:235], %0" : "+v"(g[0][2][1]) :  : ); SB();
  asm volatile("v_fma_f32 %0, %0, %1, %2" : "+v"(gt[3]) : "v"(k2[0]), "v"(k1[0]) : ); SB();
  asm volatile("v_mfma_f32_16x16x32_bf16 %0, v[196:199], a[236:239], %0" : "+v"(g[0][3][1]) :  : ); SB();
  asm volatile("buffer_load_dwordx4 %0, %1, %2 offen offset:3072 lds" :  : "v"(voS), "s"(rA), "s"(ao[7]) : "memory"); SB();
  asm volatile("s_waitcnt vmcnt(24)" :  :  : "memory"); SB();
  asm volatile("v_mfma_f32_16x16x32_bf16 %0, v[200:203], a[240:243], %0" : "+v"(g[0][0][0]) :  : ); SB();
  asm volatile("v_xor_b32 %0, 128, %0" : "+v"(voS) :  : "memory"); SB();
  asm volatile("v_mul_f32 %0, %0, %1" : "+v"(gt[0]) : "v"(a1[0][0][0]) : ); SB();
  asm volatile("v_mfma_f32_16x16x32_bf16 %0, v[200:203], a[244:247], %0" : "+v"(g[0][1][0]) :  : ); SB();
  asm volatile("v_mul_f32 %0, %0, %1" : "+v"(gt[1]) : "v"(a1[0][0][1]) : ); SB();
  asm volatile("v_mfma_f32_16x16x32_bf16 %0, v[200:203], a[248:251], %0" : "+v"(g[0][2][0]) :  : ); SB();
  asm volatile("v_mul_f32 %0, %0, %1" : "+v"(gt[2]) : "v"(a1[0][0][2]) : ); SB();
  asm volatile("v_mfma_f32_16x16x32_bf16 %0, v[200:203], a[252:255], %0" : "+v"(g[0][3][0]) :  : ); SB();
  asm volatile("buffer_load_dwordx4 v[192:195], %0, %1, %2 offen offset:0 sc1" :  : "v"(vB), "s"(rWS), "s"(0u) : "memory"); SB();
  asm volatile("v_mul_f32 %0, %0, %1" : "+v"(gt[3]) : "v"(a1[0][0][3]) : ); SB();
  asm volatile("v_mfma_f32_16x16x32_bf16 %0, v[204:207], a[240:243], %0" : "+v"(g[0][0][1]) :  : ); SB();
  asm volatile("v_exp_f32 %0, %1" : "=v"(ge[0]) : "v"(gt[0]) : ); SB();
  asm volatile("v_mfma_f32_16x16x32_bf16 %0, v[204:207], a[244:247], %0" : "+v"(g[0][1][1]) :  : ); SB();
  asm volatile("v_exp_f32 %0, %1" : "=v"(ge[1]) : "v"(gt[1]) : ); SB();
  asm volatile("v_mfma_f32_16x16x32_bf16 %0, v[204:207], a[248:251], %0" : "+v"(g[0][2][1]) :  : ); SB();
  asm volatile("buffer_load_dwordx4 v[196:199], %0, %1, %2 offen offset:1024 sc1" :  : "v"(vB), "s"(rWS), "s"(0u) : "memory"); SB();
  asm volatile("v_exp_f32 %0, %1" : "=v"(ge[2]) : "v"(gt[2]) : ); SB();
  asm volatile("v_mfma_f32_16x16x32_bf16 %0, v[204:207], a[252:255], %0" : "+v"(g[0][3][1]) :  : ); SB();
  asm volatile("v_exp_f32 %0, %1" : "=v"(ge[3]) : "v"(gt[3]) : ); SB();
  asm volatile("s_waitcnt vmcnt(24)" :  :  : "memory"); SB();
  asm volatile("v_mfma_f32_16x16x32_bf16 %0, v[208:211], a[160:163], %1" : "=&v"(g[1][0][0]) : "v"(bs4) : ); SB();
  asm volatile("v_fma_f32 %0, %0, %1, %1" : "+v"(ge[0]) : "v"(xd[0]) : ); SB();
  asm volatile("v_mfma_f32_16x16x32_bf16 %0, v[208:211], a[164:167], 0" : "=&v"(g[1][1][0]) :  : ); SB();
  asm volatile("buffer_load_dwordx4 v[200:203], %0, %1, %2 offen offset:0 sc1" :  : "v"(vB), "s"(rWS), "s"(65536u) : "memory"); SB();
  asm volatile("v_fma_f32 %0, %0, %1, %1" : "+v"(ge[1]) : "v"(xd[0]) : ); SB();
  asm volatile("v_mfma_f32_16x16x32_bf16 %0, v[208:211], a[168:171], 0" : "=&v"(g[1][2][0]) :  : ); SB();
  asm volatile("v_fma_f32 %0, %0, %1, %1" : "+v"(ge[2]) : "v"(xd[0]) : ); SB();
  asm volatile("v_mfma_f32_16x16x32_bf16 %0, v[208:211], a[172:175], 0" : "=&v"(g[1][3][0]) :  : ); SB();
  asm volatile("v_fma_f32 %0, %0, %1, %1" : "+v"(ge[3]) : "v"(xd[0]) : ); SB();
  asm volatile("v_mfma_f32_16x16x32_bf16 %0, v[212:215], a[160:163], %1" : "=&v"(g[1][0][1]) : "v"(bs4) : ); SB();
  asm volatile("v_rcp_f32 %0, %1" : "=v"(gt[0]) : "v"(ge[0]) : ); SB();
  asm volatile("v_mfma_f32_16x16x32_bf16 %0, v[212:215], a[164:167], 0" : "=&v"(g[1][1][1]) :  : ); SB();
  asm volatile("v_rcp_f32 %0, %1" : "=v"(gt[1]) : "v"(ge[1]) : ); SB();
  asm volatile("v_mfma_f32_16x16x32_bf16 %0, v[212:215], a[168:171], 0" : "=&v"(g[1][2][1]) :  : ); SB();
  asm volatile("v_rcp_f32 %0, %1" : "=v"(gt[2]) : "v"(ge[2]) : ); SB();
  asm volatile("v_mfma_f32_16x16x32_bf16 %0, v[212:215], a[172:175], 0" : "=&v"(g[1][3][1]) :  : ); SB();
  asm volatile("v_rcp_f32 %0, %1" : "=v"(gt[3]) : "v"(ge[3]) : ); SB();
  asm volatile("s_waitcnt vmcnt(23)" :  :  : "memory"); SB();
  asm volatile("v_mfma_f32_16x16x32_bf16 %0, v[216:219], a[176:179], %0" : "+v"(g[1][0][0]) :  : ); SB();
  asm volatile("v_mul_f32 %0, %0, %1" : "+v"(a1[0][0][0]) : "v"(gt[0]) : ); SB();
  asm volatile("v_mfma_f32_16x16x32_bf16 %0, v[216:219], a[180:183], %0" : "+v"(g[1][1][0]) :  : ); SB();
  asm volatile("s_waitcnt vmcnt(13)" :  :  : "memory"); SB();
  asm volatile("ds_read_b128 a[104:107], %0 offset:36864" :  : "v"(aA[0]) : "memory"); SB();
  asm volatile("v_mfma_f32_16x16x32_bf16 %0, v[216:219], a[184:187], %0" : "+v"(g[1][2][0]) :  : ); SB();
  asm volatile("ds_read_b128 a[120:123], %0 offset:36864" :  : "v"(aA[1]) : "memory"); SB();
  asm volatile("v_mul_f32 %0, %0, %1" : "+v"(a1[0][0][1]) : "v"(gt[1]) : ); SB();
  asm volatile("v_mfma_f32_16x16x32_bf16 %0, v[216:219], a[188:191], %0" : "+v"(g[1][3][0]) :  : ); SB();
  asm volatile("buffer_load_dwordx4 v[204:207], %0, %1, %2 offen offset:1024 sc1" :  : "v"(vB), "s"(rWS), "s"(65536u) : "memory"); SB();
  asm volatile("v_mul_f32 %0, %0, %1" : "+v"(a1[0][0][2]) : "v"(gt[2]) : ); SB();
  asm volatile("v_mfma_f32_16x16x32_bf16 %0, v[220:223], a[176:179], %0" : "+v"(g[1][0][1]) :  : ); SB();
  asm volatile("v_xor_b32 %0, 393216, %0" : "+v"(vB) :  : "memory"); SB();
  asm volatile("v_mul_f32 %0, %0, %1" : "+v"(a1[0][0][3]) : "v"(gt[3]) : ); SB();
  asm volatile("v_mfma_f32_16x16x32_bf16 %0, v[220:223], a[180:183], %0" : "+v"(g[1][1][1]) :  : ); SB();
  asm volatile("buffer_load_dwordx4 v[208:211], %0, %1, %2 offen offset:0 sc1" :  : "v"(vB), "s"(rWS), "s"(0u) : "memory"); SB();
  asm volatile("v_mul_f32 %0, %1, %1" : "=v"(gt[0]) : "v"(a1[0][1][0]) : ); SB();
  asm volatile("v_mfma_f32_16x16x32_bf16 %0, v[220:223], a[184:187], %0" : "+v"(g[1][2][1]) :  : ); SB();
  asm volatile("v_mul_f32 %0, %1, %1" : "=v"(gt[1]) : "v"(a1[0][1][1]) : ); SB();
  asm volatile("v_mfma_f32_16x16x32_bf16 %0, v[220:223], a[188:191], %0" : "+v"(g[1][3][1]) :  : ); SB();
  asm volatile("v_mul_f32 %0, %1, %1" : "=v"(gt[2]) : "v"(a1[0][1][2]) : ); SB();
  ST(26);
  asm volatile("v_mfma_f32_16x16x32_bf16 %0, v[224:227], a[192:195], %0" : "+v"(g[1][0][0]) :  : ); SB();
  vH_ = vH; asm volatile("" : "+v"(vH_)); SB();
  asm volatile("v_mfma_f32_16x16x32_bf16 %0, v[224:227], a[196:199], %0" : "+v"(g[1][1][0]) :  : ); SB();
  vO = W_WO + (cb * 120 + w * 30 + 0u * 15) * 1024 + l16; asm volatile("" : "+v"(vO)); SB();
  asm volatile("v_mfma_f32_16x16x32_bf16 %0, v[224:227], a[200:203], %0" : "+v"(g[1][2][0]) :  : ); SB();
  vO2 = vO + 15360 - 0u * 30720; asm volatile("" : "+v"(vO2)); SB();
  asm volatile("v_mfma_f32_16x16x32_bf16 %0, v[224:227], a[204:207], %0" : "+v"(g[1][3][0]) :  : ); SB();
  vFH = W_FH + rb * 256 + w * 64 + (lane & 7) * 8; asm volatile("" : "+v"(vFH)); SB();
  asm volatile("v_mfma_f32_16x16x32_bf16 %0, v[228:231], a[192:195], %0" : "+v"(g[1][0][1]) :  : ); SB();
  vHr = W_HH + (rb * 96 + w * 6) * 1024 + l16; asm volatile("" : "+v"(vHr)); SB();
  asm volatile("v_mfma_f32_16x16x32_bf16 %0, v[228:231], a[196:199], %0" : "+v"(g[1][1][1]) :  : ); SB();
  vX = (rb * 64 + w * 16 + l15) * 1280; asm volatile("" : "+v"(vX)); SB();
  asm volatile("v_mfma_f32_16x16x32_bf16 %0, v[228:231], a[200:203], %0" : "+v"(g[1][2][1]) :  : ); SB();
  vX = vX + cb * 160 + 8 * g4; asm volatile("" : "+v"(vX)); SB();
  asm volatile("v_mfma_f32_16x16x32_bf16 %0, v[228:231], a[204:207], %0" : "+v"(g[1][3][1]) :  : ); SB();
  sFH = W_FH + ((rb * 8 + cb) * 4 + w) * 8; asm volatile("" : "+s"(sFH)); SB();
  asm volatile("v_mfma_f32_16x16x32_bf16 %0, v[232:235], a[208:211], %0" : "+v"(g[1][0][0]) :  : ); SB();
  asm volatile("buffer_load_dwordx4 v[212:215], %0, %1, %2 offen offset:1024 sc1" :  : "v"(vB), "s"(rWS), "s"(0u) : "memory"); SB();
  asm volatile("s_add_u32 m0, %0, 98304" :  : "s"(mA) : "memory", "scc"); SB();
  asm volatile("v_mfma_f32_16x16x32_bf16 %0, v[232:235], a[212:215], %0" : "+v"(g[1][1][0]) :  : ); SB();
  asm volatile("buffer_load_dwordx4 %0, %1, %2 offen lds" :  : "v"(voS), "s"(rA), "s"(ao[0]) : "memory"); SB();
  asm volatile("v_mov_b32 %0, -1" : "=v"(ho[2][0]) :  : ); SB();
  asm volatile("v_mfma_f32_16x16x32_bf16 %0, v[232:235], a[216:219], %0" : "+v"(g[1][2][0]) :  : ); SB();
  asm volatile("v_mov_b32 %0, -1" : "=v"(ho[2][1]) :  : ); SB();
  asm volatile("v_mfma_f32_16x16x32_bf16 %0, v[232:235], a[220:223], %0" : "+v"(g[1][3][0]) :  : ); SB();
  asm volatile("v_mov_b32 %0, -1" : "=v"(ho[2][2]) :  : ); SB();
  asm volatile("v_mfma_f32_16x16x32_bf16 %0, v[236:239], a[208:211], %0" : "+v"(g[1][0][1]) :  : ); SB();
  asm volatile("buffer_load_dwordx4 %0, %1, %2 offen offset:1024 lds" :  : "v"(voS), "s"(rA), "s"(ao[1]) : "memory"); SB();
  asm volatile("v_mov_b32 %0, -1" : "=v"(ho[2][3]) :  : ); SB();
  asm volatile("v_mfma_f32_16x16x32_bf16 %0, v[236:239], a[212:215], %0" : "+v"(g[1][1][1]) :  : ); SB();
  hso[0] = (w ^ 0) * 24576 + 0u * 3072; asm volatile("" : "+s"(hso[0])); SB();
  hso[1] = hso[0] ^ 3072; asm volatile("" : "+s"(hso[1])); SB();
  asm volatile("v_mfma_f32_16x16x32_bf16 %0, v[236:239], a[216:219], %0" : "+v"(g[1][2][1]) :  : ); SB();
  hso[2] = (w ^ 1) * 24576 + 0u * 3072; asm volatile("" : "+s"(hso[2])); SB();
  hso[3] = hso[2] ^ 3072; asm volatile("" : "+s"(hso[3])); SB();
  asm volatile("v_mfma_f32_16x16x32_bf16 %0, v[236:239], a[220:223], %0" : "+v"(g[1][3][1]) :  : ); SB();
  hso[4] = (w ^ 2) * 24576 + 0u * 3072; asm volatile("" : "+s"(hso[4])); SB();
  asm volatile("v_mfma_f32_16x16x32_bf16 %0, v[240:243], a[224:227], %0" : "+v"(g[1][0][0]) :  : ); SB();
  hso[5] = hso[4] ^ 3072; asm volatile("" : "+s"(hso[5])); SB();
  hso[6] = (w ^ 3) * 24576 + 0u * 3072; asm volatile("" : "+s"(hso[6])); SB();
  asm volatile("v_mfma_f32_16x16x32_bf16 %0, v[240:243], a[228:231], %0" : "+v"(g[1][1][0]) :  : ); SB();
  hso[7] = hso[6] ^ 3072; asm volatile("" : "+s"(hso[7])); SB();
  asm volatile("buffer_load_dwordx4 %0, %1, %2 offen offset:2048 lds" :  : "v"(voS), "s"(rA), "s"(ao[2]) : "memory"); SB();
  asm volatile("v_mfma_f32_16x16x32_bf16 %0, v[240:243], a[232:235], %0" : "+v"(g[1][2][0]) :  : ); SB();
  asm volatile("v_mul_f32 %0, %1, %1" : "=v"(gt[3]) : "v"(a1[0][1][3]) : ); SB();
  asm volatile("v_mfma_f32_16x16x32_bf16 %0, v[240:243], a[236:239], %0" : "+v"(g[1][3][0]) :  : ); SB();
  asm volatile("buffer_load_dwordx4 %0, %1, %2 offen offset:3072 lds" :  : "v"(voS), "s"(rA), "s"(ao[3]) : "memory"); SB();
  asm volatile("v_fma_f32 %0, %0, %1, %2" : "+v"(gt[0]) : "v"(k2[0]), "v"(k1[0]) : ); SB();
  asm volatile("v_mfma_f32_16x16x32_bf16 %0, v[244:247], a[224:227], %0" : "+v"(g[1][0][1]) :  : ); SB();
  asm volatile("v_fma_f32 %0, %0, %1, %2" : "+v"(gt[1]) : "v"(k2[0]), "v"(k1[0]) : ); SB();
  asm volatile("v_mfma_f32_16x16x32_bf16 %0, v[244:247], a[228:231], %0" : "+v"(g[1][1][1]) :  : ); SB();
  asm volatile("v_fma_f32 %0, %0, %1, %2" : "+v"(gt[2]) : "v"(k2[0]), "v"(k1[0]) : ); SB();
  asm volatile("v_mfma_f32_16x16x32_bf16 %0, v[244:247], a[232:235], %0" : "+v"(g[1][2][1]) :  : ); SB();
  asm volatile("s_add_u32 m0, %0, 102400" :  : "s"(mA) : "memory", "scc"); SB();
  asm volatile("v_fma_f32 %0, %0, %1, %2" : "+v"(gt[3]) : "v"(k2[0]), "v"(k1[0]) : ); SB();
  asm volatile("v_mfma_f32_16x16x32_bf16 %0, v[244:247], a[236:239], %0" : "+v"(g[1][3][1]) :  : ); SB();
  asm volatile("buffer_load_dwordx4 %0, %1, %2 offen lds" :  : "v"(voS), "s"(rA), "s"(ao[4]) : "memory"); SB();
  asm volatile("v_mfma_f32_16x16x32_bf16 %0, v[248:251], a[240:243], %0" : "+v"(g[1][0][0]) :  : ); SB();
  asm volatile("v_mul_f32 %0, %0, %1" : "+v"(gt[0]) : "v"(a1[0][1][0]) : ); SB();
  asm volatile("v_mfma_f32_16x16x32_bf16 %0, v[248:251], a[244:247], %0" : "+v"(g[1][1][0]) :  : ); SB();
  asm volatile("v_mul_f32 %0, %0, %1" : "+v"(gt[1]) : "v"(a1[0][1][1]) : ); SB();
  asm volatile("v_mfma_f32_16x16x32_bf16 %0, v[248:251], a[248:251], %0" : "+v"(g[1][2][0]) :  : ); SB();
  asm volatile("buffer_load_dwordx4 %0, %1, %2 offen offset:1024 lds" :  : "v"(voS), "s"(rA), "s"(ao[5]) : "memory"); SB();
  asm volatile("v_mul_f32 %0, %0, %1" : "+v"(gt[2]) : "v"(a1[0][1][2]) : ); SB();
  asm volatile("v_mfma_f32_16x16x32_bf16 %0, v[248:251], a[252:255], %0" : "+v"(g[1][3][0]) :  : ); SB();
  asm volatile("v_mul_f32 %0, %0, %1" : "+v"(gt[3]) : "v"(a1[0][1][3]) : ); SB();
  asm volatile("v_mfma_f32_16x16x32_bf16 %0, v[252:255], a[240:243], %0" : "+v"(g[1][0][1]) :  : ); SB();
  asm volatile("s_waitcnt vmcnt(12)" :  :  : "memory"); SB();
  asm volatile("ds_read_b128 a[108:111], %0 offset:38912" :  : "v"(aA[0]) : "memory"); SB();
  asm volatile("v_mfma_f32_16x16x32_bf16 %0, v[252:255], a[244:247], %0" : "+v"(g[1][1][1]) :  : ); SB();
  asm volatile("ds_read_b128 a[124:127], %0 offset:38912" :  : "v"(aA[1]) : "memory"); SB();
  asm volatile("v_exp_f32 %0, %1" : "=v"(ge[0]) : "v"(gt[0]) : ); SB();
  asm volatile("v_mfma_f32_16x16x32_bf16 %0, v[252:255], a[248:251], %0" : "+v"(g[1][2][1]) :  : ); SB();
  asm volatile("buffer_load_dwordx4 %0, %1, %2 offen offset:2048 lds" :  : "v"(voS), "s"(rA), "s"(ao[6]) : "memory"); SB();
  asm volatile("v_exp_f32 %0, %1" : "=v"(ge[1]) : "v"(gt[1]) : ); SB();
  asm volatile("v_mfma_f32_16x16x32_bf16 %0, v[252:255], a[252:255], %0" : "+v"(g[1][3][1]) :  : ); SB();
  asm volatile("v_exp_f32 %0, %1" : "=v"(ge[2]) : "v"(gt[2]) : ); SB();
  asm volatile("v_mfma_f32_16x16x32_bf16 %0, v[160:163], a[0:3], %0" : "+v"(g[0][0][0]) :  : ); SB();
  asm volatile("buffer_load_dwordx4 %0, %1, %2 offen offset:3072 lds" :  : "v"(voS), "s"(rA), "s"(ao[7]) : "memory"); SB();
  asm volatile("v_exp_f32 %0, %1" : "=v"(ge[3]) : "v"(gt[3]) : ); SB();
  asm volatile("v_mfma_f32_16x16x32_bf16 %0, v[160:163], a[4:7], %0" : "+v"(g[0][1][0]) :  : ); SB();
  asm volatile("v_fma_f32 %0, %0, %1, %1" : "+v"(ge[0]) : "v"(xd[0]) : ); SB();
  asm volatile("v_mfma_f32_16x16x32_bf16 %0, v[160:163], a[8:11], %0" : "+v"(g[0][2][0]) :  : ); SB();
  asm volatile("v_fma_f32 %0, %0, %1, %1" : "+v"(ge[1]) : "v"(xd[0]) : ); SB();
  asm volatile("v_mfma_f32_16x16x32_bf16 %0, v[160:163], a[12:15], %0" : "+v"(g[0][3][0]) :  : ); SB();
  asm volatile("v_fma_f32 %0, %0, %1, %1" : "+v"(ge[2]) : "v"(xd[0]) : ); SB();
  asm volatile("v_mfma_f32_16x16x32_bf16 %0, v[164:167], a[0:3], %0" : "+v"(g[0][0][1]) :  : ); SB();
  asm volatile("buffer_load_dwordx4 v[216:219], %0, %1, %2 offen offset:0 sc1" :  : "v"(vB), "s"(rWS), "s"(65536u) : "memory"); SB();
  asm volatile("v_fma_f32 %0, %0, %1, %1" : "+v"(ge[3]) : "v"(xd[0]) : ); SB();
  asm volatile("v_mfma_f32_16x16x32_bf16 %0, v[164:167], a[4:7], %0" : "+v"(g[0][1][1]) :  : ); SB();
  asm volatile("v_rcp_f32 %0, %1" : "=v"(gt[0]) : "v"(ge[0]) : ); SB();
  asm volatile("v_mfma_f32_16x16x32_bf16 %0, v[164:167], a[8:11], %0" : "+v"(g[0][2][1]) :  : ); SB();
  asm volatile("v_rcp_f32 %0, %1" : "=v"(gt[1]) : "v"(ge[1]) : ); SB();
  asm volatile("v_mfma_f32_16x16x32_bf16 %0, v[164:167], a[12:15], %0" : "+v"(g[0][3][1]) :  : ); SB();
  asm volatile("buffer_load_dwordx4 v[220:223], %0, %1, %2 offen offset:1024 sc1" :  : "v"(vB), "s"(rWS), "s"(65536u) : "memory"); SB();
  asm volatile("v_mfma_f32_16x16x32_bf16 %0, v[168:171], a[16:19], %0" : "+v"(g[0][0][0]) :  : ); SB();
  asm volatile("v_xor_b32 %0, 131072, %0" : "+v"(vB) :  : "memory"); SB();
  asm volatile("v_rcp_f32 %0, %1" : "=v"(gt[2]) : "v"(ge[2]) : ); SB();
  asm volatile("v_mfma_f32_16x16x32_bf16 %0, v[168:171], a[20:23], %0" : "+v"(g[0][1][0]) :  : ); SB();
  asm volatile("v_rcp_f32 %0, %1" : "=v"(gt[3]) : "v"(ge[3]) : ); SB();
  asm volatile("v_mfma_f32_16x16x32_bf16 %0, v[168:171], a[24:27], %0" : "+v"(g[0][2][0]) :  : ); SB();
  asm volatile("v_mul_f32 %0, %0, %1" : "+v"(a1[0][1][0]) : "v"(gt[0]) : ); SB();
  asm volatile("v_mfma_f32_16x16x32_bf16 %0, v[168:171], a[28:31], %0" : "+v"(g[0][3][0]) :  : ); SB();
  asm volatile("buffer_load_dwordx4 v[224:227], %0, %1, %2 offen offset:0 sc1" :  : "v"(vB), "s"(rWS), "s"(0u) : "memory"); SB();
  asm volatile("v_mul_f32 %0, %0, %1" : "+v"(a1[0][1][1]) : "v"(gt[1]) : ); SB();
  asm volatile("v_mfma_f32_16x16x32_bf16 %0, v[172:175], a[16:19], %0" : "+v"(g[0][0][1]) :  : ); SB();
  asm volatile("v_mul_f32 %0, %0, %1" : "+v"(a1[0][1][2]) : "v"(gt[2]) : ); SB();
  asm volatile("v_mfma_f32_16x16x32_bf16 %0, v[172:175], a[20:23], %0" : "+v"(g[0][1][1]) :  : ); SB();
  asm volatile("v_mul_f32 %0, %0, %1" : "+v"(a1[0][1][3]) : "v"(gt[3]) : ); SB();
  asm volatile("v_mfma_f32_16x16x32_bf16 %0, v[172:175], a[24:27], %0" : "+v"(g[0][2][1]) :  : ); SB();
  asm volatile("buffer_load_dwordx4 v[228:231], %0, %1, %2 offen offset:1024 sc1" :  : "v"(vB), "s"(rWS), "s"(0u) : "memory"); SB();
  asm volatile("v_mul_f32 %0, %1, %1" : "=v"(gt[0]) : "v"(a1[0][2][0]) : ); SB();
  asm volatile("v_mfma_f32_16x16x32_bf16 %0, v[172:175], a[28:31], %0" : "+v"(g[0][3][1]) :  : ); SB();
  asm volatile("v_mul_f32 %0, %1, %1" : "=v"(gt[1]) : "v"(a1[0][2][1]) : ); SB();
  asm volatile("v_mfma_f32_16x16x32_bf16 %0, v[176:179], a[32:35], %0" : "+v"(g[0][0][0]) :  : ); SB();
  asm volatile("v_mul_f32 %0, %1, %1" : "=v"(gt[2]) : "v"(a1[0][2][2]) : ); SB();
  asm volatile("v_mfma_f32_16x16x32_bf16 %0, v[176:179], a[36:39], %0" : "+v"(g[0][1][0]) :  : ); SB();
  asm volatile("buffer_load_dwordx4 v[232:235], %0, %1, %2 offen offset:0 sc1" :  : "v"(vB), "s"(rWS), "s"(65536u) : "memory"); SB();
  asm volatile("v_mul_f32 %0, %1, %1" : "=v"(gt[3]) : "v"(a1[0][2][3]) : ); SB();
  asm volatile("v_mfma_f32_16x16x32_bf16 %0, v[176:179], a[40:43], %0" : "+v"(g[0][2][0]) :  : ); SB();
  asm volatile("v_fma_f32 %0, %0, %1, %2" : "+v"(gt[0]) : "v"(k2[0]), "v"(k1[0]) : ); SB();
  asm volatile("v_mfma_f32_16x16x32_bf16 %0, v[176:179], a[44:47], %0" : "+v"(g[0][3][0]) :  : ); SB();
  asm volatile("v_fma_f32 %0, %0, %1, %2" : "+v"(gt[1]) : "v"(k2[0]), "v"(k1[0]) : ); SB();
  asm volatile("v_mfma_f32_16x16x32_bf16 %0, v[180:183], a[32:35], %0" : "+v"(g[0][0][1]) :  : ); SB();
  asm volatile("v_fma_f32 %0, %0, %1, %2" : "+v"(gt[2]) : "v"(k2[0]), "v"(k1[0]) : ); SB();
  asm volatile("v_mfma_f32_16x16x32_bf16 %0, v[180:183], a[36:39], %0" : "+v"(g[0][1][1]) :  : ); SB();
  asm volatile("v_fma_f32 %0, %0, %1, %2" : "+v"(gt[3]) : "v"(k2[0]), "v"(k1[0]) : ); SB();
  asm volatile("v_mfma_f32_16x16x32_bf16 %0, v[180:183], a[40:43], %0" : "+v"(g[0][2][1]) :  : ); SB();
  asm volatile("v_mul_f32 %0, %0, %1" : "+v"(gt[0]) : "v"(a1[0][2][0]) : ); SB();
  asm volatile("v_mfma_f32_16x16x32_bf16 %0, v[180:183], a[44:47], %0" : "+v"(g[0][3][1]) :  : ); SB();
  asm volatile("v_mul_f32 %0, %0, %1" : "+v"(gt[1]) : "v"(a1[0][2][1]) : ); SB();
  asm volatile("v_mfma_f32_16x16x32_bf16 %0, v[184:187], a[48:51], %0" : "+v"(g[0][0][0]) :  : ); SB();
  asm volatile("v_mul_f32 %0, %0, %1" : "+v"(gt[2]) : "v"(a1[0][2][2]) : ); SB();
  asm volatile("v_mfma_f32_16x16x32_bf16 %0, v[184:187], a[52:55], %0" : "+v"(g[0][1][0]) :  : ); SB();
  asm volatile("buffer_load_dwordx4 v[236:239], %0, %1, %2 offen offset:1024 sc1" :  : "v"(vB), "s"(rWS), "s"(65536u) : "memory"); SB();
  asm volatile("v_mul_f32 %0, %0, %1" : "+v"(gt[3]) : "v"(a1[0][2][3]) : ); SB();
  asm volatile("v_mfma_f32_16x16x32_bf16 %0, v[184:187], a[56:59], %0" : "+v"(g[0][2][0]) :  : ); SB();
  asm volatile("v_xor_b32 %0, 524288, %0" : "+v"(vB) :  : "memory"); SB();
  asm volatile("v_exp_f32 %0, %1" : "=v"(ge[0]) : "v"(gt[0]) : ); SB();
  asm volatile("v_mfma_f32_16x16x32_bf16 %0, v[184:187], a[60:63], %0" : "+v"(g[0][3][0]) :  : ); SB();
  asm volatile("v_exp_f32 %0, %1" : "=v"(ge[1]) : "v"(gt[1]) : ); SB();
  asm volatile("v_mfma_f32_16x16x32_bf16 %0, v[188:191], a[48:51], %0" : "+v"(g[0][0][1]) :  : ); SB();
  asm volatile("buffer_load_dwordx4 v[240:243], %0, %1, %2 offen offset:0 sc1" :  : "v"(vB), "s"(rWS), "s"(2048u) : "memory"); SB();
  asm volatile("v_exp_f32 %0, %1" : "=v"(ge[2]) : "v"(gt[2]) : ); SB();
  asm volatile("v_mfma_f32_16x16x32_bf16 %0, v[188:191], a[52:55], %0" : "+v"(g[0][1][1]) :  : ); SB();
  asm volatile("v_exp_f32 %0, %1" : "=v"(ge[3]) : "v"(gt[3]) : ); SB();
  asm volatile("v_mfma_f32_16x16x32_bf16 %0, v[188:191], a[56:59], %0" : "+v"(g[0][2][1]) :  : ); SB();
  asm volatile("v_fma_f32 %0, %0, %1, %1" : "+v"(ge[0]) : "v"(xd[0]) : ); SB();
  asm volatile("v_mfma_f32_16x16x32_bf16 %0, v[188:191], a[60:63], %0" : "+v"(g[0][3][1]) :  : ); SB();
  asm volatile("v_fma_f32 %0, %0, %1, %1" : "+v"(ge[1]) : "v"(xd[0]) : ); SB();
  ST(27);
  asm volatile("s_waitcnt vmcnt(19)" :  :  : "memory"); SB();
  asm volatile("v_mfma_f32_16x16x32_bf16 %0, v[192:195], a[64:67], %0" : "+v"(g[0][0][0]) :  : ); SB();
  asm volatile("s_waitcnt vmcnt(13)" :  :  : "memory"); SB();
  asm volatile("ds_read_b128 a[128:131], %0 offset:0" :  : "v"(aA[0]) : "memory"); SB();
  asm volatile("v_mfma_f32_16x16x32_bf16 %0, v[192:195], a[68:71], %0" : "+v"(g[0][1][0]) :  : ); SB();
  asm volatile("ds_read_b128 a[144:147], %0 offset:0" :  : "v"(aA[1]) : "memory"); SB();
  asm volatile("v_fma_f32 %0, %0, %1, %1" : "+v"(ge[2]) : "v"(xd[0]) : ); SB();
  asm volatile("v_mfma_f32_16x16x32_bf16 %0, v[192:195], a[72:75], %0" : "+v"(g[0][2][0]) :  : ); SB();
  asm volatile("buffer_load_dwordx4 v[244:247], %0, %1, %2 offen offset:1024 sc1" :  : "v"(vB), "s"(rWS), "s"(2048u) : "memory"); SB();
  asm volatile("v_fma_f32 %0, %0, %1, %1" : "+v"(ge[3]) : "v"(xd[0]) : ); SB();
  asm volatile("v_mfma_f32_16x16x32_bf16 %0, v[192:195], a[76:79], %0" : "+v"(g[0][3][0]) :  : ); SB();
  asm volatile("buffer_load_dwordx4 v[248:251], %0, %1, %2 offen offset:0 sc1" :  : "v"(vB), "s"(rWS), "s"(67584u) : "memory"); SB();
  asm volatile("v_rcp_f32 %0, %1" : "=v"(gt[0]) : "v"(ge[0]) : ); SB();
  asm volatile("v_mfma_f32_16x16x32_bf16 %0, v[196:199], a[64:67], %0" : "+v"(g[0][0][1]) :  : ); SB();
  asm volatile("v_rcp_f32 %0, %1" : "=v"(gt[1]) : "v"(ge[1]) : ); SB();
  asm volatile("v_mfma_f32_16x16x32_bf16 %0, v[196:199], a[68:71], %0" : "+v"(g[0][1][1]) :  : ); SB();
  asm volatile("v_rcp_f32 %0, %1" : "=v"(gt[2]) : "v"(ge[2]) : ); SB();
  asm volatile("v_mfma_f32_16x16x32_bf16 %0, v[196:199], a[72:75], %0" : "+v"(g[0][2][1]) :  : ); SB();
  asm volatile("s_waitcnt vmcnt(13)" :  :  : "memory"); SB();
  asm volatile("ds_read_b128 a[132:135], %0 offset:2048" :  : "v"(aA[0]) : "memory"); SB();
  asm volatile("v_mfma_f32_16x16x32_bf16 %0, v[196:199], a[76:79], %0" : "+v"(g[0][3][1]) :  : ); SB();
  asm volatile("ds_read_b128 a[148:151], %0 offset:2048" :  : "v"(aA[1]) : "memory"); SB();
  asm volatile("v_mfma_f32_16x16x32_bf16 %0, v[200:203], a[80:83], %0" : "+v"(g[0][0][0]) :  : ); SB();
  asm volatile("buffer_load_dwordx4 v[252:255], %0, %1, %2 offen offset:1024 sc1" :  : "v"(vB), "s"(rWS), "s"(67584u) : "memory"); SB();
  asm volatile("v_rcp_f32 %0, %1" : "=v"(gt[3]) : "v"(ge[3]) : ); SB();
  asm volatile("v_mfma_f32_16x16x32_bf16 %0, v[200:203], a[84:87], %0" : "+v"(g[0][1][0]) :  : ); SB();
  asm volatile("v_xor_b32 %0, 917504, %0" : "+v"(vB) :  : "memory"); SB();
  asm volatile("v_mul_f32 %0, %0, %1" : "+v"(a1[0][2][0]) : "v"(gt[0]) : ); SB();
  asm volatile("v_mfma_f32_16x16x32_bf16 %0, v[200:203], a[88:91], %0" : "+v"(g[0][2][0]) :  : ); SB();
  asm volatile("buffer_load_dwordx4 v[160:163], %0, %1, %2 offen offset:0 sc1" :  : "v"(vB), "s"(rWS), "s"(2048u) : "memory"); SB();
  asm volatile("v_mul_f32 %0, %0, %1" : "+v"(a1[0][2][1]) : "v"(gt[1]) : ); SB();
  asm volatile("v_mfma_f32_16x16x32_bf16 %0, v[200:203], a[92:95], %0" : "+v"(g[0][3][0]) :  : ); SB();
  asm volatile("v_mul_f32 %0, %0, %1" : "+v"(a1[0][2][2]) : "v"(gt[2]) : ); SB();
  asm volatile("v_mfma_f32_16x16x32_bf16 %0, v[204:207], a[80:83], %0" : "+v"(g[0][0][1]) :  : ); SB();
  asm volatile("v_mul_f32 %0, %0, %1" : "+v"(a1[0][2][3]) : "v"(gt[3]) : ); SB();
  asm volatile("v_mfma_f32_16x16x32_bf16 %0, v[204:207], a[84:87], %0" : "+v"(g[0][1][1]) :  : ); SB();
  asm volatile("s_waitcnt vmcnt(13)" :  :  : "memory"); SB();
  asm volatile("ds_read_b128 a[136:139], %0 offset:4096" :  : "v"(aA[0]) : "memory"); SB();
  asm volatile("v_mfma_f32_16x16x32_bf16 %0, v[204:207], a[88:91], %0" : "+v"(g[0][2][1]) :  : ); SB();
  asm volatile("ds_read_b128 a[152:155], %0 offset:4096" :  : "v"(aA[1]) : "memory"); SB();
  asm volatile("v_mul_f32 %0, %1, %1" : "=v"(gt[0]) : "v"(a1[1][0][0]) : ); SB();
  asm volatile("v_mfma_f32_16x16x32_bf16 %0, v[204:207], a[92:95], %0" : "+v"(g[0][3][1]) :  : ); SB();
  asm volatile("buffer_load_dwordx4 v[164:167], %0, %1, %2 offen offset:1024 sc1" :  : "v"(vB), "s"(rWS), "s"(2048u) : "memory"); SB();
  asm volatile("v_mfma_f32_16x16x32_bf16 %0, v[208:211], a[96:99], %0" : "+v"(g[0][0][0]) :  : ); SB();
  asm volatile("v_mul_f32 %0, %1, %1" : "=v"(gt[1]) : "v"(a1[1][0][1]) : ); SB();
  asm volatile("v_mfma_f32_16x16x32_bf16 %0, v[208:211], a[100:103], %0" : "+v"(g[0][1][0]) :  : ); SB();
  asm volatile("buffer_load_dwordx4 v[168:171], %0, %1, %2 offen offset:0 sc1" :  : "v"(vB), "s"(rWS), "s"(67584u) : "memory"); SB();
  asm volatile("s_waitcnt lgkmcnt(2)\n\tv_mfma_f32_16x16x32_bf16 %0, v[208:211], a[104:107], %0" : "+v"(g[0][2][0]) :  : ); SB();
  asm volatile("v_mul_f32 %0, %1, %1" : "=v"(gt[2]) : "v"(a1[1][0][2]) : ); SB();
  asm volatile("v_mfma_f32_16x16x32_bf16 %0, v[208:211], a[108:111], %0" : "+v"(g[0][3][0]) :  : ); SB();
  asm volatile("v_mul_f32 %0, %1, %1" : "=v"(gt[3]) : "v"(a1[1][0][3]) : ); SB();
  asm volatile("v_mfma_f32_16x16x32_bf16 %0, v[212:215], a[96:99], %0" : "+v"(g[0][0][1]) :  : ); SB();
  asm volatile("s_waitcnt vmcnt(13)" :  :  : "memory"); SB();
  asm volatile("ds_read_b128 a[140:143], %0 offset:6144" :  : "v"(aA[0]) : "memory"); SB();
  asm volatile("v_mfma_f32_16x16x32_bf16 %0, v[212:215], a[100:103], %0" : "+v"(g[0][1][1]) :  : ); SB();
  asm volatile("ds_read_b128 a[156:159], %0 offset:6144" :  : "v"(aA[1]) : "memory"); SB();
  asm volatile("v_fma_f32 %0, %0, %1, %2" : "+v"(gt[0]) : "v"(k2[0]), "v"(k1[0]) : ); SB();
  asm volatile("v_mfma_f32_16x16x32_bf16 %0, v[212:215], a[104:107], %0" : "+v"(g[0][2][1]) :  : ); SB();
  asm volatile("buffer_load_dwordx4 v[172:175], %0, %1, %2 offen offset:1024 sc1" :  : "v"(vB), "s"(rWS), "s"(67584u) : "memory"); SB();
  asm volatile("v_fma_f32 %0, %0, %1, %2" : "+v"(gt[1]) : "v"(k2[0]), "v"(k1[0]) : ); SB();
  asm volatile("v_mfma_f32_16x16x32_bf16 %0, v[212:215], a[108:111], %0" : "+v"(g[0][3][1]) :  : ); SB();
  asm volatile("v_xor_b32 %0, 131072, %0" : "+v"(vB) :  : "memory"); SB();
  asm volatile("s_waitcnt vmcnt(12)" :  :  : "memory"); SB();
  asm volatile("v_mfma_f32_16x16x32_bf16 %0, v[216:219], a[112:115], %0" : "+v"(g[0][0][0]) :  : ); SB();
  asm volatile("buffer_load_dwordx4 v[176:179], %0, %1, %2 offen offset:0 sc1" :  : "v"(vB), "s"(rWS), "s"(2048u) : "memory"); SB();
  asm volatile("v_fma_f32 %0, %0, %1, %2" : "+v"(gt[2]) : "v"(k2[0]), "v"(k1[0]) : ); SB();
  asm volatile("v_mfma_f32_16x16x32_bf16 %0, v[216:219], a[116:119], %0" : "+v"(g[0][1][0]) :  : ); SB();
  asm volatile("buffer_load_dwordx4 v[180:183], %0, %1, %2 offen offset:1024 sc1" :  : "v"(vB), "s"(rWS), "s"(2048u) : "memory"); SB();
  asm volatile("v_fma_f32 %0, %0, %1, %2" : "+v"(gt[3]) : "v"(k2[0]), "v"(k1[0]) : ); SB();
  asm volatile("v_mfma_f32_16x16x32_bf16 %0, v[216:219], a[120:123], %0" : "+v"(g[0][2][0]) :  : ); SB();
  asm volatile("v_mul_f32 %0, %0, %1" : "+v"(gt[0]) : "v"(a1[1][0][0]) : ); SB();
  asm volatile("v_mfma_f32_16x16x32_bf16 %0, v[216:219], a[124:127], %0" : "+v"(g[0][3][0]) :  : ); SB();
  asm volatile("v_mul_f32 %0, %0, %1" : "+v"(gt[1]) : "v"(a1[1][0][1]) : ); SB();
  asm volatile("v_mfma_f32_16x16x32_bf16 %0, v[220:223], a[112:115], %0" : "+v"(g[0][0][1]) :  : ); SB();
  asm volatile("buffer_load_dwordx4 v[184:187], %0, %1, %2 offen offset:0 sc1" :  : "v"(vB), "s"(rWS), "s"(67584u) : "memory"); SB();
  asm volatile("v_mul_f32 %0, %0, %1" : "+v"(gt[2]) : "v"(a1[1][0][2]) : ); SB();
  asm volatile("v_mfma_f32_16x16x32_bf16 %0, v[220:223], a[116:119], %0" : "+v"(g[0][1][1]) :  : ); SB();
  asm volatile("v_mul_f32 %0, %0, %1" : "+v"(gt[3]) : "v"(a1[1][0][3]) : ); SB();
  asm volatile("v_mfma_f32_16x16x32_bf16 %0, v[220:223], a[120:123], %0" : "+v"(g[0][2][1]) :  : ); SB();
  asm volatile("v_exp_f32 %0, %1" : "=v"(ge[0]) : "v"(gt[0]) : ); SB();
  asm volatile("v_mfma_f32_16x16x32_bf16 %0, v[220:223], a[124:127], %0" : "+v"(g[0][3][1]) :  : ); SB();
  asm volatile("buffer_load_dwordx4 v[188:191], %0, %1, %2 offen offset:1024 sc1" :  : "v"(vB), "s"(rWS), "s"(67584u) : "memory"); SB();
  asm volatile("s_waitcnt vmcnt(14)" :  :  : "memory"); SB();
  asm volatile("v_mfma_f32_16x16x32_bf16 %0, v[224:227], a[128:131], %0" : "+v"(g[0][0][0]) :  : ); SB();
  asm volatile("v_xor_b32 %0, 393216, %0" : "+v"(vB) :  : "memory"); SB();
  asm volatile("v_exp_f32 %0, %1" : "=v"(ge[1]) : "v"(gt[1]) : ); SB();
  asm volatile("v_mfma_f32_16x16x32_bf16 %0, v[224:227], a[132:135], %0" : "+v"(g[0][1][0]) :  : ); SB();
  asm volatile("v_exp_f32 %0, %1" : "=v"(ge[2]) : "v"(gt[2]) : ); SB();
  asm volatile("s_waitcnt lgkmcnt(0)\n\tv_mfma_f32_16x16x32_bf16 %0, v[224:227], a[136:139], %0" : "+v"(g[0][2][0]) :  : ); SB();
  asm volatile("v_exp_f32 %0, %1" : "=v"(ge[3]) : "v"(gt[3]) : ); SB();
  asm volatile("v_mfma_f32_16x16x32_bf16 %0, v[224:227], a[140:143], %0" : "+v"(g[0][3][0]) :  : ); SB();
  asm volatile("buffer_load_dwordx4 v[192:195], %0, %1, %2 offen offset:0 sc1" :  : "v"(vB), "s"(rWS), "s"(2048u) : "memory"); SB();
  asm volatile("v_fma_f32 %0, %0, %1, %1" : "+v"(ge[0]) : "v"(xd[0]) : ); SB();
  asm volatile("v_mfma_f32_16x16x32_bf16 %0, v[228:231], a[128:131], %0" : "+v"(g[0][0][1]) :  : ); SB();
  asm volatile("v_fma_f32 %0, %0, %1, %1" : "+v"(ge[1]) : "v"(xd[0]) : ); SB();
  asm volatile("v_mfma_f32_16x16x32_bf16 %0, v[228:231], a[132:135], %0" : "+v"(g[0][1][1]) :  : ); SB();
  asm volatile("v_fma_f32 %0, %0, %1, %1" : "+v"(ge[2]) : "v"(xd[0]) : ); SB();
  asm volatile("v_mfma_f32_16x16x32_bf16 %0, v[228:231], a[136:139], %0" : "+v"(g[0][2][1]) :  : ); SB();
  asm volatile("buffer_load_dwordx4 v[196:199], %0, %1, %2 offen offset:1024 sc1" :  : "v"(vB), "s"(rWS), "s"(2048u) : "memory"); SB();
  asm volatile("v_fma_f32 %0, %0, %1, %1" : "+v"(ge[3]) : "v"(xd[0]) : ); SB();
  asm volatile("v_mfma_f32_16x16x32_bf16 %0, v[228:231], a[140:143], %0" : "+v"(g[0][3][1]) :  : ); SB();
  asm volatile("v_rcp_f32 %0, %1" : "=v"(gt[0]) : "v"(ge[0]) : ); SB();
  asm volatile("s_waitcnt vmcnt(14)" :  :  : "memory"); SB();
  asm volatile("v_mfma_f32_16x16x32_bf16 %0, v[232:235], a[144:147], %0" : "+v"(g[0][0][0]) :  : ); SB();
  asm volatile("v_rcp_f32 %0, %1" : "=v"(gt[1]) : "v"(ge[1]) : ); SB();
  asm volatile("v_mfma_f32_16x16x32_bf16 %0, v[232:235], a[148:151], %0" : "+v"(g[0][1][0]) :  : ); SB();
  asm volatile("v_rcp_f32 %0, %1" : "=v"(gt[2]) : "v"(ge[2]) : ); SB();
  asm volatile("v_mfma_f32_16x16x32_bf16 %0, v[232:235], a[152:155], %0" : "+v"(g[0][2][0]) :  : ); SB();
  asm volatile("buffer_load_dwordx4 v[200:203], %0, %1, %2 offen offset:0 sc1" :  : "v"(vB), "s"(rWS), "s"(67584u) : "memory"); SB();
  asm volatile("v_rcp_f32 %0, %1" : "=v"(gt[3]) : "v"(ge[3]) : ); SB();
  asm volatile("v_mfma_f32_16x16x32_bf16 %0, v[232:235], a[156:159], %0" : "+v"(g[0][3][0]) :  : ); SB();
  asm volatile("v_mul_f32 %0, %0, %1" : "+v"(a1[1][0][0]) : "v"(gt[0]) : ); SB();
  asm volatile("v_mfma_f32_16x16x32_bf16 %0, v[236:239], a[144:147], %0" : "+v"(g[0][0][1]) :  : ); SB();
  asm volatile("v_mul_f32 %0, %0, %1" : "+v"(a1[1][0][1]) : "v"(gt[1]) : ); SB();
  asm volatile("v_mfma_f32_16x16x32_bf16 %0, v[236:239], a[148:151], %0" : "+v"(g[0][1][1]) :  : ); SB();
  asm volatile("v_mul_f32 %0, %0, %1" : "+v"(a1[1][0][2]) : "v"(gt[2]) : ); SB();
  asm volatile("v_mfma_f32_16x16x32_bf16 %0, v[236:239], a[152:155], %0" : "+v"(g[0][2][1]) :  : ); SB();
  asm volatile("v_mul_f32 %0, %0, %1" : "+v"(a1[1][0][3]) : "v"(gt[3]) : ); SB();
  asm volatile("v_mfma_f32_16x16x32_bf16 %0, v[236:239], a[156:159], %0" : "+v"(g[0][3][1]) :  : ); SB();
  asm volatile("buffer_load_dwordx4 v[204:207], %0, %1, %2 offen offset:1024 sc1" :  : "v"(vB), "s"(rWS), "s"(67584u) : "memory"); SB();
  asm volatile("s_waitcnt vmcnt(14)" :  :  : "memory"); SB();
  asm volatile("v_mfma_f32_16x16x32_bf16 %0, v[240:243], a[0:3], %0" : "+v"(g[1][0][0]) :  : ); SB();
  asm volatile("s_barrier" :  :  : "memory"); SB();
  asm volatile("v_xor_b32 %0, 131072, %0" : "+v"(vB) :  : "memory"); SB();
  asm volatile("v_mfma_f32_16x16x32_bf16 %0, v[240:243], a[4:7], %0" : "+v"(g[1][1][0]) :  : ); SB();
  asm volatile("buffer_load_dwordx4 v[208:211], %0, %1, %2 offen offset:0 sc1" :  : "v"(vB), "s"(rWS), "s"(2048u) : "memory"); SB();
  asm volatile("v_mul_f32 %0, %1, %1" : "=v"(gt[0]) : "v"(a1[1][1][0]) : ); SB();
  asm volatile("v_mfma_f32_16x16x32_bf16 %0, v[240:243], a[8:11], %0" : "+v"(g[1][2][0]) :  : ); SB();
  asm volatile("ds_write_b128 %0, %1 offset:0" :  : "v"(pw2), "v"(g[0][1][0]) : "memory"); SB();
  asm volatile("v_mfma_f32_16x16x32_bf16 %0, v[240:243], a[12:15], %0" : "+v"(g[1][3][0]) :  : ); SB();
  asm volatile("buffer_load_dwordx4 v[212:215], %0, %1, %2 offen offset:1024 sc1" :  : "v"(vB), "s"(rWS), "s"(2048u) : "memory"); SB();
  asm volatile("v_mul_f32 %0, %1, %1" : "=v"(gt[1]) : "v"(a1[1][1][1]) : ); SB();
  asm volatile("v_mfma_f32_16x16x32_bf16 %0, v[244:247], a[0:3], %0" : "+v"(g[1][0][1]) :  : ); SB();
  asm volatile("v_mul_f32 %0, %1, %1" : "=v"(gt[2]) : "v"(a1[1][1][2]) : ); SB();
  asm volatile("v_mfma_f32_16x16x32_bf16 %0, v[244:247], a[4:7], %0" : "+v"(g[1][1][1]) :  : ); SB();
  asm volatile("ds_write_b128 %0, %1 offset:1024" :  : "v"(pw2), "v"(g[0][1][1]) : "memory"); SB();
  asm volatile("v_mfma_f32_16x16x32_bf16 %0, v[244:247], a[8:11], %0" : "+v"(g[1][2][1]) :  : ); SB();
  asm volatile("buffer_load_dwordx4 v[216:219], %0, %1, %2 offen offset:0 sc1" :  : "v"(vB), "s"(rWS), "s"(67584u) : "memory"); SB();
  asm volatile("v_mul_f32 %0, %1, %1" : "=v"(gt[3]) : "v"(a1[1][1][3]) : ); SB();
  asm volatile("v_mfma_f32_16x16x32_bf16 %0, v[244:247], a[12:15], %0" : "+v"(g[1][3][1]) :  : ); SB();
  asm volatile("v_fma_f32 %0, %0, %1, %2" : "+v"(gt[0]) : "v"(k2[0]), "v"(k1[0]) : ); SB();
  asm volatile("s_waitcnt vmcnt(15)" :  :  : "memory"); SB();
  asm volatile("v_mfma_f32_16x16x32_bf16 %0, v[248:251], a[16:19], %0" : "+v"(g[1][0][0]) :  : ); SB();
  asm volatile("ds_write_b128 %0, %1 offset:2048" :  : "v"(pw2), "v"(g[0][2][0]) : "memory"); SB();
  asm volatile("v_mfma_f32_16x16x32_bf16 %0, v[248:251], a[20:23], %0" : "+v"(g[1][1][0]) :  : ); SB();
  asm volatile("buffer_store_dwordx4 %1, %0, %2, %3 offen offset:2048 sc1\n\ts_nop 1" :  : "v"(vH_), "v"(ho[2]), "s"(rWS), "s"(0u) : "memory"); SB();
  asm volatile("v_fma_f32 %0, %0, %1, %2" : "+v"(gt[1]) : "v"(k2[0]), "v"(k1[0]) : ); SB();
  asm volatile("v_mfma_f32_16x16x32_bf16 %0, v[248:251], a[24:27], %0" : "+v"(g[1][2][0]) :  : ); SB();
  asm volatile("v_fma_f32 %0, %0, %1, %2" : "+v"(gt[2]) : "v"(k2[0]), "v"(k1[0]) : ); SB();
  asm volatile("v_mfma_f32_16x16x32_bf16 %0, v[248:251], a[28:31], %0" : "+v"(g[1][3][0]) :  : ); SB();
  asm volatile("ds_write_b128 %0, %1 offset:3072" :  : "v"(pw2), "v"(g[0][2][1]) : "memory"); SB();
  asm volatile("v_mfma_f32_16x16x32_bf16 %0, v[252:255], a[16:19], %0" : "+v"(g[1][0][1]) :  : ); SB();
  asm volatile("buffer_load_dwordx4 v[220:223], %0, %1, %2 offen offset:1024 sc1" :  : "v"(vB), "s"(rWS), "s"(67584u) : "memory"); SB();
  asm volatile("v_fma_f32 %0, %0, %1, %2" : "+v"(gt[3]) : "v"(k2[0]), "v"(k1[0]) : ); SB();
  asm volatile("v_mfma_f32_16x16x32_bf16 %0, v[252:255], a[20:23], %0" : "+v"(g[1][1][1]) :  : ); SB();
  asm volatile("v_xor_b32 %0, 917504, %0" : "+v"(vB) :  : "memory"); SB();
  asm volatile("v_mul_f32 %0, %0, %1" : "+v"(gt[0]) : "v"(a1[1][1][0]) : ); SB();
  asm volatile("v_mfma_f32_16x16x32_bf16 %0, v[252:255], a[24:27], %0" : "+v"(g[1][2][1]) :  : ); SB();
  asm volatile("ds_write_b128 %0, %1 offset:4096" :  : "v"(pw2), "v"(g[0][3][0]) : "memory"); SB();
  asm volatile("v_mfma_f32_16x16x32_bf16 %0, v[252:255], a[28:31], %0" : "+v"(g[1][3][1]) :  : ); SB();
  asm volatile("buffer_load_dwordx4 v[224:227], %0, %1, %2 offen offset:0 sc1" :  : "v"(vB), "s"(rWS), "s"(4096u) : "memory"); SB();
  ST(28);
  asm volatile("s_waitcnt vmcnt(16)" :  :  : "memory"); SB();
  asm volatile("v_mfma_f32_16x16x32_bf16 %0, v[160:163], a[32:35], %0" : "+v"(g[1][0][0]) :  : ); SB();
  asm volatile("v_mul_f32 %0, %0, %1" : "+v"(gt[1]) : "v"(a1[1][1][1]) : ); SB();
  asm volatile("v_mfma_f32_16x16x32_bf16 %0, v[160:163], a[36:39], %0" : "+v"(g[1][1][0]) :  : ); SB();
  asm volatile("ds_write_b128 %0, %1 offset:5120" :  : "v"(pw2), "v"(g[0][3][1]) : "memory"); SB();
  asm volatile("v_mfma_f32_16x16x32_bf16 %0, v[160:163], a[40:43], %0" : "+v"(g[1][2][0]) :  : ); SB();
  asm volatile("buffer_load_dwordx4 v[228:231], %0, %1, %2 offen offset:1024 sc1" :  : "v"(vB), "s"(rWS), "s"(4096u) : "memory"); SB();
  asm volatile("v_mul_f32 %0, %0, %1" : "+v"(gt[2]) : "v"(a1[1][1][2]) : ); SB();
  asm volatile("v_mfma_f32_16x16x32_bf16 %0, v[160:163], a[44:47], %0" : "+v"(g[1][3][0]) :  : ); SB();
  asm volatile("v_mul_f32 %0, %0, %1" : "+v"(gt[3]) : "v"(a1[1][1][3]) : ); SB();
  asm volatile("v_mfma_f32_16x16x32_bf16 %0, v[164:167], a[32:35], %0" : "+v"(g[1][0][1]) :  : ); SB();
  asm volatile("s_waitcnt lgkmcnt(0)\n\ts_barrier" :  :  : "memory"); SB();
  asm volatile("ds_read_b128 %0, %1 offset:0" : "=&v"(pr[0]) : "v"(pe[0]) : "memory"); SB();
  asm volatile("v_mfma_f32_16x16x32_bf16 %0, v[164:167], a[36:39], %0" : "+v"(g[1][1][1]) :  : ); SB();
  asm volatile("ds_read_b128 %0, %1 offset:1024" : "=&v"(pr[1]) : "v"(pe[0]) : "memory"); SB();
  asm volatile("v_exp_f32 %0, %1" : "=v"(ge[0]) : "v"(gt[0]) : ); SB();
  asm volatile("v_mfma_f32_16x16x32_bf16 %0, v[164:167], a[40:43], %0" : "+v"(g[1][2][1]) :  : ); SB();
  asm volatile("ds_read_b128 %0, %1 offset:2048" : "=&v"(pr[2]) : "v"(pe[1]) : "memory"); SB();
  asm volatile("v_exp_f32 %0, %1" : "=v"(ge[1]) : "v"(gt[1]) : ); SB();
  asm volatile("v_mfma_f32_16x16x32_bf16 %0, v[164:167], a[44:47], %0" : "+v"(g[1][3][1]) :  : ); SB();
  asm volatile("ds_read_b128 %0, %1 offset:3072" : "=&v"(pr[3]) : "v"(pe[1]) : "memory"); SB();
  asm volatile("s_waitcnt vmcnt(15)" :  :  : "memory"); SB();
  asm volatile("v_mfma_f32_16x16x32_bf16 %0, v[168:171], a[48:51], %0" : "+v"(g[1][0][0]) :  : ); SB();
  asm volatile("buffer_load_dwordx4 v[232:235], %0, %1, %2 offen offset:0 sc1" :  : "v"(vB), "s"(rWS), "s"(69632u) : "memory"); SB();
  asm volatile("v_exp_f32 %0, %1" : "=v"(ge[2]) : "v"(gt[2]) : ); SB();
  asm volatile("v_mfma_f32_16x16x32_bf16 %0, v[168:171], a[52:55], %0" : "+v"(g[1][1][0]) :  : ); SB();
  asm volatile("ds_read_b128 %0, %1 offset:4096" : "=&v"(pr[4]) : "v"(pe[2]) : "memory"); SB();
  asm volatile("v_exp_f32 %0, %1" : "=v"(ge[3]) : "v"(gt[3]) : ); SB();
  asm volatile("v_mfma_f32_16x16x32_bf16 %0, v[168:171], a[56:59], %0" : "+v"(g[1][2][0]) :  : ); SB();
  asm volatile("ds_read_b128 %0, %1 offset:5120" : "=&v"(pr[5]) : "v"(pe[2]) : "memory"); SB();
  asm volatile("v_fma_f32 %0, %0, %1, %1" : "+v"(ge[0]) : "v"(xd[0]) : ); SB();
  asm volatile("v_mfma_f32_16x16x32_bf16 %0, v[168:171], a[60:63], %0" : "+v"(g[1][3][0]) :  : ); SB();
  asm volatile("buffer_load_dwordx4 v[236:239], %0, %1, %2 offen offset:1024 sc1" :  : "v"(vB), "s"(rWS), "s"(69632u) : "memory"); SB();
  asm volatile("v_fma_f32 %0, %0, %1, %1" : "+v"(ge[1]) : "v"(xd[0]) : ); SB();
  asm volatile("v_mfma_f32_16x16x32_bf16 %0, v[172:175], a[48:51], %0" : "+v"(g[1][0][1]) :  : ); SB();
  asm volatile("v_xor_b32 %0, 131072, %0" : "+v"(vB) :  : "memory"); SB();
  asm volatile("v_fma_f32 %0, %0, %1, %1" : "+v"(ge[2]) : "v"(xd[0]) : ); SB();
  asm volatile("v_mfma_f32_16x16x32_bf16 %0, v[172:175], a[52:55], %0" : "+v"(g[1][1][1]) :  : ); SB();
  asm volatile("buffer_load_dwordx4 v[240:243], %0, %1, %2 offen offset:0 sc1" :  : "v"(vB), "s"(rWS), "s"(4096u) : "memory"); SB();
  asm volatile("v_fma_f32 %0, %0, %1, %1" : "+v"(ge[3]) : "v"(xd[0]) : ); SB();
  asm volatile("v_mfma_f32_16x16x32_bf16 %0, v[172:175], a[56:59], %0" : "+v"(g[1][2][1]) :  : ); SB();
  asm volatile("v_rcp_f32 %0, %1" : "=v"(gt[0]) : "v"(ge[0]) : ); SB();
  asm volatile("v_mfma_f32_16x16x32_bf16 %0, v[172:175], a[60:63], %0" : "+v"(g[1][3][1]) :  : ); SB();
  asm volatile("buffer_load_dwordx4 v[244:247], %0, %1, %2 offen offset:1024 sc1" :  : "v"(vB), "s"(rWS), "s"(4096u) : "memory"); SB();
  asm volatile("s_waitcnt vmcnt(17)" :  :  : "memory"); SB();
  asm volatile("v_mfma_f32_16x16x32_bf16 %0, v[176:179], a[64:67], %0" : "+v"(g[1][0][0]) :  : ); SB();
  asm volatile("s_waitcnt lgkmcnt(0)" : "+v"(pr[0]), "+v"(pr[1]), "+v"(pr[2]), "+v"(pr[3]), "+v"(pr[4]), "+v"(pr[5]) :  : "memory"); SB();
  asm volatile("v_add_f32 %0, %0, %1" : "+v"(pr[0][0]) : "v"(g[0][0][0][0]) : ); SB();
  asm volatile("v_mfma_f32_16x16x32_bf16 %0, v[176:179], a[68:71], %0" : "+v"(g[1][1][0]) :  : ); SB();
  asm volatile("v_add_f32 %0, %0, %1" : "+v"(pr[0][1]) : "v"(g[0][0][0][1]) : ); SB();
  asm volatile("v_mfma_f32_16x16x32_bf16 %0, v[176:179], a[72:75], %0" : "+v"(g[1][2][0]) :  : ); SB();
  asm volatile("buffer_load_dwordx4 v[248:251], %0, %1, %2 offen offset:0 sc1" :  : "v"(vB), "s"(rWS), "s"(69632u) : "memory"); SB();
  asm volatile("v_add_f32 %0, %0, %1" : "+v"(pr[0][2]) : "v"(g[0][0][0][2]) : ); SB();
  asm volatile("v_mfma_f32_16x16x32_bf16 %0, v[176:179], a[76:79], %0" : "+v"(g[1][3][0]) :  : ); SB();
  asm volatile("v_add_f32 %0, %0, %1" : "+v"(pr[0][3]) : "v"(g[0][0][0][3]) : ); SB();
  asm volatile("v_mfma_f32_16x16x32_bf16 %0, v[180:183], a[64:67], %0" : "+v"(g[1][0][1]) :  : ); SB();
  asm volatile("v_add_f32 %0, %0, %1" : "+v"(pr[1][0]) : "v"(g[0][0][1][0]) : ); SB();
  asm volatile("v_mfma_f32_16x16x32_bf16 %0, v[180:183], a[68:71], %0" : "+v"(g[1][1][1]) :  : ); SB();
  asm volatile("buffer_load_dwordx4 v[252:255], %0, %1, %2 offen offset:1024 sc1" :  : "v"(vB), "s"(rWS), "s"(69632u) : "memory"); SB();
  asm volatile("v_add_f32 %0, %0, %1" : "+v"(pr[1][1]) : "v"(g[0][0][1][1]) : ); SB();
  asm volatile("v_mfma_f32_16x16x32_bf16 %0, v[180:183], a[72:75], %0" : "+v"(g[1][2][1]) :  : ); SB();
  asm volatile("v_xor_b32 %0, 393216, %0" : "+v"(vB) :  : "memory"); SB();
  asm volatile("v_add_f32 %0, %0, %1" : "+v"(pr[1][2]) : "v"(g[0][0][1][2]) : ); SB();
  asm volatile("v_mfma_f32_16x16x32_bf16 %0, v[180:183], a[76:79], %0" : "+v"(g[1][3][1]) :  : ); SB();
  asm volatile("v_add_f32 %0, %0, %1" : "+v"(pr[1][3]) : "v"(g[0][0][1][3]) : ); SB();
  asm volatile("s_waitcnt vmcnt(17)" :  :  : "memory"); SB();
  asm volatile("v_mfma_f32_16x16x32_bf16 %0, v[184:187], a[80:83], %0" : "+v"(g[1][0][0]) :  : ); SB();
  asm volatile("v_add_f32 %0, %0, %1" : "+v"(pr[2][0]) : "v"(pr[4][0]) : ); SB();
  asm volatile("v_mfma_f32_16x16x32_bf16 %0, v[184:187], a[84:87], %0" : "+v"(g[1][1][0]) :  : ); SB();
  asm volatile("buffer_load_dwordx4 v[160:163], %0, %1, %2 offen offset:0 sc1" :  : "v"(vB), "s"(rWS), "s"(4096u) : "memory"); SB();
  asm volatile("v_add_f32 %0, %0, %1" : "+v"(pr[2][1]) : "v"(pr[4][1]) : ); SB();
  asm volatile("v_mfma_f32_16x16x32_bf16 %0, v[184:187], a[88:91], %0" : "+v"(g[1][2][0]) :  : ); SB();
  asm volatile("v_add_f32 %0, %0, %1" : "+v"(pr[2][2]) : "v"(pr[4][2]) : ); SB();
  asm volatile("v_mfma_f32_16x16x32_bf16 %0, v[184:187], a[92:95], %0" : "+v"(g[1][3][0]) :  : ); SB();
  asm volatile("v_add_f32 %0, %0, %1" : "+v"(pr[2][3]) : "v"(pr[4][3]) : ); SB();
  asm volatile("v_mfma_f32_16x16x32_bf16 %0, v[188:191], a[80:83], %0" : "+v"(g[1][0][1]) :  : ); SB();
  asm volatile("buffer_load_dwordx4 v[164:167], %0, %1, %2 offen offset:1024 sc1" :  : "v"(vB), "s"(rWS), "s"(4096u) : "memory"); SB();
  asm volatile("v_add_f32 %0, %0, %1" : "+v"(pr[3][0]) : "v"(pr[5][0]) : ); SB();
  asm volatile("v_mfma_f32_16x16x32_bf16 %0, v[188:191], a[84:87], %0" : "+v"(g[1][1][1]) :  : ); SB();
  asm volatile("v_add_f32 %0, %0, %1" : "+v"(pr[3][1]) : "v"(pr[5][1]) : ); SB();
  asm volatile("v_mfma_f32_16x16x32_bf16 %0, v[188:191], a[88:91], %0" : "+v"(g[1][2][1]) :  : ); SB();
  asm volatile("v_add_f32 %0, %0, %1" : "+v"(pr[3][2]) : "v"(pr[5][2]) : ); SB();
  asm volatile("v_mfma_f32_16x16x32_bf16 %0, v[188:191], a[92:95], %0" : "+v"(g[1][3][1]) :  : ); SB();
  asm volatile("v_add_f32 %0, %0, %1" : "+v"(pr[3][3]) : "v"(pr[5][3]) : ); SB();
  asm volatile("s_waitcnt vmcnt(17)" :  :  : "memory"); SB();
  asm volatile("v_mfma_f32_16x16x32_bf16 %0, v[192:195], a[96:99], %0" : "+v"(g[1][0][0]) :  : ); SB();
  asm volatile("buffer_load_dwordx4 v[168:171], %0, %1, %2 offen offset:0 sc1" :  : "v"(vB), "s"(rWS), "s"(69632u) : "memory"); SB();
  asm volatile("v_add_f32 %0, %0, %1" : "+v"(pr[0][0]) : "v"(pr[2][0]) : ); SB();
  asm volatile("v_mfma_f32_16x16x32_bf16 %0, v[192:195], a[100:103], %0" : "+v"(g[1][1][0]) :  : ); SB();
  asm volatile("v_add_f32 %0, %0, %1" : "+v"(pr[0][1]) : "v"(pr[2][1]) : ); SB();
  asm volatile("v_mfma_f32_16x16x32_bf16 %0, v[192:195], a[104:107], %0" : "+v"(g[1][2][0]) :  : ); SB();
  asm volatile("v_add_f32 %0, %0, %1" : "+v"(pr[0][2]) : "v"(pr[2][2]) : ); SB();
  asm volatile("v_mfma_f32_16x16x32_bf16 %0, v[192:195], a[108:111], %0" : "+v"(g[1][3][0]) :  : ); SB();
  asm volatile("buffer_load_dwordx4 v[172:175], %0, %1, %2 offen offset:1024 sc1" :  : "v"(vB), "s"(rWS), "s"(69632u) : "memory"); SB();
  asm volatile("v_add_f32 %0, %0, %1" : "+v"(pr[0][3]) : "v"(pr[2][3]) : ); SB();
  asm volatile("v_mfma_f32_16x16x32_bf16 %0, v[196:199], a[96:99], %0" : "+v"(g[1][0][1]) :  : ); SB();
  asm volatile("v_xor_b32 %0, 131072, %0" : "+v"(vB) :  : "memory"); SB();
  asm volatile("v_add_f32 %0, %0, %1" : "+v"(pr[1][0]) : "v"(pr[3][0]) : ); SB();
  asm volatile("v_mfma_f32_16x16x32_bf16 %0, v[196:199], a[100:103], %0" : "+v"(g[1][1][1]) :  : ); SB();
  asm volatile("v_add_f32 %0, %0, %1" : "+v"(pr[1][1]) : "v"(pr[3][1]) : ); SB();
  asm volatile("v_mfma_f32_16x16x32_bf16 %0, v[196:199], a[104:107], %0" : "+v"(g[1][2][1]) :  : ); SB();
  asm volatile("v_add_f32 %0, %0, %1" : "+v"(pr[1][2]) : "v"(pr[3][2]) : ); SB();
  asm volatile("v_mfma_f32_16x16x32_bf16 %0, v[196:199], a[108:111], %0" : "+v"(g[1][3][1]) :  : ); SB();
  asm volatile("v_add_f32 %0, %0, %1" : "+v"(pr[1][3]) : "v"(pr[3][3]) : ); SB();
  asm volatile("s_waitcnt vmcnt(17)" :  :  : "memory"); SB();
  asm volatile("v_mfma_f32_16x16x32_bf16 %0, v[200:203], a[112:115], %0" : "+v"(g[1][0][0]) :  : ); SB();
  asm volatile("v_mul_f32 %0, %0, %1" : "+v"(pr[0][0]) : "v"(a1[0][0][0]) : ); SB();
  asm volatile("v_mfma_f32_16x16x32_bf16 %0, v[200:203], a[116:119], %0" : "+v"(g[1][1][0]) :  : ); SB();
  asm volatile("v_mul_f32 %0, %0, %1" : "+v"(pr[0][1]) : "v"(a1[0][0][1]) : ); SB();
  asm volatile("v_mfma_f32_16x16x32_bf16 %0, v[200:203], a[120:123], %0" : "+v"(g[1][2][0]) :  : ); SB();
  asm volatile("v_mul_f32 %0, %0, %1" : "+v"(pr[0][2]) : "v"(a1[0][0][2]) : ); SB();
  asm volatile("v_mfma_f32_16x16x32_bf16 %0, v[200:203], a[124:127], %0" : "+v"(g[1][3][0]) :  : ); SB();
  asm volatile("v_mul_f32 %0, %0, %1" : "+v"(pr[0][3]) : "v"(a1[0][0][3]) : ); SB();
  asm volatile("v_mfma_f32_16x16x32_bf16 %0, v[204:207], a[112:115], %0" : "+v"(g[1][0][1]) :  : ); SB();
  asm volatile("v_mul_f32 %0, %0, %1" : "+v"(pr[1][0]) : "v"(a1[0][1][0]) : ); SB();
  asm volatile("v_mfma_f32_16x16x32_bf16 %0, v[204:207], a[116:119], %0" : "+v"(g[1][1][1]) :  : ); SB();
  asm volatile("v_mul_f32 %0, %0, %1" : "+v"(pr[1][1]) : "v"(a1[0][1][1]) : ); SB();
  asm volatile("v_mfma_f32_16x16x32_bf16 %0, v[204:207], a[120:123], %0" : "+v"(g[1][2][1]) :  : ); SB();
  asm volatile("v_mul_f32 %0, %0, %1" : "+v"(pr[1][2]) : "v"(a1[0][1][2]) : ); SB();
  asm volatile("v_mfma_f32_16x16x32_bf16 %0, v[204:207], a[124:127], %0" : "+v"(g[1][3][1]) :  : ); SB();
  asm volatile("buffer_load_dwordx4 v[176:179], %0, %1, %2 offen offset:0 sc1" :  : "v"(vB), "s"(rWS), "s"(4096u) : "memory"); SB();
  asm volatile("s_waitcnt vmcnt(16)" :  :  : "memory"); SB();
  asm volatile("v_mfma_f32_16x16x32_bf16 %0, v[208:211], a[128:131], %0" : "+v"(g[1][0][0]) :  : ); SB();
  asm volatile("v_mul_f32 %0, %0, %1" : "+v"(pr[1][3]) : "v"(a1[0][1][3]) : ); SB();
  asm volatile("v_mfma_f32_16x16x32_bf16 %0, v[208:211], a[132:135], %0" : "+v"(g[1][1][0]) :  : ); SB();
  asm volatile("v_cvt_pk_bf16_f32 %0, %1, %2" : "=v"(ho[0][0]) : "v"(pr[0][0]), "v"(pr[0][1]) : ); SB();
  asm volatile("v_mfma_f32_16x16x32_bf16 %0, v[208:211], a[136:139], %0" : "+v"(g[1][2][0]) :  : ); SB();
  asm volatile("buffer_load_dwordx4 v[180:183], %0, %1, %2 offen offset:1024 sc1" :  : "v"(vB), "s"(rWS), "s"(4096u) : "memory"); SB();
  asm volatile("v_cvt_pk_bf16_f32 %0, %1, %2" : "=v"(ho[0][1]) : "v"(pr[0][2]), "v"(pr[0][3]) : ); SB();
  asm volatile("v_mfma_f32_16x16x32_bf16 %0, v[208:211], a[140:143], %0" : "+v"(g[1][3][0]) :  : ); SB();
  asm volatile("v_cvt_pk_bf16_f32 %0, %1, %2" : "=v"(ho[0][2]) : "v"(pr[1][0]), "v"(pr[1][1]) : ); SB();
  asm volatile("v_mfma_f32_16x16x32_bf16 %0, v[212:215], a[128:131], %0" : "+v"(g[1][0][1]) :  : ); SB();
  asm volatile("v_cvt_pk_bf16_f32 %0, %1, %2" : "=v"(ho[0][3]) : "v"(pr[1][2]), "v"(pr[1][3]) : ); SB();
  asm volatile("v_mfma_f32_16x16x32_bf16 %0, v[212:215], a[132:135], %0" : "+v"(g[1][1][1]) :  : ); SB();
  asm volatile("v_rcp_f32 %0, %1" : "=v"(gt[1]) : "v"(ge[1]) : ); SB();
  asm volatile("v_mfma_f32_16x16x32_bf16 %0, v[212:215], a[136:139], %0" : "+v"(g[1][2][1]) :  : ); SB();
  asm volatile("buffer_store_dwordx4 %1, %0, %2, %3 offen offset:0 sc1\n\ts_nop 1" :  : "v"(vH_), "v"(ho[0]), "s"(rWS), "s"(0u) : "memory"); SB();
  asm volatile("v_rcp_f32 %0, %1" : "=v"(gt[2]) : "v"(ge[2]) : ); SB();
  asm volatile("v_mfma_f32_16x16x32_bf16 %0, v[212:215], a[140:143], %0" : "+v"(g[1][3][1]) :  : ); SB();
  asm volatile("v_rcp_f32 %0, %1" : "=v"(gt[3]) : "v"(ge[3]) : ); SB();
  asm volatile("s_waitcnt vmcnt(15)" :  :  : "memory"); SB();
  asm volatile("v_mfma_f32_16x16x32_bf16 %0, v[216:219], a[144:147], %0" : "+v"(g[1][0][0]) :  : ); SB();
  asm volatile("v_mul_f32 %0, %0, %1" : "+v"(a1[1][1][0]) : "v"(gt[0]) : ); SB();
  asm volatile("v_mfma_f32_16x16x32_bf16 %0, v[216:219], a[148:151], %0" : "+v"(g[1][1][0]) :  : ); SB();
  asm volatile("buffer_load_dwordx4 v[184:187], %0, %1, %2 offen offset:0 sc1" :  : "v"(vB), "s"(rWS), "s"(69632u) : "memory"); SB();
  asm volatile("v_mul_f32 %0, %0, %1" : "+v"(a1[1][1][1]) : "v"(gt[1]) : ); SB();
  asm volatile("v_mfma_f32_16x16x32_bf16 %0, v[216:219], a[152:155], %0" : "+v"(g[1][2][0]) :  : ); SB();
  asm volatile("v_mul_f32 %0, %0, %1" : "+v"(a1[1][1][2]) : "v"(gt[2]) : ); SB();
  asm volatile("v_mfma_f32_16x16x32_bf16 %0, v[216:219], a[156:159], %0" : "+v"(g[1][3][0]) :  : ); SB();
  asm volatile("v_mul_f32 %0, %0, %1" : "+v"(a1[1][1][3]) : "v"(gt[3]) : ); SB();
  asm volatile("v_mfma_f32_16x16x32_bf16 %0, v[220:223], a[144:147], %0" : "+v"(g[1][0][1]) :  : ); SB();
  asm volatile("v_mul_f32 %0, %1, %1" : "=v"(gt[0]) : "v"(a1[1][2][0]) : ); SB();
  asm volatile("v_mfma_f32_16x16x32_bf16 %0, v[220:223], a[148:151], %0" : "+v"(g[1][1][1]) :  : ); SB();
  asm volatile("buffer_load_dwordx4 v[188:191], %0, %1, %2 offen offset:1024 sc1" :  : "v"(vB), "s"(rWS), "s"(69632u) : "memory"); SB();
  asm volatile("v_mul_f32 %0, %1, %1" : "=v"(gt[1]) : "v"(a1[1][2][1]) : ); SB();
  asm volatile("v_mfma_f32_16x16x32_bf16 %0, v[220:223], a[152:155], %0" : "+v"(g[1][2][1]) :  : ); SB();
  asm volatile("v_xor_b32 %0, 917504, %0" : "+v"(vB) :  : "memory"); SB();
  asm volatile("v_mul_f32 %0, %1, %1" : "=v"(gt[2]) : "v"(a1[1][2][2]) : ); SB();
  asm volatile("v_mfma_f32_16x16x32_bf16 %0, v[220:223], a[156:159], %0" : "+v"(g[1][3][1]) :  : ); SB();
  asm volatile("v_mul_f32 %0, %1, %1" : "=v"(gt[3]) : "v"(a1[1][2][3]) : ); SB();
  ST(29);
  asm volatile("s_waitcnt vmcnt(15)" :  :  : "memory"); SB();
  asm volatile("v_mfma_f32_16x16x32_bf16 %0, v[224:227], a[160:163], %1" : "=&v"(g[0][0][0]) : "v"(bs4) : ); SB();
  asm volatile("s_barrier" :  :  : "memory"); SB();
  asm volatile("buffer_load_dwordx4 v[192:195], %0, %1, %2 offen offset:0 sc1" :  : "v"(vB), "s"(rWS), "s"(4096u) : "memory"); SB();
  asm volatile("v_mfma_f32_16x16x32_bf16 %0, v[224:227], a[164:167], 0" : "=&v"(g[0][1][0]) :  : ); SB();
  asm volatile("v_fma_f32 %0, %0, %1, %2" : "+v"(gt[0]) : "v"(k2[0]), "v"(k1[0]) : ); SB();
  asm volatile("v_mfma_f32_16x16x32_bf16 %0, v[224:227], a[168:171], 0" : "=&v"(g[0][2][0]) :  : ); SB();
  asm volatile("ds_write_b128 %0, %1 offset:0" :  : "v"(pw2), "v"(g[1][1][0]) : "memory"); SB();
  asm volatile("v_mfma_f32_16x16x32_bf16 %0, v[224:227], a[172:175], 0" : "=&v"(g[0][3][0]) :  : ); SB();
  asm volatile("buffer_load_dwordx4 v[196:199], %0, %1, %2 offen offset:1024 sc1" :  : "v"(vB), "s"(rWS), "s"(4096u) : "memory"); SB();
  asm volatile("v_fma_f32 %0, %0, %1, %2" : "+v"(gt[1]) : "v"(k2[0]), "v"(k1[0]) : ); SB();
  asm volatile("v_mfma_f32_16x16x32_bf16 %0, v[228:231], a[160:163], %1" : "=&v"(g[0][0][1]) : "v"(bs4) : ); SB();
  asm volatile("v_fma_f32 %0, %0, %1, %2" : "+v"(gt[2]) : "v"(k2[0]), "v"(k1[0]) : ); SB();
  asm volatile("v_mfma_f32_16x16x32_bf16 %0, v[228:231], a[164:167], 0" : "=&v"(g[0][1][1]) :  : ); SB();
  asm volatile("ds_write_b128 %0, %1 offset:1024" :  : "v"(pw2), "v"(g[1][1][1]) : "memory"); SB();
  asm volatile("v_mfma_f32_16x16x32_bf16 %0, v[228:231], a[168:171], 0" : "=&v"(g[0][2][1]) :  : ); SB();
  asm volatile("buffer_load_dwordx4 v[200:203], %0, %1, %2 offen offset:0 sc1" :  : "v"(vB), "s"(rWS), "s"(69632u) : "memory"); SB();
  asm volatile("v_fma_f32 %0, %0, %1, %2" : "+v"(gt[3]) : "v"(k2[0]), "v"(k1[0]) : ); SB();
  asm volatile("v_mfma_f32_16x16x32_bf16 %0, v[228:231], a[172:175], 0" : "=&v"(g[0][3][1]) :  : ); SB();
  asm volatile("buffer_load_dwordx4 v[204:207], %0, %1, %2 offen offset:1024 sc1" :  : "v"(vB), "s"(rWS), "s"(69632u) : "memory"); SB();
  asm volatile("s_waitcnt vmcnt(17)" :  :  : "memory"); SB();
  asm volatile("v_mfma_f32_16x16x32_bf16 %0, v[232:235], a[176:179], %0" : "+v"(g[0][0][0]) :  : ); SB();
  asm volatile("ds_write_b128 %0, %1 offset:2048" :  : "v"(pw2), "v"(g[1][2][0]) : "memory"); SB();
  asm volatile("v_mfma_f32_16x16x32_bf16 %0, v[232:235], a[180:183], %0" : "+v"(g[0][1][0]) :  : ); SB();
  asm volatile("v_xor_b32 %0, 131072, %0" : "+v"(vB) :  : "memory"); SB();
  asm volatile("v_mul_f32 %0, %0, %1" : "+v"(gt[0]) : "v"(a1[1][2][0]) : ); SB();
  asm volatile("v_mfma_f32_16x16x32_bf16 %0, v[232:235], a[184:187], %0" : "+v"(g[0][2][0]) :  : ); SB();
  asm volatile("buffer_load_dwordx4 v[208:211], %0, %1, %2 offen offset:0 sc1" :  : "v"(vB), "s"(rWS), "s"(4096u) : "memory"); SB();
  asm volatile("v_mul_f32 %0, %0, %1" : "+v"(gt[1]) : "v"(a1[1][2][1]) : ); SB();
  asm volatile("v_mfma_f32_16x16x32_bf16 %0, v[232:235], a[188:191], %0" : "+v"(g[0][3][0]) :  : ); SB();
  asm volatile("ds_write_b128 %0, %1 offset:3072" :  : "v"(pw2), "v"(g[1][2][1]) : "memory"); SB();
  asm volatile("v_mfma_f32_16x16x32_bf16 %0, v[236:239], a[176:179], %0" : "+v"(g[0][0][1]) :  : ); SB();
  asm volatile("buffer_load_dwordx4 v[212:215], %0, %1, %2 offen offset:1024 sc1" :  : "v"(vB), "s"(rWS), "s"(4096u) : "memory"); SB();
  asm volatile("v_mul_f32 %0, %0, %1" : "+v"(gt[2]) : "v"(a1[1][2][2]) : ); SB();
  asm volatile("v_mfma_f32_16x16x32_bf16 %0, v[236:239], a[180:183], %0" : "+v"(g[0][1][1]) :  : ); SB();
  asm volatile("v_mul_f32 %0, %0, %1" : "+v"(gt[3]) : "v"(a1[1][2][3]) : ); SB();
  asm volatile("v_mfma_f32_16x16x32_bf16 %0, v[236:239], a[184:187], %0" : "+v"(g[0][2][1]) :  : ); SB();
  asm volatile("ds_write_b128 %0, %1 offset:4096" :  : "v"(pw2), "v"(g[1][3][0]) : "memory"); SB();
  asm volatile("v_mfma_f32_16x16x32_bf16 %0, v[236:239], a[188:191], %0" : "+v"(g[0][3][1]) :  : ); SB();
  asm volatile("buffer_load_dwordx4 v[216:219], %0, %1, %2 offen offset:0 sc1" :  : "v"(vB), "s"(rWS), "s"(69632u) : "memory"); SB();
  asm volatile("s_waitcnt vmcnt(18)" :  :  : "memory"); SB();
  asm volatile("v_mfma_f32_16x16x32_bf16 %0, v[240:243], a[192:195], %0" : "+v"(g[0][0][0]) :  : ); SB();
  asm volatile("v_exp_f32 %0, %1" : "=v"(ge[0]) : "v"(gt[0]) : ); SB();
  asm volatile("v_mfma_f32_16x16x32_bf16 %0, v[240:243], a[196:199], %0" : "+v"(g[0][1][0]) :  : ); SB();
  asm volatile("ds_write_b128 %0, %1 offset:5120" :  : "v"(pw2), "v"(g[1][3][1]) : "memory"); SB();
  asm volatile("v_mfma_f32_16x16x32_bf16 %0, v[240:243], a[200:203], %0" : "+v"(g[0][2][0]) :  : ); SB();
  asm volatile("buffer_load_dwordx4 v[220:223], %0, %1, %2 offen offset:1024 sc1" :  : "v"(vB), "s"(rWS), "s"(69632u) : "memory"); SB();
  asm volatile("v_exp_f32 %0, %1" : "=v"(ge[1]) : "v"(gt[1]) : ); SB();
  asm volatile("v_mfma_f32_16x16x32_bf16 %0, v[240:243], a[204:207], %0" : "+v"(g[0][3][0]) :  : ); SB();
  asm volatile("v_xor_b32 %0, 393216, %0" : "+v"(vB) :  : "memory"); SB();
  asm volatile("v_exp_f32 %0, %1" : "=v"(ge[2]) : "v"(gt[2]) : ); SB();
  asm volatile("v_mfma_f32_16x16x32_bf16 %0, v[244:247], a[192:195], %0" : "+v"(g[0][0][1]) :  : ); SB();
  asm volatile("s_waitcnt lgkmcnt(0)\n\ts_barrier" :  :  : "memory"); SB();
  asm volatile("ds_read_b128 %0, %1 offset:0" : "=&v"(pr[0]) : "v"(pe[0]) : "memory"); SB();
  asm volatile("v_mfma_f32_16x16x32_bf16 %0, v[244:247], a[196:199], %0" : "+v"(g[0][1][1]) :  : ); SB();
  asm volatile("ds_read_b128 %0, %1 offset:1024" : "=&v"(pr[1]) : "v"(pe[0]) : "memory"); SB();
  asm volatile("v_exp_f32 %0, %1" : "=v"(ge[3]) : "v"(gt[3]) : ); SB();
  asm volatile("v_mfma_f32_16x16x32_bf16 %0, v[244:247], a[200:203], %0" : "+v"(g[0][2][1]) :  : ); SB();
  asm volatile("ds_read_b128 %0, %1 offset:2048" : "=&v"(pr[2]) : "v"(pe[1]) : "memory"); SB();
  asm volatile("v_fma_f32 %0, %0, %1, %1" : "+v"(ge[0]) : "v"(xd[0]) : ); SB();
  asm volatile("v_mfma_f32_16x16x32_bf16 %0, v[244:247], a[204:207], %0" : "+v"(g[0][3][1]) :  : ); SB();
  asm volatile("ds_read_b128 %0, %1 offset:3072" : "=&v"(pr[3]) : "v"(pe[1]) : "memory"); SB();
  asm volatile("s_waitcnt vmcnt(17)" :  :  : "memory"); SB();
  asm volatile("v_mfma_f32_16x16x32_bf16 %0, v[248:251], a[208:211], %0" : "+v"(g[0][0][0]) :  : ); SB();
  asm volatile("ds_read_b128 %0, %1 offset:4096" : "=&v"(pr[4]) : "v"(pe[2]) : "memory"); SB();
  asm volatile("v_fma_f32 %0, %0, %1, %1" : "+v"(ge[1]) : "v"(xd[0]) : ); SB();
  asm volatile("v_mfma_f32_16x16x32_bf16 %0, v[248:251], a[212:215], %0" : "+v"(g[0][1][0]) :  : ); SB();
  asm volatile("ds_read_b128 %0, %1 offset:5120" : "=&v"(pr[5]) : "v"(pe[2]) : "memory"); SB();
  asm volatile("v_fma_f32 %0, %0, %1, %1" : "+v"(ge[2]) : "v"(xd[0]) : ); SB();
  asm volatile("v_mfma_f32_16x16x32_bf16 %0, v[248:251], a[216:219], %0" : "+v"(g[0][2][0]) :  : ); SB();
  asm volatile("buffer_load_dwordx4 v[224:227], %0, %1, %2 offen offset:0 sc1" :  : "v"(vB), "s"(rWS), "s"(4096u) : "memory"); SB();
  asm volatile("v_fma_f32 %0, %0, %1, %1" : "+v"(ge[3]) : "v"(xd[0]) : ); SB();
  asm volatile("v_mfma_f32_16x16x32_bf16 %0, v[248:251], a[220:223], %0" : "+v"(g[0][3][0]) :  : ); SB();
  asm volatile("buffer_load_dwordx4 v[228:231], %0, %1, %2 offen offset:1024 sc1" :  : "v"(vB), "s"(rWS), "s"(4096u) : "memory"); SB();
  asm volatile("v_rcp_f32 %0, %1" : "=v"(gt[0]) : "v"(ge[0]) : ); SB();
  asm volatile("v_mfma_f32_16x16x32_bf16 %0, v[252:255], a[208:211], %0" : "+v"(g[0][0][1]) :  : ); SB();
  asm volatile("v_rcp_f32 %0, %1" : "=v"(gt[1]) : "v"(ge[1]) : ); SB();
  asm volatile("v_mfma_f32_16x16x32_bf16 %0, v[252:255], a[212:215], %0" : "+v"(g[0][1][1]) :  : ); SB();
  asm volatile("v_rcp_f32 %0, %1" : "=v"(gt[2]) : "v"(ge[2]) : ); SB();
  asm volatile("v_mfma_f32_16x16x32_bf16 %0, v[252:255], a[216:219], %0" : "+v"(g[0][2][1]) :  : ); SB();
  asm volatile("buffer_load_dwordx4 v[232:235], %0, %1, %2 offen offset:0 sc1" :  : "v"(vB), "s"(rWS), "s"(69632u) : "memory"); SB();
  asm volatile("v_rcp_f32 %0, %1" : "=v"(gt[3]) : "v"(ge[3]) : ); SB();
  asm volatile("v_mfma_f32_16x16x32_bf16 %0, v[252:255], a[220:223], %0" : "+v"(g[0][3][1]) :  : ); SB();
  asm volatile("s_waitcnt lgkmcnt(0)" : "+v"(pr[0]), "+v"(pr[1]), "+v"(pr[2]), "+v"(pr[3]), "+v"(pr[4]), "+v"(pr[5]) :  : "memory"); SB();
  asm volatile("v_add_f32 %0, %0, %1" : "+v"(pr[0][0]) : "v"(g[1][0][0][0]) : ); SB();
  asm volatile("s_waitcnt vmcnt(18)" :  :  : "memory"); SB();
  asm volatile("v_mfma_f32_16x16x32_bf16 %0, v[160:163], a[224:227], %0" : "+v"(g[0][0][0]) :  : ); SB();
  asm volatile("v_add_f32 %0, %0, %1" : "+v"(pr[0][1]) : "v"(g[1][0][0][1]) : ); SB();
  asm volatile("v_mfma_f32_16x16x32_bf16 %0, v[160:163], a[228:231], %0" : "+v"(g[0][1][0]) :  : ); SB();
  asm volatile("v_add_f32 %0, %0, %1" : "+v"(pr[0][2]) : "v"(g[1][0][0][2]) : ); SB();
  asm volatile("v_mfma_f32_16x16x32_bf16 %0, v[160:163], a[232:235], %0" : "+v"(g[0][2][0]) :  : ); SB();
  asm volatile("v_add_f32 %0, %0, %1" : "+v"(pr[0][3]) : "v"(g[1][0][0][3]) : ); SB();
  asm volatile("v_mfma_f32_16x16x32_bf16 %0, v[160:163], a[236:239], %0" : "+v"(g[0][3][0]) :  : ); SB();
  asm volatile("v_add_f32 %0, %0, %1" : "+v"(pr[1][0]) : "v"(g[1][0][1][0]) : ); SB();
  asm volatile("v_mfma_f32_16x16x32_bf16 %0, v[164:167], a[224:227], %0" : "+v"(g[0][0][1]) :  : ); SB();
  asm volatile("v_add_f32 %0, %0, %1" : "+v"(pr[1][1]) : "v"(g[1][0][1][1]) : ); SB();
  asm volatile("v_mfma_f32_16x16x32_bf16 %0, v[164:167], a[228:231], %0" : "+v"(g[0][1][1]) :  : ); SB();
  asm volatile("v_add_f32 %0, %0, %1" : "+v"(pr[1][2]) : "v"(g[1][0][1][2]) : ); SB();
  asm volatile("v_mfma_f32_16x16x32_bf16 %0, v[164:167], a[232:235], %0" : "+v"(g[0][2][1]) :  : ); SB();
  asm volatile("buffer_load_dwordx4 v[236:239], %0, %1, %2 offen offset:1024 sc1" :  : "v"(vB), "s"(rWS), "s"(69632u) : "memory"); SB();
  asm volatile("v_add_f32 %0, %0, %1" : "+v"(pr[1][3]) : "v"(g[1][0][1][3]) : ); SB();
  asm volatile("v_mfma_f32_16x16x32_bf16 %0, v[164:167], a[236:239], %0" : "+v"(g[0][3][1]) :  : ); SB();
  asm volatile("v_xor_b32 %0, 131072, %0" : "+v"(vB) :  : "memory"); SB();
  asm volatile("s_waitcnt vmcnt(17)" :  :  : "memory"); SB();
  asm volatile("v_mfma_f32_16x16x32_bf16 %0, v[168:171], a[240:243], %0" : "+v"(g[0][0][0]) :  : ); SB();
  asm volatile("v_add_f32 %0, %0, %1" : "+v"(pr[2][0]) : "v"(pr[4][0]) : ); SB();
  asm volatile("v_mfma_f32_16x16x32_bf16 %0, v[168:171], a[244:247], %0" : "+v"(g[0][1][0]) :  : ); SB();
  asm volatile("v_add_f32 %0, %0, %1" : "+v"(pr[2][1]) : "v"(pr[4][1]) : ); SB();
  asm volatile("v_mfma_f32_16x16x32_bf16 %0, v[168:171], a[248:251], %0" : "+v"(g[0][2][0]) :  : ); SB();
  asm volatile("buffer_load_dwordx4 v[240:243], %0, %1, %2 offen offset:0 sc1" :  : "v"(vB), "s"(rWS), "s"(4096u) : "memory"); SB();
  asm volatile("v_add_f32 %0, %0, %1" : "+v"(pr[2][2]) : "v"(pr[4][2]) : ); SB();
  asm volatile("v_mfma_f32_16x16x32_bf16 %0, v[168:171], a[252:255], %0" : "+v"(g[0][3][0]) :  : ); SB();
  asm volatile("v_add_f32 %0, %0, %1" : "+v"(pr[2][3]) : "v"(pr[4][3]) : ); SB();
  asm volatile("v_mfma_f32_16x16x32_bf16 %0, v[172:175], a[240:243], %0" : "+v"(g[0][0][1]) :  : ); SB();
  asm volatile("v_add_f32 %0, %0, %1" : "+v"(pr[3][0]) : "v"(pr[5][0]) : ); SB();
  asm volatile("v_mfma_f32_16x16x32_bf16 %0, v[172:175], a[244:247], %0" : "+v"(g[0][1][1]) :  : ); SB();
  asm volatile("v_add_f32 %0, %0, %1" : "+v"(pr[3][1]) : "v"(pr[5][1]) : ); SB();
  asm volatile("v_mfma_f32_16x16x32_bf16 %0, v[172:175], a[248:251], %0" : "+v"(g[0][2][1]) :  : ); SB();
  asm volatile("buffer_load_dwordx4 v[244:247], %0, %1, %2 offen offset:1024 sc1" :  : "v"(vB), "s"(rWS), "s"(4096u) : "memory"); SB();
  asm volatile("v_add_f32 %0, %0, %1" : "+v"(pr[3][2]) : "v"(pr[5][2]) : ); SB();
  asm volatile("v_mfma_f32_16x16x32_bf16 %0, v[172:175], a[252:255], %0" : "+v"(g[0][3][1]) :  : ); SB();
  asm volatile("v_add_f32 %0, %0, %1" : "+v"(pr[3][3]) : "v"(pr[5][3]) : ); SB();
  asm volatile("s_waitcnt vmcnt(17)" :  :  : "memory"); SB();
  asm volatile("v_mfma_f32_16x16x32_bf16 %0, v[176:179], a[0:3], %0" : "+v"(g[0][0][0]) :  : ); SB();
  asm volatile("v_add_f32 %0, %0, %1" : "+v"(pr[0][0]) : "v"(pr[2][0]) : ); SB();
  asm volatile("v_mfma_f32_16x16x32_bf16 %0, v[176:179], a[4:7], %0" : "+v"(g[0][1][0]) :  : ); SB();
  asm volatile("s_add_u32 m0, %0, 60032" :  : "s"(mU) : "memory", "scc"); SB();
  asm volatile("v_add_f32 %0, %0, %1" : "+v"(pr[0][1]) : "v"(pr[2][1]) : ); SB();
  asm volatile("v_mfma_f32_16x16x32_bf16 %0, v[176:179], a[8:11], %0" : "+v"(g[0][2][0]) :  : ); SB();
  asm volatile("buffer_load_dwordx4 %0, %1, %2 offen sc1 lds" :  : "v"(vF), "s"(rWS), "s"(W_KV) : "memory"); SB();
  asm volatile("v_add_f32 %0, %0, %1" : "+v"(pr[0][2]) : "v"(pr[2][2]) : ); SB();
  asm volatile("v_mfma_f32_16x16x32_bf16 %0, v[176:179], a[12:15], %0" : "+v"(g[0][3][0]) :  : ); SB();
  asm volatile("v_add_f32 %0, %0, %1" : "+v"(pr[0][3]) : "v"(pr[2][3]) : ); SB();
  asm volatile("v_mfma_f32_16x16x32_bf16 %0, v[180:183], a[0:3], %0" : "+v"(g[0][0][1]) :  : ); SB();
  asm volatile("v_add_f32 %0, %0, %1" : "+v"(pr[1][0]) : "v"(pr[3][0]) : ); SB();
  asm volatile("v_mfma_f32_16x16x32_bf16 %0, v[180:183], a[4:7], %0" : "+v"(g[0][1][1]) :  : ); SB();
  asm volatile("s_add_u32 m0, %0, 72960" :  : "s"(mU) : "memory", "scc"); SB();
  asm volatile("v_add_f32 %0, %0, %1" : "+v"(pr[1][1]) : "v"(pr[3][1]) : ); SB();
  asm volatile("v_mfma_f32_16x16x32_bf16 %0, v[180:183], a[8:11], %0" : "+v"(g[0][2][1]) :  : ); SB();
  asm volatile("buffer_load_dwordx4 %0, %1, %2 offen sc1 lds" :  : "v"(vF), "s"(rWS), "s"(W_KV + 4096) : "memory"); SB();
  asm volatile("v_add_f32 %0, %0, %1" : "+v"(pr[1][2]) : "v"(pr[3][2]) : ); SB();
  asm volatile("v_mfma_f32_16x16x32_bf16 %0, v[180:183], a[12:15], %0" : "+v"(g[0][3][1]) :  : ); SB();
  asm volatile("v_add_f32 %0, %0, %1" : "+v"(pr[1][3]) : "v"(pr[3][3]) : ); SB();
  asm volatile("s_waitcnt vmcnt(16)" :  :  : "memory"); SB();
  asm volatile("v_mfma_f32_16x16x32_bf16 %0, v[184:187], a[16:19], %0" : "+v"(g[0][0][0]) :  : ); SB();
  asm volatile("buffer_load_dwordx4 v[248:251], %0, %1, %2 offen offset:0 sc1" :  : "v"(vB), "s"(rWS), "s"(69632u) : "memory"); SB();
  asm volatile("v_mul_f32 %0, %0, %1" : "+v"(pr[0][0]) : "v"(a1[0][2][0]) : ); SB();
  asm volatile("v_mfma_f32_16x16x32_bf16 %0, v[184:187], a[20:23], %0" : "+v"(g[0][1][0]) :  : ); SB();
  asm volatile("v_mul_f32 %0, %0, %1" : "+v"(pr[0][1]) : "v"(a1[0][2][1]) : ); SB();
  asm volatile("v_mfma_f32_16x16x32_bf16 %0, v[184:187], a[24:27], %0" : "+v"(g[0][2][0]) :  : ); SB();
  asm volatile("v_mul_f32 %0, %0, %1" : "+v"(pr[0][2]) : "v"(a1[0][2][2]) : ); SB();
  asm volatile("v_mfma_f32_16x16x32_bf16 %0, v[184:187], a[28:31], %0" : "+v"(g[0][3][0]) :  : ); SB();
  asm volatile("v_mul_f32 %0, %0, %1" : "+v"(pr[0][3]) : "v"(a1[0][2][3]) : ); SB();
  asm volatile("v_mfma_f32_16x16x32_bf16 %0, v[188:191], a[16:19], %0" : "+v"(g[0][0][1]) :  : ); SB();
  asm volatile("buffer_load_dwordx4 v[252:255], %0, %1, %2 offen offset:1024 sc1" :  : "v"(vB), "s"(rWS), "s"(69632u) : "memory"); SB();
  asm volatile("v_mul_f32 %0, %0, %1" : "+v"(pr[1][0]) : "v"(a1[1][0][0]) : ); SB();
  asm volatile("v_mfma_f32_16x16x32_bf16 %0, v[188:191], a[20:23], %0" : "+v"(g[0][1][1]) :  : ); SB();
  asm volatile("v_mul_f32 %0, %0, %1" : "+v"(pr[1][1]) : "v"(a1[1][0][1]) : ); SB();
  asm volatile("v_mfma_f32_16x16x32_bf16 %0, v[188:191], a[24:27], %0" : "+v"(g[0][2][1]) :  : ); SB();
  asm volatile("v_mul_f32 %0, %0, %1" : "+v"(pr[1][2]) : "v"(a1[1][0][2]) : ); SB();
  asm volatile("v_mfma_f32_16x16x32_bf16 %0, v[188:191], a[28:31], %0" : "+v"(g[0][3][1]) :  : ); SB();
  asm volatile("buffer_load_dwordx4 a[160:163], %0, %1, %2 offen offset:0 sc1" :  : "v"(vO), "s"(rWS), "s"(0u) : "memory"); SB();
  asm volatile("s_waitcnt vmcnt(17)" :  :  : "memory"); SB();
  asm volatile("v_mfma_f32_16x16x32_bf16 %0, v[192:195], a[32:35], %0" : "+v"(g[0][0][0]) :  : ); SB();
  asm volatile("v_mul_f32 %0, %0, %1" : "+v"(pr[1][3]) : "v"(a1[1][0][3]) : ); SB();
  asm volatile("v_mfma_f32_16x16x32_bf16 %0, v[192:195], a[36:39], %0" : "+v"(g[0][1][0]) :  : ); SB();
  asm volatile("v_cvt_pk_bf16_f32 %0, %1, %2" : "=v"(ho[1][0]) : "v"(pr[0][0]), "v"(pr[0][1]) : ); SB();
  asm volatile("v_mfma_f32_16x16x32_bf16 %0, v[192:195], a[40:43], %0" : "+v"(g[0][2][0]) :  : ); SB();
  asm volatile("v_cvt_pk_bf16_f32 %0, %1, %2" : "=v"(ho[1][1]) : "v"(pr[0][2]), "v"(pr[0][3]) : ); SB();
  asm volatile("v_mfma_f32_16x16x32_bf16 %0, v[192:195], a[44:47], %0" : "+v"(g[0][3][0]) :  : ); SB();
  asm volatile("buffer_load_dwordx4 a[164:167], %0, %1, %2 offen offset:1024 sc1" :  : "v"(vO), "s"(rWS), "s"(0u) : "memory"); SB();
  asm volatile("v_cvt_pk_bf16_f32 %0, %1, %2" : "=v"(ho[1][2]) : "v"(pr[1][0]), "v"(pr[1][1]) : ); SB();
  asm volatile("v_mfma_f32_16x16x32_bf16 %0, v[196:199], a[32:35], %0" : "+v"(g[0][0][1]) :  : ); SB();
  asm volatile("v_cvt_pk_bf16_f32 %0, %1, %2" : "=v"(ho[1][3]) : "v"(pr[1][2]), "v"(pr[1][3]) : ); SB();
  asm volatile("v_mfma_f32_16x16x32_bf16 %0, v[196:199], a[36:39], %0" : "+v"(g[0][1][1]) :  : ); SB();
  asm volatile("v_mul_f32 %0, %0, %1" : "+v"(a1[1][2][0]) : "v"(gt[0]) : ); SB();
  asm volatile("v_mfma_f32_16x16x32_bf16 %0, v[196:199], a[40:43], %0" : "+v"(g[0][2][1]) :  : ); SB();
  asm volatile("v_mul_f32 %0, %0, %1" : "+v"(a1[1][2][1]) : "v"(gt[1]) : ); SB();
  asm volatile("v_mfma_f32_16x16x32_bf16 %0, v[196:199], a[44:47], %0" : "+v"(g[0][3][1]) :  : ); SB();
  asm volatile("buffer_store_dwordx4 %1, %0, %2, %3 offen offset:1024 sc1\n\ts_nop 1" :  : "v"(vH_), "v"(ho[1]), "s"(rWS), "s"(0u) : "memory"); SB();
  asm volatile("s_waitcnt vmcnt(17)" :  :  : "memory"); SB();
  asm volatile("v_mfma_f32_16x16x32_bf16 %0, v[200:203], a[48:51], %0" : "+v"(g[0][0][0]) :  : ); SB();
  asm volatile("v_mul_f32 %0, %0, %1" : "+v"(a1[1][2][2]) : "v"(gt[2]) : ); SB();
  asm volatile("v_mfma_f32_16x16x32_bf16 %0, v[200:203], a[52:55], %0" : "+v"(g[0][1][0]) :  : ); SB();
  asm volatile("v_mul_f32 %0, %0, %1" : "+v"(a1[1][2][3]) : "v"(gt[3]) : ); SB();
  asm volatile("v_mfma_f32_16x16x32_bf16 %0, v[200:203], a[56:59], %0" : "+v"(g[0][2][0]) :  : ); SB();
  asm volatile("buffer_load_dwordx4 a[168:171], %0, %1, %2 offen offset:2048 sc1" :  : "v"(vO), "s"(rWS), "s"(0u) : "memory"); SB();
  asm volatile("v_mfma_f32_16x16x32_bf16 %0, v[200:203], a[60:63], %0" : "+v"(g[0][3][0]) :  : ); SB();
  asm volatile("v_mfma_f32_16x16x32_bf16 %0, v[204:207], a[48:51], %0" : "+v"(g[0][0][1]) :  : ); SB();
  asm volatile("v_mfma_f32_16x16x32_bf16 %0, v[204:207], a[52:55], %0" : "+v"(g[0][1][1]) :  : ); SB();
  asm volatile("v_mfma_f32_16x16x32_bf16 %0, v[204:207], a[56:59], %0" : "+v"(g[0][2][1]) :  : ); SB();
  asm volatile("buffer_load_dwordx4 a[172:175], %0, %1, %2 offen offset:3072 sc1" :  : "v"(vO), "s"(rWS), "s"(0u) : "memory"); SB();
  asm volatile("v_mfma_f32_16x16x32_bf16 %0, v[204:207], a[60:63], %0" : "+v"(g[0][3][1]) :  : ); SB();
  asm volatile("s_waitcnt vmcnt(17)" :  :  : "memory"); SB();
  asm volatile("v_mfma_f32_16x16x32_bf16 %0, v[208:211], a[64:67], %0" : "+v"(g[0][0][0]) :  : ); SB();
  asm volatile("ds_read_b128 %0, %1 offset:0" : "=&v"(yq[0]) : "v"(ydr) : "memory"); SB();
  asm volatile("v_mfma_f32_16x16x32_bf16 %0, v[208:211], a[68:71], %0" : "+v"(g[0][1][0]) :  : ); SB();
  asm volatile("ds_read_b128 %0, %1 offset:256" : "=&v"(yq[1]) : "v"(ydr) : "memory"); SB();
  asm volatile("v_mfma_f32_16x16x32_bf16 %0, v[208:211], a[72:75], %0" : "+v"(g[0][2][0]) :  : ); SB();
  asm volatile("ds_read_b128 %0, %1 offset:8192" : "=&v"(yq[2]) : "v"(ydr) : "memory"); SB();
  asm volatile("v_mfma_f32_16x16x32_bf16 %0, v[208:211], a[76:79], %0" : "+v"(g[0][3][0]) :  : ); SB();
  asm volatile("ds_read_b128 %0, %1 offset:8448" : "=&v"(yq[3]) : "v"(ydr) : "memory"); SB();
  asm volatile("v_mfma_f32_16x16x32_bf16 %0, v[212:215], a[64:67], %0" : "+v"(g[0][0][1]) :  : ); SB();
  asm volatile("ds_read_b128 %0, %1 offset:16384" : "=&v"(yq[4]) : "v"(ydr) : "memory"); SB();
  asm volatile("v_mfma_f32_16x16x32_bf16 %0, v[212:215], a[68:71], %0" : "+v"(g[0][1][1]) :  : ); SB();
  asm volatile("ds_read_b128 %0, %1 offset:16640" : "=&v"(yq[5]) : "v"(ydr) : "memory"); SB();
  asm volatile("v_mfma_f32_16x16x32_bf16 %0, v[212:215], a[72:75], %0" : "+v"(g[0][2][1]) :  : ); SB();
  asm volatile("ds_read_b128 %0, %1 offset:24576" : "=&v"(yq[6]) : "v"(ydr) : "memory"); SB();
  asm volatile("v_mfma_f32_16x16x32_bf16 %0, v[212:215], a[76:79], %0" : "+v"(g[0][3][1]) :  : ); SB();
  asm volatile("ds_read_b128 %0, %1 offset:24832" : "=&v"(yq[7]) : "v"(ydr) : "memory"); SB();
  asm volatile("s_waitcnt vmcnt(15)" :  :  : "memory"); SB();
  asm volatile("v_mfma_f32_16x16x32_bf16 %0, v[216:219], a[80:83], %0" : "+v"(g[0][0][0]) :  : ); SB();
  asm volatile("ds_read_b128 %0, %1 offset:25856" : "=&v"(ykv) : "v"(ykr) : "memory"); SB();
  asm volatile("v_mfma_f32_16x16x32_bf16 %0, v[216:219], a[84:87], %0" : "+v"(g[0][1][0]) :  : ); SB();
  asm volatile("buffer_load_dwordx4 a[176:179], %0, %1, %2 offen offset:0 sc1" :  : "v"(vO), "s"(rWS), "s"(4096u) : "memory"); SB();
  asm volatile("v_mfma_f32_16x16x32_bf16 %0, v[216:219], a[88:91], %0" : "+v"(g[0][2][0]) :  : ); SB();
  asm volatile("buffer_load_dwordx4 a[180:183], %0, %1, %2 offen offset:1024 sc1" :  : "v"(vO), "s"(rWS), "s"(4096u) : "memory"); SB();
  asm volatile("v_mfma_f32_16x16x32_bf16 %0, v[216:219], a[92:95], %0" : "+v"(g[0][3][0]) :  : ); SB();
  asm volatile("v_mfma_f32_16x16x32_bf16 %0, v[220:223], a[80:83], %0" : "+v"(g[0][0][1]) :  : ); SB();
  asm volatile("v_mfma_f32_16x16x32_bf16 %0, v[220:223], a[84:87], %0" : "+v"(g[0][1][1]) :  : ); SB();
  asm volatile("buffer_load_dwordx4 a[184:187], %0, %1, %2 offen offset:2048 sc1" :  : "v"(vO), "s"(rWS), "s"(4096u) : "memory"); SB();
  asm volatile("v_mfma_f32_16x16x32_bf16 %0, v[220:223], a[88:91], %0" : "+v"(g[0][2][1]) :  : ); SB();
  asm volatile("v_mfma_f32_16x16x32_bf16 %0, v[220:223], a[92:95], %0" : "+v"(g[0][3][1]) :  : ); SB();
  asm volatile("buffer_load_dwordx4 a[188:191], %0, %1, %2 offen offset:3072 sc1" :  : "v"(vO), "s"(rWS), "s"(4096u) : "memory"); SB();
  asm volatile("buffer_load_dwordx4 a[192:195], %0, %1, %2 offen offset:0 sc1" :  : "v"(vO), "s"(rWS), "s"(8192u) : "memory"); SB();
  asm volatile("s_waitcnt vmcnt(12)\n\ts_barrier" :  :  : "memory"); SB();
  asm volatile("s_waitcnt vmcnt(10)" :  :  : "memory"); SB();
  asm volatile("v_mfma_f32_16x16x32_bf16 %0, v[224:227], a[100:103], %0" : "+v"(g[0][1][0]) :  : ); SB();
  asm volatile("buffer_load_dwordx4 a[196:199], %0, %1, %2 offen offset:1024 sc1" :  : "v"(vO), "s"(rWS), "s"(8192u) : "memory"); SB();
  asm volatile("v_mfma_f32_16x16x32_bf16 %0, v[228:231], a[100:103], %0" : "+v"(g[0][1][1]) :  : ); SB();
  asm volatile("ds_read2_b32 a[64:65], %0 offset0:176 offset1:177" :  : "v"(aa[0]) : "memory"); SB();
  asm volatile("v_mfma_f32_16x16x32_bf16 %0, v[232:235], a[116:119], %0" : "+v"(g[0][1][0]) :  : ); SB();
  asm volatile("ds_read2_b32 a[66:67], %0 offset0:178 offset1:179" :  : "v"(aa[0]) : "memory"); SB();
  asm volatile("v_mfma_f32_16x16x32_bf16 %0, v[236:239], a[116:119], %0" : "+v"(g[0][1][1]) :  : ); SB();
  asm volatile("ds_read_b128 a[60:63], %0 offset:3776" :  : "v"(bb) : "memory"); SB();
  asm volatile("v_mfma_f32_16x16x32_bf16 %0, v[240:243], a[132:135], %0" : "+v"(g[0][1][0]) :  : ); SB();
  asm volatile("buffer_load_dwordx4 a[200:203], %0, %1, %2 offen offset:0 sc1" :  : "v"(vO2), "s"(rWS), "s"(0u) : "memory"); SB();
  asm volatile("v_mfma_f32_16x16x32_bf16 %0, v[244:247], a[132:135], %0" : "+v"(g[0][1][1]) :  : ); SB();
  asm volatile("ds_read_b128 a[56:59], %0 offset:3264" :  : "v"(bb) : "memory"); SB();
  asm volatile("v_mfma_f32_16x16x32_bf16 %0, v[248:251], a[148:151], %0" : "+v"(g[0][1][0]) :  : ); SB();
  asm volatile("ds_read_b128 a[52:55], %0 offset:2752" :  : "v"(bb) : "memory"); SB();
  asm volatile("v_mfma_f32_16x16x32_bf16 %0, v[252:255], a[148:151], %0" : "+v"(g[0][1][1]) :  : ); SB();
  asm volatile("buffer_load_dwordx4 a[204:207], %0, %1, %2 offen offset:1024 sc1" :  : "v"(vO2), "s"(rWS), "s"(0u) : "memory"); SB();
  asm volatile("v_mfma_f32_16x16x32_bf16 %0, v[224:227], a[104:107], %0" : "+v"(g[0][2][0]) :  : ); SB();
  asm volatile("ds_read_b128 a[48:51], %0 offset:2240" :  : "v"(bb) : "memory"); SB();
  asm volatile("v_mfma_f32_16x16x32_bf16 %0, v[228:231], a[104:107], %0" : "+v"(g[0][2][1]) :  : ); SB();
  asm volatile("ds_read2_b32 a[68:69], %0 offset0:48 offset1:49" :  : "v"(aa[0]) : "memory"); SB();
  asm volatile("v_mfma_f32_16x16x32_bf16 %0, v[232:235], a[120:123], %0" : "+v"(g[0][2][0]) :  : ); SB();
  asm volatile("ds_write_b128 %0, %1 offset:0" :  : "v"(pw2), "v"(g[0][1][0]) : "memory"); SB();
  asm volatile("v_mfma_f32_16x16x32_bf16 %0, v[236:239], a[120:123], %0" : "+v"(g[0][2][1]) :  : ); SB();
  asm volatile("ds_read2_b32 a[70:71], %0 offset0:50 offset1:51" :  : "v"(aa[0]) : "memory"); SB();
  asm volatile("v_mfma_f32_16x16x32_bf16 %0, v[240:243], a[136:139], %0" : "+v"(g[0][2][0]) :  : ); SB();
  asm volatile("ds_read_b128 a[44:47], %0 offset:1728" :  : "v"(bb) : "memory"); SB();
  asm volatile("v_mfma_f32_16x16x32_bf16 %0, v[244:247], a[136:139], %0" : "+v"(g[0][2][1]) :  : ); SB();
  asm volatile("ds_write_b128 %0, %1 offset:1024" :  : "v"(pw2), "v"(g[0][1][1]) : "memory"); SB();
  asm volatile("v_mfma_f32_16x16x32_bf16 %0, v[248:251], a[152:155], %0" : "+v"(g[0][2][0]) :  : ); SB();
  asm volatile("ds_read2_b32 a[72:73], %0 offset0:176 offset1:177" :  : "v"(aa[1]) : "memory"); SB();
  asm volatile("v_mfma_f32_16x16x32_bf16 %0, v[252:255], a[152:155], %0" : "+v"(g[0][2][1]) :  : ); SB();
  asm volatile("ds_read2_b32 a[74:75], %0 offset0:178 offset1:179" :  : "v"(aa[1]) : "memory"); SB();
  asm volatile("v_mfma_f32_16x16x32_bf16 %0, v[224:227], a[108:111], %0" : "+v"(g[0][3][0]) :  : ); SB();
  asm volatile("ds_read2_b32 a[76:77], %0 offset0:48 offset1:49" :  : "v"(aa[1]) : "memory"); SB();
  asm volatile("v_mfma_f32_16x16x32_bf16 %0, v[228:231], a[108:111], %0" : "+v"(g[0][3][1]) :  : ); SB();
  asm volatile("ds_read2_b32 a[78:79], %0 offset0:50 offset1:51" :  : "v"(aa[1]) : "memory"); SB();
  asm volatile("v_mfma_f32_16x16x32_bf16 %0, v[232:235], a[124:127], %0" : "+v"(g[0][3][0]) :  : ); SB();
  asm volatile("ds_write_b128 %0, %1 offset:2048" :  : "v"(pw2), "v"(g[0][2][0]) : "memory"); SB();
  asm volatile("v_mfma_f32_16x16x32_bf16 %0, v[236:239], a[124:127], %0" : "+v"(g[0][3][1]) :  : ); SB();
  asm volatile("ds_read_b128 a[40:43], %0 offset:1216" :  : "v"(bb) : "memory"); SB();
  asm volatile("v_mfma_f32_16x16x32_bf16 %0, v[240:243], a[140:143], %0" : "+v"(g[0][3][0]) :  : ); SB();
  asm volatile("ds_read2_b32 a[80:81], %0 offset0:176 offset1:177" :  : "v"(aa[2]) : "memory"); SB();
  asm volatile("v_mfma_f32_16x16x32_bf16 %0, v[244:247], a[140:143], %0" : "+v"(g[0][3][1]) :  : ); SB();
  asm volatile("ds_write_b128 %0, %1 offset:3072" :  : "v"(pw2), "v"(g[0][2][1]) : "memory"); SB();
  asm volatile("v_mfma_f32_16x16x32_bf16 %0, v[248:251], a[156:159], %0" : "+v"(g[0][3][0]) :  : ); SB();
  asm volatile("s_waitcnt vmcnt(10)\n\ts_mov_b64 exec, 1\n\tbuffer_store_dwordx2 %0, off, %1, %2 sc1\n\ts_mov_b64 exec, -1" :  : "v"(((u32x2){tg0, tg1})), "s"(rWS), "s"(sFH) : "memory"); SB();
  asm volatile("buffer_load_dwordx4 a[208:211], %0, %1, %2 offen offset:2048 sc1" :  : "v"(vO2), "s"(rWS), "s"(0u) : "memory"); SB();
  asm volatile("v_mfma_f32_16x16x32_bf16 %0, v[252:255], a[156:159], %0" : "+v"(g[0][3][1]) :  : ); SB();
  asm volatile("ds_read2_b32 a[82:83], %0 offset0:178 offset1:179" :  : "v"(aa[2]) : "memory"); SB();
  asm volatile("v_mfma_f32_16x16x32_bf16 %0, v[224:227], a[96:99], %0" : "+v"(g[0][0][0]) :  : ); SB();
  asm volatile("ds_read2_b32 a[84:85], %0 offset0:48 offset1:49" :  : "v"(aa[2]) : "memory"); SB();
  asm volatile("v_mfma_f32_16x16x32_bf16 %0, v[228:231], a[96:99], %0" : "+v"(g[0][0][1]) :  : ); SB();
  asm volatile("buffer_load_dwordx4 a[212:215], %0, %1, %2 offen offset:3072 sc1" :  : "v"(vO2), "s"(rWS), "s"(0u) : "memory"); SB();
  asm volatile("v_mfma_f32_16x16x32_bf16 %0, v[232:235], a[112:115], %0" : "+v"(g[0][0][0]) :  : ); SB();
  asm volatile("ds_write_b128 %0, %1 offset:4096" :  : "v"(pw2), "v"(g[0][3][0]) : "memory"); SB();
  asm volatile("v_mfma_f32_16x16x32_bf16 %0, v[236:239], a[112:115], %0" : "+v"(g[0][0][1]) :  : ); SB();
  asm volatile("ds_read2_b32 a[86:87], %0 offset0:50 offset1:51" :  : "v"(aa[2]) : "memory"); SB();
  asm volatile("v_mfma_f32_16x16x32_bf16 %0, v[240:243], a[128:131], %0" : "+v"(g[0][0][0]) :  : ); SB();
  asm volatile("buffer_load_dwordx4 a[216:219], %0, %1, %2 offen offset:0 sc1" :  : "v"(vO2), "s"(rWS), "s"(4096u) : "memory"); SB();
  asm volatile("v_mfma_f32_16x16x32_bf16 %0, v[244:247], a[128:131], %0" : "+v"(g[0][0][1]) :  : ); SB();
  asm volatile("ds_write_b128 %0, %1 offset:5120" :  : "v"(pw2), "v"(g[0][3][1]) : "memory"); SB();
  asm volatile("v_mfma_f32_16x16x32_bf16 %0, v[248:251], a[144:147], %0" : "+v"(g[0][0][0]) :  : ); SB();
  asm volatile("buffer_load_dwordx4 a[220:223], %0, %1, %2 offen offset:1024 sc1" :  : "v"(vO2), "s"(rWS), "s"(4096u) : "memory"); SB();
  asm volatile("v_mfma_f32_16x16x32_bf16 %0, v[252:255], a[144:147], %0" : "+v"(g[0][0][1]) :  : ); SB();
  asm volatile("ds_read_b128 a[36:39], %0 offset:704" :  : "v"(bb) : "memory"); SB();
  if ((rb | cb | w) == 0) asm volatile("buffer_store_dwordx2 %0, off, %1, 0 sc1" :: "v"(((u32x2){tg0, tg1})), "s"(rWS) : "memory");
  SB();
  ST(17);
  unsigned pw3, p3[3], yw, ys[2], vYr, ya, po;
  asm volatile("s_waitcnt lgkmcnt(4)\n\tv_mfma_f32_16x16x32_bf16 %0, a[64:67], a[60:63], 0" : "=&v"(ac[7]) :  : ); SB();
  asm volatile("ds_read2_b32 a[88:89], %0 offset0:176 offset1:177" :  : "v"(aa[3]) : "memory"); SB();
  asm volatile("" : "+v"(yq[0]), "+v"(yq[1]), "+v"(yq[2]), "+v"(yq[3]), "+v"(yq[4]), "+v"(yq[5]), "+v"(yq[6]), "+v"(yq[7]), "+v"(ykv) :  : "memory"); SB();
  asm volatile("v_mfma_f32_16x16x32_bf16 %0, a[64:67], a[56:59], 0" : "=&v"(ac[6]) :  : ); SB();
  asm volatile("ds_read2_b32 a[90:91], %0 offset0:178 offset1:179" :  : "v"(aa[3]) : "memory"); SB();
  asm volatile("buffer_load_dwordx4 a[224:227], %0, %1, %2 offen offset:2048 sc1" :  : "v"(vO2), "s"(rWS), "s"(4096u) : "memory"); SB();
  asm volatile("v_mfma_f32_16x16x32_bf16 %0, a[64:67], a[52:55], 0" : "=&v"(ac[5]) :  : ); SB();
  asm volatile("ds_read2_b32 a[92:93], %0 offset0:48 offset1:49" :  : "v"(aa[3]) : "memory"); SB();
  asm volatile("v_add_f32 %0, %0, %1" : "+v"(yq[0][0]) : "v"(yq[2][0]) : ); SB();
  asm volatile("v_mfma_f32_16x16x32_bf16 %0, a[64:67], a[48:51], 0" : "=&v"(ac[4]) :  : ); SB();
  asm volatile("ds_read2_b32 a[94:95], %0 offset0:50 offset1:51" :  : "v"(aa[3]) : "memory"); SB();
  asm volatile("buffer_load_dwordx4 a[228:231], %0, %1, %2 offen offset:3072 sc1" :  : "v"(vO2), "s"(rWS), "s"(4096u) : "memory"); SB();
  asm volatile("v_mfma_f32_16x16x32_bf16 %0, a[68:71], a[56:59], %0" : "+v"(ac[7]) :  : ); SB();
  asm volatile("ds_read_b128 a[32:35], %0 offset:192" :  : "v"(bb) : "memory"); SB();
  asm volatile("s_waitcnt lgkmcnt(6)\n\ts_barrier" :  :  : "memory"); SB();
  asm volatile("v_mfma_f32_16x16x32_bf16 %0, a[68:71], a[52:55], %0" : "+v"(ac[6]) :  : ); SB();
  asm volatile("ds_read2_b32 a[128:129], %0 offset0:160 offset1:161" :  : "v"(aa[0]) : "memory"); SB();
  asm volatile("buffer_load_dwordx4 a[232:235], %0, %1, %2 offen offset:0 sc1" :  : "v"(vO2), "s"(rWS), "s"(8192u) : "memory"); SB();
  asm volatile("v_mfma_f32_16x16x32_bf16 %0, a[68:71], a[48:51], %0" : "+v"(ac[5]) :  : ); SB();
  asm volatile("ds_read2_b32 a[130:131], %0 offset0:162 offset1:163" :  : "v"(aa[0]) : "memory"); SB();
  asm volatile("v_add_f32 %0, %0, %1" : "+v"(yq[0][1]) : "v"(yq[2][1]) : ); SB();
  asm volatile("v_mfma_f32_16x16x32_bf16 %0, a[68:71], a[44:47], %0" : "+v"(ac[4]) :  : ); SB();
  asm volatile("ds_read_b128 %0, %1 offset:0" : "=&v"(pr[0]) : "v"(pe[0]) : "memory"); SB();
  asm volatile("v_add_f32 %0, %0, %1" : "+v"(yq[0][2]) : "v"(yq[2][2]) : ); SB();
  asm volatile("v_mfma_f32_16x16x32_bf16 %0, a[72:75], a[52:55], %0" : "+v"(ac[7]) :  : ); SB();
  asm volatile("ds_read_b128 a[124:127], %0 offset:3712" :  : "v"(bb) : "memory"); SB();
  asm volatile("buffer_load_dwordx4 a[236:239], %0, %1, %2 offen offset:1024 sc1" :  : "v"(vO2), "s"(rWS), "s"(8192u) : "memory"); SB();
  asm volatile("v_mfma_f32_16x16x32_bf16 %0, a[72:75], a[48:51], %0" : "+v"(ac[6]) :  : ); SB();
  asm volatile("ds_read_b128 a[120:123], %0 offset:3200" :  : "v"(bb) : "memory"); SB();
  asm volatile("v_add_f32 %0, %0, %1" : "+v"(yq[0][3]) : "v"(yq[2][3]) : ); SB();
  asm volatile("v_mfma_f32_16x16x32_bf16 %0, a[72:75], a[44:47], %0" : "+v"(ac[5]) :  : ); SB();
  asm volatile("ds_read_b128 a[116:119], %0 offset:2688" :  : "v"(bb) : "memory"); SB();
  asm volatile("v_add_f32 %0, %0, %1" : "+v"(yq[1][0]) : "v"(yq[3][0]) : ); SB();
  asm volatile("v_mfma_f32_16x16x32_bf16 %0, a[64:67], a[44:47], 0" : "=&v"(ac[3]) :  : ); SB();
  asm volatile("ds_read_b128 %0, %1 offset:1024" : "=&v"(pr[1]) : "v"(pe[0]) : "memory"); SB();
  asm volatile("v_add_f32 %0, %0, %1" : "+v"(yq[1][1]) : "v"(yq[3][1]) : ); SB();
  asm volatile("v_mfma_f32_16x16x32_bf16 %0, a[76:79], a[48:51], %0" : "+v"(ac[7]) :  : ); SB();
  asm volatile("ds_read_b128 a[112:115], %0 offset:2176" :  : "v"(bb) : "memory"); SB();
  asm volatile("buffer_load_dwordx4 a[240:243], %0, %1, %2 offen offset:2048 sc1" :  : "v"(vO), "s"(rWS), "s"(8192u) : "memory"); SB();
  asm volatile("v_mfma_f32_16x16x32_bf16 %0, a[76:79], a[44:47], %0" : "+v"(ac[6]) :  : ); SB();
  asm volatile("ds_read2_b32 a[132:133], %0 offset0:32 offset1:33" :  : "v"(aa[0]) : "memory"); SB();
  asm volatile("v_add_f32 %0, %0, %1" : "+v"(yq[1][2]) : "v"(yq[3][2]) : ); SB();
  asm volatile("v_mfma_f32_16x16x32_bf16 %0, a[76:79], a[40:43], %0" : "+v"(ac[5]) :  : ); SB();
  asm volatile("ds_read2_b32 a[134:135], %0 offset0:34 offset1:35" :  : "v"(aa[0]) : "memory"); SB();
  asm volatile("buffer_load_dwordx4 a[244:247], %0, %1, %2 offen offset:3072 sc1" :  : "v"(vO), "s"(rWS), "s"(8192u) : "memory"); SB();
  asm volatile("v_mfma_f32_16x16x32_bf16 %0, a[72:75], a[40:43], %0" : "+v"(ac[4]) :  : ); SB();
  asm volatile("ds_read_b128 %0, %1 offset:2048" : "=&v"(pr[2]) : "v"(pe[1]) : "memory"); SB();
  asm volatile("v_add_f32 %0, %0, %1" : "+v"(yq[1][3]) : "v"(yq[3][3]) : ); SB();
  asm volatile("v_mfma_f32_16x16x32_bf16 %0, a[80:83], a[44:47], %0" : "+v"(ac[7]) :  : ); SB();
  asm volatile("ds_read_b128 a[108:111], %0 offset:1664" :  : "v"(bb) : "memory"); SB();
  asm volatile("v_add_f32 %0, %0, %1" : "+v"(yq[4][0]) : "v"(yq[6][0]) : ); SB();
  asm volatile("v_mfma_f32_16x16x32_bf16 %0, a[80:83], a[40:43], %0" : "+v"(ac[6]) :  : ); SB();
  asm volatile("ds_read2_b32 a[136:137], %0 offset0:160 offset1:161" :  : "v"(aa[1]) : "memory"); SB();
  asm volatile("v_add_f32 %0, %0, %1" : "+v"(yq[4][1]) : "v"(yq[6][1]) : ); SB();
  asm volatile("v_mfma_f32_16x16x32_bf16 %0, a[68:71], a[40:43], %0" : "+v"(ac[3]) :  : ); SB();
  asm volatile("ds_read2_b32 a[138:139], %0 offset0:162 offset1:163" :  : "v"(aa[1]) : "memory"); SB();
  asm volatile("buffer_load_dwordx4 a[248:251], %0, %1, %2 offen offset:0 sc1" :  : "v"(vO), "s"(rWS), "s"(12288u) : "memory"); SB();
  asm volatile("v_mfma_f32_16x16x32_bf16 %0, a[64:67], a[40:43], 0" : "=&v"(ac[2]) :  : ); SB();
  asm volatile("ds_read_b128 %0, %1 offset:3072" : "=&v"(pr[3]) : "v"(pe[1]) : "memory"); SB();
  asm volatile("v_add_f32 %0, %0, %1" : "+v"(yq[4][2]) : "v"(yq[6][2]) : ); SB();
  asm volatile("v_mfma_f32_16x16x32_bf16 %0, a[84:87], a[40:43], %0" : "+v"(ac[7]) :  : ); SB();
  asm volatile("ds_read2_b32 a[140:141], %0 offset0:32 offset1:33" :  : "v"(aa[1]) : "memory"); SB();
  asm volatile("s_waitcnt lgkmcnt(8)\n\tv_mfma_f32_16x16x32_bf16 %0, a[84:87], a[36:39], %0" : "+v"(ac[6]) :  : ); SB();
  asm volatile("ds_read2_b32 a[142:143], %0 offset0:34 offset1:35" :  : "v"(aa[1]) : "memory"); SB();
  asm volatile("v_add_f32 %0, %0, %1" : "+v"(yq[4][3]) : "v"(yq[6][3]) : ); SB();
  asm volatile("v_mfma_f32_16x16x32_bf16 %0, a[80:83], a[36:39], %0" : "+v"(ac[5]) :  : ); SB();
  asm volatile("ds_read_b128 a[104:107], %0 offset:1152" :  : "v"(bb) : "memory"); SB();
  asm volatile("v_add_f32 %0, %0, %1" : "+v"(yq[5][0]) : "v"(yq[7][0]) : ); SB();
  asm volatile("v_mfma_f32_16x16x32_bf16 %0, a[76:79], a[36:39], %0" : "+v"(ac[4]) :  : ); SB();
  asm volatile("ds_read_b128 %0, %1 offset:4096" : "=&v"(pr[4]) : "v"(pe[2]) : "memory"); SB();
  asm volatile("v_add_f32 %0, %0, %1" : "+v"(yq[5][1]) : "v"(yq[7][1]) : ); SB();
  asm volatile("v_mfma_f32_16x16x32_bf16 %0, a[88:91], a[36:39], %0" : "+v"(ac[7]) :  : ); SB();
  asm volatile("ds_read2_b32 a[144:145], %0 offset0:160 offset1:161" :  : "v"(aa[2]) : "memory"); SB();
  asm volatile("v_add_f32 %0, %0, %1" : "+v"(yq[5][2]) : "v"(yq[7][2]) : ); SB();
  asm volatile("v_mfma_f32_16x16x32_bf16 %0, a[72:75], a[36:39], %0" : "+v"(ac[3]) :  : ); SB();
  asm volatile("ds_read2_b32 a[146:147], %0 offset0:162 offset1:163" :  : "v"(aa[2]) : "memory"); SB();
  asm volatile("v_add_f32 %0, %0, %1" : "+v"(yq[5][3]) : "v"(yq[7][3]) : ); SB();
  asm volatile("v_mfma_f32_16x16x32_bf16 %0, a[68:71], a[36:39], %0" : "+v"(ac[2]) :  : ); SB();
  asm volatile("ds_read2_b32 a[148:149], %0 offset0:32 offset1:33" :  : "v"(aa[2]) : "memory"); SB();
  asm volatile("v_add_f32 %0, %0, %1" : "+v"(yq[0][0]) : "v"(yq[4][0]) : ); SB();
  asm volatile("v_mfma_f32_16x16x32_bf16 %0, a[64:67], a[36:39], 0" : "=&v"(ac[1]) :  : ); SB();
  asm volatile("ds_read_b128 %0, %1 offset:5120" : "=&v"(pr[5]) : "v"(pe[2]) : "memory"); SB();
  asm volatile("v_add_f32 %0, %0, %1" : "+v"(yq[0][1]) : "v"(yq[4][1]) : ); SB();
  asm volatile("v_mfma_f32_16x16x32_bf16 %0, a[92:95], a[32:35], %0" : "+v"(ac[7]) :  : ); SB();
  asm volatile("ds_read2_b32 a[150:151], %0 offset0:34 offset1:35" :  : "v"(aa[2]) : "memory"); SB();
  asm volatile("v_add_f32 %0, %0, %1" : "+v"(yq[0][2]) : "v"(yq[4][2]) : ); SB();
  asm volatile("v_mfma_f32_16x16x32_bf16 %0, a[88:91], a[32:35], %0" : "+v"(ac[6]) :  : ); SB();
  asm volatile("ds_read_b128 a[100:103], %0 offset:640" :  : "v"(bb) : "memory"); SB();
  asm volatile("v_add_f32 %0, %0, %1" : "+v"(yq[0][3]) : "v"(yq[4][3]) : ); SB();
  asm volatile("v_mfma_f32_16x16x32_bf16 %0, a[84:87], a[32:35], %0" : "+v"(ac[5]) :  : ); SB();
  asm volatile("ds_read2_b32 a[152:153], %0 offset0:160 offset1:161" :  : "v"(aa[3]) : "memory"); SB();
  asm volatile("v_add_f32 %0, %0, %1" : "+v"(yq[1][0]) : "v"(yq[5][0]) : ); SB();
  asm volatile("v_mfma_f32_16x16x32_bf16 %0, a[80:83], a[32:35], %0" : "+v"(ac[4]) :  : ); SB();
  pw3 = lb + 98304 + w * 15360 + l16; asm volatile("" : "+v"(pw3)); SB();
  asm volatile("v_mfma_f32_16x16x32_bf16 %0, a[76:79], a[32:35], %0" : "+v"(ac[3]) :  : ); SB();
  asm volatile("ds_read2_b32 a[154:155], %0 offset0:162 offset1:163" :  : "v"(aa[3]) : "memory"); SB();
  asm volatile("v_add_f32 %0, %0, %1" : "+v"(yq[1][1]) : "v"(yq[5][1]) : ); SB();
  asm volatile("v_mfma_f32_16x16x32_bf16 %0, a[72:75], a[32:35], %0" : "+v"(ac[2]) :  : ); SB();
  asm volatile("ds_read2_b32 a[156:157], %0 offset0:32 offset1:33" :  : "v"(aa[3]) : "memory"); SB();
  asm volatile("s_waitcnt lgkmcnt(5)" : "+v"(pr[0]), "+v"(pr[1]), "+v"(pr[2]), "+v"(pr[3]), "+v"(pr[4]), "+v"(pr[5]) :  : "memory"); SB();
  asm volatile("v_mfma_f32_16x16x32_bf16 %0, a[68:71], a[32:35], %0" : "+v"(ac[1]) :  : ); SB();
  asm volatile("ds_read2_b32 a[158:159], %0 offset0:34 offset1:35" :  : "v"(aa[3]) : "memory"); SB();
  asm volatile("buffer_load_dwordx4 a[252:255], %0, %1, %2 offen offset:1024 sc1" :  : "v"(vO), "s"(rWS), "s"(12288u) : "memory"); SB();
  asm volatile("v_mfma_f32_16x16x32_bf16 %0, a[64:67], a[32:35], 0" : "=&v"(ac[0]) :  : ); SB();
  {
    const f32x4 h0 = ((g[0][0][0] + pr[0]) + (pr[2] + pr[4])) * a1[1][1], h1 = ((g[0][0][1] + pr[1]) + (pr[3] + pr[5])) * a1[1][2];
    ho[2] = (u32x4){pack2(h0[0], h0[1]), pack2(h0[2], h0[3]), pack2(h1[0], h1[1]), nohole(pack2(h1[2], h1[3]))};
    SB();
  }
  asm volatile("v_mfma_f32_16x16x32_bf16 %0, a[128:131], a[124:127], %0" : "+v"(ac[7]) :  : ); SB();
  asm volatile("ds_read_b128 a[96:99], %0 offset:128" :  : "v"(bb) : "memory"); SB();
  asm volatile("buffer_load_dwordx4 a[0:3], %0, %1, %2 offen offset:2048 sc1" :  : "v"(vO), "s"(rWS), "s"(12288u) : "memory"); SB();
  asm volatile("v_mfma_f32_16x16x32_bf16 %0, a[128:131], a[120:123], %0" : "+v"(ac[6]) :  : ); SB();
  asm volatile("ds_read2_b32 a[64:65], %0 offset0:144 offset1:145" :  : "v"(aa[0]) : "memory"); SB();
  asm volatile("buffer_load_dwordx4 a[4:7], %0, %1, %2 offen offset:2048 sc1" :  : "v"(vO2), "s"(rWS), "s"(8192u) : "memory"); SB();
  asm volatile("v_mfma_f32_16x16x32_bf16 %0, a[128:131], a[116:119], %0" : "+v"(ac[5]) :  : ); SB();
  asm volatile("ds_read2_b32 a[66:67], %0 offset0:146 offset1:147" :  : "v"(aa[0]) : "memory"); SB();
  asm volatile("buffer_load_dwordx4 a[8:11], %0, %1, %2 offen offset:3072 sc1" :  : "v"(vO2), "s"(rWS), "s"(8192u) : "memory"); SB();
  asm volatile("v_mfma_f32_16x16x32_bf16 %0, a[128:131], a[112:115], %0" : "+v"(ac[4]) :  : ); SB();
  asm volatile("buffer_store_dwordx4 %1, %0, %2, %3 offen offset:2048 sc1\n\ts_nop 1" :  : "v"(vH_), "v"(ho[2]), "s"(rWS), "s"(0u) : "memory"); SB();
  asm volatile("v_mfma_f32_16x16x32_bf16 %0, a[132:135], a[120:123], %0" : "+v"(ac[7]) :  : ); SB();
  asm volatile("ds_read_b128 a[60:63], %0 offset:3648" :  : "v"(bb) : "memory"); SB();
  asm volatile("v_add_f32 %0, %0, %1" : "+v"(yq[1][2]) : "v"(yq[5][2]) : ); SB();
  asm volatile("v_mfma_f32_16x16x32_bf16 %0, a[132:135], a[116:119], %0" : "+v"(ac[6]) :  : ); SB();
  asm volatile("ds_read_b128 a[56:59], %0 offset:3136" :  : "v"(bb) : "memory"); SB();
  asm volatile("v_add_f32 %0, %0, %1" : "+v"(yq[1][3]) : "v"(yq[5][3]) : ); SB();
  asm volatile("v_mfma_f32_16x16x32_bf16 %0, a[132:135], a[112:115], %0" : "+v"(ac[5]) :  : ); SB();
  asm volatile("ds_read_b128 a[52:55], %0 offset:2624" :  : "v"(bb) : "memory"); SB();
  asm volatile("v_lshlrev_b32 %0, 16, %1" : "=v"(yq[2][0]) : "v"(ykv[0]) : ); SB();
  asm volatile("v_mfma_f32_16x16x32_bf16 %0, a[132:135], a[108:111], %0" : "+v"(ac[4]) :  : ); SB();
  p3[0] = lb + 98304 + (w ^ 1) * 15360 + l16; asm volatile("" : "+v"(p3[0])); SB();
  asm volatile("v_mfma_f32_16x16x32_bf16 %0, a[136:139], a[116:119], %0" : "+v"(ac[7]) :  : ); SB();
  asm volatile("ds_read_b128 a[48:51], %0 offset:2112" :  : "v"(bb) : "memory"); SB();
  asm volatile("buffer_load_dwordx4 a[12:15], %0, %1, %2 offen offset:0 sc1" :  : "v"(vO2), "s"(rWS), "s"(12288u) : "memory"); SB();
  asm volatile("v_mfma_f32_16x16x32_bf16 %0, a[136:139], a[112:115], %0" : "+v"(ac[6]) :  : ); SB();
  asm volatile("ds_read2_b32 a[68:69], %0 offset0:16 offset1:17" :  : "v"(aa[0]) : "memory"); SB();
  asm volatile("v_and_b32 %0, 0xffff0000, %1" : "=v"(yq[2][1]) : "v"(ykv[0]) : ); SB();
  asm volatile("v_mfma_f32_16x16x32_bf16 %0, a[136:139], a[108:111], %0" : "+v"(ac[5]) :  : ); SB();
  asm volatile("ds_read2_b32 a[70:71], %0 offset0:18 offset1:19" :  : "v"(aa[0]) : "memory"); SB();
  asm volatile("v_lshlrev_b32 %0, 16, %1" : "=v"(yq[2][2]) : "v"(ykv[1]) : ); SB();
  asm volatile("v_mfma_f32_16x16x32_bf16 %0, a[128:131], a[108:111], %0" : "+v"(ac[3]) :  : ); SB();
  p3[1] = lb + 98304 + (w ^ 2) * 15360 + l16; asm volatile("" : "+v"(p3[1])); SB();
  asm volatile("v_mfma_f32_16x16x32_bf16 %0, a[140:143], a[112:115], %0" : "+v"(ac[7]) :  : ); SB();
  asm volatile("ds_read_b128 a[44:47], %0 offset:1600" :  : "v"(bb) : "memory"); SB();
  asm volatile("buffer_load_dwordx4 a[16:19], %0, %1, %2 offen offset:1024 sc1" :  : "v"(vO2), "s"(rWS), "s"(12288u) : "memory"); SB();
  asm volatile("v_mfma_f32_16x16x32_bf16 %0, a[140:143], a[108:111], %0" : "+v"(ac[6]) :  : ); SB();
  asm volatile("ds_read2_b32 a[72:73], %0 offset0:144 offset1:145" :  : "v"(aa[1]) : "memory"); SB();
  asm volatile("v_and_b32 %0, 0xffff0000, %1" : "=v"(yq[2][3]) : "v"(ykv[1]) : ); SB();
  asm volatile("v_mfma_f32_16x16x32_bf16 %0, a[140:143], a[104:107], %0" : "+v"(ac[5]) :  : ); SB();
  asm volatile("ds_read2_b32 a[74:75], %0 offset0:146 offset1:147" :  : "v"(aa[1]) : "memory"); SB();
  asm volatile("buffer_load_dwordx4 a[20:23], %0, %1, %2 offen offset:2048 sc1" :  : "v"(vO2), "s"(rWS), "s"(12288u) : "memory"); SB();
  asm volatile("v_mfma_f32_16x16x32_bf16 %0, a[136:139], a[104:107], %0" : "+v"(ac[4]) :  : ); SB();
  p3[2] = lb + 98304 + (w ^ 3) * 15360 + l16; asm volatile("" : "+v"(p3[2])); SB();
  asm volatile("v_mfma_f32_16x16x32_bf16 %0, a[144:147], a[108:111], %0" : "+v"(ac[7]) :  : ); SB();
  asm volatile("ds_read2_b32 a[76:77], %0 offset0:16 offset1:17" :  : "v"(aa[1]) : "memory"); SB();
  asm volatile("v_lshlrev_b32 %0, 16, %1" : "=v"(yq[3][0]) : "v"(ykv[2]) : ); SB();
  asm volatile("v_mfma_f32_16x16x32_bf16 %0, a[144:147], a[104:107], %0" : "+v"(ac[6]) :  : ); SB();
  asm volatile("ds_read2_b32 a[78:79], %0 offset0:18 offset1:19" :  : "v"(aa[1]) : "memory"); SB();
  asm volatile("v_and_b32 %0, 0xffff0000, %1" : "=v"(yq[3][1]) : "v"(ykv[2]) : ); SB();
  asm volatile("v_mfma_f32_16x16x32_bf16 %0, a[132:135], a[104:107], %0" : "+v"(ac[3]) :  : ); SB();
  asm volatile("ds_read_b128 a[40:43], %0 offset:1088" :  : "v"(bb) : "memory"); SB();
  asm volatile("buffer_load_dwordx2 %0, %1, %2, %3 offen sc1" : "=&v"(fh) : "v"(vFH), "s"(rWS), "s"(0u) : "memory"); SB();
  asm volatile("v_mfma_f32_16x16x32_bf16 %0, a[128:131], a[104:107], %0" : "+v"(ac[2]) :  : ); SB();
  yw = (lane ^ ((lane >> 3) & 6)) * 16; asm volatile("" : "+v"(yw)); SB();
  asm volatile("s_waitcnt lgkmcnt(6)\n\tv_mfma_f32_16x16x32_bf16 %0, a[148:151], a[104:107], %0" : "+v"(ac[7]) :  : ); SB();
  asm volatile("ds_read2_b32 a[80:81], %0 offset0:144 offset1:145" :  : "v"(aa[2]) : "memory"); SB();
  asm volatile("v_lshlrev_b32 %0, 16, %1" : "=v"(yq[3][2]) : "v"(ykv[3]) : ); SB();
  asm volatile("v_mfma_f32_16x16x32_bf16 %0, a[148:151], a[100:103], %0" : "+v"(ac[6]) :  : ); SB();
  asm volatile("ds_read2_b32 a[82:83], %0 offset0:146 offset1:147" :  : "v"(aa[2]) : "memory"); SB();
  asm volatile("v_and_b32 %0, 0xffff0000, %1" : "=v"(yq[3][3]) : "v"(ykv[3]) : ); SB();
  asm volatile("v_mfma_f32_16x16x32_bf16 %0, a[144:147], a[100:103], %0" : "+v"(ac[5]) :  : ); SB();
  asm volatile("ds_read2_b32 a[84:85], %0 offset0:16 offset1:17" :  : "v"(aa[2]) : "memory"); SB();
  asm volatile("v_fmac_f32 %0, %1, %2" : "+v"(yq[0][0]) : "v"(yq[2][0]), "v"(dD[2]) : ); SB();
  asm volatile("v_mfma_f32_16x16x32_bf16 %0, a[140:143], a[100:103], %0" : "+v"(ac[4]) :  : ); SB();
  yw = yw + lb + 32768 + w * 3072; asm volatile("" : "+v"(yw)); SB();
  asm volatile("v_mfma_f32_16x16x32_bf16 %0, a[152:155], a[100:103], %0" : "+v"(ac[7]) :  : ); SB();
  asm volatile("ds_read2_b32 a[86:87], %0 offset0:18 offset1:19" :  : "v"(aa[2]) : "memory"); SB();
  asm volatile("v_fmac_f32 %0, %1, %2" : "+v"(yq[0][1]) : "v"(yq[2][1]), "v"(dD[2]) : ); SB();
  asm volatile("v_mfma_f32_16x16x32_bf16 %0, a[136:139], a[100:103], %0" : "+v"(ac[3]) :  : ); SB();
  asm volatile("ds_read_b128 a[36:39], %0 offset:576" :  : "v"(bb) : "memory"); SB();
  asm volatile("v_fmac_f32 %0, %1, %2" : "+v"(yq[0][2]) : "v"(yq[2][2]), "v"(dD[2]) : ); SB();
  asm volatile("v_mfma_f32_16x16x32_bf16 %0, a[132:135], a[100:103], %0" : "+v"(ac[2]) :  : ); SB();
  asm volatile("ds_read2_b32 a[88:89], %0 offset0:144 offset1:145" :  : "v"(aa[3]) : "memory"); SB();
  asm volatile("v_fmac_f32 %0, %1, %2" : "+v"(yq[0][3]) : "v"(yq[2][3]), "v"(dD[2]) : ); SB();
  asm volatile("v_mfma_f32_16x16x32_bf16 %0, a[128:131], a[100:103], %0" : "+v"(ac[1]) :  : ); SB();
  ya = lb + 32768 + (4 * g4 + q4) * 128 + 8 * p4; asm volatile("" : "+v"(ya)); SB();
  asm volatile("v_mfma_f32_16x16x32_bf16 %0, a[156:159], a[96:99], %0" : "+v"(ac[7]) :  : ); SB();
  asm volatile("ds_read2_b32 a[90:91], %0 offset0:146 offset1:147" :  : "v"(aa[3]) : "memory"); SB();
  asm volatile("v_fmac_f32 %0, %1, %2" : "+v"(yq[1][0]) : "v"(yq[3][0]), "v"(dD[2]) : ); SB();
  asm volatile("v_mfma_f32_16x16x32_bf16 %0, a[152:155], a[96:99], %0" : "+v"(ac[6]) :  : ); SB();
  asm volatile("ds_read2_b32 a[92:93], %0 offset0:16 offset1:17" :  : "v"(aa[3]) : "memory"); SB();
  asm volatile("v_fmac_f32 %0, %1, %2" : "+v"(yq[1][1]) : "v"(yq[3][1]), "v"(dD[2]) : ); SB();
  asm volatile("v_mfma_f32_16x16x32_bf16 %0, a[148:151], a[96:99], %0" : "+v"(ac[5]) :  : ); SB();
  asm volatile("ds_read2_b32 a[94:95], %0 offset0:18 offset1:19" :  : "v"(aa[3]) : "memory"); SB();
  asm volatile("v_fmac_f32 %0, %1, %2" : "+v"(yq[1][2]) : "v"(yq[3][2]), "v"(dD[2]) : ); SB();
  asm volatile("v_mfma_f32_16x16x32_bf16 %0, a[144:147], a[96:99], %0" : "+v"(ac[4]) :  : ); SB();
  po = (2 * g4 + (q4 >> 1)) & 3; asm volatile("" : "+v"(po)); SB();
  asm volatile("v_mfma_f32_16x16x32_bf16 %0, a[140:143], a[96:99], %0" : "+v"(ac[3]) :  : ); SB();
  asm volatile("ds_read_b128 a[32:35], %0 offset:64" :  : "v"(bb) : "memory"); SB();
  asm volatile("v_fmac_f32 %0, %1, %2" : "+v"(yq[1][3]) : "v"(yq[3][3]), "v"(dD[2]) : ); SB();
  asm volatile("v_mfma_f32_16x16x32_bf16 %0, a[136:139], a[96:99], %0" : "+v"(ac[2]) :  : ); SB();
  ya = ya + ((w ^ po) << 5); asm volatile("" : "+v"(ya)); SB();
  asm volatile("v_mfma_f32_16x16x32_bf16 %0, a[132:135], a[96:99], %0" : "+v"(ac[1]) :  : ); SB();
  po = vX + 24 * (g4 & 1); asm volatile("" : "+v"(po)); SB();
  asm volatile("v_mfma_f32_16x16x32_bf16 %0, a[128:131], a[96:99], %0" : "+v"(ac[0]) :  : ); SB();
  asm volatile("v_cvt_pk_bf16_f32 %0, %1, %2" : "=v"(y2[0]) : "v"(yq[0][0]), "v"(yq[0][1]) : ); SB();
  asm volatile("v_mfma_f32_16x16x32_bf16 %0, a[64:67], a[60:63], %0" : "+v"(ac[7]) :  : ); SB();
  asm volatile("ds_read2_b32 a[128:129], %0 offset0:128 offset1:129" :  : "v"(aa[0]) : "memory"); SB();
  asm volatile("global_load_dwordx2 %0, %1, %2 offset:0" : "=&v"(xv[0]) : "v"(vX), "s"(x) : "memory"); SB();
  asm volatile("v_mfma_f32_16x16x32_bf16 %0, a[64:67], a[56:59], %0" : "+v"(ac[6]) :  : ); SB();
  asm volatile("ds_read2_b32 a[130:131], %0 offset0:130 offset1:131" :  : "v"(aa[0]) : "memory"); SB();
  asm volatile("global_load_dwordx2 %0, %1, %2 offset:32" : "=&v"(xv[1]) : "v"(vX), "s"(x) : "memory"); SB();
  asm volatile("v_mfma_f32_16x16x32_bf16 %0, a[64:67], a[52:55], %0" : "+v"(ac[5]) :  : ); SB();
  asm volatile("ds_read_b128 a[124:127], %0 offset:3584" :  : "v"(bb) : "memory"); SB();
  asm volatile("global_load_dwordx2 %0, %1, %2 offset:64" : "=&v"(xv[2]) : "v"(vX), "s"(x) : "memory"); SB();
  asm volatile("v_mfma_f32_16x16x32_bf16 %0, a[64:67], a[48:51], %0" : "+v"(ac[4]) :  : ); SB();
  asm volatile("v_cvt_pk_bf16_f32 %0, %1, %2" : "=v"(y2[1]) : "v"(yq[0][2]), "v"(yq[0][3]) : ); SB();
  asm volatile("v_mfma_f32_16x16x32_bf16 %0, a[68:71], a[56:59], %0" : "+v"(ac[7]) :  : ); SB();
  asm volatile("ds_read_b128 a[120:123], %0 offset:3072" :  : "v"(bb) : "memory"); SB();
  asm volatile("global_load_dwordx2 %0, %1, %2 offset:96" : "=&v"(xv[3]) : "v"(vX), "s"(x) : "memory"); SB();
  asm volatile("v_mfma_f32_16x16x32_bf16 %0, a[68:71], a[52:55], %0" : "+v"(ac[6]) :  : ); SB();
  asm volatile("ds_read_b128 a[96:99], %0 offset:0" :  : "v"(bb) : "memory"); SB();
  asm volatile("v_cvt_pk_bf16_f32 %0, %1, %2" : "=v"(y2[2]) : "v"(yq[1][0]), "v"(yq[1][1]) : ); SB();
  asm volatile("v_mfma_f32_16x16x32_bf16 %0, a[68:71], a[48:51], %0" : "+v"(ac[5]) :  : ); SB();
  asm volatile("ds_read_b128 a[100:103], %0 offset:512" :  : "v"(bb) : "memory"); SB();
  asm volatile("s_waitcnt lgkmcnt(6)\n\tv_mfma_f32_16x16x32_bf16 %0, a[68:71], a[44:47], %0" : "+v"(ac[4]) :  : ); SB();
  asm volatile("global_load_dwordx2 %0, %1, %2 offset:128" : "=&v"(xv[4]) : "v"(vX), "s"(x) : "memory"); SB();
  asm volatile("v_cvt_pk_bf16_f32 %0, %1, %2" : "=v"(y2[3]) : "v"(yq[1][2]), "v"(yq[1][3]) : ); SB();
  asm volatile("v_mfma_f32_16x16x32_bf16 %0, a[72:75], a[52:55], %0" : "+v"(ac[7]) :  : ); SB();
  asm volatile("ds_read2_b32 a[132:133], %0 offset0:0 offset1:1" :  : "v"(aa[0]) : "memory"); SB();
  asm volatile("v_min_u32 %0, -2, %0" : "+v"(y2[3]) :  : ); SB();
  asm volatile("v_mfma_f32_16x16x32_bf16 %0, a[72:75], a[48:51], %0" : "+v"(ac[6]) :  : ); SB();
  asm volatile("ds_read2_b32 a[134:135], %0 offset0:2 offset1:3" :  : "v"(aa[0]) : "memory"); SB();
  asm volatile("v_xnor_b32 %0, %0, %1" : "+v"(y2[3]) : "v"(ykv[3]) : ); SB();
  asm volatile("v_mfma_f32_16x16x32_bf16 %0, a[72:75], a[44:47], %0" : "+v"(ac[5]) :  : ); SB();
  asm volatile("ds_read_b128 a[116:119], %0 offset:2560" :  : "v"(bb) : "memory"); SB();
  asm volatile("v_mfma_f32_16x16x32_bf16 %0, a[64:67], a[44:47], %0" : "+v"(ac[3]) :  : ); SB();
  asm volatile("buffer_store_dwordx4 %1, %0, %2, %3 offen offset:0 sc1\n\ts_nop 1" :  : "v"(vF), "v"(y2), "s"(rWS), "s"(uso) : "memory"); SB();
  asm volatile("v_mfma_f32_16x16x32_bf16 %0, a[76:79], a[48:51], %0" : "+v"(ac[7]) :  : ); SB();
  asm volatile("ds_read_b128 a[104:107], %0 offset:1024" :  : "v"(bb) : "memory"); SB();
  asm volatile("v_mfma_f32_16x16x32_bf16 %0, a[76:79], a[44:47], %0" : "+v"(ac[6]) :  : ); SB();
  asm volatile("ds_read2_b32 a[136:137], %0 offset0:128 offset1:129" :  : "v"(aa[1]) : "memory"); SB();
  asm volatile("v_mfma_f32_16x16x32_bf16 %0, a[76:79], a[40:43], %0" : "+v"(ac[5]) :  : ); SB();
  asm volatile("ds_read2_b32 a[138:139], %0 offset0:130 offset1:131" :  : "v"(aa[1]) : "memory"); SB();
  asm volatile("v_mfma_f32_16x16x32_bf16 %0, a[72:75], a[40:43], %0" : "+v"(ac[4]) :  : ); SB();
  asm volatile("v_mfma_f32_16x16x32_bf16 %0, a[80:83], a[44:47], %0" : "+v"(ac[7]) :  : ); SB();
  asm volatile("ds_read_b128 a[108:111], %0 offset:1536" :  : "v"(bb) : "memory"); SB();
  asm volatile("v_mfma_f32_16x16x32_bf16 %0, a[80:83], a[40:43], %0" : "+v"(ac[6]) :  : ); SB();
  asm volatile("ds_read_b128 a[112:115], %0 offset:2048" :  : "v"(bb) : "memory"); SB();
  asm volatile("v_mfma_f32_16x16x32_bf16 %0, a[68:71], a[40:43], %0" : "+v"(ac[3]) :  : ); SB();
  asm volatile("ds_read2_b32 a[140:141], %0 offset0:0 offset1:1" :  : "v"(aa[1]) : "memory"); SB();
  asm volatile("v_mfma_f32_16x16x32_bf16 %0, a[64:67], a[40:43], %0" : "+v"(ac[2]) :  : ); SB();
  asm volatile("v_mfma_f32_16x16x32_bf16 %0, a[84:87], a[40:43], %0" : "+v"(ac[7]) :  : ); SB();
  asm volatile("ds_read2_b32 a[142:143], %0 offset0:2 offset1:3" :  : "v"(aa[1]) : "memory"); SB();
  asm volatile("v_mfma_f32_16x16x32_bf16 %0, a[84:87], a[36:39], %0" : "+v"(ac[6]) :  : ); SB();
  asm volatile("ds_read2_b32 a[144:145], %0 offset0:128 offset1:129" :  : "v"(aa[2]) : "memory"); SB();
  asm volatile("v_mfma_f32_16x16x32_bf16 %0, a[80:83], a[36:39], %0" : "+v"(ac[5]) :  : ); SB();
  asm volatile("ds_read2_b32 a[146:147], %0 offset0:130 offset1:131" :  : "v"(aa[2]) : "memory"); SB();
  asm volatile("v_mfma_f32_16x16x32_bf16 %0, a[76:79], a[36:39], %0" : "+v"(ac[4]) :  : ); SB();
  asm volatile("v_mfma_f32_16x16x32_bf16 %0, a[88:91], a[36:39], %0" : "+v"(ac[7]) :  : ); SB();
  asm volatile("ds_read2_b32 a[148:149], %0 offset0:0 offset1:1" :  : "v"(aa[2]) : "memory"); SB();
  asm volatile("v_mfma_f32_16x16x32_bf16 %0, a[72:75], a[36:39], %0" : "+v"(ac[3]) :  : ); SB();
  asm volatile("ds_read2_b32 a[150:151], %0 offset0:2 offset1:3" :  : "v"(aa[2]) : "memory"); SB();
  asm volatile("v_mfma_f32_16x16x32_bf16 %0, a[68:71], a[36:39], %0" : "+v"(ac[2]) :  : ); SB();
  asm volatile("ds_read2_b32 a[152:153], %0 offset0:128 offset1:129" :  : "v"(aa[3]) : "memory"); SB();
  asm volatile("v_mfma_f32_16x16x32_bf16 %0, a[64:67], a[36:39], %0" : "+v"(ac[1]) :  : ); SB();
  asm volatile("v_mfma_f32_16x16x32_bf16 %0, a[92:95], a[32:35], %0" : "+v"(ac[7]) :  : ); SB();
  asm volatile("ds_read2_b32 a[154:155], %0 offset0:130 offset1:131" :  : "v"(aa[3]) : "memory"); SB();
  asm volatile("v_mfma_f32_16x16x32_bf16 %0, a[88:91], a[32:35], %0" : "+v"(ac[6]) :  : ); SB();
  asm volatile("ds_read2_b32 a[156:157], %0 offset0:0 offset1:1" :  : "v"(aa[3]) : "memory"); SB();
  asm volatile("v_mfma_f32_16x16x32_bf16 %0, a[84:87], a[32:35], %0" : "+v"(ac[5]) :  : ); SB();
  asm volatile("ds_read2_b32 a[158:159], %0 offset0:2 offset1:3" :  : "v"(aa[3]) : "memory"); SB();
  asm volatile("v_mfma_f32_16x16x32_bf16 %0, a[80:83], a[32:35], %0" : "+v"(ac[4]) :  : ); SB();
  asm volatile("v_mfma_f32_16x16x32_bf16 %0, a[76:79], a[32:35], %0" : "+v"(ac[3]) :  : ); SB();
  asm volatile("v_mfma_f32_16x16x32_bf16 %0, a[72:75], a[32:35], %0" : "+v"(ac[2]) :  : ); SB();
  asm volatile("v_mfma_f32_16x16x32_bf16 %0, a[68:71], a[32:35], %0" : "+v"(ac[1]) :  : ); SB();
  asm volatile("v_mfma_f32_16x16x32_bf16 %0, a[64:67], a[32:35], %0" : "+v"(ac[0]) :  : ); SB();
  asm volatile("s_waitcnt lgkmcnt(4)\n\tv_mfma_f32_16x16x32_bf16 %0, a[128:131], a[124:127], %0" : "+v"(ac[7]) :  : ); SB();
  asm volatile("v_mfma_f32_16x16x32_bf16 %0, a[128:131], a[120:123], %0" : "+v"(ac[6]) :  : ); SB();
  asm volatile("v_mfma_f32_16x16x32_bf16 %0, a[128:131], a[96:99], %0" : "+v"(ac[0]) :  : ); SB();
  asm volatile("v_mfma_f32_16x16x32_bf16 %0, a[128:131], a[100:103], %0" : "+v"(ac[1]) :  : ); SB();
  asm volatile("v_mfma_f32_16x16x32_bf16 %0, a[132:135], a[120:123], %0" : "+v"(ac[7]) :  : ); SB();
  asm volatile("v_mfma_f32_16x16x32_bf16 %0, a[132:135], a[116:119], %0" : "+v"(ac[6]) :  : ); SB();
  asm volatile("v_mfma_f32_16x16x32_bf16 %0, a[128:131], a[104:107], %0" : "+v"(ac[2]) :  : ); SB();
  asm volatile("s_waitcnt vmcnt(6)\n\t.Lc_%=:\n\tv_cmp_ne_u64 vcc, %2, %1\n\ts_cbranch_vccnz .Lr_%=\n\t.Ld_%=:\n\t.subsection 1\n\t.Lr_%=:\n\tv_add_u32 %0, -1, %0\n\ts_nop 1\n\tv_readfirstlane_b32 vcc_hi, %0\n\ts_nop 3\n\ts_cmp_lt_i32 vcc_hi, 1\n\ts_cbranch_scc1 .Ld_%=\n\ts_sleep 2\n\ts_mov_b32 vcc_lo, 0\n\tbuffer_load_dwordx2 %1, %3, %4, vcc_lo offen sc1\n\ts_waitcnt vmcnt(0)\n\ts_branch .Lc_%=\n\t.subsection 0" : "+v"(tmo), "+v"(fh) : "s"(tg), "v"(vFH), "s"(rWS) : "memory", "vcc", "scc"); SB();
  asm volatile("buffer_load_dwordx4 v[164:167], %0, %1, %2 offen offset:0 sc1" :  : "v"(vHr), "s"(rWS), "s"(hso[2]) : "memory"); SB();
  asm volatile("v_mfma_f32_16x16x32_bf16 %0, a[132:135], a[96:99], %0" : "+v"(ac[1]) :  : ); SB();
  asm volatile("buffer_load_dwordx4 v[168:171], %0, %1, %2 offen offset:0 sc1" :  : "v"(vHr), "s"(rWS), "s"(hso[4]) : "memory"); SB();
  asm volatile("v_mfma_f32_16x16x32_bf16 %0, a[136:139], a[116:119], %0" : "+v"(ac[7]) :  : ); SB();
  asm volatile("buffer_load_dwordx4 v[172:175], %0, %1, %2 offen offset:0 sc1" :  : "v"(vHr), "s"(rWS), "s"(hso[6]) : "memory"); SB();
  asm volatile("v_mfma_f32_16x16x32_bf16 %0, a[128:131], a[108:111], %0" : "+v"(ac[3]) :  : ); SB();
  asm volatile("ds_write_b128 %0, %1 offset:0" :  : "v"(dd), "v"(ac[0]) : "memory"); SB();
  asm volatile("v_mfma_f32_16x16x32_bf16 %0, a[132:135], a[100:103], %0" : "+v"(ac[2]) :  : ); SB();
  asm volatile("buffer_load_dwordx4 v[160:163], %0, %1, %2 offen offset:0 sc1" :  : "v"(vHr), "s"(rWS), "s"(hso[0]) : "memory"); SB();
  asm volatile("v_mfma_f32_16x16x32_bf16 %0, a[128:131], a[112:115], %0" : "+v"(ac[4]) :  : ); SB();
  asm volatile("ds_write_b128 %0, %1 offset:1024" :  : "v"(dd), "v"(ac[1]) : "memory"); SB();
  asm volatile("v_mfma_f32_16x16x32_bf16 %0, a[140:143], a[112:115], %0" : "+v"(ac[7]) :  : ); SB();
  asm volatile("buffer_load_dwordx4 v[180:183], %0, %1, %2 offen offset:1024 sc1" :  : "v"(vHr), "s"(rWS), "s"(hso[2]) : "memory"); SB();
  asm volatile("v_mfma_f32_16x16x32_bf16 %0, a[132:135], a[104:107], %0" : "+v"(ac[3]) :  : ); SB();
  asm volatile("v_mfma_f32_16x16x32_bf16 %0, a[136:139], a[96:99], %0" : "+v"(ac[2]) :  : ); SB();
  asm volatile("buffer_load_dwordx4 v[184:187], %0, %1, %2 offen offset:1024 sc1" :  : "v"(vHr), "s"(rWS), "s"(hso[4]) : "memory"); SB();
  asm volatile("v_mfma_f32_16x16x32_bf16 %0, a[132:135], a[108:111], %0" : "+v"(ac[4]) :  : ); SB();
  asm volatile("v_mfma_f32_16x16x32_bf16 %0, a[128:131], a[116:119], %0" : "+v"(ac[5]) :  : ); SB();
  asm volatile("v_mfma_f32_16x16x32_bf16 %0, a[136:139], a[100:103], %0" : "+v"(ac[3]) :  : ); SB();
  asm volatile("v_mfma_f32_16x16x32_bf16 %0, a[136:139], a[112:115], %0" : "+v"(ac[6]) :  : ); SB();
  asm volatile("buffer_load_dwordx4 v[188:191], %0, %1, %2 offen offset:1024 sc1" :  : "v"(vHr), "s"(rWS), "s"(hso[6]) : "memory"); SB();
  asm volatile("v_mfma_f32_16x16x32_bf16 %0, a[136:139], a[104:107], %0" : "+v"(ac[4]) :  : ); SB();
  asm volatile("ds_write_b128 %0, %1 offset:2048" :  : "v"(dd), "v"(ac[2]) : "memory"); SB();
  asm volatile("v_mfma_f32_16x16x32_bf16 %0, a[132:135], a[112:115], %0" : "+v"(ac[5]) :  : ); SB();
  asm volatile("buffer_load_dwordx4 v[176:179], %0, %1, %2 offen offset:1024 sc1" :  : "v"(vHr), "s"(rWS), "s"(hso[0]) : "memory"); SB();
  asm volatile("v_mfma_f32_16x16x32_bf16 %0, a[140:143], a[96:99], %0" : "+v"(ac[3]) :  : ); SB();
  asm volatile("v_mfma_f32_16x16x32_bf16 %0, a[140:143], a[108:111], %0" : "+v"(ac[6]) :  : ); SB();
  asm volatile("v_mfma_f32_16x16x32_bf16 %0, a[140:143], a[100:103], %0" : "+v"(ac[4]) :  : ); SB();
  asm volatile("v_mfma_f32_16x16x32_bf16 %0, a[136:139], a[108:111], %0" : "+v"(ac[5]) :  : ); SB();
  asm volatile("buffer_load_dwordx4 v[196:199], %0, %1, %2 offen offset:0 sc1" :  : "v"(vHr), "s"(rWS), "s"(hso[3]) : "memory"); SB();
  asm volatile("v_mfma_f32_16x16x32_bf16 %0, a[144:147], a[108:111], %0" : "+v"(ac[7]) :  : ); SB();
  asm volatile("ds_write_b128 %0, %1 offset:3072" :  : "v"(dd), "v"(ac[3]) : "memory"); SB();
  asm volatile("v_mfma_f32_16x16x32_bf16 %0, a[144:147], a[104:107], %0" : "+v"(ac[6]) :  : ); SB();
  asm volatile("buffer_load_dwordx4 v[200:203], %0, %1, %2 offen offset:0 sc1" :  : "v"(vHr), "s"(rWS), "s"(hso[5]) : "memory"); SB();
  asm volatile("v_mfma_f32_16x16x32_bf16 %0, a[144:147], a[96:99], %0" : "+v"(ac[4]) :  : ); SB();
  asm volatile("v_mfma_f32_16x16x32_bf16 %0, a[140:143], a[104:107], %0" : "+v"(ac[5]) :  : ); SB();
  asm volatile("v_mfma_f32_16x16x32_bf16 %0, a[148:151], a[104:107], %0" : "+v"(ac[7]) :  : ); SB();
  asm volatile("v_mfma_f32_16x16x32_bf16 %0, a[148:151], a[100:103], %0" : "+v"(ac[6]) :  : ); SB();
  asm volatile("buffer_load_dwordx4 v[204:207], %0, %1, %2 offen offset:0 sc1" :  : "v"(vHr), "s"(rWS), "s"(hso[7]) : "memory"); SB();
  asm volatile("v_mfma_f32_16x16x32_bf16 %0, a[144:147], a[100:103], %0" : "+v"(ac[5]) :  : ); SB();
  asm volatile("ds_write_b128 %0, %1 offset:4096" :  : "v"(dd), "v"(ac[4]) : "memory"); SB();
  asm volatile("s_waitcnt lgkmcnt(2)\n\tv_mfma_f32_16x16x32_bf16 %0, a[152:155], a[100:103], %0" : "+v"(ac[7]) :  : ); SB();
  asm volatile("buffer_load_dwordx4 v[192:195], %0, %1, %2 offen offset:0 sc1" :  : "v"(vHr), "s"(rWS), "s"(hso[1]) : "memory"); SB();
  asm volatile("v_mfma_f32_16x16x32_bf16 %0, a[152:155], a[96:99], %0" : "+v"(ac[6]) :  : ); SB();
  asm volatile("v_mfma_f32_16x16x32_bf16 %0, a[148:151], a[96:99], %0" : "+v"(ac[5]) :  : ); SB();
  asm volatile("v_mfma_f32_16x16x32_bf16 %0, a[156:159], a[96:99], %0" : "+v"(ac[7]) :  : ); SB();
  asm volatile("s_nop 5" : "+v"(ac[5]), "+v"(ac[6]), "+v"(ac[7]) :  : ); SB();
  asm volatile("ds_write_b128 %0, %1 offset:6144" :  : "v"(dd), "v"(ac[6]) : "memory"); SB();
  asm volatile("ds_write_b128 %0, %1 offset:5120" :  : "v"(dd), "v"(ac[5]) : "memory"); SB();
  asm volatile("ds_write_b128 %0, %1 offset:7168" :  : "v"(dd), "v"(ac[7]) : "memory"); SB();
  ST(18);
  f32x4 yp[2][4], o3[4][5];
  u32x4 yk[2], yo[2];
  unsigned vtmp;
  asm volatile("ds_read_b128 %0, %1 offset:0" : "=&v"(yk[0]) : "v"(ykr) : "memory"); SB();
  asm volatile("ds_read_b128 %0, %1 offset:12928" : "=&v"(yk[1]) : "v"(ykr) : "memory"); SB();
  asm volatile("s_waitcnt lgkmcnt(0)\n\ts_barrier" :  :  : "memory"); SB();
  asm volatile("ds_read_b128 %0, %1 offset:0" : "=&v"(yp[0][0]) : "v"(ydr) : "memory"); SB();
  asm volatile("ds_read_b128 %0, %1 offset:256" : "=&v"(yp[0][1]) : "v"(ydr) : "memory"); SB();
  asm volatile("ds_read_b128 %0, %1 offset:8192" : "=&v"(yp[0][2]) : "v"(ydr) : "memory"); SB();
  asm volatile("ds_read_b128 %0, %1 offset:8448" : "=&v"(yp[0][3]) : "v"(ydr) : "memory"); SB();
  asm volatile("ds_read_b128 %0, %1 offset:16384" : "=&v"(yp[1][0]) : "v"(ydr) : "memory"); SB();
  asm volatile("ds_read_b128 %0, %1 offset:16640" : "=&v"(yp[1][1]) : "v"(ydr) : "memory"); SB();
  asm volatile("ds_read_b128 %0, %1 offset:24576" : "=&v"(yp[1][2]) : "v"(ydr) : "memory"); SB();
  asm volatile("ds_read_b128 %0, %1 offset:24832" : "=&v"(yp[1][3]) : "v"(ydr) : "memory"); SB();
  asm volatile("buffer_load_dwordx4 v[212:215], %0, %1, %2 offen offset:1024 sc1" :  : "v"(vHr), "s"(rWS), "s"(hso[3]) : "memory"); SB();
  asm volatile("buffer_load_dwordx4 v[216:219], %0, %1, %2 offen offset:1024 sc1" :  : "v"(vHr), "s"(rWS), "s"(hso[5]) : "memory"); SB();
  asm volatile("buffer_load_dwordx4 v[220:223], %0, %1, %2 offen offset:1024 sc1" :  : "v"(vHr), "s"(rWS), "s"(hso[7]) : "memory"); SB();
  asm volatile("" : "+v"(yk[0]), "+v"(yk[1]) :  : ); SB();
  f32x2 yd[2][4];
#pragma unroll
  for (int n = 0; n < 8; ++n) { const float Dv = dD[n >> 2]; yd[n >> 2][n & 3] = unpk(yk[n >> 2][n & 3]) * (f32x2){Dv, Dv}; asm volatile("" : "+v"(yd[n >> 2][n & 3])); }
  SB();
  asm volatile("s_waitcnt lgkmcnt(0)" : "+v"(yp[0][0]), "+v"(yp[0][1]), "+v"(yp[0][2]), "+v"(yp[0][3]) :  : "memory"); SB();
  {
    const f32x4 s0 = yp[0][0] + yp[0][2], s1 = yp[0][1] + yp[0][3];
    yo[0] = (u32x4){pk2((f32x2){s0[0], s0[1]} + yd[0][0]), pk2((f32x2){s0[2], s0[3]} + yd[0][1]), pk2((f32x2){s1[0], s1[1]} + yd[0][2]), ~(nohole(pk2((f32x2){s1[2], s1[3]} + yd[0][3])) ^ yk[0][3])};
    SB();
  }
  asm volatile("buffer_store_dwordx4 %1, %0, %2, %3 offen offset:0 sc1\n\ts_nop 1" :  : "v"(vF), "v"(yo[0]), "s"(rWS), "s"(W_KV + 0) : "memory"); SB();
  asm volatile("" : "+v"(yp[1][0]), "+v"(yp[1][1]), "+v"(yp[1][2]), "+v"(yp[1][3]) :  : "memory"); SB();
  {
    const f32x4 s0 = yp[1][0] + yp[1][2], s1 = yp[1][1] + yp[1][3];
    yo[1] = (u32x4){pk2((f32x2){s0[0], s0[1]} + yd[1][0]), pk2((f32x2){s0[2], s0[3]} + yd[1][1]), pk2((f32x2){s1[0], s1[1]} + yd[1][2]), ~(nohole(pk2((f32x2){s1[2], s1[3]} + yd[1][3])) ^ yk[1][3])};
    SB();
  }
  asm volatile("buffer_store_dwordx4 %1, %0, %2, %3 offen offset:0 sc1\n\ts_nop 1" :  : "v"(vF), "v"(yo[1]), "s"(rWS), "s"(W_KV + 4096) : "memory"); SB();
  ST(19);
  ST(20);
  f32x2 xf[5][2], sv[5][2];
  float rd, mr;
  asm volatile("s_waitcnt vmcnt(13)" :  :  : "memory"); SB();
  asm volatile("v_mfma_f32_16x16x32_bf16 %0, a[160:163], v[164:167], 0" : "=&v"(o3[1][0]) :  : ); SB();
  asm volatile("buffer_load_dwordx4 v[208:211], %0, %1, %2 offen offset:1024 sc1" :  : "v"(vHr), "s"(rWS), "s"(hso[1]) : "memory"); SB();
  asm volatile("v_mfma_f32_16x16x32_bf16 %0, a[164:167], v[164:167], 0" : "=&v"(o3[1][1]) :  : ); SB();
  asm volatile("buffer_load_dwordx4 v[228:231], %0, %1, %2 offen offset:2048 sc1" :  : "v"(vHr), "s"(rWS), "s"(hso[2]) : "memory"); SB();
  asm volatile("v_mfma_f32_16x16x32_bf16 %0, a[168:171], v[164:167], 0" : "=&v"(o3[1][2]) :  : ); SB();
  asm volatile("v_mfma_f32_16x16x32_bf16 %0, a[172:175], v[164:167], 0" : "=&v"(o3[1][3]) :  : ); SB();
  asm volatile("buffer_load_dwordx4 v[232:235], %0, %1, %2 offen offset:2048 sc1" :  : "v"(vHr), "s"(rWS), "s"(hso[4]) : "memory"); SB();
  asm volatile("v_mfma_f32_16x16x32_bf16 %0, a[176:179], v[164:167], 0" : "=&v"(o3[1][4]) :  : ); SB();
  asm volatile("v_mfma_f32_16x16x32_bf16 %0, a[160:163], v[168:171], 0" : "=&v"(o3[2][0]) :  : ); SB();
  asm volatile("v_mfma_f32_16x16x32_bf16 %0, a[164:167], v[168:171], 0" : "=&v"(o3[2][1]) :  : ); SB();
  asm volatile("buffer_load_dwordx4 v[236:239], %0, %1, %2 offen offset:2048 sc1" :  : "v"(vHr), "s"(rWS), "s"(hso[6]) : "memory"); SB();
  asm volatile("v_mfma_f32_16x16x32_bf16 %0, a[168:171], v[168:171], 0" : "=&v"(o3[2][2]) :  : ); SB();
  asm volatile("v_mfma_f32_16x16x32_bf16 %0, a[172:175], v[168:171], 0" : "=&v"(o3[2][3]) :  : ); SB();
  asm volatile("v_mfma_f32_16x16x32_bf16 %0, a[176:179], v[168:171], 0" : "=&v"(o3[2][4]) :  : ); SB();
  asm volatile("v_mfma_f32_16x16x32_bf16 %0, a[160:163], v[172:175], 0" : "=&v"(o3[3][0]) :  : ); SB();
  asm volatile("buffer_load_dwordx4 v[224:227], %0, %1, %2 offen offset:2048 sc1" :  : "v"(vHr), "s"(rWS), "s"(hso[0]) : "memory"); SB();
  asm volatile("v_mfma_f32_16x16x32_bf16 %0, a[164:167], v[172:175], 0" : "=&v"(o3[3][1]) :  : ); SB();
  asm volatile("v_mfma_f32_16x16x32_bf16 %0, a[168:171], v[172:175], 0" : "=&v"(o3[3][2]) :  : ); SB();
  asm volatile("v_mfma_f32_16x16x32_bf16 %0, a[172:175], v[172:175], 0" : "=&v"(o3[3][3]) :  : ); SB();
  asm volatile("buffer_load_dwordx4 v[244:247], %0, %1, %2 offen offset:2048 sc1" :  : "v"(vHr), "s"(rWS), "s"(hso[3]) : "memory"); SB();
  asm volatile("v_mfma_f32_16x16x32_bf16 %0, a[176:179], v[172:175], 0" : "=&v"(o3[3][4]) :  : ); SB();
  asm volatile("s_waitcnt vmcnt(15)" :  :  : "memory"); SB();
  asm volatile("v_mfma_f32_16x16x32_bf16 %0, a[180:183], v[180:183], %0" : "+v"(o3[1][0]) :  : ); SB();
  asm volatile("v_rcp_f32 %0, %1" : "=v"(rd) : "v"(xd[0]) : ); SB();
  asm volatile("v_mfma_f32_16x16x32_bf16 %0, a[184:187], v[180:183], %0" : "+v"(o3[1][1]) :  : ); SB();
  asm volatile("" : "+v"(xv[0]), "+v"(xv[1]), "+v"(xv[2]), "+v"(xv[3]), "+v"(xv[4]) :  : "memory"); SB();
  asm volatile("v_mul_f32 %0, -%1, %2" : "=v"(mr) : "v"(xm[0]), "v"(rd) : ); SB();
  asm volatile("v_mfma_f32_16x16x32_bf16 %0, a[188:191], v[180:183], %0" : "+v"(o3[1][2]) :  : ); SB();
  asm volatile("buffer_load_dwordx4 v[248:251], %0, %1, %2 offen offset:2048 sc1" :  : "v"(vHr), "s"(rWS), "s"(hso[5]) : "memory"); SB();
  asm volatile("v_lshlrev_b32 %0, 16, %1" : "=v"(xf[0][0][0]) : "v"(xv[0][0]) : ); SB();
  asm volatile("v_mfma_f32_16x16x32_bf16 %0, a[192:195], v[180:183], %0" : "+v"(o3[1][3]) :  : ); SB();
  asm volatile("v_and_b32 %0, 0xffff0000, %1" : "=v"(xf[0][0][1]) : "v"(xv[0][0]) : ); SB();
  asm volatile("v_mfma_f32_16x16x32_bf16 %0, a[196:199], v[180:183], %0" : "+v"(o3[1][4]) :  : ); SB();
  asm volatile("v_lshlrev_b32 %0, 16, %1" : "=v"(xf[0][1][0]) : "v"(xv[0][1]) : ); SB();
  asm volatile("v_mfma_f32_16x16x32_bf16 %0, a[180:183], v[184:187], %0" : "+v"(o3[2][0]) :  : ); SB();
  asm volatile("buffer_load_dwordx4 v[252:255], %0, %1, %2 offen offset:2048 sc1" :  : "v"(vHr), "s"(rWS), "s"(hso[7]) : "memory"); SB();
  asm volatile("v_and_b32 %0, 0xffff0000, %1" : "=v"(xf[0][1][1]) : "v"(xv[0][1]) : ); SB();
  asm volatile("v_mfma_f32_16x16x32_bf16 %0, a[184:187], v[184:187], %0" : "+v"(o3[2][1]) :  : ); SB();
  asm volatile("v_lshlrev_b32 %0, 16, %1" : "=v"(xf[1][0][0]) : "v"(xv[1][0]) : ); SB();
  asm volatile("v_mfma_f32_16x16x32_bf16 %0, a[188:191], v[184:187], %0" : "+v"(o3[2][2]) :  : ); SB();
  asm volatile("v_and_b32 %0, 0xffff0000, %1" : "=v"(xf[1][0][1]) : "v"(xv[1][0]) : ); SB();
  asm volatile("v_mfma_f32_16x16x32_bf16 %0, a[192:195], v[184:187], %0" : "+v"(o3[2][3]) :  : ); SB();
  asm volatile("v_lshlrev_b32 %0, 16, %1" : "=v"(xf[1][1][0]) : "v"(xv[1][1]) : ); SB();
  asm volatile("v_mfma_f32_16x16x32_bf16 %0, a[196:199], v[184:187], %0" : "+v"(o3[2][4]) :  : ); SB();
  asm volatile("buffer_load_dwordx4 v[240:243], %0, %1, %2 offen offset:2048 sc1" :  : "v"(vHr), "s"(rWS), "s"(hso[1]) : "memory"); SB();
  asm volatile("v_and_b32 %0, 0xffff0000, %1" : "=v"(xf[1][1][1]) : "v"(xv[1][1]) : ); SB();
  asm volatile("v_mfma_f32_16x16x32_bf16 %0, a[180:183], v[188:191], %0" : "+v"(o3[3][0]) :  : ); SB();
  asm volatile("v_lshlrev_b32 %0, 16, %1" : "=v"(xf[2][0][0]) : "v"(xv[2][0]) : ); SB();
  asm volatile("v_mfma_f32_16x16x32_bf16 %0, a[184:187], v[188:191], %0" : "+v"(o3[3][1]) :  : ); SB();
  asm volatile("v_and_b32 %0, 0xffff0000, %1" : "=v"(xf[2][0][1]) : "v"(xv[2][0]) : ); SB();
  asm volatile("v_mfma_f32_16x16x32_bf16 %0, a[188:191], v[188:191], %0" : "+v"(o3[3][2]) :  : ); SB();
  asm volatile("v_lshlrev_b32 %0, 16, %1" : "=v"(xf[2][1][0]) : "v"(xv[2][1]) : ); SB();
  asm volatile("v_mfma_f32_16x16x32_bf16 %0, a[192:195], v[188:191], %0" : "+v"(o3[3][3]) :  : ); SB();
  asm volatile("v_and_b32 %0, 0xffff0000, %1" : "=v"(xf[2][1][1]) : "v"(xv[2][1]) : ); SB();
  asm volatile("v_mfma_f32_16x16x32_bf16 %0, a[196:199], v[188:191], %0" : "+v"(o3[3][4]) :  : ); SB();
  asm volatile("v_lshlrev_b32 %0, 16, %1" : "=v"(xf[3][0][0]) : "v"(xv[3][0]) : ); SB();
  asm volatile("s_waitcnt vmcnt(14)" :  :  : "memory"); SB();
  asm volatile("v_mfma_f32_16x16x32_bf16 %0, a[200:203], v[196:199], %0" : "+v"(o3[1][0]) :  : ); SB();
  asm volatile("v_and_b32 %0, 0xffff0000, %1" : "=v"(xf[3][0][1]) : "v"(xv[3][0]) : ); SB();
  asm volatile("v_mfma_f32_16x16x32_bf16 %0, a[204:207], v[196:199], %0" : "+v"(o3[1][1]) :  : ); SB();
  asm volatile("v_lshlrev_b32 %0, 16, %1" : "=v"(xf[3][1][0]) : "v"(xv[3][1]) : ); SB();
  asm volatile("v_mfma_f32_16x16x32_bf16 %0, a[208:211], v[196:199], %0" : "+v"(o3[1][2]) :  : ); SB();
  asm volatile("v_and_b32 %0, 0xffff0000, %1" : "=v"(xf[3][1][1]) : "v"(xv[3][1]) : ); SB();
  asm volatile("v_mfma_f32_16x16x32_bf16 %0, a[212:215], v[196:199], %0" : "+v"(o3[1][3]) :  : ); SB();
  asm volatile("v_lshlrev_b32 %0, 16, %1" : "=v"(xf[4][0][0]) : "v"(xv[4][0]) : ); SB();
  asm volatile("v_mfma_f32_16x16x32_bf16 %0, a[216:219], v[196:199], %0" : "+v"(o3[1][4]) :  : ); SB();
  asm volatile("v_and_b32 %0, 0xffff0000, %1" : "=v"(xf[4][0][1]) : "v"(xv[4][0]) : ); SB();
  asm volatile("v_mfma_f32_16x16x32_bf16 %0, a[200:203], v[200:203], %0" : "+v"(o3[2][0]) :  : ); SB();
  asm volatile("v_lshlrev_b32 %0, 16, %1" : "=v"(xf[4][1][0]) : "v"(xv[4][1]) : ); SB();
  asm volatile("v_mfma_f32_16x16x32_bf16 %0, a[204:207], v[200:203], %0" : "+v"(o3[2][1]) :  : ); SB();
  asm volatile("v_and_b32 %0, 0xffff0000, %1" : "=v"(xf[4][1][1]) : "v"(xv[4][1]) : ); SB();
  asm volatile("v_mfma_f32_16x16x32_bf16 %0, a[208:211], v[200:203], %0" : "+v"(o3[2][2]) :  : ); SB();
  asm volatile("v_fma_f32 %0, %1, %2, %3" : "=v"(sv[0][0][0]) : "v"(xf[0][0][0]), "v"(rd), "v"(mr) : ); SB();
  asm volatile("v_mfma_f32_16x16x32_bf16 %0, a[212:215], v[200:203], %0" : "+v"(o3[2][3]) :  : ); SB();
  asm volatile("v_fma_f32 %0, %1, %2, %3" : "=v"(sv[0][0][1]) : "v"(xf[0][0][1]), "v"(rd), "v"(mr) : ); SB();
  asm volatile("v_mfma_f32_16x16x32_bf16 %0, a[216:219], v[200:203], %0" : "+v"(o3[2][4]) :  : ); SB();
  asm volatile("v_fma_f32 %0, %1, %2, %3" : "=v"(sv[0][1][0]) : "v"(xf[0][1][0]), "v"(rd), "v"(mr) : ); SB();
  asm volatile("v_mfma_f32_16x16x32_bf16 %0, a[200:203], v[204:207], %0" : "+v"(o3[3][0]) :  : ); SB();
  asm volatile("v_fma_f32 %0, %1, %2, %3" : "=v"(sv[0][1][1]) : "v"(xf[0][1][1]), "v"(rd), "v"(mr) : ); SB();
  asm volatile("v_mfma_f32_16x16x32_bf16 %0, a[204:207], v[204:207], %0" : "+v"(o3[3][1]) :  : ); SB();
  asm volatile("v_fma_f32 %0, %1, %2, %3" : "=v"(sv[1][0][0]) : "v"(xf[1][0][0]), "v"(rd), "v"(mr) : ); SB();
  asm volatile("v_mfma_f32_16x16x32_bf16 %0, a[208:211], v[204:207], %0" : "+v"(o3[3][2]) :  : ); SB();
  asm volatile("v_fma_f32 %0, %1, %2, %3" : "=v"(sv[1][0][1]) : "v"(xf[1][0][1]), "v"(rd), "v"(mr) : ); SB();
  asm volatile("v_mfma_f32_16x16x32_bf16 %0, a[212:215], v[204:207], %0" : "+v"(o3[3][3]) :  : ); SB();
  asm volatile("v_fma_f32 %0, %1, %2, %3" : "=v"(sv[1][1][0]) : "v"(xf[1][1][0]), "v"(rd), "v"(mr) : ); SB();
  asm volatile("v_mfma_f32_16x16x32_bf16 %0, a[216:219], v[204:207], %0" : "+v"(o3[3][4]) :  : ); SB();
  asm volatile("v_fma_f32 %0, %1, %2, %3" : "=v"(sv[1][1][1]) : "v"(xf[1][1][1]), "v"(rd), "v"(mr) : ); SB();
  asm volatile("s_waitcnt vmcnt(8)" :  :  : "memory"); SB();
  asm volatile("v_mfma_f32_16x16x32_bf16 %0, a[220:223], v[212:215], %0" : "+v"(o3[1][0]) :  : ); SB();
  asm volatile("v_fma_f32 %0, %1, %2, %3" : "=v"(sv[2][0][0]) : "v"(xf[2][0][0]), "v"(rd), "v"(mr) : ); SB();
  asm volatile("v_mfma_f32_16x16x32_bf16 %0, a[224:227], v[212:215], %0" : "+v"(o3[1][1]) :  : ); SB();
  asm volatile("v_fma_f32 %0, %1, %2, %3" : "=v"(sv[2][0][1]) : "v"(xf[2][0][1]), "v"(rd), "v"(mr) : ); SB();
  asm volatile("v_mfma_f32_16x16x32_bf16 %0, a[228:231], v[212:215], %0" : "+v"(o3[1][2]) :  : ); SB();
  asm volatile("v_fma_f32 %0, %1, %2, %3" : "=v"(sv[2][1][0]) : "v"(xf[2][1][0]), "v"(rd), "v"(mr) : ); SB();
  asm volatile("v_mfma_f32_16x16x32_bf16 %0, a[232:235], v[212:215], %0" : "+v"(o3[1][3]) :  : ); SB();
  asm volatile("v_fma_f32 %0, %1, %2, %3" : "=v"(sv[2][1][1]) : "v"(xf[2][1][1]), "v"(rd), "v"(mr) : ); SB();
  asm volatile("v_mfma_f32_16x16x32_bf16 %0, a[236:239], v[212:215], %0" : "+v"(o3[1][4]) :  : ); SB();
  asm volatile("v_fma_f32 %0, %1, %2, %3" : "=v"(sv[3][0][0]) : "v"(xf[3][0][0]), "v"(rd), "v"(mr) : ); SB();
  asm volatile("v_mfma_f32_16x16x32_bf16 %0, a[220:223], v[216:219], %0" : "+v"(o3[2][0]) :  : ); SB();
  asm volatile("v_fma_f32 %0, %1, %2, %3" : "=v"(sv[3][0][1]) : "v"(xf[3][0][1]), "v"(rd), "v"(mr) : ); SB();
  asm volatile("v_mfma_f32_16x16x32_bf16 %0, a[224:227], v[216:219], %0" : "+v"(o3[2][1]) :  : ); SB();
  asm volatile("v_fma_f32 %0, %1, %2, %3" : "=v"(sv[3][1][0]) : "v"(xf[3][1][0]), "v"(rd), "v"(mr) : ); SB();
  asm volatile("v_mfma_f32_16x16x32_bf16 %0, a[228:231], v[216:219], %0" : "+v"(o3[2][2]) :  : ); SB();
  asm volatile("v_fma_f32 %0, %1, %2, %3" : "=v"(sv[3][1][1]) : "v"(xf[3][1][1]), "v"(rd), "v"(mr) : ); SB();
  asm volatile("v_mfma_f32_16x16x32_bf16 %0, a[232:235], v[216:219], %0" : "+v"(o3[2][3]) :  : ); SB();
  asm volatile("v_fma_f32 %0, %1, %2, %3" : "=v"(sv[4][0][0]) : "v"(xf[4][0][0]), "v"(rd), "v"(mr) : ); SB();
  asm volatile("v_mfma_f32_16x16x32_bf16 %0, a[236:239], v[216:219], %0" : "+v"(o3[2][4]) :  : ); SB();
  asm volatile("v_fma_f32 %0, %1, %2, %3" : "=v"(sv[4][0][1]) : "v"(xf[4][0][1]), "v"(rd), "v"(mr) : ); SB();
  asm volatile("v_mfma_f32_16x16x32_bf16 %0, a[220:223], v[220:223], %0" : "+v"(o3[3][0]) :  : ); SB();
  asm volatile("v_fma_f32 %0, %1, %2, %3" : "=v"(sv[4][1][0]) : "v"(xf[4][1][0]), "v"(rd), "v"(mr) : ); SB();
  asm volatile("v_mfma_f32_16x16x32_bf16 %0, a[224:227], v[220:223], %0" : "+v"(o3[3][1]) :  : ); SB();
  asm volatile("v_fma_f32 %0, %1, %2, %3" : "=v"(sv[4][1][1]) : "v"(xf[4][1][1]), "v"(rd), "v"(mr) : ); SB();
  asm volatile("v_mfma_f32_16x16x32_bf16 %0, a[228:231], v[220:223], %0" : "+v"(o3[3][2]) :  : ); SB();
  asm volatile("v_mfma_f32_16x16x32_bf16 %0, a[232:235], v[220:223], %0" : "+v"(o3[3][3]) :  : ); SB();
  asm volatile("v_mfma_f32_16x16x32_bf16 %0, a[236:239], v[220:223], %0" : "+v"(o3[3][4]) :  : ); SB();
  ST(30);
  asm volatile("s_waitcnt vmcnt(0)\n\t.Lc_%=:\n\tv_max3_u32 %1, v231, v235, v239\n\tv_max3_u32 %1, %1, v227, v247\n\tv_max3_u32 %1, %1, v251, v255\n\tv_max3_u32 %1, %1, v243, v243\n\tv_cmp_eq_u32 vcc, -1, %1\n\ts_cbranch_vccnz .Lr_%=\n\t.Ld_%=:\n\t.subsection 1\n\t.Lr_%=:\n\tv_add_u32 %0, -1, %0\n\ts_nop 1\n\tv_readfirstlane_b32 vcc_hi, %0\n\ts_nop 3\n\ts_cmp_lt_i32 vcc_hi, 1\n\ts_cbranch_scc1 .Ld_%=\n\ts_sleep 2\n\tbuffer_load_dwordx4 v[228:231], %2, %3, %6 offen offset:2048 sc1\n\tbuffer_load_dwordx4 v[232:235], %2, %3, %8 offen offset:2048 sc1\n\tbuffer_load_dwordx4 v[236:239], %2, %3, %10 offen offset:2048 sc1\n\tbuffer_load_dwordx4 v[224:227], %2, %3, %4 offen offset:2048 sc1\n\tbuffer_load_dwordx4 v[244:247], %2, %3, %7 offen offset:2048 sc1\n\tbuffer_load_dwordx4 v[248:251], %2, %3, %9 offen offset:2048 sc1\n\tbuffer_load_dwordx4 v[252:255], %2, %3, %11 offen offset:2048 sc1\n\tbuffer_load_dwordx4 v[240:243], %2, %3, %5 offen offset:2048 sc1\n\ts_waitcnt vmcnt(0)\n\ts_branch .Lc_%=\n\t.subsection 0" : "+v"(tmo), "=&v"(vtmp) : "v"(vHr), "s"(rWS), "s"(hso[0]), "s"(hso[1]), "s"(hso[2]), "s"(hso[3]), "s"(hso[4]), "s"(hso[5]), "s"(hso[6]), "s"(hso[7]) : "memory", "vcc", "scc"); SB();
  asm volatile("v_mfma_f32_16x16x32_bf16 %0, a[240:243], v[228:231], %0" : "+v"(o3[1][0]) :  : ); SB();
  asm volatile("v_mfma_f32_16x16x32_bf16 %0, a[244:247], v[228:231], %0" : "+v"(o3[1][1]) :  : ); SB();
  asm volatile("v_mfma_f32_16x16x32_bf16 %0, a[248:251], v[228:231], %0" : "+v"(o3[1][2]) :  : ); SB();
  asm volatile("v_mfma_f32_16x16x32_bf16 %0, a[252:255], v[228:231], %0" : "+v"(o3[1][3]) :  : ); SB();
  asm volatile("v_mfma_f32_16x16x32_bf16 %0, a[0:3], v[228:231], %0" : "+v"(o3[1][4]) :  : ); SB();
  asm volatile("v_mfma_f32_16x16x32_bf16 %0, a[4:7], v[244:247], %0" : "+v"(o3[1][0]) :  : ); SB();
  asm volatile("v_mfma_f32_16x16x32_bf16 %0, a[8:11], v[244:247], %0" : "+v"(o3[1][1]) :  : ); SB();
  asm volatile("v_mfma_f32_16x16x32_bf16 %0, a[12:15], v[244:247], %0" : "+v"(o3[1][2]) :  : ); SB();
  asm volatile("v_mfma_f32_16x16x32_bf16 %0, a[16:19], v[244:247], %0" : "+v"(o3[1][3]) :  : ); SB();
  asm volatile("v_mfma_f32_16x16x32_bf16 %0, a[20:23], v[244:247], %0" : "+v"(o3[1][4]) :  : ); SB();
  asm volatile("v_mfma_f32_16x16x32_bf16 %0, a[240:243], v[232:235], %0" : "+v"(o3[2][0]) :  : ); SB();
  asm volatile("ds_write_b128 %0, %1 offset:0" :  : "v"(pw3), "v"(o3[1][0]) : "memory"); SB();
  asm volatile("v_mfma_f32_16x16x32_bf16 %0, a[244:247], v[232:235], %0" : "+v"(o3[2][1]) :  : ); SB();
  asm volatile("v_mfma_f32_16x16x32_bf16 %0, a[248:251], v[232:235], %0" : "+v"(o3[2][2]) :  : ); SB();
  asm volatile("v_mfma_f32_16x16x32_bf16 %0, a[252:255], v[232:235], %0" : "+v"(o3[2][3]) :  : ); SB();
  asm volatile("ds_write_b128 %0, %1 offset:1024" :  : "v"(pw3), "v"(o3[1][1]) : "memory"); SB();
  asm volatile("v_mfma_f32_16x16x32_bf16 %0, a[0:3], v[232:235], %0" : "+v"(o3[2][4]) :  : ); SB();
  asm volatile("v_mfma_f32_16x16x32_bf16 %0, a[4:7], v[248:251], %0" : "+v"(o3[2][0]) :  : ); SB();
  asm volatile("buffer_load_dwordx4 v[164:167], %0, %1, %2 offen offset:0 sc1" :  : "v"(vKV), "s"(rWS), "s"(0u) : "memory"); SB();
  asm volatile("v_mfma_f32_16x16x32_bf16 %0, a[8:11], v[248:251], %0" : "+v"(o3[2][1]) :  : ); SB();
  asm volatile("buffer_load_dwordx4 v[168:171], %0, %1, %2 offen offset:0 sc1" :  : "v"(vKV), "s"(rWS), "s"(w3k[0]) : "memory"); SB();
  asm volatile("v_mfma_f32_16x16x32_bf16 %0, a[12:15], v[248:251], %0" : "+v"(o3[2][2]) :  : ); SB();
  asm volatile("buffer_load_dwordx4 v[172:175], %0, %1, %2 offen offset:0 sc1" :  : "v"(vKV), "s"(rWS), "s"(w3k[1]) : "memory"); SB();
  asm volatile("v_mfma_f32_16x16x32_bf16 %0, a[16:19], v[248:251], %0" : "+v"(o3[2][3]) :  : ); SB();
  asm volatile("ds_write_b128 %0, %1 offset:2048" :  : "v"(pw3), "v"(o3[1][2]) : "memory"); SB();
  asm volatile("v_mfma_f32_16x16x32_bf16 %0, a[20:23], v[248:251], %0" : "+v"(o3[2][4]) :  : ); SB();
  asm volatile("v_mfma_f32_16x16x32_bf16 %0, a[240:243], v[236:239], %0" : "+v"(o3[3][0]) :  : ); SB();
  asm volatile("v_mfma_f32_16x16x32_bf16 %0, a[244:247], v[236:239], %0" : "+v"(o3[3][1]) :  : ); SB();
  asm volatile("ds_write_b128 %0, %1 offset:3072" :  : "v"(pw3), "v"(o3[1][3]) : "memory"); SB();
  asm volatile("v_mfma_f32_16x16x32_bf16 %0, a[248:251], v[236:239], %0" : "+v"(o3[3][2]) :  : ); SB();
  asm volatile("v_mfma_f32_16x16x32_bf16 %0, a[252:255], v[236:239], %0" : "+v"(o3[3][3]) :  : ); SB();
  asm volatile("v_mfma_f32_16x16x32_bf16 %0, a[0:3], v[236:239], %0" : "+v"(o3[3][4]) :  : ); SB();
  asm volatile("ds_write_b128 %0, %1 offset:4096" :  : "v"(pw3), "v"(o3[1][4]) : "memory"); SB();
  asm volatile("v_mfma_f32_16x16x32_bf16 %0, a[4:7], v[252:255], %0" : "+v"(o3[3][0]) :  : ); SB();
  asm volatile("v_mfma_f32_16x16x32_bf16 %0, a[8:11], v[252:255], %0" : "+v"(o3[3][1]) :  : ); SB();
  asm volatile("v_mfma_f32_16x16x32_bf16 %0, a[12:15], v[252:255], %0" : "+v"(o3[3][2]) :  : ); SB();
  asm volatile("ds_write_b128 %0, %1 offset:5120" :  : "v"(pw3), "v"(o3[2][0]) : "memory"); SB();
  asm volatile("v_mfma_f32_16x16x32_bf16 %0, a[16:19], v[252:255], %0" : "+v"(o3[3][3]) :  : ); SB();
  asm volatile("v_mfma_f32_16x16x32_bf16 %0, a[20:23], v[252:255], %0" : "+v"(o3[3][4]) :  : ); SB();
  asm volatile("v_mfma_f32_16x16x32_bf16 %0, a[160:163], v[160:163], 0" : "=&v"(o3[0][0]) :  : ); SB();
  asm volatile("ds_write_b128 %0, %1 offset:6144" :  : "v"(pw3), "v"(o3[2][1]) : "memory"); SB();
  asm volatile("v_mfma_f32_16x16x32_bf16 %0, a[164:167], v[160:163], 0" : "=&v"(o3[0][1]) :  : ); SB();
  asm volatile("v_mfma_f32_16x16x32_bf16 %0, a[168:171], v[160:163], 0" : "=&v"(o3[0][2]) :  : ); SB();
  asm volatile("v_mfma_f32_16x16x32_bf16 %0, a[172:175], v[160:163], 0" : "=&v"(o3[0][3]) :  : ); SB();
  asm volatile("ds_write_b128 %0, %1 offset:7168" :  : "v"(pw3), "v"(o3[2][2]) : "memory"); SB();
  asm volatile("v_mfma_f32_16x16x32_bf16 %0, a[176:179], v[160:163], 0" : "=&v"(o3[0][4]) :  : ); SB();
  asm volatile("v_mfma_f32_16x16x32_bf16 %0, a[180:183], v[176:179], %0" : "+v"(o3[0][0]) :  : ); SB();
  asm volatile("v_mfma_f32_16x16x32_bf16 %0, a[184:187], v[176:179], %0" : "+v"(o3[0][1]) :  : ); SB();
  asm volatile("ds_write_b128 %0, %1 offset:8192" :  : "v"(pw3), "v"(o3[2][3]) : "memory"); SB();
  asm volatile("v_mfma_f32_16x16x32_bf16 %0, a[188:191], v[176:179], %0" : "+v"(o3[0][2]) :  : ); SB();
  asm volatile("v_mfma_f32_16x16x32_bf16 %0, a[192:195], v[176:179], %0" : "+v"(o3[0][3]) :  : ); SB();
  asm volatile("v_mfma_f32_16x16x32_bf16 %0, a[196:199], v[176:179], %0" : "+v"(o3[0][4]) :  : ); SB();
  asm volatile("ds_write_b128 %0, %1 offset:9216" :  : "v"(pw3), "v"(o3[2][4]) : "memory"); SB();
  asm volatile("v_mfma_f32_16x16x32_bf16 %0, a[200:203], v[192:195], %0" : "+v"(o3[0][0]) :  : ); SB();
  asm volatile("v_mfma_f32_16x16x32_bf16 %0, a[204:207], v[192:195], %0" : "+v"(o3[0][1]) :  : ); SB();
  asm volatile("v_mfma_f32_16x16x32_bf16 %0, a[208:211], v[192:195], %0" : "+v"(o3[0][2]) :  : ); SB();
  asm volatile("ds_write_b128 %0, %1 offset:10240" :  : "v"(pw3), "v"(o3[3][0]) : "memory"); SB();
  asm volatile("v_mfma_f32_16x16x32_bf16 %0, a[212:215], v[192:195], %0" : "+v"(o3[0][3]) :  : ); SB();
  asm volatile("v_mfma_f32_16x16x32_bf16 %0, a[216:219], v[192:195], %0" : "+v"(o3[0][4]) :  : ); SB();
  asm volatile("v_mfma_f32_16x16x32_bf16 %0, a[220:223], v[208:211], %0" : "+v"(o3[0][0]) :  : ); SB();
  asm volatile("ds_write_b128 %0, %1 offset:11264" :  : "v"(pw3), "v"(o3[3][1]) : "memory"); SB();
  asm volatile("v_mfma_f32_16x16x32_bf16 %0, a[224:227], v[208:211], %0" : "+v"(o3[0][1]) :  : ); SB();
  asm volatile("v_mfma_f32_16x16x32_bf16 %0, a[228:231], v[208:211], %0" : "+v"(o3[0][2]) :  : ); SB();
  asm volatile("v_mfma_f32_16x16x32_bf16 %0, a[232:235], v[208:211], %0" : "+v"(o3[0][3]) :  : ); SB();
  asm volatile("ds_write_b128 %0, %1 offset:12288" :  : "v"(pw3), "v"(o3[3][2]) : "memory"); SB();
  asm volatile("v_mfma_f32_16x16x32_bf16 %0, a[236:239], v[208:211], %0" : "+v"(o3[0][4]) :  : ); SB();
  asm volatile("v_mfma_f32_16x16x32_bf16 %0, a[240:243], v[224:227], %0" : "+v"(o3[0][0]) :  : ); SB();
  asm volatile("v_mfma_f32_16x16x32_bf16 %0, a[244:247], v[224:227], %0" : "+v"(o3[0][1]) :  : ); SB();
  asm volatile("ds_write_b128 %0, %1 offset:13312" :  : "v"(pw3), "v"(o3[3][3]) : "memory"); SB();
  asm volatile("v_mfma_f32_16x16x32_bf16 %0, a[248:251], v[224:227], %0" : "+v"(o3[0][2]) :  : ); SB();
  asm volatile("v_mfma_f32_16x16x32_bf16 %0, a[252:255], v[224:227], %0" : "+v"(o3[0][3]) :  : ); SB();
  asm volatile("v_mfma_f32_16x16x32_bf16 %0, a[0:3], v[224:227], %0" : "+v"(o3[0][4]) :  : ); SB();
  asm volatile("ds_write_b128 %0, %1 offset:14336" :  : "v"(pw3), "v"(o3[3][4]) : "memory"); SB();
  asm volatile("v_mfma_f32_16x16x32_bf16 %0, a[4:7], v[240:243], %0" : "+v"(o3[0][0]) :  : ); SB();
  asm volatile("v_mfma_f32_16x16x32_bf16 %0, a[8:11], v[240:243], %0" : "+v"(o3[0][1]) :  : ); SB();
  asm volatile("v_mfma_f32_16x16x32_bf16 %0, a[12:15], v[240:243], %0" : "+v"(o3[0][2]) :  : ); SB();
  asm volatile("v_mfma_f32_16x16x32_bf16 %0, a[16:19], v[240:243], %0" : "+v"(o3[0][3]) :  : ); SB();
  asm volatile("v_mfma_f32_16x16x32_bf16 %0, a[20:23], v[240:243], %0" : "+v"(o3[0][4]) :  : ); SB();
  asm volatile("s_waitcnt lgkmcnt(0)\n\ts_barrier" :  :  : "memory"); SB();
  asm volatile("ds_read_b128 %0, %1 offset:0" : "=&v"(o3[1][0]) : "v"(p3[0]) : "memory"); SB();
  asm volatile("ds_read_b128 %0, %1 offset:5120" : "=&v"(o3[2][0]) : "v"(p3[1]) : "memory"); SB();
  asm volatile("ds_read_b128 %0, %1 offset:10240" : "=&v"(o3[3][0]) : "v"(p3[2]) : "memory"); SB();
  asm volatile("ds_read_b128 %0, %1 offset:1024" : "=&v"(o3[1][1]) : "v"(p3[0]) : "memory"); SB();
  asm volatile("ds_read_b128 %0, %1 offset:6144" : "=&v"(o3[2][1]) : "v"(p3[1]) : "memory"); SB();
  asm volatile("ds_read_b128 %0, %1 offset:11264" : "=&v"(o3[3][1]) : "v"(p3[2]) : "memory"); SB();
  asm volatile("ds_read_b128 %0, %1 offset:2048" : "=&v"(o3[1][2]) : "v"(p3[0]) : "memory"); SB();
  asm volatile("ds_read_b128 %0, %1 offset:7168" : "=&v"(o3[2][2]) : "v"(p3[1]) : "memory"); SB();
  asm volatile("ds_read_b128 %0, %1 offset:12288" : "=&v"(o3[3][2]) : "v"(p3[2]) : "memory"); SB();
  asm volatile("ds_read_b128 %0, %1 offset:3072" : "=&v"(o3[1][3]) : "v"(p3[0]) : "memory"); SB();
  asm volatile("ds_read_b128 %0, %1 offset:8192" : "=&v"(o3[2][3]) : "v"(p3[1]) : "memory"); SB();
  asm volatile("ds_read_b128 %0, %1 offset:13312" : "=&v"(o3[3][3]) : "v"(p3[2]) : "memory"); SB();
  asm volatile("ds_read_b128 %0, %1 offset:4096" : "=&v"(o3[1][4]) : "v"(p3[0]) : "memory"); SB();
  asm volatile("ds_read_b128 %0, %1 offset:9216" : "=&v"(o3[2][4]) : "v"(p3[1]) : "memory"); SB();
  asm volatile("ds_read_b128 %0, %1 offset:14336" : "=&v"(o3[3][4]) : "v"(p3[2]) : "memory"); SB();
  ST(21);
  u32x2 yr[5];
  asm volatile("s_waitcnt lgkmcnt(9)" : "+v"(o3[0][0]), "+v"(o3[0][1]), "+v"(o3[1][0]), "+v"(o3[1][1]), "+v"(o3[2][0]), "+v"(o3[2][1]), "+v"(o3[3][0]), "+v"(o3[3][1]) :  : "memory"); SB();
#pragma unroll
  for (int j = 0; j <= 1; ++j) {
    const f32x4 o4 = (o3[0][j] + o3[1][j]) + (o3[2][j] + o3[3][j]);
#pragma unroll
    for (int h = 0; h < 2; ++h) {
      const f32x2 o = {o4[2 * h], o4[2 * h + 1]};
      const f32x2 a = o * (f32x2){-1.4426950f, -1.4426950f};
      const f32x2 e = (f32x2){__builtin_amdgcn_exp2f(a[0]), __builtin_amdgcn_exp2f(a[1])} + (f32x2){1.0f, 1.0f};
      const f32x2 r = {__builtin_amdgcn_rcpf(e[0]), __builtin_amdgcn_rcpf(e[1])};
      sv[j][h] = (sv[j][h] * o) * r;
    }
  }
  asm volatile("" : "+v"(sv[0][0]), "+v"(sv[0][1]), "+v"(sv[1][0]), "+v"(sv[1][1]));
  SB();
  asm volatile("s_waitcnt lgkmcnt(0)" : "+v"(o3[0][2]), "+v"(o3[0][3]), "+v"(o3[1][2]), "+v"(o3[1][3]), "+v"(o3[2][2]), "+v"(o3[2][3]), "+v"(o3[3][2]), "+v"(o3[3][3]) :  : "memory"); SB();
#pragma unroll
  for (int j = 2; j <= 3; ++j) {
    const f32x4 o4 = (o3[0][j] + o3[1][j]) + (o3[2][j] + o3[3][j]);
#pragma unroll
    for (int h = 0; h < 2; ++h) {
      const f32x2 o = {o4[2 * h], o4[2 * h + 1]};
      const f32x2 a = o * (f32x2){-1.4426950f, -1.4426950f};
      const f32x2 e = (f32x2){__builtin_amdgcn_exp2f(a[0]), __builtin_amdgcn_exp2f(a[1])} + (f32x2){1.0f, 1.0f};
      const f32x2 r = {__builtin_amdgcn_rcpf(e[0]), __builtin_amdgcn_rcpf(e[1])};
      sv[j][h] = (sv[j][h] * o) * r;
    }
  }
  asm volatile("" : "+v"(sv[2][0]), "+v"(sv[2][1]), "+v"(sv[3][0]), "+v"(sv[3][1]));
  SB();
  ST(22);
  asm volatile("s_waitcnt vmcnt(0)\n\t.Lc_%=:\n\tv_xnor_b32 v167, v167, %6\n\tv_xnor_b32 v171, v171, %7\n\tv_xnor_b32 v175, v175, %8\n\tv_max3_u32 %1, v167, v171, v175\n\tv_cmp_eq_u32 vcc, -1, %1\n\ts_cbranch_vccnz .Lr_%=\n\t.Ld_%=:\n\t.subsection 1\n\t.Lr_%=:\n\tv_add_u32 %0, -1, %0\n\ts_nop 1\n\tv_readfirstlane_b32 vcc_hi, %0\n\ts_nop 3\n\ts_cmp_lt_i32 vcc_hi, 1\n\ts_cbranch_scc1 .Ld_%=\n\ts_sleep 2\n\tbuffer_load_dwordx4 v[164:167], %2, %3, 0 offen sc1\n\tbuffer_load_dwordx4 v[168:171], %2, %3, %4 offen sc1\n\tbuffer_load_dwordx4 v[172:175], %2, %3, %5 offen sc1\n\ts_waitcnt vmcnt(0)\n\ts_branch .Lc_%=\n\t.subsection 0" : "+v"(tmo), "=&v"(vtmp) : "v"(vKV), "s"(rWS), "s"(w3k[0]), "s"(w3k[1]), "v"(kz[0]), "v"(kz[1]), "v"(kz[2]) : "memory", "vcc", "scc"); SB();
  ST(31);
  asm volatile("ds_write_b128 %0, v[164:167] offset:0" :  : "v"(yw) : "memory"); SB();
  asm volatile("ds_write_b128 %0, v[168:171] offset:1024" :  : "v"(yw) : "memory"); SB();
  asm volatile("ds_write_b128 %0, v[172:175] offset:2048" :  : "v"(yw) : "memory"); SB();
  asm volatile("s_waitcnt lgkmcnt(0)\n\ts_barrier" :  :  : "memory"); SB();
  asm volatile("ds_read_b64_tr_b16 %0, %1 offset:0" : "=&v"(yr[0]) : "v"(ya) : "memory"); SB();
  asm volatile("ds_read_b64_tr_b16 %0, %1 offset:2048" : "=&v"(yr[1]) : "v"(ya) : "memory"); SB();
  asm volatile("ds_read_b64_tr_b16 %0, %1 offset:4096" : "=&v"(yr[2]) : "v"(ya) : "memory"); SB();
  asm volatile("ds_read_b64_tr_b16 %0, %1 offset:6144" : "=&v"(yr[3]) : "v"(ya) : "memory"); SB();
  asm volatile("ds_read_b64_tr_b16 %0, %1 offset:8192" : "=&v"(yr[4]) : "v"(ya) : "memory"); SB();
  asm volatile("" : "+v"(o3[0][4]), "+v"(o3[1][4]), "+v"(o3[2][4]), "+v"(o3[3][4]) :  : "memory"); SB();
#pragma unroll
  for (int j = 4; j <= 4; ++j) {
    const f32x4 o4 = (o3[0][j] + o3[1][j]) + (o3[2][j] + o3[3][j]);
#pragma unroll
    for (int h = 0; h < 2; ++h) {
      const f32x2 o = {o4[2 * h], o4[2 * h + 1]};
      const f32x2 a = o * (f32x2){-1.4426950f, -1.4426950f};
      const f32x2 e = (f32x2){__builtin_amdgcn_exp2f(a[0]), __builtin_amdgcn_exp2f(a[1])} + (f32x2){1.0f, 1.0f};
      const f32x2 r = {__builtin_amdgcn_rcpf(e[0]), __builtin_amdgcn_rcpf(e[1])};
      sv[j][h] = (sv[j][h] * o) * r;
    }
  }
  asm volatile("" : "+v"(sv[4][0]), "+v"(sv[4][1]));
  SB();
  asm volatile("s_waitcnt lgkmcnt(0)" : "+v"(yr[0]), "+v"(yr[1]), "+v"(yr[2]), "+v"(yr[3]), "+v"(yr[4]) :  : "memory"); SB();
  {
    unsigned pk[5][2];
#pragma unroll
    for (int n = 0; n < 10; ++n) pk[n >> 1][n & 1] = pk2(__builtin_elementwise_fma(sv[n >> 1][n & 1], unpk(yr[n >> 1][n & 1]), xf[n >> 1][n & 1]));
#pragma unroll
    for (int q = 0; q < 2; ++q) {
      const auto s0 = __builtin_amdgcn_permlane16_swap(pk[2 * q][0], pk[2 * q + 1][0], false, false);
      const auto s1 = __builtin_amdgcn_permlane16_swap(pk[2 * q][1], pk[2 * q + 1][1], false, false);
      asm volatile("s_nop 1\n\tglobal_store_dwordx4 %0, %1, %2 offset:%3 " :: "v"(po), "v"((u32x4){s0[0], s1[0], s0[1], s1[1]}), "s"(out), "i"(64 * q) : "memory");
    }
    asm volatile("global_store_dwordx2 %0, %1, %2 offset:128 " :: "v"(vX), "v"((u32x2){pk[4][0], pk[4][1]}), "s"(out) : "memory");
  }
  ST(23);
  ST_OUT();
  }}}
}

extern "C" __global__ void k_end() { asm volatile(".subsection 2\n.Lcode_end:\n\ts_endpgm\n\t.subsection 0"); }

extern "C" void kernel_launch(void* const* d_in, const int* in_sizes, int n_in,
                              void* d_out, int out_size, void* d_ws, size_t ws_size,
                              hipStream_t stream) {
  (void)hipFuncSetAttribute((const void*)k_all, hipFuncAttributeMaxDynamicSharedMemorySize, 163840);
  k_all<<<256, 256, 163840, stream>>>((const char*)d_in[0], (const char*)d_in[1], (const char*)d_in[2], (const char*)d_in[3], (const char*)d_in[4],
                                      (const char*)d_in[5], (const char*)d_in[7], (const char*)d_in[8], (const char*)d_in[9], (const char*)d_in[10],
                                      (char*)d_out, (char*)d_ws);
}
```

```cpp
#include <hip/hip_runtime.h>
#include <stdint.h>

typedef unsigned short u16;
typedef __attribute__((ext_vector_type(4))) float f32x4;
typedef __attribute__((ext_vector_type(2))) float f32x2;
typedef __attribute__((ext_vector_type(4))) unsigned int u32x4;
typedef __attribute__((ext_vector_type(2))) unsigned int u32x2;
typedef __attribute__((ext_vector_type(2))) __bf16 bf16x2n;
typedef __attribute__((ext_vector_type(8))) unsigned long long u64x8;
typedef __attribute__((ext_vector_type(4))) unsigned long long u64x4;
#define LDS_AS __attribute__((address_space(3)))
#define GAS __attribute__((address_space(1)))
#define SB() __builtin_amdgcn_sched_barrier(0)
#define DI __device__ __forceinline__

#define SGA
#define ST_DECL do {} while (0)
#define ST(i) do {} while (0)
#define ST_OUT() do {} while (0)

constexpr int S_ = 2048, D_ = 640;
constexpr unsigned HOLE = 0xFFFFFFFFu;
constexpr int W_ST = 4096, W_FZ = 768 << 10, W_FK = W_FZ + 8192, W_FH = W_FK + 8192, W_KV = 1 << 20, W_Z2 = 4 << 20, W_HH = 11 << 20, W_WO = 14 << 20;

#define wm (w >> 1)
#define wn (w & 1)
#define g4 (lane >> 4)
#define l15 (lane & 15)
#define q4 ((lane >> 2) & 3)
#define p4 (lane & 3)
#define l16 (lane * 16)
#define nch (3 - (rb >> 4))
#define ch0 (cb * 80 + rb * 2 + min(rb, 16u))
#define c2o ((nch - 2) * 2)
#define c2s (8192u + ((rb >> 4) << 30))
#define w3s ((w + 1) >> 2 << 30)
#define vZ (W_Z2 + ((rb * 2 + wm) * 64 + cb * 6 + wn * 3) * 1024 + l16)
#define vH (W_HH + ((rb * 4 + w) * 24 + cb * 3) * 1024 + l16)

DI unsigned pack2(float lo, float hi) {
  f32x2 v = {lo, hi};
  return __builtin_bit_cast(unsigned, __builtin_convertvector(v, bf16x2n));
}
DI float lo16(unsigned w) { return __uint_as_float(w << 16); }
DI float hi16(unsigned w) { return __uint_as_float(w & 0xFFFF0000u); }
DI float rbf(float f) { return lo16(pack2(f, f)); }
DI f32x2 unpk(unsigned w) { f32x2 r = {lo16(w), hi16(w)}; return r; }
DI unsigned pk2(f32x2 v) { return pack2(v[0], v[1]); }
DI unsigned nohole(unsigned a) { return a < HOLE - 1 ? a : HOLE - 1; }
template <int CTRL> DI float dppf(float v) {
  return __builtin_bit_cast(float, __builtin_amdgcn_update_dpp(0, __builtin_bit_cast(int, v), CTRL, 0xF, 0xF, true));
}
DI float red16(float v) {
  const auto a = __builtin_amdgcn_permlane16_swap(__float_as_uint(v), __float_as_uint(v), false, false);
  return __uint_as_float(a[0]) + __uint_as_float(a[1]);
}
DI float red32(float v) {
  const auto c = __builtin_amdgcn_permlane32_swap(__float_as_uint(v), __float_as_uint(v), false, false);
  return __uint_as_float(c[0]) + __uint_as_float(c[1]);
}
DI float sel32(float a, float b) {
  const auto c = __builtin_amdgcn_permlane32_swap(__float_as_uint(a), __float_as_uint(b), false, false);
  return __uint_as_float(c[0]) + __uint_as_float(c[1]);
}
DI unsigned m10(unsigned a) { return a >= 10 ? a - 10 : a; }
DI unsigned lds_addr(const void* p) { return (unsigned)(uintptr_t)(LDS_AS const char*)p; }

__global__ __launch_bounds__(256, 1) __attribute__((amdgpu_num_vgpr(160))) SGA void k_all(
    const char*, const char*, const char*, const char*, const char*, const char*, const char*, const char*, const char*, const char*, char*, char*) {
  extern __shared__ __attribute__((aligned(16))) char smem[];
  ST_DECL;
  ST(0);
  u64x8 ka0; u64x4 ka1;
  asm volatile("s_load_dwordx16 %0, %2, 0x0\n\ts_load_dwordx8 %1, %2, 0x40" : "=&s"(ka0), "=&s"(ka1) : "s"(__builtin_amdgcn_kernarg_segment_ptr()) : "memory"); SB();
  const unsigned tid = threadIdx.x, lane0 = tid & 63, w0 = __builtin_amdgcn_readfirstlane(tid >> 6);
  const unsigned b = blockIdx.x, xcd = b & 7, idx = b >> 3;
  const unsigned rb0 = 8 * (xcd >> 1) + (idx >> 2), cb0 = 4 * (xcd & 1) + (idx & 3);
  const unsigned lb = lds_addr(smem);
  const unsigned lane = lane0, w = w0, rb = rb0, cb = cb0;
  {
    unsigned long long pc; unsigned clen;
    asm volatile("s_getpc_b64 %0\n.Lpc_here_%=:\n\ts_mov_b32 %1, .Lcode_end-.Lpc_here_%=" : "=s"(pc), "=s"(clen));
    const unsigned o = min((((lane & 3) * 4 + w) * 32 + idx) * 128, clen - 4u) & ~3u;
    asm volatile("s_mov_b64 exec, 15\n\tglobal_load_dword v254, %0, %1\n\ts_mov_b64 exec, -1" :: "v"(o), "s"(pc) : "memory");
    SB();
  }
  unsigned voA = (rb * 64 + w * 16) * 1280 + __umul24(lane >> 3, 1280) + (((lane & 7) ^ (lane >> 3)) << 4), voB[3];
#pragma unroll
  for (int i = 0; i < 3; ++i) { const unsigned r = __umul24(i * 64 + lane, 171) >> 11; voB[i] = w * 49152 + cb * 192 + __umul24(r, 2880) + ((lane ^ ((r >> 3) << 1)) << 4); }
  unsigned mX = lb + w * 2048, mW = lb + w * 3072, mA;
  const unsigned rot = (((rb + 2 * cb + (cb >> 2)) & 7) * 10) >> 3;
  unsigned kx[10], kw_[10], voA2 = voA + 9216;
  auto kofs = [&](int s) {
    asm volatile("s_add_u32 %0, %2, %3\n\ts_cmp_ge_u32 %0, 10\n\ts_cselect_b32 %1, 10, 0\n\ts_sub_u32 %0, %0, %1\n\ts_mul_i32 %1, %0, 0x30000\n\ts_lshl_b32 %0, %0, 7"
                 : "=&s"(kx[s]), "=&s"(kw_[s]) : "s"(rot), "i"(s) : "scc");
  };
  kofs(0); kofs(1);
  asm volatile("" : "+v"(voA), "+v"(voA2), "+v"(voB[0]), "+v"(voB[1]), "+v"(voB[2]));
  asm volatile("" : "+s"(mX), "+s"(mW)); SB();
  asm volatile("s_waitcnt lgkmcnt(0)" : "+s"(ka0), "+s"(ka1) :: "memory"); SB();
  const char *x = (const char*)ka0[0], *kin = (const char*)ka0[1], *vin = (const char*)ka0[2], *shift_k = (const char*)ka0[3], *ssm_k = (const char*)ka0[4];
  const char *ssm_D = (const char*)ka0[5], *w_in = (const char*)ka0[6], *w_sp = (const char*)ka0[7], *b_sp = (const char*)ka1[0], *w_out = (const char*)ka1[1];
  char *out = (char*)ka1[2], *ws = (char*)ka1[3];
  const auto rX = __builtin_amdgcn_make_buffer_rsrc((void*)x, 0, S_ * D_ * 2, 0x00020000);
  const auto rW = __builtin_amdgcn_make_buffer_rsrc((void*)(w_in + 1536), 0, 640 * 3072 - 1536, 0x00020000);
  asm volatile("; named registers" :  :  : "v255", "a255"); SB();
  asm volatile("s_add_u32 m0, %0, 81920" :  : "s"(mX) : "memory", "scc"); SB();
  asm volatile("s_nop 0\n\tbuffer_load_dwordx4 %0, %1, %2 offen lds" :  : "v"(voA), "s"(rX), "s"(kx[0]) : "memory"); SB();
  asm volatile("buffer_load_dwordx4 %0, %1, %2 offen offset:1024 lds" :  : "v"(voA2), "s"(rX), "s"(kx[0]) : "memory"); SB();
  asm volatile("s_add_u32 m0, %0, 0" :  : "s"(mW) : "memory", "scc"); SB();
  asm volatile("s_nop 0\n\tbuffer_load_dwordx4 %0, %1, %2 offen lds" :  : "v"(voB[0]), "s"(rW), "s"(kw_[0]) : "memory"); SB();
  asm volatile("buffer_load_dwordx4 %0, %1, %2 offen offset:1024 lds" :  : "v"(voB[1]), "s"(rW), "s"(kw_[0]) : "memory"); SB();
  asm volatile("buffer_load_dwordx4 %0, %1, %2 offen offset:2048 lds" :  : "v"(voB[2]), "s"(rW), "s"(kw_[0]) : "memory"); SB();
  asm volatile("s_add_u32 m0, %0, 90112" :  : "s"(mX) : "memory", "scc"); SB();
  asm volatile("s_nop 0\n\tbuffer_load_dwordx4 %0, %1, %2 offen lds" :  : "v"(voA), "s"(rX), "s"(kx[1]) : "memory"); SB();
  asm volatile("buffer_load_dwordx4 %0, %1, %2 offen offset:1024 lds" :  : "v"(voA2), "s"(rX), "s"(kx[1]) : "memory"); SB();
  kofs(2); kofs(3); kofs(4); kofs(5); kofs(6); kofs(7); kofs(8); kofs(9);
  const auto rWS = __builtin_amdgcn_make_buffer_rsrc((void*)ws, 0, 32 << 20, 0x00020000);
  const auto rW1 = __builtin_amdgcn_make_buffer_rsrc((void*)w_in, 0, 640 * 3072, 0x00020000);
  mA = lb + w * 8192; asm volatile("" : "+s"(mA));
  { unsigned lane = lane0, w = w0, rb = rb0, cb = cb0;
  asm volatile("" : "+v"(lane)); asm volatile("" : "+s"(w), "+s"(rb), "+s"(cb)); SB();

  unsigned ao[8];
  u32x2 kp[5], kc[5], vv[5], sw[5], wt[2], ko[5];
  u32x4 kq[3];
  f32x2 kt[2], kf[2];
  unsigned ku[5];
  unsigned aX[2][2], aW[2][2][3];
#pragma unroll
  for (int n = 0; n < 4; ++n) aX[n >> 1][n & 1] = lb + 98304 + (wm * 32 + ((n & 1) ^ wn) * 16 + l15) * 128 + (((((n >> 1) ^ wm) * 4 + g4) ^ (l15 & 7)) << 4);
#pragma unroll
  for (int n = 0; n < 6; ++n) {
    aW[0][n / 3][n % 3] = lb + ((n / 3 ^ wm) * 32 + 8 * g4 + q4) * 192 + (((wn * 3 + n % 3) ^ (g4 & 1)) << 5) + 8 * p4;
    aW[1][n / 3][n % 3] = aW[0][n / 3][n % 3] + 49152;
    asm volatile("" : "+v"(aW[0][n / 3][n % 3]), "+v"(aW[1][n / 3][n % 3]));
  }
  f32x4 co[3];
  u32x4 ones = {0x3F803F80u, 0x3F803F80u, 0x3F803F80u, 0x3F803F80u};
  f32x4 a2[2][3], a1[2][3], rs, gr, c2[3], c1[3];
  u32x2 zt[3][2];
  asm volatile("" : "+v"(ones), "+v"(aX[0][0]), "+v"(aX[0][1]), "+v"(aX[1][0]), "+v"(aX[1][1]));
  int tmo = (1 << 18);
  u32x2 fk, fz, fh, fi;
  unsigned tg0, tg1;
  float xm[2], xd[2], k1[2], k2[2];
  f32x4 xs[2];
  float xt[4];
  u32x2 xv[5];
  unsigned mU, uso, w3k[2], sFK, sFZ;
  float dD[3], bs;
  u32x4 im[6], zero4;
  SB();
  asm volatile("s_add_u32 m0, %0, 12288" :  : "s"(mW) : "memory", "scc"); SB();
  asm volatile("s_nop 0\n\tbuffer_load_dwordx4 %0, %1, %2 offen lds" :  : "v"(voB[0]), "s"(rW), "s"(kw_[1]) : "memory"); SB();
  asm volatile("buffer_load_dwordx4 %0, %1, %2 offen offset:1024 lds" :  : "v"(voB[1]), "s"(rW), "s"(kw_[1]) : "memory"); SB();
  asm volatile("buffer_load_dwordx4 %0, %1, %2 offen offset:2048 lds" :  : "v"(voB[2]), "s"(rW), "s"(kw_[1]) : "memory"); SB();
  const auto rK = __builtin_amdgcn_make_buffer_rsrc((void*)kin, 0, S_ * D_ * 2, 0x00020000);
  const auto rV = __builtin_amdgcn_make_buffer_rsrc((void*)vin, 0, S_ * D_ * 2, 0x00020000);
  const auto rS = __builtin_amdgcn_make_buffer_rsrc((void*)(shift_k - 3328), 0, D_ * 8 + 3328, 0x00020000);
  const auto rO = __builtin_amdgcn_make_buffer_rsrc((void*)(w_out - 3072), 0, 768 * 1280 + 3072, 0x00020000);
  unsigned voK[4], voV[3], voH = cb * 640 + l16;
#pragma unroll
  for (int n = 0; n < 4; ++n) { const unsigned o = n * 64 + lane, r = (o * 205) >> 11; voK[n] = (rb * 64 + w * 16 - 4) * 1280 + cb * 160 + r * 1120 + lane * 16; }
#pragma unroll
  for (int n = 0; n < 3; ++n) voV[n] = voK[n] + 5120;
  const unsigned wof = (rb >> 3) * 30 + min((rb & 7) * 4 + w, 29u), wok = (wof * 205) >> 10;
  unsigned voO = (lane >> 1) * 1280 + (lane & 1) * 16 + wok * 40960 + (wof - 5 * wok) * 32 + cb * 160, vWO = W_WO + (cb * 120 + wof) * 1024 + l16;
  asm volatile("" : "+v"(vWO), "+v"(voK[0]), "+v"(voK[1]), "+v"(voK[2]), "+v"(voK[3]), "+v"(voV[0]), "+v"(voV[1]), "+v"(voV[2]), "+v"(voH), "+v"(voO));
  SB();
  const auto rF = __builtin_amdgcn_make_buffer_rsrc((void*)ssm_k, 0, S_ * D_ * 2, 0x00020000);
  unsigned vF = ch0 * 4096 + w * 1024 + l16, c2s_ = c2s;
  unsigned hpo[3], vD = ch0 * 2, vD2 = vD + c2o * 2 - 4, vBs = (rb * 64 + w * 16 + l15) * 2;
  hpo[0] = w ? ch0 * 4096 + w * 1024 - 4 : 0x40000000u; hpo[1] = hpo[0] + 4096; hpo[2] = hpo[0] + c2s;
  asm volatile("" : "+s"(hpo[0]), "+s"(hpo[1]), "+s"(hpo[2]), "+v"(vD), "+v"(vD2), "+v"(vBs), "+v"(vF), "+s"(c2s_));
  SB();
  unsigned ka = lb + 98304 + w * 8192 + (4 * p4 + q4) * 160 + 8 * g4, oa = lb + 98304 + 7168 + w * 8192 + (4 * g4 + q4) * 32 + 8 * p4;
  unsigned sa = lb + 98304 + 3328 + w * 8192 + (4 * g4 + p4) * 8;
  unsigned kw = lb + 12288 + (4 * g4 + p4) * 144 + w * 32 + q4 * 8, kr = lb + 12288 + (w * 24 + (lane >> 3)) * 144 + (lane & 7) * 16;
  unsigned vKV = W_KV + (cb * 80 + w * 24 + (lane >> 3)) * 4096 + rb * 128 + (lane & 7) * 16;
  unsigned vS = W_ST + (rb * 2 + wm) * 8192 + wn * 512 + (lane >> 5) * 512 + l16;
  asm volatile("" : "+v"(ka), "+v"(oa), "+v"(sa), "+v"(kw), "+v"(kr), "+v"(vKV), "+v"(vS));
  SB();
  ST(1);
  SB();
  asm volatile("s_add_u32 m0, %0, 98304" :  : "s"(mX) : "memory", "scc"); SB();
  asm volatile("s_nop 0\n\tbuffer_load_dwordx4 %0, %1, %2 offen lds" :  : "v"(voA), "s"(rX), "s"(kx[2]) : "memory"); SB();
  asm volatile("buffer_load_dwordx4 %0, %1, %2 offen offset:1024 lds" :  : "v"(voA2), "s"(rX), "s"(kx[2]) : "memory"); SB();
  asm volatile("buffer_load_dwordx2 v[252:253], off, %0, 0 sc1" :  : "s"(rWS) : "memory"); SB();
  asm volatile("s_add_u32 m0, %0, 106496" :  : "s"(mX) : "memory", "scc"); SB();
  asm volatile("s_nop 0\n\tbuffer_load_dwordx4 %0, %1, %2 offen lds" :  : "v"(voA), "s"(rX), "s"(kx[3]) : "memory"); SB();
  asm volatile("buffer_load_dwordx4 %0, %1, %2 offen offset:1024 lds" :  : "v"(voA2), "s"(rX), "s"(kx[3]) : "memory"); SB();
  asm volatile("s_add_u32 m0, %0, 24576" :  : "s"(mW) : "memory", "scc"); SB();
  asm volatile("s_nop 0\n\tbuffer_load_dwordx4 %0, %1, %2 offen lds" :  : "v"(voB[0]), "s"(rW), "s"(kw_[2]) : "memory"); SB();
  asm volatile("s_waitcnt vmcnt(11) lgkmcnt(0)\n\ts_barrier" :  :  : "memory"); SB();
  ST(4);
  asm volatile("ds_read_b128 a[0:3], %0 offset:0" :  : "v"((aX[0][0] - 16384)) : "memory"); SB();
  asm volatile("ds_read_b128 a[4:7], %0 offset:0" :  : "v"((aX[0][1] - 16384)) : "memory"); SB();
  asm volatile("ds_read_b64_tr_b16 v[160:161], %0 offset:0" :  : "v"(aW[0][0][0]) : "memory"); SB();
  asm volatile("ds_read_b64_tr_b16 v[162:163], %0 offset:768" :  : "v"(aW[0][0][0]) : "memory"); SB();
  asm volatile("ds_read_b64_tr_b16 v[164:165], %0 offset:0" :  : "v"(aW[0][0][1]) : "memory"); SB();
  asm volatile("ds_read_b64_tr_b16 v[166:167], %0 offset:768" :  : "v"(aW[0][0][1]) : "memory"); SB();
  asm volatile("ds_read_b64_tr_b16 v[168:169], %0 offset:0" :  : "v"(aW[0][0][2]) : "memory"); SB();
  asm volatile("ds_read_b64_tr_b16 v[170:171], %0 offset:768" :  : "v"(aW[0][0][2]) : "memory"); SB();
  asm volatile("ds_read_b128 a[8:11], %0 offset:0" :  : "v"((aX[1][0] - 16384)) : "memory"); SB();
  asm volatile("ds_read_b128 a[12:15], %0 offset:0" :  : "v"((aX[1][1] - 16384)) : "memory"); SB();
  asm volatile("s_waitcnt lgkmcnt(6)\n\tv_mfma_f32_16x16x32_bf16 %0, v[160:163], a[0:3], 0" : "=&v"(a2[0][0]) :  : ); SB();
  asm volatile("ds_read_b64_tr_b16 v[172:173], %0 offset:0" :  : "v"(aW[0][1][0]) : "memory"); SB();
  asm volatile("buffer_load_dwordx4 %0, %1, %2 offen offset:1024 lds" :  : "v"(voB[1]), "s"(rW), "s"(kw_[2]) : "memory"); SB();
  asm volatile("v_mfma_f32_16x16x32_bf16 %0, v[160:163], a[4:7], 0" : "=&v"(a2[1][0]) :  : ); SB();
  asm volatile("ds_read_b64_tr_b16 v[174:175], %0 offset:768" :  : "v"(aW[0][1][0]) : "memory"); SB();
  asm volatile("buffer_load_dwordx4 %0, %1, %2 offen offset:2048 lds" :  : "v"(voB[2]), "s"(rW), "s"(kw_[2]) : "memory"); SB();
  asm volatile("v_mfma_f32_16x16x32_bf16 %0, v[160:163], %1, 0" : "=&v"(c2[0]) : "v"(ones) : ); SB();
  asm volatile("ds_read_b64_tr_b16 v[176:177], %0 offset:0" :  : "v"(aW[0][1][1]) : "memory"); SB();
  asm volatile("s_waitcnt lgkmcnt(5)\n\tv_mfma_f32_16x16x32_bf16 %0, v[164:167], a[0:3], 0" : "=&v"(a2[0][1]) :  : ); SB();
  asm volatile("ds_read_b64_tr_b16 v[178:179], %0 offset:768" :  : "v"(aW[0][1][1]) : "memory"); SB();
  asm volatile("v_mfma_f32_16x16x32_bf16 %0, v[164:167], a[4:7], 0" : "=&v"(a2[1][1]) :  : ); SB();
  asm volatile("ds_read_b64_tr_b16 v[180:181], %0 offset:0" :  : "v"(aW[0][1][2]) : "memory"); SB();
  asm volatile("s_add_u32 m0, %0, 114688" :  : "s"(mX) : "memory", "scc"); SB();
  asm volatile("v_mfma_f32_16x16x32_bf16 %0, v[164:167], %1, 0" : "=&v"(c2[1]) : "v"(ones) : ); SB();
  asm volatile("ds_read_b64_tr_b16 v[182:183], %0 offset:768" :  : "v"(aW[0][1][2]) : "memory"); SB();
  asm volatile("buffer_load_dwordx4 %0, %1, %2 offen lds" :  : "v"(voA), "s"(rX), "s"(kx[4]) : "memory"); SB();
  asm volatile("v_mfma_f32_16x16x32_bf16 %0, v[168:171], a[0:3], 0" : "=&v"(a2[0][2]) :  : ); SB();
  unsigned aV[5];
#pragma unroll
  for (int j = 0; j < 5; ++j) {
    const unsigned c = cb * 10 + 2 * j, t = 0 ? (c >> 3) ^ (c < 64 ? rot : rot & 1) : m10((c >> 3) + 10 - rot);
    aV[j] = lb + 81920 + t * 8192 + w * 2048 + l15 * 128 + ((((c & 7) + (g4 >> 1)) ^ (l15 & 7)) << 4) + (g4 & 1) * 8;
    asm volatile("" : "+v"(aV[j]));
  }
  SB();
  asm volatile("buffer_load_dwordx4 %0, %1, %2 offen offset:1024 lds" :  : "v"(voA2), "s"(rX), "s"(kx[4]) : "memory"); SB();
  asm volatile("s_add_u32 m0, %0, 122880" :  : "s"(mX) : "memory", "scc"); SB();
  asm volatile("s_nop 0\n\tbuffer_load_dwordx4 %0, %1, %2 offen lds" :  : "v"(voA), "s"(rX), "s"(kx[5]) : "memory"); SB();
  asm volatile("s_waitcnt vmcnt(11)\n\ts_barrier" :  :  : "memory"); SB();
  asm volatile("ds_read_b64_tr_b16 v[184:185], %0 offset:12288" :  : "v"(aW[0][0][0]) : "memory"); SB();
  asm volatile("buffer_load_dwordx4 %0, %1, %2 offen offset:1024 lds" :  : "v"(voA2), "s"(rX), "s"(kx[5]) : "memory"); SB();
  asm volatile("v_mfma_f32_16x16x32_bf16 %0, v[168:171], a[4:7], 0" : "=&v"(a2[1][2]) :  : ); SB();
  asm volatile("ds_read_b64_tr_b16 v[186:187], %0 offset:13056" :  : "v"(aW[0][0][0]) : "memory"); SB();
  asm volatile("v_mfma_f32_16x16x32_bf16 %0, v[168:171], %1, 0" : "=&v"(c2[2]) : "v"(ones) : ); SB();
  asm volatile("ds_read_b64_tr_b16 v[188:189], %0 offset:12288" :  : "v"(aW[0][0][1]) : "memory"); SB();
  asm volatile("v_mfma_f32_16x16x32_bf16 %0, %1, a[0:3], 0" : "=&v"(rs) : "v"(ones) : ); SB();
  asm volatile("ds_read_b128 a[16:19], %0 offset:8192" :  : "v"((aX[0][0] - 16384)) : "memory"); SB();
  asm volatile("ds_read_b128 a[20:23], %0 offset:8192" :  : "v"((aX[0][1] - 16384)) : "memory"); SB();
  asm volatile("ds_read_b64_tr_b16 v[190:191], %0 offset:13056" :  : "v"(aW[0][0][1]) : "memory"); SB();
  asm volatile("s_add_u32 m0, %0, 36864" :  : "s"(mW) : "memory", "scc"); SB();
  asm volatile("v_mfma_f32_16x16x32_bf16 %0, a[0:3], a[0:3], 0" : "=&v"(gr) :  : ); SB();
  asm volatile("ds_read_b64_tr_b16 v[192:193], %0 offset:12288" :  : "v"(aW[0][0][2]) : "memory"); SB();
  asm volatile("buffer_load_dwordx4 %0, %1, %2 offen lds" :  : "v"(voB[0]), "s"(rW), "s"(kw_[3]) : "memory"); SB();
  asm volatile("s_waitcnt lgkmcnt(7)\n\tv_mfma_f32_16x16x32_bf16 %0, v[172:175], a[8:11], %0" : "+v"(a2[0][0]) :  : ); SB();
  asm volatile("ds_read_b64_tr_b16 v[194:195], %0 offset:13056" :  : "v"(aW[0][0][2]) : "memory"); SB();
  asm volatile("buffer_load_dwordx4 %0, %1, %2 offen offset:1024 lds" :  : "v"(voB[1]), "s"(rW), "s"(kw_[3]) : "memory"); SB();
  asm volatile("v_mfma_f32_16x16x32_bf16 %0, v[172:175], a[12:15], %0" : "+v"(a2[1][0]) :  : ); SB();
  asm volatile("ds_read_b128 a[24:27], %0 offset:8192" :  : "v"((aX[1][0] - 16384)) : "memory"); SB();
  asm volatile("v_mfma_f32_16x16x32_bf16 %0, v[176:179], a[8:11], %0" : "+v"(a2[0][1]) :  : ); SB();
  asm volatile("ds_read_b128 a[28:31], %0 offset:8192" :  : "v"((aX[1][1] - 16384)) : "memory"); SB();
  asm volatile("v_mfma_f32_16x16x32_bf16 %0, v[176:179], a[12:15], %0" : "+v"(a2[1][1]) :  : ); SB();
  asm volatile("v_mfma_f32_16x16x32_bf16 %0, v[180:183], a[8:11], %0" : "+v"(a2[0][2]) :  : ); SB();
  asm volatile("ds_read_b64_tr_b16 v[160:161], %0 offset:12288" :  : "v"(aW[0][1][0]) : "memory"); SB();
  asm volatile("buffer_load_dwordx4 %0, %1, %2 offen offset:2048 lds" :  : "v"(voB[2]), "s"(rW), "s"(kw_[3]) : "memory"); SB();
  asm volatile("v_mfma_f32_16x16x32_bf16 %0, v[180:183], a[12:15], %0" : "+v"(a2[1][2]) :  : ); SB();
  asm volatile("ds_read_b64_tr_b16 v[162:163], %0 offset:13056" :  : "v"(aW[0][1][0]) : "memory"); SB();
  asm volatile("v_mfma_f32_16x16x32_bf16 %0, %1, a[8:11], %0" : "+v"(rs) : "v"(ones) : ); SB();
  asm volatile("ds_read_b64_tr_b16 v[164:165], %0 offset:12288" :  : "v"(aW[0][1][1]) : "memory"); SB();
  asm volatile("v_mfma_f32_16x16x32_bf16 %0, a[8:11], a[8:11], %0" : "+v"(gr) :  : ); SB();
  asm volatile("ds_read_b64_tr_b16 v[166:167], %0 offset:13056" :  : "v"(aW[0][1][1]) : "memory"); SB();
  asm volatile("s_add_u32 m0, %0, 131072" :  : "s"(mX) : "memory", "scc"); SB();
  asm volatile("s_waitcnt lgkmcnt(7)\n\tv_mfma_f32_16x16x32_bf16 %0, v[184:187], a[16:19], %0" : "+v"(a2[0][0]) :  : ); SB();
  asm volatile("ds_read_b64_tr_b16 v[168:169], %0 offset:12288" :  : "v"(aW[0][1][2]) : "memory"); SB();
  asm volatile("buffer_load_dwordx4 %0, %1, %2 offen lds" :  : "v"(voA), "s"(rX), "s"(kx[6]) : "memory"); SB();
  asm volatile("v_mfma_f32_16x16x32_bf16 %0, v[184:187], a[20:23], %0" : "+v"(a2[1][0]) :  : ); SB();
  asm volatile("ds_read_b64_tr_b16 v[170:171], %0 offset:13056" :  : "v"(aW[0][1][2]) : "memory"); SB();
  asm volatile("v_mfma_f32_16x16x32_bf16 %0, v[184:187], %1, %0" : "+v"(c2[0]) : "v"(ones) : ); SB();
  asm volatile("buffer_load_dwordx4 %0, %1, %2 offen offset:1024 lds" :  : "v"(voA2), "s"(rX), "s"(kx[6]) : "memory"); SB();
  asm volatile("v_mfma_f32_16x16x32_bf16 %0, v[188:191], a[16:19], %0" : "+v"(a2[0][1]) :  : ); SB();
  asm volatile("v_mfma_f32_16x16x32_bf16 %0, v[188:191], a[20:23], %0" : "+v"(a2[1][1]) :  : ); SB();
  asm volatile("s_waitcnt vmcnt(9)\n\ts_barrier" :  :  : "memory"); SB();
  asm volatile("ds_read_b64_tr_b16 v[172:173], %0 offset:24576" :  : "v"(aW[0][0][0]) : "memory"); SB();
  asm volatile("s_add_u32 m0, %0, 139264" :  : "s"(mX) : "memory", "scc"); SB();
  asm volatile("v_mfma_f32_16x16x32_bf16 %0, v[188:191], %1, %0" : "+v"(c2[1]) : "v"(ones) : ); SB();
  asm volatile("ds_read_b64_tr_b16 v[174:175], %0 offset:25344" :  : "v"(aW[0][0][0]) : "memory"); SB();
  asm volatile("buffer_load_dwordx4 %0, %1, %2 offen lds" :  : "v"(voA), "s"(rX), "s"(kx[7]) : "memory"); SB();
  asm volatile("s_waitcnt lgkmcnt(4)\n\tv_mfma_f32_16x16x32_bf16 %0, v[192:195], a[16:19], %0" : "+v"(a2[0][2]) :  : ); SB();
  asm volatile("ds_read_b64_tr_b16 v[176:177], %0 offset:24576" :  : "v"(aW[0][0][1]) : "memory"); SB();
  asm volatile("v_mfma_f32_16x16x32_bf16 %0, v[192:195], a[20:23], %0" : "+v"(a2[1][2]) :  : ); SB();
  asm volatile("ds_read_b64_tr_b16 v[178:179], %0 offset:25344" :  : "v"(aW[0][0][1]) : "memory"); SB();
  asm volatile("v_mfma_f32_16x16x32_bf16 %0, v[192:195], %1, %0" : "+v"(c2[2]) : "v"(ones) : ); SB();
  asm volatile("ds_read_b64_tr_b16 v[180:181], %0 offset:24576" :  : "v"(aW[0][0][2]) : "memory"); SB();
  asm volatile("v_mfma_f32_16x16x32_bf16 %0, %1, a[16:19], %0" : "+v"(rs) : "v"(ones) : ); SB();
  asm volatile("ds_read_b128 a[32:35], %0 offset:0" :  : "v"(aX[0][0]) : "memory"); SB();
  asm volatile("ds_read_b128 a[36:39], %0 offset:0" :  : "v"(aX[0][1]) : "memory"); SB();
  asm volatile("ds_read_b64_tr_b16 v[182:183], %0 offset:25344" :  : "v"(aW[0][0][2]) : "memory"); SB();
  asm volatile("v_mfma_f32_16x16x32_bf16 %0, a[16:19], a[16:19], %0" : "+v"(gr) :  : ); SB();
  asm volatile("ds_read_b128 a[40:43], %0 offset:0" :  : "v"(aX[1][0]) : "memory"); SB();
  asm volatile("v_mfma_f32_16x16x32_bf16 %0, v[160:163], a[24:27], %0" : "+v"(a2[0][0]) :  : ); SB();
  asm volatile("ds_read_b128 a[44:47], %0 offset:0" :  : "v"(aX[1][1]) : "memory"); SB();
  asm volatile("v_mfma_f32_16x16x32_bf16 %0, v[160:163], a[28:31], %0" : "+v"(a2[1][0]) :  : ); SB();
  asm volatile("ds_read_b128 a[48:51], %0 offset:8192" :  : "v"(aX[0][0]) : "memory"); SB();
  asm volatile("v_mfma_f32_16x16x32_bf16 %0, v[164:167], a[24:27], %0" : "+v"(a2[0][1]) :  : ); SB();
  asm volatile("ds_read_b128 a[52:55], %0 offset:8192" :  : "v"(aX[0][1]) : "memory"); SB();
  asm volatile("v_mfma_f32_16x16x32_bf16 %0, v[164:167], a[28:31], %0" : "+v"(a2[1][1]) :  : ); SB();
  asm volatile("ds_read_b128 a[56:59], %0 offset:8192" :  : "v"(aX[1][0]) : "memory"); SB();
  asm volatile("s_waitcnt lgkmcnt(11)\n\tv_mfma_f32_16x16x32_bf16 %0, v[168:171], a[24:27], %0" : "+v"(a2[0][2]) :  : ); SB();
  asm volatile("ds_read_b128 a[60:63], %0 offset:8192" :  : "v"(aX[1][1]) : "memory"); SB();
  asm volatile("v_mfma_f32_16x16x32_bf16 %0, v[168:171], a[28:31], %0" : "+v"(a2[1][2]) :  : ); SB();
  asm volatile("buffer_load_dwordx4 %0, %1, %2 offen offset:1024 lds" :  : "v"(voA2), "s"(rX), "s"(kx[7]) : "memory"); SB();
  asm volatile("s_add_u32 m0, %0, 49152" :  : "s"(mW) : "memory", "scc"); SB();
  asm volatile("v_mfma_f32_16x16x32_bf16 %0, %1, a[24:27], %0" : "+v"(rs) : "v"(ones) : ); SB();
  asm volatile("ds_read_b64_tr_b16 v[184:185], %0 offset:24576" :  : "v"(aW[0][1][0]) : "memory"); SB();
  asm volatile("v_mfma_f32_16x16x32_bf16 %0, a[24:27], a[24:27], %0" : "+v"(gr) :  : ); SB();
  asm volatile("ds_read_b64_tr_b16 v[186:187], %0 offset:25344" :  : "v"(aW[0][1][0]) : "memory"); SB();
  asm volatile("s_waitcnt lgkmcnt(9)\n\tv_mfma_f32_16x16x32_bf16 %0, v[172:175], a[32:35], %0" : "+v"(a2[0][0]) :  : ); SB();
  asm volatile("ds_read_b64_tr_b16 v[188:189], %0 offset:24576" :  : "v"(aW[0][1][1]) : "memory"); SB();
  asm volatile("buffer_load_dwordx4 %0, %1, %2 offen lds" :  : "v"(voB[0]), "s"(rW), "s"(kw_[4]) : "memory"); SB();
  asm volatile("v_mfma_f32_16x16x32_bf16 %0, v[172:175], a[36:39], %0" : "+v"(a2[1][0]) :  : ); SB();
  asm volatile("ds_read_b64_tr_b16 v[190:191], %0 offset:25344" :  : "v"(aW[0][1][1]) : "memory"); SB();
  asm volatile("v_mfma_f32_16x16x32_bf16 %0, v[172:175], %1, %0" : "+v"(c2[0]) : "v"(ones) : ); SB();
  asm volatile("ds_read_b64_tr_b16 v[192:193], %0 offset:24576" :  : "v"(aW[0][1][2]) : "memory"); SB();
  asm volatile("v_mfma_f32_16x16x32_bf16 %0, v[176:179], a[32:35], %0" : "+v"(a2[0][1]) :  : ); SB();
  asm volatile("ds_read_b64_tr_b16 v[194:195], %0 offset:25344" :  : "v"(aW[0][1][2]) : "memory"); SB();
  asm volatile("v_mfma_f32_16x16x32_bf16 %0, v[176:179], a[36:39], %0" : "+v"(a2[1][1]) :  : ); SB();
  asm volatile("buffer_load_dwordx4 %0, %1, %2 offen offset:1024 lds" :  : "v"(voB[1]), "s"(rW), "s"(kw_[4]) : "memory"); SB();
  asm volatile("v_mfma_f32_16x16x32_bf16 %0, v[176:179], %1, %0" : "+v"(c2[1]) : "v"(ones) : ); SB();
  asm volatile("buffer_load_dwordx4 %0, %1, %2 offen offset:2048 lds" :  : "v"(voB[2]), "s"(rW), "s"(kw_[4]) : "memory"); SB();
  asm volatile("s_waitcnt lgkmcnt(6)\n\tv_mfma_f32_16x16x32_bf16 %0, v[180:183], a[32:35], %0" : "+v"(a2[0][2]) :  : ); SB();
  asm volatile("v_mfma_f32_16x16x32_bf16 %0, v[180:183], a[36:39], %0" : "+v"(a2[1][2]) :  : ); SB();
  asm volatile("v_mfma_f32_16x16x32_bf16 %0, v[180:183], %1, %0" : "+v"(c2[2]) : "v"(ones) : ); SB();
  const auto rA = __builtin_amdgcn_make_buffer_rsrc((void*)(w_sp - 3072), 0, S_ * S_ * 2 + 3072, 0x00020000);
  unsigned ca = lb + 98304 + 6912 + w * 8192 + g4 * 16, cr = lb + 98304 + 6912 + (w ^ 2) * 8192 + g4 * 16;
  unsigned voS = (rb * 64 + (lane >> 3)) * 4096 + w * 1024 + (cb & 5) * 128 + (((lane & 7) ^ (lane >> 3)) << 4), aA[2];
#pragma unroll
  for (int k = 0; k < 2; ++k) aA[k] = lb + 98304 + w * 8192 + l15 * 128 + (((k * 4 + g4) ^ (l15 & 7)) << 4);
  unsigned xw = lb + 98304 + 6656 + w * 8192 + l15 * 16, xr_ = lb + 98304 + 6656 + (w ^ 1) * 8192 + l15 * 16;
  unsigned vo = W_ST + ((rb * 2 + wm) * 8 + cb) * 1024 + wn * 128 + ((lane >> 5) ^ wn) * 512 + (l15 >> 3) * 256 + (l15 & 7) * 16;
  asm volatile("" : "+v"(ca), "+v"(cr), "+v"(voS), "+v"(aA[0]), "+v"(aA[1]), "+v"(xw), "+v"(xr_), "+v"(vo));
  SB();
  asm volatile("s_add_u32 m0, %0, 147456" :  : "s"(mX) : "memory", "scc"); SB();
  asm volatile("s_nop 0\n\tbuffer_load_dwordx4 %0, %1, %2 offen lds" :  : "v"(voA), "s"(rX), "s"(kx[8]) : "memory"); SB();
  asm volatile("buffer_load_dwordx4 %0, %1, %2 offen offset:1024 lds" :  : "v"(voA2), "s"(rX), "s"(kx[8]) : "memory"); SB();
  asm volatile("s_waitcnt vmcnt(9)\n\ts_barrier" :  :  : "memory"); SB();
  asm volatile("ds_read_b64 %0, %1 offset:0" : "=&v"(xv[0]) : "v"(aV[0]) : "memory"); SB();
  asm volatile("ds_read_b64 %0, %1 offset:0" : "=&v"(xv[1]) : "v"(aV[1]) : "memory"); SB();
  asm volatile("ds_read_b64_tr_b16 v[160:161], %0 offset:36864" :  : "v"(aW[0][0][0]) : "memory"); SB();
  asm volatile("s_add_u32 m0, %0, 155648" :  : "s"(mX) : "memory", "scc"); SB();
  asm volatile("v_mfma_f32_16x16x32_bf16 %0, %1, a[32:35], %0" : "+v"(rs) : "v"(ones) : ); SB();
  asm volatile("ds_read_b64 %0, %1 offset:0" : "=&v"(xv[2]) : "v"(aV[2]) : "memory"); SB();
  asm volatile("ds_read_b64 %0, %1 offset:0" : "=&v"(xv[3]) : "v"(aV[3]) : "memory"); SB();
  asm volatile("ds_read_b64_tr_b16 v[162:163], %0 offset:37632" :  : "v"(aW[0][0][0]) : "memory"); SB();
  asm volatile("buffer_load_dwordx4 %0, %1, %2 offen lds" :  : "v"(voA), "s"(rX), "s"(kx[9]) : "memory"); SB();
  asm volatile("v_mfma_f32_16x16x32_bf16 %0, a[32:35], a[32:35], %0" : "+v"(gr) :  : ); SB();
  asm volatile("ds_read_b64 %0, %1 offset:0" : "=&v"(xv[4]) : "v"(aV[4]) : "memory"); SB();
  asm volatile("ds_read_b64_tr_b16 v[164:165], %0 offset:36864" :  : "v"(aW[0][0][1]) : "memory"); SB();
  asm volatile("buffer_load_dwordx4 %0, %1, %2 offen offset:1024 lds" :  : "v"(voA2), "s"(rX), "s"(kx[9]) : "memory"); SB();
  asm volatile("v_mfma_f32_16x16x32_bf16 %0, %1, a[40:43], %0" : "+v"(rs) : "v"(ones) : ); SB();
  asm volatile("ds_read_b64_tr_b16 v[166:167], %0 offset:37632" :  : "v"(aW[0][0][1]) : "memory"); SB();
  asm volatile("v_mfma_f32_16x16x32_bf16 %0, a[40:43], a[40:43], %0" : "+v"(gr) :  : ); SB();
  asm volatile("ds_read_b64_tr_b16 v[168:169], %0 offset:36864" :  : "v"(aW[0][0][2]) : "memory"); SB();
  asm volatile("s_add_u32 m0, %0, 61440" :  : "s"(mW) : "memory", "scc"); SB();
  asm volatile("s_waitcnt lgkmcnt(10)\n\tv_mfma_f32_16x16x32_bf16 %0, v[184:187], a[40:43], %0" : "+v"(a2[0][0]) :  : ); SB();
  asm volatile("ds_read_b64_tr_b16 v[170:171], %0 offset:37632" :  : "v"(aW[0][0][2]) : "memory"); SB();
  asm volatile("buffer_load_dwordx4 %0, %1, %2 offen lds" :  : "v"(voB[0]), "s"(rW), "s"(kw_[5]) : "memory"); SB();
  asm volatile("v_mfma_f32_16x16x32_bf16 %0, v[184:187], a[44:47], %0" : "+v"(a2[1][0]) :  : ); SB();
  asm volatile("ds_read_b128 a[64:67], %0 offset:16384" :  : "v"(aX[0][0]) : "memory"); SB();
  asm volatile("v_mfma_f32_16x16x32_bf16 %0, v[188:191], a[40:43], %0" : "+v"(a2[0][1]) :  : ); SB();
  asm volatile("ds_read_b128 a[68:71], %0 offset:16384" :  : "v"(aX[0][1]) : "memory"); SB();
  asm volatile("v_mfma_f32_16x16x32_bf16 %0, v[188:191], a[44:47], %0" : "+v"(a2[1][1]) :  : ); SB();
  asm volatile("buffer_load_dwordx4 %0, %1, %2 offen offset:1024 lds" :  : "v"(voB[1]), "s"(rW), "s"(kw_[5]) : "memory"); SB();
  asm volatile("v_mfma_f32_16x16x32_bf16 %0, v[192:195], a[40:43], %0" : "+v"(a2[0][2]) :  : ); SB();
  asm volatile("ds_read_b128 a[72:75], %0 offset:16384" :  : "v"(aX[1][0]) : "memory"); SB();
  asm volatile("v_mfma_f32_16x16x32_bf16 %0, v[192:195], a[44:47], %0" : "+v"(a2[1][2]) :  : ); SB();
  asm volatile("ds_read_b128 a[76:79], %0 offset:16384" :  : "v"(aX[1][1]) : "memory"); SB();
  asm volatile("v_mfma_f32_16x16x32_bf16 %0, %1, a[48:51], %0" : "+v"(rs) : "v"(ones) : ); SB();
  asm volatile("ds_read_b128 a[80:83], %0 offset:24576" :  : "v"(aX[0][0]) : "memory"); SB();
  asm volatile("v_mfma_f32_16x16x32_bf16 %0, a[48:51], a[48:51], %0" : "+v"(gr) :  : ); SB();
  asm volatile("buffer_load_dwordx4 %0, %1, %2 offen offset:2048 lds" :  : "v"(voB[2]), "s"(rW), "s"(kw_[5]) : "memory"); SB();
  asm volatile("v_mfma_f32_16x16x32_bf16 %0, %1, a[56:59], %0" : "+v"(rs) : "v"(ones) : ); SB();
  asm volatile("ds_read_b64_tr_b16 v[172:173], %0 offset:36864" :  : "v"(aW[0][1][0]) : "memory"); SB();
  asm volatile("v_mfma_f32_16x16x32_bf16 %0, a[56:59], a[56:59], %0" : "+v"(gr) :  : ); SB();
  asm volatile("ds_read_b64_tr_b16 v[174:175], %0 offset:37632" :  : "v"(aW[0][1][0]) : "memory"); SB();
  asm volatile("s_waitcnt lgkmcnt(7)\n\tv_mfma_f32_16x16x32_bf16 %0, v[160:163], a[48:51], %0" : "+v"(a2[0][0]) :  : ); SB();
  asm volatile("ds_read_b64_tr_b16 v[176:177], %0 offset:36864" :  : "v"(aW[0][1][1]) : "memory"); SB();
  asm volatile("buffer_load_dwordx4 v[228:231], %0, %1, %2 offen" :  : "v"(vF), "s"(rF), "s"(0u) : "memory"); SB();
  asm volatile("v_mfma_f32_16x16x32_bf16 %0, v[160:163], a[52:55], %0" : "+v"(a2[1][0]) :  : ); SB();
  asm volatile("ds_read_b64_tr_b16 v[178:179], %0 offset:37632" :  : "v"(aW[0][1][1]) : "memory"); SB();
  asm volatile("v_mfma_f32_16x16x32_bf16 %0, v[160:163], %1, %0" : "+v"(c2[0]) : "v"(ones) : ); SB();
  asm volatile("ds_read_b64_tr_b16 v[180:181], %0 offset:36864" :  : "v"(aW[0][1][2]) : "memory"); SB();
  asm volatile("v_mfma_f32_16x16x32_bf16 %0, v[164:167], a[48:51], %0" : "+v"(a2[0][1]) :  : ); SB();
  asm volatile("ds_read_b64_tr_b16 v[182:183], %0 offset:37632" :  : "v"(aW[0][1][2]) : "memory"); SB();
  asm volatile("v_mfma_f32_16x16x32_bf16 %0, v[164:167], a[52:55], %0" : "+v"(a2[1][1]) :  : ); SB();
  asm volatile("ds_read_b128 a[84:87], %0 offset:24576" :  : "v"(aX[0][1]) : "memory"); SB();
  asm volatile("v_mfma_f32_16x16x32_bf16 %0, v[164:167], %1, %0" : "+v"(c2[1]) : "v"(ones) : ); SB();
  asm volatile("buffer_load_dwordx4 v[232:235], %0, %1, %2 offen" :  : "v"(vF), "s"(rF), "s"(4096u) : "memory"); SB();
  asm volatile("v_mfma_f32_16x16x32_bf16 %0, v[168:171], a[48:51], %0" : "+v"(a2[0][2]) :  : ); SB();
  asm volatile("ds_read_b128 a[88:91], %0 offset:24576" :  : "v"(aX[1][0]) : "memory"); SB();
  asm volatile("v_mfma_f32_16x16x32_bf16 %0, v[168:171], a[52:55], %0" : "+v"(a2[1][2]) :  : ); SB();
  asm volatile("ds_read_b128 a[92:95], %0 offset:24576" :  : "v"(aX[1][1]) : "memory"); SB();
  asm volatile("v_mfma_f32_16x16x32_bf16 %0, v[168:171], %1, %0" : "+v"(c2[2]) : "v"(ones) : ); SB();
#pragma unroll
  for (int n = 0; n < 8; ++n) { ao[n] = (n ^ (2 * w)) * 32768 + (3 - (n & 3)) * 1024; asm volatile("" : "+s"(ao[n])); }
  w3k[0] = w3s + 32768; w3k[1] = w3s + 65536; sFZ = W_FZ + ((cb * 32 + rb) * 4 + w) * 8; sFK = sFZ + 8192; mU = lb + w * 1024; uso = W_KV + c2s;
  asm volatile("" : "+s"(w3k[0]), "+s"(w3k[1]), "+s"(sFZ), "+s"(sFK), "+s"(mU), "+s"(uso));
  SB();
  asm volatile("buffer_load_dwordx4 v[236:239], %0, %1, %2 offen" :  : "v"(vF), "s"(rF), "s"(c2s_) : "memory"); SB();
  asm volatile("v_readfirstlane_b32 %0, v252\n\tv_readfirstlane_b32 %1, v253\n\ts_nop 0\n\ts_add_u32 %0, %0, 1\n\ts_addc_u32 %1, %1, 0" : "=&s"(tg0), "=&s"(tg1) :  : "memory", "scc"); SB();
  asm volatile("s_waitcnt vmcnt(10)\n\ts_barrier" :  :  : "memory"); SB();
  asm volatile("ds_read_b64_tr_b16 v[184:185], %0 offset:0" :  : "v"(aW[1][0][0]) : "memory"); SB();
  asm volatile("s_add_u32 m0, %0, 73728" :  : "s"(mW) : "memory", "scc"); SB();
  asm volatile("s_waitcnt lgkmcnt(1)\n\tv_mfma_f32_16x16x32_bf16 %0, %1, a[64:67], %0" : "+v"(rs) : "v"(ones) : ); SB();
  asm volatile("ds_read_b64_tr_b16 v[186:187], %0 offset:768" :  : "v"(aW[1][0][0]) : "memory"); SB();
  asm volatile("buffer_load_dwordx4 %0, %1, %2 offen lds" :  : "v"(voB[0]), "s"(rW), "s"(kw_[6]) : "memory"); SB();
  asm volatile("v_mfma_f32_16x16x32_bf16 %0, a[64:67], a[64:67], %0" : "+v"(gr) :  : ); SB();
  asm volatile("ds_read_b64_tr_b16 v[188:189], %0 offset:0" :  : "v"(aW[1][0][1]) : "memory"); SB();
  asm volatile("buffer_load_dwordx4 %0, %1, %2 offen offset:1024 lds" :  : "v"(voB[1]), "s"(rW), "s"(kw_[6]) : "memory"); SB();
  asm volatile("v_mfma_f32_16x16x32_bf16 %0, %1, a[72:75], %0" : "+v"(rs) : "v"(ones) : ); SB();
  asm volatile("ds_read_b64_tr_b16 v[190:191], %0 offset:768" :  : "v"(aW[1][0][1]) : "memory"); SB();
  asm volatile("v_mfma_f32_16x16x32_bf16 %0, a[72:75], a[72:75], %0" : "+v"(gr) :  : ); SB();
  asm volatile("ds_read_b64_tr_b16 v[192:193], %0 offset:0" :  : "v"(aW[1][0][2]) : "memory"); SB();
  asm volatile("v_mfma_f32_16x16x32_bf16 %0, v[172:175], a[56:59], %0" : "+v"(a2[0][0]) :  : ); SB();
  asm volatile("ds_read_b64_tr_b16 v[194:195], %0 offset:768" :  : "v"(aW[1][0][2]) : "memory"); SB();
  asm volatile("buffer_load_dwordx4 %0, %1, %2 offen offset:2048 lds" :  : "v"(voB[2]), "s"(rW), "s"(kw_[6]) : "memory"); SB();
  asm volatile("v_mfma_f32_16x16x32_bf16 %0, v[172:175], a[60:63], %0" : "+v"(a2[1][0]) :  : ); SB();
  asm volatile("ds_read_b128 a[96:99], %0 offset:32768" :  : "v"(aX[0][0]) : "memory"); SB();
  asm volatile("v_mfma_f32_16x16x32_bf16 %0, v[176:179], a[56:59], %0" : "+v"(a2[0][1]) :  : ); SB();
  asm volatile("ds_read_b128 a[100:103], %0 offset:32768" :  : "v"(aX[0][1]) : "memory"); SB();
  asm volatile("v_mfma_f32_16x16x32_bf16 %0, v[176:179], a[60:63], %0" : "+v"(a2[1][1]) :  : ); SB();
  asm volatile("ds_read_b128 a[104:107], %0 offset:32768" :  : "v"(aX[1][0]) : "memory"); SB();
  asm volatile("s_add_u32 m0, %0, 86016" :  : "s"(mW) : "memory", "scc"); SB();
  asm volatile("v_mfma_f32_16x16x32_bf16 %0, v[180:183], a[56:59], %0" : "+v"(a2[0][2]) :  : ); SB();
  asm volatile("ds_read_b128 a[108:111], %0 offset:32768" :  : "v"(aX[1][1]) : "memory"); SB();
  asm volatile("v_mfma_f32_16x16x32_bf16 %0, v[180:183], a[60:63], %0" : "+v"(a2[1][2]) :  : ); SB();
  asm volatile("ds_read_b128 a[112:115], %0 offset:40960" :  : "v"(aX[0][0]) : "memory"); SB();
  asm volatile("v_mfma_f32_16x16x32_bf16 %0, %1, a[80:83], %0" : "+v"(rs) : "v"(ones) : ); SB();
  asm volatile("buffer_load_dwordx4 %0, %1, %2 offen lds" :  : "v"(voB[0]), "s"(rW), "s"(kw_[7]) : "memory"); SB();
  asm volatile("v_mfma_f32_16x16x32_bf16 %0, a[80:83], a[80:83], %0" : "+v"(gr) :  : ); SB();
  asm volatile("ds_read_b128 a[116:119], %0 offset:40960" :  : "v"(aX[0][1]) : "memory"); SB();
  asm volatile("v_mfma_f32_16x16x32_bf16 %0, %1, a[88:91], %0" : "+v"(rs) : "v"(ones) : ); SB();
  asm volatile("ds_read_b64_tr_b16 v[160:161], %0 offset:0" :  : "v"(aW[1][1][0]) : "memory"); SB();
  asm volatile("v_mfma_f32_16x16x32_bf16 %0, a[88:91], a[88:91], %0" : "+v"(gr) :  : ); SB();
  asm volatile("ds_read_b64_tr_b16 v[162:163], %0 offset:768" :  : "v"(aW[1][1][0]) : "memory"); SB();
  asm volatile("s_waitcnt lgkmcnt(8)\n\tv_mfma_f32_16x16x32_bf16 %0, v[184:187], a[64:67], %0" : "+v"(a2[0][0]) :  : ); SB();
  asm volatile("ds_read_b64_tr_b16 v[164:165], %0 offset:0" :  : "v"(aW[1][1][1]) : "memory"); SB();
  asm volatile("buffer_load_dwordx4 %0, %1, %2 offen offset:1024 lds" :  : "v"(voB[1]), "s"(rW), "s"(kw_[7]) : "memory"); SB();
  asm volatile("v_mfma_f32_16x16x32_bf16 %0, v[184:187], a[68:71], %0" : "+v"(a2[1][0]) :  : ); SB();
  asm volatile("ds_read_b64_tr_b16 v[166:167], %0 offset:768" :  : "v"(aW[1][1][1]) : "memory"); SB();
  asm volatile("v_mfma_f32_16x16x32_bf16 %0, v[184:187], %1, %0" : "+v"(c2[0]) : "v"(ones) : ); SB();
  asm volatile("ds_read_b64_tr_b16 v[168:169], %0 offset:0" :  : "v"(aW[1][1][2]) : "memory"); SB();
  asm volatile("v_mfma_f32_16x16x32_bf16 %0, v[188:191], a[64:67], %0" : "+v"(a2[0][1]) :  : ); SB();
  asm volatile("ds_read_b64_tr_b16 v[170:171], %0 offset:768" :  : "v"(aW[1][1][2]) : "memory"); SB();
  asm volatile("v_mfma_f32_16x16x32_bf16 %0, v[188:191], a[68:71], %0" : "+v"(a2[1][1]) :  : ); SB();
  asm volatile("ds_read_b128 a[120:123], %0 offset:40960" :  : "v"(aX[1][0]) : "memory"); SB();
  asm volatile("v_mfma_f32_16x16x32_bf16 %0, v[188:191], %1, %0" : "+v"(c2[1]) : "v"(ones) : ); SB();
  asm volatile("buffer_load_dwordx4 %0, %1, %2 offen offset:2048 lds" :  : "v"(voB[2]), "s"(rW), "s"(kw_[7]) : "memory"); SB();
  asm volatile("s_add_u32 m0, %0, 0" :  : "s"(mW) : "memory", "scc"); SB();
  asm volatile("v_mfma_f32_16x16x32_bf16 %0, v[192:195], a[64:67], %0" : "+v"(a2[0][2]) :  : ); SB();
  asm volatile("ds_read_b128 a[124:127], %0 offset:40960" :  : "v"(aX[1][1]) : "memory"); SB();
  asm volatile("v_mfma_f32_16x16x32_bf16 %0, v[192:195], a[68:71], %0" : "+v"(a2[1][2]) :  : ); SB();
  asm volatile("buffer_load_dwordx4 %0, %1, %2 offen lds" :  : "v"(voB[0]), "s"(rW), "s"(kw_[8]) : "memory"); SB();
  asm volatile("v_mfma_f32_16x16x32_bf16 %0, v[192:195], %1, %0" : "+v"(c2[2]) : "v"(ones) : ); SB();
  asm volatile("s_waitcnt vmcnt(10)\n\ts_barrier" :  :  : "memory"); SB();
  asm volatile("ds_read_b64_tr_b16 v[172:173], %0 offset:12288" :  : "v"(aW[1][0][0]) : "memory"); SB();
  asm volatile("s_waitcnt lgkmcnt(5)\n\tv_mfma_f32_16x16x32_bf16 %0, %1, a[96:99], %0" : "+v"(rs) : "v"(ones) : ); SB();
  asm volatile("ds_read_b64_tr_b16 v[174:175], %0 offset:13056" :  : "v"(aW[1][0][0]) : "memory"); SB();
  asm volatile("v_mfma_f32_16x16x32_bf16 %0, a[96:99], a[96:99], %0" : "+v"(gr) :  : ); SB();
  asm volatile("ds_read_b64_tr_b16 v[176:177], %0 offset:12288" :  : "v"(aW[1][0][1]) : "memory"); SB();
  asm volatile("v_mfma_f32_16x16x32_bf16 %0, %1, a[104:107], %0" : "+v"(rs) : "v"(ones) : ); SB();
  asm volatile("ds_read_b64_tr_b16 v[178:179], %0 offset:13056" :  : "v"(aW[1][0][1]) : "memory"); SB();
  asm volatile("v_mfma_f32_16x16x32_bf16 %0, a[104:107], a[104:107], %0" : "+v"(gr) :  : ); SB();
  asm volatile("ds_read_b64_tr_b16 v[180:181], %0 offset:12288" :  : "v"(aW[1][0][2]) : "memory"); SB();
  asm volatile("v_mfma_f32_16x16x32_bf16 %0, v[160:163], a[72:75], %0" : "+v"(a2[0][0]) :  : ); SB();
  asm volatile("ds_read_b64_tr_b16 v[182:183], %0 offset:13056" :  : "v"(aW[1][0][2]) : "memory"); SB();
  asm volatile("v_mfma_f32_16x16x32_bf16 %0, v[160:163], a[76:79], %0" : "+v"(a2[1][0]) :  : ); SB();
  asm volatile("ds_read_b128 a[128:131], %0 offset:49152" :  : "v"(aX[0][0]) : "memory"); SB();
  asm volatile("v_mfma_f32_16x16x32_bf16 %0, v[164:167], a[72:75], %0" : "+v"(a2[0][1]) :  : ); SB();
  asm volatile("ds_read_b128 a[132:135], %0 offset:49152" :  : "v"(aX[0][1]) : "memory"); SB();
  asm volatile("v_mfma_f32_16x16x32_bf16 %0, v[164:167], a[76:79], %0" : "+v"(a2[1][1]) :  : ); SB();
  asm volatile("ds_read_b128 a[136:139], %0 offset:49152" :  : "v"(aX[1][0]) : "memory"); SB();
  asm volatile("s_waitcnt lgkmcnt(9)\n\tv_mfma_f32_16x16x32_bf16 %0, v[168:171], a[72:75], %0" : "+v"(a2[0][2]) :  : ); SB();
  asm volatile("ds_read_b128 a[140:143], %0 offset:49152" :  : "v"(aX[1][1]) : "memory"); SB();
  asm volatile("v_mfma_f32_16x16x32_bf16 %0, v[168:171], a[76:79], %0" : "+v"(a2[1][2]) :  : ); SB();
  asm volatile("ds_read_b128 a[144:147], %0 offset:57344" :  : "v"(aX[0][0]) : "memory"); SB();
  asm volatile("v_mfma_f32_16x16x32_bf16 %0, %1, a[112:115], %0" : "+v"(rs) : "v"(ones) : ); SB();
  asm volatile("ds_read_b128 a[148:151], %0 offset:57344" :  : "v"(aX[0][1]) : "memory"); SB();
  asm volatile("v_mfma_f32_16x16x32_bf16 %0, a[112:115], a[112:115], %0" : "+v"(gr) :  : ); SB();
  asm volatile("ds_read_b128 a[152:155], %0 offset:57344" :  : "v"(aX[1][0]) : "memory"); SB();
  asm volatile("v_mfma_f32_16x16x32_bf16 %0, %1, a[120:123], %0" : "+v"(rs) : "v"(ones) : ); SB();
  asm volatile("ds_read_b64_tr_b16 v[184:185], %0 offset:12288" :  : "v"(aW[1][1][0]) : "memory"); SB();
  asm volatile("buffer_load_dwordx4 %0, %1, %2 offen offset:1024 lds" :  : "v"(voB[1]), "s"(rW), "s"(kw_[8]) : "memory"); SB();
  asm volatile("v_mfma_f32_16x16x32_bf16 %0, a[120:123], a[120:123], %0" : "+v"(gr) :  : ); SB();
  asm volatile("ds_read_b64_tr_b16 v[186:187], %0 offset:13056" :  : "v"(aW[1][1][0]) : "memory"); SB();
  asm volatile("s_waitcnt lgkmcnt(9)\n\tv_mfma_f32_16x16x32_bf16 %0, v[172:175], a[80:83], %0" : "+v"(a2[0][0]) :  : ); SB();
  asm volatile("ds_read_b64_tr_b16 v[188:189], %0 offset:12288" :  : "v"(aW[1][1][1]) : "memory"); SB();
  asm volatile("v_mfma_f32_16x16x32_bf16 %0, v[172:175], a[84:87], %0" : "+v"(a2[1][0]) :  : ); SB();
  asm volatile("ds_read_b64_tr_b16 v[190:191], %0 offset:13056" :  : "v"(aW[1][1][1]) : "memory"); SB();
  asm volatile("v_mfma_f32_16x16x32_bf16 %0, v[172:175], %1, %0" : "+v"(c2[0]) : "v"(ones) : ); SB();
  asm volatile("ds_read_b64_tr_b16 v[192:193], %0 offset:12288" :  : "v"(aW[1][1][2]) : "memory"); SB();
  asm volatile("v_mfma_f32_16x16x32_bf16 %0, v[176:179], a[80:83], %0" : "+v"(a2[0][1]) :  : ); SB();
  asm volatile("ds_read_b64_tr_b16 v[194:195], %0 offset:13056" :  : "v"(aW[1][1][2]) : "memory"); SB();
  asm volatile("v_mfma_f32_16x16x32_bf16 %0, v[176:179], a[84:87], %0" : "+v"(a2[1][1]) :  : ); SB();
  asm volatile("ds_read_b128 a[156:159], %0 offset:57344" :  : "v"(aX[1][1]) : "memory"); SB();
  asm volatile("v_mfma_f32_16x16x32_bf16 %0, v[176:179], %1, %0" : "+v"(c2[1]) : "v"(ones) : ); SB();
  asm volatile("v_cmp_le_u32 vcc, %2, %1\n\ts_mov_b64 exec, vcc\n\tds_read_b64 %0, %1\n\ts_mov_b64 exec, -1" : "+v"(xv[0]) : "v"(aV[0]), "s"(lb + 131072) : "memory", "vcc"); SB();
  asm volatile("v_mfma_f32_16x16x32_bf16 %0, v[180:183], a[80:83], %0" : "+v"(a2[0][2]) :  : ); SB();
  asm volatile("v_cmp_le_u32 vcc, %2, %1\n\ts_mov_b64 exec, vcc\n\tds_read_b64 %0, %1\n\ts_mov_b64 exec, -1" : "+v"(xv[1]) : "v"(aV[1]), "s"(lb + 131072) : "memory", "vcc"); SB();
  asm volatile("v_mfma_f32_16x16x32_bf16 %0, v[180:183], a[84:87], %0" : "+v"(a2[1][2]) :  : ); SB();
  asm volatile("v_cmp_le_u32 vcc, %2, %1\n\ts_mov_b64 exec, vcc\n\tds_read_b64 %0, %1\n\ts_mov_b64 exec, -1" : "+v"(xv[2]) : "v"(aV[2]), "s"(lb + 131072) : "memory", "vcc"); SB();
  asm volatile("v_mfma_f32_16x16x32_bf16 %0, v[180:183], %1, %0" : "+v"(c2[2]) : "v"(ones) : ); SB();
  asm volatile("buffer_load_dwordx4 %0, %1, %2 offen offset:2048 lds" :  : "v"(voB[2]), "s"(rW), "s"(kw_[8]) : "memory"); SB();
  asm volatile("s_add_u32 m0, %0, 12288" :  : "s"(mW) : "memory", "scc"); SB();
  asm volatile("s_nop 0\n\tbuffer_load_dwordx4 %0, %1, %2 offen lds" :  : "v"(voB[0]), "s"(rW), "s"(kw_[9]) : "memory"); SB();
  asm volatile("buffer_load_dwordx4 %0, %1, %2 offen offset:1024 lds" :  : "v"(voB[1]), "s"(rW), "s"(kw_[9]) : "memory"); SB();
  asm volatile("buffer_load_dwordx4 %0, %1, %2 offen offset:2048 lds" :  : "v"(voB[2]), "s"(rW), "s"(kw_[9]) : "memory"); SB();
  asm volatile("buffer_load_dword v242, off, %0, %1" :  : "s"(rF), "s"(hpo[0]) : "memory"); SB();
  asm volatile("s_add_u32 m0, %0, 98304" :  : "s"(mA) : "memory", "scc"); SB();
  asm volatile("s_nop 0\n\tbuffer_load_dwordx4 %0, %1, %2 offen lds" :  : "v"(voK[0]), "s"(rK), "s"(0u) : "memory"); SB();
  asm volatile("s_waitcnt vmcnt(11)\n\ts_barrier" :  :  : "memory"); SB();
  asm volatile("ds_read_b64_tr_b16 v[160:161], %0 offset:24576" :  : "v"(aW[1][0][0]) : "memory"); SB();
  asm volatile("s_waitcnt lgkmcnt(1)\n\tv_mfma_f32_16x16x32_bf16 %0, %1, a[128:131], %0" : "+v"(rs) : "v"(ones) : ); SB();
  asm volatile("ds_read_b64_tr_b16 v[162:163], %0 offset:25344" :  : "v"(aW[1][0][0]) : "memory"); SB();
  asm volatile("v_mfma_f32_16x16x32_bf16 %0, a[128:131], a[128:131], %0" : "+v"(gr) :  : ); SB();
  asm volatile("ds_read_b64_tr_b16 v[164:165], %0 offset:24576" :  : "v"(aW[1][0][1]) : "memory"); SB();
  asm volatile("v_mfma_f32_16x16x32_bf16 %0, %1, a[136:139], %0" : "+v"(rs) : "v"(ones) : ); SB();
  asm volatile("ds_read_b64_tr_b16 v[166:167], %0 offset:25344" :  : "v"(aW[1][0][1]) : "memory"); SB();
  asm volatile("v_mfma_f32_16x16x32_bf16 %0, a[136:139], a[136:139], %0" : "+v"(gr) :  : ); SB();
  asm volatile("ds_read_b64_tr_b16 v[168:169], %0 offset:24576" :  : "v"(aW[1][0][2]) : "memory"); SB();
  asm volatile("v_mfma_f32_16x16x32_bf16 %0, v[184:187], a[88:91], %0" : "+v"(a2[0][0]) :  : ); SB();
  asm volatile("ds_read_b64_tr_b16 v[170:171], %0 offset:25344" :  : "v"(aW[1][0][2]) : "memory"); SB();
  asm volatile("v_mfma_f32_16x16x32_bf16 %0, v[184:187], a[92:95], %0" : "+v"(a2[1][0]) :  : ); SB();
  asm volatile("v_cmp_le_u32 vcc, %2, %1\n\ts_mov_b64 exec, vcc\n\tds_read_b64 %0, %1\n\ts_mov_b64 exec, -1" : "+v"(xv[3]) : "v"(aV[3]), "s"(lb + 131072) : "memory", "vcc"); SB();
  asm volatile("v_mfma_f32_16x16x32_bf16 %0, v[188:191], a[88:91], %0" : "+v"(a2[0][1]) :  : ); SB();
  asm volatile("global_load_ushort v245, %0, %1 offset:0" :  : "v"(vD), "s"(ssm_D) : "memory"); SB();
  asm volatile("v_mfma_f32_16x16x32_bf16 %0, v[188:191], a[92:95], %0" : "+v"(a2[1][1]) :  : ); SB();
  asm volatile("buffer_load_dwordx4 %0, %1, %2 offen offset:1024 lds" :  : "v"(voK[1]), "s"(rK), "s"(0u) : "memory"); SB();
  asm volatile("v_mfma_f32_16x16x32_bf16 %0, v[192:195], a[88:91], %0" : "+v"(a2[0][2]) :  : ); SB();
  asm volatile("ds_read_b64_tr_b16 v[172:173], %0 offset:24576" :  : "v"(aW[1][1][0]) : "memory"); SB();
  asm volatile("v_mfma_f32_16x16x32_bf16 %0, v[192:195], a[92:95], %0" : "+v"(a2[1][2]) :  : ); SB();
  asm volatile("ds_read_b64_tr_b16 v[174:175], %0 offset:25344" :  : "v"(aW[1][1][0]) : "memory"); SB();
  asm volatile("v_mfma_f32_16x16x32_bf16 %0, %1, a[144:147], %0" : "+v"(rs) : "v"(ones) : ); SB();
  asm volatile("ds_read_b64_tr_b16 v[176:177], %0 offset:24576" :  : "v"(aW[1][1][1]) : "memory"); SB();
  asm volatile("v_mfma_f32_16x16x32_bf16 %0, a[144:147], a[144:147], %0" : "+v"(gr) :  : ); SB();
  asm volatile("ds_read_b64_tr_b16 v[178:179], %0 offset:25344" :  : "v"(aW[1][1][1]) : "memory"); SB();
  asm volatile("buffer_load_dwordx4 %0, %1, %2 offen offset:2048 lds" :  : "v"(voK[2]), "s"(rK), "s"(0u) : "memory"); SB();
  asm volatile("v_mfma_f32_16x16x32_bf16 %0, %1, a[152:155], %0" : "+v"(rs) : "v"(ones) : ); SB();
  asm volatile("ds_read_b64_tr_b16 v[180:181], %0 offset:24576" :  : "v"(aW[1][1][2]) : "memory"); SB();
  asm volatile("v_mfma_f32_16x16x32_bf16 %0, a[152:155], a[152:155], %0" : "+v"(gr) :  : ); SB();
  asm volatile("ds_read_b64_tr_b16 v[182:183], %0 offset:25344" :  : "v"(aW[1][1][2]) : "memory"); SB();
  asm volatile("s_waitcnt lgkmcnt(7)\n\tv_mfma_f32_16x16x32_bf16 %0, v[160:163], a[96:99], %0" : "+v"(a2[0][0]) :  : ); SB();
  asm volatile("s_waitcnt vmcnt(11)\n\ts_barrier" :  :  : "memory"); SB();
  asm volatile("ds_read_b64_tr_b16 v[184:185], %0 offset:36864" :  : "v"(aW[1][0][0]) : "memory"); SB();
  asm volatile("buffer_load_dword v243, off, %0, %1" :  : "s"(rF), "s"(hpo[1]) : "memory"); SB();
  asm volatile("v_mfma_f32_16x16x32_bf16 %0, v[160:163], a[100:103], %0" : "+v"(a2[1][0]) :  : ); SB();
  asm volatile("ds_read_b64_tr_b16 v[186:187], %0 offset:37632" :  : "v"(aW[1][0][0]) : "memory"); SB();
  asm volatile("s_mov_b32 exec_lo, 0xff\n\ts_mov_b32 exec_hi, 0\n\tbuffer_load_dwordx4 %0, %1, %2 offen offset:3072 lds\n\ts_mov_b64 exec, -1" :  : "v"(voK[3]), "s"(rK), "s"(0u) : "memory"); SB();
  asm volatile("v_mfma_f32_16x16x32_bf16 %0, v[160:163], %1, %0" : "+v"(c2[0]) : "v"(ones) : ); SB();
  asm volatile("ds_read_b64_tr_b16 v[188:189], %0 offset:36864" :  : "v"(aW[1][0][1]) : "memory"); SB();
  asm volatile("v_mfma_f32_16x16x32_bf16 %0, v[164:167], a[96:99], %0" : "+v"(a2[0][1]) :  : ); SB();
  asm volatile("ds_read_b64_tr_b16 v[190:191], %0 offset:37632" :  : "v"(aW[1][0][1]) : "memory"); SB();
  asm volatile("s_mov_b32 exec_lo, -1\n\ts_mov_b32 exec_hi, 0xff\n\tbuffer_load_dwordx4 %0, %1, %2 offen offset:3328 lds\n\ts_mov_b64 exec, -1" :  : "v"(voH), "s"(rS), "s"(0u) : "memory"); SB();
  asm volatile("v_mfma_f32_16x16x32_bf16 %0, v[164:167], a[100:103], %0" : "+v"(a2[1][1]) :  : ); SB();
  asm volatile("v_cmp_le_u32 vcc, %2, %1\n\ts_mov_b64 exec, vcc\n\tds_read_b64 %0, %1\n\ts_mov_b64 exec, -1" : "+v"(xv[4]) : "v"(aV[4]), "s"(lb + 131072) : "memory", "vcc"); SB();
  asm volatile("ds_read_b64_tr_b16 v[192:193], %0 offset:36864" :  : "v"(aW[1][0][2]) : "memory"); SB();
  asm volatile("s_add_u32 m0, %0, 102400" :  : "s"(mA) : "memory", "scc"); SB();
  asm volatile("v_mfma_f32_16x16x32_bf16 %0, v[164:167], %1, %0" : "+v"(c2[1]) : "v"(ones) : ); SB();
  asm volatile("ds_read_b64_tr_b16 v[194:195], %0 offset:37632" :  : "v"(aW[1][0][2]) : "memory"); SB();
  asm volatile("buffer_load_dwordx4 %0, %1, %2 offen lds" :  : "v"(voV[0]), "s"(rV), "s"(0u) : "memory"); SB();
  asm volatile("v_mfma_f32_16x16x32_bf16 %0, v[168:171], a[96:99], %0" : "+v"(a2[0][2]) :  : ); SB();
  asm volatile("global_load_ushort v246, %0, %1 offset:2" :  : "v"(vD), "s"(ssm_D) : "memory"); SB();
  asm volatile("v_mfma_f32_16x16x32_bf16 %0, v[168:171], a[100:103], %0" : "+v"(a2[1][2]) :  : ); SB();
  asm volatile("v_mfma_f32_16x16x32_bf16 %0, v[168:171], %1, %0" : "+v"(c2[2]) : "v"(ones) : ); SB();
  asm volatile("s_waitcnt lgkmcnt(5)\n\tv_mfma_f32_16x16x32_bf16 %0, v[172:175], a[104:107], %0" : "+v"(a2[0][0]) :  : ); SB();
  asm volatile("ds_read_b64_tr_b16 v[160:161], %0 offset:36864" :  : "v"(aW[1][1][0]) : "memory"); SB();
  asm volatile("v_cndmask_b32 %0, %1, %2, %3" : "=v"(xt[0]) : "v"(gr[0]), "v"(gr[1]), "s"(0xAAAAAAAAAAAAAAAAull) : ); SB();
  asm volatile("v_mfma_f32_16x16x32_bf16 %0, v[172:175], a[108:111], %0" : "+v"(a2[1][0]) :  : ); SB();
  asm volatile("s_mov_b32 exec_lo, 0x10001\n\ts_mov_b32 exec_hi, 0x10001\n\tds_write_b128 %0, %1 offset:0\n\ts_mov_b64 exec, -1" :  : "v"(ca), "v"(c2[0]) : "memory"); SB();
  asm volatile("ds_read_b64_tr_b16 v[162:163], %0 offset:37632" :  : "v"(aW[1][1][0]) : "memory"); SB();
  asm volatile("buffer_load_dwordx4 %0, %1, %2 offen offset:1024 lds" :  : "v"(voV[1]), "s"(rV), "s"(0u) : "memory"); SB();
  asm volatile("v_mfma_f32_16x16x32_bf16 %0, v[176:179], a[104:107], %0" : "+v"(a2[0][1]) :  : ); SB();
  asm volatile("ds_read_b64_tr_b16 v[164:165], %0 offset:36864" :  : "v"(aW[1][1][1]) : "memory"); SB();
  asm volatile("s_mov_b32 exec_lo, -1\n\ts_mov_b32 exec_hi, 0\n\tbuffer_load_dwordx4 %0, %1, %2 offen offset:2048 lds\n\ts_mov_b64 exec, -1" :  : "v"(voV[2]), "s"(rV), "s"(0u) : "memory"); SB();
  asm volatile("v_mfma_f32_16x16x32_bf16 %0, v[176:179], a[108:111], %0" : "+v"(a2[1][1]) :  : ); SB();
  asm volatile("s_mov_b32 exec_lo, 0x10001\n\ts_mov_b32 exec_hi, 0x10001\n\tds_write_b128 %0, %1 offset:64\n\ts_mov_b64 exec, -1" :  : "v"(ca), "v"(c2[1]) : "memory"); SB();
  asm volatile("ds_read_b64_tr_b16 v[166:167], %0 offset:37632" :  : "v"(aW[1][1][1]) : "memory"); SB();
  asm volatile("buffer_load_dword v244, off, %0, %1" :  : "s"(rF), "s"(hpo[2]) : "memory"); SB();
  asm volatile("v_mfma_f32_16x16x32_bf16 %0, v[180:183], a[104:107], %0" : "+v"(a2[0][2]) :  : ); SB();
  asm volatile("ds_read_b64_tr_b16 v[168:169], %0 offset:36864" :  : "v"(aW[1][1][2]) : "memory"); SB();
  asm volatile("v_cndmask_b32 %0, %1, %2, %3" : "=v"(xt[1]) : "v"(gr[2]), "v"(gr[3]), "s"(0xAAAAAAAAAAAAAAAAull) : ); SB();
  asm volatile("v_mfma_f32_16x16x32_bf16 %0, v[180:183], a[108:111], %0" : "+v"(a2[1][2]) :  : ); SB();
  asm volatile("s_mov_b32 exec_lo, 0x10001\n\ts_mov_b32 exec_hi, 0x10001\n\tds_write_b128 %0, %1 offset:128\n\ts_mov_b64 exec, -1" :  : "v"(ca), "v"(c2[2]) : "memory"); SB();
  asm volatile("ds_read_b64_tr_b16 v[170:171], %0 offset:37632" :  : "v"(aW[1][1][2]) : "memory"); SB();
  asm volatile("buffer_load_dwordx4 %0, %1, %2 offen offset:3072 lds" :  : "v"(voO), "s"(rO), "s"(0u) : "memory"); SB();
  asm volatile("v_mfma_f32_16x16x32_bf16 %0, v[184:187], a[112:115], %0" : "+v"(a2[0][0]) :  : ); SB();
  asm volatile("s_add_u32 m0, %0, 24576" :  : "s"(mW) : "memory", "scc"); SB();
  asm volatile("v_mul_f32 %0, 0x3acccccd, %1" : "=v"(xs[0][0]) : "v"(rs[0]) : ); SB();
  asm volatile("v_mfma_f32_16x16x32_bf16 %0, v[184:187], a[116:119], %0" : "+v"(a2[1][0]) :  : ); SB();
  asm volatile("buffer_load_dwordx4 %0, %1, %2 offen lds" :  : "v"(voB[0]), "s"(rW1), "s"(kw_[0]) : "memory"); SB();
  asm volatile("v_cndmask_b32 %0, %0, %1, %2" : "+v"(xt[0]) : "v"(xt[1]), "s"(0xCCCCCCCCCCCCCCCCull) : ); SB();
  asm volatile("v_mfma_f32_16x16x32_bf16 %0, v[184:187], %1, %0" : "+v"(c2[0]) : "v"(ones) : ); SB();
  asm volatile("v_mul_f32 %0, 0x3a83126f, %1" : "=v"(xt[1]) : "v"(rs[0]) : ); SB();
  asm volatile("s_waitcnt lgkmcnt(5)\n\tv_mfma_f32_16x16x32_bf16 %0, v[188:191], a[112:115], %0" : "+v"(a2[0][1]) :  : ); SB();
  asm volatile("s_waitcnt vmcnt(18)\n\ts_barrier" :  :  : "memory"); SB();
  asm volatile("ds_read_b64_tr_b16 v[172:173], %0 offset:0" :  : "v"(aW[0][0][0]) : "memory"); SB();
  asm volatile("v_fma_f32 %0, -%1, %2, %3" : "=v"(xs[0][1]) : "v"(rs[0]), "v"(xs[0][0]), "v"(xt[0]) : ); SB();
  asm volatile("v_mfma_f32_16x16x32_bf16 %0, v[188:191], a[116:119], %0" : "+v"(a2[1][1]) :  : ); SB();
  asm volatile("ds_read_b64_tr_b16 v[174:175], %0 offset:768" :  : "v"(aW[0][0][0]) : "memory"); SB();
  asm volatile("v_mul_f32 %0, %0, %1" : "+v"(xt[1]) : "v"(xs[0][0]) : ); SB();
  asm volatile("v_mfma_f32_16x16x32_bf16 %0, v[188:191], %1, %0" : "+v"(c2[1]) : "v"(ones) : ); SB();
  asm volatile("ds_read_b64_tr_b16 v[176:177], %0 offset:0" :  : "v"(aW[0][0][1]) : "memory"); SB();
  asm volatile("v_mfma_f32_16x16x32_bf16 %0, v[192:195], a[112:115], %0" : "+v"(a2[0][2]) :  : ); SB();
  asm volatile("ds_read_b64_tr_b16 v[178:179], %0 offset:768" :  : "v"(aW[0][0][1]) : "memory"); SB();
  asm volatile("v_mfma_f32_16x16x32_bf16 %0, v[192:195], a[116:119], %0" : "+v"(a2[1][2]) :  : ); SB();
  asm volatile("ds_read_b64_tr_b16 v[180:181], %0 offset:0" :  : "v"(aW[0][0][2]) : "memory"); SB();
  asm volatile("v_mfma_f32_16x16x32_bf16 %0, v[192:195], %1, %0" : "+v"(c2[2]) : "v"(ones) : ); SB();
  asm volatile("ds_read_b64_tr_b16 v[182:183], %0 offset:768" :  : "v"(aW[0][0][2]) : "memory"); SB();
  asm volatile("v_mfma_f32_16x16x32_bf16 %0, v[160:163], a[120:123], %0" : "+v"(a2[0][0]) :  : ); SB();
  if (__builtin_expect((__builtin_amdgcn_ballot_w64(!(xs[0][1] >= xt[1])) & 0xF0000F0000F0000Full) != 0, 0)) {
    const f32x2 mm = {xs[0][0], xs[0][0]};
    f32x2 q2 = {0.f, 0.f};
    const u32x4* xp = (const u32x4*)(x + (size_t)(rb * 64 + w * 16 + l15) * 1280 + g4 * 320);
#pragma unroll 1
    for (int c = 0; c < 20; ++c) {
      u32x4 xq;
      asm volatile("global_load_dwordx4 %0, %1, off\n\ts_waitcnt vmcnt(0)" : "=&v"(xq) : "v"(xp + c) : "memory");
#pragma unroll
      for (int e = 0; e < 4; ++e) { const f32x2 d = unpk(xq[e]) - mm; q2 = __builtin_elementwise_fma(d, d, q2); }
    }
    xs[0][1] = red32(red16(q2[0] + q2[1]));
  }
  SB();
  asm volatile("v_mfma_f32_16x16x32_bf16 %0, v[160:163], a[124:127], %0" : "+v"(a2[1][0]) :  : ); SB();
  asm volatile("global_load_ushort v247, %0, %1 offset:4" :  : "v"(vD2), "s"(ssm_D) : "memory"); SB();
  asm volatile("v_max_f32 %0, 0, %0" : "+v"(xs[0][1]) :  : ); SB();
  asm volatile("v_mfma_f32_16x16x32_bf16 %0, v[160:163], %1, %0" : "+v"(c2[0]) : "v"(ones) : ); SB();
  asm volatile("buffer_load_dwordx4 %0, %1, %2 offen offset:1024 lds" :  : "v"(voB[1]), "s"(rW1), "s"(kw_[0]) : "memory"); SB();
  asm volatile("s_waitcnt lgkmcnt(5)\n\tv_mfma_f32_16x16x32_bf16 %0, v[164:167], a[120:123], %0" : "+v"(a2[0][1]) :  : ); SB();
  asm volatile("ds_read_b64_tr_b16 v[184:185], %0 offset:0" :  : "v"(aW[0][1][0]) : "memory"); SB();
  asm volatile("v_mul_f32 %0, 0x3acccccd, %0" : "+v"(xs[0][1]) :  : ); SB();
  asm volatile("v_mfma_f32_16x16x32_bf16 %0, v[164:167], a[124:127], %0" : "+v"(a2[1][1]) :  : ); SB();
  asm volatile("ds_read_b64_tr_b16 v[186:187], %0 offset:768" :  : "v"(aW[0][1][0]) : "memory"); SB();
  asm volatile("v_cvt_pk_bf16_f32 %0, %0, %0" : "+v"(xs[0][1]) :  : ); SB();
  asm volatile("v_mfma_f32_16x16x32_bf16 %0, v[164:167], %1, %0" : "+v"(c2[1]) : "v"(ones) : ); SB();
  asm volatile("ds_read_b64_tr_b16 v[188:189], %0 offset:0" :  : "v"(aW[0][1][1]) : "memory"); SB();
  asm volatile("v_lshlrev_b32 %0, 16, %0" : "+v"(xs[0][1]) :  : ); SB();
  asm volatile("v_mfma_f32_16x16x32_bf16 %0, v[168:171], a[120:123], %0" : "+v"(a2[0][2]) :  : ); SB();
  asm volatile("ds_read_b64_tr_b16 v[190:191], %0 offset:768" :  : "v"(aW[0][1][1]) : "memory"); SB();
  asm volatile("v_add_f32 %0, 0x3727c5ac, %0" : "+v"(xs[0][1]) :  : ); SB();
  asm volatile("v_mfma_f32_16x16x32_bf16 %0, v[168:171], a[124:127], %0" : "+v"(a2[1][2]) :  : ); SB();
  asm volatile("ds_read_b64_tr_b16 v[192:193], %0 offset:0" :  : "v"(aW[0][1][2]) : "memory"); SB();
  asm volatile("v_cvt_pk_bf16_f32 %0, %0, %0" : "+v"(xs[0][1]) :  : ); SB();
  asm volatile("v_mfma_f32_16x16x32_bf16 %0, v[168:171], %1, %0" : "+v"(c2[2]) : "v"(ones) : ); SB();
  asm volatile("ds_read_b64_tr_b16 v[194:195], %0 offset:768" :  : "v"(aW[0][1][2]) : "memory"); SB();
  asm volatile("s_waitcnt lgkmcnt(6)\n\tv_mfma_f32_16x16x32_bf16 %0, v[172:175], a[128:131], %0" : "+v"(a2[0][0]) :  : ); SB();
  asm volatile("global_load_ushort v248, %0, %1" :  : "v"(vBs), "s"(b_sp) : "memory"); SB();
  asm volatile("v_lshlrev_b32 %0, 16, %0" : "+v"(xs[0][1]) :  : ); SB();
  asm volatile("v_mfma_f32_16x16x32_bf16 %0, v[172:175], a[132:135], %0" : "+v"(a2[1][0]) :  : ); SB();
  asm volatile("buffer_load_dwordx4 %0, %1, %2 offen offset:2048 lds" :  : "v"(voB[2]), "s"(rW1), "s"(kw_[0]) : "memory"); SB();
  asm volatile("v_mfma_f32_16x16x32_bf16 %0, v[172:175], %1, %0" : "+v"(c2[0]) : "v"(ones) : ); SB();
  asm volatile("v_sqrt_f32 %0, %0" : "+v"(xs[0][1]) :  : ); SB();
  asm volatile("v_mfma_f32_16x16x32_bf16 %0, v[176:179], a[128:131], %0" : "+v"(a2[0][1]) :  : ); SB();
  asm volatile("v_cvt_pk_bf16_f32 %0, %0, %0" : "+v"(xs[0][0]) :  : ); SB();
  asm volatile("v_lshlrev_b32 %0, 16, %0" : "+v"(xs[0][0]) :  : ); SB();
  asm volatile("v_cvt_pk_bf16_f32 %0, %0, %0" : "+v"(xs[0][1]) :  : ); SB();
  asm volatile("v_lshlrev_b32 %0, 16, %0" : "+v"(xs[0][1]) :  : ); SB();
  asm volatile("v_rcp_f32 %0, %1" : "=v"(xt[0]) : "v"(xs[0][1]) : ); SB();
  asm volatile("s_nop 0\n\tv_mul_f32 %0, 0xc0135761, %1" : "=v"(xs[0][2]) : "v"(xt[0]) : ); SB();
  asm volatile("v_mul_f32 %0, %1, %1" : "=v"(xt[1]) : "v"(xt[0]) : ); SB();
  asm volatile("v_mul_f32 %0, 0xbdd2d3e8, %0" : "+v"(xt[0]) :  : ); SB();
  asm volatile("v_mul_f32 %0, %1, %2" : "=v"(xs[0][3]) : "v"(xt[0]), "v"(xt[1]) : ); SB();
  asm volatile("s_mov_b32 exec_lo, 0xf0000f\n\ts_mov_b32 exec_hi, 0xf0000f00\n\tds_write_b128 %0, %1\n\ts_mov_b64 exec, -1" :  : "v"(xw), "v"(xs[0]) : "memory"); SB();
  asm volatile("" : "+v"(xv[0]), "+v"(xv[1]), "+v"(xv[2]), "+v"(xv[3]), "+v"(xv[4]) :  : "memory"); SB();
  asm volatile("s_waitcnt vmcnt(19) lgkmcnt(0)\n\ts_barrier" :  :  : "memory"); SB();
  asm volatile("ds_read_b64_tr_b16 v[160:161], %0 offset:12288" :  : "v"(aW[0][0][0]) : "memory"); SB();
  asm volatile("v_mfma_f32_16x16x32_bf16 %0, v[176:179], a[132:135], %0" : "+v"(a2[1][1]) :  : ); SB();
  asm volatile("ds_read_b64_tr_b16 v[162:163], %0 offset:13056" :  : "v"(aW[0][0][0]) : "memory"); SB();
  asm volatile("v_mfma_f32_16x16x32_bf16 %0, v[176:179], %1, %0" : "+v"(c2[1]) : "v"(ones) : ); SB();
  asm volatile("ds_read_b64_tr_b16 v[164:165], %0 offset:12288" :  : "v"(aW[0][0][1]) : "memory"); SB();
  asm volatile("v_mfma_f32_16x16x32_bf16 %0, v[180:183], a[128:131], %0" : "+v"(a2[0][2]) :  : ); SB();
  asm volatile("ds_read_b64_tr_b16 v[166:167], %0 offset:13056" :  : "v"(aW[0][0][1]) : "memory"); SB();
  asm volatile("v_mfma_f32_16x16x32_bf16 %0, v[180:183], a[132:135], %0" : "+v"(a2[1][2]) :  : ); SB();
  asm volatile("ds_read_b64_tr_b16 v[168:169], %0 offset:12288" :  : "v"(aW[0][0][2]) : "memory"); SB();
  asm volatile("v_mfma_f32_16x16x32_bf16 %0, v[180:183], %1, %0" : "+v"(c2[2]) : "v"(ones) : ); SB();
  asm volatile("ds_read_b64_tr_b16 v[170:171], %0 offset:13056" :  : "v"(aW[0][0][2]) : "memory"); SB();
  asm volatile("v_mfma_f32_16x16x32_bf16 %0, v[184:187], a[136:139], %0" : "+v"(a2[0][0]) :  : ); SB();
  asm volatile("ds_read_b128 %0, %1 offset:0" : "=&v"(xs[0]) : "v"(xw) : "memory"); SB();
  asm volatile("v_mfma_f32_16x16x32_bf16 %0, v[184:187], a[140:143], %0" : "+v"(a2[1][0]) :  : ); SB();
  asm volatile("ds_read_b128 %0, %1 offset:0" : "=&v"(xs[1]) : "v"(xr_) : "memory"); SB();
  asm volatile("v_mfma_f32_16x16x32_bf16 %0, v[184:187], %1, %0" : "+v"(c2[0]) : "v"(ones) : ); SB();
  asm volatile("ds_read_b128 %0, %1 offset:0" : "=&v"(co[0]) : "v"(cr) : "memory"); SB();
  asm volatile("v_mfma_f32_16x16x32_bf16 %0, v[188:191], a[136:139], %0" : "+v"(a2[0][1]) :  : ); SB();
  asm volatile("ds_read_b64_tr_b16 v[172:173], %0 offset:12288" :  : "v"(aW[0][1][0]) : "memory"); SB();
  asm volatile("v_mfma_f32_16x16x32_bf16 %0, v[188:191], a[140:143], %0" : "+v"(a2[1][1]) :  : ); SB();
  asm volatile("ds_read_b64_tr_b16 v[174:175], %0 offset:13056" :  : "v"(aW[0][1][0]) : "memory"); SB();
  asm volatile("v_mfma_f32_16x16x32_bf16 %0, v[188:191], %1, %0" : "+v"(c2[1]) : "v"(ones) : ); SB();
  asm volatile("ds_read_b64_tr_b16 v[176:177], %0 offset:12288" :  : "v"(aW[0][1][1]) : "memory"); SB();
  asm volatile("v_mfma_f32_16x16x32_bf16 %0, v[192:195], a[136:139], %0" : "+v"(a2[0][2]) :  : ); SB();
  asm volatile("ds_read_b64_tr_b16 v[178:179], %0 offset:13056" :  : "v"(aW[0][1][1]) : "memory"); SB();
  asm volatile("v_mfma_f32_16x16x32_bf16 %0, v[192:195], a[140:143], %0" : "+v"(a2[1][2]) :  : ); SB();
  asm volatile("ds_read_b64_tr_b16 v[180:181], %0 offset:12288" :  : "v"(aW[0][1][2]) : "memory"); SB();
  asm volatile("v_mfma_f32_16x16x32_bf16 %0, v[192:195], %1, %0" : "+v"(c2[2]) : "v"(ones) : ); SB();
  asm volatile("ds_read_b64_tr_b16 v[182:183], %0 offset:13056" :  : "v"(aW[0][1][2]) : "memory"); SB();
  asm volatile("s_waitcnt lgkmcnt(9)\n\tv_mfma_f32_16x16x32_bf16 %0, v[160:163], a[144:147], %0" : "+v"(a2[0][0]) :  : ); SB();
  asm volatile("ds_read_b128 %0, %1 offset:64" : "=&v"(co[1]) : "v"(cr) : "memory"); SB();
  asm volatile("v_mfma_f32_16x16x32_bf16 %0, v[160:163], a[148:151], %0" : "+v"(a2[1][0]) :  : ); SB();
  asm volatile("ds_read_b128 %0, %1 offset:128" : "=&v"(co[2]) : "v"(cr) : "memory"); SB();
  asm volatile("v_mfma_f32_16x16x32_bf16 %0, v[160:163], %1, %0" : "+v"(c2[0]) : "v"(ones) : ); SB();
  asm volatile("v_mfma_f32_16x16x32_bf16 %0, v[164:167], a[144:147], %0" : "+v"(a2[0][1]) :  : ); SB();
  asm volatile("v_mfma_f32_16x16x32_bf16 %0, v[164:167], a[148:151], %0" : "+v"(a2[1][1]) :  : ); SB();
  asm volatile("v_mfma_f32_16x16x32_bf16 %0, v[164:167], %1, %0" : "+v"(c2[1]) : "v"(ones) : ); SB();
  asm volatile("v_mfma_f32_16x16x32_bf16 %0, v[168:171], a[144:147], %0" : "+v"(a2[0][2]) :  : ); SB();
  asm volatile("v_mfma_f32_16x16x32_bf16 %0, v[168:171], a[148:151], %0" : "+v"(a2[1][2]) :  : ); SB();
  asm volatile("v_mfma_f32_16x16x32_bf16 %0, v[168:171], %1, %0" : "+v"(c2[2]) : "v"(ones) : ); SB();
  asm volatile("s_waitcnt lgkmcnt(3)\n\tv_mfma_f32_16x16x32_bf16 %0, v[172:175], a[152:155], %0" : "+v"(a2[0][0]) :  : ); SB();
  asm volatile("v_mfma_f32_16x16x32_bf16 %0, v[172:175], a[156:159], %0" : "+v"(a2[1][0]) :  : ); SB();
  asm volatile("s_add_u32 m0, %0, 36864" :  : "s"(mW) : "memory", "scc"); SB();
  asm volatile("v_mfma_f32_16x16x32_bf16 %0, v[172:175], %1, %0" : "+v"(c2[0]) : "v"(ones) : ); SB();
  asm volatile("buffer_load_dwordx4 %0, %1, %2 offen lds" :  : "v"(voB[0]), "s"(rW1), "s"(kw_[1]) : "memory"); SB();
  asm volatile("v_mfma_f32_16x16x32_bf16 %0, v[176:179], a[152:155], %0" : "+v"(a2[0][1]) :  : ); SB();
  asm volatile("v_mfma_f32_16x16x32_bf16 %0, v[176:179], a[156:159], %0" : "+v"(a2[1][1]) :  : ); SB();
  asm volatile("v_mfma_f32_16x16x32_bf16 %0, v[176:179], %1, %0" : "+v"(c2[1]) : "v"(ones) : ); SB();
  asm volatile("s_waitcnt lgkmcnt(0)\n\tv_mfma_f32_16x16x32_bf16 %0, v[180:183], a[152:155], %0" : "+v"(a2[0][2]) :  : ); SB();
  asm volatile("v_mfma_f32_16x16x32_bf16 %0, v[180:183], a[156:159], %0" : "+v"(a2[1][2]) :  : ); SB();
  asm volatile("v_mfma_f32_16x16x32_bf16 %0, v[180:183], %1, %0" : "+v"(c2[2]) : "v"(ones) : ); SB();
  ST(3);
  ST(5);
  const unsigned long long tg = ((unsigned long long)tg1 << 32) | tg0;
  asm volatile("" : "+v"(xs[0]), "+v"(xs[1]), "+v"(co[0]), "+v"(co[1]), "+v"(co[2]) :  : "memory"); SB();
  asm volatile("s_nop 3" : "+v"(a2[0][0]), "+v"(a2[0][1]), "+v"(a2[0][2]), "+v"(a2[1][0]), "+v"(a2[1][1]), "+v"(a2[1][2]), "+v"(c2[0]), "+v"(c2[1]), "+v"(c2[2]) :  : ); SB();
  for (int i = 0; i < 2; ++i) { xm[i] = xs[i][0]; xd[i] = xs[i][1]; k1[i] = xs[i][2]; k2[i] = xs[i][3]; }
  c2[0] += co[0]; c2[1] += co[1]; c2[2] += co[2];
  f32x2 ge2[2];
  auto act = [&](f32x2& t, f32x2& e, const f32x4& a, const f32x4& cs, int i, int h) {
    t = __builtin_elementwise_fma((f32x2){-xm[i], -xm[i]}, (f32x2){cs[2 * h], cs[2 * h + 1]}, (f32x2){a[2 * h], a[2 * h + 1]});
    const f32x2 u = t * __builtin_elementwise_fma(t * t, (f32x2){k2[i], k2[i]}, (f32x2){k1[i], k1[i]});
    e = (f32x2){__builtin_amdgcn_exp2f(u[0]), __builtin_amdgcn_exp2f(u[1])};
  };
  auto act2 = [&](f32x2& t, f32x2 e, int i) {
    e = __builtin_elementwise_fma(e, (f32x2){xd[i], xd[i]}, (f32x2){xd[i], xd[i]});
    t *= (f32x2){__builtin_amdgcn_rcpf(e[0]), __builtin_amdgcn_rcpf(e[1])};
  };
  f32x2 gv[2][3][2];
  float sv[2][2], pv[2];
  act(gv[0][0][0], ge2[0], a2[0][0], c2[0], 0, 0); act(gv[0][0][1], ge2[1], a2[0][0], c2[0], 0, 1);
  asm volatile("" : "+v"(gv[0][0][0]), "+v"(gv[0][0][1]), "+v"(ge2[0]), "+v"(ge2[1]));
  SB();
  asm volatile("buffer_load_dwordx4 %0, %1, %2 offen offset:1024 lds" :  : "v"(voB[1]), "s"(rW1), "s"(kw_[1]) : "memory"); SB();
  asm volatile("buffer_load_dwordx4 %0, %1, %2 offen offset:2048 lds" :  : "v"(voB[2]), "s"(rW1), "s"(kw_[1]) : "memory"); SB();
  act2(gv[0][0][0], ge2[0], 0); act2(gv[0][0][1], ge2[1], 0);
  asm volatile("" : "+v"(gv[0][0][0]), "+v"(gv[0][0][1]));
  SB();
  asm volatile("s_add_u32 m0, %0, 49152" :  : "s"(mW) : "memory", "scc"); SB();
  asm volatile("s_nop 0\n\tbuffer_load_dwordx4 %0, %1, %2 offen lds" :  : "v"(voB[0]), "s"(rW1), "s"(kw_[2]) : "memory"); SB();
  act(gv[0][1][0], ge2[0], a2[0][1], c2[1], 0, 0); act(gv[0][1][1], ge2[1], a2[0][1], c2[1], 0, 1);
  asm volatile("" : "+v"(gv[0][1][0]), "+v"(gv[0][1][1]), "+v"(ge2[0]), "+v"(ge2[1]));
  SB();
  asm volatile("buffer_load_dwordx4 %0, %1, %2 offen offset:1024 lds" :  : "v"(voB[1]), "s"(rW1), "s"(kw_[2]) : "memory"); SB();
  asm volatile("buffer_load_dwordx4 %0, %1, %2 offen offset:2048 lds" :  : "v"(voB[2]), "s"(rW1), "s"(kw_[2]) : "memory"); SB();
  act2(gv[0][1][0], ge2[0], 0); act2(gv[0][1][1], ge2[1], 0);
  asm volatile("" : "+v"(gv[0][1][0]), "+v"(gv[0][1][1]));
  SB();
  asm volatile("s_add_u32 m0, %0, 61440" :  : "s"(mW) : "memory", "scc"); SB();
  asm volatile("s_nop 0\n\tbuffer_load_dwordx4 %0, %1, %2 offen lds" :  : "v"(voB[0]), "s"(rW1), "s"(kw_[3]) : "memory"); SB();
  act(gv[0][2][0], ge2[0], a2[0][2], c2[2], 0, 0); act(gv[0][2][1], ge2[1], a2[0][2], c2[2], 0, 1);
  asm volatile("" : "+v"(gv[0][2][0]), "+v"(gv[0][2][1]), "+v"(ge2[0]), "+v"(ge2[1]));
  SB();
  asm volatile("buffer_load_dwordx4 %0, %1, %2 offen offset:1024 lds" :  : "v"(voB[1]), "s"(rW1), "s"(kw_[3]) : "memory"); SB();
  act2(gv[0][2][0], ge2[0], 0); act2(gv[0][2][1], ge2[1], 0);
  asm volatile("" : "+v"(gv[0][2][0]), "+v"(gv[0][2][1]));
  SB();
  asm volatile("buffer_load_dwordx4 %0, %1, %2 offen offset:2048 lds" :  : "v"(voB[2]), "s"(rW1), "s"(kw_[3]) : "memory"); SB();
  act(gv[1][0][0], ge2[0], a2[1][0], c2[0], 1, 0); act(gv[1][0][1], ge2[1], a2[1][0], c2[0], 1, 1);
  asm volatile("" : "+v"(gv[1][0][0]), "+v"(gv[1][0][1]), "+v"(ge2[0]), "+v"(ge2[1]));
  SB();
  asm volatile("s_add_u32 m0, %0, 73728" :  : "s"(mW) : "memory", "scc"); SB();
  asm volatile("s_nop 0\n\tbuffer_load_dwordx4 %0, %1, %2 offen lds" :  : "v"(voB[0]), "s"(rW1), "s"(kw_[4]) : "memory"); SB();
  act2(gv[1][0][0], ge2[0], 1); act2(gv[1][0][1], ge2[1], 1);
  asm volatile("" : "+v"(gv[1][0][0]), "+v"(gv[1][0][1]));
  SB();
  asm volatile("buffer_load_dwordx4 %0, %1, %2 offen offset:1024 lds" :  : "v"(voB[1]), "s"(rW1), "s"(kw_[4]) : "memory"); SB();
  act(gv[1][1][0], ge2[0], a2[1][1], c2[1], 1, 0); act(gv[1][1][1], ge2[1], a2[1][1], c2[1], 1, 1);
  asm volatile("" : "+v"(gv[1][1][0]), "+v"(gv[1][1][1]), "+v"(ge2[0]), "+v"(ge2[1]));
  SB();
  act2(gv[1][1][0], ge2[0], 1); act2(gv[1][1][1], ge2[1], 1);
  asm volatile("" : "+v"(gv[1][1][0]), "+v"(gv[1][1][1]));
  SB();
  asm volatile("buffer_load_dwordx4 %0, %1, %2 offen offset:2048 lds" :  : "v"(voB[2]), "s"(rW1), "s"(kw_[4]) : "memory"); SB();
  act(gv[1][2][0], ge2[0], a2[1][2], c2[2], 1, 0); act(gv[1][2][1], ge2[1], a2[1][2], c2[2], 1, 1);
  asm volatile("" : "+v"(gv[1][2][0]), "+v"(gv[1][2][1]), "+v"(ge2[0]), "+v"(ge2[1]));
  SB();
  asm volatile("s_add_u32 m0, %0, 86016" :  : "s"(mW) : "memory", "scc"); SB();
  asm volatile("s_nop 0\n\tbuffer_load_dwordx4 %0, %1, %2 offen lds" :  : "v"(voB[0]), "s"(rW1), "s"(kw_[5]) : "memory"); SB();
  act2(gv[1][2][0], ge2[0], 1); act2(gv[1][2][1], ge2[1], 1);
  asm volatile("" : "+v"(gv[1][2][0]), "+v"(gv[1][2][1]));
  SB();
  asm volatile("buffer_load_dwordx4 %0, %1, %2 offen offset:1024 lds" :  : "v"(voB[1]), "s"(rW1), "s"(kw_[5]) : "memory"); SB();
#pragma unroll
  for (int i = 0; i < 2; ++i) {
    const unsigned p0 = __float_as_uint(gv[i][0][0][0]);
    const auto pa = __builtin_amdgcn_permlane16_swap(p0, p0, false, false);
    const auto pb = __builtin_amdgcn_permlane32_swap(pa[0], pa[0], false, false);
    pv[i] = __uint_as_float(pb[0]);
  }
  asm volatile("" : "+v"(pv[0]), "+v"(pv[1]));
  f32x2 s1[2], s2[2];
  SB();
  asm volatile("buffer_load_dwordx4 %0, %1, %2 offen offset:2048 lds" :  : "v"(voB[2]), "s"(rW1), "s"(kw_[5]) : "memory"); SB();
#pragma unroll
  for (int m = 0; m < 4; ++m) {
    const int i = m & 1, n = 0 + (m >> 1);
    const f32x2 d = gv[i][n >> 1][n & 1] - (f32x2){pv[i], pv[i]};
    s1[i] = n ? s1[i] + d : d; s2[i] = n ? __builtin_elementwise_fma(d, d, s2[i]) : d * d;
  }
  asm volatile("" : "+v"(s1[0]), "+v"(s1[1]), "+v"(s2[0]), "+v"(s2[1]));
  SB();
  asm volatile("s_add_u32 m0, %0, 0" :  : "s"(mW) : "memory", "scc"); SB();
  asm volatile("s_nop 0\n\tbuffer_load_dwordx4 %0, %1, %2 offen lds" :  : "v"(voB[0]), "s"(rW1), "s"(kw_[6]) : "memory"); SB();
#pragma unroll
  for (int m = 0; m < 4; ++m) {
    const int i = m & 1, n = 2 + (m >> 1);
    const f32x2 d = gv[i][n >> 1][n & 1] - (f32x2){pv[i], pv[i]};
    s1[i] = n ? s1[i] + d : d; s2[i] = n ? __builtin_elementwise_fma(d, d, s2[i]) : d * d;
  }
  asm volatile("" : "+v"(s1[0]), "+v"(s1[1]), "+v"(s2[0]), "+v"(s2[1]));
  SB();
  asm volatile("buffer_load_dwordx4 %0, %1, %2 offen offset:1024 lds" :  : "v"(voB[1]), "s"(rW1), "s"(kw_[6]) : "memory"); SB();
#pragma unroll
  for (int m = 0; m < 4; ++m) {
    const int i = m & 1, n = 4 + (m >> 1);
    const f32x2 d = gv[i][n >> 1][n & 1] - (f32x2){pv[i], pv[i]};
    s1[i] = n ? s1[i] + d : d; s2[i] = n ? __builtin_elementwise_fma(d, d, s2[i]) : d * d;
  }
  asm volatile("" : "+v"(s1[0]), "+v"(s1[1]), "+v"(s2[0]), "+v"(s2[1]));
  SB();
  asm volatile("buffer_load_dwordx4 %0, %1, %2 offen offset:2048 lds" :  : "v"(voB[2]), "s"(rW1), "s"(kw_[6]) : "memory"); SB();
  for (int i = 0; i < 2; ++i) { sv[i][0] = s1[i][0] + s1[i][1]; sv[i][1] = s2[i][0] + s2[i][1]; }
  {
    const float t1 = red16(sel32(sv[0][0], sv[1][0])), t2 = red16(sel32(sv[0][1], sv[1][1]));
    const float dl = t1 * (1.0f / 48.0f);
    const u32x4 so = {__float_as_uint((lane < 32 ? pv[0] : pv[1]) + dl), __float_as_uint(fmaxf(t2 - t1 * dl, 0.f)), tg0, tg1};
    SB();
  asm volatile("s_mov_b32 exec_lo, 0xffff\n\ts_mov_b32 exec_hi, 0xffff\n\tbuffer_store_dwordx4 %1, %0, %2, %3 offen offset:0 sc1\n\ts_mov_b64 exec, -1\n\ts_nop 0" :  : "v"(vo), "v"(so), "s"(rWS), "s"(0u) : "memory"); SB();
  }
  ST(6);
  unsigned zp[2][3][2];
#pragma unroll
  for (int n = 0; n < 12; ++n) { zp[n / 6][(n >> 1) % 3][n & 1] = pk2(gv[n / 6][(n >> 1) % 3][n & 1]); asm volatile("" : "+v"(zp[n / 6][(n >> 1) % 3][n & 1])); }
  SB();
  asm volatile("s_waitcnt vmcnt(24)\n\ts_barrier" :  :  : "memory"); SB();
  ST(2);
  asm volatile("ds_read_b64_tr_b16 %0, %1 offset:0" : "=&v"(wt[0]) : "v"(oa) : "memory"); SB();
  asm volatile("ds_read_b64_tr_b16 %0, %1 offset:512" : "=&v"(wt[1]) : "v"(oa) : "memory"); SB();
  asm volatile("ds_read_b64_tr_b16 %0, %1 offset:0" : "=&v"(kp[0]) : "v"(ka) : "memory"); SB();
  asm volatile("ds_read_b64_tr_b16 %0, %1 offset:640" : "=&v"(kc[0]) : "v"(ka) : "memory"); SB();
  asm volatile("ds_read_b64_tr_b16 %0, %1 offset:4096" : "=&v"(vv[0]) : "v"(ka) : "memory"); SB();
  asm volatile("ds_read_b64 %0, %1 offset:0" : "=&v"(sw[0]) : "v"(sa) : "memory"); SB();
  asm volatile("s_add_u32 m0, %0, 131072" :  : "s"(mA) : "memory", "scc"); SB();
  asm volatile("s_nop 0\n\tbuffer_load_dwordx4 %0, %1, %2 offen lds" :  : "v"(voS), "s"(rA), "s"(ao[0]) : "memory"); SB();
  asm volatile("ds_read_b64_tr_b16 %0, %1 offset:32" : "=&v"(kp[1]) : "v"(ka) : "memory"); SB();
  asm volatile("ds_read_b64_tr_b16 %0, %1 offset:672" : "=&v"(kc[1]) : "v"(ka) : "memory"); SB();
  asm volatile("ds_read_b64_tr_b16 %0, %1 offset:4128" : "=&v"(vv[1]) : "v"(ka) : "memory"); SB();
  asm volatile("ds_read_b64 %0, %1 offset:128" : "=&v"(sw[1]) : "v"(sa) : "memory"); SB();
  asm volatile("buffer_load_dwordx4 %0, %1, %2 offen offset:1024 lds" :  : "v"(voS), "s"(rA), "s"(ao[1]) : "memory"); SB();
  asm volatile("ds_read_b64_tr_b16 %0, %1 offset:64" : "=&v"(kp[2]) : "v"(ka) : "memory"); SB();
  asm volatile("ds_read_b64_tr_b16 %0, %1 offset:704" : "=&v"(kc[2]) : "v"(ka) : "memory"); SB();
  asm volatile("ds_read_b64_tr_b16 %0, %1 offset:4160" : "=&v"(vv[2]) : "v"(ka) : "memory"); SB();
  asm volatile("ds_read_b64 %0, %1 offset:256" : "=&v"(sw[2]) : "v"(sa) : "memory"); SB();
  asm volatile("buffer_load_dwordx4 %0, %1, %2 offen offset:2048 lds" :  : "v"(voS), "s"(rA), "s"(ao[2]) : "memory"); SB();
  asm volatile("ds_read_b64_tr_b16 %0, %1 offset:96" : "=&v"(kp[3]) : "v"(ka) : "memory"); SB();
  asm volatile("ds_read_b64_tr_b16 %0, %1 offset:736" : "=&v"(kc[3]) : "v"(ka) : "memory"); SB();
  asm volatile("ds_read_b64_tr_b16 %0, %1 offset:4192" : "=&v"(vv[3]) : "v"(ka) : "memory"); SB();
  asm volatile("ds_read_b64 %0, %1 offset:384" : "=&v"(sw[3]) : "v"(sa) : "memory"); SB();
  asm volatile("ds_read_b64_tr_b16 %0, %1 offset:128" : "=&v"(kp[4]) : "v"(ka) : "memory"); SB();
  asm volatile("ds_read_b64_tr_b16 %0, %1 offset:768" : "=&v"(kc[4]) : "v"(ka) : "memory"); SB();
  asm volatile("ds_read_b64_tr_b16 %0, %1 offset:4224" : "=&v"(vv[4]) : "v"(ka) : "memory"); SB();
  asm volatile("ds_read_b64 %0, %1 offset:512" : "=&v"(sw[4]) : "v"(sa) : "memory"); SB();
  asm volatile("buffer_load_dwordx4 %0, %1, %2 offen offset:3072 lds" :  : "v"(voS), "s"(rA), "s"(ao[3]) : "memory"); SB();
  asm volatile("s_waitcnt lgkmcnt(8)" : "+v"(kp[0]), "+v"(kc[0]), "+v"(vv[0]), "+v"(sw[0]), "+v"(wt[0]), "+v"(wt[1]) :  : "memory"); SB();
  asm volatile("v_alignbit_b32 %4, %14, %14, 16\n\tv_alignbit_b32 %5, %10, %9, 16\n\tv_alignbit_b32 %6, %11, %10, 16\n\tv_alignbit_b32 %7, %13, %13, 16\n\tv_alignbit_b32 %8, %12, %11, 16\n\tv_dot2_f32_bf16 %0, %5, %4, 0\n\tv_dot2_f32_bf16 %1, %10, %4, 0\n\tv_dot2_f32_bf16 %2, %6, %4, 0\n\tv_dot2_f32_bf16 %3, %11, %4, 0\n\tv_dot2_f32_bf16 %0, %6, %7, %0\n\tv_dot2_f32_bf16 %1, %11, %7, %1\n\tv_dot2_f32_bf16 %2, %8, %7, %2\n\tv_dot2_f32_bf16 %3, %12, %7, %3" : "=&v"(kt[0][0]), "=&v"(kt[0][1]), "=&v"(kt[1][0]), "=&v"(kt[1][1]), "=&v"(ku[0]), "=&v"(ku[1]), "=&v"(ku[2]), "=&v"(ku[3]), "=&v"(ku[4]) : "v"(kp[0][0]), "v"(kp[0][1]), "v"(kc[0][0]), "v"(kc[0][1]), "v"(sw[0][0]), "v"(sw[0][1]) : ); SB();
  asm volatile("s_add_u32 m0, %0, 135168" :  : "s"(mA) : "memory", "scc"); SB();
  asm volatile("s_nop 0\n\tbuffer_load_dwordx4 %0, %1, %2 offen lds" :  : "v"(voS), "s"(rA), "s"(ao[4]) : "memory"); SB();
  asm volatile("v_lshlrev_b32 %0, 16, %4\n\tv_and_b32 %1, 0xffff0000, %4\n\tv_lshlrev_b32 %2, 16, %5\n\tv_and_b32 %3, 0xffff0000, %5" : "=&v"(kf[0][0]), "=&v"(kf[0][1]), "=&v"(kf[1][0]), "=&v"(kf[1][1]) : "v"(vv[0][0]), "v"(vv[0][1]) : ); SB();
  kt[0] *= kf[0]; kt[1] *= kf[1]; SB();
  ko[0] = (u32x2){pk2(kt[0]), pk2(kt[1])};
  asm volatile("" : "+v"(ko[0]));
  SB();
  asm volatile("buffer_load_dwordx4 %0, %1, %2 offen offset:1024 lds" :  : "v"(voS), "s"(rA), "s"(ao[5]) : "memory"); SB();
  asm volatile("ds_write_b64 %0, %1 offset:0" :  : "v"(kw), "v"(ko[0]) : "memory"); SB();
  asm volatile("" : "+v"(kp[1]), "+v"(kc[1]), "+v"(vv[1]), "+v"(sw[1]) :  : "memory"); SB();
  asm volatile("v_alignbit_b32 %4, %14, %14, 16\n\tv_alignbit_b32 %5, %10, %9, 16\n\tv_alignbit_b32 %6, %11, %10, 16\n\tv_alignbit_b32 %7, %13, %13, 16\n\tv_alignbit_b32 %8, %12, %11, 16\n\tv_dot2_f32_bf16 %0, %5, %4, 0\n\tv_dot2_f32_bf16 %1, %10, %4, 0\n\tv_dot2_f32_bf16 %2, %6, %4, 0\n\tv_dot2_f32_bf16 %3, %11, %4, 0\n\tv_dot2_f32_bf16 %0, %6, %7, %0\n\tv_dot2_f32_bf16 %1, %11, %7, %1\n\tv_dot2_f32_bf16 %2, %8, %7, %2\n\tv_dot2_f32_bf16 %3, %12, %7, %3" : "=&v"(kt[0][0]), "=&v"(kt[0][1]), "=&v"(kt[1][0]), "=&v"(kt[1][1]), "=&v"(ku[0]), "=&v"(ku[1]), "=&v"(ku[2]), "=&v"(ku[3]), "=&v"(ku[4]) : "v"(kp[1][0]), "v"(kp[1][1]), "v"(kc[1][0]), "v"(kc[1][1]), "v"(sw[1][0]), "v"(sw[1][1]) : ); SB();
  asm volatile("buffer_load_dwordx4 %0, %1, %2 offen offset:2048 lds" :  : "v"(voS), "s"(rA), "s"(ao[6]) : "memory"); SB();
  asm volatile("v_lshlrev_b32 %0, 16, %4\n\tv_and_b32 %1, 0xffff0000, %4\n\tv_lshlrev_b32 %2, 16, %5\n\tv_and_b32 %3, 0xffff0000, %5" : "=&v"(kf[0][0]), "=&v"(kf[0][1]), "=&v"(kf[1][0]), "=&v"(kf[1][1]) : "v"(vv[1][0]), "v"(vv[1][1]) : ); SB();
  kt[0] *= kf[0]; kt[1] *= kf[1]; SB();
  ko[1] = (u32x2){pk2(kt[0]), pk2(kt[1])};
  asm volatile("" : "+v"(ko[1]));
  SB();
  asm volatile("buffer_load_dwordx4 %0, %1, %2 offen offset:3072 lds" :  : "v"(voS), "s"(rA), "s"(ao[7]) : "memory"); SB();
  asm volatile("v_xor_b32 %0, 128, %0" : "+v"(voS) :  : "memory"); SB();
  asm volatile("ds_write_b64 %0, %1 offset:2304" :  : "v"(kw), "v"(ko[1]) : "memory"); SB();
  asm volatile("" : "+v"(kp[2]), "+v"(kc[2]), "+v"(vv[2]), "+v"(sw[2]) :  : "memory"); SB();
  asm volatile("v_alignbit_b32 %4, %14, %14, 16\n\tv_alignbit_b32 %5, %10, %9, 16\n\tv_alignbit_b32 %6, %11, %10, 16\n\tv_alignbit_b32 %7, %13, %13, 16\n\tv_alignbit_b32 %8, %12, %11, 16\n\tv_dot2_f32_bf16 %0, %5, %4, 0\n\tv_dot2_f32_bf16 %1, %10, %4, 0\n\tv_dot2_f32_bf16 %2, %6, %4, 0\n\tv_dot2_f32_bf16 %3, %11, %4, 0\n\tv_dot2_f32_bf16 %0, %6, %7, %0\n\tv_dot2_f32_bf16 %1, %11, %7, %1\n\tv_dot2_f32_bf16 %2, %8, %7, %2\n\tv_dot2_f32_bf16 %3, %12, %7, %3" : "=&v"(kt[0][0]), "=&v"(kt[0][1]), "=&v"(kt[1][0]), "=&v"(kt[1][1]), "=&v"(ku[0]), "=&v"(ku[1]), "=&v"(ku[2]), "=&v"(ku[3]), "=&v"(ku[4]) : "v"(kp[2][0]), "v"(kp[2][1]), "v"(kc[2][0]), "v"(kc[2][1]), "v"(sw[2][0]), "v"(sw[2][1]) : ); SB();
  asm volatile("v_lshlrev_b32 %0, 16, %4\n\tv_and_b32 %1, 0xffff0000, %4\n\tv_lshlrev_b32 %2, 16, %5\n\tv_and_b32 %3, 0xffff0000, %5" : "=&v"(kf[0][0]), "=&v"(kf[0][1]), "=&v"(kf[1][0]), "=&v"(kf[1][1]) : "v"(vv[2][0]), "v"(vv[2][1]) : ); SB();
  kt[0] *= kf[0]; kt[1] *= kf[1]; SB();
  ko[2] = (u32x2){pk2(kt[0]), pk2(kt[1])};
  asm volatile("" : "+v"(ko[2]));
  SB();
  asm volatile("ds_write_b64 %0, %1 offset:4608" :  : "v"(kw), "v"(ko[2]) : "memory"); SB();
  asm volatile("s_waitcnt lgkmcnt(2)" : "+v"(kp[3]), "+v"(kc[3]), "+v"(vv[3]), "+v"(sw[3]) :  : "memory"); SB();
  asm volatile("v_alignbit_b32 %4, %14, %14, 16\n\tv_alignbit_b32 %5, %10, %9, 16\n\tv_alignbit_b32 %6, %11, %10, 16\n\tv_alignbit_b32 %7, %13, %13, 16\n\tv_alignbit_b32 %8, %12, %11, 16\n\tv_dot2_f32_bf16 %0, %5, %4, 0\n\tv_dot2_f32_bf16 %1, %10, %4, 0\n\tv_dot2_f32_bf16 %2, %6, %4, 0\n\tv_dot2_f32_bf16 %3, %11, %4, 0\n\tv_dot2_f32_bf16 %0, %6, %7, %0\n\tv_dot2_f32_bf16 %1, %11, %7, %1\n\tv_dot2_f32_bf16 %2, %8, %7, %2\n\tv_dot2_f32_bf16 %3, %12, %7, %3" : "=&v"(kt[0][0]), "=&v"(kt[0][1]), "=&v"(kt[1][0]), "=&v"(kt[1][1]), "=&v"(ku[0]), "=&v"(ku[1]), "=&v"(ku[2]), "=&v"(ku[3]), "=&v"(ku[4]) : "v"(kp[3][0]), "v"(kp[3][1]), "v"(kc[3][0]), "v"(kc[3][1]), "v"(sw[3][0]), "v"(sw[3][1]) : ); SB();
  asm volatile("s_add_u32 m0, %0, 98304" :  : "s"(mA) : "memory", "scc"); SB();
  asm volatile("s_nop 0\n\tbuffer_load_dwordx4 %0, %1, %2 offen lds" :  : "v"(voS), "s"(rA), "s"(ao[0]) : "memory"); SB();
  asm volatile("buffer_load_dwordx4 %0, %1, %2 offen offset:1024 lds" :  : "v"(voS), "s"(rA), "s"(ao[1]) : "memory"); SB();
  asm volatile("v_lshlrev_b32 %0, 16, %4\n\tv_and_b32 %1, 0xffff0000, %4\n\tv_lshlrev_b32 %2, 16, %5\n\tv_and_b32 %3, 0xffff0000, %5" : "=&v"(kf[0][0]), "=&v"(kf[0][1]), "=&v"(kf[1][0]), "=&v"(kf[1][1]) : "v"(vv[3][0]), "v"(vv[3][1]) : ); SB();
  kt[0] *= kf[0]; kt[1] *= kf[1]; SB();
  ko[3] = (u32x2){pk2(kt[0]), pk2(kt[1])};
  asm volatile("" : "+v"(ko[3]));
  SB();
  asm volatile("buffer_load_dwordx4 %0, %1, %2 offen offset:2048 lds" :  : "v"(voS), "s"(rA), "s"(ao[2]) : "memory"); SB();
  asm volatile("ds_write_b64 %0, %1 offset:6912" :  : "v"(kw), "v"(ko[3]) : "memory"); SB();
  asm volatile("" : "+v"(kp[4]), "+v"(kc[4]), "+v"(vv[4]), "+v"(sw[4]) :  : "memory"); SB();
  asm volatile("v_alignbit_b32 %4, %14, %14, 16\n\tv_alignbit_b32 %5, %10, %9, 16\n\tv_alignbit_b32 %6, %11, %10, 16\n\tv_alignbit_b32 %7, %13, %13, 16\n\tv_alignbit_b32 %8, %12, %11, 16\n\tv_dot2_f32_bf16 %0, %5, %4, 0\n\tv_dot2_f32_bf16 %1, %10, %4, 0\n\tv_dot2_f32_bf16 %2, %6, %4, 0\n\tv_dot2_f32_bf16 %3, %11, %4, 0\n\tv_dot2_f32_bf16 %0, %6, %7, %0\n\tv_dot2_f32_bf16 %1, %11, %7, %1\n\tv_dot2_f32_bf16 %2, %8, %7, %2\n\tv_dot2_f32_bf16 %3, %12, %7, %3" : "=&v"(kt[0][0]), "=&v"(kt[0][1]), "=&v"(kt[1][0]), "=&v"(kt[1][1]), "=&v"(ku[0]), "=&v"(ku[1]), "=&v"(ku[2]), "=&v"(ku[3]), "=&v"(ku[4]) : "v"(kp[4][0]), "v"(kp[4][1]), "v"(kc[4][0]), "v"(kc[4][1]), "v"(sw[4][0]), "v"(sw[4][1]) : ); SB();
  asm volatile("buffer_load_dwordx4 %0, %1, %2 offen offset:3072 lds" :  : "v"(voS), "s"(rA), "s"(ao[3]) : "memory"); SB();
  asm volatile("v_lshlrev_b32 %0, 16, %4\n\tv_and_b32 %1, 0xffff0000, %4\n\tv_lshlrev_b32 %2, 16, %5\n\tv_and_b32 %3, 0xffff0000, %5" : "=&v"(kf[0][0]), "=&v"(kf[0][1]), "=&v"(kf[1][0]), "=&v"(kf[1][1]) : "v"(vv[4][0]), "v"(vv[4][1]) : ); SB();
  kt[0] *= kf[0]; kt[1] *= kf[1]; SB();
  ko[4] = (u32x2){pk2(kt[0]), pk2(kt[1])};
  asm volatile("" : "+v"(ko[4]));
  SB();
  asm volatile("ds_write_b64 %0, %1 offset:9216" :  : "v"(kw), "v"(ko[4]) : "memory"); SB();
  ST(16);
  asm volatile("s_waitcnt vmcnt(28) lgkmcnt(0)\n\ts_barrier" :  :  : "memory"); SB();
  asm volatile("ds_read_b128 %0, %1 offset:0" : "=&v"(kq[0]) : "v"(kr) : "memory"); SB();
  asm volatile("ds_read_b128 %0, %1 offset:1152" : "=&v"(kq[1]) : "v"(kr) : "memory"); SB();
  asm volatile("ds_read_b128 %0, %1 offset:2304" : "=&v"(kq[2]) : "v"(kr) : "memory"); SB();
  asm volatile("ds_read_b64_tr_b16 v[184:185], %0 offset:24576" :  : "v"(aW[0][0][0]) : "memory"); SB();
  asm volatile("ds_read_b64_tr_b16 v[186:187], %0 offset:25344" :  : "v"(aW[0][0][0]) : "memory"); SB();
  asm volatile("ds_read_b64_tr_b16 v[188:189], %0 offset:24576" :  : "v"(aW[0][0][1]) : "memory"); SB();
  asm volatile("ds_read_b64_tr_b16 v[190:191], %0 offset:25344" :  : "v"(aW[0][0][1]) : "memory"); SB();
  asm volatile("ds_read_b64_tr_b16 v[192:193], %0 offset:24576" :  : "v"(aW[0][0][2]) : "memory"); SB();
  asm volatile("ds_read_b64_tr_b16 v[194:195], %0 offset:25344" :  : "v"(aW[0][0][2]) : "memory"); SB();
  ST(7);
  asm volatile("s_waitcnt lgkmcnt(6)" : "+v"(kq[0]), "+v"(kq[1]), "+v"(kq[2]) :  : "memory"); SB();
  unsigned kz[3] = {kq[0][3], kq[1][3] | (w + 1) >> 2, kq[2][3] | (w + 1) >> 2};
  asm volatile("" : "+v"(kz[0]), "+v"(kz[1]), "+v"(kz[2])); SB();
  asm volatile("buffer_load_dwordx4 v[196:199], %0, %1, %2 offen offset:0 sc1" :  : "v"(vS), "s"(rWS), "s"(0u) : "memory"); SB();
  asm volatile("buffer_load_dwordx4 v[200:203], %0, %1, %2 offen offset:2048 sc1" :  : "v"(vS), "s"(rWS), "s"(0u) : "memory"); SB();
  asm volatile("s_waitcnt lgkmcnt(3)\n\tv_mfma_f32_16x16x32_bf16 %0, v[184:187], a[0:3], 0" : "=&v"(a1[0][0]) :  : ); SB();
  asm volatile("ds_read_b64_tr_b16 v[160:161], %0 offset:24576" :  : "v"(aW[0][1][0]) : "memory"); SB();
  asm volatile("v_mfma_f32_16x16x32_bf16 %0, v[184:187], a[4:7], 0" : "=&v"(a1[1][0]) :  : ); SB();
  asm volatile("ds_read_b64_tr_b16 v[162:163], %0 offset:25344" :  : "v"(aW[0][1][0]) : "memory"); SB();
  asm volatile("v_mfma_f32_16x16x32_bf16 %0, v[184:187], %1, 0" : "=&v"(c1[0]) : "v"(ones) : ); SB();
  asm volatile("ds_read_b64_tr_b16 v[164:165], %0 offset:24576" :  : "v"(aW[0][1][1]) : "memory"); SB();
  asm volatile("s_waitcnt lgkmcnt(3)\n\tv_mfma_f32_16x16x32_bf16 %0, v[188:191], a[0:3], 0" : "=&v"(a1[0][1]) :  : ); SB();
  asm volatile("ds_read_b64_tr_b16 v[166:167], %0 offset:25344" :  : "v"(aW[0][1][1]) : "memory"); SB();
  asm volatile("v_mfma_f32_16x16x32_bf16 %0, v[188:191], a[4:7], 0" : "=&v"(a1[1][1]) :  : ); SB();
  asm volatile("ds_read_b64_tr_b16 v[168:169], %0 offset:24576" :  : "v"(aW[0][1][2]) : "memory"); SB();
  asm volatile("v_mfma_f32_16x16x32_bf16 %0, v[188:191], %1, 0" : "=&v"(c1[1]) : "v"(ones) : ); SB();
  asm volatile("ds_read_b64_tr_b16 v[170:171], %0 offset:25344" :  : "v"(aW[0][1][2]) : "memory"); SB();
  asm volatile("v_mfma_f32_16x16x32_bf16 %0, v[192:195], a[0:3], 0" : "=&v"(a1[0][2]) :  : ); SB();
  asm volatile("ds_read_b64_tr_b16 v[172:173], %0 offset:36864" :  : "v"(aW[0][0][0]) : "memory"); SB();
  asm volatile("v_mfma_f32_16x16x32_bf16 %0, v[192:195], a[4:7], 0" : "=&v"(a1[1][2]) :  : ); SB();
  asm volatile("ds_read_b64_tr_b16 v[174:175], %0 offset:37632" :  : "v"(aW[0][0][0]) : "memory"); SB();
  asm volatile("v_mfma_f32_16x16x32_bf16 %0, v[192:195], %1, 0" : "=&v"(c1[2]) : "v"(ones) : ); SB();
  asm volatile("ds_read_b64_tr_b16 v[176:177], %0 offset:36864" :  : "v"(aW[0][0][1]) : "memory"); SB();
  asm volatile("s_waitcnt lgkmcnt(7)\n\tv_mfma_f32_16x16x32_bf16 %0, v[160:163], a[8:11], %0" : "+v"(a1[0][0]) :  : ); SB();
  asm volatile("ds_read_b64_tr_b16 v[178:179], %0 offset:37632" :  : "v"(aW[0][0][1]) : "memory"); SB();
  asm volatile("v_mfma_f32_16x16x32_bf16 %0, v[160:163], a[12:15], %0" : "+v"(a1[1][0]) :  : ); SB();
  asm volatile("ds_read_b64_tr_b16 v[180:181], %0 offset:36864" :  : "v"(aW[0][0][2]) : "memory"); SB();
  asm volatile("s_waitcnt lgkmcnt(7)\n\tv_mfma_f32_16x16x32_bf16 %0, v[164:167], a[8:11], %0" : "+v"(a1[0][1]) :  : ); SB();
  asm volatile("ds_read_b64_tr_b16 v[182:183], %0 offset:37632" :  : "v"(aW[0][0][2]) : "memory"); SB();
  asm volatile("v_mfma_f32_16x16x32_bf16 %0, v[164:167], a[12:15], %0" : "+v"(a1[1][1]) :  : ); SB();
  asm volatile("ds_read_b64_tr_b16 v[184:185], %0 offset:36864" :  : "v"(aW[0][1][0]) : "memory"); SB();
  asm volatile("s_waitcnt lgkmcnt(7)\n\tv_mfma_f32_16x16x32_bf16 %0, v[168:171], a[8:11], %0" : "+v"(a1[0][2]) :  : ); SB();
  asm volatile("ds_read_b64_tr_b16 v[186:187], %0 offset:37632" :  : "v"(aW[0][1][0]) : "memory"); SB();
  asm volatile("v_mfma_f32_16x16x32_bf16 %0, v[168:171], a[12:15], %0" : "+v"(a1[1][2]) :  : ); SB();
  asm volatile("ds_read_b64_tr_b16 v[188:189], %0 offset:36864" :  : "v"(aW[0][1][1]) : "memory"); SB();
  asm volatile("s_waitcnt lgkmcnt(7)\n\tv_mfma_f32_16x16x32_bf16 %0, v[172:175], a[16:19], %0" : "+v"(a1[0][0]) :  : ); SB();
  asm volatile("ds_read_b64_tr_b16 v[190:191], %0 offset:37632" :  : "v"(aW[0][1][1]) : "memory"); SB();
  asm volatile("v_mfma_f32_16x16x32_bf16 %0, v[172:175], a[20:23], %0" : "+v"(a1[1][0]) :  : ); SB();
  asm volatile("ds_read_b64_tr_b16 v[192:193], %0 offset:36864" :  : "v"(aW[0][1][2]) : "memory"); SB();
  asm volatile("v_mfma_f32_16x16x32_bf16 %0, v[172:175], %1, %0" : "+v"(c1[0]) : "v"(ones) : ); SB();
  asm volatile("ds_read_b64_tr_b16 v[194:195], %0 offset:37632" :  : "v"(aW[0][1][2]) : "memory"); SB();
  asm volatile("s_waitcnt lgkmcnt(8)\n\tv_mfma_f32_16x16x32_bf16 %0, v[176:179], a[16:19], %0" : "+v"(a1[0][1]) :  : ); SB();
  asm volatile("s_waitcnt vmcnt(24)\n\ts_barrier" :  :  : "memory"); SB();
  asm volatile("ds_read_b64_tr_b16 v[160:161], %0 offset:0" :  : "v"(aW[1][0][0]) : "memory"); SB();
  asm volatile("v_mfma_f32_16x16x32_bf16 %0, v[176:179], a[20:23], %0" : "+v"(a1[1][1]) :  : ); SB();
  asm volatile("ds_read_b64_tr_b16 v[162:163], %0 offset:768" :  : "v"(aW[1][0][0]) : "memory"); SB();
  asm volatile("v_mfma_f32_16x16x32_bf16 %0, v[176:179], %1, %0" : "+v"(c1[1]) : "v"(ones) : ); SB();
  asm volatile("ds_read_b64_tr_b16 v[164:165], %0 offset:0" :  : "v"(aW[1][0][1]) : "memory"); SB();
  asm volatile("s_waitcnt lgkmcnt(7)\n\tv_mfma_f32_16x16x32_bf16 %0, v[180:183], a[16:19], %0" : "+v"(a1[0][2]) :  : ); SB();
  asm volatile("ds_read_b64_tr_b16 v[166:167], %0 offset:768" :  : "v"(aW[1][0][1]) : "memory"); SB();
  asm volatile("v_mfma_f32_16x16x32_bf16 %0, v[180:183], a[20:23], %0" : "+v"(a1[1][2]) :  : ); SB();
  asm volatile("ds_read_b64_tr_b16 v[168:169], %0 offset:0" :  : "v"(aW[1][0][2]) : "memory"); SB();
  asm volatile("v_mfma_f32_16x16x32_bf16 %0, v[180:183], %1, %0" : "+v"(c1[2]) : "v"(ones) : ); SB();
  asm volatile("ds_read_b64_tr_b16 v[170:171], %0 offset:768" :  : "v"(aW[1][0][2]) : "memory"); SB();
  asm volatile("v_mfma_f32_16x16x32_bf16 %0, v[184:187], a[24:27], %0" : "+v"(a1[0][0]) :  : ); SB();
  asm volatile("ds_read_b64_tr_b16 v[172:173], %0 offset:0" :  : "v"(aW[1][1][0]) : "memory"); SB();
  asm volatile("v_mfma_f32_16x16x32_bf16 %0, v[184:187], a[28:31], %0" : "+v"(a1[1][0]) :  : ); SB();
  asm volatile("ds_read_b64_tr_b16 v[174:175], %0 offset:768" :  : "v"(aW[1][1][0]) : "memory"); SB();
  asm volatile("s_waitcnt lgkmcnt(8)\n\tv_mfma_f32_16x16x32_bf16 %0, v[188:191], a[24:27], %0" : "+v"(a1[0][1]) :  : ); SB();
  asm volatile("ds_read_b64_tr_b16 v[176:177], %0 offset:0" :  : "v"(aW[1][1][1]) : "memory"); SB();
  asm volatile("v_mfma_f32_16x16x32_bf16 %0, v[188:191], a[28:31], %0" : "+v"(a1[1][1]) :  : ); SB();
  asm volatile("ds_read_b64_tr_b16 v[178:179], %0 offset:768" :  : "v"(aW[1][1][1]) : "memory"); SB();
  asm volatile("v_mfma_f32_16x16x32_bf16 %0, v[192:195], a[24:27], %0" : "+v"(a1[0][2]) :  : ); SB();
  asm volatile("ds_read_b64_tr_b16 v[180:181], %0 offset:0" :  : "v"(aW[1][1][2]) : "memory"); SB();
  asm volatile("v_mfma_f32_16x16x32_bf16 %0, v[192:195], a[28:31], %0" : "+v"(a1[1][2]) :  : ); SB();
  asm volatile("ds_read_b64_tr_b16 v[182:183], %0 offset:768" :  : "v"(aW[1][1][2]) : "memory"); SB();
  asm volatile("s_waitcnt lgkmcnt(9)\n\tv_mfma_f32_16x16x32_bf16 %0, v[160:163], a[32:35], %0" : "+v"(a1[0][0]) :  : ); SB();
  asm volatile("ds_read_b64_tr_b16 v[184:185], %0 offset:12288" :  : "v"(aW[1][0][0]) : "memory"); SB();
  asm volatile("buffer_load_dwordx4 v[204:207], %0, %1, %2 offen offset:0 sc1" :  : "v"(vS), "s"(rWS), "s"(4096u) : "memory"); SB();
  asm volatile("v_mfma_f32_16x16x32_bf16 %0, v[160:163], a[36:39], %0" : "+v"(a1[1][0]) :  : ); SB();
  asm volatile("ds_read_b64_tr_b16 v[186:187], %0 offset:13056" :  : "v"(aW[1][0][0]) : "memory"); SB();
  asm volatile("v_mfma_f32_16x16x32_bf16 %0, v[160:163], %1, %0" : "+v"(c1[0]) : "v"(ones) : ); SB();
  asm volatile("ds_read_b64_tr_b16 v[188:189], %0 offset:12288" :  : "v"(aW[1][0][1]) : "memory"); SB();
  asm volatile("s_waitcnt lgkmcnt(9)\n\tv_mfma_f32_16x16x32_bf16 %0, v[164:167], a[32:35], %0" : "+v"(a1[0][1]) :  : ); SB();
  asm volatile("ds_read_b64_tr_b16 v[190:191], %0 offset:13056" :  : "v"(aW[1][0][1]) : "memory"); SB();
  asm volatile("v_mfma_f32_16x16x32_bf16 %0, v[164:167], a[36:39], %0" : "+v"(a1[1][1]) :  : ); SB();
  asm volatile("ds_read_b64_tr_b16 v[192:193], %0 offset:12288" :  : "v"(aW[1][0][2]) : "memory"); SB();
  asm volatile("buffer_load_dwordx4 v[208:211], %0, %1, %2 offen offset:2048 sc1" :  : "v"(vS), "s"(rWS), "s"(4096u) : "memory"); SB();
  asm volatile("v_mfma_f32_16x16x32_bf16 %0, v[164:167], %1, %0" : "+v"(c1[1]) : "v"(ones) : ); SB();
  asm volatile("ds_read_b64_tr_b16 v[194:195], %0 offset:13056" :  : "v"(aW[1][0][2]) : "memory"); SB();
  asm volatile("v_mfma_f32_16x16x32_bf16 %0, v[168:171], a[32:35], %0" : "+v"(a1[0][2]) :  : ); SB();
  ca = lb + 57344 + w * 192 + g4 * 16; asm volatile("" : "+v"(ca)); SB();
  asm volatile("v_mfma_f32_16x16x32_bf16 %0, v[168:171], a[36:39], %0" : "+v"(a1[1][2]) :  : ); SB();
  cr = lb + 57344 + (w ^ 2) * 192 + g4 * 16; asm volatile("" : "+v"(cr)); SB();
  asm volatile("v_mfma_f32_16x16x32_bf16 %0, v[168:171], %1, %0" : "+v"(c1[2]) : "v"(ones) : ); SB();
  asm volatile("buffer_store_dwordx4 %1, %0, %2, %3 offen offset:0 sc1\n\ts_nop 1" :  : "v"(vKV), "v"(kq[0]), "s"(rWS), "s"(0u) : "memory"); SB();
  asm volatile("s_waitcnt lgkmcnt(5)\n\tv_mfma_f32_16x16x32_bf16 %0, v[172:175], a[40:43], %0" : "+v"(a1[0][0]) :  : ); SB();
  asm volatile("ds_read_b64_tr_b16 v[160:161], %0 offset:12288" :  : "v"(aW[1][1][0]) : "memory"); SB();
  asm volatile("v_mfma_f32_16x16x32_bf16 %0, v[172:175], a[44:47], %0" : "+v"(a1[1][0]) :  : ); SB();
  asm volatile("ds_read_b64_tr_b16 v[162:163], %0 offset:13056" :  : "v"(aW[1][1][0]) : "memory"); SB();
  asm volatile("v_lshlrev_b32 %0, 16, v245" : "=v"(dD[0]) :  : ); SB();
  asm volatile("v_mfma_f32_16x16x32_bf16 %0, v[176:179], a[40:43], %0" : "+v"(a1[0][1]) :  : ); SB();
  asm volatile("ds_read_b64_tr_b16 v[164:165], %0 offset:12288" :  : "v"(aW[1][1][1]) : "memory"); SB();
  asm volatile("buffer_store_dwordx4 %1, %0, %2, %3 offen offset:0 sc1\n\ts_nop 1" :  : "v"(vKV), "v"(kq[1]), "s"(rWS), "s"(w3k[0]) : "memory"); SB();
  asm volatile("v_mfma_f32_16x16x32_bf16 %0, v[176:179], a[44:47], %0" : "+v"(a1[1][1]) :  : ); SB();
  asm volatile("ds_read_b64_tr_b16 v[166:167], %0 offset:13056" :  : "v"(aW[1][1][1]) : "memory"); SB();
  asm volatile("v_lshlrev_b32 %0, 16, v246" : "=v"(dD[1]) :  : ); SB();
  asm volatile("v_mfma_f32_16x16x32_bf16 %0, v[180:183], a[40:43], %0" : "+v"(a1[0][2]) :  : ); SB();
  asm volatile("ds_read_b64_tr_b16 v[168:169], %0 offset:12288" :  : "v"(aW[1][1][2]) : "memory"); SB();
  asm volatile("v_lshlrev_b32 %0, 16, v247" : "=v"(dD[2]) :  : ); SB();
  asm volatile("v_mfma_f32_16x16x32_bf16 %0, v[180:183], a[44:47], %0" : "+v"(a1[1][2]) :  : ); SB();
  asm volatile("ds_read_b64_tr_b16 v[170:171], %0 offset:13056" :  : "v"(aW[1][1][2]) : "memory"); SB();
  asm volatile("buffer_store_dwordx4 %1, %0, %2, %3 offen offset:0 sc1\n\ts_nop 1" :  : "v"(vKV), "v"(kq[2]), "s"(rWS), "s"(w3k[1]) : "memory"); SB();
  ST(8);
  { unsigned lane = lane0, w = w0, rb = rb0, cb = cb0;
  asm volatile("" : "+v"(lane)); asm volatile("" : "+s"(w), "+s"(rb), "+s"(cb)); SB();

  unsigned zwa[2] = {lb + 98304 + 4096 + w * 8192 + (wn * 16 + l15) * 104 + g4 * 8, lb + 98304 + 4096 + w * 8192 + ((wn ^ 1) * 16 + l15) * 104 + g4 * 8};
  unsigned zra = lb + 98304 + 4096 + w * 8192 + (8 * g4 + q4) * 104 + 8 * p4, vZ_ = vZ;
  unsigned nw = lb + 98304 + 7424 + wm * 16384 + wn * 64 + (g4 & 1) * 32 + (lane & 7) * 4, nr = lb + 98304 + 7424 + wm * 16384 + g4 * 32;
  asm volatile("" : "+v"(zwa[0]), "+v"(zwa[1]), "+v"(zra), "+v"(vZ_), "+v"(nw), "+v"(nr));
  SB();
  asm volatile("buffer_store_dwordx4 %1, %0, %2, %3 offen offset:0 sc1\n\ts_nop 1" :  : "v"(vWO), "v"(((u32x4){wt[0][0], wt[0][1], wt[1][0], wt[1][1]})), "s"(rWS), "s"(0u) : "memory"); SB();
  asm volatile("s_waitcnt vmcnt(18)" :  :  : "memory"); SB();
  asm volatile("ds_read_b128 a[160:163], %0 offset:32768" :  : "v"(aA[0]) : "memory"); SB();
  asm volatile("ds_read_b128 a[176:179], %0 offset:32768" :  : "v"(aA[1]) : "memory"); SB();
  asm volatile("s_waitcnt vmcnt(16)" :  :  : "memory"); SB();
  asm volatile("ds_read_b128 a[164:167], %0 offset:34816" :  : "v"(aA[0]) : "memory"); SB();
  asm volatile("ds_read_b128 a[180:183], %0 offset:34816" :  : "v"(aA[1]) : "memory"); SB();
  asm volatile("s_waitcnt vmcnt(14)" :  :  : "memory"); SB();
  asm volatile("ds_read_b128 a[168:171], %0 offset:36864" :  : "v"(aA[0]) : "memory"); SB();
  asm volatile("ds_read_b128 a[184:187], %0 offset:36864" :  : "v"(aA[1]) : "memory"); SB();
  asm volatile("s_waitcnt vmcnt(12)" :  :  : "memory"); SB();
  asm volatile("ds_read_b128 a[172:175], %0 offset:38912" :  : "v"(aA[0]) : "memory"); SB();
  asm volatile("ds_read_b128 a[188:191], %0 offset:38912" :  : "v"(aA[1]) : "memory"); SB();
  asm volatile("s_waitcnt vmcnt(10)" :  :  : "memory"); SB();
  asm volatile("ds_read_b128 a[192:195], %0 offset:0" :  : "v"(aA[0]) : "memory"); SB();
  asm volatile("ds_read_b128 a[208:211], %0 offset:0" :  : "v"(aA[1]) : "memory"); SB();
  asm volatile("s_waitcnt vmcnt(8)" :  :  : "memory"); SB();
  asm volatile("ds_read_b128 a[196:199], %0 offset:2048" :  : "v"(aA[0]) : "memory"); SB();
  asm volatile("ds_read_b128 a[212:215], %0 offset:2048" :  : "v"(aA[1]) : "memory"); SB();
  asm volatile("s_add_u32 m0, %0, 12288" :  : "s"(mW) : "memory", "scc"); SB();
  asm volatile("s_nop 0\n\tbuffer_load_dwordx4 %0, %1, %2 offen lds" :  : "v"(voB[0]), "s"(rW1), "s"(kw_[7]) : "memory"); SB();
  asm volatile("buffer_load_dwordx4 %0, %1, %2 offen offset:1024 lds" :  : "v"(voB[1]), "s"(rW1), "s"(kw_[7]) : "memory"); SB();
  asm volatile("buffer_load_dwordx4 %0, %1, %2 offen offset:2048 lds" :  : "v"(voB[2]), "s"(rW1), "s"(kw_[7]) : "memory"); SB();
  asm volatile("s_add_u32 m0, %0, 24576" :  : "s"(mW) : "memory", "scc"); SB();
  asm volatile("s_nop 0\n\tbuffer_load_dwordx4 %0, %1, %2 offen lds" :  : "v"(voB[0]), "s"(rW1), "s"(kw_[8]) : "memory"); SB();
  asm volatile("buffer_load_dwordx4 %0, %1, %2 offen offset:1024 lds" :  : "v"(voB[1]), "s"(rW1), "s"(kw_[8]) : "memory"); SB();
  asm volatile("buffer_load_dwordx4 %0, %1, %2 offen offset:2048 lds" :  : "v"(voB[2]), "s"(rW1), "s"(kw_[8]) : "memory"); SB();
  asm volatile("s_waitcnt vmcnt(10)\n\t.Lc_%=:\n\tv_cmpx_eq_u64 vcc, %1, v[198:199]\n\tv_cmpx_eq_u64 vcc, %1, v[202:203]\n\tv_cmpx_eq_u64 vcc, %1, v[206:207]\n\tv_cmpx_eq_u64 vcc, %1, v[210:211]\n\ts_cmp_eq_u64 vcc, -1\n\ts_mov_b64 exec, -1\n\ts_cbranch_scc0 .Lr_%=\n\t.Ld_%=:\n\t.subsection 1\n\t.Lr_%=:\n\tv_add_u32 %0, -1, %0\n\ts_nop 1\n\tv_readfirstlane_b32 vcc_hi, %0\n\ts_nop 3\n\ts_cmp_lt_i32 vcc_hi, 1\n\ts_cbranch_scc1 .Ld_%=\n\ts_sleep 2\n\ts_movk_i32 vcc_lo, 0x1000\n\tbuffer_load_dwordx4 v[196:199], %2, %3, 0 offen offset:0 sc1\n\tbuffer_load_dwordx4 v[200:203], %2, %3, 0 offen offset:2048 sc1\n\tbuffer_load_dwordx4 v[204:207], %2, %3, vcc_lo offen offset:0 sc1\n\tbuffer_load_dwordx4 v[208:211], %2, %3, vcc_lo offen offset:2048 sc1\n\ts_waitcnt vmcnt(0)\n\ts_branch .Lc_%=\n\t.subsection 0" : "+v"(tmo) : "s"(tg), "v"(vS), "s"(rWS) : "memory", "vcc", "scc"); SB();
  ST(9);
  {
    f32x2 s4[2], dm[2];
  asm volatile("v_pk_add_f32 %0, v[196:197], v[200:201]\n\tv_pk_add_f32 %1, v[204:205], v[208:209]" : "=&v"(s4[0]), "=&v"(s4[1]) :  : ); SB();
    const f32x2 sm = s4[0] + s4[1];
    const float mean = red32(sm[0] + dppf<0x128>(sm[0])) * (1.0f / 16.0f);
  asm volatile("v_sub_f32 %0, v196, %4\n\tv_sub_f32 %1, v200, %4\n\tv_sub_f32 %2, v204, %4\n\tv_sub_f32 %3, v208, %4" : "=&v"(dm[0][0]), "=&v"(dm[0][1]), "=&v"(dm[1][0]), "=&v"(dm[1][1]) : "v"(mean) : ); SB();
    const f32x2 dq = __builtin_elementwise_fma(dm[0], dm[0], dm[1] * dm[1]);
    float qs = __builtin_fmaf(dq[0] + dq[1], 48.0f, sm[1]);
    qs = red32(qs + dppf<0x128>(qs));
    float r = __builtin_amdgcn_rcpf(rbf(__builtin_amdgcn_sqrtf(rbf(rbf(qs * (1.0f / 768.0f)) + 1e-5f))));
    float mr = -rbf(mean) * r;
    f32x4 rm[4];
    asm volatile("" : "+v"(r), "+v"(mr)); SB();
  asm volatile("s_mov_b64 exec, 0xff00ff\n\tds_write2_b32 %0, %1, %2 offset1:32\n\ts_mov_b64 exec, -1" :  : "v"(nw), "v"(r), "v"(mr) : "memory"); SB();
  asm volatile("ds_write_b64 %0, %1 offset:0" :  : "v"(zwa[0]), "v"(((u32x2){zp[0][0][0], zp[0][0][1]})) : "memory"); SB();
  asm volatile("ds_write_b64 %0, %1 offset:32" :  : "v"(zwa[0]), "v"(((u32x2){zp[0][1][0], zp[0][1][1]})) : "memory"); SB();
  asm volatile("ds_write_b64 %0, %1 offset:64" :  : "v"(zwa[0]), "v"(((u32x2){zp[0][2][0], zp[0][2][1]})) : "memory"); SB();
  asm volatile("ds_write_b64 %0, %1 offset:0" :  : "v"(zwa[1]), "v"(((u32x2){zp[1][0][0], zp[1][0][1]})) : "memory"); SB();
  asm volatile("ds_write_b64 %0, %1 offset:32" :  : "v"(zwa[1]), "v"(((u32x2){zp[1][1][0], zp[1][1][1]})) : "memory"); SB();
  asm volatile("ds_write_b64 %0, %1 offset:64" :  : "v"(zwa[1]), "v"(((u32x2){zp[1][2][0], zp[1][2][1]})) : "memory"); SB();
  asm volatile("ds_read_b64_tr_b16 %0, %1 offset:0" : "=&v"(zt[0][0]) : "v"(zra) : "memory"); SB();
  asm volatile("ds_read_b64_tr_b16 %0, %1 offset:416" : "=&v"(zt[0][1]) : "v"(zra) : "memory"); SB();
  asm volatile("ds_read_b64_tr_b16 %0, %1 offset:32" : "=&v"(zt[1][0]) : "v"(zra) : "memory"); SB();
  asm volatile("ds_read_b64_tr_b16 %0, %1 offset:448" : "=&v"(zt[1][1]) : "v"(zra) : "memory"); SB();
  asm volatile("ds_read_b64_tr_b16 %0, %1 offset:64" : "=&v"(zt[2][0]) : "v"(zra) : "memory"); SB();
  asm volatile("ds_read_b64_tr_b16 %0, %1 offset:480" : "=&v"(zt[2][1]) : "v"(zra) : "memory"); SB();
  asm volatile("s_waitcnt lgkmcnt(7)\n\ts_barrier" :  :  : "memory"); SB();
  asm volatile("ds_read_b128 %0, %1 offset:0" : "=&v"(rm[0]) : "v"(nr) : "memory"); SB();
  asm volatile("ds_read_b128 %0, %1 offset:16" : "=&v"(rm[1]) : "v"(nr) : "memory"); SB();
  asm volatile("ds_read_b128 %0, %1 offset:128" : "=&v"(rm[2]) : "v"(nr) : "memory"); SB();
  asm volatile("ds_read_b128 %0, %1 offset:144" : "=&v"(rm[3]) : "v"(nr) : "memory"); SB();
  asm volatile("s_add_u32 m0, %0, 36864" :  : "s"(mW) : "memory", "scc"); SB();
  asm volatile("s_nop 0\n\tbuffer_load_dwordx4 %0, %1, %2 offen lds" :  : "v"(voB[0]), "s"(rW1), "s"(kw_[9]) : "memory"); SB();
  asm volatile("buffer_load_dwordx4 %0, %1, %2 offen offset:1024 lds" :  : "v"(voB[1]), "s"(rW1), "s"(kw_[9]) : "memory"); SB();
  asm volatile("s_waitcnt lgkmcnt(4)" : "+v"(zt[0][0]), "+v"(zt[0][1]), "+v"(zt[1][0]), "+v"(zt[1][1]), "+v"(zt[2][0]), "+v"(zt[2][1]) :  : "memory"); SB();
  asm volatile("s_waitcnt lgkmcnt(0)" : "+v"(rm[0]), "+v"(rm[1]), "+v"(rm[2]), "+v"(rm[3]) :  : "memory"); SB();
    const f32x2 re[4] = {{rm[0][0], rm[0][1]}, {rm[0][2], rm[0][3]}, {rm[1][0], rm[1][1]}, {rm[1][2], rm[1][3]}};
    const f32x2 me[4] = {{rm[2][0], rm[2][1]}, {rm[2][2], rm[2][3]}, {rm[3][0], rm[3][1]}, {rm[3][2], rm[3][3]}};
    u32x4 zo[3];
#pragma unroll
    for (int e = 0; e < 4; ++e) zo[0][e] = pk2(__builtin_elementwise_fma(unpk(zt[0][e >> 1][e & 1]), re[e], me[e]));
    SB();
  asm volatile("buffer_store_dwordx4 %1, %0, %2, %3 offen offset:0 sc1\n\ts_nop 1" :  : "v"(vZ_), "v"(zo[0]), "s"(rWS), "s"(0u) : "memory"); SB();
#pragma unroll
    for (int e = 0; e < 4; ++e) zo[1][e] = pk2(__builtin_elementwise_fma(unpk(zt[1][e >> 1][e & 1]), re[e], me[e]));
    SB();
  asm volatile("buffer_store_dwordx4 %1, %0, %2, %3 offen offset:1024 sc1\n\ts_nop 1" :  : "v"(vZ_), "v"(zo[1]), "s"(rWS), "s"(0u) : "memory"); SB();
#pragma unroll
    for (int e = 0; e < 4; ++e) zo[2][e] = pk2(__builtin_elementwise_fma(unpk(zt[2][e >> 1][e & 1]), re[e], me[e]));
    SB();
  asm volatile("buffer_store_dwordx4 %1, %0, %2, %3 offen offset:2048 sc1\n\ts_nop 1" :  : "v"(vZ_), "v"(zo[2]), "s"(rWS), "s"(0u) : "memory"); SB();
  }
  ST(10);
  asm volatile("v_mfma_f32_16x16x32_bf16 %0, v[184:187], a[48:51], %0" : "+v"(a1[0][0]) :  : ); SB();
  asm volatile("s_barrier" :  :  : "memory"); SB();
  asm volatile("ds_read_b64_tr_b16 v[172:173], %0 offset:24576" :  : "v"(aW[1][0][0]) : "memory"); SB();
  asm volatile("s_waitcnt vmcnt(11)\n\ts_mov_b64 exec, 1\n\tbuffer_store_dwordx2 %0, off, %1, %2 sc1\n\ts_mov_b64 exec, -1" :  : "v"(((u32x2){tg0, tg1})), "s"(rWS), "s"(sFK) : "memory"); SB();
  asm volatile("v_mfma_f32_16x16x32_bf16 %0, v[184:187], a[52:55], %0" : "+v"(a1[1][0]) :  : ); SB();
  asm volatile("ds_read_b64_tr_b16 v[174:175], %0 offset:25344" :  : "v"(aW[1][0][0]) : "memory"); SB();
  asm volatile("v_lshlrev_b32 %0, 16, v248" : "=v"(bs) :  : ); SB();
  asm volatile("v_mfma_f32_16x16x32_bf16 %0, v[184:187], %1, %0" : "+v"(c1[0]) : "v"(ones) : ); SB();
  asm volatile("ds_read_b64_tr_b16 v[176:177], %0 offset:24576" :  : "v"(aW[1][0][1]) : "memory"); SB();
  asm volatile("buffer_load_dwordx4 %0, %1, %2 offen offset:2048 lds" :  : "v"(voB[2]), "s"(rW1), "s"(kw_[9]) : "memory"); SB();
  asm volatile("v_mfma_f32_16x16x32_bf16 %0, v[188:191], a[48:51], %0" : "+v"(a1[0][1]) :  : ); SB();
  asm volatile("ds_read_b64_tr_b16 v[178:179], %0 offset:25344" :  : "v"(aW[1][0][1]) : "memory"); SB();
  asm volatile("v_mov_b32_dpp v242, v231 wave_shr:1 row_mask:0xf bank_mask:0xf" :  :  : ); SB();
  asm volatile("v_mfma_f32_16x16x32_bf16 %0, v[188:191], a[52:55], %0" : "+v"(a1[1][1]) :  : ); SB();
  asm volatile("ds_read_b64_tr_b16 v[180:181], %0 offset:24576" :  : "v"(aW[1][0][2]) : "memory"); SB();
  asm volatile("s_add_u32 m0, %0, 102400" :  : "s"(mA) : "memory", "scc"); SB();
  asm volatile("v_mfma_f32_16x16x32_bf16 %0, v[188:191], %1, %0" : "+v"(c1[1]) : "v"(ones) : ); SB();
  asm volatile("ds_read_b64_tr_b16 v[182:183], %0 offset:25344" :  : "v"(aW[1][0][2]) : "memory"); SB();
  asm volatile("v_alignbit_b32 %0, v231, v231, 16" : "=v"(im[0][0]) :  : ); SB();
  asm volatile("v_mfma_f32_16x16x32_bf16 %0, v[192:195], a[48:51], %0" : "+v"(a1[0][2]) :  : ); SB();
  asm volatile("buffer_load_dwordx4 %0, %1, %2 offen lds" :  : "v"(voS), "s"(rA), "s"(ao[4]) : "memory"); SB();
  asm volatile("v_alignbit_b32 %0, v230, v230, 16" : "=v"(im[0][1]) :  : ); SB();
  asm volatile("v_mfma_f32_16x16x32_bf16 %0, v[192:195], a[52:55], %0" : "+v"(a1[1][2]) :  : ); SB();
  asm volatile("v_alignbit_b32 %0, v229, v229, 16" : "=v"(im[0][2]) :  : ); SB();
  asm volatile("v_mfma_f32_16x16x32_bf16 %0, v[192:195], %1, %0" : "+v"(c1[2]) : "v"(ones) : ); SB();
  asm volatile("buffer_load_dwordx4 %0, %1, %2 offen offset:1024 lds" :  : "v"(voS), "s"(rA), "s"(ao[5]) : "memory"); SB();
  asm volatile("v_alignbit_b32 %0, v228, v228, 16" : "=v"(im[0][3]) :  : ); SB();
  asm volatile("v_mfma_f32_16x16x32_bf16 %0, v[160:163], a[56:59], %0" : "+v"(a1[0][0]) :  : ); SB();
  asm volatile("ds_read_b64_tr_b16 v[184:185], %0 offset:24576" :  : "v"(aW[1][1][0]) : "memory"); SB();
  asm volatile("v_bfi_b32 %0, %1, v231, v230" : "=v"(im[1][0]) : "s"(0xFFFFu) : ); SB();
  asm volatile("v_mfma_f32_16x16x32_bf16 %0, v[160:163], a[60:63], %0" : "+v"(a1[1][0]) :  : ); SB();
  asm volatile("ds_read_b64_tr_b16 v[186:187], %0 offset:25344" :  : "v"(aW[1][1][0]) : "memory"); SB();
  asm volatile("v_bfi_b32 %0, %1, v230, v229" : "=v"(im[1][1]) : "s"(0xFFFFu) : ); SB();
  asm volatile("v_mfma_f32_16x16x32_bf16 %0, v[164:167], a[56:59], %0" : "+v"(a1[0][1]) :  : ); SB();
  asm volatile("ds_read_b64_tr_b16 v[188:189], %0 offset:24576" :  : "v"(aW[1][1][1]) : "memory"); SB();
  asm volatile("v_bfi_b32 %0, %1, v229, v228" : "=v"(im[1][2]) : "s"(0xFFFFu) : ); SB();
  asm volatile("v_mfma_f32_16x16x32_bf16 %0, v[164:167], a[60:63], %0" : "+v"(a1[1][1]) :  : ); SB();
  asm volatile("ds_read_b64_tr_b16 v[190:191], %0 offset:25344" :  : "v"(aW[1][1][1]) : "memory"); SB();
  asm volatile("v_bfi_b32 %0, %1, v228, v242" : "=v"(im[1][3]) : "s"(0xFFFFu) : ); SB();
  asm volatile("v_mfma_f32_16x16x32_bf16 %0, v[168:171], a[56:59], %0" : "+v"(a1[0][2]) :  : ); SB();
  asm volatile("ds_read_b64_tr_b16 v[192:193], %0 offset:24576" :  : "v"(aW[1][1][2]) : "memory"); SB();
  asm volatile("v_mov_b32_dpp v243, v235 wave_shr:1 row_mask:0xf bank_mask:0xf" :  :  : ); SB();
  asm volatile("v_mfma_f32_16x16x32_bf16 %0, v[168:171], a[60:63], %0" : "+v"(a1[1][2]) :  : ); SB();
  asm volatile("ds_read_b64_tr_b16 v[194:195], %0 offset:25344" :  : "v"(aW[1][1][2]) : "memory"); SB();
  asm volatile("s_waitcnt lgkmcnt(6)\n\tv_mfma_f32_16x16x32_bf16 %0, v[172:175], a[64:67], %0" : "+v"(a1[0][0]) :  : ); SB();
  asm volatile("ds_read_b64_tr_b16 v[160:161], %0 offset:36864" :  : "v"(aW[1][0][0]) : "memory"); SB();
  asm volatile("buffer_load_dwordx4 %0, %1, %2 offen offset:2048 lds" :  : "v"(voS), "s"(rA), "s"(ao[6]) : "memory"); SB();
  asm volatile("v_mfma_f32_16x16x32_bf16 %0, v[172:175], a[68:71], %0" : "+v"(a1[1][0]) :  : ); SB();
  asm volatile("ds_read_b64_tr_b16 v[162:163], %0 offset:37632" :  : "v"(aW[1][0][0]) : "memory"); SB();
  asm volatile("v_alignbit_b32 %0, v235, v235, 16" : "=v"(im[2][0]) :  : ); SB();
  asm volatile("v_mfma_f32_16x16x32_bf16 %0, v[172:175], %1, %0" : "+v"(c1[0]) : "v"(ones) : ); SB();
  asm volatile("ds_read_b64_tr_b16 v[164:165], %0 offset:36864" :  : "v"(aW[1][0][1]) : "memory"); SB();
  asm volatile("v_alignbit_b32 %0, v234, v234, 16" : "=v"(im[2][1]) :  : ); SB();
  asm volatile("v_mfma_f32_16x16x32_bf16 %0, v[176:179], a[64:67], %0" : "+v"(a1[0][1]) :  : ); SB();
  asm volatile("ds_read_b64_tr_b16 v[166:167], %0 offset:37632" :  : "v"(aW[1][0][1]) : "memory"); SB();
  asm volatile("v_alignbit_b32 %0, v233, v233, 16" : "=v"(im[2][2]) :  : ); SB();
  asm volatile("v_mfma_f32_16x16x32_bf16 %0, v[176:179], a[68:71], %0" : "+v"(a1[1][1]) :  : ); SB();
  asm volatile("ds_read_b64_tr_b16 v[168:169], %0 offset:36864" :  : "v"(aW[1][0][2]) : "memory"); SB();
  asm volatile("v_alignbit_b32 %0, v232, v232, 16" : "=v"(im[2][3]) :  : ); SB();
  asm volatile("v_mfma_f32_16x16x32_bf16 %0, v[176:179], %1, %0" : "+v"(c1[1]) : "v"(ones) : ); SB();
  asm volatile("ds_read_b64_tr_b16 v[170:171], %0 offset:37632" :  : "v"(aW[1][0][2]) : "memory"); SB();
  asm volatile("v_bfi_b32 %0, %1, v235, v234" : "=v"(im[3][0]) : "s"(0xFFFFu) : ); SB();
  asm volatile("v_mfma_f32_16x16x32_bf16 %0, v[180:183], a[64:67], %0" : "+v"(a1[0][2]) :  : ); SB();
  asm volatile("buffer_load_dwordx4 %0, %1, %2 offen offset:3072 lds" :  : "v"(voS), "s"(rA), "s"(ao[7]) : "memory"); SB();
  asm volatile("v_bfi_b32 %0, %1, v234, v233" : "=v"(im[3][1]) : "s"(0xFFFFu) : ); SB();
  asm volatile("v_mfma_f32_16x16x32_bf16 %0, v[180:183], a[68:71], %0" : "+v"(a1[1][2]) :  : ); SB();
  asm volatile("v_xor_b32 %0, 384, %0" : "+v"(voS) :  : "memory"); SB();
  asm volatile("s_add_u32 m0, %0, 131072" :  : "s"(mA) : "memory", "scc"); SB();
  asm volatile("v_mfma_f32_16x16x32_bf16 %0, v[180:183], %1, %0" : "+v"(c1[2]) : "v"(ones) : ); SB();
  asm volatile("buffer_load_dwordx4 %0, %1, %2 offen lds" :  : "v"(voS), "s"(rA), "s"(ao[0]) : "memory"); SB();
  asm volatile("s_waitcnt lgkmcnt(6)\n\tv_mfma_f32_16x16x32_bf16 %0, v[184:187], a[72:75], %0" : "+v"(a1[0][0]) :  : ); SB();
  asm volatile("ds_read_b64_tr_b16 v[172:173], %0 offset:36864" :  : "v"(aW[1][1][0]) : "memory"); SB();
  asm volatile("v_bfi_b32 %0, %1, v233, v232" : "=v"(im[3][2]) : "s"(0xFFFFu) : ); SB();
  asm volatile("v_mfma_f32_16x16x32_bf16 %0, v[184:187], a[76:79], %0" : "+v"(a1[1][0]) :  : ); SB();
  asm volatile("ds_read_b64_tr_b16 v[174:175], %0 offset:37632" :  : "v"(aW[1][1][0]) : "memory"); SB();
  asm volatile("v_bfi_b32 %0, %1, v232, v243" : "=v"(im[3][3]) : "s"(0xFFFFu) : ); SB();
  asm volatile("v_mfma_f32_16x16x32_bf16 %0, v[188:191], a[72:75], %0" : "+v"(a1[0][1]) :  : ); SB();
  asm volatile("ds_read_b64_tr_b16 v[176:177], %0 offset:36864" :  : "v"(aW[1][1][1]) : "memory"); SB();
  asm volatile("v_mov_b32_dpp v244, v239 wave_shr:1 row_mask:0xf bank_mask:0xf" :  :  : ); SB();
  asm volatile("v_mfma_f32_16x16x32_bf16 %0, v[188:191], a[76:79], %0" : "+v"(a1[1][1]) :  : ); SB();
  asm volatile("ds_read_b64_tr_b16 v[178:179], %0 offset:37632" :  : "v"(aW[1][1][1]) : "memory"); SB();
  asm volatile("v_alignbit_b32 %0, v239, v239, 16" : "=v"(im[4][0]) :  : ); SB();
  asm volatile("v_mfma_f32_16x16x32_bf16 %0, v[192:195], a[72:75], %0" : "+v"(a1[0][2]) :  : ); SB();
  asm volatile("ds_read_b64_tr_b16 v[180:181], %0 offset:36864" :  : "v"(aW[1][1][2]) : "memory"); SB();
  asm volatile("v_alignbit_b32 %0, v238, v238, 16" : "=v"(im[4][1]) :  : ); SB();
  asm volatile("v_mfma_f32_16x16x32_bf16 %0, v[192:195], a[76:79], %0" : "+v"(a1[1][2]) :  : ); SB();
  asm volatile("ds_read_b64_tr_b16 v[182:183], %0 offset:37632" :  : "v"(aW[1][1][2]) : "memory"); SB();
  asm volatile("s_waitcnt lgkmcnt(6)\n\tv_mfma_f32_16x16x32_bf16 %0, v[160:163], a[80:83], %0" : "+v"(a1[0][0]) :  : ); SB();
  unsigned vFZ = W_FZ + cb * 1024 + w * 256 + (lane & 31) * 8;
  unsigned vB = W_Z2 + (w * 1024 + (cb & 5) * 128 + cb * 6) * 1024 + l16;
  unsigned ha = lb + 59520 + 4608 + (2040 - 8 * (w * 64 + lane)) * 2;
  unsigned za = lb + 59520 + min(w, 2u) * 12928 + (lane < 32 ? lane * 16 : 4608 + ((lane >> 1) & 1) * 4160 + 4096 + (lane & 1) * 16);
  unsigned ew[2] = {lb + 32768 + w * 6144 + wn * 3072 + l16, lb + 32768 + (w ^ 1) * 6144 + wn * 3072 + l16};
  unsigned aa[4], bb;
  {
    const unsigned c = (l15 + 1) & 1;
    const unsigned o = lb + 59520 + 2 * 12928 - w * 128 - 192;
#pragma unroll
    for (int k = 0; k < 4; ++k) aa[k] = o + 4608 + c * 4160 + (2031 - l15 + 8 * g4 - c) * 2 - 512 - 1024 * k;
    bb = o + 512 + 32 * l15 - 32 + 16 * g4;
  }
  unsigned dd = lb + 0 + w * 8192 + g4 * 256 + ((l15 ^ ((g4 >> 1) << 3)) << 4);
  asm volatile("" : "+v"(aa[0]), "+v"(aa[1]), "+v"(aa[2]), "+v"(aa[3]), "+v"(bb), "+v"(dd), "+v"(vFZ), "+v"(vB), "+v"(ha), "+v"(za), "+v"(ew[0]), "+v"(ew[1]));
  SB();
  asm volatile("buffer_load_dwordx4 %0, %1, %2 offen offset:1024 lds" :  : "v"(voS), "s"(rA), "s"(ao[1]) : "memory"); SB();
  asm volatile("buffer_load_dwordx4 %0, %1, %2 offen offset:2048 lds" :  : "v"(voS), "s"(rA), "s"(ao[2]) : "memory"); SB();
  asm volatile("s_waitcnt vmcnt(17)\n\ts_barrier" :  :  : "memory"); SB();
  asm volatile("ds_read_b64_tr_b16 v[184:185], %0 offset:0" :  : "v"(aW[0][0][0]) : "memory"); SB();
  asm volatile("v_alignbit_b32 %0, v237, v237, 16" : "=v"(im[4][2]) :  : ); SB();
  asm volatile("v_mfma_f32_16x16x32_bf16 %0, v[160:163], a[84:87], %0" : "+v"(a1[1][0]) :  : ); SB();
  asm volatile("ds_read_b64_tr_b16 v[186:187], %0 offset:768" :  : "v"(aW[0][0][0]) : "memory"); SB();
  asm volatile("v_alignbit_b32 %0, v236, v236, 16" : "=v"(im[4][3]) :  : ); SB();
  asm volatile("v_mfma_f32_16x16x32_bf16 %0, v[160:163], %1, %0" : "+v"(c1[0]) : "v"(ones) : ); SB();
  asm volatile("ds_read_b64_tr_b16 v[188:189], %0 offset:0" :  : "v"(aW[0][0][1]) : "memory"); SB();
  asm volatile("v_bfi_b32 %0, %1, v239, v238" : "=v"(im[5][0]) : "s"(0xFFFFu) : ); SB();
  asm volatile("v_mfma_f32_16x16x32_bf16 %0, v[164:167], a[80:83], %0" : "+v"(a1[0][1]) :  : ); SB();
  asm volatile("ds_read_b64_tr_b16 v[190:191], %0 offset:768" :  : "v"(aW[0][0][1]) : "memory"); SB();
  asm volatile("v_bfi_b32 %0, %1, v238, v237" : "=v"(im[5][1]) : "s"(0xFFFFu) : ); SB();
  asm volatile("v_mfma_f32_16x16x32_bf16 %0, v[164:167], a[84:87], %0" : "+v"(a1[1][1]) :  : ); SB();
  asm volatile("ds_read_b64_tr_b16 v[192:193], %0 offset:0" :  : "v"(aW[0][0][2]) : "memory"); SB();
  asm volatile("v_bfi_b32 %0, %1, v237, v236" : "=v"(im[5][2]) : "s"(0xFFFFu) : ); SB();
  asm volatile("v_mfma_f32_16x16x32_bf16 %0, v[164:167], %1, %0" : "+v"(c1[1]) : "v"(ones) : ); SB();
  asm volatile("ds_read_b64_tr_b16 v[194:195], %0 offset:768" :  : "v"(aW[0][0][2]) : "memory"); SB();
  asm volatile("v_bfi_b32 %0, %1, v236, v244" : "=v"(im[5][3]) : "s"(0xFFFFu) : ); SB();
  asm volatile("v_mfma_f32_16x16x32_bf16 %0, v[168:171], a[80:83], %0" : "+v"(a1[0][2]) :  : ); SB();
  asm volatile("buffer_load_dwordx4 %0, %1, %2 offen offset:3072 lds" :  : "v"(voS), "s"(rA), "s"(ao[3]) : "memory"); SB();
  asm volatile("s_add_u32 m0, %0, 135168" :  : "s"(mA) : "memory", "scc"); SB();
  asm volatile("v_mfma_f32_16x16x32_bf16 %0, v[168:171], a[84:87], %0" : "+v"(a1[1][2]) :  : ); SB();
  asm volatile("buffer_load_dwordx4 %0, %1, %2 offen lds" :  : "v"(voS), "s"(rA), "s"(ao[4]) : "memory"); SB();
  asm volatile("v_mov_b32 %0, 0" : "=v"(zero4[0]) :  : ); SB();
  asm volatile("v_mfma_f32_16x16x32_bf16 %0, v[168:171], %1, %0" : "+v"(c1[2]) : "v"(ones) : ); SB();
  asm volatile("v_mov_b32 %0, 0" : "=v"(zero4[1]) :  : ); SB();
  asm volatile("s_waitcnt lgkmcnt(5)\n\tv_mfma_f32_16x16x32_bf16 %0, v[172:175], a[88:91], %0" : "+v"(a1[0][0]) :  : ); SB();
  asm volatile("ds_read_b64_tr_b16 v[160:161], %0 offset:0" :  : "v"(aW[0][1][0]) : "memory"); SB();
  asm volatile("v_mov_b32 %0, 0" : "=v"(zero4[2]) :  : ); SB();
  asm volatile("v_mfma_f32_16x16x32_bf16 %0, v[172:175], a[92:95], %0" : "+v"(a1[1][0]) :  : ); SB();
  asm volatile("ds_read_b64_tr_b16 v[162:163], %0 offset:768" :  : "v"(aW[0][1][0]) : "memory"); SB();
  asm volatile("v_mov_b32 %0, 0" : "=v"(zero4[3]) :  : ); SB();
  asm volatile("v_mfma_f32_16x16x32_bf16 %0, v[176:179], a[88:91], %0" : "+v"(a1[0][1]) :  : ); SB();
  asm volatile("ds_read_b64_tr_b16 v[164:165], %0 offset:0" :  : "v"(aW[0][1][1]) : "memory"); SB();
  asm volatile("v_mfma_f32_16x16x32_bf16 %0, v[176:179], a[92:95], %0" : "+v"(a1[1][1]) :  : ); SB();
  asm volatile("ds_read_b64_tr_b16 v[166:167], %0 offset:768" :  : "v"(aW[0][1][1]) : "memory"); SB();
  asm volatile("v_mfma_f32_16x16x32_bf16 %0, v[180:183], a[88:91], %0" : "+v"(a1[0][2]) :  : ); SB();
  asm volatile("ds_read_b64_tr_b16 v[168:169], %0 offset:0" :  : "v"(aW[0][1][2]) : "memory"); SB();
  asm volatile("v_mfma_f32_16x16x32_bf16 %0, v[180:183], a[92:95], %0" : "+v"(a1[1][2]) :  : ); SB();
  asm volatile("ds_read_b64_tr_b16 v[170:171], %0 offset:768" :  : "v"(aW[0][1][2]) : "memory"); SB();
  asm volatile("s_waitcnt lgkmcnt(6)\n\tv_mfma_f32_16x16x32_bf16 %0, v[184:187], a[96:99], %0" : "+v"(a1[0][0]) :  : ); SB();
  asm volatile("ds_read_b64_tr_b16 v[172:173], %0 offset:12288" :  : "v"(aW[0][0][0]) : "memory"); SB();
  asm volatile("v_mfma_f32_16x16x32_bf16 %0, v[184:187], a[100:103], %0" : "+v"(a1[1][0]) :  : ); SB();
  asm volatile("ds_read_b64_tr_b16 v[174:175], %0 offset:13056" :  : "v"(aW[0][0][0]) : "memory"); SB();
  asm volatile("v_mfma_f32_16x16x32_bf16 %0, v[184:187], %1, %0" : "+v"(c1[0]) : "v"(ones) : ); SB();
  asm volatile("ds_read_b64_tr_b16 v[176:177], %0 offset:12288" :  : "v"(aW[0][0][1]) : "memory"); SB();
  asm volatile("v_mfma_f32_16x16x32_bf16 %0, v[188:191], a[96:99], %0" : "+v"(a1[0][1]) :  : ); SB();
  asm volatile("ds_read_b64_tr_b16 v[178:179], %0 offset:13056" :  : "v"(aW[0][0][1]) : "memory"); SB();
  asm volatile("v_mfma_f32_16x16x32_bf16 %0, v[188:191], a[100:103], %0" : "+v"(a1[1][1]) :  : ); SB();
  asm volatile("ds_read_b64_tr_b16 v[180:181], %0 offset:12288" :  : "v"(aW[0][0][2]) : "memory"); SB();
  asm volatile("v_mfma_f32_16x16x32_bf16 %0, v[188:191], %1, %0" : "+v"(c1[1]) : "v"(ones) : ); SB();
  asm volatile("ds_read_b64_tr_b16 v[182:183], %0 offset:13056" :  : "v"(aW[0][0][2]) : "memory"); SB();
  asm volatile("v_mfma_f32_16x16x32_bf16 %0, v[192:195], a[96:99], %0" : "+v"(a1[0][2]) :  : ); SB();
  asm volatile("buffer_load_dwordx4 %0, %1, %2 offen offset:1024 lds" :  : "v"(voS), "s"(rA), "s"(ao[5]) : "memory"); SB();
  asm volatile("v_mfma_f32_16x16x32_bf16 %0, v[192:195], a[100:103], %0" : "+v"(a1[1][2]) :  : ); SB();
  asm volatile("v_mfma_f32_16x16x32_bf16 %0, v[192:195], %1, %0" : "+v"(c1[2]) : "v"(ones) : ); SB();
  asm volatile("s_waitcnt vmcnt(12)\n\ts_mov_b64 exec, 1\n\tbuffer_store_dwordx2 %0, off, %1, %2 sc1\n\ts_mov_b64 exec, -1" :  : "v"(((u32x2){tg0, tg1})), "s"(rWS), "s"(sFZ) : "memory"); SB();
  asm volatile("s_waitcnt lgkmcnt(6)\n\tv_mfma_f32_16x16x32_bf16 %0, v[160:163], a[104:107], %0" : "+v"(a1[0][0]) :  : ); SB();
  asm volatile("ds_read_b64_tr_b16 v[184:185], %0 offset:12288" :  : "v"(aW[0][1][0]) : "memory"); SB();
  asm volatile("v_mfma_f32_16x16x32_bf16 %0, v[160:163], a[108:111], %0" : "+v"(a1[1][0]) :  : ); SB();
  asm volatile("s_mov_b32 exec_lo, 0x10001\n\ts_mov_b32 exec_hi, 0x10001\n\tds_write_b128 %0, %1 offset:0\n\ts_mov_b64 exec, -1" :  : "v"(ca), "v"(c1[0]) : "memory"); SB();
  asm volatile("ds_read_b64_tr_b16 v[186:187], %0 offset:13056" :  : "v"(aW[0][1][0]) : "memory"); SB();
  asm volatile("v_mfma_f32_16x16x32_bf16 %0, v[164:167], a[104:107], %0" : "+v"(a1[0][1]) :  : ); SB();
  asm volatile("ds_read_b64_tr_b16 v[188:189], %0 offset:12288" :  : "v"(aW[0][1][1]) : "memory"); SB();
  asm volatile("v_mfma_f32_16x16x32_bf16 %0, v[164:167], a[108:111], %0" : "+v"(a1[1][1]) :  : ); SB();
  asm volatile("s_mov_b32 exec_lo, 0x10001\n\ts_mov_b32 exec_hi, 0x10001\n\tds_write_b128 %0, %1 offset:64\n\ts_mov_b64 exec, -1" :  : "v"(ca), "v"(c1[1]) : "memory"); SB();
  asm volatile("ds_read_b64_tr_b16 v[190:191], %0 offset:13056" :  : "v"(aW[0][1][1]) : "memory"); SB();
  asm volatile("v_mfma_f32_16x16x32_bf16 %0, v[168:171], a[104:107], %0" : "+v"(a1[0][2]) :  : ); SB();
  asm volatile("ds_read_b64_tr_b16 v[192:193], %0 offset:12288" :  : "v"(aW[0][1][2]) : "memory"); SB();
  asm volatile("buffer_load_dwordx4 %0, %1, %2 offen offset:2048 lds" :  : "v"(voS), "s"(rA), "s"(ao[6]) : "memory"); SB();
  asm volatile("v_mfma_f32_16x16x32_bf16 %0, v[168:171], a[108:111], %0" : "+v"(a1[1][2]) :  : ); SB();
  asm volatile("s_mov_b32 exec_lo, 0x10001\n\ts_mov_b32 exec_hi, 0x10001\n\tds_write_b128 %0, %1 offset:128\n\ts_mov_b64 exec, -1" :  : "v"(ca), "v"(c1[2]) : "memory"); SB();
  asm volatile("ds_read_b64_tr_b16 v[194:195], %0 offset:13056" :  : "v"(aW[0][1][2]) : "memory"); SB();
  asm volatile("buffer_load_dwordx4 %0, %1, %2 offen offset:3072 lds" :  : "v"(voS), "s"(rA), "s"(ao[7]) : "memory"); SB();
  asm volatile("s_waitcnt lgkmcnt(5)\n\tv_mfma_f32_16x16x32_bf16 %0, v[172:175], a[112:115], %0" : "+v"(a1[0][0]) :  : ); SB();
  asm volatile("v_xor_b32 %0, 128, %0" : "+v"(voS) :  : "memory"); SB();
  asm volatile("s_add_u32 m0, %0, 98304" :  : "s"(mA) : "memory", "scc"); SB();
  asm volatile("v_mfma_f32_16x16x32_bf16 %0, v[172:175], a[116:119], %0" : "+v"(a1[1][0]) :  : ); SB();
  asm volatile("buffer_load_dwordx4 %0, %1, %2 offen lds" :  : "v"(voS), "s"(rA), "s"(ao[0]) : "memory"); SB();
  asm volatile("v_mfma_f32_16x16x32_bf16 %0, v[172:175], %1, %0" : "+v"(c1[0]) : "v"(ones) : ); SB();
  asm volatile("v_mfma_f32_16x16x32_bf16 %0, v[176:179], a[112:115], %0" : "+v"(a1[0][1]) :  : ); SB();
  asm volatile("s_waitcnt vmcnt(14) lgkmcnt(1)\n\ts_barrier" :  :  : "memory"); SB();
  asm volatile("ds_read_b64_tr_b16 v[160:161], %0 offset:24576" :  : "v"(aW[0][0][0]) : "memory"); SB();
  asm volatile("s_waitcnt vmcnt(12)" :  :  : "memory"); SB();
  asm volatile("v_mfma_f32_16x16x32_bf16 %0, v[176:179], a[116:119], %0" : "+v"(a1[1][1]) :  : ); SB();
  asm volatile("ds_read_b64_tr_b16 v[162:163], %0 offset:25344" :  : "v"(aW[0][0][0]) : "memory"); SB();
  asm volatile("ds_read_b128 a[200:203], %0 offset:4096" :  : "v"(aA[0]) : "memory"); SB();
  asm volatile("v_mfma_f32_16x16x32_bf16 %0, v[176:179], %1, %0" : "+v"(c1[1]) : "v"(ones) : ); SB();
  asm volatile("ds_read_b64_tr_b16 v[164:165], %0 offset:24576" :  : "v"(aW[0][0][1]) : "memory"); SB();
  asm volatile("ds_read_b128 a[216:219], %0 offset:4096" :  : "v"(aA[1]) : "memory"); SB();
  asm volatile("v_mfma_f32_16x16x32_bf16 %0, v[180:183], a[112:115], %0" : "+v"(a1[0][2]) :  : ); SB();
  asm volatile("ds_read_b64_tr_b16 v[166:167], %0 offset:25344" :  : "v"(aW[0][0][1]) : "memory"); SB();
  asm volatile("s_waitcnt vmcnt(10)" :  :  : "memory"); SB();
  asm volatile("v_mfma_f32_16x16x32_bf16 %0, v[180:183], a[116:119], %0" : "+v"(a1[1][2]) :  : ); SB();
  asm volatile("ds_read_b64_tr_b16 v[168:169], %0 offset:24576" :  : "v"(aW[0][0][2]) : "memory"); SB();
  asm volatile("ds_read_b128 a[204:207], %0 offset:6144" :  : "v"(aA[0]) : "memory"); SB();
  asm volatile("v_mfma_f32_16x16x32_bf16 %0, v[180:183], %1, %0" : "+v"(c1[2]) : "v"(ones) : ); SB();
  asm volatile("ds_read_b64_tr_b16 v[170:171], %0 offset:25344" :  : "v"(aW[0][0][2]) : "memory"); SB();
  asm volatile("ds_read_b128 a[220:223], %0 offset:6144" :  : "v"(aA[1]) : "memory"); SB();
  asm volatile("v_mfma_f32_16x16x32_bf16 %0, v[184:187], a[120:123], %0" : "+v"(a1[0][0]) :  : ); SB();
  asm volatile("buffer_load_dwordx4 %0, %1, %2 offen offset:1024 lds" :  : "v"(voS), "s"(rA), "s"(ao[1]) : "memory"); SB();
  asm volatile("v_mfma_f32_16x16x32_bf16 %0, v[184:187], a[124:127], %0" : "+v"(a1[1][0]) :  : ); SB();
  asm volatile("buffer_load_dwordx4 %0, %1, %2 offen offset:2048 lds" :  : "v"(voS), "s"(rA), "s"(ao[2]) : "memory"); SB();
  asm volatile("s_waitcnt vmcnt(10)" :  :  : "memory"); SB();
  asm volatile("v_mfma_f32_16x16x32_bf16 %0, v[184:187], %1, %0" : "+v"(c1[0]) : "v"(ones) : ); SB();
  asm volatile("ds_read_b128 a[224:227], %0 offset:32768" :  : "v"(aA[0]) : "memory"); SB();
  asm volatile("v_mfma_f32_16x16x32_bf16 %0, v[188:191], a[120:123], %0" : "+v"(a1[0][1]) :  : ); SB();
  asm volatile("ds_read_b64_tr_b16 v[172:173], %0 offset:24576" :  : "v"(aW[0][1][0]) : "memory"); SB();
  asm volatile("ds_read_b128 a[240:243], %0 offset:32768" :  : "v"(aA[1]) : "memory"); SB();
  asm volatile("v_mfma_f32_16x16x32_bf16 %0, v[188:191], a[124:127], %0" : "+v"(a1[1][1]) :  : ); SB();
  asm volatile("ds_read_b64_tr_b16 v[174:175], %0 offset:25344" :  : "v"(aW[0][1][0]) : "memory"); SB();
  asm volatile("buffer_load_dwordx4 %0, %1, %2 offen offset:3072 lds" :  : "v"(voS), "s"(rA), "s"(ao[3]) : "memory"); SB();
  asm volatile("v_mfma_f32_16x16x32_bf16 %0, v[188:191], %1, %0" : "+v"(c1[1]) : "v"(ones) : ); SB();
  asm volatile("ds_read_b64_tr_b16 v[176:177], %0 offset:24576" :  : "v"(aW[0][1][1]) : "memory"); SB();
  asm volatile("s_waitcnt lgkmcnt(9)" :  :  : "memory"); SB();
  asm volatile("v_mfma_f32_16x16x32_bf16 %0, v[192:195], a[120:123], %0" : "+v"(a1[0][2]) :  : ); SB();
  asm volatile("ds_read_b64_tr_b16 v[178:179], %0 offset:25344" :  : "v"(aW[0][1][1]) : "memory"); SB();
  asm volatile("s_add_u32 m0, %0, 102400" :  : "s"(mA) : "memory", "scc"); SB();
  asm volatile("v_mfma_f32_16x16x32_bf16 %0, v[192:195], a[124:127], %0" : "+v"(a1[1][2]) :  : ); SB();
  asm volatile("ds_read_b64_tr_b16 v[180:181], %0 offset:24576" :  : "v"(aW[0][1][2]) : "memory"); SB();
  asm volatile("buffer_load_dwordx4 %0, %1, %2 offen lds" :  : "v"(voS), "s"(rA), "s"(ao[4]) : "memory"); SB();
  asm volatile("v_mfma_f32_16x16x32_bf16 %0, v[192:195], %1, %0" : "+v"(c1[2]) : "v"(ones) : ); SB();
  asm volatile("ds_read_b64_tr_b16 v[182:183], %0 offset:25344" :  : "v"(aW[0][1][2]) : "memory"); SB();
  asm volatile("s_waitcnt vmcnt(10)" :  :  : "memory"); SB();
  asm volatile("v_mfma_f32_16x16x32_bf16 %0, v[160:163], a[128:131], %0" : "+v"(a1[0][0]) :  : ); SB();
  asm volatile("ds_read_b128 a[228:231], %0 offset:34816" :  : "v"(aA[0]) : "memory"); SB();
  asm volatile("v_mfma_f32_16x16x32_bf16 %0, v[160:163], a[132:135], %0" : "+v"(a1[1][0]) :  : ); SB();
  asm volatile("ds_read_b128 a[244:247], %0 offset:34816" :  : "v"(aA[1]) : "memory"); SB();
  asm volatile("v_mfma_f32_16x16x32_bf16 %0, v[160:163], %1, %0" : "+v"(c1[0]) : "v"(ones) : ); SB();
  asm volatile("buffer_load_dwordx4 %0, %1, %2 offen offset:1024 lds" :  : "v"(voS), "s"(rA), "s"(ao[5]) : "memory"); SB();
  asm volatile("s_waitcnt lgkmcnt(8)" :  :  : "memory"); SB();
  asm volatile("v_mfma_f32_16x16x32_bf16 %0, v[164:167], a[128:131], %0" : "+v"(a1[0][1]) :  : ); SB();
  asm volatile("ds_read_b64_tr_b16 v[184:185], %0 offset:36864" :  : "v"(aW[0][0][0]) : "memory"); SB();
  asm volatile("v_mfma_f32_16x16x32_bf16 %0, v[164:167], a[132:135], %0" : "+v"(a1[1][1]) :  : ); SB();
  asm volatile("ds_read_b64_tr_b16 v[186:187], %0 offset:37632" :  : "v"(aW[0][0][0]) : "memory"); SB();
  asm volatile("buffer_load_dwordx4 %0, %1, %2 offen offset:2048 lds" :  : "v"(voS), "s"(rA), "s"(ao[6]) : "memory"); SB();
  asm volatile("v_mfma_f32_16x16x32_bf16 %0, v[164:167], %1, %0" : "+v"(c1[1]) : "v"(ones) : ); SB();
  asm volatile("ds_read_b64_tr_b16 v[188:189], %0 offset:36864" :  : "v"(aW[0][0][1]) : "memory"); SB();
  asm volatile("v_mfma_f32_16x16x32_bf16 %0, v[168:171], a[128:131], %0" : "+v"(a1[0][2]) :  : ); SB();
  asm volatile("ds_read_b64_tr_b16 v[190:191], %0 offset:37632" :  : "v"(aW[0][0][1]) : "memory"); SB();
  asm volatile("v_mfma_f32_16x16x32_bf16 %0, v[168:171], a[132:135], %0" : "+v"(a1[1][2]) :  : ); SB();
  asm volatile("ds_read_b64_tr_b16 v[192:193], %0 offset:36864" :  : "v"(aW[0][0][2]) : "memory"); SB();
  asm volatile("v_mfma_f32_16x16x32_bf16 %0, v[168:171], %1, %0" : "+v"(c1[2]) : "v"(ones) : ); SB();
  asm volatile("ds_read_b64_tr_b16 v[194:195], %0 offset:37632" :  : "v"(aW[0][0][2]) : "memory"); SB();
  asm volatile("buffer_load_dwordx4 %0, %1, %2 offen offset:3072 lds" :  : "v"(voS), "s"(rA), "s"(ao[7]) : "memory"); SB();
  asm volatile("s_waitcnt lgkmcnt(8)\n\tv_mfma_f32_16x16x32_bf16 %0, v[172:175], a[136:139], %0" : "+v"(a1[0][0]) :  : ); SB();
  asm volatile("ds_read_b128 %0, %1 offset:0" : "=&v"(co[0]) : "v"(cr) : "memory"); SB();
  asm volatile("v_mfma_f32_16x16x32_bf16 %0, v[172:175], a[140:143], %0" : "+v"(a1[1][0]) :  : ); SB();
  asm volatile("ds_read_b128 %0, %1 offset:64" : "=&v"(co[1]) : "v"(cr) : "memory"); SB();
  asm volatile("v_mfma_f32_16x16x32_bf16 %0, v[172:175], %1, %0" : "+v"(c1[0]) : "v"(ones) : ); SB();
  asm volatile("ds_read_b128 %0, %1 offset:128" : "=&v"(co[2]) : "v"(cr) : "memory"); SB();
  asm volatile("v_mfma_f32_16x16x32_bf16 %0, v[176:179], a[136:139], %0" : "+v"(a1[0][1]) :  : ); SB();
  asm volatile("ds_write_b128 %0, %1 offset:0" :  : "v"(ha), "v"(im[0]) : "memory"); SB();
  asm volatile("ds_write_b128 %0, %1 offset:4160" :  : "v"(ha), "v"(im[1]) : "memory"); SB();
  asm volatile("ds_read_b64_tr_b16 v[160:161], %0 offset:36864" :  : "v"(aW[0][1][0]) : "memory"); SB();
  asm volatile("buffer_load_dwordx2 %0, %1, %2, %3 offen sc1" : "=&v"(fk) : "v"(vFZ), "s"(rWS), "s"(8192u) : "memory"); SB();
  asm volatile("v_mfma_f32_16x16x32_bf16 %0, v[176:179], a[140:143], %0" : "+v"(a1[1][1]) :  : ); SB();
  asm volatile("ds_write_b128 %0, %1 offset:12928" :  : "v"(ha), "v"(im[2]) : "memory"); SB();
  asm volatile("ds_write_b128 %0, %1 offset:17088" :  : "v"(ha), "v"(im[3]) : "memory"); SB();
  asm volatile("ds_read_b64_tr_b16 v[162:163], %0 offset:37632" :  : "v"(aW[0][1][0]) : "memory"); SB();
  asm volatile("s_waitcnt vmcnt(12)" :  :  : "memory"); SB();
  asm volatile("v_mfma_f32_16x16x32_bf16 %0, v[176:179], %1, %0" : "+v"(c1[1]) : "v"(ones) : ); SB();
  asm volatile("ds_write_b128 %0, %1 offset:25856" :  : "v"(ha), "v"(im[4]) : "memory"); SB();
  asm volatile("ds_read_b64_tr_b16 v[164:165], %0 offset:36864" :  : "v"(aW[0][1][1]) : "memory"); SB();
  asm volatile("ds_read_b128 a[232:235], %0 offset:36864" :  : "v"(aA[0]) : "memory"); SB();
  asm volatile("v_mfma_f32_16x16x32_bf16 %0, v[180:183], a[136:139], %0" : "+v"(a1[0][2]) :  : ); SB();
  asm volatile("ds_read_b64_tr_b16 v[166:167], %0 offset:37632" :  : "v"(aW[0][1][1]) : "memory"); SB();
  asm volatile("ds_read_b128 a[248:251], %0 offset:36864" :  : "v"(aA[1]) : "memory"); SB();
  asm volatile("v_mfma_f32_16x16x32_bf16 %0, v[180:183], a[140:143], %0" : "+v"(a1[1][2]) :  : ); SB();
  asm volatile("ds_read_b64_tr_b16 v[168:169], %0 offset:36864" :  : "v"(aW[0][1][2]) : "memory"); SB();
  asm volatile("v_xor_b32 %0, 896, %0" : "+v"(voS) :  : "memory"); SB();
  asm volatile("v_mfma_f32_16x16x32_bf16 %0, v[180:183], %1, %0" : "+v"(c1[2]) : "v"(ones) : ); SB();
  asm volatile("ds_read_b64_tr_b16 v[170:171], %0 offset:37632" :  : "v"(aW[0][1][2]) : "memory"); SB();
  asm volatile("s_add_u32 m0, %0, 131072" :  : "s"(mA) : "memory", "scc"); SB();
  asm volatile("s_waitcnt lgkmcnt(9)\n\tv_mfma_f32_16x16x32_bf16 %0, v[184:187], a[144:147], %0" : "+v"(a1[0][0]) :  : ); SB();
  asm volatile("buffer_load_dwordx4 %0, %1, %2 offen lds" :  : "v"(voS), "s"(rA), "s"(ao[0]) : "memory"); SB();
  asm volatile("s_waitcnt vmcnt(10)" :  :  : "memory"); SB();
  asm volatile("v_mfma_f32_16x16x32_bf16 %0, v[184:187], a[148:151], %0" : "+v"(a1[1][0]) :  : ); SB();
  asm volatile("ds_write_b128 %0, %1 offset:30016" :  : "v"(ha), "v"(im[5]) : "memory"); SB();
  asm volatile("ds_read_b128 a[236:239], %0 offset:38912" :  : "v"(aA[0]) : "memory"); SB();
  asm volatile("v_mfma_f32_16x16x32_bf16 %0, v[184:187], %1, %0" : "+v"(c1[0]) : "v"(ones) : ); SB();
  asm volatile("ds_read_b128 a[252:255], %0 offset:38912" :  : "v"(aA[1]) : "memory"); SB();
  asm volatile("v_mfma_f32_16x16x32_bf16 %0, v[188:191], a[144:147], %0" : "+v"(a1[0][1]) :  : ); SB();
  asm volatile("buffer_load_dwordx4 %0, %1, %2 offen offset:1024 lds" :  : "v"(voS), "s"(rA), "s"(ao[1]) : "memory"); SB();
  asm volatile("v_mfma_f32_16x16x32_bf16 %0, v[188:191], a[148:151], %0" : "+v"(a1[1][1]) :  : ); SB();
  asm volatile("buffer_load_dwordx4 %0, %1, %2 offen offset:2048 lds" :  : "v"(voS), "s"(rA), "s"(ao[2]) : "memory"); SB();
  asm volatile("v_mfma_f32_16x16x32_bf16 %0, v[188:191], %1, %0" : "+v"(c1[1]) : "v"(ones) : ); SB();
  asm volatile("v_mfma_f32_16x16x32_bf16 %0, v[192:195], a[144:147], %0" : "+v"(a1[0][2]) :  : ); SB();
  asm volatile("ds_write_b128 %0, %1 offset:0" :  : "v"(za), "v"(zero4) : "memory"); SB();
  asm volatile("buffer_load_dwordx4 %0, %1, %2 offen offset:3072 lds" :  : "v"(voS), "s"(rA), "s"(ao[3]) : "memory"); SB();
  asm volatile("s_waitcnt lgkmcnt(4)" :  :  : "memory"); SB();
  asm volatile("v_mfma_f32_16x16x32_bf16 %0, v[192:195], a[148:151], %0" : "+v"(a1[1][2]) :  : ); SB();
  asm volatile("v_mfma_f32_16x16x32_bf16 %0, v[192:195], %1, %0" : "+v"(c1[2]) : "v"(ones) : ); SB();
  asm volatile("v_mfma_f32_16x16x32_bf16 %0, v[160:163], a[152:155], %0" : "+v"(a1[0][0]) :  : ); SB();
  asm volatile("s_add_u32 m0, %0, 135168" :  : "s"(mA) : "memory", "scc"); SB();
  asm volatile("v_mfma_f32_16x16x32_bf16 %0, v[160:163], a[156:159], %0" : "+v"(a1[1][0]) :  : ); SB();
  asm volatile("buffer_load_dwordx4 %0, %1, %2 offen lds" :  : "v"(voS), "s"(rA), "s"(ao[4]) : "memory"); SB();
  asm volatile("v_mfma_f32_16x16x32_bf16 %0, v[160:163], %1, %0" : "+v"(c1[0]) : "v"(ones) : ); SB();
  asm volatile("v_mfma_f32_16x16x32_bf16 %0, v[164:167], a[152:155], %0" : "+v"(a1[0][1]) :  : ); SB();
  asm volatile("s_waitcnt vmcnt(12)" :  :  : "memory"); SB();
  asm volatile("ds_read_b128 a[0:3], %0 offset:0" :  : "v"(aA[0]) : "memory"); SB();
  asm volatile("v_mfma_f32_16x16x32_bf16 %0, v[164:167], a[156:159], %0" : "+v"(a1[1][1]) :  : ); SB();
  asm volatile("ds_read_b128 a[16:19], %0 offset:0" :  : "v"(aA[1]) : "memory"); SB();
  asm volatile("v_mfma_f32_16x16x32_bf16 %0, v[164:167], %1, %0" : "+v"(c1[1]) : "v"(ones) : ); SB();
  asm volatile("buffer_load_dwordx4 %0, %1, %2 offen offset:1024 lds" :  : "v"(voS), "s"(rA), "s"(ao[5]) : "memory"); SB();
  asm volatile("s_waitcnt lgkmcnt(3)" :  :  : "memory"); SB();
  asm volatile("v_mfma_f32_16x16x32_bf16 %0, v[168:171], a[152:155], %0" : "+v"(a1[0][2]) :  : ); SB();
  asm volatile("v_mfma_f32_16x16x32_bf16 %0, v[168:171], a[156:159], %0" : "+v"(a1[1][2]) :  : ); SB();
  asm volatile("buffer_load_dwordx4 %0, %1, %2 offen offset:2048 lds" :  : "v"(voS), "s"(rA), "s"(ao[6]) : "memory"); SB();
  asm volatile("v_mfma_f32_16x16x32_bf16 %0, v[168:171], %1, %0" : "+v"(c1[2]) : "v"(ones) : ); SB();
  ST(11);
  f32x4 ac[8];
  u32x2 cA[2][8][2];
  asm volatile("buffer_load_dwordx4 %0, %1, %2 offen offset:3072 lds" :  : "v"(voS), "s"(rA), "s"(ao[7]) : "memory"); SB();
  asm volatile("s_waitcnt vmcnt(13)" :  :  : "memory"); SB();
  asm volatile("ds_read_b128 a[4:7], %0 offset:2048" :  : "v"(aA[0]) : "memory"); SB();
  asm volatile("ds_read_b128 a[20:23], %0 offset:2048" :  : "v"(aA[1]) : "memory"); SB();
  asm volatile("s_waitcnt lgkmcnt(2)" :  :  : "memory"); SB();
  asm volatile("v_xor_b32 %0, 128, %0" : "+v"(voS) :  : "memory"); SB();
  asm volatile("s_add_u32 m0, %0, 98304" :  : "s"(mA) : "memory", "scc"); SB();
  asm volatile("s_nop 0\n\tbuffer_load_dwordx4 %0, %1, %2 offen lds" :  : "v"(voS), "s"(rA), "s"(ao[0]) : "memory"); SB();
  asm volatile("buffer_load_dwordx4 %0, %1, %2 offen offset:1024 lds" :  : "v"(voS), "s"(rA), "s"(ao[1]) : "memory"); SB();
  asm volatile("buffer_load_dwordx2 %0, %1, %2, %3 offen sc1" : "=&v"(fz) : "v"(vFZ), "s"(rWS), "s"(0u) : "memory"); SB();
  asm volatile("s_waitcnt vmcnt(14)" :  :  : "memory"); SB();
  asm volatile("ds_read_b128 a[8:11], %0 offset:4096" :  : "v"(aA[0]) : "memory"); SB();
  asm volatile("ds_read_b128 a[24:27], %0 offset:4096" :  : "v"(aA[1]) : "memory"); SB();
  asm volatile("s_waitcnt lgkmcnt(2)" :  :  : "memory"); SB();
  asm volatile("buffer_load_dwordx4 %0, %1, %2 offen offset:2048 lds" :  : "v"(voS), "s"(rA), "s"(ao[2]) : "memory"); SB();
  asm volatile("buffer_load_dwordx4 %0, %1, %2 offen offset:3072 lds" :  : "v"(voS), "s"(rA), "s"(ao[3]) : "memory"); SB();
  asm volatile("s_waitcnt vmcnt(14)" :  :  : "memory"); SB();
  asm volatile("ds_read_b128 a[12:15], %0 offset:6144" :  : "v"(aA[0]) : "memory"); SB();
  asm volatile("ds_read_b128 a[28:31], %0 offset:6144" :  : "v"(aA[1]) : "memory"); SB();
  asm volatile("s_waitcnt lgkmcnt(2)" :  :  : "memory"); SB();
  asm volatile("s_add_u32 m0, %0, 102400" :  : "s"(mA) : "memory", "scc"); SB();
  asm volatile("s_nop 0\n\tbuffer_load_dwordx4 %0, %1, %2 offen lds" :  : "v"(voS), "s"(rA), "s"(ao[4]) : "memory"); SB();
  asm volatile("buffer_load_dwordx4 %0, %1, %2 offen offset:1024 lds" :  : "v"(voS), "s"(rA), "s"(ao[5]) : "memory"); SB();
  asm volatile("s_waitcnt vmcnt(15)\n\t.Lc_%=:\n\tv_cmp_ne_u64 vcc, %2, %1\n\ts_cbranch_vccnz .Lr_%=\n\t.Ld_%=:\n\t.subsection 1\n\t.Lr_%=:\n\tv_add_u32 %0, -1, %0\n\ts_nop 1\n\tv_readfirstlane_b32 vcc_hi, %0\n\ts_nop 3\n\ts_cmp_lt_i32 vcc_hi, 1\n\ts_cbranch_scc1 .Ld_%=\n\ts_sleep 2\n\ts_mov_b32 vcc_lo, 8192\n\tbuffer_load_dwordx2 %1, %3, %4, vcc_lo offen sc1\n\ts_waitcnt vmcnt(0)\n\ts_branch .Lc_%=\n\t.subsection 0" : "+v"(tmo), "+v"(fk) : "s"(tg), "v"(vFZ), "s"(rWS) : "memory", "vcc", "scc"); SB();
  asm volatile("s_add_u32 m0, %0, 85888" :  : "s"(mU) : "memory", "scc"); SB();
  asm volatile("s_nop 0\n\tbuffer_load_dwordx4 %0, %1, %2 offen sc1 lds" :  : "v"(vF), "s"(rWS), "s"(uso) : "memory"); SB();
  asm volatile("s_waitcnt lgkmcnt(0)" :  :  : "memory"); SB();
  asm volatile("s_waitcnt vmcnt(14)" :  :  : "memory"); SB();
  asm volatile("ds_read_b128 a[32:35], %0 offset:32768" :  : "v"(aA[0]) : "memory"); SB();
  asm volatile("ds_read_b128 a[48:51], %0 offset:32768" :  : "v"(aA[1]) : "memory"); SB();
  asm volatile("s_add_u32 m0, %0, 102400" :  : "s"(mA) : "memory", "scc"); SB();
  asm volatile("s_nop 0\n\tbuffer_load_dwordx4 %0, %1, %2 offen offset:2048 lds" :  : "v"(voS), "s"(rA), "s"(ao[6]) : "memory"); SB();
  asm volatile("buffer_load_dwordx4 %0, %1, %2 offen offset:3072 lds" :  : "v"(voS), "s"(rA), "s"(ao[7]) : "memory"); SB();
  asm volatile("s_waitcnt vmcnt(14)" :  :  : "memory"); SB();
  asm volatile("ds_read_b128 a[36:39], %0 offset:34816" :  : "v"(aA[0]) : "memory"); SB();
  asm volatile("ds_read_b128 a[52:55], %0 offset:34816" :  : "v"(aA[1]) : "memory"); SB();
  asm volatile("s_waitcnt lgkmcnt(2)" :  :  : "memory"); SB();
  asm volatile("v_xor_b32 %0, 384, %0" : "+v"(voS) :  : "memory"); SB();
  asm volatile("s_add_u32 m0, %0, 131072" :  : "s"(mA) : "memory", "scc"); SB();
  asm volatile("s_nop 0\n\tbuffer_load_dwordx4 %0, %1, %2 offen lds" :  : "v"(voS), "s"(rA), "s"(ao[0]) : "memory"); SB();
  asm volatile("buffer_load_dwordx4 %0, %1, %2 offen offset:1024 lds" :  : "v"(voS), "s"(rA), "s"(ao[1]) : "memory"); SB();
  asm volatile("s_waitcnt vmcnt(14)" :  :  : "memory"); SB();
  asm volatile("ds_read_b128 a[40:43], %0 offset:36864" :  : "v"(aA[0]) : "memory"); SB();
  asm volatile("ds_read_b128 a[56:59], %0 offset:36864" :  : "v"(aA[1]) : "memory"); SB();
  asm volatile("s_waitcnt lgkmcnt(2)" :  :  : "memory"); SB();
  asm volatile("buffer_load_dwordx4 %0, %1, %2 offen offset:2048 lds" :  : "v"(voS), "s"(rA), "s"(ao[2]) : "memory"); SB();
  asm volatile("buffer_load_dwordx4 %0, %1, %2 offen offset:3072 lds" :  : "v"(voS), "s"(rA), "s"(ao[3]) : "memory"); SB();
  asm volatile("s_waitcnt vmcnt(14)" :  :  : "memory"); SB();
  asm volatile("ds_read_b128 a[44:47], %0 offset:38912" :  : "v"(aA[0]) : "memory"); SB();
  asm volatile("ds_read_b128 a[60:63], %0 offset:38912" :  : "v"(aA[1]) : "memory"); SB();
  asm volatile("s_waitcnt lgkmcnt(2)" :  :  : "memory"); SB();
  asm volatile("s_add_u32 m0, %0, 135168" :  : "s"(mA) : "memory", "scc"); SB();
  asm volatile("s_nop 0\n\tbuffer_load_dwordx4 %0, %1, %2 offen lds" :  : "v"(voS), "s"(rA), "s"(ao[4]) : "memory"); SB();
  asm volatile("buffer_load_dwordx4 %0, %1, %2 offen offset:1024 lds" :  : "v"(voS), "s"(rA), "s"(ao[5]) : "memory"); SB();
  asm volatile("s_waitcnt vmcnt(14)" :  :  : "memory"); SB();
  asm volatile("ds_read_b128 a[64:67], %0 offset:0" :  : "v"(aA[0]) : "memory"); SB();
  asm volatile("ds_read_b128 a[80:83], %0 offset:0" :  : "v"(aA[1]) : "memory"); SB();
  asm volatile("s_waitcnt vmcnt(13)\n\t.Lc_%=:\n\tv_cmp_ne_u64 vcc, %2, %1\n\ts_cbranch_vccnz .Lr_%=\n\t.Ld_%=:\n\t.subsection 1\n\t.Lr_%=:\n\tv_add_u32 %0, -1, %0\n\ts_nop 1\n\tv_readfirstlane_b32 vcc_hi, %0\n\ts_nop 3\n\ts_cmp_lt_i32 vcc_hi, 1\n\ts_cbranch_scc1 .Ld_%=\n\ts_sleep 2\n\ts_mov_b32 vcc_lo, 0\n\tbuffer_load_dwordx2 %1, %3, %4, vcc_lo offen sc1\n\ts_waitcnt vmcnt(0)\n\ts_branch .Lc_%=\n\t.subsection 0" : "+v"(tmo), "+v"(fz) : "s"(tg), "v"(vFZ), "s"(rWS) : "memory", "vcc", "scc"); SB();
  asm volatile("buffer_load_dwordx4 v[160:163], %0, %1, %2 offen offset:0 sc1" :  : "v"(vB), "s"(rWS), "s"(0u) : "memory"); SB();
  asm volatile("s_waitcnt lgkmcnt(2)" :  :  : "memory"); SB();
  asm volatile("buffer_load_dwordx4 v[164:167], %0, %1, %2 offen offset:1024 sc1" :  : "v"(vB), "s"(rWS), "s"(0u) : "memory"); SB();
  asm volatile("s_waitcnt vmcnt(13)" :  :  : "memory"); SB();
  asm volatile("ds_read_b128 a[68:71], %0 offset:2048" :  : "v"(aA[0]) : "memory"); SB();
  asm volatile("ds_read_b128 a[84:87], %0 offset:2048" :  : "v"(aA[1]) : "memory"); SB();
  asm volatile("buffer_load_dwordx4 v[168:171], %0, %1, %2 offen offset:0 sc1" :  : "v"(vB), "s"(rWS), "s"(65536u) : "memory"); SB();
  asm volatile("buffer_load_dwordx4 v[172:175], %0, %1, %2 offen offset:1024 sc1" :  : "v"(vB), "s"(rWS), "s"(65536u) : "memory"); SB();
  asm volatile("v_xor_b32 %0, 131072, %0" : "+v"(vB) :  : "memory"); SB();
  asm volatile("s_waitcnt vmcnt(12) lgkmcnt(0)\n\ts_barrier" :  :  : "memory"); SB();
  ST(14);
  asm volatile("ds_read2_b32 %0, %1 offset0:176 offset1:177" : "=&v"(cA[0][0][0]) : "v"(aa[0]) : "memory"); SB();
  asm volatile("ds_read2_b32 %0, %1 offset0:178 offset1:179" : "=&v"(cA[0][0][1]) : "v"(aa[0]) : "memory"); SB();
  asm volatile("ds_read_b128 a[124:127], %0 offset:3776" :  : "v"(bb) : "memory"); SB();
  asm volatile("ds_read_b128 a[120:123], %0 offset:3264" :  : "v"(bb) : "memory"); SB();
  asm volatile("ds_read_b128 a[116:119], %0 offset:2752" :  : "v"(bb) : "memory"); SB();
  asm volatile("ds_read_b128 a[112:115], %0 offset:2240" :  : "v"(bb) : "memory"); SB();
  asm volatile("ds_read2_b32 %0, %1 offset0:48 offset1:49" : "=&v"(cA[0][1][0]) : "v"(aa[0]) : "memory"); SB();
  asm volatile("ds_read2_b32 %0, %1 offset0:50 offset1:51" : "=&v"(cA[0][1][1]) : "v"(aa[0]) : "memory"); SB();
  asm volatile("ds_read_b128 a[108:111], %0 offset:1728" :  : "v"(bb) : "memory"); SB();
  asm volatile("ds_read2_b32 %0, %1 offset0:176 offset1:177" : "=&v"(cA[0][2][0]) : "v"(aa[1]) : "memory"); SB();
  asm volatile("" : "+v"(a1[0][0]), "+v"(a1[0][1]), "+v"(a1[0][2]), "+v"(a1[1][0]), "+v"(a1[1][1]), "+v"(a1[1][2]), "+v"(c1[0]), "+v"(c1[1]), "+v"(c1[2]), "+v"(co[0]), "+v"(co[1]), "+v"(co[2]) :  : ); SB();
  c1[0] += co[0]; c1[1] += co[1]; c1[2] += co[2];
#pragma unroll
  for (int n = 0; n < 6; ++n) { a1[n / 3][n % 3] = __builtin_elementwise_fma((f32x4){-xm[n / 3], -xm[n / 3], -xm[n / 3], -xm[n / 3]}, c1[n % 3], a1[n / 3][n % 3]); asm volatile("" : "+v"(a1[n / 3][n % 3])); }
  SB();
  asm volatile("ds_read_b128 a[72:75], %0 offset:4096" :  : "v"(aA[0]) : "memory"); SB();
  asm volatile("ds_read_b128 a[88:91], %0 offset:4096" :  : "v"(aA[1]) : "memory"); SB();
  asm volatile("buffer_load_dwordx4 v[176:179], %0, %1, %2 offen offset:0 sc1" :  : "v"(vB), "s"(rWS), "s"(0u) : "memory"); SB();
  asm volatile("buffer_load_dwordx4 v[180:183], %0, %1, %2 offen offset:1024 sc1" :  : "v"(vB), "s"(rWS), "s"(0u) : "memory"); SB();
  asm volatile("s_waitcnt vmcnt(12)" :  :  : "memory"); SB();
  asm volatile("ds_read_b128 a[76:79], %0 offset:6144" :  : "v"(aA[0]) : "memory"); SB();
  asm volatile("ds_read_b128 a[92:95], %0 offset:6144" :  : "v"(aA[1]) : "memory"); SB();
  if (nch == 3) {
  asm volatile("s_waitcnt lgkmcnt(6)\n\tv_mfma_f32_16x16x32_bf16 %0, %1, a[124:127], 0" : "=&v"(ac[7]) : "v"(((u32x4){cA[0][0][0][0], cA[0][0][0][1], cA[0][0][1][0], cA[0][0][1][1]})) : ); SB();
  asm volatile("ds_read2_b32 %0, %1 offset0:178 offset1:179" : "=&v"(cA[0][2][1]) : "v"(aa[1]) : "memory"); SB();
  asm volatile("v_mfma_f32_16x16x32_bf16 %0, %1, a[120:123], 0" : "=&v"(ac[6]) : "v"(((u32x4){cA[0][0][0][0], cA[0][0][0][1], cA[0][0][1][0], cA[0][0][1][1]})) : ); SB();
  asm volatile("ds_read2_b32 %0, %1 offset0:48 offset1:49" : "=&v"(cA[0][3][0]) : "v"(aa[1]) : "memory"); SB();
  asm volatile("v_mfma_f32_16x16x32_bf16 %0, %1, a[116:119], 0" : "=&v"(ac[5]) : "v"(((u32x4){cA[0][0][0][0], cA[0][0][0][1], cA[0][0][1][0], cA[0][0][1][1]})) : ); SB();
  asm volatile("ds_read2_b32 %0, %1 offset0:50 offset1:51" : "=&v"(cA[0][3][1]) : "v"(aa[1]) : "memory"); SB();
  asm volatile("v_mfma_f32_16x16x32_bf16 %0, %1, a[112:115], 0" : "=&v"(ac[4]) : "v"(((u32x4){cA[0][0][0][0], cA[0][0][0][1], cA[0][0][1][0], cA[0][0][1][1]})) : ); SB();
  asm volatile("ds_read_b128 a[104:107], %0 offset:1216" :  : "v"(bb) : "memory"); SB();
  asm volatile("v_mfma_f32_16x16x32_bf16 %0, %1, a[120:123], %0" : "+v"(ac[7]) : "v"(((u32x4){cA[0][1][0][0], cA[0][1][0][1], cA[0][1][1][0], cA[0][1][1][1]})) : ); SB();
  asm volatile("ds_read2_b32 %0, %1 offset0:176 offset1:177" : "=&v"(cA[0][4][0]) : "v"(aa[2]) : "memory"); SB();
  asm volatile("v_mfma_f32_16x16x32_bf16 %0, %1, a[116:119], %0" : "+v"(ac[6]) : "v"(((u32x4){cA[0][1][0][0], cA[0][1][0][1], cA[0][1][1][0], cA[0][1][1][1]})) : ); SB();
  asm volatile("ds_read2_b32 %0, %1 offset0:178 offset1:179" : "=&v"(cA[0][4][1]) : "v"(aa[2]) : "memory"); SB();
  asm volatile("buffer_load_dwordx4 v[184:187], %0, %1, %2 offen offset:0 sc1" :  : "v"(vB), "s"(rWS), "s"(65536u) : "memory"); SB();
  asm volatile("v_mfma_f32_16x16x32_bf16 %0, %1, a[112:115], %0" : "+v"(ac[5]) : "v"(((u32x4){cA[0][1][0][0], cA[0][1][0][1], cA[0][1][1][0], cA[0][1][1][1]})) : ); SB();
  asm volatile("ds_read2_b32 %0, %1 offset0:48 offset1:49" : "=&v"(cA[0][5][0]) : "v"(aa[2]) : "memory"); SB();
  asm volatile("s_waitcnt lgkmcnt(9)\n\tv_mfma_f32_16x16x32_bf16 %0, %1, a[108:111], %0" : "+v"(ac[4]) : "v"(((u32x4){cA[0][1][0][0], cA[0][1][0][1], cA[0][1][1][0], cA[0][1][1][1]})) : ); SB();
  asm volatile("ds_read2_b32 %0, %1 offset0:50 offset1:51" : "=&v"(cA[0][5][1]) : "v"(aa[2]) : "memory"); SB();
  asm volatile("s_waitcnt lgkmcnt(7)\n\tv_mfma_f32_16x16x32_bf16 %0, %1, a[116:119], %0" : "+v"(ac[7]) : "v"(((u32x4){cA[0][2][0][0], cA[0][2][0][1], cA[0][2][1][0], cA[0][2][1][1]})) : ); SB();
  asm volatile("ds_read_b128 a[100:103], %0 offset:704" :  : "v"(bb) : "memory"); SB();
  asm volatile("buffer_load_dwordx4 v[188:191], %0, %1, %2 offen offset:1024 sc1" :  : "v"(vB), "s"(rWS), "s"(65536u) : "memory"); SB();
  asm volatile("v_mfma_f32_16x16x32_bf16 %0, %1, a[112:115], %0" : "+v"(ac[6]) : "v"(((u32x4){cA[0][2][0][0], cA[0][2][0][1], cA[0][2][1][0], cA[0][2][1][1]})) : ); SB();
  asm volatile("ds_read2_b32 %0, %1 offset0:176 offset1:177" : "=&v"(cA[0][6][0]) : "v"(aa[3]) : "memory"); SB();
  asm volatile("v_mfma_f32_16x16x32_bf16 %0, %1, a[108:111], %0" : "+v"(ac[5]) : "v"(((u32x4){cA[0][2][0][0], cA[0][2][0][1], cA[0][2][1][0], cA[0][2][1][1]})) : ); SB();
  asm volatile("ds_read2_b32 %0, %1 offset0:178 offset1:179" : "=&v"(cA[0][6][1]) : "v"(aa[3]) : "memory"); SB();
  asm volatile("v_mfma_f32_16x16x32_bf16 %0, %1, a[108:111], 0" : "=&v"(ac[3]) : "v"(((u32x4){cA[0][0][0][0], cA[0][0][0][1], cA[0][0][1][0], cA[0][0][1][1]})) : ); SB();
  asm volatile("ds_read2_b32 %0, %1 offset0:48 offset1:49" : "=&v"(cA[0][7][0]) : "v"(aa[3]) : "memory"); SB();
  asm volatile("s_waitcnt lgkmcnt(9)\n\tv_mfma_f32_16x16x32_bf16 %0, %1, a[112:115], %0" : "+v"(ac[7]) : "v"(((u32x4){cA[0][3][0][0], cA[0][3][0][1], cA[0][3][1][0], cA[0][3][1][1]})) : ); SB();
  asm volatile("ds_read2_b32 %0, %1 offset0:50 offset1:51" : "=&v"(cA[0][7][1]) : "v"(aa[3]) : "memory"); SB();
  asm volatile("v_xor_b32 %0, 393216, %0" : "+v"(vB) :  : "memory"); SB();
  asm volatile("v_mfma_f32_16x16x32_bf16 %0, %1, a[108:111], %0" : "+v"(ac[6]) : "v"(((u32x4){cA[0][3][0][0], cA[0][3][0][1], cA[0][3][1][0], cA[0][3][1][1]})) : ); SB();
  asm volatile("ds_read_b128 a[96:99], %0 offset:192" :  : "v"(bb) : "memory"); SB();
  asm volatile("buffer_load_dwordx4 v[192:195], %0, %1, %2 offen offset:0 sc1" :  : "v"(vB), "s"(rWS), "s"(0u) : "memory"); SB();
  asm volatile("s_waitcnt lgkmcnt(8)\n\tv_mfma_f32_16x16x32_bf16 %0, %1, a[104:107], %0" : "+v"(ac[5]) : "v"(((u32x4){cA[0][3][0][0], cA[0][3][0][1], cA[0][3][1][0], cA[0][3][1][1]})) : ); SB();
  asm volatile("ds_read2_b32 %0, %1 offset0:160 offset1:161" : "=&v"(cA[1][0][0]) : "v"(aa[0]) : "memory"); SB();
  asm volatile("v_mfma_f32_16x16x32_bf16 %0, %1, a[104:107], %0" : "+v"(ac[4]) : "v"(((u32x4){cA[0][2][0][0], cA[0][2][0][1], cA[0][2][1][0], cA[0][2][1][1]})) : ); SB();
  asm volatile("buffer_load_dwordx4 v[196:199], %0, %1, %2 offen offset:1024 sc1" :  : "v"(vB), "s"(rWS), "s"(0u) : "memory"); SB();
  asm volatile("v_mfma_f32_16x16x32_bf16 %0, %1, a[108:111], %0" : "+v"(ac[7]) : "v"(((u32x4){cA[0][4][0][0], cA[0][4][0][1], cA[0][4][1][0], cA[0][4][1][1]})) : ); SB();
  asm volatile("ds_read2_b32 %0, %1 offset0:162 offset1:163" : "=&v"(cA[1][0][1]) : "v"(aa[0]) : "memory"); SB();
  asm volatile("v_mfma_f32_16x16x32_bf16 %0, %1, a[104:107], %0" : "+v"(ac[6]) : "v"(((u32x4){cA[0][4][0][0], cA[0][4][0][1], cA[0][4][1][0], cA[0][4][1][1]})) : ); SB();
  asm volatile("ds_read_b128 a[156:159], %0 offset:3712" :  : "v"(bb) : "memory"); SB();
  asm volatile("v_mfma_f32_16x16x32_bf16 %0, %1, a[104:107], %0" : "+v"(ac[3]) : "v"(((u32x4){cA[0][1][0][0], cA[0][1][0][1], cA[0][1][1][0], cA[0][1][1][1]})) : ); SB();
  asm volatile("ds_read_b128 a[152:155], %0 offset:3200" :  : "v"(bb) : "memory"); SB();
  asm volatile("buffer_load_dwordx4 v[200:203], %0, %1, %2 offen offset:0 sc1" :  : "v"(vB), "s"(rWS), "s"(65536u) : "memory"); SB();
  asm volatile("v_mfma_f32_16x16x32_bf16 %0, %1, a[104:107], 0" : "=&v"(ac[2]) : "v"(((u32x4){cA[0][0][0][0], cA[0][0][0][1], cA[0][0][1][0], cA[0][0][1][1]})) : ); SB();
  asm volatile("s_waitcnt lgkmcnt(7)\n\tv_mfma_f32_16x16x32_bf16 %0, %1, a[104:107], %0" : "+v"(ac[7]) : "v"(((u32x4){cA[0][5][0][0], cA[0][5][0][1], cA[0][5][1][0], cA[0][5][1][1]})) : ); SB();
  asm volatile("ds_read_b128 a[128:131], %0 offset:128" :  : "v"(bb) : "memory"); SB();
  asm volatile("v_mfma_f32_16x16x32_bf16 %0, %1, a[100:103], %0" : "+v"(ac[6]) : "v"(((u32x4){cA[0][5][0][0], cA[0][5][0][1], cA[0][5][1][0], cA[0][5][1][1]})) : ); SB();
  asm volatile("ds_read_b128 a[132:135], %0 offset:640" :  : "v"(bb) : "memory"); SB();
  asm volatile("v_mfma_f32_16x16x32_bf16 %0, %1, a[100:103], %0" : "+v"(ac[5]) : "v"(((u32x4){cA[0][4][0][0], cA[0][4][0][1], cA[0][4][1][0], cA[0][4][1][1]})) : ); SB();
  asm volatile("ds_read2_b32 %0, %1 offset0:32 offset1:33" : "=&v"(cA[1][1][0]) : "v"(aa[0]) : "memory"); SB();
  asm volatile("buffer_load_dwordx4 v[204:207], %0, %1, %2 offen offset:1024 sc1" :  : "v"(vB), "s"(rWS), "s"(65536u) : "memory"); SB();
  asm volatile("v_mfma_f32_16x16x32_bf16 %0, %1, a[100:103], %0" : "+v"(ac[4]) : "v"(((u32x4){cA[0][3][0][0], cA[0][3][0][1], cA[0][3][1][0], cA[0][3][1][1]})) : ); SB();
  asm volatile("v_xor_b32 %0, 262144, %0" : "+v"(vB) :  : "memory"); SB();
  asm volatile("v_mfma_f32_16x16x32_bf16 %0, %1, a[100:103], %0" : "+v"(ac[7]) : "v"(((u32x4){cA[0][6][0][0], cA[0][6][0][1], cA[0][6][1][0], cA[0][6][1][1]})) : ); SB();
  asm volatile("ds_read2_b32 %0, %1 offset0:34 offset1:35" : "=&v"(cA[1][1][1]) : "v"(aa[0]) : "memory"); SB();
  asm volatile("v_mfma_f32_16x16x32_bf16 %0, %1, a[100:103], %0" : "+v"(ac[3]) : "v"(((u32x4){cA[0][2][0][0], cA[0][2][0][1], cA[0][2][1][0], cA[0][2][1][1]})) : ); SB();
  asm volatile("ds_read_b128 a[148:151], %0 offset:2688" :  : "v"(bb) : "memory"); SB();
  asm volatile("buffer_load_dwordx4 v[208:211], %0, %1, %2 offen offset:0 sc1" :  : "v"(vB), "s"(rWS), "s"(2048u) : "memory"); SB();
  asm volatile("v_mfma_f32_16x16x32_bf16 %0, %1, a[100:103], %0" : "+v"(ac[2]) : "v"(((u32x4){cA[0][1][0][0], cA[0][1][0][1], cA[0][1][1][0], cA[0][1][1][1]})) : ); SB();
  asm volatile("ds_read_b128 a[136:139], %0 offset:1152" :  : "v"(bb) : "memory"); SB();
  asm volatile("v_mfma_f32_16x16x32_bf16 %0, %1, a[100:103], 0" : "=&v"(ac[1]) : "v"(((u32x4){cA[0][0][0][0], cA[0][0][0][1], cA[0][0][1][0], cA[0][0][1][1]})) : ); SB();
  asm volatile("buffer_load_dwordx4 v[212:215], %0, %1, %2 offen offset:1024 sc1" :  : "v"(vB), "s"(rWS), "s"(2048u) : "memory"); SB();
  asm volatile("s_waitcnt lgkmcnt(6)\n\tv_mfma_f32_16x16x32_bf16 %0, %1, a[96:99], %0" : "+v"(ac[7]) : "v"(((u32x4){cA[0][7][0][0], cA[0][7][0][1], cA[0][7][1][0], cA[0][7][1][1]})) : ); SB();
  asm volatile("ds_read2_b32 %0, %1 offset0:160 offset1:161" : "=&v"(cA[1][2][0]) : "v"(aa[1]) : "memory"); SB();
  asm volatile("v_mfma_f32_16x16x32_bf16 %0, %1, a[96:99], %0" : "+v"(ac[6]) : "v"(((u32x4){cA[0][6][0][0], cA[0][6][0][1], cA[0][6][1][0], cA[0][6][1][1]})) : ); SB();
  asm volatile("ds_read2_b32 %0, %1 offset0:162 offset1:163" : "=&v"(cA[1][2][1]) : "v"(aa[1]) : "memory"); SB();
  asm volatile("v_mfma_f32_16x16x32_bf16 %0, %1, a[96:99], %0" : "+v"(ac[5]) : "v"(((u32x4){cA[0][5][0][0], cA[0][5][0][1], cA[0][5][1][0], cA[0][5][1][1]})) : ); SB();
  asm volatile("ds_read_b128 a[140:143], %0 offset:1664" :  : "v"(bb) : "memory"); SB();
  asm volatile("v_mfma_f32_16x16x32_bf16 %0, %1, a[96:99], %0" : "+v"(ac[4]) : "v"(((u32x4){cA[0][4][0][0], cA[0][4][0][1], cA[0][4][1][0], cA[0][4][1][1]})) : ); SB();
  asm volatile("buffer_load_dwordx4 v[216:219], %0, %1, %2 offen offset:0 sc1" :  : "v"(vB), "s"(rWS), "s"(67584u) : "memory"); SB();
  asm volatile("v_mfma_f32_16x16x32_bf16 %0, %1, a[96:99], %0" : "+v"(ac[3]) : "v"(((u32x4){cA[0][3][0][0], cA[0][3][0][1], cA[0][3][1][0], cA[0][3][1][1]})) : ); SB();
  asm volatile("ds_read_b128 a[144:147], %0 offset:2176" :  : "v"(bb) : "memory"); SB();
  asm volatile("v_mfma_f32_16x16x32_bf16 %0, %1, a[96:99], %0" : "+v"(ac[2]) : "v"(((u32x4){cA[0][2][0][0], cA[0][2][0][1], cA[0][2][1][0], cA[0][2][1][1]})) : ); SB();
  asm volatile("ds_read2_b32 %0, %1 offset0:32 offset1:33" : "=&v"(cA[1][3][0]) : "v"(aa[1]) : "memory"); SB();
  asm volatile("v_mfma_f32_16x16x32_bf16 %0, %1, a[96:99], %0" : "+v"(ac[1]) : "v"(((u32x4){cA[0][1][0][0], cA[0][1][0][1], cA[0][1][1][0], cA[0][1][1][1]})) : ); SB();
  asm volatile("ds_read2_b32 %0, %1 offset0:34 offset1:35" : "=&v"(cA[1][3][1]) : "v"(aa[1]) : "memory"); SB();
  asm volatile("v_mfma_f32_16x16x32_bf16 %0, %1, a[96:99], 0" : "=&v"(ac[0]) : "v"(((u32x4){cA[0][0][0][0], cA[0][0][0][1], cA[0][0][1][0], cA[0][0][1][1]})) : ); SB();
  asm volatile("buffer_load_dwordx4 v[220:223], %0, %1, %2 offen offset:1024 sc1" :  : "v"(vB), "s"(rWS), "s"(67584u) : "memory"); SB();
  asm volatile("v_mfma_f32_16x16x32_bf16 %0, %1, a[156:159], %0" : "+v"(ac[7]) : "v"(((u32x4){cA[1][0][0][0], cA[1][0][0][1], cA[1][0][1][0], cA[1][0][1][1]})) : ); SB();
  asm volatile("ds_read2_b32 %0, %1 offset0:160 offset1:161" : "=&v"(cA[1][4][0]) : "v"(aa[2]) : "memory"); SB();
  asm volatile("v_mfma_f32_16x16x32_bf16 %0, %1, a[152:155], %0" : "+v"(ac[6]) : "v"(((u32x4){cA[1][0][0][0], cA[1][0][0][1], cA[1][0][1][0], cA[1][0][1][1]})) : ); SB();
  asm volatile("ds_read2_b32 %0, %1 offset0:162 offset1:163" : "=&v"(cA[1][4][1]) : "v"(aa[2]) : "memory"); SB();
  asm volatile("s_waitcnt lgkmcnt(7)\n\tv_mfma_f32_16x16x32_bf16 %0, %1, a[128:131], %0" : "+v"(ac[0]) : "v"(((u32x4){cA[1][0][0][0], cA[1][0][0][1], cA[1][0][1][0], cA[1][0][1][1]})) : ); SB();
  asm volatile("ds_read2_b32 %0, %1 offset0:32 offset1:33" : "=&v"(cA[1][5][0]) : "v"(aa[2]) : "memory"); SB();
  asm volatile("v_mfma_f32_16x16x32_bf16 %0, %1, a[132:135], %0" : "+v"(ac[1]) : "v"(((u32x4){cA[1][0][0][0], cA[1][0][0][1], cA[1][0][1][0], cA[1][0][1][1]})) : ); SB();
  asm volatile("v_xor_b32 %0, 131072, %0" : "+v"(vB) :  : "memory"); SB();
  asm volatile("v_mfma_f32_16x16x32_bf16 %0, %1, a[152:155], %0" : "+v"(ac[7]) : "v"(((u32x4){cA[1][1][0][0], cA[1][1][0][1], cA[1][1][1][0], cA[1][1][1][1]})) : ); SB();
  asm volatile("ds_read2_b32 %0, %1 offset0:34 offset1:35" : "=&v"(cA[1][5][1]) : "v"(aa[2]) : "memory"); SB();
  asm volatile("v_mfma_f32_16x16x32_bf16 %0, %1, a[148:151], %0" : "+v"(ac[6]) : "v"(((u32x4){cA[1][1][0][0], cA[1][1][0][1], cA[1][1][1][0], cA[1][1][1][1]})) : ); SB();
  asm volatile("ds_read2_b32 %0, %1 offset0:160 offset1:161" : "=&v"(cA[1][6][0]) : "v"(aa[3]) : "memory"); SB();
  asm volatile("v_mfma_f32_16x16x32_bf16 %0, %1, a[136:139], %0" : "+v"(ac[2]) : "v"(((u32x4){cA[1][0][0][0], cA[1][0][0][1], cA[1][0][1][0], cA[1][0][1][1]})) : ); SB();
  asm volatile("ds_read2_b32 %0, %1 offset0:162 offset1:163" : "=&v"(cA[1][6][1]) : "v"(aa[3]) : "memory"); SB();
  asm volatile("v_mfma_f32_16x16x32_bf16 %0, %1, a[128:131], %0" : "+v"(ac[1]) : "v"(((u32x4){cA[1][1][0][0], cA[1][1][0][1], cA[1][1][1][0], cA[1][1][1][1]})) : ); SB();
  asm volatile("ds_write_b128 %0, %1 offset:0" :  : "v"(dd), "v"(ac[0]) : "memory"); SB();
  asm volatile("s_waitcnt lgkmcnt(7)\n\tv_mfma_f32_16x16x32_bf16 %0, %1, a[148:151], %0" : "+v"(ac[7]) : "v"(((u32x4){cA[1][2][0][0], cA[1][2][0][1], cA[1][2][1][0], cA[1][2][1][1]})) : ); SB();
  asm volatile("ds_read2_b32 %0, %1 offset0:32 offset1:33" : "=&v"(cA[1][7][0]) : "v"(aa[3]) : "memory"); SB();
  asm volatile("v_mfma_f32_16x16x32_bf16 %0, %1, a[140:143], %0" : "+v"(ac[3]) : "v"(((u32x4){cA[1][0][0][0], cA[1][0][0][1], cA[1][0][1][0], cA[1][0][1][1]})) : ); SB();
  asm volatile("ds_read2_b32 %0, %1 offset0:34 offset1:35" : "=&v"(cA[1][7][1]) : "v"(aa[3]) : "memory"); SB();
  asm volatile("v_mfma_f32_16x16x32_bf16 %0, %1, a[132:135], %0" : "+v"(ac[2]) : "v"(((u32x4){cA[1][1][0][0], cA[1][1][0][1], cA[1][1][1][0], cA[1][1][1][1]})) : ); SB();
  asm volatile("buffer_load_dwordx4 v[224:227], %0, %1, %2 offen offset:0 sc1" :  : "v"(vB), "s"(rWS), "s"(2048u) : "memory"); SB();
  asm volatile("v_mfma_f32_16x16x32_bf16 %0, %1, a[144:147], %0" : "+v"(ac[4]) : "v"(((u32x4){cA[1][0][0][0], cA[1][0][0][1], cA[1][0][1][0], cA[1][0][1][1]})) : ); SB();
  asm volatile("ds_write_b128 %0, %1 offset:1024" :  : "v"(dd), "v"(ac[1]) : "memory"); SB();
  asm volatile("v_mfma_f32_16x16x32_bf16 %0, %1, a[144:147], %0" : "+v"(ac[7]) : "v"(((u32x4){cA[1][3][0][0], cA[1][3][0][1], cA[1][3][1][0], cA[1][3][1][1]})) : ); SB();
  asm volatile("buffer_load_dwordx4 v[228:231], %0, %1, %2 offen offset:1024 sc1" :  : "v"(vB), "s"(rWS), "s"(2048u) : "memory"); SB();
  asm volatile("v_mfma_f32_16x16x32_bf16 %0, %1, a[136:139], %0" : "+v"(ac[3]) : "v"(((u32x4){cA[1][1][0][0], cA[1][1][0][1], cA[1][1][1][0], cA[1][1][1][1]})) : ); SB();
  asm volatile("buffer_load_dwordx4 v[232:235], %0, %1, %2 offen offset:0 sc1" :  : "v"(vB), "s"(rWS), "s"(67584u) : "memory"); SB();
  asm volatile("v_mfma_f32_16x16x32_bf16 %0, %1, a[128:131], %0" : "+v"(ac[2]) : "v"(((u32x4){cA[1][2][0][0], cA[1][2][0][1], cA[1][2][1][0], cA[1][2][1][1]})) : ); SB();
  asm volatile("ds_write_b128 %0, %1 offset:0" :  : "v"(ew[0]), "v"(a1[0][0]) : "memory"); SB();
  asm volatile("v_mfma_f32_16x16x32_bf16 %0, %1, a[140:143], %0" : "+v"(ac[4]) : "v"(((u32x4){cA[1][1][0][0], cA[1][1][0][1], cA[1][1][1][0], cA[1][1][1][1]})) : ); SB();
  asm volatile("buffer_load_dwordx4 v[236:239], %0, %1, %2 offen offset:1024 sc1" :  : "v"(vB), "s"(rWS), "s"(67584u) : "memory"); SB();
  asm volatile("v_mfma_f32_16x16x32_bf16 %0, %1, a[148:151], %0" : "+v"(ac[5]) : "v"(((u32x4){cA[1][0][0][0], cA[1][0][0][1], cA[1][0][1][0], cA[1][0][1][1]})) : ); SB();
  asm volatile("v_xor_b32 %0, 393216, %0" : "+v"(vB) :  : "memory"); SB();
  asm volatile("v_mfma_f32_16x16x32_bf16 %0, %1, a[132:135], %0" : "+v"(ac[3]) : "v"(((u32x4){cA[1][2][0][0], cA[1][2][0][1], cA[1][2][1][0], cA[1][2][1][1]})) : ); SB();
  asm volatile("ds_write_b128 %0, %1 offset:1024" :  : "v"(ew[0]), "v"(a1[0][1]) : "memory"); SB();
  asm volatile("v_mfma_f32_16x16x32_bf16 %0, %1, a[144:147], %0" : "+v"(ac[6]) : "v"(((u32x4){cA[1][2][0][0], cA[1][2][0][1], cA[1][2][1][0], cA[1][2][1][1]})) : ); SB();
  asm volatile("ds_write_b128 %0, %1 offset:2048" :  : "v"(dd), "v"(ac[2]) : "memory"); SB();
  asm volatile("v_mfma_f32_16x16x32_bf16 %0, %1, a[136:139], %0" : "+v"(ac[4]) : "v"(((u32x4){cA[1][2][0][0], cA[1][2][0][1], cA[1][2][1][0], cA[1][2][1][1]})) : ); SB();
  asm volatile("buffer_load_dwordx4 v[240:243], %0, %1, %2 offen offset:0 sc1" :  : "v"(vB), "s"(rWS), "s"(2048u) : "memory"); SB();
  asm volatile("v_mfma_f32_16x16x32_bf16 %0, %1, a[144:147], %0" : "+v"(ac[5]) : "v"(((u32x4){cA[1][1][0][0], cA[1][1][0][1], cA[1][1][1][0], cA[1][1][1][1]})) : ); SB();
  asm volatile("ds_write_b128 %0, %1 offset:2048" :  : "v"(ew[0]), "v"(a1[0][2]) : "memory"); SB();
  asm volatile("v_mfma_f32_16x16x32_bf16 %0, %1, a[128:131], %0" : "+v"(ac[3]) : "v"(((u32x4){cA[1][3][0][0], cA[1][3][0][1], cA[1][3][1][0], cA[1][3][1][1]})) : ); SB();
  asm volatile("buffer_load_dwordx4 v[244:247], %0, %1, %2 offen offset:1024 sc1" :  : "v"(vB), "s"(rWS), "s"(2048u) : "memory"); SB();
  asm volatile("v_mfma_f32_16x16x32_bf16 %0, %1, a[140:143], %0" : "+v"(ac[6]) : "v"(((u32x4){cA[1][3][0][0], cA[1][3][0][1], cA[1][3][1][0], cA[1][3][1][1]})) : ); SB();
  asm volatile("ds_write_b128 %0, %1 offset:0" :  : "v"(ew[1]), "v"(a1[1][0]) : "memory"); SB();
  asm volatile("v_mfma_f32_16x16x32_bf16 %0, %1, a[132:135], %0" : "+v"(ac[4]) : "v"(((u32x4){cA[1][3][0][0], cA[1][3][0][1], cA[1][3][1][0], cA[1][3][1][1]})) : ); SB();
  asm volatile("ds_write_b128 %0, %1 offset:1024" :  : "v"(ew[1]), "v"(a1[1][1]) : "memory"); SB();
  asm volatile("v_mfma_f32_16x16x32_bf16 %0, %1, a[140:143], %0" : "+v"(ac[5]) : "v"(((u32x4){cA[1][2][0][0], cA[1][2][0][1], cA[1][2][1][0], cA[1][2][1][1]})) : ); SB();
  asm volatile("buffer_load_dwordx4 v[248:251], %0, %1, %2 offen offset:0 sc1" :  : "v"(vB), "s"(rWS), "s"(67584u) : "memory"); SB();
  asm volatile("s_waitcnt lgkmcnt(3)\n\tv_mfma_f32_16x16x32_bf16 %0, %1, a[140:143], %0" : "+v"(ac[7]) : "v"(((u32x4){cA[1][4][0][0], cA[1][4][0][1], cA[1][4][1][0], cA[1][4][1][1]})) : ); SB();
  asm volatile("ds_write_b128 %0, %1 offset:3072" :  : "v"(dd), "v"(ac[3]) : "memory"); SB();
  asm volatile("v_mfma_f32_16x16x32_bf16 %0, %1, a[136:139], %0" : "+v"(ac[6]) : "v"(((u32x4){cA[1][4][0][0], cA[1][4][0][1], cA[1][4][1][0], cA[1][4][1][1]})) : ); SB();
  asm volatile("buffer_load_dwordx4 v[252:255], %0, %1, %2 offen offset:1024 sc1" :  : "v"(vB), "s"(rWS), "s"(67584u) : "memory"); SB();
  asm volatile("v_mfma_f32_16x16x32_bf16 %0, %1, a[128:131], %0" : "+v"(ac[4]) : "v"(((u32x4){cA[1][4][0][0], cA[1][4][0][1], cA[1][4][1][0], cA[1][4][1][1]})) : ); SB();
  asm volatile("v_xor_b32 %0, 131072, %0" : "+v"(vB) :  : "memory"); SB();
  asm volatile("v_mfma_f32_16x16x32_bf16 %0, %1, a[136:139], %0" : "+v"(ac[5]) : "v"(((u32x4){cA[1][3][0][0], cA[1][3][0][1], cA[1][3][1][0], cA[1][3][1][1]})) : ); SB();
  asm volatile("ds_write_b128 %0, %1 offset:2048" :  : "v"(ew[1]), "v"(a1[1][2]) : "memory"); SB();
  asm volatile("v_mfma_f32_16x16x32_bf16 %0, %1, a[136:139], %0" : "+v"(ac[7]) : "v"(((u32x4){cA[1][5][0][0], cA[1][5][0][1], cA[1][5][1][0], cA[1][5][1][1]})) : ); SB();
  asm volatile("buffer_load_dwordx4 %0, %1, %2 offen offset:2048 lds" :  : "v"(voS), "s"(rA), "s"(ao[6]) : "memory"); SB();
  asm volatile("v_mfma_f32_16x16x32_bf16 %0, %1, a[132:135], %0" : "+v"(ac[6]) : "v"(((u32x4){cA[1][5][0][0], cA[1][5][0][1], cA[1][5][1][0], cA[1][5][1][1]})) : ); SB();
  asm volatile("v_mfma_f32_16x16x32_bf16 %0, %1, a[132:135], %0" : "+v"(ac[5]) : "v"(((u32x4){cA[1][4][0][0], cA[1][4][0][1], cA[1][4][1][0], cA[1][4][1][1]})) : ); SB();
  asm volatile("ds_write_b128 %0, %1 offset:4096" :  : "v"(dd), "v"(ac[4]) : "memory"); SB();
  asm volatile("v_mfma_f32_16x16x32_bf16 %0, %1, a[132:135], %0" : "+v"(ac[7]) : "v"(((u32x4){cA[1][6][0][0], cA[1][6][0][1], cA[1][6][1][0], cA[1][6][1][1]})) : ); SB();
  asm volatile("buffer_load_dwordx4 %0, %1, %2 offen offset:3072 lds" :  : "v"(voS), "s"(rA), "s"(ao[7]) : "memory"); SB();
  asm volatile("v_mfma_f32_16x16x32_bf16 %0, %1, a[128:131], %0" : "+v"(ac[6]) : "v"(((u32x4){cA[1][6][0][0], cA[1][6][0][1], cA[1][6][1][0], cA[1][6][1][1]})) : ); SB();
  asm volatile("v_xor_b32 %0, 128, %0" : "+v"(voS) :  : "memory"); SB();
  asm volatile("v_mfma_f32_16x16x32_bf16 %0, %1, a[128:131], %0" : "+v"(ac[5]) : "v"(((u32x4){cA[1][5][0][0], cA[1][5][0][1], cA[1][5][1][0], cA[1][5][1][1]})) : ); SB();
  asm volatile("s_add_u32 m0, %0, 98304" :  : "s"(mA) : "memory", "scc"); SB();
  asm volatile("v_mfma_f32_16x16x32_bf16 %0, %1, a[128:131], %0" : "+v"(ac[7]) : "v"(((u32x4){cA[1][7][0][0], cA[1][7][0][1], cA[1][7][1][0], cA[1][7][1][1]})) : ); SB();
  asm volatile("buffer_load_dwordx4 %0, %1, %2 offen lds" :  : "v"(voS), "s"(rA), "s"(ao[0]) : "memory"); SB();
  asm volatile("s_nop 5" : "+v"(ac[5]), "+v"(ac[6]), "+v"(ac[7]) :  : ); SB();
  asm volatile("ds_write_b128 %0, %1 offset:6144" :  : "v"(dd), "v"(ac[6]) : "memory"); SB();
  asm volatile("ds_write_b128 %0, %1 offset:5120" :  : "v"(dd), "v"(ac[5]) : "memory"); SB();
  asm volatile("ds_write_b128 %0, %1 offset:7168" :  : "v"(dd), "v"(ac[7]) : "memory"); SB();
  } else {
  asm volatile("; ELSE_BEGIN"); SB();
  asm volatile("s_waitcnt lgkmcnt(0)" ::: "memory"); SB();
  asm volatile("buffer_load_dwordx4 v[184:187], %0, %1, %2 offen offset:0 sc1" :  : "v"(vB), "s"(rWS), "s"(65536u) : "memory"); SB();
  asm volatile("s_waitcnt lgkmcnt(0)" ::: "memory"); SB();
  asm volatile("buffer_load_dwordx4 v[188:191], %0, %1, %2 offen offset:1024 sc1" :  : "v"(vB), "s"(rWS), "s"(65536u) : "memory"); SB();
  asm volatile("s_waitcnt lgkmcnt(0)" ::: "memory"); SB();
  asm volatile("v_xor_b32 %0, 393216, %0" : "+v"(vB) :  : "memory"); SB();
  asm volatile("s_waitcnt lgkmcnt(0)" ::: "memory"); SB();
  asm volatile("buffer_load_dwordx4 v[192:195], %0, %1, %2 offen offset:0 sc1" :  : "v"(vB), "s"(rWS), "s"(0u) : "memory"); SB();
  asm volatile("s_waitcnt lgkmcnt(0)" ::: "memory"); SB();
  asm volatile("buffer_load_dwordx4 v[196:199], %0, %1, %2 offen offset:1024 sc1" :  : "v"(vB), "s"(rWS), "s"(0u) : "memory"); SB();
  asm volatile("s_waitcnt lgkmcnt(0)" ::: "memory"); SB();
  asm volatile("buffer_load_dwordx4 v[200:203], %0, %1, %2 offen offset:0 sc1" :  : "v"(vB), "s"(rWS), "s"(65536u) : "memory"); SB();
  asm volatile("s_waitcnt lgkmcnt(0)" ::: "memory"); SB();
  asm volatile("buffer_load_dwordx4 v[204:207], %0, %1, %2 offen offset:1024 sc1" :  : "v"(vB), "s"(rWS), "s"(65536u) : "memory"); SB();
  asm volatile("s_waitcnt lgkmcnt(0)" ::: "memory"); SB();
  asm volatile("v_xor_b32 %0, 262144, %0" : "+v"(vB) :  : "memory"); SB();
  asm volatile("s_waitcnt lgkmcnt(0)" ::: "memory"); SB();
  asm volatile("buffer_load_dwordx4 v[208:211], %0, %1, %2 offen offset:0 sc1" :  : "v"(vB), "s"(rWS), "s"(2048u) : "memory"); SB();
  asm volatile("s_waitcnt lgkmcnt(0)" ::: "memory"); SB();
  asm volatile("buffer_load_dwordx4 v[212:215], %0, %1, %2 offen offset:1024 sc1" :  : "v"(vB), "s"(rWS), "s"(2048u) : "memory"); SB();
  asm volatile("s_waitcnt lgkmcnt(0)" ::: "memory"); SB();
  asm volatile("buffer_load_dwordx4 v[216:219], %0, %1, %2 offen offset:0 sc1" :  : "v"(vB), "s"(rWS), "s"(67584u) : "memory"); SB();
  asm volatile("s_waitcnt lgkmcnt(0)" ::: "memory"); SB();
  asm volatile("buffer_load_dwordx4 v[220:223], %0, %1, %2 offen offset:1024 sc1" :  : "v"(vB), "s"(rWS), "s"(67584u) : "memory"); SB();
  asm volatile("s_waitcnt lgkmcnt(0)" ::: "memory"); SB();
  asm volatile("v_xor_b32 %0, 131072, %0" : "+v"(vB) :  : "memory"); SB();
  asm volatile("s_waitcnt lgkmcnt(0)" ::: "memory"); SB();
  asm volatile("buffer_load_dwordx4 v[224:227], %0, %1, %2 offen offset:0 sc1" :  : "v"(vB), "s"(rWS), "s"(2048u) : "memory"); SB();
  asm volatile("s_waitcnt lgkmcnt(0)" ::: "memory"); SB();
  asm volatile("buffer_load_dwordx4 v[228:231], %0, %1, %2 offen offset:1024 sc1" :  : "v"(vB), "s"(rWS), "s"(2048u) : "memory"); SB();
  asm volatile("s_waitcnt lgkmcnt(0)" ::: "memory"); SB();
  asm volatile("buffer_load_dwordx4 v[232:235], %0, %1, %2 offen offset:0 sc1" :  : "v"(vB), "s"(rWS), "s"(67584u) : "memory"); SB();
  asm volatile("s_waitcnt lgkmcnt(0)" ::: "memory"); SB();
  asm volatile("ds_write_b128 %0, %1 offset:0" :  : "v"(ew[0]), "v"(a1[0][0]) : "memory"); SB();
  asm volatile("s_waitcnt lgkmcnt(0)" ::: "memory"); SB();
  asm volatile("buffer_load_dwordx4 v[236:239], %0, %1, %2 offen offset:1024 sc1" :  : "v"(vB), "s"(rWS), "s"(67584u) : "memory"); SB();
  asm volatile("s_waitcnt lgkmcnt(0)" ::: "memory"); SB();
  asm volatile("v_xor_b32 %0, 393216, %0" : "+v"(vB) :  : "memory"); SB();
  asm volatile("s_waitcnt lgkmcnt(0)" ::: "memory"); SB();
  asm volatile("ds_write_b128 %0, %1 offset:1024" :  : "v"(ew[0]), "v"(a1[0][1]) : "memory"); SB();
  asm volatile("s_waitcnt lgkmcnt(0)" ::: "memory"); SB();
  asm volatile("buffer_load_dwordx4 v[240:243], %0, %1, %2 offen offset:0 sc1" :  : "v"(vB), "s"(rWS), "s"(2048u) : "memory"); SB();
  asm volatile("s_waitcnt lgkmcnt(0)" ::: "memory"); SB();
  asm volatile("ds_write_b128 %0, %1 offset:2048" :  : "v"(ew[0]), "v"(a1[0][2]) : "memory"); SB();
  asm volatile("s_waitcnt lgkmcnt(0)" ::: "memory"); SB();
  asm volatile("buffer_load_dwordx4 v[244:247], %0, %1, %2 offen offset:1024 sc1" :  : "v"(vB), "s"(rWS), "s"(2048u) : "memory"); SB();
  asm volatile("s_waitcnt lgkmcnt(0)" ::: "memory"); SB();
  asm volatile("ds_write_b128 %0, %1 offset:0" :  : "v"(ew[1]), "v"(a1[1][0]) : "memory"); SB();
  asm volatile("s_waitcnt lgkmcnt(0)" ::: "memory"); SB();
  asm volatile("ds_write_b128 %0, %1 offset:1024" :  : "v"(ew[1]), "v"(a1[1][1]) : "memory"); SB();
  asm volatile("s_waitcnt lgkmcnt(0)" ::: "memory"); SB();
  asm volatile("buffer_load_dwordx4 v[248:251], %0, %1, %2 offen offset:0 sc1" :  : "v"(vB), "s"(rWS), "s"(67584u) : "memory"); SB();
  asm volatile("s_waitcnt lgkmcnt(0)" ::: "memory"); SB();
  asm volatile("buffer_load_dwordx4 v[252:255], %0, %1, %2 offen offset:1024 sc1" :  : "v"(vB), "s"(rWS), "s"(67584u) : "memory"); SB();
  asm volatile("s_waitcnt lgkmcnt(0)" ::: "memory"); SB();
  asm volatile("v_xor_b32 %0, 131072, %0" : "+v"(vB) :  : "memory"); SB();
  asm volatile("s_waitcnt lgkmcnt(0)" ::: "memory"); SB();
  asm volatile("ds_write_b128 %0, %1 offset:2048" :  : "v"(ew[1]), "v"(a1[1][2]) : "memory"); SB();
  asm volatile("s_waitcnt lgkmcnt(0)" ::: "memory"); SB();
  asm volatile("s_waitcnt lgkmcnt(0)" ::: "memory"); SB();
  asm volatile("buffer_load_dwordx4 %0, %1, %2 offen offset:2048 lds" :  : "v"(voS), "s"(rA), "s"(ao[6]) : "memory"); SB();
  asm volatile("s_waitcnt lgkmcnt(0)" ::: "memory"); SB();
  asm volatile("s_waitcnt lgkmcnt(0)" ::: "memory"); SB();
  asm volatile("buffer_load_dwordx4 %0, %1, %2 offen offset:3072 lds" :  : "v"(voS), "s"(rA), "s"(ao[7]) : "memory"); SB();
  asm volatile("s_waitcnt lgkmcnt(0)" ::: "memory"); SB();
  asm volatile("s_waitcnt lgkmcnt(0)" ::: "memory"); SB();
  asm volatile("v_xor_b32 %0, 128, %0" : "+v"(voS) :  : "memory"); SB();
  asm volatile("s_waitcnt lgkmcnt(0)" ::: "memory"); SB();
  asm volatile("s_add_u32 m0, %0, 98304" :  : "s"(mA) : "memory", "scc"); SB();
  asm volatile("s_waitcnt lgkmcnt(0)" ::: "memory"); SB();
  asm volatile("buffer_load_dwordx4 %0, %1, %2 offen lds" :  : "v"(voS), "s"(rA), "s"(ao[0]) : "memory"); SB();
  asm volatile("s_waitcnt lgkmcnt(0) ; ELSE_END" ::: "memory"); SB();
  }
  ST(15);
  f32x4 g[2][4][2], pr[6], yq[8], bs4 = {bs, bs, bs, bs};
  u32x4 ho[3], ykv, y2;
  float gt[4], ge[4];
  unsigned vH_, pw2, pe[3], vX, vO, vO2, vP, vFH, vFY, vHr, hso[8], ys2, ydr, ykr, er, sFH, sFY;
  int dl;
  asm volatile("" : "+v"(bs4)); SB();
  asm volatile("s_waitcnt vmcnt(25)" :  :  : "memory"); SB();
  asm volatile("v_mfma_f32_16x16x32_bf16 %0, v[160:163], a[160:163], %1" : "=&v"(g[0][0][0]) : "v"(bs4) : ); SB();
  { unsigned lane = lane0, w = w0, rb = rb0, cb = cb0;
  asm volatile("" : "+v"(lane)); asm volatile("" : "+s"(w), "+s"(rb), "+s"(cb)); SB();

  asm volatile("v_mfma_f32_16x16x32_bf16 %0, v[160:163], a[164:167], 0" : "=&v"(g[0][1][0]) :  : ); SB();
  er = lb + 32768 + w * 6144 + l16; asm volatile("" : "+v"(er)); SB();
  asm volatile("v_mfma_f32_16x16x32_bf16 %0, v[160:163], a[168:171], 0" : "=&v"(g[0][2][0]) :  : ); SB();
  asm volatile("s_waitcnt lgkmcnt(0)\n\ts_barrier" :  :  : "memory"); SB();
  asm volatile("ds_read_b128 %0, %1 offset:0" : "=&v"(a1[0][0]) : "v"(er) : "memory"); SB();
  asm volatile("v_mfma_f32_16x16x32_bf16 %0, v[160:163], a[172:175], 0" : "=&v"(g[0][3][0]) :  : ); SB();
  asm volatile("ds_read_b128 %0, %1 offset:1024" : "=&v"(a1[0][1]) : "v"(er) : "memory"); SB();
  asm volatile("v_mfma_f32_16x16x32_bf16 %0, v[164:167], a[160:163], %1" : "=&v"(g[0][0][1]) : "v"(bs4) : ); SB();
  asm volatile("ds_read_b128 %0, %1 offset:2048" : "=&v"(a1[0][2]) : "v"(er) : "memory"); SB();
  asm volatile("v_mfma_f32_16x16x32_bf16 %0, v[164:167], a[164:167], 0" : "=&v"(g[0][1][1]) :  : ); SB();
  asm volatile("ds_read_b128 %0, %1 offset:3072" : "=&v"(a1[1][0]) : "v"(er) : "memory"); SB();
  asm volatile("v_mfma_f32_16x16x32_bf16 %0, v[164:167], a[168:171], 0" : "=&v"(g[0][2][1]) :  : ); SB();
  asm volatile("ds_read_b128 a[96:99], %0 offset:32768" :  : "v"(aA[0]) : "memory"); SB();
  asm volatile("v_mfma_f32_16x16x32_bf16 %0, v[164:167], a[172:175], 0" : "=&v"(g[0][3][1]) :  : ); SB();
  asm volatile("ds_read_b128 a[112:115], %0 offset:32768" :  : "v"(aA[1]) : "memory"); SB();
  ST(25);
  asm volatile("s_waitcnt vmcnt(23)" :  :  : "memory"); SB();
  asm volatile("v_mfma_f32_16x16x32_bf16 %0, v[168:171], a[176:179], %0" : "+v"(g[0][0][0]) :  : ); SB();
  asm volatile("ds_read_b128 a[100:103], %0 offset:34816" :  : "v"(aA[0]) : "memory"); SB();
  asm volatile("v_mfma_f32_16x16x32_bf16 %0, v[168:171], a[180:183], %0" : "+v"(g[0][1][0]) :  : ); SB();
  asm volatile("ds_read_b128 a[116:119], %0 offset:34816" :  : "v"(aA[1]) : "memory"); SB();
  asm volatile("v_mfma_f32_16x16x32_bf16 %0, v[168:171], a[184:187], %0" : "+v"(g[0][2][0]) :  : ); SB();
  asm volatile("ds_read_b128 a[104:107], %0 offset:36864" :  : "v"(aA[0]) : "memory"); SB();
  asm volatile("v_mfma_f32_16x16x32_bf16 %0, v[168:171], a[188:191], %0" : "+v"(g[0][3][0]) :  : ); SB();
  asm volatile("ds_read_b128 a[120:123], %0 offset:36864" :  : "v"(aA[1]) : "memory"); SB();
  asm volatile("v_mfma_f32_16x16x32_bf16 %0, v[172:175], a[176:179], %0" : "+v"(g[0][0][1]) :  : ); SB();
  asm volatile("buffer_load_dwordx4 v[160:163], %0, %1, %2 offen offset:0 sc1" :  : "v"(vB), "s"(rWS), "s"(0u) : "memory"); SB();
  asm volatile("v_mfma_f32_16x16x32_bf16 %0, v[172:175], a[180:183], %0" : "+v"(g[0][1][1]) :  : ); SB();
  asm volatile("buffer_load_dwordx4 v[164:167], %0, %1, %2 offen offset:1024 sc1" :  : "v"(vB), "s"(rWS), "s"(0u) : "memory"); SB();
  asm volatile("v_mfma_f32_16x16x32_bf16 %0, v[172:175], a[184:187], %0" : "+v"(g[0][2][1]) :  : ); SB();
  asm volatile("ds_read_b128 %0, %1 offset:4096" : "=&v"(a1[1][1]) : "v"(er) : "memory"); SB();
  asm volatile("v_mfma_f32_16x16x32_bf16 %0, v[172:175], a[188:191], %0" : "+v"(g[0][3][1]) :  : ); SB();
  asm volatile("ds_read_b128 %0, %1 offset:5120" : "=&v"(a1[1][2]) : "v"(er) : "memory"); SB();
  asm volatile("s_waitcnt vmcnt(23)" :  :  : "memory"); SB();
  asm volatile("v_mfma_f32_16x16x32_bf16 %0, v[176:179], a[192:195], %0" : "+v"(g[0][0][0]) :  : ); SB();
  asm volatile("buffer_load_dwordx4 v[168:171], %0, %1, %2 offen offset:0 sc1" :  : "v"(vB), "s"(rWS), "s"(65536u) : "memory"); SB();
  asm volatile("v_mfma_f32_16x16x32_bf16 %0, v[176:179], a[196:199], %0" : "+v"(g[0][1][0]) :  : ); SB();
  ydr = lb + 0 + (w * 2 + (lane >> 5)) * 1024 + (lane & 1) * 512; asm volatile("" : "+v"(ydr)); SB();
  asm volatile("v_mfma_f32_16x16x32_bf16 %0, v[176:179], a[200:203], %0" : "+v"(g[0][2][0]) :  : ); SB();
  ydr = ydr + ((((lane >> 1) & 15) ^ ((lane & 1) << 3)) << 4); asm volatile("" : "+v"(ydr)); SB();
  asm volatile("v_mfma_f32_16x16x32_bf16 %0, v[176:179], a[204:207], %0" : "+v"(g[0][3][0]) :  : ); SB();
  ykr = lb + 59520 + 512 + w * 1024 + l16; asm volatile("" : "+v"(ykr)); SB();
  asm volatile("v_mfma_f32_16x16x32_bf16 %0, v[180:183], a[192:195], %0" : "+v"(g[0][0][1]) :  : ); SB();
  asm volatile("buffer_load_dwordx4 v[172:175], %0, %1, %2 offen offset:1024 sc1" :  : "v"(vB), "s"(rWS), "s"(65536u) : "memory"); SB();
  pw2 = er + (122880 - 32768); asm volatile("" : "+v"(pw2)); SB();
  asm volatile("v_mfma_f32_16x16x32_bf16 %0, v[180:183], a[196:199], %0" : "+v"(g[0][1][1]) :  : ); SB();
  asm volatile("v_xor_b32 %0, 917504, %0" : "+v"(vB) :  : "memory"); SB();
  asm volatile("v_mfma_f32_16x16x32_bf16 %0, v[180:183], a[200:203], %0" : "+v"(g[0][2][1]) :  : ); SB();
  pe[0] = lb + 122880 + (w ^ 1) * 6144 + l16; asm volatile("" : "+v"(pe[0])); SB();
  asm volatile("v_mfma_f32_16x16x32_bf16 %0, v[180:183], a[204:207], %0" : "+v"(g[0][3][1]) :  : ); SB();
  asm volatile("buffer_load_dwordx4 %0, %1, %2 offen offset:1024 lds" :  : "v"(voS), "s"(rA), "s"(ao[1]) : "memory"); SB();
  asm volatile("s_waitcnt vmcnt(24)" :  :  : "memory"); SB();
  asm volatile("v_mfma_f32_16x16x32_bf16 %0, v[184:187], a[208:211], %0" : "+v"(g[0][0][0]) :  : ); SB();
  pe[1] = lb + 122880 + (w ^ 2) * 6144 + l16; asm volatile("" : "+v"(pe[1])); SB();
  asm volatile("v_mfma_f32_16x16x32_bf16 %0, v[184:187], a[212:215], %0" : "+v"(g[0][1][0]) :  : ); SB();
  pe[2] = lb + 122880 + (w ^ 3) * 6144 + l16; asm volatile("" : "+v"(pe[2])); SB();
  asm volatile("v_mfma_f32_16x16x32_bf16 %0, v[184:187], a[216:219], %0" : "+v"(g[0][2][0]) :  : ); SB();
  asm volatile("s_waitcnt lgkmcnt(0)" : "+v"(a1[0][0]), "+v"(a1[0][1]), "+v"(a1[0][2]), "+v"(a1[1][0]), "+v"(a1[1][1]), "+v"(a1[1][2]) :  : "memory"); SB();
  asm volatile("v_mul_f32 %0, %1, %1" : "=v"(gt[0]) : "v"(a1[0][0][0]) : ); SB();
  asm volatile("v_mfma_f32_16x16x32_bf16 %0, v[184:187], a[220:223], %0" : "+v"(g[0][3][0]) :  : ); SB();
  asm volatile("buffer_load_dwordx4 v[176:179], %0, %1, %2 offen offset:0 sc1" :  : "v"(vB), "s"(rWS), "s"(0u) : "memory"); SB();
  asm volatile("v_mul_f32 %0, %1, %1" : "=v"(gt[1]) : "v"(a1[0][0][1]) : ); SB();
  asm volatile("v_mfma_f32_16x16x32_bf16 %0, v[188:191], a[208:211], %0" : "+v"(g[0][0][1]) :  : ); SB();
  dl = (int)(wm * 12928 - wn * 256 + w * 128) - 2 * 12928; asm volatile("" : "+s"(dl)); SB();
  asm volatile("v_mfma_f32_16x16x32_bf16 %0, v[188:191], a[212:215], %0" : "+v"(g[0][1][1]) :  : ); SB();
  asm volatile("v_add_u32 %0, %1, %0" : "+v"(aa[0]) : "s"(dl) : ); SB();
  asm volatile("v_mfma_f32_16x16x32_bf16 %0, v[188:191], a[216:219], %0" : "+v"(g[0][2][1]) :  : ); SB();
  asm volatile("buffer_load_dwordx4 v[180:183], %0, %1, %2 offen offset:1024 sc1" :  : "v"(vB), "s"(rWS), "s"(0u) : "memory"); SB();
  asm volatile("v_add_u32 %0, %1, %0" : "+v"(aa[1]) : "s"(dl) : ); SB();
  asm volatile("v_mfma_f32_16x16x32_bf16 %0, v[188:191], a[220:223], %0" : "+v"(g[0][3][1]) :  : ); SB();
  asm volatile("v_add_u32 %0, %1, %0" : "+v"(aa[2]) : "s"(dl) : ); SB();
  asm volatile("s_waitcnt vmcnt(24)" :  :  : "memory"); SB();
  asm volatile("v_mfma_f32_16x16x32_bf16 %0, v[192:195], a[224:227], %0" : "+v"(g[0][0][0]) :  : ); SB();
  asm volatile("v_add_u32 %0, %1, %0" : "+v"(aa[3]) : "s"(dl) : ); SB();
  asm volatile("v_mfma_f32_16x16x32_bf16 %0, v[192:195], a[228:231], %0" : "+v"(g[0][1][0]) :  : ); SB();
  asm volatile("v_add_u32 %0, %1, %0" : "+v"(bb) : "s"(dl) : ); SB();
  asm volatile("v_mfma_f32_16x16x32_bf16 %0, v[192:195], a[232:235], %0" : "+v"(g[0][2][0]) :  : ); SB();
  asm volatile("buffer_load_dwordx4 v[184:187], %0, %1, %2 offen offset:0 sc1" :  : "v"(vB), "s"(rWS), "s"(65536u) : "memory"); SB();
  asm volatile("v_mul_f32 %0, %1, %1" : "=v"(gt[2]) : "v"(a1[0][0][2]) : ); SB();
  asm volatile("v_mfma_f32_16x16x32_bf16 %0, v[192:195], a[236:239], %0" : "+v"(g[0][3][0]) :  : ); SB();
  asm volatile("v_mul_f32 %0, %1, %1" : "=v"(gt[3]) : "v"(a1[0][0][3]) : ); SB();
  asm volatile("v_mfma_f32_16x16x32_bf16 %0, v[196:199], a[224:227], %0" : "+v"(g[0][0][1]) :  : ); SB();
  asm volatile("v_fma_f32 %0, %0, %1, %2" : "+v"(gt[0]) : "v"(k2[0]), "v"(k1[0]) : ); SB();
  asm volatile("v_mfma_f32_16x16x32_bf16 %0, v[196:199], a[228:231], %0" : "+v"(g[0][1][1]) :  : ); SB();
  asm volatile("buffer_load_dwordx4 v[188:191], %0, %1, %2 offen offset:1024 sc1" :  : "v"(vB), "s"(rWS), "s"(65536u) : "memory"); SB();
  asm volatile("v_fma_f32 %0, %0, %1, %2" : "+v"(gt[1]) : "v"(k2[0]), "v"(k1[0]) : ); SB();
  asm volatile("v_mfma_f32_16x16x32_bf16 %0, v[196:199], a[232:235], %0" : "+v"(g[0][2][1]) :  : ); SB();
  asm volatile("v_xor_b32 %0, 131072, %0" : "+v"(vB) :  : "memory"); SB();
  asm volatile("s_waitcnt vmcnt(10)" :  :  : "memory"); SB();
  asm volatile("v_mfma_f32_16x16x32_bf16 %0, v[196:199], a[236:239], %0" : "+v"(g[0][3][1]) :  : ); SB();
  asm volatile("ds_read_b128 a[108:111], %0 offset:38912" :  : "v"(aA[0]) : "memory"); SB();
  asm volatile("v_mfma_f32_16x16x32_bf16 %0, v[200:203], a[240:243], %0" : "+v"(g[0][0][0]) :  : ); SB();
  asm volatile("ds_read_b128 a[124:127], %0 offset:38912" :  : "v"(aA[1]) : "memory"); SB();
  asm volatile("v_fma_f32 %0, %0, %1, %2" : "+v"(gt[2]) : "v"(k2[0]), "v"(k1[0]) : ); SB();
  asm volatile("v_mfma_f32_16x16x32_bf16 %0, v[200:203], a[244:247], %0" : "+v"(g[0][1][0]) :  : ); SB();
  asm volatile("buffer_load_dwordx4 v[192:195], %0, %1, %2 offen offset:0 sc1" :  : "v"(vB), "s"(rWS), "s"(0u) : "memory"); SB();
  asm volatile("v_fma_f32 %0, %0, %1, %2" : "+v"(gt[3]) : "v"(k2[0]), "v"(k1[0]) : ); SB();
  asm volatile("v_mfma_f32_16x16x32_bf16 %0, v[200:203], a[248:251], %0" : "+v"(g[0][2][0]) :  : ); SB();
  asm volatile("v_mul_f32 %0, %0, %1" : "+v"(gt[0]) : "v"(a1[0][0][0]) : ); SB();
  asm volatile("v_mfma_f32_16x16x32_bf16 %0, v[200:203], a[252:255], %0" : "+v"(g[0][3][0]) :  : ); SB();
  asm volatile("v_mul_f32 %0, %0, %1" : "+v"(gt[1]) : "v"(a1[0][0][1]) : ); SB();
  asm volatile("v_mfma_f32_16x16x32_bf16 %0, v[204:207], a[240:243], %0" : "+v"(g[0][0][1]) :  : ); SB();
  asm volatile("buffer_load_dwordx4 v[196:199], %0, %1, %2 offen offset:1024 sc1" :  : "v"(vB), "s"(rWS), "s"(0u) : "memory"); SB();
  asm volatile("v_mul_f32 %0, %0, %1" : "+v"(gt[2]) : "v"(a1[0][0][2]) : ); SB();
  asm volatile("v_mfma_f32_16x16x32_bf16 %0, v[204:207], a[244:247], %0" : "+v"(g[0][1][1]) :  : ); SB();
  asm volatile("v_mul_f32 %0, %0, %1" : "+v"(gt[3]) : "v"(a1[0][0][3]) : ); SB();
  asm volatile("v_mfma_f32_16x16x32_bf16 %0, v[204:207], a[248:251], %0" : "+v"(g[0][2][1]) :  : ); SB();
  asm volatile("v_exp_f32 %0, %1" : "=v"(ge[0]) : "v"(gt[0]) : ); SB();
  asm volatile("v_mfma_f32_16x16x32_bf16 %0, v[204:207], a[252:255], %0" : "+v"(g[0][3][1]) :  : ); SB();
  asm volatile("v_exp_f32 %0, %1" : "=v"(ge[1]) : "v"(gt[1]) : ); SB();
  asm volatile("v_mfma_f32_16x16x32_bf16 %0, v[208:211], a[160:163], %1" : "=&v"(g[1][0][0]) : "v"(bs4) : ); SB();
  asm volatile("buffer_load_dwordx4 v[200:203], %0, %1, %2 offen offset:0 sc1" :  : "v"(vB), "s"(rWS), "s"(65536u) : "memory"); SB();
  asm volatile("v_exp_f32 %0, %1" : "=v"(ge[2]) : "v"(gt[2]) : ); SB();
  asm volatile("v_mfma_f32_16x16x32_bf16 %0, v[208:211], a[164:167], 0" : "=&v"(g[1][1][0]) :  : ); SB();
  asm volatile("v_exp_f32 %0, %1" : "=v"(ge[3]) : "v"(gt[3]) : ); SB();
  asm volatile("v_mfma_f32_16x16x32_bf16 %0, v[208:211], a[168:171], 0" : "=&v"(g[1][2][0]) :  : ); SB();
  asm volatile("v_fma_f32 %0, %0, %1, %1" : "+v"(ge[0]) : "v"(xd[0]) : ); SB();
  asm volatile("v_mfma_f32_16x16x32_bf16 %0, v[208:211], a[172:175], 0" : "=&v"(g[1][3][0]) :  : ); SB();
  asm volatile("buffer_load_dwordx4 v[204:207], %0, %1, %2 offen offset:1024 sc1" :  : "v"(vB), "s"(rWS), "s"(65536u) : "memory"); SB();
  asm volatile("v_fma_f32 %0, %0, %1, %1" : "+v"(ge[1]) : "v"(xd[0]) : ); SB();
  asm volatile("v_mfma_f32_16x16x32_bf16 %0, v[212:215], a[160:163], %1" : "=&v"(g[1][0][1]) : "v"(bs4) : ); SB();
  asm volatile("v_xor_b32 %0, 393216, %0" : "+v"(vB) :  : "memory"); SB();
  asm volatile("v_fma_f32 %0, %0, %1, %1" : "+v"(ge[2]) : "v"(xd[0]) : ); SB();
  asm volatile("v_mfma_f32_16x16x32_bf16 %0, v[212:215], a[164:167], 0" : "=&v"(g[1][1][1]) :  : ); SB();
  asm volatile("v_fma_f32 %0, %0, %1, %1" : "+v"(ge[3]) : "v"(xd[0]) : ); SB();
  asm volatile("v_mfma_f32_16x16x32_bf16 %0, v[212:215], a[168:171], 0" : "=&v"(g[1][2][1]) :  : ); SB();
  asm volatile("buffer_load_dwordx4 %0, %1, %2 offen offset:2048 lds" :  : "v"(voS), "s"(rA), "s"(ao[2]) : "memory"); SB();
  asm volatile("v_rcp_f32 %0, %1" : "=v"(gt[0]) : "v"(ge[0]) : ); SB();
  asm volatile("v_mfma_f32_16x16x32_bf16 %0, v[212:215], a[172:175], 0" : "=&v"(g[1][3][1]) :  : ); SB();
  asm volatile("v_rcp_f32 %0, %1" : "=v"(gt[1]) : "v"(ge[1]) : ); SB();
  asm volatile("v_mfma_f32_16x16x32_bf16 %0, v[216:219], a[176:179], %0" : "+v"(g[1][0][0]) :  : ); SB();
  asm volatile("v_rcp_f32 %0, %1" : "=v"(gt[2]) : "v"(ge[2]) : ); SB();
  asm volatile("v_mfma_f32_16x16x32_bf16 %0, v[216:219], a[180:183], %0" : "+v"(g[1][1][0]) :  : ); SB();
  asm volatile("v_rcp_f32 %0, %1" : "=v"(gt[3]) : "v"(ge[3]) : ); SB();
  asm volatile("v_mfma_f32_16x16x32_bf16 %0, v[216:219], a[184:187], %0" : "+v"(g[1][2][0]) :  : ); SB();
  asm volatile("v_mul_f32 %0, %0, %1" : "+v"(a1[0][0][0]) : "v"(gt[0]) : ); SB();
  asm volatile("v_mfma_f32_16x16x32_bf16 %0, v[216:219], a[188:191], %0" : "+v"(g[1][3][0]) :  : ); SB();
  asm volatile("v_mul_f32 %0, %0, %1" : "+v"(a1[0][0][1]) : "v"(gt[1]) : ); SB();
  asm volatile("v_mfma_f32_16x16x32_bf16 %0, v[220:223], a[176:179], %0" : "+v"(g[1][0][1]) :  : ); SB();
  asm volatile("v_mul_f32 %0, %0, %1" : "+v"(a1[0][0][2]) : "v"(gt[2]) : ); SB();
  asm volatile("v_mfma_f32_16x16x32_bf16 %0, v[220:223], a[180:183], %0" : "+v"(g[1][1][1]) :  : ); SB();
  asm volatile("buffer_load_dwordx4 v[208:211], %0, %1, %2 offen offset:0 sc1" :  : "v"(vB), "s"(rWS), "s"(0u) : "memory"); SB();
  asm volatile("v_mul_f32 %0, %0, %1" : "+v"(a1[0][0][3]) : "v"(gt[3]) : ); SB();
  asm volatile("v_mfma_f32_16x16x32_bf16 %0, v[220:223], a[184:187], %0" : "+v"(g[1][2][1]) :  : ); SB();
  asm volatile("v_mul_f32 %0, %1, %1" : "=v"(gt[0]) : "v"(a1[0][1][0]) : ); SB();
  asm volatile("v_mfma_f32_16x16x32_bf16 %0, v[220:223], a[188:191], %0" : "+v"(g[1][3][1]) :  : ); SB();
  asm volatile("v_mul_f32 %0, %1, %1" : "=v"(gt[1]) : "v"(a1[0][1][1]) : ); SB();
  ST(26);
  asm volatile("v_mfma_f32_16x16x32_bf16 %0, v[224:227], a[192:195], %0" : "+v"(g[1][0][0]) :  : ); SB();
  vH_ = vH; asm volatile("" : "+v"(vH_)); SB();
  asm volatile("v_mfma_f32_16x16x32_bf16 %0, v[224:227], a[196:199], %0" : "+v"(g[1][1][0]) :  : ); SB();
  vO = W_WO + (cb * 120 + w * 30 + 0u * 15) * 1024 + l16; asm volatile("" : "+v"(vO)); SB();
  asm volatile("v_mfma_f32_16x16x32_bf16 %0, v[224:227], a[200:203], %0" : "+v"(g[1][2][0]) :  : ); SB();
  vO2 = vO + 15360 - 0u * 30720; asm volatile("" : "+v"(vO2)); SB();
  asm volatile("v_mfma_f32_16x16x32_bf16 %0, v[224:227], a[204:207], %0" : "+v"(g[1][3][0]) :  : ); SB();
  vFH = W_FH + rb * 256 + w * 64 + (lane & 7) * 8; asm volatile("" : "+v"(vFH)); SB();
  asm volatile("v_mfma_f32_16x16x32_bf16 %0, v[228:231], a[192:195], %0" : "+v"(g[1][0][1]) :  : ); SB();
  vHr = W_HH + (rb * 96 + w * 6) * 1024 + l16; asm volatile("" : "+v"(vHr)); SB();
  asm volatile("v_mfma_f32_16x16x32_bf16 %0, v[228:231], a[196:199], %0" : "+v"(g[1][1][1]) :  : ); SB();
  vX = (rb * 64 + w * 16 + l15) * 1280; asm volatile("" : "+v"(vX)); SB();
  asm volatile("v_mfma_f32_16x16x32_bf16 %0, v[228:231], a[200:203], %0" : "+v"(g[1][2][1]) :  : ); SB();
  vX = vX + cb * 160 + 8 * g4; asm volatile("" : "+v"(vX)); SB();
  asm volatile("v_mfma_f32_16x16x32_bf16 %0, v[228:231], a[204:207], %0" : "+v"(g[1][3][1]) :  : ); SB();
  sFH = W_FH + ((rb * 8 + cb) * 4 + w) * 8; asm volatile("" : "+s"(sFH)); SB();
  asm volatile("v_mfma_f32_16x16x32_bf16 %0, v[232:235], a[208:211], %0" : "+v"(g[1][0][0]) :  : ); SB();
  asm volatile("buffer_load_dwordx4 v[212:215], %0, %1, %2 offen offset:1024 sc1" :  : "v"(vB), "s"(rWS), "s"(0u) : "memory"); SB();
  asm volatile("v_mov_b32 %0, -1" : "=v"(ho[2][0]) :  : ); SB();
  asm volatile("v_mfma_f32_16x16x32_bf16 %0, v[232:235], a[212:215], %0" : "+v"(g[1][1][0]) :  : ); SB();
  asm volatile("buffer_load_dwordx4 %0, %1, %2 offen offset:3072 lds" :  : "v"(voS), "s"(rA), "s"(ao[3]) : "memory"); SB();
  asm volatile("v_mov_b32 %0, -1" : "=v"(ho[2][1]) :  : ); SB();
  asm volatile("v_mfma_f32_16x16x32_bf16 %0, v[232:235], a[216:219], %0" : "+v"(g[1][2][0]) :  : ); SB();
  asm volatile("v_mov_b32 %0, -1" : "=v"(ho[2][2]) :  : ); SB();
  asm volatile("v_mfma_f32_16x16x32_bf16 %0, v[232:235], a[220:223], %0" : "+v"(g[1][3][0]) :  : ); SB();
  asm volatile("s_waitcnt vmcnt(12)" :  :  : "memory"); SB();
  asm volatile("ds_read_b128 a[128:131], %0 offset:0" :  : "v"(aA[0]) : "memory"); SB();
  asm volatile("v_mfma_f32_16x16x32_bf16 %0, v[236:239], a[208:211], %0" : "+v"(g[1][0][1]) :  : ); SB();
  asm volatile("ds_read_b128 a[144:147], %0 offset:0" :  : "v"(aA[1]) : "memory"); SB();
  asm volatile("s_add_u32 m0, %0, 102400" :  : "s"(mA) : "memory", "scc"); SB();
  asm volatile("v_mfma_f32_16x16x32_bf16 %0, v[236:239], a[212:215], %0" : "+v"(g[1][1][1]) :  : ); SB();
  asm volatile("buffer_load_dwordx4 %0, %1, %2 offen lds" :  : "v"(voS), "s"(rA), "s"(ao[4]) : "memory"); SB();
  asm volatile("v_mov_b32 %0, -1" : "=v"(ho[2][3]) :  : ); SB();
  asm volatile("v_mfma_f32_16x16x32_bf16 %0, v[236:239], a[216:219], %0" : "+v"(g[1][2][1]) :  : ); SB();
  hso[0] = (w ^ 0) * 24576 + 0u * 3072; asm volatile("" : "+s"(hso[0])); SB();
  hso[1] = hso[0] ^ 3072; asm volatile("" : "+s"(hso[1])); SB();
  asm volatile("v_mfma_f32_16x16x32_bf16 %0, v[236:239], a[220:223], %0" : "+v"(g[1][3][1]) :  : ); SB();
  hso[2] = (w ^ 1) * 24576 + 0u * 3072; asm volatile("" : "+s"(hso[2])); SB();
  asm volatile("v_mfma_f32_16x16x32_bf16 %0, v[240:243], a[224:227], %0" : "+v"(g[1][0][0]) :  : ); SB();
  hso[3] = hso[2] ^ 3072; asm volatile("" : "+s"(hso[3])); SB();
  hso[4] = (w ^ 2) * 24576 + 0u * 3072; asm volatile("" : "+s"(hso[4])); SB();
  asm volatile("v_mfma_f32_16x16x32_bf16 %0, v[240:243], a[228:231], %0" : "+v"(g[1][1][0]) :  : ); SB();
  hso[5] = hso[4] ^ 3072; asm volatile("" : "+s"(hso[5])); SB();
  hso[6] = (w ^ 3) * 24576 + 0u * 3072; asm volatile("" : "+s"(hso[6])); SB();
  asm volatile("v_mfma_f32_16x16x32_bf16 %0, v[240:243], a[232:235], %0" : "+v"(g[1][2][0]) :  : ); SB();
  hso[7] = hso[6] ^ 3072; asm volatile("" : "+s"(hso[7])); SB();
  asm volatile("buffer_load_dwordx4 %0, %1, %2 offen offset:1024 lds" :  : "v"(voS), "s"(rA), "s"(ao[5]) : "memory"); SB();
  asm volatile("v_mfma_f32_16x16x32_bf16 %0, v[240:243], a[236:239], %0" : "+v"(g[1][3][0]) :  : ); SB();
  asm volatile("buffer_load_dwordx4 %0, %1, %2 offen offset:2048 lds" :  : "v"(voS), "s"(rA), "s"(ao[6]) : "memory"); SB();
  asm volatile("v_mul_f32 %0, %1, %1" : "=v"(gt[2]) : "v"(a1[0][1][2]) : ); SB();
  asm volatile("v_mfma_f32_16x16x32_bf16 %0, v[244:247], a[224:227], %0" : "+v"(g[1][0][1]) :  : ); SB();
  asm volatile("v_mul_f32 %0, %1, %1" : "=v"(gt[3]) : "v"(a1[0][1][3]) : ); SB();
  asm volatile("v_mfma_f32_16x16x32_bf16 %0, v[244:247], a[228:231], %0" : "+v"(g[1][1][1]) :  : ); SB();
  asm volatile("v_fma_f32 %0, %0, %1, %2" : "+v"(gt[0]) : "v"(k2[0]), "v"(k1[0]) : ); SB();
  asm volatile("v_mfma_f32_16x16x32_bf16 %0, v[244:247], a[232:235], %0" : "+v"(g[1][2][1]) :  : ); SB();
  asm volatile("buffer_load_dwordx4 %0, %1, %2 offen offset:3072 lds" :  : "v"(voS), "s"(rA), "s"(ao[7]) : "memory"); SB();
  asm volatile("v_fma_f32 %0, %0, %1, %2" : "+v"(gt[1]) : "v"(k2[0]), "v"(k1[0]) : ); SB();
  asm volatile("v_mfma_f32_16x16x32_bf16 %0, v[244:247], a[236:239], %0" : "+v"(g[1][3][1]) :  : ); SB();
  asm volatile("v_fma_f32 %0, %0, %1, %2" : "+v"(gt[2]) : "v"(k2[0]), "v"(k1[0]) : ); SB();
  asm volatile("v_mfma_f32_16x16x32_bf16 %0, v[248:251], a[240:243], %0" : "+v"(g[1][0][0]) :  : ); SB();
  asm volatile("v_fma_f32 %0, %0, %1, %2" : "+v"(gt[3]) : "v"(k2[0]), "v"(k1[0]) : ); SB();
  asm volatile("v_mfma_f32_16x16x32_bf16 %0, v[248:251], a[244:247], %0" : "+v"(g[1][1][0]) :  : ); SB();
  asm volatile("buffer_load_dwordx4 v[216:219], %0, %1, %2 offen offset:0 sc1" :  : "v"(vB), "s"(rWS), "s"(65536u) : "memory"); SB();
  asm volatile("v_mul_f32 %0, %0, %1" : "+v"(gt[0]) : "v"(a1[0][1][0]) : ); SB();
  asm volatile("v_mfma_f32_16x16x32_bf16 %0, v[248:251], a[248:251], %0" : "+v"(g[1][2][0]) :  : ); SB();
  asm volatile("v_mul_f32 %0, %0, %1" : "+v"(gt[1]) : "v"(a1[0][1][1]) : ); SB();
  asm volatile("v_mfma_f32_16x16x32_bf16 %0, v[248:251], a[252:255], %0" : "+v"(g[1][3][0]) :  : ); SB();
  asm volatile("v_mul_f32 %0, %0, %1" : "+v"(gt[2]) : "v"(a1[0][1][2]) : ); SB();
  asm volatile("v_mfma_f32_16x16x32_bf16 %0, v[252:255], a[240:243], %0" : "+v"(g[1][0][1]) :  : ); SB();
  asm volatile("v_mul_f32 %0, %0, %1" : "+v"(gt[3]) : "v"(a1[0][1][3]) : ); SB();
  asm volatile("v_mfma_f32_16x16x32_bf16 %0, v[252:255], a[244:247], %0" : "+v"(g[1][1][1]) :  : ); SB();
  asm volatile("buffer_load_dwordx4 v[220:223], %0, %1, %2 offen offset:1024 sc1" :  : "v"(vB), "s"(rWS), "s"(65536u) : "memory"); SB();
  asm volatile("v_exp_f32 %0, %1" : "=v"(ge[0]) : "v"(gt[0]) : ); SB();
  asm volatile("v_mfma_f32_16x16x32_bf16 %0, v[252:255], a[248:251], %0" : "+v"(g[1][2][1]) :  : ); SB();
  asm volatile("v_xor_b32 %0, 131072, %0" : "+v"(vB) :  : "memory"); SB();
  asm volatile("v_exp_f32 %0, %1" : "=v"(ge[1]) : "v"(gt[1]) : ); SB();
  asm volatile("v_mfma_f32_16x16x32_bf16 %0, v[252:255], a[252:255], %0" : "+v"(g[1][3][1]) :  : ); SB();
  asm volatile("v_exp_f32 %0, %1" : "=v"(ge[2]) : "v"(gt[2]) : ); SB();
  asm volatile("v_mfma_f32_16x16x32_bf16 %0, v[160:163], a[0:3], %0" : "+v"(g[0][0][0]) :  : ); SB();
  asm volatile("buffer_load_dwordx4 v[224:227], %0, %1, %2 offen offset:0 sc1" :  : "v"(vB), "s"(rWS), "s"(0u) : "memory"); SB();
  asm volatile("v_exp_f32 %0, %1" : "=v"(ge[3]) : "v"(gt[3]) : ); SB();
  asm volatile("v_mfma_f32_16x16x32_bf16 %0, v[160:163], a[4:7], %0" : "+v"(g[0][1][0]) :  : ); SB();
  asm volatile("v_fma_f32 %0, %0, %1, %1" : "+v"(ge[0]) : "v"(xd[0]) : ); SB();
  asm volatile("v_mfma_f32_16x16x32_bf16 %0, v[160:163], a[8:11], %0" : "+v"(g[0][2][0]) :  : ); SB();
  asm volatile("v_fma_f32 %0, %0, %1, %1" : "+v"(ge[1]) : "v"(xd[0]) : ); SB();
  asm volatile("v_mfma_f32_16x16x32_bf16 %0, v[160:163], a[12:15], %0" : "+v"(g[0][3][0]) :  : ); SB();
  asm volatile("buffer_load_dwordx4 v[228:231], %0, %1, %2 offen offset:1024 sc1" :  : "v"(vB), "s"(rWS), "s"(0u) : "memory"); SB();
  asm volatile("v_fma_f32 %0, %0, %1, %1" : "+v"(ge[2]) : "v"(xd[0]) : ); SB();
  asm volatile("v_mfma_f32_16x16x32_bf16 %0, v[164:167], a[0:3], %0" : "+v"(g[0][0][1]) :  : ); SB();
  asm volatile("v_fma_f32 %0, %0, %1, %1" : "+v"(ge[3]) : "v"(xd[0]) : ); SB();
  asm volatile("v_mfma_f32_16x16x32_bf16 %0, v[164:167], a[4:7], %0" : "+v"(g[0][1][1]) :  : ); SB();
  asm volatile("v_rcp_f32 %0, %1" : "=v"(gt[0]) : "v"(ge[0]) : ); SB();
  asm volatile("v_mfma_f32_16x16x32_bf16 %0, v[164:167], a[8:11], %0" : "+v"(g[0][2][1]) :  : ); SB();
  asm volatile("v_rcp_f32 %0, %1" : "=v"(gt[1]) : "v"(ge[1]) : ); SB();
  asm volatile("v_mfma_f32_16x16x32_bf16 %0, v[164:167], a[12:15], %0" : "+v"(g[0][3][1]) :  : ); SB();
  asm volatile("buffer_load_dwordx4 v[232:235], %0, %1, %2 offen offset:0 sc1" :  : "v"(vB), "s"(rWS), "s"(65536u) : "memory"); SB();
  asm volatile("v_mfma_f32_16x16x32_bf16 %0, v[168:171], a[16:19], %0" : "+v"(g[0][0][0]) :  : ); SB();
  asm volatile("v_rcp_f32 %0, %1" : "=v"(gt[2]) : "v"(ge[2]) : ); SB();
  asm volatile("v_mfma_f32_16x16x32_bf16 %0, v[168:171], a[20:23], %0" : "+v"(g[0][1][0]) :  : ); SB();
  asm volatile("v_rcp_f32 %0, %1" : "=v"(gt[3]) : "v"(ge[3]) : ); SB();
  asm volatile("v_mfma_f32_16x16x32_bf16 %0, v[168:171], a[24:27], %0" : "+v"(g[0][2][0]) :  : ); SB();
  asm volatile("buffer_load_dwordx4 v[236:239], %0, %1, %2 offen offset:1024 sc1" :  : "v"(vB), "s"(rWS), "s"(65536u) : "memory"); SB();
  asm volatile("v_mul_f32 %0, %0, %1" : "+v"(a1[0][1][0]) : "v"(gt[0]) : ); SB();
  asm volatile("v_mfma_f32_16x16x32_bf16 %0, v[168:171], a[28:31], %0" : "+v"(g[0][3][0]) :  : ); SB();
  asm volatile("v_xor_b32 %0, 524288, %0" : "+v"(vB) :  : "memory"); SB();
  asm volatile("v_mul_f32 %0, %0, %1" : "+v"(a1[0][1][1]) : "v"(gt[1]) : ); SB();
  asm volatile("v_mfma_f32_16x16x32_bf16 %0, v[172:175], a[16:19], %0" : "+v"(g[0][0][1]) :  : ); SB();
  asm volatile("v_mul_f32 %0, %0, %1" : "+v"(a1[0][1][2]) : "v"(gt[2]) : ); SB();
  asm volatile("v_mfma_f32_16x16x32_bf16 %0, v[172:175], a[20:23], %0" : "+v"(g[0][1][1]) :  : ); SB();
  asm volatile("v_mul_f32 %0, %0, %1" : "+v"(a1[0][1][3]) : "v"(gt[3]) : ); SB();
  asm volatile("v_mfma_f32_16x16x32_bf16 %0, v[172:175], a[24:27], %0" : "+v"(g[0][2][1]) :  : ); SB();
  asm volatile("buffer_load_dwordx4 v[240:243], %0, %1, %2 offen offset:0 sc1" :  : "v"(vB), "s"(rWS), "s"(2048u) : "memory"); SB();
  asm volatile("v_mul_f32 %0, %1, %1" : "=v"(gt[0]) : "v"(a1[0][2][0]) : ); SB();
  asm volatile("v_mfma_f32_16x16x32_bf16 %0, v[172:175], a[28:31], %0" : "+v"(g[0][3][1]) :  : ); SB();
  asm volatile("v_mul_f32 %0, %1, %1" : "=v"(gt[1]) : "v"(a1[0][2][1]) : ); SB();
  asm volatile("s_waitcnt vmcnt(21)" :  :  : "memory"); SB();
  asm volatile("v_mfma_f32_16x16x32_bf16 %0, v[176:179], a[32:35], %0" : "+v"(g[0][0][0]) :  : ); SB();
  asm volatile("v_mul_f32 %0, %1, %1" : "=v"(gt[2]) : "v"(a1[0][2][2]) : ); SB();
  asm volatile("v_mfma_f32_16x16x32_bf16 %0, v[176:179], a[36:39], %0" : "+v"(g[0][1][0]) :  : ); SB();
  asm volatile("buffer_load_dwordx4 v[244:247], %0, %1, %2 offen offset:1024 sc1" :  : "v"(vB), "s"(rWS), "s"(2048u) : "memory"); SB();
  asm volatile("v_mul_f32 %0, %1, %1" : "=v"(gt[3]) : "v"(a1[0][2][3]) : ); SB();
  asm volatile("v_mfma_f32_16x16x32_bf16 %0, v[176:179], a[40:43], %0" : "+v"(g[0][2][0]) :  : ); SB();
  asm volatile("v_fma_f32 %0, %0, %1, %2" : "+v"(gt[0]) : "v"(k2[0]), "v"(k1[0]) : ); SB();
  asm volatile("v_mfma_f32_16x16x32_bf16 %0, v[176:179], a[44:47], %0" : "+v"(g[0][3][0]) :  : ); SB();
  asm volatile("v_fma_f32 %0, %0, %1, %2" : "+v"(gt[1]) : "v"(k2[0]), "v"(k1[0]) : ); SB();
  asm volatile("v_mfma_f32_16x16x32_bf16 %0, v[180:183], a[32:35], %0" : "+v"(g[0][0][1]) :  : ); SB();
  asm volatile("v_fma_f32 %0, %0, %1, %2" : "+v"(gt[2]) : "v"(k2[0]), "v"(k1[0]) : ); SB();
  asm volatile("v_mfma_f32_16x16x32_bf16 %0, v[180:183], a[36:39], %0" : "+v"(g[0][1][1]) :  : ); SB();
  asm volatile("v_fma_f32 %0, %0, %1, %2" : "+v"(gt[3]) : "v"(k2[0]), "v"(k1[0]) : ); SB();
  asm volatile("v_mfma_f32_16x16x32_bf16 %0, v[180:183], a[40:43], %0" : "+v"(g[0][2][1]) :  : ); SB();
  asm volatile("v_mul_f32 %0, %0, %1" : "+v"(gt[0]) : "v"(a1[0][2][0]) : ); SB();
  asm volatile("v_mfma_f32_16x16x32_bf16 %0, v[180:183], a[44:47], %0" : "+v"(g[0][3][1]) :  : ); SB();
  asm volatile("v_mul_f32 %0, %0, %1" : "+v"(gt[1]) : "v"(a1[0][2][1]) : ); SB();
  asm volatile("s_waitcnt vmcnt(20)" :  :  : "memory"); SB();
  asm volatile("v_mfma_f32_16x16x32_bf16 %0, v[184:187], a[48:51], %0" : "+v"(g[0][0][0]) :  : ); SB();
  asm volatile("buffer_load_dwordx4 v[248:251], %0, %1, %2 offen offset:0 sc1" :  : "v"(vB), "s"(rWS), "s"(67584u) : "memory"); SB();
  asm volatile("v_mul_f32 %0, %0, %1" : "+v"(gt[2]) : "v"(a1[0][2][2]) : ); SB();
  asm volatile("v_mfma_f32_16x16x32_bf16 %0, v[184:187], a[52:55], %0" : "+v"(g[0][1][0]) :  : ); SB();
  asm volatile("v_mul_f32 %0, %0, %1" : "+v"(gt[3]) : "v"(a1[0][2][3]) : ); SB();
  asm volatile("v_mfma_f32_16x16x32_bf16 %0, v[184:187], a[56:59], %0" : "+v"(g[0][2][0]) :  : ); SB();
  asm volatile("v_exp_f32 %0, %1" : "=v"(ge[0]) : "v"(gt[0]) : ); SB();
  asm volatile("v_mfma_f32_16x16x32_bf16 %0, v[184:187], a[60:63], %0" : "+v"(g[0][3][0]) :  : ); SB();
  asm volatile("v_exp_f32 %0, %1" : "=v"(ge[1]) : "v"(gt[1]) : ); SB();
  asm volatile("v_mfma_f32_16x16x32_bf16 %0, v[188:191], a[48:51], %0" : "+v"(g[0][0][1]) :  : ); SB();
  asm volatile("s_waitcnt vmcnt(13)" :  :  : "memory"); SB();
  asm volatile("ds_read_b128 a[132:135], %0 offset:2048" :  : "v"(aA[0]) : "memory"); SB();
  asm volatile("v_mfma_f32_16x16x32_bf16 %0, v[188:191], a[52:55], %0" : "+v"(g[0][1][1]) :  : ); SB();
  asm volatile("ds_read_b128 a[148:151], %0 offset:2048" :  : "v"(aA[1]) : "memory"); SB();
  asm volatile("v_exp_f32 %0, %1" : "=v"(ge[2]) : "v"(gt[2]) : ); SB();
  asm volatile("v_mfma_f32_16x16x32_bf16 %0, v[188:191], a[56:59], %0" : "+v"(g[0][2][1]) :  : ); SB();
  asm volatile("buffer_load_dwordx4 v[252:255], %0, %1, %2 offen offset:1024 sc1" :  : "v"(vB), "s"(rWS), "s"(67584u) : "memory"); SB();
  asm volatile("v_exp_f32 %0, %1" : "=v"(ge[3]) : "v"(gt[3]) : ); SB();
  asm volatile("v_mfma_f32_16x16x32_bf16 %0, v[188:191], a[60:63], %0" : "+v"(g[0][3][1]) :  : ); SB();
  asm volatile("v_xor_b32 %0, 917504, %0" : "+v"(vB) :  : "memory"); SB();
  ST(27);
  asm volatile("v_mfma_f32_16x16x32_bf16 %0, v[192:195], a[64:67], %0" : "+v"(g[0][0][0]) :  : ); SB();
  asm volatile("buffer_load_dwordx4 v[160:163], %0, %1, %2 offen offset:0 sc1" :  : "v"(vB), "s"(rWS), "s"(2048u) : "memory"); SB();
  asm volatile("v_fma_f32 %0, %0, %1, %1" : "+v"(ge[0]) : "v"(xd[0]) : ); SB();
  asm volatile("v_mfma_f32_16x16x32_bf16 %0, v[192:195], a[68:71], %0" : "+v"(g[0][1][0]) :  : ); SB();
  asm volatile("v_fma_f32 %0, %0, %1, %1" : "+v"(ge[1]) : "v"(xd[0]) : ); SB();
  asm volatile("v_mfma_f32_16x16x32_bf16 %0, v[192:195], a[72:75], %0" : "+v"(g[0][2][0]) :  : ); SB();
  asm volatile("s_waitcnt vmcnt(13)" :  :  : "memory"); SB();
  asm volatile("ds_read_b128 a[136:139], %0 offset:4096" :  : "v"(aA[0]) : "memory"); SB();
  asm volatile("v_mfma_f32_16x16x32_bf16 %0, v[192:195], a[76:79], %0" : "+v"(g[0][3][0]) :  : ); SB();
  asm volatile("ds_read_b128 a[152:155], %0 offset:4096" :  : "v"(aA[1]) : "memory"); SB();
  asm volatile("v_fma_f32 %0, %0, %1, %1" : "+v"(ge[2]) : "v"(xd[0]) : ); SB();
  asm volatile("v_mfma_f32_16x16x32_bf16 %0, v[196:199], a[64:67], %0" : "+v"(g[0][0][1]) :  : ); SB();
  asm volatile("buffer_load_dwordx4 v[164:167], %0, %1, %2 offen offset:1024 sc1" :  : "v"(vB), "s"(rWS), "s"(2048u) : "memory"); SB();
  asm volatile("v_fma_f32 %0, %0, %1, %1" : "+v"(ge[3]) : "v"(xd[0]) : ); SB();
  asm volatile("v_mfma_f32_16x16x32_bf16 %0, v[196:199], a[68:71], %0" : "+v"(g[0][1][1]) :  : ); SB();
  asm volatile("v_rcp_f32 %0, %1" : "=v"(gt[0]) : "v"(ge[0]) : ); SB();
  asm volatile("v_mfma_f32_16x16x32_bf16 %0, v[196:199], a[72:75], %0" : "+v"(g[0][2][1]) :  : ); SB();
  asm volatile("buffer_load_dwordx4 v[168:171], %0, %1, %2 offen offset:0 sc1" :  : "v"(vB), "s"(rWS), "s"(67584u) : "memory"); SB();
  asm volatile("v_rcp_f32 %0, %1" : "=v"(gt[1]) : "v"(ge[1]) : ); SB();
  asm volatile("v_mfma_f32_16x16x32_bf16 %0, v[196:199], a[76:79], %0" : "+v"(g[0][3][1]) :  : ); SB();
  asm volatile("v_rcp_f32 %0, %1" : "=v"(gt[2]) : "v"(ge[2]) : ); SB();
  asm volatile("v_mfma_f32_16x16x32_bf16 %0, v[200:203], a[80:83], %0" : "+v"(g[0][0][0]) :  : ); SB();
  asm volatile("v_rcp_f32 %0, %1" : "=v"(gt[3]) : "v"(ge[3]) : ); SB();
  asm volatile("v_mfma_f32_16x16x32_bf16 %0, v[200:203], a[84:87], %0" : "+v"(g[0][1][0]) :  : ); SB();
  asm volatile("s_waitcnt vmcnt(13)" :  :  : "memory"); SB();
  asm volatile("ds_read_b128 a[140:143], %0 offset:6144" :  : "v"(aA[0]) : "memory"); SB();
  asm volatile("v_mfma_f32_16x16x32_bf16 %0, v[200:203], a[88:91], %0" : "+v"(g[0][2][0]) :  : ); SB();
  asm volatile("ds_read_b128 a[156:159], %0 offset:6144" :  : "v"(aA[1]) : "memory"); SB();
  asm volatile("v_mul_f32 %0, %0, %1" : "+v"(a1[0][2][0]) : "v"(gt[0]) : ); SB();
  asm volatile("v_mfma_f32_16x16x32_bf16 %0, v[200:203], a[92:95], %0" : "+v"(g[0][3][0]) :  : ); SB();
  asm volatile("buffer_load_dwordx4 v[172:175], %0, %1, %2 offen offset:1024 sc1" :  : "v"(vB), "s"(rWS), "s"(67584u) : "memory"); SB();
  asm volatile("v_mul_f32 %0, %0, %1" : "+v"(a1[0][2][1]) : "v"(gt[1]) : ); SB();
  asm volatile("v_mfma_f32_16x16x32_bf16 %0, v[204:207], a[80:83], %0" : "+v"(g[0][0][1]) :  : ); SB();
  asm volatile("v_xor_b32 %0, 131072, %0" : "+v"(vB) :  : "memory"); SB();
  asm volatile("v_mul_f32 %0, %0, %1" : "+v"(a1[0][2][2]) : "v"(gt[2]) : ); SB();
  asm volatile("v_mfma_f32_16x16x32_bf16 %0, v[204:207], a[84:87], %0" : "+v"(g[0][1][1]) :  : ); SB();
  asm volatile("buffer_load_dwordx4 v[176:179], %0, %1, %2 offen offset:0 sc1" :  : "v"(vB), "s"(rWS), "s"(2048u) : "memory"); SB();
  asm volatile("v_mul_f32 %0, %0, %1" : "+v"(a1[0][2][3]) : "v"(gt[3]) : ); SB();
  asm volatile("v_mfma_f32_16x16x32_bf16 %0, v[204:207], a[88:91], %0" : "+v"(g[0][2][1]) :  : ); SB();
  asm volatile("v_mul_f32 %0, %1, %1" : "=v"(gt[0]) : "v"(a1[1][0][0]) : ); SB();
  asm volatile("v_mfma_f32_16x16x32_bf16 %0, v[204:207], a[92:95], %0" : "+v"(g[0][3][1]) :  : ); SB();
  asm volatile("v_mul_f32 %0, %1, %1" : "=v"(gt[1]) : "v"(a1[1][0][1]) : ); SB();
  asm volatile("v_mfma_f32_16x16x32_bf16 %0, v[208:211], a[96:99], %0" : "+v"(g[0][0][0]) :  : ); SB();
  asm volatile("buffer_load_dwordx4 v[180:183], %0, %1, %2 offen offset:1024 sc1" :  : "v"(vB), "s"(rWS), "s"(2048u) : "memory"); SB();
  asm volatile("v_mul_f32 %0, %1, %1" : "=v"(gt[2]) : "v"(a1[1][0][2]) : ); SB();
  asm volatile("v_mfma_f32_16x16x32_bf16 %0, v[208:211], a[100:103], %0" : "+v"(g[0][1][0]) :  : ); SB();
  asm volatile("v_mul_f32 %0, %1, %1" : "=v"(gt[3]) : "v"(a1[1][0][3]) : ); SB();
  asm volatile("v_mfma_f32_16x16x32_bf16 %0, v[208:211], a[104:107], %0" : "+v"(g[0][2][0]) :  : ); SB();
  asm volatile("v_fma_f32 %0, %0, %1, %2" : "+v"(gt[0]) : "v"(k2[0]), "v"(k1[0]) : ); SB();
  asm volatile("s_waitcnt lgkmcnt(1)\n\tv_mfma_f32_16x16x32_bf16 %0, v[208:211], a[108:111], %0" : "+v"(g[0][3][0]) :  : ); SB();
  asm volatile("v_fma_f32 %0, %0, %1, %2" : "+v"(gt[1]) : "v"(k2[0]), "v"(k1[0]) : ); SB();
  asm volatile("v_mfma_f32_16x16x32_bf16 %0, v[212:215], a[96:99], %0" : "+v"(g[0][0][1]) :  : ); SB();
  asm volatile("buffer_load_dwordx4 v[184:187], %0, %1, %2 offen offset:0 sc1" :  : "v"(vB), "s"(rWS), "s"(67584u) : "memory"); SB();
  asm volatile("v_fma_f32 %0, %0, %1, %2" : "+v"(gt[2]) : "v"(k2[0]), "v"(k1[0]) : ); SB();
  asm volatile("v_mfma_f32_16x16x32_bf16 %0, v[212:215], a[100:103], %0" : "+v"(g[0][1][1]) :  : ); SB();
  asm volatile("v_fma_f32 %0, %0, %1, %2" : "+v"(gt[3]) : "v"(k2[0]), "v"(k1[0]) : ); SB();
  asm volatile("v_mfma_f32_16x16x32_bf16 %0, v[212:215], a[104:107], %0" : "+v"(g[0][2][1]) :  : ); SB();
  asm volatile("v_mul_f32 %0, %0, %1" : "+v"(gt[0]) : "v"(a1[1][0][0]) : ); SB();
  asm volatile("v_mfma_f32_16x16x32_bf16 %0, v[212:215], a[108:111], %0" : "+v"(g[0][3][1]) :  : ); SB();
  asm volatile("v_mul_f32 %0, %0, %1" : "+v"(gt[1]) : "v"(a1[1][0][1]) : ); SB();
  asm volatile("s_waitcnt vmcnt(15)" :  :  : "memory"); SB();
  asm volatile("v_mfma_f32_16x16x32_bf16 %0, v[216:219], a[112:115], %0" : "+v"(g[0][0][0]) :  : ); SB();
  asm volatile("buffer_load_dwordx4 v[188:191], %0, %1, %2 offen offset:1024 sc1" :  : "v"(vB), "s"(rWS), "s"(67584u) : "memory"); SB();
  asm volatile("v_mul_f32 %0, %0, %1" : "+v"(gt[2]) : "v"(a1[1][0][2]) : ); SB();
  asm volatile("v_mfma_f32_16x16x32_bf16 %0, v[216:219], a[116:119], %0" : "+v"(g[0][1][0]) :  : ); SB();
  asm volatile("v_xor_b32 %0, 393216, %0" : "+v"(vB) :  : "memory"); SB();
  asm volatile("v_mul_f32 %0, %0, %1" : "+v"(gt[3]) : "v"(a1[1][0][3]) : ); SB();
  asm volatile("v_mfma_f32_16x16x32_bf16 %0, v[216:219], a[120:123], %0" : "+v"(g[0][2][0]) :  : ); SB();
  asm volatile("v_exp_f32 %0, %1" : "=v"(ge[0]) : "v"(gt[0]) : ); SB();
  asm volatile("v_mfma_f32_16x16x32_bf16 %0, v[216:219], a[124:127], %0" : "+v"(g[0][3][0]) :  : ); SB();
  asm volatile("buffer_load_dwordx4 v[192:195], %0, %1, %2 offen offset:0 sc1" :  : "v"(vB), "s"(rWS), "s"(2048u) : "memory"); SB();
  asm volatile("v_exp_f32 %0, %1" : "=v"(ge[1]) : "v"(gt[1]) : ); SB();
  asm volatile("v_mfma_f32_16x16x32_bf16 %0, v[220:223], a[112:115], %0" : "+v"(g[0][0][1]) :  : ); SB();
  asm volatile("v_exp_f32 %0, %1" : "=v"(ge[2]) : "v"(gt[2]) : ); SB();
  asm volatile("v_mfma_f32_16x16x32_bf16 %0, v[220:223], a[116:119], %0" : "+v"(g[0][1][1]) :  : ); SB();
  asm volatile("v_exp_f32 %0, %1" : "=v"(ge[3]) : "v"(gt[3]) : ); SB();
  asm volatile("v_mfma_f32_16x16x32_bf16 %0, v[220:223], a[120:123], %0" : "+v"(g[0][2][1]) :  : ); SB();
  asm volatile("v_fma_f32 %0, %0, %1, %1" : "+v"(ge[0]) : "v"(xd[0]) : ); SB();
  asm volatile("v_mfma_f32_16x16x32_bf16 %0, v[220:223], a[124:127], %0" : "+v"(g[0][3][1]) :  : ); SB();
  asm volatile("buffer_load_dwordx4 v[196:199], %0, %1, %2 offen offset:1024 sc1" :  : "v"(vB), "s"(rWS), "s"(2048u) : "memory"); SB();
  asm volatile("s_waitcnt vmcnt(16)" :  :  : "memory"); SB();
  asm volatile("v_mfma_f32_16x16x32_bf16 %0, v[224:227], a[128:131], %0" : "+v"(g[0][0][0]) :  : ); SB();
  asm volatile("v_fma_f32 %0, %0, %1, %1" : "+v"(ge[1]) : "v"(xd[0]) : ); SB();
  asm volatile("v_mfma_f32_16x16x32_bf16 %0, v[224:227], a[132:135], %0" : "+v"(g[0][1][0]) :  : ); SB();
  asm volatile("v_fma_f32 %0, %0, %1, %1" : "+v"(ge[2]) : "v"(xd[0]) : ); SB();
  asm volatile("v_mfma_f32_16x16x32_bf16 %0, v[224:227], a[136:139], %0" : "+v"(g[0][2][0]) :  : ); SB();
  asm volatile("buffer_load_dwordx4 v[200:203], %0, %1, %2 offen offset:0 sc1" :  : "v"(vB), "s"(rWS), "s"(67584u) : "memory"); SB();
  asm volatile("s_waitcnt lgkmcnt(0)\n\tv_mfma_f32_16x16x32_bf16 %0, v[224:227], a[140:143], %0" : "+v"(g[0][3][0]) :  : ); SB();
  asm volatile("v_fma_f32 %0, %0, %1, %1" : "+v"(ge[3]) : "v"(xd[0]) : ); SB();
  asm volatile("v_mfma_f32_16x16x32_bf16 %0, v[228:231], a[128:131], %0" : "+v"(g[0][0][1]) :  : ); SB();
  asm volatile("v_rcp_f32 %0, %1" : "=v"(gt[0]) : "v"(ge[0]) : ); SB();
  asm volatile("v_mfma_f32_16x16x32_bf16 %0, v[228:231], a[132:135], %0" : "+v"(g[0][1][1]) :  : ); SB();
  asm volatile("v_rcp_f32 %0, %1" : "=v"(gt[1]) : "v"(ge[1]) : ); SB();
  asm volatile("v_mfma_f32_16x16x32_bf16 %0, v[228:231], a[136:139], %0" : "+v"(g[0][2][1]) :  : ); SB();
  asm volatile("buffer_load_dwordx4 v[204:207], %0, %1, %2 offen offset:1024 sc1" :  : "v"(vB), "s"(rWS), "s"(67584u) : "memory"); SB();
  asm volatile("v_rcp_f32 %0, %1" : "=v"(gt[2]) : "v"(ge[2]) : ); SB();
  asm volatile("v_mfma_f32_16x16x32_bf16 %0, v[228:231], a[140:143], %0" : "+v"(g[0][3][1]) :  : ); SB();
  asm volatile("v_xor_b32 %0, 131072, %0" : "+v"(vB) :  : "memory"); SB();
  asm volatile("s_waitcnt vmcnt(16)" :  :  : "memory"); SB();
  asm volatile("v_mfma_f32_16x16x32_bf16 %0, v[232:235], a[144:147], %0" : "+v"(g[0][0][0]) :  : ); SB();
  asm volatile("v_rcp_f32 %0, %1" : "=v"(gt[3]) : "v"(ge[3]) : ); SB();
  asm volatile("v_mfma_f32_16x16x32_bf16 %0, v[232:235], a[148:151], %0" : "+v"(g[0][1][0]) :  : ); SB();
  asm volatile("buffer_load_dwordx4 v[208:211], %0, %1, %2 offen offset:0 sc1" :  : "v"(vB), "s"(rWS), "s"(2048u) : "memory"); SB();
  asm volatile("v_mul_f32 %0, %0, %1" : "+v"(a1[1][0][0]) : "v"(gt[0]) : ); SB();
  asm volatile("v_mfma_f32_16x16x32_bf16 %0, v[232:235], a[152:155], %0" : "+v"(g[0][2][0]) :  : ); SB();
  asm volatile("v_mul_f32 %0, %0, %1" : "+v"(a1[1][0][1]) : "v"(gt[1]) : ); SB();
  asm volatile("v_mfma_f32_16x16x32_bf16 %0, v[232:235], a[156:159], %0" : "+v"(g[0][3][0]) :  : ); SB();
  asm volatile("v_mul_f32 %0, %0, %1" : "+v"(a1[1][0][2]) : "v"(gt[2]) : ); SB();
  asm volatile("v_mfma_f32_16x16x32_bf16 %0, v[236:239], a[144:147], %0" : "+v"(g[0][0][1]) :  : ); SB();
  asm volatile("v_mul_f32 %0, %0, %1" : "+v"(a1[1][0][3]) : "v"(gt[3]) : ); SB();
  asm volatile("v_mfma_f32_16x16x32_bf16 %0, v[236:239], a[148:151], %0" : "+v"(g[0][1][1]) :  : ); SB();
  asm volatile("v_mul_f32 %0, %1, %1" : "=v"(gt[0]) : "v"(a1[1][1][0]) : ); SB();
  asm volatile("v_mfma_f32_16x16x32_bf16 %0, v[236:239], a[152:155], %0" : "+v"(g[0][2][1]) :  : ); SB();
  asm volatile("v_mul_f32 %0, %1, %1" : "=v"(gt[1]) : "v"(a1[1][1][1]) : ); SB();
  asm volatile("v_mfma_f32_16x16x32_bf16 %0, v[236:239], a[156:159], %0" : "+v"(g[0][3][1]) :  : ); SB();
  asm volatile("buffer_load_dwordx4 v[212:215], %0, %1, %2 offen offset:1024 sc1" :  : "v"(vB), "s"(rWS), "s"(2048u) : "memory"); SB();
  asm volatile("s_waitcnt vmcnt(16)" :  :  : "memory"); SB();
  asm volatile("v_mfma_f32_16x16x32_bf16 %0, v[240:243], a[0:3], %0" : "+v"(g[1][0][0]) :  : ); SB();
  asm volatile("s_barrier" :  :  : "memory"); SB();
  asm volatile("v_mul_f32 %0, %1, %1" : "=v"(gt[2]) : "v"(a1[1][1][2]) : ); SB();
  asm volatile("v_mfma_f32_16x16x32_bf16 %0, v[240:243], a[4:7], %0" : "+v"(g[1][1][0]) :  : ); SB();
  asm volatile("buffer_load_dwordx4 v[216:219], %0, %1, %2 offen offset:0 sc1" :  : "v"(vB), "s"(rWS), "s"(67584u) : "memory"); SB();
  asm volatile("v_mul_f32 %0, %1, %1" : "=v"(gt[3]) : "v"(a1[1][1][3]) : ); SB();
  asm volatile("v_mfma_f32_16x16x32_bf16 %0, v[240:243], a[8:11], %0" : "+v"(g[1][2][0]) :  : ); SB();
  asm volatile("ds_write_b128 %0, %1 offset:0" :  : "v"(pw2), "v"(g[0][1][0]) : "memory"); SB();
  asm volatile("v_mfma_f32_16x16x32_bf16 %0, v[240:243], a[12:15], %0" : "+v"(g[1][3][0]) :  : ); SB();
  asm volatile("v_fma_f32 %0, %0, %1, %2" : "+v"(gt[0]) : "v"(k2[0]), "v"(k1[0]) : ); SB();
  asm volatile("v_mfma_f32_16x16x32_bf16 %0, v[244:247], a[0:3], %0" : "+v"(g[1][0][1]) :  : ); SB();
  asm volatile("buffer_load_dwordx4 v[220:223], %0, %1, %2 offen offset:1024 sc1" :  : "v"(vB), "s"(rWS), "s"(67584u) : "memory"); SB();
  asm volatile("v_fma_f32 %0, %0, %1, %2" : "+v"(gt[1]) : "v"(k2[0]), "v"(k1[0]) : ); SB();
  asm volatile("v_mfma_f32_16x16x32_bf16 %0, v[244:247], a[4:7], %0" : "+v"(g[1][1][1]) :  : ); SB();
  asm volatile("ds_write_b128 %0, %1 offset:1024" :  : "v"(pw2), "v"(g[0][1][1]) : "memory"); SB();
  asm volatile("v_mfma_f32_16x16x32_bf16 %0, v[244:247], a[8:11], %0" : "+v"(g[1][2][1]) :  : ); SB();
  asm volatile("v_xor_b32 %0, 917504, %0" : "+v"(vB) :  : "memory"); SB();
  asm volatile("v_fma_f32 %0, %0, %1, %2" : "+v"(gt[2]) : "v"(k2[0]), "v"(k1[0]) : ); SB();
  asm volatile("v_mfma_f32_16x16x32_bf16 %0, v[244:247], a[12:15], %0" : "+v"(g[1][3][1]) :  : ); SB();
  asm volatile("buffer_load_dwordx4 v[224:227], %0, %1, %2 offen offset:0 sc1" :  : "v"(vB), "s"(rWS), "s"(4096u) : "memory"); SB();
  asm volatile("s_waitcnt vmcnt(17)" :  :  : "memory"); SB();
  asm volatile("v_mfma_f32_16x16x32_bf16 %0, v[248:251], a[16:19], %0" : "+v"(g[1][0][0]) :  : ); SB();
  asm volatile("ds_write_b128 %0, %1 offset:2048" :  : "v"(pw2), "v"(g[0][2][0]) : "memory"); SB();
  asm volatile("v_mfma_f32_16x16x32_bf16 %0, v[248:251], a[20:23], %0" : "+v"(g[1][1][0]) :  : ); SB();
  asm volatile("buffer_store_dwordx4 %1, %0, %2, %3 offen offset:2048 sc1\n\ts_nop 1" :  : "v"(vH_), "v"(ho[2]), "s"(rWS), "s"(0u) : "memory"); SB();
  asm volatile("v_fma_f32 %0, %0, %1, %2" : "+v"(gt[3]) : "v"(k2[0]), "v"(k1[0]) : ); SB();
  asm volatile("v_mfma_f32_16x16x32_bf16 %0, v[248:251], a[24:27], %0" : "+v"(g[1][2][0]) :  : ); SB();
  asm volatile("v_mul_f32 %0, %0, %1" : "+v"(gt[0]) : "v"(a1[1][1][0]) : ); SB();
  asm volatile("v_mfma_f32_16x16x32_bf16 %0, v[248:251], a[28:31], %0" : "+v"(g[1][3][0]) :  : ); SB();
  asm volatile("ds_write_b128 %0, %1 offset:3072" :  : "v"(pw2), "v"(g[0][2][1]) : "memory"); SB();
  asm volatile("v_mfma_f32_16x16x32_bf16 %0, v[252:255], a[16:19], %0" : "+v"(g[1][0][1]) :  : ); SB();
  asm volatile("buffer_load_dwordx4 v[228:231], %0, %1, %2 offen offset:1024 sc1" :  : "v"(vB), "s"(rWS), "s"(4096u) : "memory"); SB();
  asm volatile("v_mul_f32 %0, %0, %1" : "+v"(gt[1]) : "v"(a1[1][1][1]) : ); SB();
  asm volatile("v_mfma_f32_16x16x32_bf16 %0, v[252:255], a[20:23], %0" : "+v"(g[1][1][1]) :  : ); SB();
  asm volatile("v_mul_f32 %0, %0, %1" : "+v"(gt[2]) : "v"(a1[1][1][2]) : ); SB();
  asm volatile("v_mfma_f32_16x16x32_bf16 %0, v[252:255], a[24:27], %0" : "+v"(g[1][2][1]) :  : ); SB();
  asm volatile("ds_write_b128 %0, %1 offset:4096" :  : "v"(pw2), "v"(g[0][3][0]) : "memory"); SB();
  asm volatile("v_mfma_f32_16x16x32_bf16 %0, v[252:255], a[28:31], %0" : "+v"(g[1][3][1]) :  : ); SB();
  asm volatile("buffer_load_dwordx4 v[232:235], %0, %1, %2 offen offset:0 sc1" :  : "v"(vB), "s"(rWS), "s"(69632u) : "memory"); SB();
  ST(28);
  asm volatile("s_waitcnt vmcnt(18)" :  :  : "memory"); SB();
  asm volatile("v_mfma_f32_16x16x32_bf16 %0, v[160:163], a[32:35], %0" : "+v"(g[1][0][0]) :  : ); SB();
  asm volatile("v_mul_f32 %0, %0, %1" : "+v"(gt[3]) : "v"(a1[1][1][3]) : ); SB();
  asm volatile("v_mfma_f32_16x16x32_bf16 %0, v[160:163], a[36:39], %0" : "+v"(g[1][1][0]) :  : ); SB();
  asm volatile("ds_write_b128 %0, %1 offset:5120" :  : "v"(pw2), "v"(g[0][3][1]) : "memory"); SB();
  asm volatile("v_mfma_f32_16x16x32_bf16 %0, v[160:163], a[40:43], %0" : "+v"(g[1][2][0]) :  : ); SB();
  asm volatile("buffer_load_dwordx4 v[236:239], %0, %1, %2 offen offset:1024 sc1" :  : "v"(vB), "s"(rWS), "s"(69632u) : "memory"); SB();
  asm volatile("v_exp_f32 %0, %1" : "=v"(ge[0]) : "v"(gt[0]) : ); SB();
  asm volatile("v_mfma_f32_16x16x32_bf16 %0, v[160:163], a[44:47], %0" : "+v"(g[1][3][0]) :  : ); SB();
  asm volatile("v_xor_b32 %0, 131072, %0" : "+v"(vB) :  : "memory"); SB();
  asm volatile("v_exp_f32 %0, %1" : "=v"(ge[1]) : "v"(gt[1]) : ); SB();
  asm volatile("v_mfma_f32_16x16x32_bf16 %0, v[164:167], a[32:35], %0" : "+v"(g[1][0][1]) :  : ); SB();
  asm volatile("s_waitcnt lgkmcnt(0)\n\ts_barrier" :  :  : "memory"); SB();
  asm volatile("ds_read_b128 %0, %1 offset:0" : "=&v"(pr[0]) : "v"(pe[0]) : "memory"); SB();
  asm volatile("v_mfma_f32_16x16x32_bf16 %0, v[164:167], a[36:39], %0" : "+v"(g[1][1][1]) :  : ); SB();
  asm volatile("ds_read_b128 %0, %1 offset:1024" : "=&v"(pr[1]) : "v"(pe[0]) : "memory"); SB();
  asm volatile("v_exp_f32 %0, %1" : "=v"(ge[2]) : "v"(gt[2]) : ); SB();
  asm volatile("v_mfma_f32_16x16x32_bf16 %0, v[164:167], a[40:43], %0" : "+v"(g[1][2][1]) :  : ); SB();
  asm volatile("ds_read_b128 %0, %1 offset:2048" : "=&v"(pr[2]) : "v"(pe[1]) : "memory"); SB();
  asm volatile("v_exp_f32 %0, %1" : "=v"(ge[3]) : "v"(gt[3]) : ); SB();
  asm volatile("v_mfma_f32_16x16x32_bf16 %0, v[164:167], a[44:47], %0" : "+v"(g[1][3][1]) :  : ); SB();
  asm volatile("buffer_load_dwordx4 v[240:243], %0, %1, %2 offen offset:0 sc1" :  : "v"(vB), "s"(rWS), "s"(4096u) : "memory"); SB();
  asm volatile("s_waitcnt vmcnt(18)" :  :  : "memory"); SB();
  asm volatile("v_mfma_f32_16x16x32_bf16 %0, v[168:171], a[48:51], %0" : "+v"(g[1][0][0]) :  : ); SB();
  asm volatile("ds_read_b128 %0, %1 offset:3072" : "=&v"(pr[3]) : "v"(pe[1]) : "memory"); SB();
  asm volatile("v_fma_f32 %0, %0, %1, %1" : "+v"(ge[0]) : "v"(xd[0]) : ); SB();
  asm volatile("v_mfma_f32_16x16x32_bf16 %0, v[168:171], a[52:55], %0" : "+v"(g[1][1][0]) :  : ); SB();
  asm volatile("ds_read_b128 %0, %1 offset:4096" : "=&v"(pr[4]) : "v"(pe[2]) : "memory"); SB();
  asm volatile("v_fma_f32 %0, %0, %1, %1" : "+v"(ge[1]) : "v"(xd[0]) : ); SB();
  asm volatile("v_mfma_f32_16x16x32_bf16 %0, v[168:171], a[56:59], %0" : "+v"(g[1][2][0]) :  : ); SB();
  asm volatile("buffer_load_dwordx4 v[244:247], %0, %1, %2 offen offset:1024 sc1" :  : "v"(vB), "s"(rWS), "s"(4096u) : "memory"); SB();
  asm volatile("v_fma_f32 %0, %0, %1, %1" : "+v"(ge[2]) : "v"(xd[0]) : ); SB();
  asm volatile("v_mfma_f32_16x16x32_bf16 %0, v[168:171], a[60:63], %0" : "+v"(g[1][3][0]) :  : ); SB();
  asm volatile("ds_read_b128 %0, %1 offset:5120" : "=&v"(pr[5]) : "v"(pe[2]) : "memory"); SB();
  asm volatile("v_fma_f32 %0, %0, %1, %1" : "+v"(ge[3]) : "v"(xd[0]) : ); SB();
  asm volatile("v_mfma_f32_16x16x32_bf16 %0, v[172:175], a[48:51], %0" : "+v"(g[1][0][1]) :  : ); SB();
  asm volatile("buffer_load_dwordx4 v[248:251], %0, %1, %2 offen offset:0 sc1" :  : "v"(vB), "s"(rWS), "s"(69632u) : "memory"); SB();
  asm volatile("v_rcp_f32 %0, %1" : "=v"(gt[0]) : "v"(ge[0]) : ); SB();
  asm volatile("v_mfma_f32_16x16x32_bf16 %0, v[172:175], a[52:55], %0" : "+v"(g[1][1][1]) :  : ); SB();
  asm volatile("v_rcp_f32 %0, %1" : "=v"(gt[1]) : "v"(ge[1]) : ); SB();
  asm volatile("v_mfma_f32_16x16x32_bf16 %0, v[172:175], a[56:59], %0" : "+v"(g[1][2][1]) :  : ); SB();
  asm volatile("buffer_load_dwordx4 v[252:255], %0, %1, %2 offen offset:1024 sc1" :  : "v"(vB), "s"(rWS), "s"(69632u) : "memory"); SB();
  asm volatile("v_rcp_f32 %0, %1" : "=v"(gt[2]) : "v"(ge[2]) : ); SB();
  asm volatile("v_mfma_f32_16x16x32_bf16 %0, v[172:175], a[60:63], %0" : "+v"(g[1][3][1]) :  : ); SB();
  asm volatile("v_xor_b32 %0, 393216, %0" : "+v"(vB) :  : "memory"); SB();
  asm volatile("s_waitcnt vmcnt(19)" :  :  : "memory"); SB();
  asm volatile("v_mfma_f32_16x16x32_bf16 %0, v[176:179], a[64:67], %0" : "+v"(g[1][0][0]) :  : ); SB();
  asm volatile("v_rcp_f32 %0, %1" : "=v"(gt[3]) : "v"(ge[3]) : ); SB();
  asm volatile("v_mfma_f32_16x16x32_bf16 %0, v[176:179], a[68:71], %0" : "+v"(g[1][1][0]) :  : ); SB();
  asm volatile("s_waitcnt lgkmcnt(0)" : "+v"(pr[0]), "+v"(pr[1]), "+v"(pr[2]), "+v"(pr[3]), "+v"(pr[4]), "+v"(pr[5]) :  : "memory"); SB();
  asm volatile("buffer_load_dwordx4 v[160:163], %0, %1, %2 offen offset:0 sc1" :  : "v"(vB), "s"(rWS), "s"(4096u) : "memory"); SB();
  asm volatile("v_mfma_f32_16x16x32_bf16 %0, v[176:179], a[72:75], %0" : "+v"(g[1][2][0]) :  : ); SB();
  asm volatile("v_add_f32 %0, %0, %1" : "+v"(pr[0][0]) : "v"(g[0][0][0][0]) : ); SB();
  asm volatile("v_mfma_f32_16x16x32_bf16 %0, v[176:179], a[76:79], %0" : "+v"(g[1][3][0]) :  : ); SB();
  asm volatile("v_add_f32 %0, %0, %1" : "+v"(pr[0][1]) : "v"(g[0][0][0][1]) : ); SB();
  asm volatile("v_mfma_f32_16x16x32_bf16 %0, v[180:183], a[64:67], %0" : "+v"(g[1][0][1]) :  : ); SB();
  asm volatile("buffer_load_dwordx4 v[164:167], %0, %1, %2 offen offset:1024 sc1" :  : "v"(vB), "s"(rWS), "s"(4096u) : "memory"); SB();
  asm volatile("v_add_f32 %0, %0, %1" : "+v"(pr[0][2]) : "v"(g[0][0][0][2]) : ); SB();
  asm volatile("v_mfma_f32_16x16x32_bf16 %0, v[180:183], a[68:71], %0" : "+v"(g[1][1][1]) :  : ); SB();
  asm volatile("v_add_f32 %0, %0, %1" : "+v"(pr[0][3]) : "v"(g[0][0][0][3]) : ); SB();
  asm volatile("v_mfma_f32_16x16x32_bf16 %0, v[180:183], a[72:75], %0" : "+v"(g[1][2][1]) :  : ); SB();
  asm volatile("v_add_f32 %0, %0, %1" : "+v"(pr[1][0]) : "v"(g[0][0][1][0]) : ); SB();
  asm volatile("v_mfma_f32_16x16x32_bf16 %0, v[180:183], a[76:79], %0" : "+v"(g[1][3][1]) :  : ); SB();
  asm volatile("v_add_f32 %0, %0, %1" : "+v"(pr[1][1]) : "v"(g[0][0][1][1]) : ); SB();
  asm volatile("s_waitcnt vmcnt(19)" :  :  : "memory"); SB();
  asm volatile("v_mfma_f32_16x16x32_bf16 %0, v[184:187], a[80:83], %0" : "+v"(g[1][0][0]) :  : ); SB();
  asm volatile("buffer_load_dwordx4 v[168:171], %0, %1, %2 offen offset:0 sc1" :  : "v"(vB), "s"(rWS), "s"(69632u) : "memory"); SB();
  asm volatile("v_add_f32 %0, %0, %1" : "+v"(pr[1][2]) : "v"(g[0][0][1][2]) : ); SB();
  asm volatile("v_mfma_f32_16x16x32_bf16 %0, v[184:187], a[84:87], %0" : "+v"(g[1][1][0]) :  : ); SB();
  asm volatile("v_add_f32 %0, %0, %1" : "+v"(pr[1][3]) : "v"(g[0][0][1][3]) : ); SB();
  asm volatile("v_mfma_f32_16x16x32_bf16 %0, v[184:187], a[88:91], %0" : "+v"(g[1][2][0]) :  : ); SB();
  asm volatile("v_add_f32 %0, %0, %1" : "+v"(pr[2][0]) : "v"(pr[4][0]) : ); SB();
  asm volatile("v_mfma_f32_16x16x32_bf16 %0, v[184:187], a[92:95], %0" : "+v"(g[1][3][0]) :  : ); SB();
  asm volatile("buffer_load_dwordx4 v[172:175], %0, %1, %2 offen offset:1024 sc1" :  : "v"(vB), "s"(rWS), "s"(69632u) : "memory"); SB();
  asm volatile("v_add_f32 %0, %0, %1" : "+v"(pr[2][1]) : "v"(pr[4][1]) : ); SB();
  asm volatile("v_mfma_f32_16x16x32_bf16 %0, v[188:191], a[80:83], %0" : "+v"(g[1][0][1]) :  : ); SB();
  asm volatile("v_xor_b32 %0, 131072, %0" : "+v"(vB) :  : "memory"); SB();
  asm volatile("v_add_f32 %0, %0, %1" : "+v"(pr[2][2]) : "v"(pr[4][2]) : ); SB();
  asm volatile("v_mfma_f32_16x16x32_bf16 %0, v[188:191], a[84:87], %0" : "+v"(g[1][1][1]) :  : ); SB();
  asm volatile("v_add_f32 %0, %0, %1" : "+v"(pr[2][3]) : "v"(pr[4][3]) : ); SB();
  asm volatile("v_mfma_f32_16x16x32_bf16 %0, v[188:191], a[88:91], %0" : "+v"(g[1][2][1]) :  : ); SB();
  asm volatile("v_add_f32 %0, %0, %1" : "+v"(pr[3][0]) : "v"(pr[5][0]) : ); SB();
  asm volatile("v_mfma_f32_16x16x32_bf16 %0, v[188:191], a[92:95], %0" : "+v"(g[1][3][1]) :  : ); SB();
  asm volatile("buffer_load_dwordx4 v[176:179], %0, %1, %2 offen offset:0 sc1" :  : "v"(vB), "s"(rWS), "s"(4096u) : "memory"); SB();
  asm volatile("s_waitcnt vmcnt(20)" :  :  : "memory"); SB();
  asm volatile("v_mfma_f32_16x16x32_bf16 %0, v[192:195], a[96:99], %0" : "+v"(g[1][0][0]) :  : ); SB();
  asm volatile("v_add_f32 %0, %0, %1" : "+v"(pr[3][1]) : "v"(pr[5][1]) : ); SB();
  asm volatile("v_mfma_f32_16x16x32_bf16 %0, v[192:195], a[100:103], %0" : "+v"(g[1][1][0]) :  : ); SB();
  asm volatile("v_add_f32 %0, %0, %1" : "+v"(pr[3][2]) : "v"(pr[5][2]) : ); SB();
  asm volatile("v_mfma_f32_16x16x32_bf16 %0, v[192:195], a[104:107], %0" : "+v"(g[1][2][0]) :  : ); SB();
  asm volatile("buffer_load_dwordx4 v[180:183], %0, %1, %2 offen offset:1024 sc1" :  : "v"(vB), "s"(rWS), "s"(4096u) : "memory"); SB();
  asm volatile("v_add_f32 %0, %0, %1" : "+v"(pr[3][3]) : "v"(pr[5][3]) : ); SB();
  asm volatile("v_mfma_f32_16x16x32_bf16 %0, v[192:195], a[108:111], %0" : "+v"(g[1][3][0]) :  : ); SB();
  asm volatile("v_add_f32 %0, %0, %1" : "+v"(pr[0][0]) : "v"(pr[2][0]) : ); SB();
  asm volatile("v_mfma_f32_16x16x32_bf16 %0, v[196:199], a[96:99], %0" : "+v"(g[1][0][1]) :  : ); SB();
  asm volatile("v_add_f32 %0, %0, %1" : "+v"(pr[0][1]) : "v"(pr[2][1]) : ); SB();
  asm volatile("v_mfma_f32_16x16x32_bf16 %0, v[196:199], a[100:103], %0" : "+v"(g[1][1][1]) :  : ); SB();
  asm volatile("v_add_f32 %0, %0, %1" : "+v"(pr[0][2]) : "v"(pr[2][2]) : ); SB();
  asm volatile("v_mfma_f32_16x16x32_bf16 %0, v[196:199], a[104:107], %0" : "+v"(g[1][2][1]) :  : ); SB();
  asm volatile("v_add_f32 %0, %0, %1" : "+v"(pr[0][3]) : "v"(pr[2][3]) : ); SB();
  asm volatile("v_mfma_f32_16x16x32_bf16 %0, v[196:199], a[108:111], %0" : "+v"(g[1][3][1]) :  : ); SB();
  asm volatile("v_add_f32 %0, %0, %1" : "+v"(pr[1][0]) : "v"(pr[3][0]) : ); SB();
  asm volatile("s_waitcnt vmcnt(19)" :  :  : "memory"); SB();
  asm volatile("v_mfma_f32_16x16x32_bf16 %0, v[200:203], a[112:115], %0" : "+v"(g[1][0][0]) :  : ); SB();
  asm volatile("v_add_f32 %0, %0, %1" : "+v"(pr[1][1]) : "v"(pr[3][1]) : ); SB();
  asm volatile("v_mfma_f32_16x16x32_bf16 %0, v[200:203], a[116:119], %0" : "+v"(g[1][1][0]) :  : ); SB();
  asm volatile("v_add_f32 %0, %0, %1" : "+v"(pr[1][2]) : "v"(pr[3][2]) : ); SB();
  asm volatile("v_mfma_f32_16x16x32_bf16 %0, v[200:203], a[120:123], %0" : "+v"(g[1][2][0]) :  : ); SB();
  asm volatile("v_add_f32 %0, %0, %1" : "+v"(pr[1][3]) : "v"(pr[3][3]) : ); SB();
  asm volatile("v_mfma_f32_16x16x32_bf16 %0, v[200:203], a[124:127], %0" : "+v"(g[1][3][0]) :  : ); SB();
  asm volatile("v_mul_f32 %0, %0, %1" : "+v"(pr[0][0]) : "v"(a1[0][0][0]) : ); SB();
  asm volatile("v_mfma_f32_16x16x32_bf16 %0, v[204:207], a[112:115], %0" : "+v"(g[1][0][1]) :  : ); SB();
  asm volatile("v_mul_f32 %0, %0, %1" : "+v"(pr[0][1]) : "v"(a1[0][0][1]) : ); SB();
  asm volatile("v_mfma_f32_16x16x32_bf16 %0, v[204:207], a[116:119], %0" : "+v"(g[1][1][1]) :  : ); SB();
  asm volatile("v_mul_f32 %0, %0, %1" : "+v"(pr[0][2]) : "v"(a1[0][0][2]) : ); SB();
  asm volatile("v_mfma_f32_16x16x32_bf16 %0, v[204:207], a[120:123], %0" : "+v"(g[1][2][1]) :  : ); SB();
  asm volatile("v_mul_f32 %0, %0, %1" : "+v"(pr[0][3]) : "v"(a1[0][0][3]) : ); SB();
  asm volatile("v_mfma_f32_16x16x32_bf16 %0, v[204:207], a[124:127], %0" : "+v"(g[1][3][1]) :  : ); SB();
  asm volatile("buffer_load_dwordx4 v[184:187], %0, %1, %2 offen offset:0 sc1" :  : "v"(vB), "s"(rWS), "s"(69632u) : "memory"); SB();
  asm volatile("s_waitcnt vmcnt(18)" :  :  : "memory"); SB();
  asm volatile("v_mfma_f32_16x16x32_bf16 %0, v[208:211], a[128:131], %0" : "+v"(g[1][0][0]) :  : ); SB();
  asm volatile("v_mul_f32 %0, %0, %1" : "+v"(pr[1][0]) : "v"(a1[0][1][0]) : ); SB();
  asm volatile("v_mfma_f32_16x16x32_bf16 %0, v[208:211], a[132:135], %0" : "+v"(g[1][1][0]) :  : ); SB();
  asm volatile("v_mul_f32 %0, %0, %1" : "+v"(pr[1][1]) : "v"(a1[0][1][1]) : ); SB();
  asm volatile("v_mfma_f32_16x16x32_bf16 %0, v[208:211], a[136:139], %0" : "+v"(g[1][2][0]) :  : ); SB();
  asm volatile("v_mul_f32 %0, %0, %1" : "+v"(pr[1][2]) : "v"(a1[0][1][2]) : ); SB();
  asm volatile("v_mfma_f32_16x16x32_bf16 %0, v[208:211], a[140:143], %0" : "+v"(g[1][3][0]) :  : ); SB();
  asm volatile("buffer_load_dwordx4 v[188:191], %0, %1, %2 offen offset:1024 sc1" :  : "v"(vB), "s"(rWS), "s"(69632u) : "memory"); SB();
  asm volatile("v_mul_f32 %0, %0, %1" : "+v"(pr[1][3]) : "v"(a1[0][1][3]) : ); SB();
  asm volatile("v_mfma_f32_16x16x32_bf16 %0, v[212:215], a[128:131], %0" : "+v"(g[1][0][1]) :  : ); SB();
  asm volatile("v_xor_b32 %0, 917504, %0" : "+v"(vB) :  : "memory"); SB();
  asm volatile("v_cvt_pk_bf16_f32 %0, %1, %2" : "=v"(ho[0][0]) : "v"(pr[0][0]), "v"(pr[0][1]) : ); SB();
  asm volatile("v_mfma_f32_16x16x32_bf16 %0, v[212:215], a[132:135], %0" : "+v"(g[1][1][1]) :  : ); SB();
  asm volatile("v_cvt_pk_bf16_f32 %0, %1, %2" : "=v"(ho[0][1]) : "v"(pr[0][2]), "v"(pr[0][3]) : ); SB();
  asm volatile("v_mfma_f32_16x16x32_bf16 %0, v[212:215], a[136:139], %0" : "+v"(g[1][2][1]) :  : ); SB();
  asm volatile("buffer_load_dwordx4 v[192:195], %0, %1, %2 offen offset:0 sc1" :  : "v"(vB), "s"(rWS), "s"(4096u) : "memory"); SB();
  asm volatile("v_cvt_pk_bf16_f32 %0, %1, %2" : "=v"(ho[0][2]) : "v"(pr[1][0]), "v"(pr[1][1]) : ); SB();
  asm volatile("v_mfma_f32_16x16x32_bf16 %0, v[212:215], a[140:143], %0" : "+v"(g[1][3][1]) :  : ); SB();
  asm volatile("v_cvt_pk_bf16_f32 %0, %1, %2" : "=v"(ho[0][3]) : "v"(pr[1][2]), "v"(pr[1][3]) : ); SB();
  asm volatile("s_waitcnt vmcnt(18)" :  :  : "memory"); SB();
  asm volatile("v_mfma_f32_16x16x32_bf16 %0, v[216:219], a[144:147], %0" : "+v"(g[1][0][0]) :  : ); SB();
  asm volatile("v_mul_f32 %0, %0, %1" : "+v"(a1[1][1][0]) : "v"(gt[0]) : ); SB();
  asm volatile("v_mfma_f32_16x16x32_bf16 %0, v[216:219], a[148:151], %0" : "+v"(g[1][1][0]) :  : ); SB();
  asm volatile("v_mul_f32 %0, %0, %1" : "+v"(a1[1][1][1]) : "v"(gt[1]) : ); SB();
  asm volatile("v_mfma_f32_16x16x32_bf16 %0, v[216:219], a[152:155], %0" : "+v"(g[1][2][0]) :  : ); SB();
  asm volatile("buffer_store_dwordx4 %1, %0, %2, %3 offen offset:0 sc1\n\ts_nop 1" :  : "v"(vH_), "v"(ho[0]), "s"(rWS), "s"(0u) : "memory"); SB();
  asm volatile("v_mul_f32 %0, %0, %1" : "+v"(a1[1][1][2]) : "v"(gt[2]) : ); SB();
  asm volatile("v_mfma_f32_16x16x32_bf16 %0, v[216:219], a[156:159], %0" : "+v"(g[1][3][0]) :  : ); SB();
  asm volatile("v_mul_f32 %0, %0, %1" : "+v"(a1[1][1][3]) : "v"(gt[3]) : ); SB();
  asm volatile("v_mfma_f32_16x16x32_bf16 %0, v[220:223], a[144:147], %0" : "+v"(g[1][0][1]) :  : ); SB();
  asm volatile("v_mul_f32 %0, %1, %1" : "=v"(gt[0]) : "v"(a1[1][2][0]) : ); SB();
  asm volatile("v_mfma_f32_16x16x32_bf16 %0, v[220:223], a[148:151], %0" : "+v"(g[1][1][1]) :  : ); SB();
  asm volatile("buffer_load_dwordx4 v[196:199], %0, %1, %2 offen offset:1024 sc1" :  : "v"(vB), "s"(rWS), "s"(4096u) : "memory"); SB();
  asm volatile("v_mul_f32 %0, %1, %1" : "=v"(gt[1]) : "v"(a1[1][2][1]) : ); SB();
  asm volatile("v_mfma_f32_16x16x32_bf16 %0, v[220:223], a[152:155], %0" : "+v"(g[1][2][1]) :  : ); SB();
  asm volatile("v_mul_f32 %0, %1, %1" : "=v"(gt[2]) : "v"(a1[1][2][2]) : ); SB();
  asm volatile("v_mfma_f32_16x16x32_bf16 %0, v[220:223], a[156:159], %0" : "+v"(g[1][3][1]) :  : ); SB();
  asm volatile("v_mul_f32 %0, %1, %1" : "=v"(gt[3]) : "v"(a1[1][2][3]) : ); SB();
  ST(29);
  asm volatile("s_waitcnt vmcnt(17)" :  :  : "memory"); SB();
  asm volatile("v_mfma_f32_16x16x32_bf16 %0, v[224:227], a[160:163], %1" : "=&v"(g[0][0][0]) : "v"(bs4) : ); SB();
  asm volatile("s_barrier" :  :  : "memory"); SB();
  asm volatile("buffer_load_dwordx4 v[200:203], %0, %1, %2 offen offset:0 sc1" :  : "v"(vB), "s"(rWS), "s"(69632u) : "memory"); SB();
  asm volatile("v_mfma_f32_16x16x32_bf16 %0, v[224:227], a[164:167], 0" : "=&v"(g[0][1][0]) :  : ); SB();
  asm volatile("v_fma_f32 %0, %0, %1, %2" : "+v"(gt[0]) : "v"(k2[0]), "v"(k1[0]) : ); SB();
  asm volatile("v_mfma_f32_16x16x32_bf16 %0, v[224:227], a[168:171], 0" : "=&v"(g[0][2][0]) :  : ); SB();
  asm volatile("ds_write_b128 %0, %1 offset:0" :  : "v"(pw2), "v"(g[1][1][0]) : "memory"); SB();
  asm volatile("v_mfma_f32_16x16x32_bf16 %0, v[224:227], a[172:175], 0" : "=&v"(g[0][3][0]) :  : ); SB();
  asm volatile("buffer_load_dwordx4 v[204:207], %0, %1, %2 offen offset:1024 sc1" :  : "v"(vB), "s"(rWS), "s"(69632u) : "memory"); SB();
  asm volatile("v_fma_f32 %0, %0, %1, %2" : "+v"(gt[1]) : "v"(k2[0]), "v"(k1[0]) : ); SB();
  asm volatile("v_mfma_f32_16x16x32_bf16 %0, v[228:231], a[160:163], %1" : "=&v"(g[0][0][1]) : "v"(bs4) : ); SB();
  asm volatile("v_xor_b32 %0, 131072, %0" : "+v"(vB) :  : "memory"); SB();
  asm volatile("v_fma_f32 %0, %0, %1, %2" : "+v"(gt[2]) : "v"(k2[0]), "v"(k1[0]) : ); SB();
  asm volatile("v_mfma_f32_16x16x32_bf16 %0, v[228:231], a[164:167], 0" : "=&v"(g[0][1][1]) :  : ); SB();
  asm volatile("ds_write_b128 %0, %1 offset:1024" :  : "v"(pw2), "v"(g[1][1][1]) : "memory"); SB();
  asm volatile("v_mfma_f32_16x16x32_bf16 %0, v[228:231], a[168:171], 0" : "=&v"(g[0][2][1]) :  : ); SB();
  asm volatile("buffer_load_dwordx4 v[208:211], %0, %1, %2 offen offset:0 sc1" :  : "v"(vB), "s"(rWS), "s"(4096u) : "memory"); SB();
  asm volatile("v_fma_f32 %0, %0, %1, %2" : "+v"(gt[3]) : "v"(k2[0]), "v"(k1[0]) : ); SB();
  asm volatile("v_mfma_f32_16x16x32_bf16 %0, v[228:231], a[172:175], 0" : "=&v"(g[0][3][1]) :  : ); SB();
  asm volatile("v_mul_f32 %0, %0, %1" : "+v"(gt[0]) : "v"(a1[1][2][0]) : ); SB();
  asm volatile("s_waitcnt vmcnt(18)" :  :  : "memory"); SB();
  asm volatile("v_mfma_f32_16x16x32_bf16 %0, v[232:235], a[176:179], %0" : "+v"(g[0][0][0]) :  : ); SB();
  asm volatile("ds_write_b128 %0, %1 offset:2048" :  : "v"(pw2), "v"(g[1][2][0]) : "memory"); SB();
  asm volatile("v_mfma_f32_16x16x32_bf16 %0, v[232:235], a[180:183], %0" : "+v"(g[0][1][0]) :  : ); SB();
  asm volatile("buffer_load_dwordx4 v[212:215], %0, %1, %2 offen offset:1024 sc1" :  : "v"(vB), "s"(rWS), "s"(4096u) : "memory"); SB();
  asm volatile("v_mul_f32 %0, %0, %1" : "+v"(gt[1]) : "v"(a1[1][2][1]) : ); SB();
  asm volatile("v_mfma_f32_16x16x32_bf16 %0, v[232:235], a[184:187], %0" : "+v"(g[0][2][0]) :  : ); SB();
  asm volatile("buffer_load_dwordx4 v[216:219], %0, %1, %2 offen offset:0 sc1" :  : "v"(vB), "s"(rWS), "s"(69632u) : "memory"); SB();
  asm volatile("v_mul_f32 %0, %0, %1" : "+v"(gt[2]) : "v"(a1[1][2][2]) : ); SB();
  asm volatile("v_mfma_f32_16x16x32_bf16 %0, v[232:235], a[188:191], %0" : "+v"(g[0][3][0]) :  : ); SB();
  asm volatile("ds_write_b128 %0, %1 offset:3072" :  : "v"(pw2), "v"(g[1][2][1]) : "memory"); SB();
  asm volatile("v_mfma_f32_16x16x32_bf16 %0, v[236:239], a[176:179], %0" : "+v"(g[0][0][1]) :  : ); SB();
  asm volatile("buffer_load_dwordx4 v[220:223], %0, %1, %2 offen offset:1024 sc1" :  : "v"(vB), "s"(rWS), "s"(69632u) : "memory"); SB();
  asm volatile("v_mul_f32 %0, %0, %1" : "+v"(gt[3]) : "v"(a1[1][2][3]) : ); SB();
  asm volatile("v_mfma_f32_16x16x32_bf16 %0, v[236:239], a[180:183], %0" : "+v"(g[0][1][1]) :  : ); SB();
  asm volatile("v_xor_b32 %0, 393216, %0" : "+v"(vB) :  : "memory"); SB();
  asm volatile("v_exp_f32 %0, %1" : "=v"(ge[0]) : "v"(gt[0]) : ); SB();
  asm volatile("v_mfma_f32_16x16x32_bf16 %0, v[236:239], a[184:187], %0" : "+v"(g[0][2][1]) :  : ); SB();
  asm volatile("ds_write_b128 %0, %1 offset:4096" :  : "v"(pw2), "v"(g[1][3][0]) : "memory"); SB();
  asm volatile("v_mfma_f32_16x16x32_bf16 %0, v[236:239], a[188:191], %0" : "+v"(g[0][3][1]) :  : ); SB();
  asm volatile("buffer_load_dwordx4 v[224:227], %0, %1, %2 offen offset:0 sc1" :  : "v"(vB), "s"(rWS), "s"(4096u) : "memory"); SB();
  asm volatile("s_waitcnt vmcnt(20)" :  :  : "memory"); SB();
  asm volatile("v_mfma_f32_16x16x32_bf16 %0, v[240:243], a[192:195], %0" : "+v"(g[0][0][0]) :  : ); SB();
  asm volatile("v_exp_f32 %0, %1" : "=v"(ge[1]) : "v"(gt[1]) : ); SB();
  asm volatile("v_mfma_f32_16x16x32_bf16 %0, v[240:243], a[196:199], %0" : "+v"(g[0][1][0]) :  : ); SB();
  asm volatile("ds_write_b128 %0, %1 offset:5120" :  : "v"(pw2), "v"(g[1][3][1]) : "memory"); SB();
  asm volatile("v_mfma_f32_16x16x32_bf16 %0, v[240:243], a[200:203], %0" : "+v"(g[0][2][0]) :  : ); SB();
  asm volatile("buffer_load_dwordx4 v[228:231], %0, %1, %2 offen offset:1024 sc1" :  : "v"(vB), "s"(rWS), "s"(4096u) : "memory"); SB();
  asm volatile("v_exp_f32 %0, %1" : "=v"(ge[2]) : "v"(gt[2]) : ); SB();
  asm volatile("v_mfma_f32_16x16x32_bf16 %0, v[240:243], a[204:207], %0" : "+v"(g[0][3][0]) :  : ); SB();
  asm volatile("v_exp_f32 %0, %1" : "=v"(ge[3]) : "v"(gt[3]) : ); SB();
  asm volatile("v_mfma_f32_16x16x32_bf16 %0, v[244:247], a[192:195], %0" : "+v"(g[0][0][1]) :  : ); SB();
  asm volatile("s_waitcnt lgkmcnt(0)\n\ts_barrier" :  :  : "memory"); SB();
  asm volatile("ds_read_b128 %0, %1 offset:0" : "=&v"(pr[0]) : "v"(pe[0]) : "memory"); SB();
  asm volatile("v_mfma_f32_16x16x32_bf16 %0, v[244:247], a[196:199], %0" : "+v"(g[0][1][1]) :  : ); SB();
  asm volatile("ds_read_b128 %0, %1 offset:1024" : "=&v"(pr[1]) : "v"(pe[0]) : "memory"); SB();
  asm volatile("v_fma_f32 %0, %0, %1, %1" : "+v"(ge[0]) : "v"(xd[0]) : ); SB();
  asm volatile("v_mfma_f32_16x16x32_bf16 %0, v[244:247], a[200:203], %0" : "+v"(g[0][2][1]) :  : ); SB();
  asm volatile("ds_read_b128 %0, %1 offset:2048" : "=&v"(pr[2]) : "v"(pe[1]) : "memory"); SB();
  asm volatile("v_fma_f32 %0, %0, %1, %1" : "+v"(ge[1]) : "v"(xd[0]) : ); SB();
  asm volatile("v_mfma_f32_16x16x32_bf16 %0, v[244:247], a[204:207], %0" : "+v"(g[0][3][1]) :  : ); SB();
  asm volatile("ds_read_b128 %0, %1 offset:3072" : "=&v"(pr[3]) : "v"(pe[1]) : "memory"); SB();
  asm volatile("s_waitcnt vmcnt(19)" :  :  : "memory"); SB();
  asm volatile("v_mfma_f32_16x16x32_bf16 %0, v[248:251], a[208:211], %0" : "+v"(g[0][0][0]) :  : ); SB();
  asm volatile("buffer_load_dwordx4 v[232:235], %0, %1, %2 offen offset:0 sc1" :  : "v"(vB), "s"(rWS), "s"(69632u) : "memory"); SB();
  asm volatile("v_fma_f32 %0, %0, %1, %1" : "+v"(ge[2]) : "v"(xd[0]) : ); SB();
  asm volatile("v_mfma_f32_16x16x32_bf16 %0, v[248:251], a[212:215], %0" : "+v"(g[0][1][0]) :  : ); SB();
  asm volatile("ds_read_b128 %0, %1 offset:4096" : "=&v"(pr[4]) : "v"(pe[2]) : "memory"); SB();
  asm volatile("v_fma_f32 %0, %0, %1, %1" : "+v"(ge[3]) : "v"(xd[0]) : ); SB();
  asm volatile("v_mfma_f32_16x16x32_bf16 %0, v[248:251], a[216:219], %0" : "+v"(g[0][2][0]) :  : ); SB();
  asm volatile("ds_read_b128 %0, %1 offset:5120" : "=&v"(pr[5]) : "v"(pe[2]) : "memory"); SB();
  asm volatile("v_rcp_f32 %0, %1" : "=v"(gt[0]) : "v"(ge[0]) : ); SB();
  asm volatile("v_mfma_f32_16x16x32_bf16 %0, v[248:251], a[220:223], %0" : "+v"(g[0][3][0]) :  : ); SB();
  asm volatile("buffer_load_dwordx4 v[236:239], %0, %1, %2 offen offset:1024 sc1" :  : "v"(vB), "s"(rWS), "s"(69632u) : "memory"); SB();
  asm volatile("v_rcp_f32 %0, %1" : "=v"(gt[1]) : "v"(ge[1]) : ); SB();
  asm volatile("v_mfma_f32_16x16x32_bf16 %0, v[252:255], a[208:211], %0" : "+v"(g[0][0][1]) :  : ); SB();
  asm volatile("v_xor_b32 %0, 131072, %0" : "+v"(vB) :  : "memory"); SB();
  asm volatile("v_rcp_f32 %0, %1" : "=v"(gt[2]) : "v"(ge[2]) : ); SB();
  asm volatile("v_mfma_f32_16x16x32_bf16 %0, v[252:255], a[212:215], %0" : "+v"(g[0][1][1]) :  : ); SB();
  asm volatile("buffer_load_dwordx4 v[240:243], %0, %1, %2 offen offset:0 sc1" :  : "v"(vB), "s"(rWS), "s"(4096u) : "memory"); SB();
  asm volatile("v_rcp_f32 %0, %1" : "=v"(gt[3]) : "v"(ge[3]) : ); SB();
  asm volatile("v_mfma_f32_16x16x32_bf16 %0, v[252:255], a[216:219], %0" : "+v"(g[0][2][1]) :  : ); SB();
  asm volatile("v_mul_f32 %0, %0, %1" : "+v"(a1[1][2][0]) : "v"(gt[0]) : ); SB();
  asm volatile("v_mfma_f32_16x16x32_bf16 %0, v[252:255], a[220:223], %0" : "+v"(g[0][3][1]) :  : ); SB();
  asm volatile("buffer_load_dwordx4 v[244:247], %0, %1, %2 offen offset:1024 sc1" :  : "v"(vB), "s"(rWS), "s"(4096u) : "memory"); SB();
  asm volatile("s_waitcnt vmcnt(21)" :  :  : "memory"); SB();
  asm volatile("v_mfma_f32_16x16x32_bf16 %0, v[160:163], a[224:227], %0" : "+v"(g[0][0][0]) :  : ); SB();
  asm volatile("s_waitcnt lgkmcnt(0)" : "+v"(pr[0]), "+v"(pr[1]), "+v"(pr[2]), "+v"(pr[3]), "+v"(pr[4]), "+v"(pr[5]) :  : "memory"); SB();
  asm volatile("v_add_f32 %0, %0, %1" : "+v"(pr[0][0]) : "v"(g[1][0][0][0]) : ); SB();
  asm volatile("v_mfma_f32_16x16x32_bf16 %0, v[160:163], a[228:231], %0" : "+v"(g[0][1][0]) :  : ); SB();
  asm volatile("v_add_f32 %0, %0, %1" : "+v"(pr[0][1]) : "v"(g[1][0][0][1]) : ); SB();
  asm volatile("v_mfma_f32_16x16x32_bf16 %0, v[160:163], a[232:235], %0" : "+v"(g[0][2][0]) :  : ); SB();
  asm volatile("v_add_f32 %0, %0, %1" : "+v"(pr[0][2]) : "v"(g[1][0][0][2]) : ); SB();
  asm volatile("v_mfma_f32_16x16x32_bf16 %0, v[160:163], a[236:239], %0" : "+v"(g[0][3][0]) :  : ); SB();
  asm volatile("v_add_f32 %0, %0, %1" : "+v"(pr[0][3]) : "v"(g[1][0][0][3]) : ); SB();
  asm volatile("v_mfma_f32_16x16x32_bf16 %0, v[164:167], a[224:227], %0" : "+v"(g[0][0][1]) :  : ); SB();
  asm volatile("v_add_f32 %0, %0, %1" : "+v"(pr[1][0]) : "v"(g[1][0][1][0]) : ); SB();
  asm volatile("v_mfma_f32_16x16x32_bf16 %0, v[164:167], a[228:231], %0" : "+v"(g[0][1][1]) :  : ); SB();
  asm volatile("v_add_f32 %0, %0, %1" : "+v"(pr[1][1]) : "v"(g[1][0][1][1]) : ); SB();
  asm volatile("v_mfma_f32_16x16x32_bf16 %0, v[164:167], a[232:235], %0" : "+v"(g[0][2][1]) :  : ); SB();
  asm volatile("v_add_f32 %0, %0, %1" : "+v"(pr[1][2]) : "v"(g[1][0][1][2]) : ); SB();
  asm volatile("v_mfma_f32_16x16x32_bf16 %0, v[164:167], a[236:239], %0" : "+v"(g[0][3][1]) :  : ); SB();
  asm volatile("v_add_f32 %0, %0, %1" : "+v"(pr[1][3]) : "v"(g[1][0][1][3]) : ); SB();
  asm volatile("s_waitcnt vmcnt(19)" :  :  : "memory"); SB();
  asm volatile("v_mfma_f32_16x16x32_bf16 %0, v[168:171], a[240:243], %0" : "+v"(g[0][0][0]) :  : ); SB();
  asm volatile("v_add_f32 %0, %0, %1" : "+v"(pr[2][0]) : "v"(pr[4][0]) : ); SB();
  asm volatile("v_mfma_f32_16x16x32_bf16 %0, v[168:171], a[244:247], %0" : "+v"(g[0][1][0]) :  : ); SB();
  asm volatile("v_add_f32 %0, %0, %1" : "+v"(pr[2][1]) : "v"(pr[4][1]) : ); SB();
  asm volatile("v_mfma_f32_16x16x32_bf16 %0, v[168:171], a[248:251], %0" : "+v"(g[0][2][0]) :  : ); SB();
  asm volatile("v_add_f32 %0, %0, %1" : "+v"(pr[2][2]) : "v"(pr[4][2]) : ); SB();
  asm volatile("v_mfma_f32_16x16x32_bf16 %0, v[168:171], a[252:255], %0" : "+v"(g[0][3][0]) :  : ); SB();
  asm volatile("s_add_u32 m0, %0, 60032" :  : "s"(mU) : "memory", "scc"); SB();
  asm volatile("v_add_f32 %0, %0, %1" : "+v"(pr[2][3]) : "v"(pr[4][3]) : ); SB();
  asm volatile("v_mfma_f32_16x16x32_bf16 %0, v[172:175], a[240:243], %0" : "+v"(g[0][0][1]) :  : ); SB();
  asm volatile("buffer_load_dwordx4 %0, %1, %2 offen sc1 lds" :  : "v"(vF), "s"(rWS), "s"(W_KV) : "memory"); SB();
  asm volatile("v_add_f32 %0, %0, %1" : "+v"(pr[3][0]) : "v"(pr[5][0]) : ); SB();
  asm volatile("v_mfma_f32_16x16x32_bf16 %0, v[172:175], a[244:247], %0" : "+v"(g[0][1][1]) :  : ); SB();
  asm volatile("v_add_f32 %0, %0, %1" : "+v"(pr[3][1]) : "v"(pr[5][1]) : ); SB();
  asm volatile("v_mfma_f32_16x16x32_bf16 %0, v[172:175], a[248:251], %0" : "+v"(g[0][2][1]) :  : ); SB();
  asm volatile("v_add_f32 %0, %0, %1" : "+v"(pr[3][2]) : "v"(pr[5][2]) : ); SB();
  asm volatile("v_mfma_f32_16x16x32_bf16 %0, v[172:175], a[252:255], %0" : "+v"(g[0][3][1]) :  : ); SB();
  asm volatile("s_add_u32 m0, %0, 72960" :  : "s"(mU) : "memory", "scc"); SB();
  asm volatile("v_add_f32 %0, %0, %1" : "+v"(pr[3][3]) : "v"(pr[5][3]) : ); SB();
  asm volatile("s_waitcnt vmcnt(18)" :  :  : "memory"); SB();
  asm volatile("v_mfma_f32_16x16x32_bf16 %0, v[176:179], a[0:3], %0" : "+v"(g[0][0][0]) :  : ); SB();
  asm volatile("buffer_load_dwordx4 %0, %1, %2 offen sc1 lds" :  : "v"(vF), "s"(rWS), "s"(W_KV + 4096) : "memory"); SB();
  asm volatile("v_add_f32 %0, %0, %1" : "+v"(pr[0][0]) : "v"(pr[2][0]) : ); SB();
  asm volatile("v_mfma_f32_16x16x32_bf16 %0, v[176:179], a[4:7], %0" : "+v"(g[0][1][0]) :  : ); SB();
  asm volatile("v_add_f32 %0, %0, %1" : "+v"(pr[0][1]) : "v"(pr[2][1]) : ); SB();
  asm volatile("v_mfma_f32_16x16x32_bf16 %0, v[176:179], a[8:11], %0" : "+v"(g[0][2][0]) :  : ); SB();
  asm volatile("buffer_load_dwordx4 v[248:251], %0, %1, %2 offen offset:0 sc1" :  : "v"(vB), "s"(rWS), "s"(69632u) : "memory"); SB();
  asm volatile("v_add_f32 %0, %0, %1" : "+v"(pr[0][2]) : "v"(pr[2][2]) : ); SB();
  asm volatile("v_mfma_f32_16x16x32_bf16 %0, v[176:179], a[12:15], %0" : "+v"(g[0][3][0]) :  : ); SB();
  asm volatile("v_add_f32 %0, %0, %1" : "+v"(pr[0][3]) : "v"(pr[2][3]) : ); SB();
  asm volatile("v_mfma_f32_16x16x32_bf16 %0, v[180:183], a[0:3], %0" : "+v"(g[0][0][1]) :  : ); SB();
  asm volatile("v_add_f32 %0, %0, %1" : "+v"(pr[1][0]) : "v"(pr[3][0]) : ); SB();
  asm volatile("v_mfma_f32_16x16x32_bf16 %0, v[180:183], a[4:7], %0" : "+v"(g[0][1][1]) :  : ); SB();
  asm volatile("v_add_f32 %0, %0, %1" : "+v"(pr[1][1]) : "v"(pr[3][1]) : ); SB();
  asm volatile("v_mfma_f32_16x16x32_bf16 %0, v[180:183], a[8:11], %0" : "+v"(g[0][2][1]) :  : ); SB();
  asm volatile("buffer_load_dwordx4 v[252:255], %0, %1, %2 offen offset:1024 sc1" :  : "v"(vB), "s"(rWS), "s"(69632u) : "memory"); SB();
  asm volatile("v_add_f32 %0, %0, %1" : "+v"(pr[1][2]) : "v"(pr[3][2]) : ); SB();
  asm volatile("v_mfma_f32_16x16x32_bf16 %0, v[180:183], a[12:15], %0" : "+v"(g[0][3][1]) :  : ); SB();
  asm volatile("v_add_f32 %0, %0, %1" : "+v"(pr[1][3]) : "v"(pr[3][3]) : ); SB();
  asm volatile("s_waitcnt vmcnt(19)" :  :  : "memory"); SB();
  asm volatile("v_mfma_f32_16x16x32_bf16 %0, v[184:187], a[16:19], %0" : "+v"(g[0][0][0]) :  : ); SB();
  asm volatile("v_mul_f32 %0, %0, %1" : "+v"(pr[0][0]) : "v"(a1[0][2][0]) : ); SB();
  asm volatile("v_mfma_f32_16x16x32_bf16 %0, v[184:187], a[20:23], %0" : "+v"(g[0][1][0]) :  : ); SB();
  asm volatile("buffer_load_dwordx4 a[160:163], %0, %1, %2 offen offset:0 sc1" :  : "v"(vO), "s"(rWS), "s"(0u) : "memory"); SB();
  asm volatile("v_mul_f32 %0, %0, %1" : "+v"(pr[0][1]) : "v"(a1[0][2][1]) : ); SB();
  asm volatile("v_mfma_f32_16x16x32_bf16 %0, v[184:187], a[24:27], %0" : "+v"(g[0][2][0]) :  : ); SB();
  asm volatile("v_mul_f32 %0, %0, %1" : "+v"(pr[0][2]) : "v"(a1[0][2][2]) : ); SB();
  asm volatile("v_mfma_f32_16x16x32_bf16 %0, v[184:187], a[28:31], %0" : "+v"(g[0][3][0]) :  : ); SB();
  asm volatile("v_mul_f32 %0, %0, %1" : "+v"(pr[0][3]) : "v"(a1[0][2][3]) : ); SB();
  asm volatile("v_mfma_f32_16x16x32_bf16 %0, v[188:191], a[16:19], %0" : "+v"(g[0][0][1]) :  : ); SB();
  asm volatile("v_mul_f32 %0, %0, %1" : "+v"(pr[1][0]) : "v"(a1[1][0][0]) : ); SB();
  asm volatile("v_mfma_f32_16x16x32_bf16 %0, v[188:191], a[20:23], %0" : "+v"(g[0][1][1]) :  : ); SB();
  asm volatile("buffer_load_dwordx4 a[164:167], %0, %1, %2 offen offset:1024 sc1" :  : "v"(vO), "s"(rWS), "s"(0u) : "memory"); SB();
  asm volatile("v_mul_f32 %0, %0, %1" : "+v"(pr[1][1]) : "v"(a1[1][0][1]) : ); SB();
  asm volatile("v_mfma_f32_16x16x32_bf16 %0, v[188:191], a[24:27], %0" : "+v"(g[0][2][1]) :  : ); SB();
  asm volatile("v_mul_f32 %0, %0, %1" : "+v"(pr[1][2]) : "v"(a1[1][0][2]) : ); SB();
  asm volatile("v_mfma_f32_16x16x32_bf16 %0, v[188:191], a[28:31], %0" : "+v"(g[0][3][1]) :  : ); SB();
  asm volatile("v_mul_f32 %0, %0, %1" : "+v"(pr[1][3]) : "v"(a1[1][0][3]) : ); SB();
  asm volatile("s_waitcnt vmcnt(18)" :  :  : "memory"); SB();
  asm volatile("v_mfma_f32_16x16x32_bf16 %0, v[192:195], a[32:35], %0" : "+v"(g[0][0][0]) :  : ); SB();
  asm volatile("buffer_load_dwordx4 a[168:171], %0, %1, %2 offen offset:2048 sc1" :  : "v"(vO), "s"(rWS), "s"(0u) : "memory"); SB();
  asm volatile("v_cvt_pk_bf16_f32 %0, %1, %2" : "=v"(ho[1][0]) : "v"(pr[0][0]), "v"(pr[0][1]) : ); SB();
  asm volatile("v_mfma_f32_16x16x32_bf16 %0, v[192:195], a[36:39], %0" : "+v"(g[0][1][0]) :  : ); SB();
  asm volatile("v_cvt_pk_bf16_f32 %0, %1, %2" : "=v"(ho[1][1]) : "v"(pr[0][2]), "v"(pr[0][3]) : ); SB();
  asm volatile("v_mfma_f32_16x16x32_bf16 %0, v[192:195], a[40:43], %0" : "+v"(g[0][2][0]) :  : ); SB();
  asm volatile("v_cvt_pk_bf16_f32 %0, %1, %2" : "=v"(ho[1][2]) : "v"(pr[1][0]), "v"(pr[1][1]) : ); SB();
  asm volatile("v_mfma_f32_16x16x32_bf16 %0, v[192:195], a[44:47], %0" : "+v"(g[0][3][0]) :  : ); SB();
  asm volatile("v_cvt_pk_bf16_f32 %0, %1, %2" : "=v"(ho[1][3]) : "v"(pr[1][2]), "v"(pr[1][3]) : ); SB();
  asm volatile("v_mfma_f32_16x16x32_bf16 %0, v[196:199], a[32:35], %0" : "+v"(g[0][0][1]) :  : ); SB();
  asm volatile("buffer_store_dwordx4 %1, %0, %2, %3 offen offset:1024 sc1\n\ts_nop 1" :  : "v"(vH_), "v"(ho[1]), "s"(rWS), "s"(0u) : "memory"); SB();
  asm volatile("v_mul_f32 %0, %0, %1" : "+v"(a1[1][2][1]) : "v"(gt[1]) : ); SB();
  asm volatile("v_mfma_f32_16x16x32_bf16 %0, v[196:199], a[36:39], %0" : "+v"(g[0][1][1]) :  : ); SB();
  asm volatile("v_mul_f32 %0, %0, %1" : "+v"(a1[1][2][2]) : "v"(gt[2]) : ); SB();
  asm volatile("v_mfma_f32_16x16x32_bf16 %0, v[196:199], a[40:43], %0" : "+v"(g[0][2][1]) :  : ); SB();
  asm volatile("v_mul_f32 %0, %0, %1" : "+v"(a1[1][2][3]) : "v"(gt[3]) : ); SB();
  asm volatile("v_mfma_f32_16x16x32_bf16 %0, v[196:199], a[44:47], %0" : "+v"(g[0][3][1]) :  : ); SB();
  asm volatile("buffer_load_dwordx4 a[172:175], %0, %1, %2 offen offset:3072 sc1" :  : "v"(vO), "s"(rWS), "s"(0u) : "memory"); SB();
  asm volatile("s_waitcnt vmcnt(19)" :  :  : "memory"); SB();
  asm volatile("v_mfma_f32_16x16x32_bf16 %0, v[200:203], a[48:51], %0" : "+v"(g[0][0][0]) :  : ); SB();
  asm volatile("v_mfma_f32_16x16x32_bf16 %0, v[200:203], a[52:55], %0" : "+v"(g[0][1][0]) :  : ); SB();
  asm volatile("v_mfma_f32_16x16x32_bf16 %0, v[200:203], a[56:59], %0" : "+v"(g[0][2][0]) :  : ); SB();
  asm volatile("v_mfma_f32_16x16x32_bf16 %0, v[200:203], a[60:63], %0" : "+v"(g[0][3][0]) :  : ); SB();
  asm volatile("buffer_load_dwordx4 a[176:179], %0, %1, %2 offen offset:0 sc1" :  : "v"(vO), "s"(rWS), "s"(4096u) : "memory"); SB();
  asm volatile("v_mfma_f32_16x16x32_bf16 %0, v[204:207], a[48:51], %0" : "+v"(g[0][0][1]) :  : ); SB();
  asm volatile("v_mfma_f32_16x16x32_bf16 %0, v[204:207], a[52:55], %0" : "+v"(g[0][1][1]) :  : ); SB();
  asm volatile("v_mfma_f32_16x16x32_bf16 %0, v[204:207], a[56:59], %0" : "+v"(g[0][2][1]) :  : ); SB();
  asm volatile("buffer_load_dwordx4 a[180:183], %0, %1, %2 offen offset:1024 sc1" :  : "v"(vO), "s"(rWS), "s"(4096u) : "memory"); SB();
  asm volatile("v_mfma_f32_16x16x32_bf16 %0, v[204:207], a[60:63], %0" : "+v"(g[0][3][1]) :  : ); SB();
  asm volatile("s_waitcnt vmcnt(19)" :  :  : "memory"); SB();
  asm volatile("v_mfma_f32_16x16x32_bf16 %0, v[208:211], a[64:67], %0" : "+v"(g[0][0][0]) :  : ); SB();
  asm volatile("ds_read_b128 %0, %1 offset:0" : "=&v"(yq[0]) : "v"(ydr) : "memory"); SB();
  asm volatile("v_mfma_f32_16x16x32_bf16 %0, v[208:211], a[68:71], %0" : "+v"(g[0][1][0]) :  : ); SB();
  asm volatile("ds_read_b128 %0, %1 offset:256" : "=&v"(yq[1]) : "v"(ydr) : "memory"); SB();
  asm volatile("v_mfma_f32_16x16x32_bf16 %0, v[208:211], a[72:75], %0" : "+v"(g[0][2][0]) :  : ); SB();
  asm volatile("ds_read_b128 %0, %1 offset:8192" : "=&v"(yq[2]) : "v"(ydr) : "memory"); SB();
  asm volatile("v_mfma_f32_16x16x32_bf16 %0, v[208:211], a[76:79], %0" : "+v"(g[0][3][0]) :  : ); SB();
  asm volatile("ds_read_b128 %0, %1 offset:8448" : "=&v"(yq[3]) : "v"(ydr) : "memory"); SB();
  asm volatile("v_mfma_f32_16x16x32_bf16 %0, v[212:215], a[64:67], %0" : "+v"(g[0][0][1]) :  : ); SB();
  asm volatile("ds_read_b128 %0, %1 offset:16384" : "=&v"(yq[4]) : "v"(ydr) : "memory"); SB();
  asm volatile("v_mfma_f32_16x16x32_bf16 %0, v[212:215], a[68:71], %0" : "+v"(g[0][1][1]) :  : ); SB();
  asm volatile("ds_read_b128 %0, %1 offset:16640" : "=&v"(yq[5]) : "v"(ydr) : "memory"); SB();
  asm volatile("v_mfma_f32_16x16x32_bf16 %0, v[212:215], a[72:75], %0" : "+v"(g[0][2][1]) :  : ); SB();
  asm volatile("ds_read_b128 %0, %1 offset:24576" : "=&v"(yq[6]) : "v"(ydr) : "memory"); SB();
  asm volatile("v_mfma_f32_16x16x32_bf16 %0, v[212:215], a[76:79], %0" : "+v"(g[0][3][1]) :  : ); SB();
  asm volatile("ds_read_b128 %0, %1 offset:24832" : "=&v"(yq[7]) : "v"(ydr) : "memory"); SB();
  asm volatile("s_waitcnt vmcnt(17)" :  :  : "memory"); SB();
  asm volatile("v_mfma_f32_16x16x32_bf16 %0, v[216:219], a[80:83], %0" : "+v"(g[0][0][0]) :  : ); SB();
  asm volatile("ds_read_b128 %0, %1 offset:25856" : "=&v"(ykv) : "v"(ykr) : "memory"); SB();
  asm volatile("v_mfma_f32_16x16x32_bf16 %0, v[216:219], a[84:87], %0" : "+v"(g[0][1][0]) :  : ); SB();
  asm volatile("buffer_load_dwordx4 a[184:187], %0, %1, %2 offen offset:2048 sc1" :  : "v"(vO), "s"(rWS), "s"(4096u) : "memory"); SB();
  asm volatile("v_mfma_f32_16x16x32_bf16 %0, v[216:219], a[88:91], %0" : "+v"(g[0][2][0]) :  : ); SB();
  asm volatile("buffer_load_dwordx4 a[188:191], %0, %1, %2 offen offset:3072 sc1" :  : "v"(vO), "s"(rWS), "s"(4096u) : "memory"); SB();
  asm volatile("v_mfma_f32_16x16x32_bf16 %0, v[216:219], a[92:95], %0" : "+v"(g[0][3][0]) :  : ); SB();
  asm volatile("v_mfma_f32_16x16x32_bf16 %0, v[220:223], a[80:83], %0" : "+v"(g[0][0][1]) :  : ); SB();
  asm volatile("v_mfma_f32_16x16x32_bf16 %0, v[220:223], a[84:87], %0" : "+v"(g[0][1][1]) :  : ); SB();
  asm volatile("buffer_load_dwordx4 a[192:195], %0, %1, %2 offen offset:0 sc1" :  : "v"(vO), "s"(rWS), "s"(8192u) : "memory"); SB();
  asm volatile("v_mfma_f32_16x16x32_bf16 %0, v[220:223], a[88:91], %0" : "+v"(g[0][2][1]) :  : ); SB();
  asm volatile("v_mfma_f32_16x16x32_bf16 %0, v[220:223], a[92:95], %0" : "+v"(g[0][3][1]) :  : ); SB();
  asm volatile("s_waitcnt vmcnt(12)\n\ts_barrier" :  :  : "memory"); SB();
  asm volatile("s_waitcnt vmcnt(10)" :  :  : "memory"); SB();
  asm volatile("v_mfma_f32_16x16x32_bf16 %0, v[224:227], a[100:103], %0" : "+v"(g[0][1][0]) :  : ); SB();
  asm volatile("buffer_load_dwordx4 a[196:199], %0, %1, %2 offen offset:1024 sc1" :  : "v"(vO), "s"(rWS), "s"(8192u) : "memory"); SB();
  asm volatile("v_mfma_f32_16x16x32_bf16 %0, v[228:231], a[100:103], %0" : "+v"(g[0][1][1]) :  : ); SB();
  asm volatile("ds_read2_b32 a[64:65], %0 offset0:176 offset1:177" :  : "v"(aa[0]) : "memory"); SB();
  asm volatile("v_mfma_f32_16x16x32_bf16 %0, v[232:235], a[116:119], %0" : "+v"(g[0][1][0]) :  : ); SB();
  asm volatile("ds_read2_b32 a[66:67], %0 offset0:178 offset1:179" :  : "v"(aa[0]) : "memory"); SB();
  asm volatile("v_mfma_f32_16x16x32_bf16 %0, v[236:239], a[116:119], %0" : "+v"(g[0][1][1]) :  : ); SB();
  asm volatile("ds_read_b128 a[60:63], %0 offset:3776" :  : "v"(bb) : "memory"); SB();
  asm volatile("v_mfma_f32_16x16x32_bf16 %0, v[240:243], a[132:135], %0" : "+v"(g[0][1][0]) :  : ); SB();
  asm volatile("buffer_load_dwordx4 a[200:203], %0, %1, %2 offen offset:0 sc1" :  : "v"(vO2), "s"(rWS), "s"(0u) : "memory"); SB();
  asm volatile("v_mfma_f32_16x16x32_bf16 %0, v[244:247], a[132:135], %0" : "+v"(g[0][1][1]) :  : ); SB();
  asm volatile("ds_read_b128 a[56:59], %0 offset:3264" :  : "v"(bb) : "memory"); SB();
  asm volatile("v_mfma_f32_16x16x32_bf16 %0, v[248:251], a[148:151], %0" : "+v"(g[0][1][0]) :  : ); SB();
  asm volatile("ds_read_b128 a[52:55], %0 offset:2752" :  : "v"(bb) : "memory"); SB();
  asm volatile("v_mfma_f32_16x16x32_bf16 %0, v[252:255], a[148:151], %0" : "+v"(g[0][1][1]) :  : ); SB();
  asm volatile("buffer_load_dwordx4 a[204:207], %0, %1, %2 offen offset:1024 sc1" :  : "v"(vO2), "s"(rWS), "s"(0u) : "memory"); SB();
  asm volatile("v_mfma_f32_16x16x32_bf16 %0, v[224:227], a[104:107], %0" : "+v"(g[0][2][0]) :  : ); SB();
  asm volatile("ds_read_b128 a[48:51], %0 offset:2240" :  : "v"(bb) : "memory"); SB();
  asm volatile("v_mfma_f32_16x16x32_bf16 %0, v[228:231], a[104:107], %0" : "+v"(g[0][2][1]) :  : ); SB();
  asm volatile("ds_read2_b32 a[68:69], %0 offset0:48 offset1:49" :  : "v"(aa[0]) : "memory"); SB();
  asm volatile("v_mfma_f32_16x16x32_bf16 %0, v[232:235], a[120:123], %0" : "+v"(g[0][2][0]) :  : ); SB();
  asm volatile("ds_write_b128 %0, %1 offset:0" :  : "v"(pw2), "v"(g[0][1][0]) : "memory"); SB();
  asm volatile("v_mfma_f32_16x16x32_bf16 %0, v[236:239], a[120:123], %0" : "+v"(g[0][2][1]) :  : ); SB();
  asm volatile("buffer_load_dwordx4 a[208:211], %0, %1, %2 offen offset:2048 sc1" :  : "v"(vO2), "s"(rWS), "s"(0u) : "memory"); SB();
  asm volatile("v_mfma_f32_16x16x32_bf16 %0, v[240:243], a[136:139], %0" : "+v"(g[0][2][0]) :  : ); SB();
  asm volatile("ds_read2_b32 a[70:71], %0 offset0:50 offset1:51" :  : "v"(aa[0]) : "memory"); SB();
  asm volatile("v_mfma_f32_16x16x32_bf16 %0, v[244:247], a[136:139], %0" : "+v"(g[0][2][1]) :  : ); SB();
  asm volatile("ds_write_b128 %0, %1 offset:1024" :  : "v"(pw2), "v"(g[0][1][1]) : "memory"); SB();
  asm volatile("v_mfma_f32_16x16x32_bf16 %0, v[248:251], a[152:155], %0" : "+v"(g[0][2][0]) :  : ); SB();
  asm volatile("ds_read_b128 a[44:47], %0 offset:1728" :  : "v"(bb) : "memory"); SB();
  asm volatile("v_mfma_f32_16x16x32_bf16 %0, v[252:255], a[152:155], %0" : "+v"(g[0][2][1]) :  : ); SB();
  asm volatile("ds_read2_b32 a[72:73], %0 offset0:176 offset1:177" :  : "v"(aa[1]) : "memory"); SB();
  asm volatile("v_mfma_f32_16x16x32_bf16 %0, v[224:227], a[108:111], %0" : "+v"(g[0][3][0]) :  : ); SB();
  asm volatile("ds_read2_b32 a[74:75], %0 offset0:178 offset1:179" :  : "v"(aa[1]) : "memory"); SB();
  asm volatile("v_mfma_f32_16x16x32_bf16 %0, v[228:231], a[108:111], %0" : "+v"(g[0][3][1]) :  : ); SB();
  asm volatile("ds_read2_b32 a[76:77], %0 offset0:48 offset1:49" :  : "v"(aa[1]) : "memory"); SB();
  asm volatile("v_mfma_f32_16x16x32_bf16 %0, v[232:235], a[124:127], %0" : "+v"(g[0][3][0]) :  : ); SB();
  asm volatile("ds_write_b128 %0, %1 offset:2048" :  : "v"(pw2), "v"(g[0][2][0]) : "memory"); SB();
  asm volatile("v_mfma_f32_16x16x32_bf16 %0, v[236:239], a[124:127], %0" : "+v"(g[0][3][1]) :  : ); SB();
  asm volatile("ds_read2_b32 a[78:79], %0 offset0:50 offset1:51" :  : "v"(aa[1]) : "memory"); SB();
  asm volatile("v_mfma_f32_16x16x32_bf16 %0, v[240:243], a[140:143], %0" : "+v"(g[0][3][0]) :  : ); SB();
  asm volatile("ds_read_b128 a[40:43], %0 offset:1216" :  : "v"(bb) : "memory"); SB();
  asm volatile("v_mfma_f32_16x16x32_bf16 %0, v[244:247], a[140:143], %0" : "+v"(g[0][3][1]) :  : ); SB();
  asm volatile("ds_write_b128 %0, %1 offset:3072" :  : "v"(pw2), "v"(g[0][2][1]) : "memory"); SB();
  asm volatile("v_mfma_f32_16x16x32_bf16 %0, v[248:251], a[156:159], %0" : "+v"(g[0][3][0]) :  : ); SB();
  asm volatile("ds_read2_b32 a[80:81], %0 offset0:176 offset1:177" :  : "v"(aa[2]) : "memory"); SB();
  asm volatile("v_mfma_f32_16x16x32_bf16 %0, v[252:255], a[156:159], %0" : "+v"(g[0][3][1]) :  : ); SB();
  asm volatile("ds_read2_b32 a[82:83], %0 offset0:178 offset1:179" :  : "v"(aa[2]) : "memory"); SB();
  asm volatile("v_mfma_f32_16x16x32_bf16 %0, v[224:227], a[96:99], %0" : "+v"(g[0][0][0]) :  : ); SB();
  asm volatile("ds_read2_b32 a[84:85], %0 offset0:48 offset1:49" :  : "v"(aa[2]) : "memory"); SB();
  asm volatile("v_mfma_f32_16x16x32_bf16 %0, v[228:231], a[96:99], %0" : "+v"(g[0][0][1]) :  : ); SB();
  asm volatile("s_waitcnt vmcnt(10)\n\ts_mov_b64 exec, 1\n\tbuffer_store_dwordx2 %0, off, %1, %2 sc1\n\ts_mov_b64 exec, -1" :  : "v"(((u32x2){tg0, tg1})), "s"(rWS), "s"(sFH) : "memory"); SB();
  asm volatile("buffer_load_dwordx4 a[212:215], %0, %1, %2 offen offset:3072 sc1" :  : "v"(vO2), "s"(rWS), "s"(0u) : "memory"); SB();
  asm volatile("v_mfma_f32_16x16x32_bf16 %0, v[232:235], a[112:115], %0" : "+v"(g[0][0][0]) :  : ); SB();
  asm volatile("ds_write_b128 %0, %1 offset:4096" :  : "v"(pw2), "v"(g[0][3][0]) : "memory"); SB();
  asm volatile("v_mfma_f32_16x16x32_bf16 %0, v[236:239], a[112:115], %0" : "+v"(g[0][0][1]) :  : ); SB();
  asm volatile("buffer_load_dwordx4 a[216:219], %0, %1, %2 offen offset:0 sc1" :  : "v"(vO2), "s"(rWS), "s"(4096u) : "memory"); SB();
  asm volatile("v_mfma_f32_16x16x32_bf16 %0, v[240:243], a[128:131], %0" : "+v"(g[0][0][0]) :  : ); SB();
  asm volatile("ds_read2_b32 a[86:87], %0 offset0:50 offset1:51" :  : "v"(aa[2]) : "memory"); SB();
  asm volatile("v_mfma_f32_16x16x32_bf16 %0, v[244:247], a[128:131], %0" : "+v"(g[0][0][1]) :  : ); SB();
  asm volatile("ds_write_b128 %0, %1 offset:5120" :  : "v"(pw2), "v"(g[0][3][1]) : "memory"); SB();
  asm volatile("v_mfma_f32_16x16x32_bf16 %0, v[248:251], a[144:147], %0" : "+v"(g[0][0][0]) :  : ); SB();
  asm volatile("buffer_load_dwordx4 a[220:223], %0, %1, %2 offen offset:1024 sc1" :  : "v"(vO2), "s"(rWS), "s"(4096u) : "memory"); SB();
  asm volatile("v_mfma_f32_16x16x32_bf16 %0, v[252:255], a[144:147], %0" : "+v"(g[0][0][1]) :  : ); SB();
  asm volatile("ds_read_b128 a[36:39], %0 offset:704" :  : "v"(bb) : "memory"); SB();
  if ((rb | cb | w) == 0) asm volatile("buffer_store_dwordx2 %0, off, %1, 0 sc1" :: "v"(((u32x2){tg0, tg1})), "s"(rWS) : "memory");
  SB();
  ST(17);
  unsigned pw3, p3[3], yw, ys[2], vYr, ya, po;
  asm volatile("s_waitcnt lgkmcnt(4)\n\tv_mfma_f32_16x16x32_bf16 %0, a[64:67], a[60:63], 0" : "=&v"(ac[7]) :  : ); SB();
  asm volatile("ds_read2_b32 a[88:89], %0 offset0:176 offset1:177" :  : "v"(aa[3]) : "memory"); SB();
  asm volatile("" : "+v"(yq[0]), "+v"(yq[1]), "+v"(yq[2]), "+v"(yq[3]), "+v"(yq[4]), "+v"(yq[5]), "+v"(yq[6]), "+v"(yq[7]), "+v"(ykv) :  : "memory"); SB();
  asm volatile("v_mfma_f32_16x16x32_bf16 %0, a[64:67], a[56:59], 0" : "=&v"(ac[6]) :  : ); SB();
  asm volatile("ds_read2_b32 a[90:91], %0 offset0:178 offset1:179" :  : "v"(aa[3]) : "memory"); SB();
  asm volatile("buffer_load_dwordx4 a[224:227], %0, %1, %2 offen offset:2048 sc1" :  : "v"(vO2), "s"(rWS), "s"(4096u) : "memory"); SB();
  asm volatile("v_mfma_f32_16x16x32_bf16 %0, a[64:67], a[52:55], 0" : "=&v"(ac[5]) :  : ); SB();
  asm volatile("ds_read2_b32 a[92:93], %0 offset0:48 offset1:49" :  : "v"(aa[3]) : "memory"); SB();
  asm volatile("v_add_f32 %0, %0, %1" : "+v"(yq[0][0]) : "v"(yq[2][0]) : ); SB();
  asm volatile("v_mfma_f32_16x16x32_bf16 %0, a[64:67], a[48:51], 0" : "=&v"(ac[4]) :  : ); SB();
  asm volatile("ds_read2_b32 a[94:95], %0 offset0:50 offset1:51" :  : "v"(aa[3]) : "memory"); SB();
  asm volatile("buffer_load_dwordx4 a[228:231], %0, %1, %2 offen offset:3072 sc1" :  : "v"(vO2), "s"(rWS), "s"(4096u) : "memory"); SB();
  asm volatile("v_mfma_f32_16x16x32_bf16 %0, a[68:71], a[56:59], %0" : "+v"(ac[7]) :  : ); SB();
  asm volatile("ds_read_b128 a[32:35], %0 offset:192" :  : "v"(bb) : "memory"); SB();
  asm volatile("s_waitcnt lgkmcnt(6)\n\ts_barrier" :  :  : "memory"); SB();
  asm volatile("v_mfma_f32_16x16x32_bf16 %0, a[68:71], a[52:55], %0" : "+v"(ac[6]) :  : ); SB();
  asm volatile("ds_read2_b32 a[128:129], %0 offset0:160 offset1:161" :  : "v"(aa[0]) : "memory"); SB();
  asm volatile("buffer_load_dwordx4 a[232:235], %0, %1, %2 offen offset:0 sc1" :  : "v"(vO2), "s"(rWS), "s"(8192u) : "memory"); SB();
  asm volatile("v_mfma_f32_16x16x32_bf16 %0, a[68:71], a[48:51], %0" : "+v"(ac[5]) :  : ); SB();
  asm volatile("ds_read2_b32 a[130:131], %0 offset0:162 offset1:163" :  : "v"(aa[0]) : "memory"); SB();
  asm volatile("v_add_f32 %0, %0, %1" : "+v"(yq[0][1]) : "v"(yq[2][1]) : ); SB();
  asm volatile("v_mfma_f32_16x16x32_bf16 %0, a[68:71], a[44:47], %0" : "+v"(ac[4]) :  : ); SB();
  asm volatile("ds_read_b128 %0, %1 offset:0" : "=&v"(pr[0]) : "v"(pe[0]) : "memory"); SB();
  asm volatile("v_add_f32 %0, %0, %1" : "+v"(yq[0][2]) : "v"(yq[2][2]) : ); SB();
  asm volatile("v_mfma_f32_16x16x32_bf16 %0, a[72:75], a[52:55], %0" : "+v"(ac[7]) :  : ); SB();
  asm volatile("ds_read_b128 a[124:127], %0 offset:3712" :  : "v"(bb) : "memory"); SB();
  asm volatile("buffer_load_dwordx4 a[236:239], %0, %1, %2 offen offset:1024 sc1" :  : "v"(vO2), "s"(rWS), "s"(8192u) : "memory"); SB();
  asm volatile("v_mfma_f32_16x16x32_bf16 %0, a[72:75], a[48:51], %0" : "+v"(ac[6]) :  : ); SB();
  asm volatile("ds_read_b128 a[120:123], %0 offset:3200" :  : "v"(bb) : "memory"); SB();
  asm volatile("v_add_f32 %0, %0, %1" : "+v"(yq[0][3]) : "v"(yq[2][3]) : ); SB();
  asm volatile("v_mfma_f32_16x16x32_bf16 %0, a[72:75], a[44:47], %0" : "+v"(ac[5]) :  : ); SB();
  asm volatile("ds_read_b128 a[116:119], %0 offset:2688" :  : "v"(bb) : "memory"); SB();
  asm volatile("v_add_f32 %0, %0, %1" : "+v"(yq[1][0]) : "v"(yq[3][0]) : ); SB();
  asm volatile("v_mfma_f32_16x16x32_bf16 %0, a[64:67], a[44:47], 0" : "=&v"(ac[3]) :  : ); SB();
  asm volatile("ds_read_b128 %0, %1 offset:1024" : "=&v"(pr[1]) : "v"(pe[0]) : "memory"); SB();
  asm volatile("buffer_load_dwordx4 a[240:243], %0, %1, %2 offen offset:2048 sc1" :  : "v"(vO), "s"(rWS), "s"(8192u) : "memory"); SB();
  asm volatile("v_mfma_f32_16x16x32_bf16 %0, a[76:79], a[48:51], %0" : "+v"(ac[7]) :  : ); SB();
  asm volatile("ds_read_b128 a[112:115], %0 offset:2176" :  : "v"(bb) : "memory"); SB();
  asm volatile("v_add_f32 %0, %0, %1" : "+v"(yq[1][1]) : "v"(yq[3][1]) : ); SB();
  asm volatile("v_mfma_f32_16x16x32_bf16 %0, a[76:79], a[44:47], %0" : "+v"(ac[6]) :  : ); SB();
  asm volatile("ds_read2_b32 a[132:133], %0 offset0:32 offset1:33" :  : "v"(aa[0]) : "memory"); SB();
  asm volatile("v_add_f32 %0, %0, %1" : "+v"(yq[1][2]) : "v"(yq[3][2]) : ); SB();
  asm volatile("v_mfma_f32_16x16x32_bf16 %0, a[76:79], a[40:43], %0" : "+v"(ac[5]) :  : ); SB();
  asm volatile("ds_read2_b32 a[134:135], %0 offset0:34 offset1:35" :  : "v"(aa[0]) : "memory"); SB();
  asm volatile("buffer_load_dwordx4 a[244:247], %0, %1, %2 offen offset:3072 sc1" :  : "v"(vO), "s"(rWS), "s"(8192u) : "memory"); SB();
  asm volatile("v_mfma_f32_16x16x32_bf16 %0, a[72:75], a[40:43], %0" : "+v"(ac[4]) :  : ); SB();
  asm volatile("ds_read_b128 %0, %1 offset:2048" : "=&v"(pr[2]) : "v"(pe[1]) : "memory"); SB();
  asm volatile("v_add_f32 %0, %0, %1" : "+v"(yq[1][3]) : "v"(yq[3][3]) : ); SB();
  asm volatile("v_mfma_f32_16x16x32_bf16 %0, a[80:83], a[44:47], %0" : "+v"(ac[7]) :  : ); SB();
  asm volatile("ds_read_b128 a[108:111], %0 offset:1664" :  : "v"(bb) : "memory"); SB();
  asm volatile("v_add_f32 %0, %0, %1" : "+v"(yq[4][0]) : "v"(yq[6][0]) : ); SB();
  asm volatile("v_mfma_f32_16x16x32_bf16 %0, a[80:83], a[40:43], %0" : "+v"(ac[6]) :  : ); SB();
  asm volatile("ds_read2_b32 a[136:137], %0 offset0:160 offset1:161" :  : "v"(aa[1]) : "memory"); SB();
  asm volatile("buffer_load_dwordx4 a[248:251], %0, %1, %2 offen offset:0 sc1" :  : "v"(vO), "s"(rWS), "s"(12288u) : "memory"); SB();
  asm volatile("v_mfma_f32_16x16x32_bf16 %0, a[68:71], a[40:43], %0" : "+v"(ac[3]) :  : ); SB();
  asm volatile("ds_read2_b32 a[138:139], %0 offset0:162 offset1:163" :  : "v"(aa[1]) : "memory"); SB();
  asm volatile("v_add_f32 %0, %0, %1" : "+v"(yq[4][1]) : "v"(yq[6][1]) : ); SB();
  asm volatile("v_mfma_f32_16x16x32_bf16 %0, a[64:67], a[40:43], 0" : "=&v"(ac[2]) :  : ); SB();
  asm volatile("ds_read_b128 %0, %1 offset:3072" : "=&v"(pr[3]) : "v"(pe[1]) : "memory"); SB();
  asm volatile("v_add_f32 %0, %0, %1" : "+v"(yq[4][2]) : "v"(yq[6][2]) : ); SB();
  asm volatile("v_mfma_f32_16x16x32_bf16 %0, a[84:87], a[40:43], %0" : "+v"(ac[7]) :  : ); SB();
  asm volatile("ds_read2_b32 a[140:141], %0 offset0:32 offset1:33" :  : "v"(aa[1]) : "memory"); SB();
  asm volatile("buffer_load_dwordx4 a[252:255], %0, %1, %2 offen offset:1024 sc1" :  : "v"(vO), "s"(rWS), "s"(12288u) : "memory"); SB();
  asm volatile("s_waitcnt lgkmcnt(8)\n\tv_mfma_f32_16x16x32_bf16 %0, a[84:87], a[36:39], %0" : "+v"(ac[6]) :  : ); SB();
  asm volatile("ds_read2_b32 a[142:143], %0 offset0:34 offset1:35" :  : "v"(aa[1]) : "memory"); SB();
  asm volatile("v_add_f32 %0, %0, %1" : "+v"(yq[4][3]) : "v"(yq[6][3]) : ); SB();
  asm volatile("v_mfma_f32_16x16x32_bf16 %0, a[80:83], a[36:39], %0" : "+v"(ac[5]) :  : ); SB();
  asm volatile("ds_read_b128 a[104:107], %0 offset:1152" :  : "v"(bb) : "memory"); SB();
  asm volatile("v_add_f32 %0, %0, %1" : "+v"(yq[5][0]) : "v"(yq[7][0]) : ); SB();
  asm volatile("v_mfma_f32_16x16x32_bf16 %0, a[76:79], a[36:39], %0" : "+v"(ac[4]) :  : ); SB();
  asm volatile("ds_read_b128 %0, %1 offset:4096" : "=&v"(pr[4]) : "v"(pe[2]) : "memory"); SB();
  asm volatile("v_add_f32 %0, %0, %1" : "+v"(yq[5][1]) : "v"(yq[7][1]) : ); SB();
  asm volatile("v_mfma_f32_16x16x32_bf16 %0, a[88:91], a[36:39], %0" : "+v"(ac[7]) :  : ); SB();
  asm volatile("ds_read2_b32 a[144:145], %0 offset0:160 offset1:161" :  : "v"(aa[2]) : "memory"); SB();
  asm volatile("v_add_f32 %0, %0, %1" : "+v"(yq[5][2]) : "v"(yq[7][2]) : ); SB();
  asm volatile("v_mfma_f32_16x16x32_bf16 %0, a[72:75], a[36:39], %0" : "+v"(ac[3]) :  : ); SB();
  asm volatile("ds_read2_b32 a[146:147], %0 offset0:162 offset1:163" :  : "v"(aa[2]) : "memory"); SB();
  asm volatile("v_add_f32 %0, %0, %1" : "+v"(yq[5][3]) : "v"(yq[7][3]) : ); SB();
  asm volatile("v_mfma_f32_16x16x32_bf16 %0, a[68:71], a[36:39], %0" : "+v"(ac[2]) :  : ); SB();
  asm volatile("ds_read2_b32 a[148:149], %0 offset0:32 offset1:33" :  : "v"(aa[2]) : "memory"); SB();
  asm volatile("v_add_f32 %0, %0, %1" : "+v"(yq[0][0]) : "v"(yq[4][0]) : ); SB();
  asm volatile("v_mfma_f32_16x16x32_bf16 %0, a[64:67], a[36:39], 0" : "=&v"(ac[1]) :  : ); SB();
  asm volatile("ds_read_b128 %0, %1 offset:5120" : "=&v"(pr[5]) : "v"(pe[2]) : "memory"); SB();
  asm volatile("v_add_f32 %0, %0, %1" : "+v"(yq[0][1]) : "v"(yq[4][1]) : ); SB();
  asm volatile("v_mfma_f32_16x16x32_bf16 %0, a[92:95], a[32:35], %0" : "+v"(ac[7]) :  : ); SB();
  asm volatile("ds_read2_b32 a[150:151], %0 offset0:34 offset1:35" :  : "v"(aa[2]) : "memory"); SB();
  asm volatile("v_add_f32 %0, %0, %1" : "+v"(yq[0][2]) : "v"(yq[4][2]) : ); SB();
  asm volatile("v_mfma_f32_16x16x32_bf16 %0, a[88:91], a[32:35], %0" : "+v"(ac[6]) :  : ); SB();
  asm volatile("ds_read_b128 a[100:103], %0 offset:640" :  : "v"(bb) : "memory"); SB();
  asm volatile("v_add_f32 %0, %0, %1" : "+v"(yq[0][3]) : "v"(yq[4][3]) : ); SB();
  asm volatile("v_mfma_f32_16x16x32_bf16 %0, a[84:87], a[32:35], %0" : "+v"(ac[5]) :  : ); SB();
  asm volatile("ds_read2_b32 a[152:153], %0 offset0:160 offset1:161" :  : "v"(aa[3]) : "memory"); SB();
  asm volatile("v_add_f32 %0, %0, %1" : "+v"(yq[1][0]) : "v"(yq[5][0]) : ); SB();
  asm volatile("v_mfma_f32_16x16x32_bf16 %0, a[80:83], a[32:35], %0" : "+v"(ac[4]) :  : ); SB();
  pw3 = lb + 98304 + w * 15360 + l16; asm volatile("" : "+v"(pw3)); SB();
  asm volatile("v_mfma_f32_16x16x32_bf16 %0, a[76:79], a[32:35], %0" : "+v"(ac[3]) :  : ); SB();
  asm volatile("ds_read2_b32 a[154:155], %0 offset0:162 offset1:163" :  : "v"(aa[3]) : "memory"); SB();
  asm volatile("v_add_f32 %0, %0, %1" : "+v"(yq[1][1]) : "v"(yq[5][1]) : ); SB();
  asm volatile("v_mfma_f32_16x16x32_bf16 %0, a[72:75], a[32:35], %0" : "+v"(ac[2]) :  : ); SB();
  asm volatile("ds_read2_b32 a[156:157], %0 offset0:32 offset1:33" :  : "v"(aa[3]) : "memory"); SB();
  asm volatile("s_waitcnt lgkmcnt(5)" : "+v"(pr[0]), "+v"(pr[1]), "+v"(pr[2]), "+v"(pr[3]), "+v"(pr[4]), "+v"(pr[5]) :  : "memory"); SB();
  asm volatile("v_mfma_f32_16x16x32_bf16 %0, a[68:71], a[32:35], %0" : "+v"(ac[1]) :  : ); SB();
  asm volatile("ds_read2_b32 a[158:159], %0 offset0:34 offset1:35" :  : "v"(aa[3]) : "memory"); SB();
  asm volatile("v_add_f32 %0, %0, %1" : "+v"(yq[1][2]) : "v"(yq[5][2]) : ); SB();
  asm volatile("v_mfma_f32_16x16x32_bf16 %0, a[64:67], a[32:35], 0" : "=&v"(ac[0]) :  : ); SB();
  {
    const f32x4 h0 = ((g[0][0][0] + pr[0]) + (pr[2] + pr[4])) * a1[1][1], h1 = ((g[0][0][1] + pr[1]) + (pr[3] + pr[5])) * a1[1][2];
    ho[2] = (u32x4){pack2(h0[0], h0[1]), pack2(h0[2], h0[3]), pack2(h1[0], h1[1]), nohole(pack2(h1[2], h1[3]))};
    SB();
  }
  asm volatile("v_mfma_f32_16x16x32_bf16 %0, a[128:131], a[124:127], %0" : "+v"(ac[7]) :  : ); SB();
  asm volatile("ds_read_b128 a[96:99], %0 offset:128" :  : "v"(bb) : "memory"); SB();
  asm volatile("buffer_load_dwordx4 a[0:3], %0, %1, %2 offen offset:2048 sc1" :  : "v"(vO), "s"(rWS), "s"(12288u) : "memory"); SB();
  asm volatile("v_mfma_f32_16x16x32_bf16 %0, a[128:131], a[120:123], %0" : "+v"(ac[6]) :  : ); SB();
  asm volatile("ds_read2_b32 a[64:65], %0 offset0:144 offset1:145" :  : "v"(aa[0]) : "memory"); SB();
  asm volatile("buffer_load_dwordx4 a[4:7], %0, %1, %2 offen offset:2048 sc1" :  : "v"(vO2), "s"(rWS), "s"(8192u) : "memory"); SB();
  asm volatile("v_mfma_f32_16x16x32_bf16 %0, a[128:131], a[116:119], %0" : "+v"(ac[5]) :  : ); SB();
  asm volatile("ds_read2_b32 a[66:67], %0 offset0:146 offset1:147" :  : "v"(aa[0]) : "memory"); SB();
  asm volatile("buffer_load_dwordx4 a[8:11], %0, %1, %2 offen offset:3072 sc1" :  : "v"(vO2), "s"(rWS), "s"(8192u) : "memory"); SB();
  asm volatile("v_mfma_f32_16x16x32_bf16 %0, a[128:131], a[112:115], %0" : "+v"(ac[4]) :  : ); SB();
  asm volatile("buffer_store_dwordx4 %1, %0, %2, %3 offen offset:2048 sc1\n\ts_nop 1" :  : "v"(vH_), "v"(ho[2]), "s"(rWS), "s"(0u) : "memory"); SB();
  asm volatile("v_mfma_f32_16x16x32_bf16 %0, a[132:135], a[120:123], %0" : "+v"(ac[7]) :  : ); SB();
  asm volatile("ds_read_b128 a[60:63], %0 offset:3648" :  : "v"(bb) : "memory"); SB();
  asm volatile("v_add_f32 %0, %0, %1" : "+v"(yq[1][3]) : "v"(yq[5][3]) : ); SB();
  asm volatile("v_mfma_f32_16x16x32_bf16 %0, a[132:135], a[116:119], %0" : "+v"(ac[6]) :  : ); SB();
  asm volatile("ds_read_b128 a[56:59], %0 offset:3136" :  : "v"(bb) : "memory"); SB();
  asm volatile("v_lshlrev_b32 %0, 16, %1" : "=v"(yq[2][0]) : "v"(ykv[0]) : ); SB();
  asm volatile("v_mfma_f32_16x16x32_bf16 %0, a[132:135], a[112:115], %0" : "+v"(ac[5]) :  : ); SB();
  asm volatile("ds_read_b128 a[52:55], %0 offset:2624" :  : "v"(bb) : "memory"); SB();
  asm volatile("v_and_b32 %0, 0xffff0000, %1" : "=v"(yq[2][1]) : "v"(ykv[0]) : ); SB();
  asm volatile("v_mfma_f32_16x16x32_bf16 %0, a[132:135], a[108:111], %0" : "+v"(ac[4]) :  : ); SB();
  p3[0] = lb + 98304 + (w ^ 1) * 15360 + l16; asm volatile("" : "+v"(p3[0])); SB();
  asm volatile("v_mfma_f32_16x16x32_bf16 %0, a[136:139], a[116:119], %0" : "+v"(ac[7]) :  : ); SB();
  asm volatile("ds_read_b128 a[48:51], %0 offset:2112" :  : "v"(bb) : "memory"); SB();
  asm volatile("buffer_load_dwordx4 a[12:15], %0, %1, %2 offen offset:0 sc1" :  : "v"(vO2), "s"(rWS), "s"(12288u) : "memory"); SB();
  asm volatile("v_mfma_f32_16x16x32_bf16 %0, a[136:139], a[112:115], %0" : "+v"(ac[6]) :  : ); SB();
  asm volatile("ds_read2_b32 a[68:69], %0 offset0:16 offset1:17" :  : "v"(aa[0]) : "memory"); SB();
  asm volatile("v_lshlrev_b32 %0, 16, %1" : "=v"(yq[2][2]) : "v"(ykv[1]) : ); SB();
  asm volatile("v_mfma_f32_16x16x32_bf16 %0, a[136:139], a[108:111], %0" : "+v"(ac[5]) :  : ); SB();
  asm volatile("ds_read2_b32 a[70:71], %0 offset0:18 offset1:19" :  : "v"(aa[0]) : "memory"); SB();
  asm volatile("buffer_load_dwordx4 a[16:19], %0, %1, %2 offen offset:1024 sc1" :  : "v"(vO2), "s"(rWS), "s"(12288u) : "memory"); SB();
  asm volatile("v_mfma_f32_16x16x32_bf16 %0, a[128:131], a[108:111], %0" : "+v"(ac[3]) :  : ); SB();
  p3[1] = lb + 98304 + (w ^ 2) * 15360 + l16; asm volatile("" : "+v"(p3[1])); SB();
  asm volatile("v_mfma_f32_16x16x32_bf16 %0, a[140:143], a[112:115], %0" : "+v"(ac[7]) :  : ); SB();
  asm volatile("ds_read_b128 a[44:47], %0 offset:1600" :  : "v"(bb) : "memory"); SB();
  asm volatile("v_and_b32 %0, 0xffff0000, %1" : "=v"(yq[2][3]) : "v"(ykv[1]) : ); SB();
  asm volatile("v_mfma_f32_16x16x32_bf16 %0, a[140:143], a[108:111], %0" : "+v"(ac[6]) :  : ); SB();
  asm volatile("ds_read2_b32 a[72:73], %0 offset0:144 offset1:145" :  : "v"(aa[1]) : "memory"); SB();
  asm volatile("buffer_load_dwordx4 a[20:23], %0, %1, %2 offen offset:2048 sc1" :  : "v"(vO2), "s"(rWS), "s"(12288u) : "memory"); SB();
  asm volatile("v_mfma_f32_16x16x32_bf16 %0, a[140:143], a[104:107], %0" : "+v"(ac[5]) :  : ); SB();
  asm volatile("ds_read2_b32 a[74:75], %0 offset0:146 offset1:147" :  : "v"(aa[1]) : "memory"); SB();
  asm volatile("v_lshlrev_b32 %0, 16, %1" : "=v"(yq[3][0]) : "v"(ykv[2]) : ); SB();
  asm volatile("v_mfma_f32_16x16x32_bf16 %0, a[136:139], a[104:107], %0" : "+v"(ac[4]) :  : ); SB();
  p3[2] = lb + 98304 + (w ^ 3) * 15360 + l16; asm volatile("" : "+v"(p3[2])); SB();
  asm volatile("v_mfma_f32_16x16x32_bf16 %0, a[144:147], a[108:111], %0" : "+v"(ac[7]) :  : ); SB();
  asm volatile("ds_read2_b32 a[76:77], %0 offset0:16 offset1:17" :  : "v"(aa[1]) : "memory"); SB();
  asm volatile("v_and_b32 %0, 0xffff0000, %1" : "=v"(yq[3][1]) : "v"(ykv[2]) : ); SB();
  asm volatile("v_mfma_f32_16x16x32_bf16 %0, a[144:147], a[104:107], %0" : "+v"(ac[6]) :  : ); SB();
  asm volatile("ds_read2_b32 a[78:79], %0 offset0:18 offset1:19" :  : "v"(aa[1]) : "memory"); SB();
  asm volatile("v_lshlrev_b32 %0, 16, %1" : "=v"(yq[3][2]) : "v"(ykv[3]) : ); SB();
  asm volatile("v_mfma_f32_16x16x32_bf16 %0, a[132:135], a[104:107], %0" : "+v"(ac[3]) :  : ); SB();
  asm volatile("ds_read_b128 a[40:43], %0 offset:1088" :  : "v"(bb) : "memory"); SB();
  asm volatile("v_and_b32 %0, 0xffff0000, %1" : "=v"(yq[3][3]) : "v"(ykv[3]) : ); SB();
  asm volatile("v_mfma_f32_16x16x32_bf16 %0, a[128:131], a[104:107], %0" : "+v"(ac[2]) :  : ); SB();
  yw = (lane ^ ((lane >> 3) & 6)) * 16; asm volatile("" : "+v"(yw)); SB();
  asm volatile("s_waitcnt lgkmcnt(6)\n\tv_mfma_f32_16x16x32_bf16 %0, a[148:151], a[104:107], %0" : "+v"(ac[7]) :  : ); SB();
  asm volatile("ds_read2_b32 a[80:81], %0 offset0:144 offset1:145" :  : "v"(aa[2]) : "memory"); SB();
  asm volatile("buffer_load_dwordx2 %0, %1, %2, %3 offen sc1" : "=&v"(fh) : "v"(vFH), "s"(rWS), "s"(0u) : "memory"); SB();
  asm volatile("v_mfma_f32_16x16x32_bf16 %0, a[148:151], a[100:103], %0" : "+v"(ac[6]) :  : ); SB();
  asm volatile("ds_read2_b32 a[82:83], %0 offset0:146 offset1:147" :  : "v"(aa[2]) : "memory"); SB();
  asm volatile("v_fmac_f32 %0, %1, %2" : "+v"(yq[0][0]) : "v"(yq[2][0]), "v"(dD[2]) : ); SB();
  asm volatile("v_mfma_f32_16x16x32_bf16 %0, a[144:147], a[100:103], %0" : "+v"(ac[5]) :  : ); SB();
  asm volatile("ds_read2_b32 a[84:85], %0 offset0:16 offset1:17" :  : "v"(aa[2]) : "memory"); SB();
  asm volatile("v_fmac_f32 %0, %1, %2" : "+v"(yq[0][1]) : "v"(yq[2][1]), "v"(dD[2]) : ); SB();
  asm volatile("v_mfma_f32_16x16x32_bf16 %0, a[140:143], a[100:103], %0" : "+v"(ac[4]) :  : ); SB();
  yw = yw + lb + 32768 + w * 3072; asm volatile("" : "+v"(yw)); SB();
  asm volatile("v_mfma_f32_16x16x32_bf16 %0, a[152:155], a[100:103], %0" : "+v"(ac[7]) :  : ); SB();
  asm volatile("ds_read2_b32 a[86:87], %0 offset0:18 offset1:19" :  : "v"(aa[2]) : "memory"); SB();
  asm volatile("v_fmac_f32 %0, %1, %2" : "+v"(yq[0][2]) : "v"(yq[2][2]), "v"(dD[2]) : ); SB();
  asm volatile("v_mfma_f32_16x16x32_bf16 %0, a[136:139], a[100:103], %0" : "+v"(ac[3]) :  : ); SB();
  asm volatile("ds_read_b128 a[36:39], %0 offset:576" :  : "v"(bb) : "memory"); SB();
  asm volatile("v_fmac_f32 %0, %1, %2" : "+v"(yq[0][3]) : "v"(yq[2][3]), "v"(dD[2]) : ); SB();
  asm volatile("v_mfma_f32_16x16x32_bf16 %0, a[132:135], a[100:103], %0" : "+v"(ac[2]) :  : ); SB();
  asm volatile("ds_read2_b32 a[88:89], %0 offset0:144 offset1:145" :  : "v"(aa[3]) : "memory"); SB();
  asm volatile("v_fmac_f32 %0, %1, %2" : "+v"(yq[1][0]) : "v"(yq[3][0]), "v"(dD[2]) : ); SB();
  asm volatile("v_mfma_f32_16x16x32_bf16 %0, a[128:131], a[100:103], %0" : "+v"(ac[1]) :  : ); SB();
  ya = lb + 32768 + (4 * g4 + q4) * 128 + 8 * p4; asm volatile("" : "+v"(ya)); SB();
  asm volatile("v_mfma_f32_16x16x32_bf16 %0, a[156:159], a[96:99], %0" : "+v"(ac[7]) :  : ); SB();
  asm volatile("ds_read2_b32 a[90:91], %0 offset0:146 offset1:147" :  : "v"(aa[3]) : "memory"); SB();
  asm volatile("v_fmac_f32 %0, %1, %2" : "+v"(yq[1][1]) : "v"(yq[3][1]), "v"(dD[2]) : ); SB();
  asm volatile("v_mfma_f32_16x16x32_bf16 %0, a[152:155], a[96:99], %0" : "+v"(ac[6]) :  : ); SB();
  asm volatile("ds_read2_b32 a[92:93], %0 offset0:16 offset1:17" :  : "v"(aa[3]) : "memory"); SB();
  asm volatile("v_fmac_f32 %0, %1, %2" : "+v"(yq[1][2]) : "v"(yq[3][2]), "v"(dD[2]) : ); SB();
  asm volatile("v_mfma_f32_16x16x32_bf16 %0, a[148:151], a[96:99], %0" : "+v"(ac[5]) :  : ); SB();
  asm volatile("ds_read2_b32 a[94:95], %0 offset0:18 offset1:19" :  : "v"(aa[3]) : "memory"); SB();
  asm volatile("v_fmac_f32 %0, %1, %2" : "+v"(yq[1][3]) : "v"(yq[3][3]), "v"(dD[2]) : ); SB();
  asm volatile("v_mfma_f32_16x16x32_bf16 %0, a[144:147], a[96:99], %0" : "+v"(ac[4]) :  : ); SB();
  po = (2 * g4 + (q4 >> 1)) & 3; asm volatile("" : "+v"(po)); SB();
  asm volatile("v_mfma_f32_16x16x32_bf16 %0, a[140:143], a[96:99], %0" : "+v"(ac[3]) :  : ); SB();
  asm volatile("ds_read_b128 a[32:35], %0 offset:64" :  : "v"(bb) : "memory"); SB();
  asm volatile("v_cvt_pk_bf16_f32 %0, %1, %2" : "=v"(y2[0]) : "v"(yq[0][0]), "v"(yq[0][1]) : ); SB();
  asm volatile("v_mfma_f32_16x16x32_bf16 %0, a[136:139], a[96:99], %0" : "+v"(ac[2]) :  : ); SB();
  ya = ya + ((w ^ po) << 5); asm volatile("" : "+v"(ya)); SB();
  asm volatile("v_mfma_f32_16x16x32_bf16 %0, a[132:135], a[96:99], %0" : "+v"(ac[1]) :  : ); SB();
  po = vX + 24 * (g4 & 1); asm volatile("" : "+v"(po)); SB();
  asm volatile("v_mfma_f32_16x16x32_bf16 %0, a[128:131], a[96:99], %0" : "+v"(ac[0]) :  : ); SB();
  asm volatile("v_cvt_pk_bf16_f32 %0, %1, %2" : "=v"(y2[1]) : "v"(yq[0][2]), "v"(yq[0][3]) : ); SB();
  asm volatile("v_mfma_f32_16x16x32_bf16 %0, a[64:67], a[60:63], %0" : "+v"(ac[7]) :  : ); SB();
  asm volatile("ds_read2_b32 a[128:129], %0 offset0:128 offset1:129" :  : "v"(aa[0]) : "memory"); SB();
  asm volatile("v_cvt_pk_bf16_f32 %0, %1, %2" : "=v"(y2[2]) : "v"(yq[1][0]), "v"(yq[1][1]) : ); SB();
  asm volatile("v_mfma_f32_16x16x32_bf16 %0, a[64:67], a[56:59], %0" : "+v"(ac[6]) :  : ); SB();
  asm volatile("ds_read2_b32 a[130:131], %0 offset0:130 offset1:131" :  : "v"(aa[0]) : "memory"); SB();
  asm volatile("v_cvt_pk_bf16_f32 %0, %1, %2" : "=v"(y2[3]) : "v"(yq[1][2]), "v"(yq[1][3]) : ); SB();
  asm volatile("v_mfma_f32_16x16x32_bf16 %0, a[64:67], a[52:55], %0" : "+v"(ac[5]) :  : ); SB();
  asm volatile("ds_read_b128 a[124:127], %0 offset:3584" :  : "v"(bb) : "memory"); SB();
  asm volatile("v_min_u32 %0, -2, %0" : "+v"(y2[3]) :  : ); SB();
  asm volatile("v_mfma_f32_16x16x32_bf16 %0, a[64:67], a[48:51], %0" : "+v"(ac[4]) :  : ); SB();
  asm volatile("v_xnor_b32 %0, %0, %1" : "+v"(y2[3]) : "v"(ykv[3]) : ); SB();
  asm volatile("v_mfma_f32_16x16x32_bf16 %0, a[68:71], a[56:59], %0" : "+v"(ac[7]) :  : ); SB();
  asm volatile("ds_read_b128 a[120:123], %0 offset:3072" :  : "v"(bb) : "memory"); SB();
  asm volatile("v_mfma_f32_16x16x32_bf16 %0, a[68:71], a[52:55], %0" : "+v"(ac[6]) :  : ); SB();
  asm volatile("ds_read_b128 a[96:99], %0 offset:0" :  : "v"(bb) : "memory"); SB();
  asm volatile("buffer_store_dwordx4 %1, %0, %2, %3 offen offset:0 sc1\n\ts_nop 1" :  : "v"(vF), "v"(y2), "s"(rWS), "s"(uso) : "memory"); SB();
  asm volatile("v_mfma_f32_16x16x32_bf16 %0, a[68:71], a[48:51], %0" : "+v"(ac[5]) :  : ); SB();
  asm volatile("ds_read_b128 a[100:103], %0 offset:512" :  : "v"(bb) : "memory"); SB();
  asm volatile("s_waitcnt lgkmcnt(6)\n\tv_mfma_f32_16x16x32_bf16 %0, a[68:71], a[44:47], %0" : "+v"(ac[4]) :  : ); SB();
  asm volatile("v_mfma_f32_16x16x32_bf16 %0, a[72:75], a[52:55], %0" : "+v"(ac[7]) :  : ); SB();
  asm volatile("ds_read2_b32 a[132:133], %0 offset0:0 offset1:1" :  : "v"(aa[0]) : "memory"); SB();
  asm volatile("v_mfma_f32_16x16x32_bf16 %0, a[72:75], a[48:51], %0" : "+v"(ac[6]) :  : ); SB();
  asm volatile("ds_read2_b32 a[134:135], %0 offset0:2 offset1:3" :  : "v"(aa[0]) : "memory"); SB();
  asm volatile("v_mfma_f32_16x16x32_bf16 %0, a[72:75], a[44:47], %0" : "+v"(ac[5]) :  : ); SB();
  asm volatile("ds_read_b128 a[116:119], %0 offset:2560" :  : "v"(bb) : "memory"); SB();
  asm volatile("v_mfma_f32_16x16x32_bf16 %0, a[64:67], a[44:47], %0" : "+v"(ac[3]) :  : ); SB();
  asm volatile("v_mfma_f32_16x16x32_bf16 %0, a[76:79], a[48:51], %0" : "+v"(ac[7]) :  : ); SB();
  asm volatile("ds_read_b128 a[104:107], %0 offset:1024" :  : "v"(bb) : "memory"); SB();
  asm volatile("v_mfma_f32_16x16x32_bf16 %0, a[76:79], a[44:47], %0" : "+v"(ac[6]) :  : ); SB();
  asm volatile("ds_read2_b32 a[136:137], %0 offset0:128 offset1:129" :  : "v"(aa[1]) : "memory"); SB();
  asm volatile("v_mfma_f32_16x16x32_bf16 %0, a[76:79], a[40:43], %0" : "+v"(ac[5]) :  : ); SB();
  asm volatile("ds_read2_b32 a[138:139], %0 offset0:130 offset1:131" :  : "v"(aa[1]) : "memory"); SB();
  asm volatile("v_mfma_f32_16x16x32_bf16 %0, a[72:75], a[40:43], %0" : "+v"(ac[4]) :  : ); SB();
  asm volatile("v_mfma_f32_16x16x32_bf16 %0, a[80:83], a[44:47], %0" : "+v"(ac[7]) :  : ); SB();
  asm volatile("ds_read_b128 a[108:111], %0 offset:1536" :  : "v"(bb) : "memory"); SB();
  asm volatile("v_mfma_f32_16x16x32_bf16 %0, a[80:83], a[40:43], %0" : "+v"(ac[6]) :  : ); SB();
  asm volatile("ds_read_b128 a[112:115], %0 offset:2048" :  : "v"(bb) : "memory"); SB();
  asm volatile("v_mfma_f32_16x16x32_bf16 %0, a[68:71], a[40:43], %0" : "+v"(ac[3]) :  : ); SB();
  asm volatile("ds_read2_b32 a[140:141], %0 offset0:0 offset1:1" :  : "v"(aa[1]) : "memory"); SB();
  asm volatile("v_mfma_f32_16x16x32_bf16 %0, a[64:67], a[40:43], %0" : "+v"(ac[2]) :  : ); SB();
  asm volatile("v_mfma_f32_16x16x32_bf16 %0, a[84:87], a[40:43], %0" : "+v"(ac[7]) :  : ); SB();
  asm volatile("ds_read2_b32 a[142:143], %0 offset0:2 offset1:3" :  : "v"(aa[1]) : "memory"); SB();
  asm volatile("v_mfma_f32_16x16x32_bf16 %0, a[84:87], a[36:39], %0" : "+v"(ac[6]) :  : ); SB();
  asm volatile("ds_read2_b32 a[144:145], %0 offset0:128 offset1:129" :  : "v"(aa[2]) : "memory"); SB();
  asm volatile("v_mfma_f32_16x16x32_bf16 %0, a[80:83], a[36:39], %0" : "+v"(ac[5]) :  : ); SB();
  asm volatile("ds_read2_b32 a[146:147], %0 offset0:130 offset1:131" :  : "v"(aa[2]) : "memory"); SB();
  asm volatile("v_mfma_f32_16x16x32_bf16 %0, a[76:79], a[36:39], %0" : "+v"(ac[4]) :  : ); SB();
  asm volatile("v_mfma_f32_16x16x32_bf16 %0, a[88:91], a[36:39], %0" : "+v"(ac[7]) :  : ); SB();
  asm volatile("ds_read2_b32 a[148:149], %0 offset0:0 offset1:1" :  : "v"(aa[2]) : "memory"); SB();
  asm volatile("v_mfma_f32_16x16x32_bf16 %0, a[72:75], a[36:39], %0" : "+v"(ac[3]) :  : ); SB();
  asm volatile("ds_read2_b32 a[150:151], %0 offset0:2 offset1:3" :  : "v"(aa[2]) : "memory"); SB();
  asm volatile("v_mfma_f32_16x16x32_bf16 %0, a[68:71], a[36:39], %0" : "+v"(ac[2]) :  : ); SB();
  asm volatile("ds_read2_b32 a[152:153], %0 offset0:128 offset1:129" :  : "v"(aa[3]) : "memory"); SB();
  asm volatile("v_mfma_f32_16x16x32_bf16 %0, a[64:67], a[36:39], %0" : "+v"(ac[1]) :  : ); SB();
  asm volatile("v_mfma_f32_16x16x32_bf16 %0, a[92:95], a[32:35], %0" : "+v"(ac[7]) :  : ); SB();
  asm volatile("ds_read2_b32 a[154:155], %0 offset0:130 offset1:131" :  : "v"(aa[3]) : "memory"); SB();
  asm volatile("v_mfma_f32_16x16x32_bf16 %0, a[88:91], a[32:35], %0" : "+v"(ac[6]) :  : ); SB();
  asm volatile("ds_read2_b32 a[156:157], %0 offset0:0 offset1:1" :  : "v"(aa[3]) : "memory"); SB();
  asm volatile("v_mfma_f32_16x16x32_bf16 %0, a[84:87], a[32:35], %0" : "+v"(ac[5]) :  : ); SB();
  asm volatile("ds_read2_b32 a[158:159], %0 offset0:2 offset1:3" :  : "v"(aa[3]) : "memory"); SB();
  asm volatile("v_mfma_f32_16x16x32_bf16 %0, a[80:83], a[32:35], %0" : "+v"(ac[4]) :  : ); SB();
  asm volatile("v_mfma_f32_16x16x32_bf16 %0, a[76:79], a[32:35], %0" : "+v"(ac[3]) :  : ); SB();
  asm volatile("v_mfma_f32_16x16x32_bf16 %0, a[72:75], a[32:35], %0" : "+v"(ac[2]) :  : ); SB();
  asm volatile("v_mfma_f32_16x16x32_bf16 %0, a[68:71], a[32:35], %0" : "+v"(ac[1]) :  : ); SB();
  asm volatile("v_mfma_f32_16x16x32_bf16 %0, a[64:67], a[32:35], %0" : "+v"(ac[0]) :  : ); SB();
  asm volatile("s_waitcnt lgkmcnt(4)\n\tv_mfma_f32_16x16x32_bf16 %0, a[128:131], a[124:127], %0" : "+v"(ac[7]) :  : ); SB();
  asm volatile("v_mfma_f32_16x16x32_bf16 %0, a[128:131], a[120:123], %0" : "+v"(ac[6]) :  : ); SB();
  asm volatile("v_mfma_f32_16x16x32_bf16 %0, a[128:131], a[96:99], %0" : "+v"(ac[0]) :  : ); SB();
  asm volatile("v_mfma_f32_16x16x32_bf16 %0, a[128:131], a[100:103], %0" : "+v"(ac[1]) :  : ); SB();
  asm volatile("v_mfma_f32_16x16x32_bf16 %0, a[132:135], a[120:123], %0" : "+v"(ac[7]) :  : ); SB();
  asm volatile("v_mfma_f32_16x16x32_bf16 %0, a[132:135], a[116:119], %0" : "+v"(ac[6]) :  : ); SB();
  asm volatile("v_mfma_f32_16x16x32_bf16 %0, a[128:131], a[104:107], %0" : "+v"(ac[2]) :  : ); SB();
  asm volatile("ds_write_b128 %0, %1 offset:0" :  : "v"(dd), "v"(ac[0]) : "memory"); SB();
  asm volatile("v_mfma_f32_16x16x32_bf16 %0, a[132:135], a[96:99], %0" : "+v"(ac[1]) :  : ); SB();
  asm volatile("s_waitcnt vmcnt(1)\n\t.Lc_%=:\n\tv_cmp_ne_u64 vcc, %2, %1\n\ts_cbranch_vccnz .Lr_%=\n\t.Ld_%=:\n\t.subsection 1\n\t.Lr_%=:\n\tv_add_u32 %0, -1, %0\n\ts_nop 1\n\tv_readfirstlane_b32 vcc_hi, %0\n\ts_nop 3\n\ts_cmp_lt_i32 vcc_hi, 1\n\ts_cbranch_scc1 .Ld_%=\n\ts_sleep 2\n\ts_mov_b32 vcc_lo, 0\n\tbuffer_load_dwordx2 %1, %3, %4, vcc_lo offen sc1\n\ts_waitcnt vmcnt(0)\n\ts_branch .Lc_%=\n\t.subsection 0" : "+v"(tmo), "+v"(fh) : "s"(tg), "v"(vFH), "s"(rWS) : "memory", "vcc", "scc"); SB();
  asm volatile("buffer_load_dwordx4 v[164:167], %0, %1, %2 offen offset:0 sc1" :  : "v"(vHr), "s"(rWS), "s"(hso[2]) : "memory"); SB();
  asm volatile("v_mfma_f32_16x16x32_bf16 %0, a[136:139], a[116:119], %0" : "+v"(ac[7]) :  : ); SB();
  asm volatile("buffer_load_dwordx4 v[168:171], %0, %1, %2 offen offset:0 sc1" :  : "v"(vHr), "s"(rWS), "s"(hso[4]) : "memory"); SB();
  asm volatile("v_mfma_f32_16x16x32_bf16 %0, a[128:131], a[108:111], %0" : "+v"(ac[3]) :  : ); SB();
  asm volatile("buffer_load_dwordx4 v[172:175], %0, %1, %2 offen offset:0 sc1" :  : "v"(vHr), "s"(rWS), "s"(hso[6]) : "memory"); SB();
  asm volatile("v_mfma_f32_16x16x32_bf16 %0, a[132:135], a[100:103], %0" : "+v"(ac[2]) :  : ); SB();
  asm volatile("v_mfma_f32_16x16x32_bf16 %0, a[128:131], a[112:115], %0" : "+v"(ac[4]) :  : ); SB();
  asm volatile("buffer_load_dwordx4 v[160:163], %0, %1, %2 offen offset:0 sc1" :  : "v"(vHr), "s"(rWS), "s"(hso[0]) : "memory"); SB();
  asm volatile("v_mfma_f32_16x16x32_bf16 %0, a[140:143], a[112:115], %0" : "+v"(ac[7]) :  : ); SB();
  asm volatile("ds_write_b128 %0, %1 offset:1024" :  : "v"(dd), "v"(ac[1]) : "memory"); SB();
  asm volatile("v_mfma_f32_16x16x32_bf16 %0, a[132:135], a[104:107], %0" : "+v"(ac[3]) :  : ); SB();
  asm volatile("buffer_load_dwordx4 v[180:183], %0, %1, %2 offen offset:1024 sc1" :  : "v"(vHr), "s"(rWS), "s"(hso[2]) : "memory"); SB();
  asm volatile("v_mfma_f32_16x16x32_bf16 %0, a[136:139], a[96:99], %0" : "+v"(ac[2]) :  : ); SB();
  asm volatile("v_mfma_f32_16x16x32_bf16 %0, a[132:135], a[108:111], %0" : "+v"(ac[4]) :  : ); SB();
  asm volatile("v_mfma_f32_16x16x32_bf16 %0, a[128:131], a[116:119], %0" : "+v"(ac[5]) :  : ); SB();
  asm volatile("v_mfma_f32_16x16x32_bf16 %0, a[136:139], a[100:103], %0" : "+v"(ac[3]) :  : ); SB();
  asm volatile("buffer_load_dwordx4 v[184:187], %0, %1, %2 offen offset:1024 sc1" :  : "v"(vHr), "s"(rWS), "s"(hso[4]) : "memory"); SB();
  asm volatile("v_mfma_f32_16x16x32_bf16 %0, a[136:139], a[112:115], %0" : "+v"(ac[6]) :  : ); SB();
  asm volatile("ds_write_b128 %0, %1 offset:2048" :  : "v"(dd), "v"(ac[2]) : "memory"); SB();
  asm volatile("v_mfma_f32_16x16x32_bf16 %0, a[136:139], a[104:107], %0" : "+v"(ac[4]) :  : ); SB();
  asm volatile("buffer_load_dwordx4 v[188:191], %0, %1, %2 offen offset:1024 sc1" :  : "v"(vHr), "s"(rWS), "s"(hso[6]) : "memory"); SB();
  asm volatile("v_mfma_f32_16x16x32_bf16 %0, a[132:135], a[112:115], %0" : "+v"(ac[5]) :  : ); SB();
  asm volatile("v_mfma_f32_16x16x32_bf16 %0, a[140:143], a[96:99], %0" : "+v"(ac[3]) :  : ); SB();
  asm volatile("v_mfma_f32_16x16x32_bf16 %0, a[140:143], a[108:111], %0" : "+v"(ac[6]) :  : ); SB();
  asm volatile("v_mfma_f32_16x16x32_bf16 %0, a[140:143], a[100:103], %0" : "+v"(ac[4]) :  : ); SB();
  asm volatile("buffer_load_dwordx4 v[176:179], %0, %1, %2 offen offset:1024 sc1" :  : "v"(vHr), "s"(rWS), "s"(hso[0]) : "memory"); SB();
  asm volatile("v_mfma_f32_16x16x32_bf16 %0, a[136:139], a[108:111], %0" : "+v"(ac[5]) :  : ); SB();
  asm volatile("v_mfma_f32_16x16x32_bf16 %0, a[144:147], a[108:111], %0" : "+v"(ac[7]) :  : ); SB();
  asm volatile("ds_write_b128 %0, %1 offset:3072" :  : "v"(dd), "v"(ac[3]) : "memory"); SB();
  asm volatile("v_mfma_f32_16x16x32_bf16 %0, a[144:147], a[104:107], %0" : "+v"(ac[6]) :  : ); SB();
  asm volatile("buffer_load_dwordx4 v[196:199], %0, %1, %2 offen offset:0 sc1" :  : "v"(vHr), "s"(rWS), "s"(hso[3]) : "memory"); SB();
  asm volatile("v_mfma_f32_16x16x32_bf16 %0, a[144:147], a[96:99], %0" : "+v"(ac[4]) :  : ); SB();
  asm volatile("v_mfma_f32_16x16x32_bf16 %0, a[140:143], a[104:107], %0" : "+v"(ac[5]) :  : ); SB();
  asm volatile("v_mfma_f32_16x16x32_bf16 %0, a[148:151], a[104:107], %0" : "+v"(ac[7]) :  : ); SB();
  asm volatile("buffer_load_dwordx4 v[200:203], %0, %1, %2 offen offset:0 sc1" :  : "v"(vHr), "s"(rWS), "s"(hso[5]) : "memory"); SB();
  asm volatile("v_mfma_f32_16x16x32_bf16 %0, a[148:151], a[100:103], %0" : "+v"(ac[6]) :  : ); SB();
  asm volatile("v_mfma_f32_16x16x32_bf16 %0, a[144:147], a[100:103], %0" : "+v"(ac[5]) :  : ); SB();
  asm volatile("ds_write_b128 %0, %1 offset:4096" :  : "v"(dd), "v"(ac[4]) : "memory"); SB();
  asm volatile("s_waitcnt lgkmcnt(2)\n\tv_mfma_f32_16x16x32_bf16 %0, a[152:155], a[100:103], %0" : "+v"(ac[7]) :  : ); SB();
  asm volatile("buffer_load_dwordx4 v[204:207], %0, %1, %2 offen offset:0 sc1" :  : "v"(vHr), "s"(rWS), "s"(hso[7]) : "memory"); SB();
  asm volatile("v_mfma_f32_16x16x32_bf16 %0, a[152:155], a[96:99], %0" : "+v"(ac[6]) :  : ); SB();
  asm volatile("v_mfma_f32_16x16x32_bf16 %0, a[148:151], a[96:99], %0" : "+v"(ac[5]) :  : ); SB();
  asm volatile("buffer_load_dwordx4 v[192:195], %0, %1, %2 offen offset:0 sc1" :  : "v"(vHr), "s"(rWS), "s"(hso[1]) : "memory"); SB();
  asm volatile("v_mfma_f32_16x16x32_bf16 %0, a[156:159], a[96:99], %0" : "+v"(ac[7]) :  : ); SB();
  asm volatile("s_nop 5" : "+v"(ac[5]), "+v"(ac[6]), "+v"(ac[7]) :  : ); SB();
  asm volatile("ds_write_b128 %0, %1 offset:6144" :  : "v"(dd), "v"(ac[6]) : "memory"); SB();
  asm volatile("ds_write_b128 %0, %1 offset:5120" :  : "v"(dd), "v"(ac[5]) : "memory"); SB();
  asm volatile("ds_write_b128 %0, %1 offset:7168" :  : "v"(dd), "v"(ac[7]) : "memory"); SB();
  ST(18);
  f32x4 yp[2][4], o3[4][5];
  u32x4 yk[2], yo[2];
  unsigned vtmp;
  asm volatile("ds_read_b128 %0, %1 offset:0" : "=&v"(yk[0]) : "v"(ykr) : "memory"); SB();
  asm volatile("ds_read_b128 %0, %1 offset:12928" : "=&v"(yk[1]) : "v"(ykr) : "memory"); SB();
  asm volatile("s_waitcnt lgkmcnt(0)\n\ts_barrier" :  :  : "memory"); SB();
  asm volatile("ds_read_b128 %0, %1 offset:0" : "=&v"(yp[0][0]) : "v"(ydr) : "memory"); SB();
  asm volatile("ds_read_b128 %0, %1 offset:256" : "=&v"(yp[0][1]) : "v"(ydr) : "memory"); SB();
  asm volatile("ds_read_b128 %0, %1 offset:8192" : "=&v"(yp[0][2]) : "v"(ydr) : "memory"); SB();
  asm volatile("ds_read_b128 %0, %1 offset:8448" : "=&v"(yp[0][3]) : "v"(ydr) : "memory"); SB();
  asm volatile("ds_read_b128 %0, %1 offset:16384" : "=&v"(yp[1][0]) : "v"(ydr) : "memory"); SB();
  asm volatile("ds_read_b128 %0, %1 offset:16640" : "=&v"(yp[1][1]) : "v"(ydr) : "memory"); SB();
  asm volatile("ds_read_b128 %0, %1 offset:24576" : "=&v"(yp[1][2]) : "v"(ydr) : "memory"); SB();
  asm volatile("ds_read_b128 %0, %1 offset:24832" : "=&v"(yp[1][3]) : "v"(ydr) : "memory"); SB();
  asm volatile("buffer_load_dwordx4 v[212:215], %0, %1, %2 offen offset:1024 sc1" :  : "v"(vHr), "s"(rWS), "s"(hso[3]) : "memory"); SB();
  asm volatile("buffer_load_dwordx4 v[216:219], %0, %1, %2 offen offset:1024 sc1" :  : "v"(vHr), "s"(rWS), "s"(hso[5]) : "memory"); SB();
  asm volatile("buffer_load_dwordx4 v[220:223], %0, %1, %2 offen offset:1024 sc1" :  : "v"(vHr), "s"(rWS), "s"(hso[7]) : "memory"); SB();
  asm volatile("" : "+v"(yk[0]), "+v"(yk[1]) :  : ); SB();
  f32x2 yd[2][4];
#pragma unroll
  for (int n = 0; n < 8; ++n) { const float Dv = dD[n >> 2]; yd[n >> 2][n & 3] = unpk(yk[n >> 2][n & 3]) * (f32x2){Dv, Dv}; asm volatile("" : "+v"(yd[n >> 2][n & 3])); }
  SB();
  asm volatile("s_waitcnt lgkmcnt(0)" : "+v"(yp[0][0]), "+v"(yp[0][1]), "+v"(yp[0][2]), "+v"(yp[0][3]) :  : "memory"); SB();
  {
    const f32x4 s0 = yp[0][0] + yp[0][2], s1 = yp[0][1] + yp[0][3];
    yo[0] = (u32x4){pk2((f32x2){s0[0], s0[1]} + yd[0][0]), pk2((f32x2){s0[2], s0[3]} + yd[0][1]), pk2((f32x2){s1[0], s1[1]} + yd[0][2]), ~(nohole(pk2((f32x2){s1[2], s1[3]} + yd[0][3])) ^ yk[0][3])};
    SB();
  }
  asm volatile("buffer_store_dwordx4 %1, %0, %2, %3 offen offset:0 sc1\n\ts_nop 1" :  : "v"(vF), "v"(yo[0]), "s"(rWS), "s"(W_KV + 0) : "memory"); SB();
  asm volatile("" : "+v"(yp[1][0]), "+v"(yp[1][1]), "+v"(yp[1][2]), "+v"(yp[1][3]) :  : "memory"); SB();
  {
    const f32x4 s0 = yp[1][0] + yp[1][2], s1 = yp[1][1] + yp[1][3];
    yo[1] = (u32x4){pk2((f32x2){s0[0], s0[1]} + yd[1][0]), pk2((f32x2){s0[2], s0[3]} + yd[1][1]), pk2((f32x2){s1[0], s1[1]} + yd[1][2]), ~(nohole(pk2((f32x2){s1[2], s1[3]} + yd[1][3])) ^ yk[1][3])};
    SB();
  }
  asm volatile("buffer_store_dwordx4 %1, %0, %2, %3 offen offset:0 sc1\n\ts_nop 1" :  : "v"(vF), "v"(yo[1]), "s"(rWS), "s"(W_KV + 4096) : "memory"); SB();
  ST(19);
  ST(20);
  f32x2 xf[5][2], sv[5][2];
  float rd, mr;
  asm volatile("s_waitcnt vmcnt(13)" :  :  : "memory"); SB();
  asm volatile("v_mfma_f32_16x16x32_bf16 %0, a[160:163], v[164:167], 0" : "=&v"(o3[1][0]) :  : ); SB();
  asm volatile("buffer_load_dwordx4 v[208:211], %0, %1, %2 offen offset:1024 sc1" :  : "v"(vHr), "s"(rWS), "s"(hso[1]) : "memory"); SB();
  asm volatile("v_mfma_f32_16x16x32_bf16 %0, a[164:167], v[164:167], 0" : "=&v"(o3[1][1]) :  : ); SB();
  asm volatile("buffer_load_dwordx4 v[228:231], %0, %1, %2 offen offset:2048 sc1" :  : "v"(vHr), "s"(rWS), "s"(hso[2]) : "memory"); SB();
  asm volatile("v_mfma_f32_16x16x32_bf16 %0, a[168:171], v[164:167], 0" : "=&v"(o3[1][2]) :  : ); SB();
  asm volatile("v_mfma_f32_16x16x32_bf16 %0, a[172:175], v[164:167], 0" : "=&v"(o3[1][3]) :  : ); SB();
  asm volatile("buffer_load_dwordx4 v[232:235], %0, %1, %2 offen offset:2048 sc1" :  : "v"(vHr), "s"(rWS), "s"(hso[4]) : "memory"); SB();
  asm volatile("v_mfma_f32_16x16x32_bf16 %0, a[176:179], v[164:167], 0" : "=&v"(o3[1][4]) :  : ); SB();
  asm volatile("v_mfma_f32_16x16x32_bf16 %0, a[160:163], v[168:171], 0" : "=&v"(o3[2][0]) :  : ); SB();
  asm volatile("v_mfma_f32_16x16x32_bf16 %0, a[164:167], v[168:171], 0" : "=&v"(o3[2][1]) :  : ); SB();
  asm volatile("buffer_load_dwordx4 v[236:239], %0, %1, %2 offen offset:2048 sc1" :  : "v"(vHr), "s"(rWS), "s"(hso[6]) : "memory"); SB();
  asm volatile("v_mfma_f32_16x16x32_bf16 %0, a[168:171], v[168:171], 0" : "=&v"(o3[2][2]) :  : ); SB();
  asm volatile("v_mfma_f32_16x16x32_bf16 %0, a[172:175], v[168:171], 0" : "=&v"(o3[2][3]) :  : ); SB();
  asm volatile("v_mfma_f32_16x16x32_bf16 %0, a[176:179], v[168:171], 0" : "=&v"(o3[2][4]) :  : ); SB();
  asm volatile("v_mfma_f32_16x16x32_bf16 %0, a[160:163], v[172:175], 0" : "=&v"(o3[3][0]) :  : ); SB();
  asm volatile("buffer_load_dwordx4 v[224:227], %0, %1, %2 offen offset:2048 sc1" :  : "v"(vHr), "s"(rWS), "s"(hso[0]) : "memory"); SB();
  asm volatile("v_mfma_f32_16x16x32_bf16 %0, a[164:167], v[172:175], 0" : "=&v"(o3[3][1]) :  : ); SB();
  asm volatile("v_mfma_f32_16x16x32_bf16 %0, a[168:171], v[172:175], 0" : "=&v"(o3[3][2]) :  : ); SB();
  asm volatile("v_mfma_f32_16x16x32_bf16 %0, a[172:175], v[172:175], 0" : "=&v"(o3[3][3]) :  : ); SB();
  asm volatile("buffer_load_dwordx4 v[244:247], %0, %1, %2 offen offset:2048 sc1" :  : "v"(vHr), "s"(rWS), "s"(hso[3]) : "memory"); SB();
  asm volatile("v_mfma_f32_16x16x32_bf16 %0, a[176:179], v[172:175], 0" : "=&v"(o3[3][4]) :  : ); SB();
  asm volatile("s_waitcnt vmcnt(15)" :  :  : "memory"); SB();
  asm volatile("v_mfma_f32_16x16x32_bf16 %0, a[180:183], v[180:183], %0" : "+v"(o3[1][0]) :  : ); SB();
  asm volatile("v_rcp_f32 %0, %1" : "=v"(rd) : "v"(xd[0]) : ); SB();
  asm volatile("v_mfma_f32_16x16x32_bf16 %0, a[184:187], v[180:183], %0" : "+v"(o3[1][1]) :  : ); SB();
  asm volatile("v_mul_f32 %0, -%1, %2" : "=v"(mr) : "v"(xm[0]), "v"(rd) : ); SB();
  asm volatile("v_mfma_f32_16x16x32_bf16 %0, a[188:191], v[180:183], %0" : "+v"(o3[1][2]) :  : ); SB();
  asm volatile("buffer_load_dwordx4 v[248:251], %0, %1, %2 offen offset:2048 sc1" :  : "v"(vHr), "s"(rWS), "s"(hso[5]) : "memory"); SB();
  asm volatile("v_lshlrev_b32 %0, 16, %1" : "=v"(xf[0][0][0]) : "v"(xv[0][0]) : ); SB();
  asm volatile("v_mfma_f32_16x16x32_bf16 %0, a[192:195], v[180:183], %0" : "+v"(o3[1][3]) :  : ); SB();
  asm volatile("v_and_b32 %0, 0xffff0000, %1" : "=v"(xf[0][0][1]) : "v"(xv[0][0]) : ); SB();
  asm volatile("v_mfma_f32_16x16x32_bf16 %0, a[196:199], v[180:183], %0" : "+v"(o3[1][4]) :  : ); SB();
  asm volatile("v_lshlrev_b32 %0, 16, %1" : "=v"(xf[0][1][0]) : "v"(xv[0][1]) : ); SB();
  asm volatile("v_mfma_f32_16x16x32_bf16 %0, a[180:183], v[184:187], %0" : "+v"(o3[2][0]) :  : ); SB();
  asm volatile("buffer_load_dwordx4 v[252:255], %0, %1, %2 offen offset:2048 sc1" :  : "v"(vHr), "s"(rWS), "s"(hso[7]) : "memory"); SB();
  asm volatile("v_and_b32 %0, 0xffff0000, %1" : "=v"(xf[0][1][1]) : "v"(xv[0][1]) : ); SB();
  asm volatile("v_mfma_f32_16x16x32_bf16 %0, a[184:187], v[184:187], %0" : "+v"(o3[2][1]) :  : ); SB();
  asm volatile("v_lshlrev_b32 %0, 16, %1" : "=v"(xf[1][0][0]) : "v"(xv[1][0]) : ); SB();
  asm volatile("v_mfma_f32_16x16x32_bf16 %0, a[188:191], v[184:187], %0" : "+v"(o3[2][2]) :  : ); SB();
  asm volatile("v_and_b32 %0, 0xffff0000, %1" : "=v"(xf[1][0][1]) : "v"(xv[1][0]) : ); SB();
  asm volatile("v_mfma_f32_16x16x32_bf16 %0, a[192:195], v[184:187], %0" : "+v"(o3[2][3]) :  : ); SB();
  asm volatile("v_lshlrev_b32 %0, 16, %1" : "=v"(xf[1][1][0]) : "v"(xv[1][1]) : ); SB();
  asm volatile("v_mfma_f32_16x16x32_bf16 %0, a[196:199], v[184:187], %0" : "+v"(o3[2][4]) :  : ); SB();
  asm volatile("buffer_load_dwordx4 v[240:243], %0, %1, %2 offen offset:2048 sc1" :  : "v"(vHr), "s"(rWS), "s"(hso[1]) : "memory"); SB();
  asm volatile("v_and_b32 %0, 0xffff0000, %1" : "=v"(xf[1][1][1]) : "v"(xv[1][1]) : ); SB();
  asm volatile("v_mfma_f32_16x16x32_bf16 %0, a[180:183], v[188:191], %0" : "+v"(o3[3][0]) :  : ); SB();
  asm volatile("v_lshlrev_b32 %0, 16, %1" : "=v"(xf[2][0][0]) : "v"(xv[2][0]) : ); SB();
  asm volatile("v_mfma_f32_16x16x32_bf16 %0, a[184:187], v[188:191], %0" : "+v"(o3[3][1]) :  : ); SB();
  asm volatile("v_and_b32 %0, 0xffff0000, %1" : "=v"(xf[2][0][1]) : "v"(xv[2][0]) : ); SB();
  asm volatile("v_mfma_f32_16x16x32_bf16 %0, a[188:191], v[188:191], %0" : "+v"(o3[3][2]) :  : ); SB();
  asm volatile("v_lshlrev_b32 %0, 16, %1" : "=v"(xf[2][1][0]) : "v"(xv[2][1]) : ); SB();
  asm volatile("v_mfma_f32_16x16x32_bf16 %0, a[192:195], v[188:191], %0" : "+v"(o3[3][3]) :  : ); SB();
  asm volatile("v_and_b32 %0, 0xffff0000, %1" : "=v"(xf[2][1][1]) : "v"(xv[2][1]) : ); SB();
  asm volatile("v_mfma_f32_16x16x32_bf16 %0, a[196:199], v[188:191], %0" : "+v"(o3[3][4]) :  : ); SB();
  asm volatile("v_lshlrev_b32 %0, 16, %1" : "=v"(xf[3][0][0]) : "v"(xv[3][0]) : ); SB();
  asm volatile("s_waitcnt vmcnt(14)" :  :  : "memory"); SB();
  asm volatile("v_mfma_f32_16x16x32_bf16 %0, a[200:203], v[196:199], %0" : "+v"(o3[1][0]) :  : ); SB();
  asm volatile("v_and_b32 %0, 0xffff0000, %1" : "=v"(xf[3][0][1]) : "v"(xv[3][0]) : ); SB();
  asm volatile("v_mfma_f32_16x16x32_bf16 %0, a[204:207], v[196:199], %0" : "+v"(o3[1][1]) :  : ); SB();
  asm volatile("v_lshlrev_b32 %0, 16, %1" : "=v"(xf[3][1][0]) : "v"(xv[3][1]) : ); SB();
  asm volatile("v_mfma_f32_16x16x32_bf16 %0, a[208:211], v[196:199], %0" : "+v"(o3[1][2]) :  : ); SB();
  asm volatile("v_and_b32 %0, 0xffff0000, %1" : "=v"(xf[3][1][1]) : "v"(xv[3][1]) : ); SB();
  asm volatile("v_mfma_f32_16x16x32_bf16 %0, a[212:215], v[196:199], %0" : "+v"(o3[1][3]) :  : ); SB();
  asm volatile("v_lshlrev_b32 %0, 16, %1" : "=v"(xf[4][0][0]) : "v"(xv[4][0]) : ); SB();
  asm volatile("v_mfma_f32_16x16x32_bf16 %0, a[216:219], v[196:199], %0" : "+v"(o3[1][4]) :  : ); SB();
  asm volatile("v_and_b32 %0, 0xffff0000, %1" : "=v"(xf[4][0][1]) : "v"(xv[4][0]) : ); SB();
  asm volatile("v_mfma_f32_16x16x32_bf16 %0, a[200:203], v[200:203], %0" : "+v"(o3[2][0]) :  : ); SB();
  asm volatile("v_lshlrev_b32 %0, 16, %1" : "=v"(xf[4][1][0]) : "v"(xv[4][1]) : ); SB();
  asm volatile("v_mfma_f32_16x16x32_bf16 %0, a[204:207], v[200:203], %0" : "+v"(o3[2][1]) :  : ); SB();
  asm volatile("v_and_b32 %0, 0xffff0000, %1" : "=v"(xf[4][1][1]) : "v"(xv[4][1]) : ); SB();
  asm volatile("v_mfma_f32_16x16x32_bf16 %0, a[208:211], v[200:203], %0" : "+v"(o3[2][2]) :  : ); SB();
  asm volatile("v_fma_f32 %0, %1, %2, %3" : "=v"(sv[0][0][0]) : "v"(xf[0][0][0]), "v"(rd), "v"(mr) : ); SB();
  asm volatile("v_mfma_f32_16x16x32_bf16 %0, a[212:215], v[200:203], %0" : "+v"(o3[2][3]) :  : ); SB();
  asm volatile("v_fma_f32 %0, %1, %2, %3" : "=v"(sv[0][0][1]) : "v"(xf[0][0][1]), "v"(rd), "v"(mr) : ); SB();
  asm volatile("v_mfma_f32_16x16x32_bf16 %0, a[216:219], v[200:203], %0" : "+v"(o3[2][4]) :  : ); SB();
  asm volatile("v_fma_f32 %0, %1, %2, %3" : "=v"(sv[0][1][0]) : "v"(xf[0][1][0]), "v"(rd), "v"(mr) : ); SB();
  asm volatile("v_mfma_f32_16x16x32_bf16 %0, a[200:203], v[204:207], %0" : "+v"(o3[3][0]) :  : ); SB();
  asm volatile("v_fma_f32 %0, %1, %2, %3" : "=v"(sv[0][1][1]) : "v"(xf[0][1][1]), "v"(rd), "v"(mr) : ); SB();
  asm volatile("v_mfma_f32_16x16x32_bf16 %0, a[204:207], v[204:207], %0" : "+v"(o3[3][1]) :  : ); SB();
  asm volatile("v_fma_f32 %0, %1, %2, %3" : "=v"(sv[1][0][0]) : "v"(xf[1][0][0]), "v"(rd), "v"(mr) : ); SB();
  asm volatile("v_mfma_f32_16x16x32_bf16 %0, a[208:211], v[204:207], %0" : "+v"(o3[3][2]) :  : ); SB();
  asm volatile("v_fma_f32 %0, %1, %2, %3" : "=v"(sv[1][0][1]) : "v"(xf[1][0][1]), "v"(rd), "v"(mr) : ); SB();
  asm volatile("v_mfma_f32_16x16x32_bf16 %0, a[212:215], v[204:207], %0" : "+v"(o3[3][3]) :  : ); SB();
  asm volatile("v_fma_f32 %0, %1, %2, %3" : "=v"(sv[1][1][0]) : "v"(xf[1][1][0]), "v"(rd), "v"(mr) : ); SB();
  asm volatile("v_mfma_f32_16x16x32_bf16 %0, a[216:219], v[204:207], %0" : "+v"(o3[3][4]) :  : ); SB();
  asm volatile("v_fma_f32 %0, %1, %2, %3" : "=v"(sv[1][1][1]) : "v"(xf[1][1][1]), "v"(rd), "v"(mr) : ); SB();
  asm volatile("s_waitcnt vmcnt(8)" :  :  : "memory"); SB();
  asm volatile("v_mfma_f32_16x16x32_bf16 %0, a[220:223], v[212:215], %0" : "+v"(o3[1][0]) :  : ); SB();
  asm volatile("v_fma_f32 %0, %1, %2, %3" : "=v"(sv[2][0][0]) : "v"(xf[2][0][0]), "v"(rd), "v"(mr) : ); SB();
  asm volatile("v_mfma_f32_16x16x32_bf16 %0, a[224:227], v[212:215], %0" : "+v"(o3[1][1]) :  : ); SB();
  asm volatile("v_fma_f32 %0, %1, %2, %3" : "=v"(sv[2][0][1]) : "v"(xf[2][0][1]), "v"(rd), "v"(mr) : ); SB();
  asm volatile("v_mfma_f32_16x16x32_bf16 %0, a[228:231], v[212:215], %0" : "+v"(o3[1][2]) :  : ); SB();
  asm volatile("v_fma_f32 %0, %1, %2, %3" : "=v"(sv[2][1][0]) : "v"(xf[2][1][0]), "v"(rd), "v"(mr) : ); SB();
  asm volatile("v_mfma_f32_16x16x32_bf16 %0, a[232:235], v[212:215], %0" : "+v"(o3[1][3]) :  : ); SB();
  asm volatile("v_fma_f32 %0, %1, %2, %3" : "=v"(sv[2][1][1]) : "v"(xf[2][1][1]), "v"(rd), "v"(mr) : ); SB();
  asm volatile("v_mfma_f32_16x16x32_bf16 %0, a[236:239], v[212:215], %0" : "+v"(o3[1][4]) :  : ); SB();
  asm volatile("v_fma_f32 %0, %1, %2, %3" : "=v"(sv[3][0][0]) : "v"(xf[3][0][0]), "v"(rd), "v"(mr) : ); SB();
  asm volatile("v_mfma_f32_16x16x32_bf16 %0, a[220:223], v[216:219], %0" : "+v"(o3[2][0]) :  : ); SB();
  asm volatile("v_fma_f32 %0, %1, %2, %3" : "=v"(sv[3][0][1]) : "v"(xf[3][0][1]), "v"(rd), "v"(mr) : ); SB();
  asm volatile("v_mfma_f32_16x16x32_bf16 %0, a[224:227], v[216:219], %0" : "+v"(o3[2][1]) :  : ); SB();
  asm volatile("v_fma_f32 %0, %1, %2, %3" : "=v"(sv[3][1][0]) : "v"(xf[3][1][0]), "v"(rd), "v"(mr) : ); SB();
  asm volatile("v_mfma_f32_16x16x32_bf16 %0, a[228:231], v[216:219], %0" : "+v"(o3[2][2]) :  : ); SB();
  asm volatile("v_fma_f32 %0, %1, %2, %3" : "=v"(sv[3][1][1]) : "v"(xf[3][1][1]), "v"(rd), "v"(mr) : ); SB();
  asm volatile("v_mfma_f32_16x16x32_bf16 %0, a[232:235], v[216:219], %0" : "+v"(o3[2][3]) :  : ); SB();
  asm volatile("v_fma_f32 %0, %1, %2, %3" : "=v"(sv[4][0][0]) : "v"(xf[4][0][0]), "v"(rd), "v"(mr) : ); SB();
  asm volatile("v_mfma_f32_16x16x32_bf16 %0, a[236:239], v[216:219], %0" : "+v"(o3[2][4]) :  : ); SB();
  asm volatile("v_fma_f32 %0, %1, %2, %3" : "=v"(sv[4][0][1]) : "v"(xf[4][0][1]), "v"(rd), "v"(mr) : ); SB();
  asm volatile("v_mfma_f32_16x16x32_bf16 %0, a[220:223], v[220:223], %0" : "+v"(o3[3][0]) :  : ); SB();
  asm volatile("v_fma_f32 %0, %1, %2, %3" : "=v"(sv[4][1][0]) : "v"(xf[4][1][0]), "v"(rd), "v"(mr) : ); SB();
  asm volatile("v_mfma_f32_16x16x32_bf16 %0, a[224:227], v[220:223], %0" : "+v"(o3[3][1]) :  : ); SB();
  asm volatile("v_fma_f32 %0, %1, %2, %3" : "=v"(sv[4][1][1]) : "v"(xf[4][1][1]), "v"(rd), "v"(mr) : ); SB();
  asm volatile("v_mfma_f32_16x16x32_bf16 %0, a[228:231], v[220:223], %0" : "+v"(o3[3][2]) :  : ); SB();
  asm volatile("v_mfma_f32_16x16x32_bf16 %0, a[232:235], v[220:223], %0" : "+v"(o3[3][3]) :  : ); SB();
  asm volatile("v_mfma_f32_16x16x32_bf16 %0, a[236:239], v[220:223], %0" : "+v"(o3[3][4]) :  : ); SB();
  ST(30);
  asm volatile("s_waitcnt vmcnt(0)\n\t.Lc_%=:\n\tv_max3_u32 %1, v231, v235, v239\n\tv_max3_u32 %1, %1, v227, v247\n\tv_max3_u32 %1, %1, v251, v255\n\tv_max3_u32 %1, %1, v243, v243\n\tv_cmp_eq_u32 vcc, -1, %1\n\ts_cbranch_vccnz .Lr_%=\n\t.Ld_%=:\n\t.subsection 1\n\t.Lr_%=:\n\tv_add_u32 %0, -1, %0\n\ts_nop 1\n\tv_readfirstlane_b32 vcc_hi, %0\n\ts_nop 3\n\ts_cmp_lt_i32 vcc_hi, 1\n\ts_cbranch_scc1 .Ld_%=\n\ts_sleep 2\n\tbuffer_load_dwordx4 v[228:231], %2, %3, %6 offen offset:2048 sc1\n\tbuffer_load_dwordx4 v[232:235], %2, %3, %8 offen offset:2048 sc1\n\tbuffer_load_dwordx4 v[236:239], %2, %3, %10 offen offset:2048 sc1\n\tbuffer_load_dwordx4 v[224:227], %2, %3, %4 offen offset:2048 sc1\n\tbuffer_load_dwordx4 v[244:247], %2, %3, %7 offen offset:2048 sc1\n\tbuffer_load_dwordx4 v[248:251], %2, %3, %9 offen offset:2048 sc1\n\tbuffer_load_dwordx4 v[252:255], %2, %3, %11 offen offset:2048 sc1\n\tbuffer_load_dwordx4 v[240:243], %2, %3, %5 offen offset:2048 sc1\n\ts_waitcnt vmcnt(0)\n\ts_branch .Lc_%=\n\t.subsection 0" : "+v"(tmo), "=&v"(vtmp) : "v"(vHr), "s"(rWS), "s"(hso[0]), "s"(hso[1]), "s"(hso[2]), "s"(hso[3]), "s"(hso[4]), "s"(hso[5]), "s"(hso[6]), "s"(hso[7]) : "memory", "vcc", "scc"); SB();
  asm volatile("v_mfma_f32_16x16x32_bf16 %0, a[240:243], v[228:231], %0" : "+v"(o3[1][0]) :  : ); SB();
  asm volatile("v_mfma_f32_16x16x32_bf16 %0, a[244:247], v[228:231], %0" : "+v"(o3[1][1]) :  : ); SB();
  asm volatile("v_mfma_f32_16x16x32_bf16 %0, a[248:251], v[228:231], %0" : "+v"(o3[1][2]) :  : ); SB();
  asm volatile("v_mfma_f32_16x16x32_bf16 %0, a[252:255], v[228:231], %0" : "+v"(o3[1][3]) :  : ); SB();
  asm volatile("v_mfma_f32_16x16x32_bf16 %0, a[0:3], v[228:231], %0" : "+v"(o3[1][4]) :  : ); SB();
  asm volatile("v_mfma_f32_16x16x32_bf16 %0, a[4:7], v[244:247], %0" : "+v"(o3[1][0]) :  : ); SB();
  asm volatile("v_mfma_f32_16x16x32_bf16 %0, a[8:11], v[244:247], %0" : "+v"(o3[1][1]) :  : ); SB();
  asm volatile("v_mfma_f32_16x16x32_bf16 %0, a[12:15], v[244:247], %0" : "+v"(o3[1][2]) :  : ); SB();
  asm volatile("v_mfma_f32_16x16x32_bf16 %0, a[16:19], v[244:247], %0" : "+v"(o3[1][3]) :  : ); SB();
  asm volatile("v_mfma_f32_16x16x32_bf16 %0, a[20:23], v[244:247], %0" : "+v"(o3[1][4]) :  : ); SB();
  asm volatile("v_mfma_f32_16x16x32_bf16 %0, a[240:243], v[232:235], %0" : "+v"(o3[2][0]) :  : ); SB();
  asm volatile("ds_write_b128 %0, %1 offset:0" :  : "v"(pw3), "v"(o3[1][0]) : "memory"); SB();
  asm volatile("v_mfma_f32_16x16x32_bf16 %0, a[244:247], v[232:235], %0" : "+v"(o3[2][1]) :  : ); SB();
  asm volatile("v_mfma_f32_16x16x32_bf16 %0, a[248:251], v[232:235], %0" : "+v"(o3[2][2]) :  : ); SB();
  asm volatile("v_mfma_f32_16x16x32_bf16 %0, a[252:255], v[232:235], %0" : "+v"(o3[2][3]) :  : ); SB();
  asm volatile("ds_write_b128 %0, %1 offset:1024" :  : "v"(pw3), "v"(o3[1][1]) : "memory"); SB();
  asm volatile("v_mfma_f32_16x16x32_bf16 %0, a[0:3], v[232:235], %0" : "+v"(o3[2][4]) :  : ); SB();
  asm volatile("v_mfma_f32_16x16x32_bf16 %0, a[4:7], v[248:251], %0" : "+v"(o3[2][0]) :  : ); SB();
  asm volatile("buffer_load_dwordx4 v[164:167], %0, %1, %2 offen offset:0 sc1" :  : "v"(vKV), "s"(rWS), "s"(0u) : "memory"); SB();
  asm volatile("v_mfma_f32_16x16x32_bf16 %0, a[8:11], v[248:251], %0" : "+v"(o3[2][1]) :  : ); SB();
  asm volatile("buffer_load_dwordx4 v[168:171], %0, %1, %2 offen offset:0 sc1" :  : "v"(vKV), "s"(rWS), "s"(w3k[0]) : "memory"); SB();
  asm volatile("v_mfma_f32_16x16x32_bf16 %0, a[12:15], v[248:251], %0" : "+v"(o3[2][2]) :  : ); SB();
  asm volatile("buffer_load_dwordx4 v[172:175], %0, %1, %2 offen offset:0 sc1" :  : "v"(vKV), "s"(rWS), "s"(w3k[1]) : "memory"); SB();
  asm volatile("v_mfma_f32_16x16x32_bf16 %0, a[16:19], v[248:251], %0" : "+v"(o3[2][3]) :  : ); SB();
  asm volatile("ds_write_b128 %0, %1 offset:2048" :  : "v"(pw3), "v"(o3[1][2]) : "memory"); SB();
  asm volatile("v_mfma_f32_16x16x32_bf16 %0, a[20:23], v[248:251], %0" : "+v"(o3[2][4]) :  : ); SB();
  asm volatile("v_mfma_f32_16x16x32_bf16 %0, a[240:243], v[236:239], %0" : "+v"(o3[3][0]) :  : ); SB();
  asm volatile("v_mfma_f32_16x16x32_bf16 %0, a[244:247], v[236:239], %0" : "+v"(o3[3][1]) :  : ); SB();
  asm volatile("ds_write_b128 %0, %1 offset:3072" :  : "v"(pw3), "v"(o3[1][3]) : "memory"); SB();
  asm volatile("v_mfma_f32_16x16x32_bf16 %0, a[248:251], v[236:239], %0" : "+v"(o3[3][2]) :  : ); SB();
  asm volatile("v_mfma_f32_16x16x32_bf16 %0, a[252:255], v[236:239], %0" : "+v"(o3[3][3]) :  : ); SB();
  asm volatile("v_mfma_f32_16x16x32_bf16 %0, a[0:3], v[236:239], %0" : "+v"(o3[3][4]) :  : ); SB();
  asm volatile("ds_write_b128 %0, %1 offset:4096" :  : "v"(pw3), "v"(o3[1][4]) : "memory"); SB();
  asm volatile("v_mfma_f32_16x16x32_bf16 %0, a[4:7], v[252:255], %0" : "+v"(o3[3][0]) :  : ); SB();
  asm volatile("v_mfma_f32_16x16x32_bf16 %0, a[8:11], v[252:255], %0" : "+v"(o3[3][1]) :  : ); SB();
  asm volatile("v_mfma_f32_16x16x32_bf16 %0, a[12:15], v[252:255], %0" : "+v"(o3[3][2]) :  : ); SB();
  asm volatile("ds_write_b128 %0, %1 offset:5120" :  : "v"(pw3), "v"(o3[2][0]) : "memory"); SB();
  asm volatile("v_mfma_f32_16x16x32_bf16 %0, a[16:19], v[252:255], %0" : "+v"(o3[3][3]) :  : ); SB();
  asm volatile("v_mfma_f32_16x16x32_bf16 %0, a[20:23], v[252:255], %0" : "+v"(o3[3][4]) :  : ); SB();
  asm volatile("v_mfma_f32_16x16x32_bf16 %0, a[160:163], v[160:163], 0" : "=&v"(o3[0][0]) :  : ); SB();
  asm volatile("ds_write_b128 %0, %1 offset:6144" :  : "v"(pw3), "v"(o3[2][1]) : "memory"); SB();
  asm volatile("v_mfma_f32_16x16x32_bf16 %0, a[164:167], v[160:163], 0" : "=&v"(o3[0][1]) :  : ); SB();
  asm volatile("v_mfma_f32_16x16x32_bf16 %0, a[168:171], v[160:163], 0" : "=&v"(o3[0][2]) :  : ); SB();
  asm volatile("v_mfma_f32_16x16x32_bf16 %0, a[172:175], v[160:163], 0" : "=&v"(o3[0][3]) :  : ); SB();
  asm volatile("ds_write_b128 %0, %1 offset:7168" :  : "v"(pw3), "v"(o3[2][2]) : "memory"); SB();
  asm volatile("v_mfma_f32_16x16x32_bf16 %0, a[176:179], v[160:163], 0" : "=&v"(o3[0][4]) :  : ); SB();
  asm volatile("v_mfma_f32_16x16x32_bf16 %0, a[180:183], v[176:179], %0" : "+v"(o3[0][0]) :  : ); SB();
  asm volatile("v_mfma_f32_16x16x32_bf16 %0, a[184:187], v[176:179], %0" : "+v"(o3[0][1]) :  : ); SB();
  asm volatile("ds_write_b128 %0, %1 offset:8192" :  : "v"(pw3), "v"(o3[2][3]) : "memory"); SB();
  asm volatile("v_mfma_f32_16x16x32_bf16 %0, a[188:191], v[176:179], %0" : "+v"(o3[0][2]) :  : ); SB();
  asm volatile("v_mfma_f32_16x16x32_bf16 %0, a[192:195], v[176:179], %0" : "+v"(o3[0][3]) :  : ); SB();
  asm volatile("v_mfma_f32_16x16x32_bf16 %0, a[196:199], v[176:179], %0" : "+v"(o3[0][4]) :  : ); SB();
  asm volatile("ds_write_b128 %0, %1 offset:9216" :  : "v"(pw3), "v"(o3[2][4]) : "memory"); SB();
  asm volatile("v_mfma_f32_16x16x32_bf16 %0, a[200:203], v[192:195], %0" : "+v"(o3[0][0]) :  : ); SB();
  asm volatile("v_mfma_f32_16x16x32_bf16 %0, a[204:207], v[192:195], %0" : "+v"(o3[0][1]) :  : ); SB();
  asm volatile("v_mfma_f32_16x16x32_bf16 %0, a[208:211], v[192:195], %0" : "+v"(o3[0][2]) :  : ); SB();
  asm volatile("ds_write_b128 %0, %1 offset:10240" :  : "v"(pw3), "v"(o3[3][0]) : "memory"); SB();
  asm volatile("v_mfma_f32_16x16x32_bf16 %0, a[212:215], v[192:195], %0" : "+v"(o3[0][3]) :  : ); SB();
  asm volatile("v_mfma_f32_16x16x32_bf16 %0, a[216:219], v[192:195], %0" : "+v"(o3[0][4]) :  : ); SB();
  asm volatile("v_mfma_f32_16x16x32_bf16 %0, a[220:223], v[208:211], %0" : "+v"(o3[0][0]) :  : ); SB();
  asm volatile("ds_write_b128 %0, %1 offset:11264" :  : "v"(pw3), "v"(o3[3][1]) : "memory"); SB();
  asm volatile("v_mfma_f32_16x16x32_bf16 %0, a[224:227], v[208:211], %0" : "+v"(o3[0][1]) :  : ); SB();
  asm volatile("v_mfma_f32_16x16x32_bf16 %0, a[228:231], v[208:211], %0" : "+v"(o3[0][2]) :  : ); SB();
  asm volatile("v_mfma_f32_16x16x32_bf16 %0, a[232:235], v[208:211], %0" : "+v"(o3[0][3]) :  : ); SB();
  asm volatile("ds_write_b128 %0, %1 offset:12288" :  : "v"(pw3), "v"(o3[3][2]) : "memory"); SB();
  asm volatile("v_mfma_f32_16x16x32_bf16 %0, a[236:239], v[208:211], %0" : "+v"(o3[0][4]) :  : ); SB();
  asm volatile("v_mfma_f32_16x16x32_bf16 %0, a[240:243], v[224:227], %0" : "+v"(o3[0][0]) :  : ); SB();
  asm volatile("v_mfma_f32_16x16x32_bf16 %0, a[244:247], v[224:227], %0" : "+v"(o3[0][1]) :  : ); SB();
  asm volatile("ds_write_b128 %0, %1 offset:13312" :  : "v"(pw3), "v"(o3[3][3]) : "memory"); SB();
  asm volatile("v_mfma_f32_16x16x32_bf16 %0, a[248:251], v[224:227], %0" : "+v"(o3[0][2]) :  : ); SB();
  asm volatile("v_mfma_f32_16x16x32_bf16 %0, a[252:255], v[224:227], %0" : "+v"(o3[0][3]) :  : ); SB();
  asm volatile("v_mfma_f32_16x16x32_bf16 %0, a[0:3], v[224:227], %0" : "+v"(o3[0][4]) :  : ); SB();
  asm volatile("ds_write_b128 %0, %1 offset:14336" :  : "v"(pw3), "v"(o3[3][4]) : "memory"); SB();
  asm volatile("v_mfma_f32_16x16x32_bf16 %0, a[4:7], v[240:243], %0" : "+v"(o3[0][0]) :  : ); SB();
  asm volatile("v_mfma_f32_16x16x32_bf16 %0, a[8:11], v[240:243], %0" : "+v"(o3[0][1]) :  : ); SB();
  asm volatile("v_mfma_f32_16x16x32_bf16 %0, a[12:15], v[240:243], %0" : "+v"(o3[0][2]) :  : ); SB();
  asm volatile("v_mfma_f32_16x16x32_bf16 %0, a[16:19], v[240:243], %0" : "+v"(o3[0][3]) :  : ); SB();
  asm volatile("v_mfma_f32_16x16x32_bf16 %0, a[20:23], v[240:243], %0" : "+v"(o3[0][4]) :  : ); SB();
  asm volatile("s_waitcnt lgkmcnt(0)\n\ts_barrier" :  :  : "memory"); SB();
  asm volatile("ds_read_b128 %0, %1 offset:0" : "=&v"(o3[1][0]) : "v"(p3[0]) : "memory"); SB();
  asm volatile("ds_read_b128 %0, %1 offset:5120" : "=&v"(o3[2][0]) : "v"(p3[1]) : "memory"); SB();
  asm volatile("ds_read_b128 %0, %1 offset:10240" : "=&v"(o3[3][0]) : "v"(p3[2]) : "memory"); SB();
  asm volatile("ds_read_b128 %0, %1 offset:1024" : "=&v"(o3[1][1]) : "v"(p3[0]) : "memory"); SB();
  asm volatile("ds_read_b128 %0, %1 offset:6144" : "=&v"(o3[2][1]) : "v"(p3[1]) : "memory"); SB();
  asm volatile("ds_read_b128 %0, %1 offset:11264" : "=&v"(o3[3][1]) : "v"(p3[2]) : "memory"); SB();
  asm volatile("ds_read_b128 %0, %1 offset:2048" : "=&v"(o3[1][2]) : "v"(p3[0]) : "memory"); SB();
  asm volatile("ds_read_b128 %0, %1 offset:7168" : "=&v"(o3[2][2]) : "v"(p3[1]) : "memory"); SB();
  asm volatile("ds_read_b128 %0, %1 offset:12288" : "=&v"(o3[3][2]) : "v"(p3[2]) : "memory"); SB();
  asm volatile("ds_read_b128 %0, %1 offset:3072" : "=&v"(o3[1][3]) : "v"(p3[0]) : "memory"); SB();
  asm volatile("ds_read_b128 %0, %1 offset:8192" : "=&v"(o3[2][3]) : "v"(p3[1]) : "memory"); SB();
  asm volatile("ds_read_b128 %0, %1 offset:13312" : "=&v"(o3[3][3]) : "v"(p3[2]) : "memory"); SB();
  asm volatile("ds_read_b128 %0, %1 offset:4096" : "=&v"(o3[1][4]) : "v"(p3[0]) : "memory"); SB();
  asm volatile("ds_read_b128 %0, %1 offset:9216" : "=&v"(o3[2][4]) : "v"(p3[1]) : "memory"); SB();
  asm volatile("ds_read_b128 %0, %1 offset:14336" : "=&v"(o3[3][4]) : "v"(p3[2]) : "memory"); SB();
  ST(21);
  u32x2 yr[5];
  asm volatile("s_waitcnt lgkmcnt(9)" : "+v"(o3[0][0]), "+v"(o3[0][1]), "+v"(o3[1][0]), "+v"(o3[1][1]), "+v"(o3[2][0]), "+v"(o3[2][1]), "+v"(o3[3][0]), "+v"(o3[3][1]) :  : "memory"); SB();
#pragma unroll
  for (int j = 0; j <= 1; ++j) {
    const f32x4 o4 = (o3[0][j] + o3[1][j]) + (o3[2][j] + o3[3][j]);
#pragma unroll
    for (int h = 0; h < 2; ++h) {
      const f32x2 o = {o4[2 * h], o4[2 * h + 1]};
      const f32x2 a = o * (f32x2){-1.4426950f, -1.4426950f};
      const f32x2 e = (f32x2){__builtin_amdgcn_exp2f(a[0]), __builtin_amdgcn_exp2f(a[1])} + (f32x2){1.0f, 1.0f};
      const f32x2 r = {__builtin_amdgcn_rcpf(e[0]), __builtin_amdgcn_rcpf(e[1])};
      sv[j][h] = (sv[j][h] * o) * r;
    }
  }
  asm volatile("" : "+v"(sv[0][0]), "+v"(sv[0][1]), "+v"(sv[1][0]), "+v"(sv[1][1]));
  SB();
  asm volatile("s_waitcnt lgkmcnt(0)" : "+v"(o3[0][2]), "+v"(o3[0][3]), "+v"(o3[1][2]), "+v"(o3[1][3]), "+v"(o3[2][2]), "+v"(o3[2][3]), "+v"(o3[3][2]), "+v"(o3[3][3]) :  : "memory"); SB();
#pragma unroll
  for (int j = 2; j <= 3; ++j) {
    const f32x4 o4 = (o3[0][j] + o3[1][j]) + (o3[2][j] + o3[3][j]);
#pragma unroll
    for (int h = 0; h < 2; ++h) {
      const f32x2 o = {o4[2 * h], o4[2 * h + 1]};
      const f32x2 a = o * (f32x2){-1.4426950f, -1.4426950f};
      const f32x2 e = (f32x2){__builtin_amdgcn_exp2f(a[0]), __builtin_amdgcn_exp2f(a[1])} + (f32x2){1.0f, 1.0f};
      const f32x2 r = {__builtin_amdgcn_rcpf(e[0]), __builtin_amdgcn_rcpf(e[1])};
      sv[j][h] = (sv[j][h] * o) * r;
    }
  }
  asm volatile("" : "+v"(sv[2][0]), "+v"(sv[2][1]), "+v"(sv[3][0]), "+v"(sv[3][1]));
  SB();
  ST(22);
  asm volatile("s_waitcnt vmcnt(0)\n\t.Lc_%=:\n\tv_xnor_b32 v167, v167, %6\n\tv_xnor_b32 v171, v171, %7\n\tv_xnor_b32 v175, v175, %8\n\tv_max3_u32 %1, v167, v171, v175\n\tv_cmp_eq_u32 vcc, -1, %1\n\ts_cbranch_vccnz .Lr_%=\n\t.Ld_%=:\n\t.subsection 1\n\t.Lr_%=:\n\tv_add_u32 %0, -1, %0\n\ts_nop 1\n\tv_readfirstlane_b32 vcc_hi, %0\n\ts_nop 3\n\ts_cmp_lt_i32 vcc_hi, 1\n\ts_cbranch_scc1 .Ld_%=\n\ts_sleep 2\n\tbuffer_load_dwordx4 v[164:167], %2, %3, 0 offen sc1\n\tbuffer_load_dwordx4 v[168:171], %2, %3, %4 offen sc1\n\tbuffer_load_dwordx4 v[172:175], %2, %3, %5 offen sc1\n\ts_waitcnt vmcnt(0)\n\ts_branch .Lc_%=\n\t.subsection 0" : "+v"(tmo), "=&v"(vtmp) : "v"(vKV), "s"(rWS), "s"(w3k[0]), "s"(w3k[1]), "v"(kz[0]), "v"(kz[1]), "v"(kz[2]) : "memory", "vcc", "scc"); SB();
  ST(31);
  asm volatile("ds_write_b128 %0, v[164:167] offset:0" :  : "v"(yw) : "memory"); SB();
  asm volatile("ds_write_b128 %0, v[168:171] offset:1024" :  : "v"(yw) : "memory"); SB();
  asm volatile("ds_write_b128 %0, v[172:175] offset:2048" :  : "v"(yw) : "memory"); SB();
  asm volatile("s_waitcnt lgkmcnt(0)\n\ts_barrier" :  :  : "memory"); SB();
  asm volatile("ds_read_b64_tr_b16 %0, %1 offset:0" : "=&v"(yr[0]) : "v"(ya) : "memory"); SB();
  asm volatile("ds_read_b64_tr_b16 %0, %1 offset:2048" : "=&v"(yr[1]) : "v"(ya) : "memory"); SB();
  asm volatile("ds_read_b64_tr_b16 %0, %1 offset:4096" : "=&v"(yr[2]) : "v"(ya) : "memory"); SB();
  asm volatile("ds_read_b64_tr_b16 %0, %1 offset:6144" : "=&v"(yr[3]) : "v"(ya) : "memory"); SB();
  asm volatile("ds_read_b64_tr_b16 %0, %1 offset:8192" : "=&v"(yr[4]) : "v"(ya) : "memory"); SB();
  asm volatile("" : "+v"(o3[0][4]), "+v"(o3[1][4]), "+v"(o3[2][4]), "+v"(o3[3][4]) :  : "memory"); SB();
#pragma unroll
  for (int j = 4; j <= 4; ++j) {
    const f32x4 o4 = (o3[0][j] + o3[1][j]) + (o3[2][j] + o3[3][j]);
#pragma unroll
    for (int h = 0; h < 2; ++h) {
      const f32x2 o = {o4[2 * h], o4[2 * h + 1]};
      const f32x2 a = o * (f32x2){-1.4426950f, -1.4426950f};
      const f32x2 e = (f32x2){__builtin_amdgcn_exp2f(a[0]), __builtin_amdgcn_exp2f(a[1])} + (f32x2){1.0f, 1.0f};
      const f32x2 r = {__builtin_amdgcn_rcpf(e[0]), __builtin_amdgcn_rcpf(e[1])};
      sv[j][h] = (sv[j][h] * o) * r;
    }
  }
  asm volatile("" : "+v"(sv[4][0]), "+v"(sv[4][1]));
  SB();
  asm volatile("s_waitcnt lgkmcnt(0)" : "+v"(yr[0]), "+v"(yr[1]), "+v"(yr[2]), "+v"(yr[3]), "+v"(yr[4]) :  : "memory"); SB();
  {
    unsigned pk[5][2];
#pragma unroll
    for (int n = 0; n < 10; ++n) pk[n >> 1][n & 1] = pk2(__builtin_elementwise_fma(sv[n >> 1][n & 1], unpk(yr[n >> 1][n & 1]), xf[n >> 1][n & 1]));
#pragma unroll
    for (int q = 0; q < 2; ++q) {
      const auto s0 = __builtin_amdgcn_permlane16_swap(pk[2 * q][0], pk[2 * q + 1][0], false, false);
      const auto s1 = __builtin_amdgcn_permlane16_swap(pk[2 * q][1], pk[2 * q + 1][1], false, false);
      asm volatile("s_nop 1\n\tglobal_store_dwordx4 %0, %1, %2 offset:%3 " :: "v"(po), "v"((u32x4){s0[0], s1[0], s0[1], s1[1]}), "s"(out), "i"(64 * q) : "memory");
    }
    asm volatile("global_store_dwordx2 %0, %1, %2 offset:128 " :: "v"(vX), "v"((u32x2){pk[4][0], pk[4][1]}), "s"(out) : "memory");
  }
  ST(23);
  ST_OUT();
  }}}
}

extern "C" __global__ void k_end() { asm volatile(".subsection 2\n.Lcode_end:\n\ts_endpgm\n\t.subsection 0"); }

extern "C" void kernel_launch(void* const* d_in, const int* in_sizes, int n_in,
                              void* d_out, int out_size, void* d_ws, size_t ws_size,
                              hipStream_t stream) {
  (void)hipFuncSetAttribute((const void*)k_all, hipFuncAttributeMaxDynamicSharedMemorySize, 163840);
  k_all<<<256, 256, 163840, stream>>>((const char*)d_in[0], (const char*)d_in[1], (const char*)d_in[2], (const char*)d_in[3], (const char*)d_in[4],
                                      (const char*)d_in[5], (const char*)d_in[7], (const char*)d_in[8], (const char*)d_in[9], (const char*)d_in[10],
                                      (char*)d_out, (char*)d_ws);
}
```
